```python
import torch

def _softmax(x, axis=-1):
    return torch.softmax(x, dim=axis)
def _gelu(x):
    return torch.nn.functional.gelu(x, approximate="tanh")
def _silu(x):
    return torch.nn.functional.silu(x)
def _sigmoid(x):
    return torch.sigmoid(x)
def _layernorm(x, axis=-1, eps=1e-5):
    m = x.mean(dim=axis, keepdim=True)
    v = x.var(dim=axis, keepdim=True, unbiased=False)
    return (x - m) / torch.sqrt(v + eps)
def _scatter_add(tgt, idx, src, axis=0):
    return tgt.clone().index_add_(axis, idx, src)
def _segment_sum(src, seg, n):
    out = torch.zeros((n,) + src.shape[1:], dtype=src.dtype)
    return out.index_add_(0, seg, src)
def _pad(x, pw):
    flat = tuple(v for pair in reversed(pw) for v in pair)
    return torch.nn.functional.pad(x, flat)

kernel_name = 'concat_heads__sliding_local_dilated__compressed_kv_cla__prompt'

def setup_inputs():

    _w = (lambda a: (lambda t: t.to(torch.bfloat16) if t.is_floating_point() else t)(torch.as_tensor(a)))
    def _A_setup():
        B, S, D = 1, 2048, 1024
        return {
            "x": _w((lambda *s: torch.randn(*s, dtype=torch.bfloat16) * 0.2)(B, S, D)),
            "wqkv": _w((lambda *s: torch.randn(*s, dtype=torch.bfloat16) * 0.2)(D, 3 * D)),
            "wo": _w((lambda *s: torch.randn(*s, dtype=torch.bfloat16) * 0.05)(D, D)),
        }
    _A_in = _A_setup()

    def _B_setup():
        B, H, Sq, Sk, D, M = 1, 32, 2048, 2048, 128, 1024
        Dc = 4 * D
        return {
            "x": _w((lambda *s: torch.randn(*s, dtype=torch.bfloat16) * 0.2)(B, Sq, M)),
            "c_kv": _w((lambda *s: torch.randn(*s, dtype=torch.bfloat16) * 1.6)(B, Sk, Dc)),
            "w_q": _w((lambda *s: torch.randn(*s, dtype=torch.bfloat16) * 3.2)(M, H, D) * (M ** -0.5)),
            "w_uk": _w((lambda *s: torch.randn(*s, dtype=torch.bfloat16) * 3.2)(Dc, H, D) * (Dc ** -0.5)),
            "w_uv": _w((lambda *s: torch.randn(*s, dtype=torch.bfloat16) * 1.6)(Dc, H, D) * (Dc ** -0.5)),
            "w_o": _w((lambda *s: torch.randn(*s, dtype=torch.bfloat16) * 1.6)(H, D, M) * ((H * D) ** -0.5)),
        }
    _B_in = _B_setup()

    return {
        "A_x": _A_in["x"],
        "A_wqkv": _A_in["wqkv"],
        "A_wo": _A_in["wo"],
        "B_x": _B_in["x"],
        "B_c_kv": _B_in["c_kv"],
        "B_w_q": _B_in["w_q"],
        "B_w_uk": _B_in["w_uk"],
        "B_w_uv": _B_in["w_uv"],
        "B_w_o": _B_in["w_o"],
    }

def reference(A_x, A_wqkv, A_wo, B_x, B_c_kv, B_w_q, B_w_uk, B_w_uv, B_w_o):
    def _A_ref(x, wqkv, wo):
        B, S, D = x.shape
        H = 32; Dh = D // H
        W, dil = 8, 2
        qkv = (lambda x, *d: x.permute(*d))((x @ wqkv).reshape(B, S, 3, H, Dh), 2, 0, 3, 1, 4)
        q, k, v = qkv[0], qkv[1], qkv[2]
        scale = Dh ** -0.5
        pos = torch.arange(S)
        off = torch.arange(W) * dil
        idx = pos[:, None] - off[None, :]
        mask = idx >= 0
        idx_c = torch.where(mask, idx, 0).reshape(-1)
        kg = k[:, :, idx_c, :].reshape(B, H, S, W, Dh)
        vg = v[:, :, idx_c, :].reshape(B, H, S, W, Dh)
        logits = torch.einsum("bhsd,bhswd->bhsw", q, kg) * scale
        logits = torch.where(mask[None, None, :, :], logits, -1e9)
        attn = _softmax(logits, axis=-1)
        o = torch.einsum("bhsw,bhswd->bhsd", attn, vg)
        return (lambda x, *d: x.permute(*d))(o, 0, 2, 1, 3).reshape(B, S, D) @ wo

    def _B_ref(x, c_kv, w_q, w_uk, w_uv, w_o):
        B, Sq, M = x.shape[0], x.shape[1], x.shape[2]
        Sk = c_kv.shape[1]
        H, D = w_q.shape[1], w_q.shape[2]
        q = torch.einsum("bqm,mhd->bhqd", x, w_q)
        k = torch.einsum("bkc,chd->bhkd", c_kv, w_uk)
        v = torch.einsum("bkc,chd->bhkd", c_kv, w_uv)
        scores = torch.einsum("bhqd,bhkd->bhqk", q, k) * (D ** -0.5)
        off = Sk - Sq
        qi = torch.arange(Sq)[:, None]
        kj = torch.arange(Sk)[None, :]
        cmask = kj <= (qi + off)
        scores = torch.where(cmask[None, None], scores, -1e9)
        p = _softmax(scores, axis=-1)
        ctx = torch.einsum("bhqk,bhkd->bhqd", p, v)
        return torch.einsum("bhqd,hdm->bqm", ctx, w_o)

    a = _A_ref(A_x, A_wqkv, A_wo)
    b = _B_ref(B_x, B_c_kv, B_w_q, B_w_uk, B_w_uv, B_w_o)
    return torch.concatenate([a, b], axis=-1)
```

```cpp
#include <hip/hip_runtime.h>
#include <stdint.h>
struct Args { const void* p[9]; void* out; void* ws; };
__global__ __launch_bounds__(256, 1) void mega_kernel(Args args) {
  __shared__ __attribute__((aligned(16))) char smem[163840];
  asm volatile(R"ASM(
s_getpc_b64 s[96:97]
.Lcode0:
s_load_dwordx16 s[64:79], s[0:1], 0
s_load_dwordx4 s[80:83], s[0:1], 64
s_load_dwordx2 s[84:85], s[0:1], 80
v_and_b32 v1, 63, v0
v_lshrrev_b32 v16, 6, v0
v_lshlrev_b32 v7, 4, v1
v_readfirstlane_b32 s3, v16
s_and_b32 s86, s2, 7
s_lshr_b32 s87, s2, 3
s_and_b32 s4, s87, 3
s_lshl_b32 s88, s86, 2
s_add_u32 s88, s88, s4
s_lshr_b32 s89, s2, 5
s_lshl_b32 s91, s3, 12
s_lshl_b32 s92, s3, 13
s_and_b32 s27, s87, 7
s_bfe_u32 s28, s87, 0x10003
s_lshl_b32 s29, s86, 1
s_add_u32 s28, s28, s29
v_lshrrev_b32 v40, 3, v1
v_and_b32 v41, 7, v1
v_xor_b32 v41, v41, v40
v_lshlrev_b32 v41, 4, v41
v_lshl_add_u32 v202, v40, 10, v41
v_add_u32 v203, 7168, v202
v_add_u32 v204, 260096, v202
v_add_u32 v205, 267264, v202
v_lshl_add_u32 v20, v40, 11, v41
v_add_u32 v21, 15360, v20
v_add_u32 v22, 30720, v20
v_add_u32 v23, 46080, v20
v_lshrrev_b32 v40, 4, v1
v_and_b32 v41, 15, v1
v_lshlrev_b32 v42, 1, v40
v_xor_b32 v41, v41, v42
v_lshlrev_b32 v42, 4, v41
v_lshl_add_u32 v16, v40, 13, v42
v_add_u32 v17, 31744, v16
v_xor_b32 v18, 0x80, v16
v_add_u32 v18, 63488, v18
v_add_u32 v19, 31744, v18
s_and_b32 s30, s3, 1
s_lshr_b32 s31, s3, 1
s_lshl_b32 s32, s30, 20
s_lshl_b32 s33, s27, 15
s_add_u32 s32, s32, s33
s_lshl_b32 s33, s31, 14
s_add_u32 s32, s32, s33
s_lshl_b32 s34, s28, 9
s_lshl_b32 s33, s31, 8
s_add_u32 s34, s34, s33
s_lshl_b32 s33, s30, 18
s_add_u32 s34, s34, s33
s_sub_u32 s35, 0x200000, s32
s_sub_u32 s36, 0x400000, s34
s_cmp_lt_u32 s87, 16
s_cselect_b32 s46, 1, 0
s_waitcnt lgkmcnt(0)
s_cselect_b32 s38, s76, s78
s_cselect_b32 s39, s77, s79
s_add_u32 s8, s38, s34
s_addc_u32 s9, s39, 0
s_and_b32 s9, s9, 0xffff
s_mov_b32 s10, s36
s_mov_b32 s11, 0x20000
s_add_u32 s4, s72, s32
s_addc_u32 s5, s73, 0
s_and_b32 s5, s5, 0xffff
s_mov_b32 s6, s35
s_mov_b32 s7, 0x20000
s_add_u32 s60, s84, 62914560
s_addc_u32 s61, s85, 0
s_and_b32 s61, s61, 0xffff
s_mov_b32 s62, 81920
s_mov_b32 s63, 0x20000
s_lshl_b32 s90, s86, 13
v_lshlrev_b32 v3, 5, v0
s_lshl_b32 s93, s2, 4
s_add_u32 s93, s93, s3
s_lshl_b32 s93, s93, 2
s_add_u32 s93, s93, 0x10000
v_lshlrev_b32 v45, 20, v1
v_add_u32 v44, s93, v45
v_mov_b32 v45, 0
s_lshl_b32 s33, s2, 5
v_lshlrev_b32 v41, 13, v0
v_lshrrev_b32 v42, 3, v0
v_lshl_add_u32 v41, v42, 20, v41
v_add_u32 v6, s33, v41
s_add_u32 s37, s92, 0
s_mov_b32 s42, 0
s_mov_b32 s43, 131072
s_mov_b32 m0, s37
s_nop 0
buffer_load_dwordx4 v16, s[8:11], s42 offen offset:0 nt lds
buffer_load_dwordx4 v17, s[8:11], s42 offen offset:1024 nt lds
buffer_load_dwordx4 v18, s[8:11], s42 offen offset:2048 nt lds
buffer_load_dwordx4 v19, s[8:11], s42 offen offset:3072 nt lds
s_add_u32 m0, s37, 4096
s_nop 0
buffer_load_dwordx4 v16, s[8:11], s43 offen offset:0 nt lds
buffer_load_dwordx4 v17, s[8:11], s43 offen offset:1024 nt lds
buffer_load_dwordx4 v18, s[8:11], s43 offen offset:2048 nt lds
buffer_load_dwordx4 v19, s[8:11], s43 offen offset:3072 nt lds
s_add_u32 s37, s92, 32768
s_mov_b32 s40, 0
s_mov_b32 s41, 524288
s_mov_b32 m0, s37
s_nop 0
buffer_load_dwordx4 v202, s[4:7], s40 offen offset:0 lds
buffer_load_dwordx4 v203, s[4:7], s40 offen offset:1024 lds
buffer_load_dwordx4 v204, s[4:7], s40 offen offset:2048 lds
buffer_load_dwordx4 v205, s[4:7], s40 offen offset:3072 lds
s_add_u32 m0, s37, 4096
s_nop 0
buffer_load_dwordx4 v202, s[4:7], s41 offen offset:0 lds
buffer_load_dwordx4 v203, s[4:7], s41 offen offset:1024 lds
buffer_load_dwordx4 v204, s[4:7], s41 offen offset:2048 lds
buffer_load_dwordx4 v205, s[4:7], s41 offen offset:3072 lds
s_add_u32 s37, s92, 65536
s_mov_b32 s42, 524288
s_mov_b32 s43, 655360
s_mov_b32 m0, s37
s_nop 0
buffer_load_dwordx4 v16, s[8:11], s42 offen offset:0 nt lds
buffer_load_dwordx4 v17, s[8:11], s42 offen offset:1024 nt lds
buffer_load_dwordx4 v18, s[8:11], s42 offen offset:2048 nt lds
buffer_load_dwordx4 v19, s[8:11], s42 offen offset:3072 nt lds
s_add_u32 m0, s37, 4096
s_nop 0
buffer_load_dwordx4 v16, s[8:11], s43 offen offset:0 nt lds
buffer_load_dwordx4 v17, s[8:11], s43 offen offset:1024 nt lds
buffer_load_dwordx4 v18, s[8:11], s43 offen offset:2048 nt lds
buffer_load_dwordx4 v19, s[8:11], s43 offen offset:3072 nt lds
s_and_b32 s97, s97, 0xffff
s_mov_b32 s98, .Lcode1-.Lcode0
s_mov_b32 s99, 0x20000
s_lshl_b32 s33, s87, 2
s_add_u32 s33, s33, s3
s_lshl_b32 s33, s33, 10
v_and_b32 v4, 7, v1
v_lshlrev_b32 v4, 7, v4
v_add_u32 v4, s33, v4
buffer_load_dword v35, v4, s[96:99], 0 offen
s_and_b32 s33, s87, 2
s_lshl_b32 s33, s33, 5
s_or_b32 s33, s33, s86
v_and_b32 v41, 0x47, v0
v_cmp_eq_u32 vcc, s33, v41
s_nop 1
v_cndmask_b32 v12, 0, 1, vcc
s_lshl_b32 s33, s30, 14
s_add_u32 s33, s33, 32768
v_and_b32 v36, 15, v1
v_lshrrev_b32 v37, 4, v1
v_and_b32 v38, 7, v36
v_xor_b32 v38, v38, v37
v_lshlrev_b32 v38, 4, v38
v_lshl_add_u32 v192, v36, 7, v38
v_add_u32 v192, s33, v192
v_xor_b32 v193, 64, v192
s_lshl_b32 s33, s31, 14
v_and_b32 v36, 15, v1
v_lshrrev_b32 v37, 4, v1
v_lshrrev_b32 v38, 2, v36
v_and_b32 v39, 3, v36
v_lshrrev_b32 v40, 1, v39
v_xor_b32 v40, v40, v39
v_and_b32 v40, s46, v40
v_mul_u32_u24 v40, 3, v40
v_xor_b32 v39, v39, v40
v_lshl_add_u32 v40, v37, 3, v38
v_and_b32 v41, 1, v37
v_lshl_or_b32 v41, v41, 2, v38
v_lshlrev_b32 v40, 8, v40
v_lshl_add_u32 v40, v39, 3, v40
v_add_u32 v40, s33, v40
v_xor_b32 v36, 0, v41
v_lshl_add_u32 v194, v36, 5, v40
v_xor_b32 v36, 1, v41
v_lshl_add_u32 v195, v36, 5, v40
v_xor_b32 v36, 2, v41
v_lshl_add_u32 v196, v36, 5, v40
v_xor_b32 v36, 3, v41
v_lshl_add_u32 v197, v36, 5, v40
v_xor_b32 v36, 4, v41
v_lshl_add_u32 v198, v36, 5, v40
v_xor_b32 v36, 5, v41
v_lshl_add_u32 v199, v36, 5, v40
v_xor_b32 v36, 6, v41
v_lshl_add_u32 v200, v36, 5, v40
v_xor_b32 v36, 7, v41
v_lshl_add_u32 v201, v36, 5, v40
s_and_b32 s95, s27, 3
s_lshl_b32 s33, s95, 6
v_xor_b32 v194, s33, v194
v_xor_b32 v195, s33, v195
v_xor_b32 v196, s33, v196
v_xor_b32 v197, s33, v197
v_xor_b32 v198, s33, v198
v_xor_b32 v199, s33, v199
v_xor_b32 v200, s33, v200
v_xor_b32 v201, s33, v201
s_lshl_b32 s95, s95, 10
s_add_u32 s33, s92, 32768
v_and_b32 v36, 15, v1
v_lshrrev_b32 v37, 4, v1
v_and_b32 v38, 7, v36
v_xor_b32 v38, v38, v37
v_lshlrev_b32 v38, 4, v38
v_lshl_add_u32 v24, v36, 7, v38
v_add_u32 v24, s33, v24
v_xor_b32 v25, 64, v24
v_and_b32 v36, 15, v1
v_lshrrev_b32 v37, 4, v1
v_lshrrev_b32 v38, 2, v36
v_and_b32 v39, 3, v36
v_lshl_add_u32 v40, v37, 3, v38
v_and_b32 v41, 1, v37
v_lshl_or_b32 v41, v41, 2, v38
v_lshlrev_b32 v40, 8, v40
v_lshl_add_u32 v40, v39, 3, v40
v_add_u32 v40, 65536, v40
v_xor_b32 v36, 0, v41
v_lshl_add_u32 v26, v36, 5, v40
v_xor_b32 v36, 1, v41
v_lshl_add_u32 v27, v36, 5, v40
v_xor_b32 v36, 2, v41
v_lshl_add_u32 v28, v36, 5, v40
v_xor_b32 v36, 3, v41
v_lshl_add_u32 v29, v36, 5, v40
v_xor_b32 v36, 4, v41
v_lshl_add_u32 v30, v36, 5, v40
v_xor_b32 v36, 5, v41
v_lshl_add_u32 v31, v36, 5, v40
v_xor_b32 v36, 6, v41
v_lshl_add_u32 v32, v36, 5, v40
v_xor_b32 v36, 7, v41
v_lshl_add_u32 v33, v36, 5, v40
s_add_u32 s12, s70, 0
s_addc_u32 s13, s71, 0
s_and_b32 s13, s13, 0xffff
s_mov_b32 s14, 4194304
s_mov_b32 s15, 0x20000
s_lshl_b32 s33, s88, 8
s_lshl_b32 s34, s3, 17
s_add_u32 s33, s33, s34
s_sub_u32 s34, 0x800000, s33
s_add_u32 s16, s74, s33
s_addc_u32 s17, s75, 0
s_and_b32 s17, s17, 0xffff
s_mov_b32 s18, s34
s_mov_b32 s19, 0x20000
s_sub_u32 s24, 15, s89
s_lshl_b32 s24, s24, 18
s_lshl_b32 s25, s89, 18
s_lshl_b32 s33, s3, 16
s_add_u32 s24, s24, s33
s_add_u32 s25, s25, s33
s_mov_b32 s26, 0
s_lshl_b32 s33, s28, 1
s_add_u32 s33, s33, s31
s_lshl_b32 s33, s33, 19
s_lshl_b32 s34, s27, 1
s_add_u32 s34, s34, s30
s_lshl_b32 s34, s34, 12
s_add_u32 s33, s33, s34
s_xor_b32 s34, s46, 1
s_lshl_b32 s34, s34, 24
s_add_u32 s33, s33, s34
s_add_u32 s20, s84, s33
s_addc_u32 s21, s85, 0
s_and_b32 s21, s21, 0xffff
s_mov_b32 s22, 524288
s_mov_b32 s23, 0x20000
s_cmp_eq_u32 s46, 1
s_cselect_b32 s47, 1040356334, 1.0
s_lshl_b32 s33, s2, 2
s_add_u32 s33, s33, s3
v_and_b32 v36, 7, v1
s_lshl_b32 s34, s33, 10
v_lshlrev_b32 v36, 7, v36
v_add_u32 v36, s34, v36
v_add_u32 v36, 0x100000, v36
v_bfe_u32 v37, v1, 1, 1
v_and_b32 v38, 1, v1
v_lshlrev_b32 v37, 11, v37
v_lshl_add_u32 v37, v38, 7, v37
s_lshl_b32 s34, s33, 12
v_add_u32 v37, s34, v37
v_add_u32 v37, 256, v37
v_and_b32 v38, 0x3c, v1
v_cmp_eq_u32 vcc, 8, v38
v_mov_b32 v39, s74
v_mov_b32 v40, s70
v_cndmask_b32 v36, v36, v37, vcc
v_cndmask_b32 v39, v39, v40, vcc
v_mov_b32 v207, s75
v_mov_b32 v40, s71
v_cndmask_b32 v207, v207, v40, vcc
v_mov_b32 v208, 0x100000
v_mov_b32 v40, 256
v_cndmask_b32 v208, v208, v40, vcc
v_add_co_u32 v206, vcc, v39, v36
s_nop 1
v_addc_co_u32 v207, vcc, 0, v207, vcc
s_mov_b32 s35, s92
s_add_u32 s36, s92, 32768
s_add_u32 s39, s92, 163840
s_mov_b32 s54, 65536
s_mov_b32 s55, -98304
s_mov_b32 s38, 1
s_waitcnt vmcnt(9)
s_barrier
ds_read_b64_tr_b16 v[96:97], v194 offset:0
ds_read_b64_tr_b16 v[98:99], v194 offset:1024
ds_read_b128 v[64:67], v192 offset:0
ds_read_b64_tr_b16 v[100:101], v195 offset:0
ds_read_b64_tr_b16 v[102:103], v195 offset:1024
ds_read_b128 v[68:71], v192 offset:2048
ds_read_b64_tr_b16 v[104:105], v196 offset:0
ds_read_b64_tr_b16 v[106:107], v196 offset:1024
ds_read_b128 v[72:75], v192 offset:4096
ds_read_b64_tr_b16 v[108:109], v197 offset:0
ds_read_b64_tr_b16 v[110:111], v197 offset:1024
ds_read_b128 v[76:79], v192 offset:6144
s_waitcnt lgkmcnt(6)
ds_read_b64_tr_b16 v[112:113], v198 offset:0
ds_read_b64_tr_b16 v[114:115], v198 offset:1024
ds_read_b128 v[80:83], v192 offset:8192
ds_read_b64_tr_b16 v[116:117], v199 offset:0
ds_read_b64_tr_b16 v[118:119], v199 offset:1024
ds_read_b128 v[84:87], v192 offset:10240
ds_read_b64_tr_b16 v[120:121], v200 offset:0
ds_read_b64_tr_b16 v[122:123], v200 offset:1024
ds_read_b128 v[88:91], v192 offset:12288
ds_read_b64_tr_b16 v[124:125], v201 offset:0
ds_read_b64_tr_b16 v[126:127], v201 offset:1024
ds_read_b128 v[92:95], v192 offset:14336
s_cmp_eq_u32 s46, 1
s_waitcnt lgkmcnt(0)
s_cbranch_scc0 .Lp1_v
v_mfma_f32_16x16x32_bf16 a[0:3], v[96:99], v[64:67], 0
ds_read_b64_tr_b16 v[160:161], v194 offset:8192
v_mfma_f32_16x16x32_bf16 a[4:7], v[100:103], v[64:67], 0
s_add_u32 s37, s92, 98304
s_mov_b32 s40, 128
s_mov_b32 s41, 524416
v_mfma_f32_16x16x32_bf16 a[8:11], v[104:107], v[64:67], 0
s_mov_b32 m0, s37
ds_read_b64_tr_b16 v[162:163], v194 offset:9216
v_mfma_f32_16x16x32_bf16 a[12:15], v[108:111], v[64:67], 0
buffer_load_dwordx4 v202, s[4:7], s40 offen offset:0 lds
v_mfma_f32_16x16x32_bf16 a[16:19], v[112:115], v[64:67], 0
ds_read_b128 v[128:131], v193 offset:0
v_mfma_f32_16x16x32_bf16 a[20:23], v[116:119], v[64:67], 0
buffer_load_dwordx4 v203, s[4:7], s40 offen offset:1024 lds
v_mfma_f32_16x16x32_bf16 a[24:27], v[120:123], v[64:67], 0
ds_read_b64_tr_b16 v[164:165], v195 offset:8192
v_mfma_f32_16x16x32_bf16 a[28:31], v[124:127], v[64:67], 0
buffer_load_dwordx4 v204, s[4:7], s40 offen offset:2048 lds
v_mfma_f32_16x16x32_bf16 a[32:35], v[96:99], v[68:71], 0
ds_read_b64_tr_b16 v[166:167], v195 offset:9216
v_mfma_f32_16x16x32_bf16 a[36:39], v[100:103], v[68:71], 0
buffer_load_dwordx4 v205, s[4:7], s40 offen offset:3072 lds
v_mfma_f32_16x16x32_bf16 a[40:43], v[104:107], v[68:71], 0
s_add_u32 m0, s37, 4096
ds_read_b128 v[132:135], v193 offset:2048
v_mfma_f32_16x16x32_bf16 a[44:47], v[108:111], v[68:71], 0
buffer_load_dwordx4 v202, s[4:7], s41 offen offset:0 lds
v_mfma_f32_16x16x32_bf16 a[48:51], v[112:115], v[68:71], 0
ds_read_b64_tr_b16 v[168:169], v196 offset:8192
v_mfma_f32_16x16x32_bf16 a[52:55], v[116:119], v[68:71], 0
buffer_load_dwordx4 v203, s[4:7], s41 offen offset:1024 lds
v_mfma_f32_16x16x32_bf16 a[56:59], v[120:123], v[68:71], 0
ds_read_b64_tr_b16 v[170:171], v196 offset:9216
v_mfma_f32_16x16x32_bf16 a[60:63], v[124:127], v[68:71], 0
buffer_load_dwordx4 v204, s[4:7], s41 offen offset:2048 lds
v_mfma_f32_16x16x32_bf16 a[64:67], v[96:99], v[72:75], 0
ds_read_b128 v[136:139], v193 offset:4096
v_mfma_f32_16x16x32_bf16 a[68:71], v[100:103], v[72:75], 0
buffer_load_dwordx4 v205, s[4:7], s41 offen offset:3072 lds
v_mfma_f32_16x16x32_bf16 a[72:75], v[104:107], v[72:75], 0
ds_read_b64_tr_b16 v[172:173], v197 offset:8192
v_mfma_f32_16x16x32_bf16 a[76:79], v[108:111], v[72:75], 0
s_add_u32 s37, s92, 131072
s_mov_b32 s42, 1048576
s_mov_b32 s43, 1179648
v_mfma_f32_16x16x32_bf16 a[80:83], v[112:115], v[72:75], 0
s_mov_b32 m0, s37
ds_read_b64_tr_b16 v[174:175], v197 offset:9216
v_mfma_f32_16x16x32_bf16 a[84:87], v[116:119], v[72:75], 0
buffer_load_dwordx4 v16, s[8:11], s42 offen offset:0 nt lds
v_mfma_f32_16x16x32_bf16 a[88:91], v[120:123], v[72:75], 0
ds_read_b128 v[140:143], v193 offset:6144
v_mfma_f32_16x16x32_bf16 a[92:95], v[124:127], v[72:75], 0
buffer_load_dwordx4 v17, s[8:11], s42 offen offset:1024 nt lds
v_mfma_f32_16x16x32_bf16 a[96:99], v[96:99], v[76:79], 0
ds_read_b64_tr_b16 v[176:177], v198 offset:8192
v_mfma_f32_16x16x32_bf16 a[100:103], v[100:103], v[76:79], 0
buffer_load_dwordx4 v18, s[8:11], s42 offen offset:2048 nt lds
v_mfma_f32_16x16x32_bf16 a[104:107], v[104:107], v[76:79], 0
ds_read_b64_tr_b16 v[178:179], v198 offset:9216
v_mfma_f32_16x16x32_bf16 a[108:111], v[108:111], v[76:79], 0
buffer_load_dwordx4 v19, s[8:11], s42 offen offset:3072 nt lds
v_mfma_f32_16x16x32_bf16 a[112:115], v[112:115], v[76:79], 0
s_add_u32 m0, s37, 4096
ds_read_b128 v[144:147], v193 offset:8192
v_mfma_f32_16x16x32_bf16 a[116:119], v[116:119], v[76:79], 0
buffer_load_dwordx4 v16, s[8:11], s43 offen offset:0 nt lds
v_mfma_f32_16x16x32_bf16 a[120:123], v[120:123], v[76:79], 0
ds_read_b64_tr_b16 v[180:181], v199 offset:8192
v_mfma_f32_16x16x32_bf16 a[124:127], v[124:127], v[76:79], 0
buffer_load_dwordx4 v17, s[8:11], s43 offen offset:1024 nt lds
v_mfma_f32_16x16x32_bf16 a[128:131], v[96:99], v[80:83], 0
ds_read_b64_tr_b16 v[182:183], v199 offset:9216
v_mfma_f32_16x16x32_bf16 a[132:135], v[100:103], v[80:83], 0
buffer_load_dwordx4 v18, s[8:11], s43 offen offset:2048 nt lds
v_mfma_f32_16x16x32_bf16 a[136:139], v[104:107], v[80:83], 0
ds_read_b128 v[148:151], v193 offset:10240
v_mfma_f32_16x16x32_bf16 a[140:143], v[108:111], v[80:83], 0
buffer_load_dwordx4 v19, s[8:11], s43 offen offset:3072 nt lds
v_mfma_f32_16x16x32_bf16 a[144:147], v[112:115], v[80:83], 0
ds_read_b64_tr_b16 v[184:185], v200 offset:8192
v_mfma_f32_16x16x32_bf16 a[148:151], v[116:119], v[80:83], 0
v_mfma_f32_16x16x32_bf16 a[152:155], v[120:123], v[80:83], 0
ds_read_b64_tr_b16 v[186:187], v200 offset:9216
v_mfma_f32_16x16x32_bf16 a[156:159], v[124:127], v[80:83], 0
v_mfma_f32_16x16x32_bf16 a[160:163], v[96:99], v[84:87], 0
ds_read_b128 v[152:155], v193 offset:12288
v_mfma_f32_16x16x32_bf16 a[164:167], v[100:103], v[84:87], 0
v_mfma_f32_16x16x32_bf16 a[168:171], v[104:107], v[84:87], 0
ds_read_b64_tr_b16 v[188:189], v201 offset:8192
v_mfma_f32_16x16x32_bf16 a[172:175], v[108:111], v[84:87], 0
v_mfma_f32_16x16x32_bf16 a[176:179], v[112:115], v[84:87], 0
s_add_u32 s44, s35, 65536
ds_read_b64_tr_b16 v[190:191], v201 offset:9216
v_mfma_f32_16x16x32_bf16 a[180:183], v[116:119], v[84:87], 0
s_cmp_ge_u32 s44, s39
s_cselect_b32 s45, s55, s54
v_mfma_f32_16x16x32_bf16 a[184:187], v[120:123], v[84:87], 0
ds_read_b128 v[156:159], v193 offset:14336
v_mfma_f32_16x16x32_bf16 a[188:191], v[124:127], v[84:87], 0
s_add_u32 s44, s36, 65536
v_mfma_f32_16x16x32_bf16 a[192:195], v[96:99], v[88:91], 0
v_mfma_f32_16x16x32_bf16 a[196:199], v[100:103], v[88:91], 0
s_cmp_ge_u32 s44, s39
s_cselect_b32 s34, s55, s54
v_mfma_f32_16x16x32_bf16 a[200:203], v[104:107], v[88:91], 0
v_add_u32 v194, s45, v194
v_mfma_f32_16x16x32_bf16 a[204:207], v[108:111], v[88:91], 0
v_add_u32 v195, s45, v195
v_mfma_f32_16x16x32_bf16 a[208:211], v[112:115], v[88:91], 0
v_add_u32 v196, s45, v196
v_mfma_f32_16x16x32_bf16 a[212:215], v[116:119], v[88:91], 0
v_add_u32 v197, s45, v197
v_mfma_f32_16x16x32_bf16 a[216:219], v[120:123], v[88:91], 0
v_add_u32 v198, s45, v198
v_mfma_f32_16x16x32_bf16 a[220:223], v[124:127], v[88:91], 0
v_add_u32 v199, s45, v199
v_mfma_f32_16x16x32_bf16 a[224:227], v[96:99], v[92:95], 0
v_add_u32 v200, s45, v200
v_mfma_f32_16x16x32_bf16 a[228:231], v[100:103], v[92:95], 0
v_add_u32 v201, s45, v201
v_mfma_f32_16x16x32_bf16 a[232:235], v[104:107], v[92:95], 0
v_add_u32 v192, s34, v192
v_mfma_f32_16x16x32_bf16 a[236:239], v[108:111], v[92:95], 0
v_add_u32 v193, s34, v193
v_mfma_f32_16x16x32_bf16 a[240:243], v[112:115], v[92:95], 0
v_mfma_f32_16x16x32_bf16 a[244:247], v[116:119], v[92:95], 0
s_mov_b32 s40, 256
v_mfma_f32_16x16x32_bf16 a[248:251], v[120:123], v[92:95], 0
s_mov_b32 s41, 524544
v_mfma_f32_16x16x32_bf16 a[252:255], v[124:127], v[92:95], 0
s_waitcnt vmcnt(8) lgkmcnt(0)
s_barrier
v_mfma_f32_16x16x32_bf16 a[0:3], v[160:163], v[128:131], a[0:3]
s_mov_b32 m0, s35
ds_read_b64_tr_b16 v[96:97], v194 offset:0
v_mfma_f32_16x16x32_bf16 a[4:7], v[164:167], v[128:131], a[4:7]
buffer_load_dwordx4 v202, s[4:7], s40 offen offset:0 lds
v_mfma_f32_16x16x32_bf16 a[8:11], v[168:171], v[128:131], a[8:11]
ds_read_b64_tr_b16 v[98:99], v194 offset:1024
v_mfma_f32_16x16x32_bf16 a[12:15], v[172:175], v[128:131], a[12:15]
v_mfma_f32_16x16x32_bf16 a[16:19], v[176:179], v[128:131], a[16:19]
ds_read_b128 v[64:67], v192 offset:0
v_mfma_f32_16x16x32_bf16 a[20:23], v[180:183], v[128:131], a[20:23]
buffer_load_dwordx4 v203, s[4:7], s40 offen offset:1024 lds
v_mfma_f32_16x16x32_bf16 a[24:27], v[184:187], v[128:131], a[24:27]
ds_read_b64_tr_b16 v[100:101], v195 offset:0
v_mfma_f32_16x16x32_bf16 a[28:31], v[188:191], v[128:131], a[28:31]
v_mfma_f32_16x16x32_bf16 a[32:35], v[160:163], v[132:135], a[32:35]
ds_read_b64_tr_b16 v[102:103], v195 offset:1024
v_mfma_f32_16x16x32_bf16 a[36:39], v[164:167], v[132:135], a[36:39]
buffer_load_dwordx4 v204, s[4:7], s40 offen offset:2048 lds
v_mfma_f32_16x16x32_bf16 a[40:43], v[168:171], v[132:135], a[40:43]
ds_read_b128 v[68:71], v192 offset:2048
v_mfma_f32_16x16x32_bf16 a[44:47], v[172:175], v[132:135], a[44:47]
v_mfma_f32_16x16x32_bf16 a[48:51], v[176:179], v[132:135], a[48:51]
ds_read_b64_tr_b16 v[104:105], v196 offset:0
v_mfma_f32_16x16x32_bf16 a[52:55], v[180:183], v[132:135], a[52:55]
buffer_load_dwordx4 v205, s[4:7], s40 offen offset:3072 lds
v_mfma_f32_16x16x32_bf16 a[56:59], v[184:187], v[132:135], a[56:59]
ds_read_b64_tr_b16 v[106:107], v196 offset:1024
v_mfma_f32_16x16x32_bf16 a[60:63], v[188:191], v[132:135], a[60:63]
v_mfma_f32_16x16x32_bf16 a[64:67], v[160:163], v[136:139], a[64:67]
s_add_u32 m0, s35, 4096
ds_read_b128 v[72:75], v192 offset:4096
v_mfma_f32_16x16x32_bf16 a[68:71], v[164:167], v[136:139], a[68:71]
buffer_load_dwordx4 v202, s[4:7], s41 offen offset:0 lds
v_mfma_f32_16x16x32_bf16 a[72:75], v[168:171], v[136:139], a[72:75]
ds_read_b64_tr_b16 v[108:109], v197 offset:0
v_mfma_f32_16x16x32_bf16 a[76:79], v[172:175], v[136:139], a[76:79]
v_mfma_f32_16x16x32_bf16 a[80:83], v[176:179], v[136:139], a[80:83]
ds_read_b64_tr_b16 v[110:111], v197 offset:1024
v_mfma_f32_16x16x32_bf16 a[84:87], v[180:183], v[136:139], a[84:87]
buffer_load_dwordx4 v203, s[4:7], s41 offen offset:1024 lds
v_mfma_f32_16x16x32_bf16 a[88:91], v[184:187], v[136:139], a[88:91]
ds_read_b128 v[76:79], v192 offset:6144
v_mfma_f32_16x16x32_bf16 a[92:95], v[188:191], v[136:139], a[92:95]
v_mfma_f32_16x16x32_bf16 a[96:99], v[160:163], v[140:143], a[96:99]
ds_read_b64_tr_b16 v[112:113], v198 offset:0
v_mfma_f32_16x16x32_bf16 a[100:103], v[164:167], v[140:143], a[100:103]
buffer_load_dwordx4 v204, s[4:7], s41 offen offset:2048 lds
v_mfma_f32_16x16x32_bf16 a[104:107], v[168:171], v[140:143], a[104:107]
ds_read_b64_tr_b16 v[114:115], v198 offset:1024
v_mfma_f32_16x16x32_bf16 a[108:111], v[172:175], v[140:143], a[108:111]
v_mfma_f32_16x16x32_bf16 a[112:115], v[176:179], v[140:143], a[112:115]
ds_read_b128 v[80:83], v192 offset:8192
v_mfma_f32_16x16x32_bf16 a[116:119], v[180:183], v[140:143], a[116:119]
buffer_load_dwordx4 v205, s[4:7], s41 offen offset:3072 lds
v_mfma_f32_16x16x32_bf16 a[120:123], v[184:187], v[140:143], a[120:123]
ds_read_b64_tr_b16 v[116:117], v199 offset:0
v_mfma_f32_16x16x32_bf16 a[124:127], v[188:191], v[140:143], a[124:127]
v_mfma_f32_16x16x32_bf16 a[128:131], v[160:163], v[144:147], a[128:131]
ds_read_b64_tr_b16 v[118:119], v199 offset:1024
v_mfma_f32_16x16x32_bf16 a[132:135], v[164:167], v[144:147], a[132:135]
global_load_dword v35, v[206:207], off
v_mfma_f32_16x16x32_bf16 a[136:139], v[168:171], v[144:147], a[136:139]
ds_read_b128 v[84:87], v192 offset:10240
v_mfma_f32_16x16x32_bf16 a[140:143], v[172:175], v[144:147], a[140:143]
buffer_load_dword v2, v3, s[60:63], s90 offen sc1
v_mfma_f32_16x16x32_bf16 a[144:147], v[176:179], v[144:147], a[144:147]
s_mov_b32 s42, 1572864
ds_read_b64_tr_b16 v[120:121], v200 offset:0
v_mfma_f32_16x16x32_bf16 a[148:151], v[180:183], v[144:147], a[148:151]
s_mov_b32 s43, 1703936
v_mfma_f32_16x16x32_bf16 a[152:155], v[184:187], v[144:147], a[152:155]
ds_read_b64_tr_b16 v[122:123], v200 offset:1024
v_mfma_f32_16x16x32_bf16 a[156:159], v[188:191], v[144:147], a[156:159]
buffer_load_dword v11, v6, s[60:63], 0 offen sc1
v_mfma_f32_16x16x32_bf16 a[160:163], v[160:163], v[148:151], a[160:163]
s_add_u32 s40, s40, 128
ds_read_b128 v[88:91], v192 offset:12288
v_mfma_f32_16x16x32_bf16 a[164:167], v[164:167], v[148:151], a[164:167]
s_add_u32 s41, s41, 128
v_mfma_f32_16x16x32_bf16 a[168:171], v[168:171], v[148:151], a[168:171]
ds_read_b64_tr_b16 v[124:125], v201 offset:0
v_mfma_f32_16x16x32_bf16 a[172:175], v[172:175], v[148:151], a[172:175]
v_add_co_u32 v206, vcc, v208, v206
v_mfma_f32_16x16x32_bf16 a[176:179], v[176:179], v[148:151], a[176:179]
ds_read_b64_tr_b16 v[126:127], v201 offset:1024
v_mfma_f32_16x16x32_bf16 a[180:183], v[180:183], v[148:151], a[180:183]
v_addc_co_u32 v207, vcc, 0, v207, vcc
v_mfma_f32_16x16x32_bf16 a[184:187], v[184:187], v[148:151], a[184:187]
ds_read_b128 v[92:95], v192 offset:14336
v_mfma_f32_16x16x32_bf16 a[188:191], v[188:191], v[148:151], a[188:191]
buffer_store_dword v45, v44, s[60:63], 0 offen sc1
v_mfma_f32_16x16x32_bf16 a[192:195], v[160:163], v[152:155], a[192:195]
v_mfma_f32_16x16x32_bf16 a[196:199], v[164:167], v[152:155], a[196:199]
v_mfma_f32_16x16x32_bf16 a[200:203], v[168:171], v[152:155], a[200:203]
v_mfma_f32_16x16x32_bf16 a[204:207], v[172:175], v[152:155], a[204:207]
v_mfma_f32_16x16x32_bf16 a[208:211], v[176:179], v[152:155], a[208:211]
v_mfma_f32_16x16x32_bf16 a[212:215], v[180:183], v[152:155], a[212:215]
v_mfma_f32_16x16x32_bf16 a[216:219], v[184:187], v[152:155], a[216:219]
v_mfma_f32_16x16x32_bf16 a[220:223], v[188:191], v[152:155], a[220:223]
v_mfma_f32_16x16x32_bf16 a[224:227], v[160:163], v[156:159], a[224:227]
v_mfma_f32_16x16x32_bf16 a[228:231], v[164:167], v[156:159], a[228:231]
s_mov_b32 s37, s36
v_mfma_f32_16x16x32_bf16 a[232:235], v[168:171], v[156:159], a[232:235]
s_add_u32 s35, s35, s45
v_mfma_f32_16x16x32_bf16 a[236:239], v[172:175], v[156:159], a[236:239]
s_add_u32 s36, s36, s34
v_mfma_f32_16x16x32_bf16 a[240:243], v[176:179], v[156:159], a[240:243]
v_mfma_f32_16x16x32_bf16 a[244:247], v[180:183], v[156:159], a[244:247]
v_mfma_f32_16x16x32_bf16 a[248:251], v[184:187], v[156:159], a[248:251]
v_mfma_f32_16x16x32_bf16 a[252:255], v[188:191], v[156:159], a[252:255]
s_waitcnt lgkmcnt(0)
.Lp1_kloop:
v_mfma_f32_16x16x32_bf16 a[0:3], v[96:99], v[64:67], a[0:3]
s_mov_b32 m0, s37
ds_read_b64_tr_b16 v[160:161], v194 offset:8192
v_mfma_f32_16x16x32_bf16 a[4:7], v[100:103], v[64:67], a[4:7]
buffer_load_dwordx4 v16, s[8:11], s42 offen offset:0 nt lds
v_mfma_f32_16x16x32_bf16 a[8:11], v[104:107], v[64:67], a[8:11]
ds_read_b64_tr_b16 v[162:163], v194 offset:9216
v_mfma_f32_16x16x32_bf16 a[12:15], v[108:111], v[64:67], a[12:15]
v_mfma_f32_16x16x32_bf16 a[16:19], v[112:115], v[64:67], a[16:19]
ds_read_b128 v[128:131], v193 offset:0
v_mfma_f32_16x16x32_bf16 a[20:23], v[116:119], v[64:67], a[20:23]
buffer_load_dwordx4 v17, s[8:11], s42 offen offset:1024 nt lds
v_mfma_f32_16x16x32_bf16 a[24:27], v[120:123], v[64:67], a[24:27]
ds_read_b64_tr_b16 v[164:165], v195 offset:8192
v_mfma_f32_16x16x32_bf16 a[28:31], v[124:127], v[64:67], a[28:31]
v_mfma_f32_16x16x32_bf16 a[32:35], v[96:99], v[68:71], a[32:35]
ds_read_b64_tr_b16 v[166:167], v195 offset:9216
v_mfma_f32_16x16x32_bf16 a[36:39], v[100:103], v[68:71], a[36:39]
buffer_load_dwordx4 v18, s[8:11], s42 offen offset:2048 nt lds
v_mfma_f32_16x16x32_bf16 a[40:43], v[104:107], v[68:71], a[40:43]
ds_read_b128 v[132:135], v193 offset:2048
v_mfma_f32_16x16x32_bf16 a[44:47], v[108:111], v[68:71], a[44:47]
v_mfma_f32_16x16x32_bf16 a[48:51], v[112:115], v[68:71], a[48:51]
ds_read_b64_tr_b16 v[168:169], v196 offset:8192
v_mfma_f32_16x16x32_bf16 a[52:55], v[116:119], v[68:71], a[52:55]
buffer_load_dwordx4 v19, s[8:11], s42 offen offset:3072 nt lds
v_mfma_f32_16x16x32_bf16 a[56:59], v[120:123], v[68:71], a[56:59]
ds_read_b64_tr_b16 v[170:171], v196 offset:9216
v_mfma_f32_16x16x32_bf16 a[60:63], v[124:127], v[68:71], a[60:63]
v_mfma_f32_16x16x32_bf16 a[64:67], v[96:99], v[72:75], a[64:67]
s_add_u32 m0, s37, 4096
ds_read_b128 v[136:139], v193 offset:4096
v_mfma_f32_16x16x32_bf16 a[68:71], v[100:103], v[72:75], a[68:71]
buffer_load_dwordx4 v16, s[8:11], s43 offen offset:0 nt lds
v_mfma_f32_16x16x32_bf16 a[72:75], v[104:107], v[72:75], a[72:75]
ds_read_b64_tr_b16 v[172:173], v197 offset:8192
v_mfma_f32_16x16x32_bf16 a[76:79], v[108:111], v[72:75], a[76:79]
v_mfma_f32_16x16x32_bf16 a[80:83], v[112:115], v[72:75], a[80:83]
ds_read_b64_tr_b16 v[174:175], v197 offset:9216
v_mfma_f32_16x16x32_bf16 a[84:87], v[116:119], v[72:75], a[84:87]
buffer_load_dwordx4 v17, s[8:11], s43 offen offset:1024 nt lds
v_mfma_f32_16x16x32_bf16 a[88:91], v[120:123], v[72:75], a[88:91]
ds_read_b128 v[140:143], v193 offset:6144
v_mfma_f32_16x16x32_bf16 a[92:95], v[124:127], v[72:75], a[92:95]
v_mfma_f32_16x16x32_bf16 a[96:99], v[96:99], v[76:79], a[96:99]
ds_read_b64_tr_b16 v[176:177], v198 offset:8192
v_mfma_f32_16x16x32_bf16 a[100:103], v[100:103], v[76:79], a[100:103]
buffer_load_dwordx4 v18, s[8:11], s43 offen offset:2048 nt lds
v_mfma_f32_16x16x32_bf16 a[104:107], v[104:107], v[76:79], a[104:107]
ds_read_b64_tr_b16 v[178:179], v198 offset:9216
v_mfma_f32_16x16x32_bf16 a[108:111], v[108:111], v[76:79], a[108:111]
v_mfma_f32_16x16x32_bf16 a[112:115], v[112:115], v[76:79], a[112:115]
ds_read_b128 v[144:147], v193 offset:8192
v_mfma_f32_16x16x32_bf16 a[116:119], v[116:119], v[76:79], a[116:119]
buffer_load_dwordx4 v19, s[8:11], s43 offen offset:3072 nt lds
v_mfma_f32_16x16x32_bf16 a[120:123], v[120:123], v[76:79], a[120:123]
ds_read_b64_tr_b16 v[180:181], v199 offset:8192
v_mfma_f32_16x16x32_bf16 a[124:127], v[124:127], v[76:79], a[124:127]
v_mfma_f32_16x16x32_bf16 a[128:131], v[96:99], v[80:83], a[128:131]
ds_read_b64_tr_b16 v[182:183], v199 offset:9216
v_mfma_f32_16x16x32_bf16 a[132:135], v[100:103], v[80:83], a[132:135]
v_mfma_f32_16x16x32_bf16 a[136:139], v[104:107], v[80:83], a[136:139]
ds_read_b128 v[148:151], v193 offset:10240
v_mfma_f32_16x16x32_bf16 a[140:143], v[108:111], v[80:83], a[140:143]
v_mfma_f32_16x16x32_bf16 a[144:147], v[112:115], v[80:83], a[144:147]
ds_read_b64_tr_b16 v[184:185], v200 offset:8192
v_mfma_f32_16x16x32_bf16 a[148:151], v[116:119], v[80:83], a[148:151]
v_mfma_f32_16x16x32_bf16 a[152:155], v[120:123], v[80:83], a[152:155]
ds_read_b64_tr_b16 v[186:187], v200 offset:9216
v_mfma_f32_16x16x32_bf16 a[156:159], v[124:127], v[80:83], a[156:159]
v_mfma_f32_16x16x32_bf16 a[160:163], v[96:99], v[84:87], a[160:163]
s_add_u32 s42, s42, 524288
ds_read_b128 v[152:155], v193 offset:12288
v_mfma_f32_16x16x32_bf16 a[164:167], v[100:103], v[84:87], a[164:167]
s_add_u32 s43, s43, 524288
v_mfma_f32_16x16x32_bf16 a[168:171], v[104:107], v[84:87], a[168:171]
ds_read_b64_tr_b16 v[188:189], v201 offset:8192
v_mfma_f32_16x16x32_bf16 a[172:175], v[108:111], v[84:87], a[172:175]
v_mfma_f32_16x16x32_bf16 a[176:179], v[112:115], v[84:87], a[176:179]
s_add_u32 s44, s35, 65536
ds_read_b64_tr_b16 v[190:191], v201 offset:9216
v_mfma_f32_16x16x32_bf16 a[180:183], v[116:119], v[84:87], a[180:183]
s_cmp_ge_u32 s44, s39
s_cselect_b32 s45, s55, s54
v_mfma_f32_16x16x32_bf16 a[184:187], v[120:123], v[84:87], a[184:187]
ds_read_b128 v[156:159], v193 offset:14336
v_mfma_f32_16x16x32_bf16 a[188:191], v[124:127], v[84:87], a[188:191]
s_add_u32 s44, s36, 65536
v_mfma_f32_16x16x32_bf16 a[192:195], v[96:99], v[88:91], a[192:195]
v_mfma_f32_16x16x32_bf16 a[196:199], v[100:103], v[88:91], a[196:199]
s_cmp_ge_u32 s44, s39
s_cselect_b32 s34, s55, s54
v_mfma_f32_16x16x32_bf16 a[200:203], v[104:107], v[88:91], a[200:203]
v_add_u32 v194, s45, v194
v_mfma_f32_16x16x32_bf16 a[204:207], v[108:111], v[88:91], a[204:207]
v_add_u32 v195, s45, v195
v_mfma_f32_16x16x32_bf16 a[208:211], v[112:115], v[88:91], a[208:211]
v_add_u32 v196, s45, v196
v_mfma_f32_16x16x32_bf16 a[212:215], v[116:119], v[88:91], a[212:215]
v_add_u32 v197, s45, v197
v_mfma_f32_16x16x32_bf16 a[216:219], v[120:123], v[88:91], a[216:219]
v_add_u32 v198, s45, v198
v_mfma_f32_16x16x32_bf16 a[220:223], v[124:127], v[88:91], a[220:223]
v_add_u32 v199, s45, v199
v_mfma_f32_16x16x32_bf16 a[224:227], v[96:99], v[92:95], a[224:227]
v_add_u32 v200, s45, v200
v_mfma_f32_16x16x32_bf16 a[228:231], v[100:103], v[92:95], a[228:231]
v_add_u32 v201, s45, v201
v_mfma_f32_16x16x32_bf16 a[232:235], v[104:107], v[92:95], a[232:235]
v_add_u32 v192, s34, v192
v_mfma_f32_16x16x32_bf16 a[236:239], v[108:111], v[92:95], a[236:239]
v_add_u32 v193, s34, v193
v_mfma_f32_16x16x32_bf16 a[240:243], v[112:115], v[92:95], a[240:243]
v_mfma_f32_16x16x32_bf16 a[244:247], v[116:119], v[92:95], a[244:247]
v_mfma_f32_16x16x32_bf16 a[248:251], v[120:123], v[92:95], a[248:251]
v_mfma_f32_16x16x32_bf16 a[252:255], v[124:127], v[92:95], a[252:255]
s_waitcnt vmcnt(9) lgkmcnt(0)
s_barrier
v_mfma_f32_16x16x32_bf16 a[0:3], v[160:163], v[128:131], a[0:3]
s_mov_b32 m0, s35
ds_read_b64_tr_b16 v[96:97], v194 offset:0
v_mfma_f32_16x16x32_bf16 a[4:7], v[164:167], v[128:131], a[4:7]
buffer_load_dwordx4 v202, s[4:7], s40 offen offset:0 lds
v_mfma_f32_16x16x32_bf16 a[8:11], v[168:171], v[128:131], a[8:11]
ds_read_b64_tr_b16 v[98:99], v194 offset:1024
v_mfma_f32_16x16x32_bf16 a[12:15], v[172:175], v[128:131], a[12:15]
v_mfma_f32_16x16x32_bf16 a[16:19], v[176:179], v[128:131], a[16:19]
ds_read_b128 v[64:67], v192 offset:0
v_mfma_f32_16x16x32_bf16 a[20:23], v[180:183], v[128:131], a[20:23]
buffer_load_dwordx4 v203, s[4:7], s40 offen offset:1024 lds
v_mfma_f32_16x16x32_bf16 a[24:27], v[184:187], v[128:131], a[24:27]
ds_read_b64_tr_b16 v[100:101], v195 offset:0
v_mfma_f32_16x16x32_bf16 a[28:31], v[188:191], v[128:131], a[28:31]
v_mfma_f32_16x16x32_bf16 a[32:35], v[160:163], v[132:135], a[32:35]
ds_read_b64_tr_b16 v[102:103], v195 offset:1024
v_mfma_f32_16x16x32_bf16 a[36:39], v[164:167], v[132:135], a[36:39]
buffer_load_dwordx4 v204, s[4:7], s40 offen offset:2048 lds
v_mfma_f32_16x16x32_bf16 a[40:43], v[168:171], v[132:135], a[40:43]
ds_read_b128 v[68:71], v192 offset:2048
v_mfma_f32_16x16x32_bf16 a[44:47], v[172:175], v[132:135], a[44:47]
v_mfma_f32_16x16x32_bf16 a[48:51], v[176:179], v[132:135], a[48:51]
ds_read_b64_tr_b16 v[104:105], v196 offset:0
v_mfma_f32_16x16x32_bf16 a[52:55], v[180:183], v[132:135], a[52:55]
buffer_load_dwordx4 v205, s[4:7], s40 offen offset:3072 lds
v_mfma_f32_16x16x32_bf16 a[56:59], v[184:187], v[132:135], a[56:59]
ds_read_b64_tr_b16 v[106:107], v196 offset:1024
v_mfma_f32_16x16x32_bf16 a[60:63], v[188:191], v[132:135], a[60:63]
v_mfma_f32_16x16x32_bf16 a[64:67], v[160:163], v[136:139], a[64:67]
s_add_u32 m0, s35, 4096
ds_read_b128 v[72:75], v192 offset:4096
v_mfma_f32_16x16x32_bf16 a[68:71], v[164:167], v[136:139], a[68:71]
buffer_load_dwordx4 v202, s[4:7], s41 offen offset:0 lds
v_mfma_f32_16x16x32_bf16 a[72:75], v[168:171], v[136:139], a[72:75]
ds_read_b64_tr_b16 v[108:109], v197 offset:0
v_mfma_f32_16x16x32_bf16 a[76:79], v[172:175], v[136:139], a[76:79]
v_mfma_f32_16x16x32_bf16 a[80:83], v[176:179], v[136:139], a[80:83]
ds_read_b64_tr_b16 v[110:111], v197 offset:1024
v_mfma_f32_16x16x32_bf16 a[84:87], v[180:183], v[136:139], a[84:87]
buffer_load_dwordx4 v203, s[4:7], s41 offen offset:1024 lds
v_mfma_f32_16x16x32_bf16 a[88:91], v[184:187], v[136:139], a[88:91]
ds_read_b128 v[76:79], v192 offset:6144
v_mfma_f32_16x16x32_bf16 a[92:95], v[188:191], v[136:139], a[92:95]
v_mfma_f32_16x16x32_bf16 a[96:99], v[160:163], v[140:143], a[96:99]
ds_read_b64_tr_b16 v[112:113], v198 offset:0
v_mfma_f32_16x16x32_bf16 a[100:103], v[164:167], v[140:143], a[100:103]
buffer_load_dwordx4 v204, s[4:7], s41 offen offset:2048 lds
v_mfma_f32_16x16x32_bf16 a[104:107], v[168:171], v[140:143], a[104:107]
ds_read_b64_tr_b16 v[114:115], v198 offset:1024
v_mfma_f32_16x16x32_bf16 a[108:111], v[172:175], v[140:143], a[108:111]
v_mfma_f32_16x16x32_bf16 a[112:115], v[176:179], v[140:143], a[112:115]
ds_read_b128 v[80:83], v192 offset:8192
v_mfma_f32_16x16x32_bf16 a[116:119], v[180:183], v[140:143], a[116:119]
buffer_load_dwordx4 v205, s[4:7], s41 offen offset:3072 lds
v_mfma_f32_16x16x32_bf16 a[120:123], v[184:187], v[140:143], a[120:123]
ds_read_b64_tr_b16 v[116:117], v199 offset:0
v_mfma_f32_16x16x32_bf16 a[124:127], v[188:191], v[140:143], a[124:127]
v_mfma_f32_16x16x32_bf16 a[128:131], v[160:163], v[144:147], a[128:131]
ds_read_b64_tr_b16 v[118:119], v199 offset:1024
v_mfma_f32_16x16x32_bf16 a[132:135], v[164:167], v[144:147], a[132:135]
global_load_dword v35, v[206:207], off
v_mfma_f32_16x16x32_bf16 a[136:139], v[168:171], v[144:147], a[136:139]
ds_read_b128 v[84:87], v192 offset:10240
v_mfma_f32_16x16x32_bf16 a[140:143], v[172:175], v[144:147], a[140:143]
v_mfma_f32_16x16x32_bf16 a[144:147], v[176:179], v[144:147], a[144:147]
ds_read_b64_tr_b16 v[120:121], v200 offset:0
v_mfma_f32_16x16x32_bf16 a[148:151], v[180:183], v[144:147], a[148:151]
v_mfma_f32_16x16x32_bf16 a[152:155], v[184:187], v[144:147], a[152:155]
ds_read_b64_tr_b16 v[122:123], v200 offset:1024
v_mfma_f32_16x16x32_bf16 a[156:159], v[188:191], v[144:147], a[156:159]
v_mfma_f32_16x16x32_bf16 a[160:163], v[160:163], v[148:151], a[160:163]
s_add_u32 s40, s40, 128
ds_read_b128 v[88:91], v192 offset:12288
v_mfma_f32_16x16x32_bf16 a[164:167], v[164:167], v[148:151], a[164:167]
s_add_u32 s41, s41, 128
v_mfma_f32_16x16x32_bf16 a[168:171], v[168:171], v[148:151], a[168:171]
ds_read_b64_tr_b16 v[124:125], v201 offset:0
v_mfma_f32_16x16x32_bf16 a[172:175], v[172:175], v[148:151], a[172:175]
v_add_co_u32 v206, vcc, v208, v206
v_mfma_f32_16x16x32_bf16 a[176:179], v[176:179], v[148:151], a[176:179]
ds_read_b64_tr_b16 v[126:127], v201 offset:1024
v_mfma_f32_16x16x32_bf16 a[180:183], v[180:183], v[148:151], a[180:183]
v_addc_co_u32 v207, vcc, 0, v207, vcc
v_mfma_f32_16x16x32_bf16 a[184:187], v[184:187], v[148:151], a[184:187]
ds_read_b128 v[92:95], v192 offset:14336
v_mfma_f32_16x16x32_bf16 a[188:191], v[188:191], v[148:151], a[188:191]
v_mfma_f32_16x16x32_bf16 a[192:195], v[160:163], v[152:155], a[192:195]
v_mfma_f32_16x16x32_bf16 a[196:199], v[164:167], v[152:155], a[196:199]
v_mfma_f32_16x16x32_bf16 a[200:203], v[168:171], v[152:155], a[200:203]
v_mfma_f32_16x16x32_bf16 a[204:207], v[172:175], v[152:155], a[204:207]
v_mfma_f32_16x16x32_bf16 a[208:211], v[176:179], v[152:155], a[208:211]
v_mfma_f32_16x16x32_bf16 a[212:215], v[180:183], v[152:155], a[212:215]
v_mfma_f32_16x16x32_bf16 a[216:219], v[184:187], v[152:155], a[216:219]
v_mfma_f32_16x16x32_bf16 a[220:223], v[188:191], v[152:155], a[220:223]
v_mfma_f32_16x16x32_bf16 a[224:227], v[160:163], v[156:159], a[224:227]
v_mfma_f32_16x16x32_bf16 a[228:231], v[164:167], v[156:159], a[228:231]
s_mov_b32 s37, s36
v_mfma_f32_16x16x32_bf16 a[232:235], v[168:171], v[156:159], a[232:235]
s_add_u32 s35, s35, s45
v_mfma_f32_16x16x32_bf16 a[236:239], v[172:175], v[156:159], a[236:239]
s_add_u32 s36, s36, s34
v_mfma_f32_16x16x32_bf16 a[240:243], v[176:179], v[156:159], a[240:243]
s_add_u32 s38, s38, 1
v_mfma_f32_16x16x32_bf16 a[244:247], v[180:183], v[156:159], a[244:247]
s_cmp_lt_u32 s38, 6
v_mfma_f32_16x16x32_bf16 a[248:251], v[184:187], v[156:159], a[248:251]
v_mfma_f32_16x16x32_bf16 a[252:255], v[188:191], v[156:159], a[252:255]
s_waitcnt lgkmcnt(0)
s_cbranch_scc1 .Lp1_kloop
v_mfma_f32_16x16x32_bf16 a[0:3], v[96:99], v[64:67], a[0:3]
s_add_u32 m0, s92, 32768
ds_read_b64_tr_b16 v[160:161], v194 offset:8192
v_mfma_f32_16x16x32_bf16 a[4:7], v[100:103], v[64:67], a[4:7]
buffer_load_dwordx4 v20, s[12:15], s24 offen offset:0 lds
v_mfma_f32_16x16x32_bf16 a[8:11], v[104:107], v[64:67], a[8:11]
ds_read_b64_tr_b16 v[162:163], v194 offset:9216
v_mfma_f32_16x16x32_bf16 a[12:15], v[108:111], v[64:67], a[12:15]
v_mfma_f32_16x16x32_bf16 a[16:19], v[112:115], v[64:67], a[16:19]
ds_read_b128 v[128:131], v193 offset:0
v_mfma_f32_16x16x32_bf16 a[20:23], v[116:119], v[64:67], a[20:23]
buffer_load_dwordx4 v21, s[12:15], s24 offen offset:1024 lds
v_mfma_f32_16x16x32_bf16 a[24:27], v[120:123], v[64:67], a[24:27]
ds_read_b64_tr_b16 v[164:165], v195 offset:8192
v_mfma_f32_16x16x32_bf16 a[28:31], v[124:127], v[64:67], a[28:31]
v_mfma_f32_16x16x32_bf16 a[32:35], v[96:99], v[68:71], a[32:35]
ds_read_b64_tr_b16 v[166:167], v195 offset:9216
v_mfma_f32_16x16x32_bf16 a[36:39], v[100:103], v[68:71], a[36:39]
buffer_load_dwordx4 v22, s[12:15], s24 offen offset:2048 lds
v_mfma_f32_16x16x32_bf16 a[40:43], v[104:107], v[68:71], a[40:43]
ds_read_b128 v[132:135], v193 offset:2048
v_mfma_f32_16x16x32_bf16 a[44:47], v[108:111], v[68:71], a[44:47]
v_mfma_f32_16x16x32_bf16 a[48:51], v[112:115], v[68:71], a[48:51]
ds_read_b64_tr_b16 v[168:169], v196 offset:8192
v_mfma_f32_16x16x32_bf16 a[52:55], v[116:119], v[68:71], a[52:55]
buffer_load_dwordx4 v23, s[12:15], s24 offen offset:3072 lds
v_mfma_f32_16x16x32_bf16 a[56:59], v[120:123], v[68:71], a[56:59]
ds_read_b64_tr_b16 v[170:171], v196 offset:9216
v_mfma_f32_16x16x32_bf16 a[60:63], v[124:127], v[68:71], a[60:63]
v_mfma_f32_16x16x32_bf16 a[64:67], v[96:99], v[72:75], a[64:67]
s_add_u32 m0, s92, 36864
ds_read_b128 v[136:139], v193 offset:4096
v_mfma_f32_16x16x32_bf16 a[68:71], v[100:103], v[72:75], a[68:71]
buffer_load_dwordx4 v20, s[12:15], s25 offen offset:0 lds
v_mfma_f32_16x16x32_bf16 a[72:75], v[104:107], v[72:75], a[72:75]
ds_read_b64_tr_b16 v[172:173], v197 offset:8192
v_mfma_f32_16x16x32_bf16 a[76:79], v[108:111], v[72:75], a[76:79]
v_mfma_f32_16x16x32_bf16 a[80:83], v[112:115], v[72:75], a[80:83]
ds_read_b64_tr_b16 v[174:175], v197 offset:9216
v_mfma_f32_16x16x32_bf16 a[84:87], v[116:119], v[72:75], a[84:87]
buffer_load_dwordx4 v21, s[12:15], s25 offen offset:1024 lds
v_mfma_f32_16x16x32_bf16 a[88:91], v[120:123], v[72:75], a[88:91]
ds_read_b128 v[140:143], v193 offset:6144
v_mfma_f32_16x16x32_bf16 a[92:95], v[124:127], v[72:75], a[92:95]
v_mfma_f32_16x16x32_bf16 a[96:99], v[96:99], v[76:79], a[96:99]
ds_read_b64_tr_b16 v[176:177], v198 offset:8192
v_mfma_f32_16x16x32_bf16 a[100:103], v[100:103], v[76:79], a[100:103]
buffer_load_dwordx4 v22, s[12:15], s25 offen offset:2048 lds
v_mfma_f32_16x16x32_bf16 a[104:107], v[104:107], v[76:79], a[104:107]
ds_read_b64_tr_b16 v[178:179], v198 offset:9216
v_mfma_f32_16x16x32_bf16 a[108:111], v[108:111], v[76:79], a[108:111]
v_mfma_f32_16x16x32_bf16 a[112:115], v[112:115], v[76:79], a[112:115]
ds_read_b128 v[144:147], v193 offset:8192
v_mfma_f32_16x16x32_bf16 a[116:119], v[116:119], v[76:79], a[116:119]
buffer_load_dwordx4 v23, s[12:15], s25 offen offset:3072 lds
v_mfma_f32_16x16x32_bf16 a[120:123], v[120:123], v[76:79], a[120:123]
ds_read_b64_tr_b16 v[180:181], v199 offset:8192
v_mfma_f32_16x16x32_bf16 a[124:127], v[124:127], v[76:79], a[124:127]
v_mfma_f32_16x16x32_bf16 a[128:131], v[96:99], v[80:83], a[128:131]
ds_read_b64_tr_b16 v[182:183], v199 offset:9216
v_mfma_f32_16x16x32_bf16 a[132:135], v[100:103], v[80:83], a[132:135]
v_mfma_f32_16x16x32_bf16 a[136:139], v[104:107], v[80:83], a[136:139]
ds_read_b128 v[148:151], v193 offset:10240
v_mfma_f32_16x16x32_bf16 a[140:143], v[108:111], v[80:83], a[140:143]
v_mfma_f32_16x16x32_bf16 a[144:147], v[112:115], v[80:83], a[144:147]
ds_read_b64_tr_b16 v[184:185], v200 offset:8192
v_mfma_f32_16x16x32_bf16 a[148:151], v[116:119], v[80:83], a[148:151]
v_mfma_f32_16x16x32_bf16 a[152:155], v[120:123], v[80:83], a[152:155]
ds_read_b64_tr_b16 v[186:187], v200 offset:9216
v_mfma_f32_16x16x32_bf16 a[156:159], v[124:127], v[80:83], a[156:159]
v_mfma_f32_16x16x32_bf16 a[160:163], v[96:99], v[84:87], a[160:163]
s_add_u32 s24, s24, 128
s_add_u32 s25, s25, 128
ds_read_b128 v[152:155], v193 offset:12288
v_mfma_f32_16x16x32_bf16 a[164:167], v[100:103], v[84:87], a[164:167]
v_mfma_f32_16x16x32_bf16 a[168:171], v[104:107], v[84:87], a[168:171]
ds_read_b64_tr_b16 v[188:189], v201 offset:8192
v_mfma_f32_16x16x32_bf16 a[172:175], v[108:111], v[84:87], a[172:175]
v_mfma_f32_16x16x32_bf16 a[176:179], v[112:115], v[84:87], a[176:179]
s_add_u32 s44, s35, 65536
ds_read_b64_tr_b16 v[190:191], v201 offset:9216
v_mfma_f32_16x16x32_bf16 a[180:183], v[116:119], v[84:87], a[180:183]
s_cmp_ge_u32 s44, s39
s_cselect_b32 s45, s55, s54
v_mfma_f32_16x16x32_bf16 a[184:187], v[120:123], v[84:87], a[184:187]
ds_read_b128 v[156:159], v193 offset:14336
v_mfma_f32_16x16x32_bf16 a[188:191], v[124:127], v[84:87], a[188:191]
s_add_u32 s44, s36, 65536
v_mfma_f32_16x16x32_bf16 a[192:195], v[96:99], v[88:91], a[192:195]
v_mfma_f32_16x16x32_bf16 a[196:199], v[100:103], v[88:91], a[196:199]
s_cmp_ge_u32 s44, s39
s_cselect_b32 s34, s55, s54
v_mfma_f32_16x16x32_bf16 a[200:203], v[104:107], v[88:91], a[200:203]
v_add_u32 v194, s45, v194
v_mfma_f32_16x16x32_bf16 a[204:207], v[108:111], v[88:91], a[204:207]
v_add_u32 v195, s45, v195
v_mfma_f32_16x16x32_bf16 a[208:211], v[112:115], v[88:91], a[208:211]
v_add_u32 v196, s45, v196
v_mfma_f32_16x16x32_bf16 a[212:215], v[116:119], v[88:91], a[212:215]
v_add_u32 v197, s45, v197
v_mfma_f32_16x16x32_bf16 a[216:219], v[120:123], v[88:91], a[216:219]
v_add_u32 v198, s45, v198
v_mfma_f32_16x16x32_bf16 a[220:223], v[124:127], v[88:91], a[220:223]
v_add_u32 v199, s45, v199
v_mfma_f32_16x16x32_bf16 a[224:227], v[96:99], v[92:95], a[224:227]
v_add_u32 v200, s45, v200
v_mfma_f32_16x16x32_bf16 a[228:231], v[100:103], v[92:95], a[228:231]
v_add_u32 v201, s45, v201
v_mfma_f32_16x16x32_bf16 a[232:235], v[104:107], v[92:95], a[232:235]
v_add_u32 v192, s34, v192
v_mfma_f32_16x16x32_bf16 a[236:239], v[108:111], v[92:95], a[236:239]
v_add_u32 v193, s34, v193
v_mfma_f32_16x16x32_bf16 a[240:243], v[112:115], v[92:95], a[240:243]
v_mfma_f32_16x16x32_bf16 a[244:247], v[116:119], v[92:95], a[244:247]
v_mfma_f32_16x16x32_bf16 a[248:251], v[120:123], v[92:95], a[248:251]
v_mfma_f32_16x16x32_bf16 a[252:255], v[124:127], v[92:95], a[252:255]
s_waitcnt vmcnt(9) lgkmcnt(0)
s_barrier
v_mfma_f32_16x16x32_bf16 a[0:3], v[160:163], v[128:131], a[0:3]
s_add_u32 m0, s91, 65536
ds_read_b64_tr_b16 v[96:97], v194 offset:0
v_mfma_f32_16x16x32_bf16 a[4:7], v[164:167], v[128:131], a[4:7]
buffer_load_dwordx4 v16, s[16:19], s26 offen offset:0 lds
v_mfma_f32_16x16x32_bf16 a[8:11], v[168:171], v[128:131], a[8:11]
ds_read_b64_tr_b16 v[98:99], v194 offset:1024
v_mfma_f32_16x16x32_bf16 a[12:15], v[172:175], v[128:131], a[12:15]
v_mfma_f32_16x16x32_bf16 a[16:19], v[176:179], v[128:131], a[16:19]
ds_read_b128 v[64:67], v192 offset:0
v_mfma_f32_16x16x32_bf16 a[20:23], v[180:183], v[128:131], a[20:23]
buffer_load_dwordx4 v17, s[16:19], s26 offen offset:1024 lds
v_mfma_f32_16x16x32_bf16 a[24:27], v[184:187], v[128:131], a[24:27]
ds_read_b64_tr_b16 v[100:101], v195 offset:0
v_mfma_f32_16x16x32_bf16 a[28:31], v[188:191], v[128:131], a[28:31]
v_mfma_f32_16x16x32_bf16 a[32:35], v[160:163], v[132:135], a[32:35]
ds_read_b64_tr_b16 v[102:103], v195 offset:1024
v_mfma_f32_16x16x32_bf16 a[36:39], v[164:167], v[132:135], a[36:39]
buffer_load_dwordx4 v18, s[16:19], s26 offen offset:2048 lds
v_mfma_f32_16x16x32_bf16 a[40:43], v[168:171], v[132:135], a[40:43]
ds_read_b128 v[68:71], v192 offset:2048
v_mfma_f32_16x16x32_bf16 a[44:47], v[172:175], v[132:135], a[44:47]
v_mfma_f32_16x16x32_bf16 a[48:51], v[176:179], v[132:135], a[48:51]
ds_read_b64_tr_b16 v[104:105], v196 offset:0
v_mfma_f32_16x16x32_bf16 a[52:55], v[180:183], v[132:135], a[52:55]
buffer_load_dwordx4 v19, s[16:19], s26 offen offset:3072 lds
v_mfma_f32_16x16x32_bf16 a[56:59], v[184:187], v[132:135], a[56:59]
ds_read_b64_tr_b16 v[106:107], v196 offset:1024
v_mfma_f32_16x16x32_bf16 a[60:63], v[188:191], v[132:135], a[60:63]
s_add_u32 s26, s26, 524288
v_mfma_f32_16x16x32_bf16 a[64:67], v[160:163], v[136:139], a[64:67]
s_add_u32 m0, s91, 81920
ds_read_b128 v[72:75], v192 offset:4096
v_mfma_f32_16x16x32_bf16 a[68:71], v[164:167], v[136:139], a[68:71]
buffer_load_dwordx4 v16, s[16:19], s26 offen offset:0 lds
v_mfma_f32_16x16x32_bf16 a[72:75], v[168:171], v[136:139], a[72:75]
ds_read_b64_tr_b16 v[108:109], v197 offset:0
v_mfma_f32_16x16x32_bf16 a[76:79], v[172:175], v[136:139], a[76:79]
v_mfma_f32_16x16x32_bf16 a[80:83], v[176:179], v[136:139], a[80:83]
ds_read_b64_tr_b16 v[110:111], v197 offset:1024
v_mfma_f32_16x16x32_bf16 a[84:87], v[180:183], v[136:139], a[84:87]
buffer_load_dwordx4 v17, s[16:19], s26 offen offset:1024 lds
v_mfma_f32_16x16x32_bf16 a[88:91], v[184:187], v[136:139], a[88:91]
ds_read_b128 v[76:79], v192 offset:6144
v_mfma_f32_16x16x32_bf16 a[92:95], v[188:191], v[136:139], a[92:95]
v_mfma_f32_16x16x32_bf16 a[96:99], v[160:163], v[140:143], a[96:99]
ds_read_b64_tr_b16 v[112:113], v198 offset:0
v_mfma_f32_16x16x32_bf16 a[100:103], v[164:167], v[140:143], a[100:103]
buffer_load_dwordx4 v18, s[16:19], s26 offen offset:2048 lds
v_mfma_f32_16x16x32_bf16 a[104:107], v[168:171], v[140:143], a[104:107]
ds_read_b64_tr_b16 v[114:115], v198 offset:1024
v_mfma_f32_16x16x32_bf16 a[108:111], v[172:175], v[140:143], a[108:111]
v_mfma_f32_16x16x32_bf16 a[112:115], v[176:179], v[140:143], a[112:115]
ds_read_b128 v[80:83], v192 offset:8192
v_mfma_f32_16x16x32_bf16 a[116:119], v[180:183], v[140:143], a[116:119]
buffer_load_dwordx4 v19, s[16:19], s26 offen offset:3072 lds
v_mfma_f32_16x16x32_bf16 a[120:123], v[184:187], v[140:143], a[120:123]
ds_read_b64_tr_b16 v[116:117], v199 offset:0
v_mfma_f32_16x16x32_bf16 a[124:127], v[188:191], v[140:143], a[124:127]
s_add_u32 s26, s26, 524288
v_mfma_f32_16x16x32_bf16 a[128:131], v[160:163], v[144:147], a[128:131]
ds_read_b64_tr_b16 v[118:119], v199 offset:1024
v_mfma_f32_16x16x32_bf16 a[132:135], v[164:167], v[144:147], a[132:135]
global_load_dword v35, v[206:207], off
v_mfma_f32_16x16x32_bf16 a[136:139], v[168:171], v[144:147], a[136:139]
ds_read_b128 v[84:87], v192 offset:10240
v_mfma_f32_16x16x32_bf16 a[140:143], v[172:175], v[144:147], a[140:143]
v_mfma_f32_16x16x32_bf16 a[144:147], v[176:179], v[144:147], a[144:147]
ds_read_b64_tr_b16 v[120:121], v200 offset:0
v_mfma_f32_16x16x32_bf16 a[148:151], v[180:183], v[144:147], a[148:151]
v_add_u32 v11, 8, v11
v_and_b32 v11, -8, v11
v_mfma_f32_16x16x32_bf16 a[152:155], v[184:187], v[144:147], a[152:155]
ds_read_b64_tr_b16 v[122:123], v200 offset:1024
v_mfma_f32_16x16x32_bf16 a[156:159], v[188:191], v[144:147], a[156:159]
v_mfma_f32_16x16x32_bf16 a[160:163], v[160:163], v[148:151], a[160:163]
ds_read_b128 v[88:91], v192 offset:12288
v_mfma_f32_16x16x32_bf16 a[164:167], v[164:167], v[148:151], a[164:167]
s_nop 0
v_or_b32 v0, 0, v11
v_mfma_f32_16x16x32_bf16 a[168:171], v[168:171], v[148:151], a[168:171]
ds_read_b64_tr_b16 v[124:125], v201 offset:0
v_mfma_f32_16x16x32_bf16 a[172:175], v[172:175], v[148:151], a[172:175]
v_add_co_u32 v206, vcc, v208, v206
v_mfma_f32_16x16x32_bf16 a[176:179], v[176:179], v[148:151], a[176:179]
ds_read_b64_tr_b16 v[126:127], v201 offset:1024
v_mfma_f32_16x16x32_bf16 a[180:183], v[180:183], v[148:151], a[180:183]
v_addc_co_u32 v207, vcc, 0, v207, vcc
v_mfma_f32_16x16x32_bf16 a[184:187], v[184:187], v[148:151], a[184:187]
ds_read_b128 v[92:95], v192 offset:14336
v_mfma_f32_16x16x32_bf16 a[188:191], v[188:191], v[148:151], a[188:191]
buffer_store_dword v0, v6, s[60:63], 0 offen sc1
v_mfma_f32_16x16x32_bf16 a[192:195], v[160:163], v[152:155], a[192:195]
v_mfma_f32_16x16x32_bf16 a[196:199], v[164:167], v[152:155], a[196:199]
v_mfma_f32_16x16x32_bf16 a[200:203], v[168:171], v[152:155], a[200:203]
v_mfma_f32_16x16x32_bf16 a[204:207], v[172:175], v[152:155], a[204:207]
v_mfma_f32_16x16x32_bf16 a[208:211], v[176:179], v[152:155], a[208:211]
v_mfma_f32_16x16x32_bf16 a[212:215], v[180:183], v[152:155], a[212:215]
v_mfma_f32_16x16x32_bf16 a[216:219], v[184:187], v[152:155], a[216:219]
v_mfma_f32_16x16x32_bf16 a[220:223], v[188:191], v[152:155], a[220:223]
v_mfma_f32_16x16x32_bf16 a[224:227], v[160:163], v[156:159], a[224:227]
v_mfma_f32_16x16x32_bf16 a[228:231], v[164:167], v[156:159], a[228:231]
s_mov_b32 s37, s36
v_mfma_f32_16x16x32_bf16 a[232:235], v[168:171], v[156:159], a[232:235]
s_add_u32 s35, s35, s45
v_mfma_f32_16x16x32_bf16 a[236:239], v[172:175], v[156:159], a[236:239]
s_add_u32 s36, s36, s34
v_mfma_f32_16x16x32_bf16 a[240:243], v[176:179], v[156:159], a[240:243]
v_mfma_f32_16x16x32_bf16 a[244:247], v[180:183], v[156:159], a[244:247]
v_mfma_f32_16x16x32_bf16 a[248:251], v[184:187], v[156:159], a[248:251]
v_mfma_f32_16x16x32_bf16 a[252:255], v[188:191], v[156:159], a[252:255]
s_waitcnt lgkmcnt(0)
v_mfma_f32_16x16x32_bf16 a[0:3], v[96:99], v[64:67], a[0:3]
s_add_u32 m0, s92, 98304
ds_read_b64_tr_b16 v[160:161], v194 offset:8192
v_mfma_f32_16x16x32_bf16 a[4:7], v[100:103], v[64:67], a[4:7]
buffer_load_dwordx4 v20, s[12:15], s24 offen offset:0 lds
v_mfma_f32_16x16x32_bf16 a[8:11], v[104:107], v[64:67], a[8:11]
ds_read_b64_tr_b16 v[162:163], v194 offset:9216
v_mfma_f32_16x16x32_bf16 a[12:15], v[108:111], v[64:67], a[12:15]
v_mfma_f32_16x16x32_bf16 a[16:19], v[112:115], v[64:67], a[16:19]
ds_read_b128 v[128:131], v193 offset:0
v_mfma_f32_16x16x32_bf16 a[20:23], v[116:119], v[64:67], a[20:23]
buffer_load_dwordx4 v21, s[12:15], s24 offen offset:1024 lds
v_mfma_f32_16x16x32_bf16 a[24:27], v[120:123], v[64:67], a[24:27]
ds_read_b64_tr_b16 v[164:165], v195 offset:8192
v_mfma_f32_16x16x32_bf16 a[28:31], v[124:127], v[64:67], a[28:31]
v_mfma_f32_16x16x32_bf16 a[32:35], v[96:99], v[68:71], a[32:35]
ds_read_b64_tr_b16 v[166:167], v195 offset:9216
v_mfma_f32_16x16x32_bf16 a[36:39], v[100:103], v[68:71], a[36:39]
buffer_load_dwordx4 v22, s[12:15], s24 offen offset:2048 lds
v_mfma_f32_16x16x32_bf16 a[40:43], v[104:107], v[68:71], a[40:43]
ds_read_b128 v[132:135], v193 offset:2048
v_mfma_f32_16x16x32_bf16 a[44:47], v[108:111], v[68:71], a[44:47]
v_mfma_f32_16x16x32_bf16 a[48:51], v[112:115], v[68:71], a[48:51]
ds_read_b64_tr_b16 v[168:169], v196 offset:8192
v_mfma_f32_16x16x32_bf16 a[52:55], v[116:119], v[68:71], a[52:55]
buffer_load_dwordx4 v23, s[12:15], s24 offen offset:3072 lds
v_mfma_f32_16x16x32_bf16 a[56:59], v[120:123], v[68:71], a[56:59]
ds_read_b64_tr_b16 v[170:171], v196 offset:9216
v_mfma_f32_16x16x32_bf16 a[60:63], v[124:127], v[68:71], a[60:63]
v_mfma_f32_16x16x32_bf16 a[64:67], v[96:99], v[72:75], a[64:67]
s_add_u32 m0, s92, 102400
ds_read_b128 v[136:139], v193 offset:4096
v_mfma_f32_16x16x32_bf16 a[68:71], v[100:103], v[72:75], a[68:71]
buffer_load_dwordx4 v20, s[12:15], s25 offen offset:0 lds
v_mfma_f32_16x16x32_bf16 a[72:75], v[104:107], v[72:75], a[72:75]
ds_read_b64_tr_b16 v[172:173], v197 offset:8192
v_mfma_f32_16x16x32_bf16 a[76:79], v[108:111], v[72:75], a[76:79]
v_mfma_f32_16x16x32_bf16 a[80:83], v[112:115], v[72:75], a[80:83]
ds_read_b64_tr_b16 v[174:175], v197 offset:9216
v_mfma_f32_16x16x32_bf16 a[84:87], v[116:119], v[72:75], a[84:87]
buffer_load_dwordx4 v21, s[12:15], s25 offen offset:1024 lds
v_mfma_f32_16x16x32_bf16 a[88:91], v[120:123], v[72:75], a[88:91]
ds_read_b128 v[140:143], v193 offset:6144
v_mfma_f32_16x16x32_bf16 a[92:95], v[124:127], v[72:75], a[92:95]
v_mfma_f32_16x16x32_bf16 a[96:99], v[96:99], v[76:79], a[96:99]
ds_read_b64_tr_b16 v[176:177], v198 offset:8192
v_mfma_f32_16x16x32_bf16 a[100:103], v[100:103], v[76:79], a[100:103]
buffer_load_dwordx4 v22, s[12:15], s25 offen offset:2048 lds
v_mfma_f32_16x16x32_bf16 a[104:107], v[104:107], v[76:79], a[104:107]
ds_read_b64_tr_b16 v[178:179], v198 offset:9216
v_mfma_f32_16x16x32_bf16 a[108:111], v[108:111], v[76:79], a[108:111]
v_mfma_f32_16x16x32_bf16 a[112:115], v[112:115], v[76:79], a[112:115]
ds_read_b128 v[144:147], v193 offset:8192
v_mfma_f32_16x16x32_bf16 a[116:119], v[116:119], v[76:79], a[116:119]
buffer_load_dwordx4 v23, s[12:15], s25 offen offset:3072 lds
v_mfma_f32_16x16x32_bf16 a[120:123], v[120:123], v[76:79], a[120:123]
ds_read_b64_tr_b16 v[180:181], v199 offset:8192
v_mfma_f32_16x16x32_bf16 a[124:127], v[124:127], v[76:79], a[124:127]
v_mfma_f32_16x16x32_bf16 a[128:131], v[96:99], v[80:83], a[128:131]
ds_read_b64_tr_b16 v[182:183], v199 offset:9216
v_mfma_f32_16x16x32_bf16 a[132:135], v[100:103], v[80:83], a[132:135]
v_mfma_f32_16x16x32_bf16 a[136:139], v[104:107], v[80:83], a[136:139]
ds_read_b128 v[148:151], v193 offset:10240
v_mfma_f32_16x16x32_bf16 a[140:143], v[108:111], v[80:83], a[140:143]
v_mfma_f32_16x16x32_bf16 a[144:147], v[112:115], v[80:83], a[144:147]
ds_read_b64_tr_b16 v[184:185], v200 offset:8192
v_mfma_f32_16x16x32_bf16 a[148:151], v[116:119], v[80:83], a[148:151]
v_mfma_f32_16x16x32_bf16 a[152:155], v[120:123], v[80:83], a[152:155]
ds_read_b64_tr_b16 v[186:187], v200 offset:9216
v_mfma_f32_16x16x32_bf16 a[156:159], v[124:127], v[80:83], a[156:159]
v_mfma_f32_16x16x32_bf16 a[160:163], v[96:99], v[84:87], a[160:163]
s_add_u32 s24, s24, 128
s_add_u32 s25, s25, 128
ds_read_b128 v[152:155], v193 offset:12288
v_mfma_f32_16x16x32_bf16 a[164:167], v[100:103], v[84:87], a[164:167]
v_mfma_f32_16x16x32_bf16 a[168:171], v[104:107], v[84:87], a[168:171]
ds_read_b64_tr_b16 v[188:189], v201 offset:8192
v_mfma_f32_16x16x32_bf16 a[172:175], v[108:111], v[84:87], a[172:175]
v_mfma_f32_16x16x32_bf16 a[176:179], v[112:115], v[84:87], a[176:179]
ds_read_b64_tr_b16 v[190:191], v201 offset:9216
v_mfma_f32_16x16x32_bf16 a[180:183], v[116:119], v[84:87], a[180:183]
v_mfma_f32_16x16x32_bf16 a[184:187], v[120:123], v[84:87], a[184:187]
ds_read_b128 v[156:159], v193 offset:14336
v_mfma_f32_16x16x32_bf16 a[188:191], v[124:127], v[84:87], a[188:191]
v_mfma_f32_16x16x32_bf16 a[192:195], v[96:99], v[88:91], a[192:195]
v_mfma_f32_16x16x32_bf16 a[196:199], v[100:103], v[88:91], a[196:199]
v_mfma_f32_16x16x32_bf16 a[200:203], v[104:107], v[88:91], a[200:203]
v_mfma_f32_16x16x32_bf16 a[204:207], v[108:111], v[88:91], a[204:207]
v_mfma_f32_16x16x32_bf16 a[208:211], v[112:115], v[88:91], a[208:211]
v_mfma_f32_16x16x32_bf16 a[212:215], v[116:119], v[88:91], a[212:215]
v_mfma_f32_16x16x32_bf16 a[216:219], v[120:123], v[88:91], a[216:219]
v_mfma_f32_16x16x32_bf16 a[220:223], v[124:127], v[88:91], a[220:223]
v_mfma_f32_16x16x32_bf16 a[224:227], v[96:99], v[92:95], a[224:227]
v_mfma_f32_16x16x32_bf16 a[228:231], v[100:103], v[92:95], a[228:231]
v_mfma_f32_16x16x32_bf16 a[232:235], v[104:107], v[92:95], a[232:235]
v_mfma_f32_16x16x32_bf16 a[236:239], v[108:111], v[92:95], a[236:239]
v_mfma_f32_16x16x32_bf16 a[240:243], v[112:115], v[92:95], a[240:243]
v_mfma_f32_16x16x32_bf16 a[244:247], v[116:119], v[92:95], a[244:247]
v_mfma_f32_16x16x32_bf16 a[248:251], v[120:123], v[92:95], a[248:251]
v_mfma_f32_16x16x32_bf16 a[252:255], v[124:127], v[92:95], a[252:255]
s_waitcnt vmcnt(14) lgkmcnt(0)
s_barrier
v_mfma_f32_16x16x32_bf16 a[0:3], v[160:163], v[128:131], a[0:3]
s_add_u32 m0, s91, 0
ds_read_b64_tr_b16 v[64:65], v26 offset:0
v_mfma_f32_16x16x32_bf16 a[4:7], v[164:167], v[128:131], a[4:7]
buffer_load_dwordx4 v16, s[16:19], s26 offen offset:0 lds
v_mfma_f32_16x16x32_bf16 a[8:11], v[168:171], v[128:131], a[8:11]
ds_read_b64_tr_b16 v[66:67], v26 offset:1024
v_mfma_f32_16x16x32_bf16 a[12:15], v[172:175], v[128:131], a[12:15]
v_mfma_f32_16x16x32_bf16 a[16:19], v[176:179], v[128:131], a[16:19]
ds_read_b64_tr_b16 v[68:69], v27 offset:0
v_mfma_f32_16x16x32_bf16 a[20:23], v[180:183], v[128:131], a[20:23]
buffer_load_dwordx4 v17, s[16:19], s26 offen offset:1024 lds
v_mfma_f32_16x16x32_bf16 a[24:27], v[184:187], v[128:131], a[24:27]
ds_read_b64_tr_b16 v[70:71], v27 offset:1024
v_mfma_f32_16x16x32_bf16 a[28:31], v[188:191], v[128:131], a[28:31]
v_mfma_f32_16x16x32_bf16 a[32:35], v[160:163], v[132:135], a[32:35]
ds_read_b64_tr_b16 v[72:73], v28 offset:0
v_mfma_f32_16x16x32_bf16 a[36:39], v[164:167], v[132:135], a[36:39]
buffer_load_dwordx4 v18, s[16:19], s26 offen offset:2048 lds
v_mfma_f32_16x16x32_bf16 a[40:43], v[168:171], v[132:135], a[40:43]
ds_read_b64_tr_b16 v[74:75], v28 offset:1024
v_mfma_f32_16x16x32_bf16 a[44:47], v[172:175], v[132:135], a[44:47]
v_mfma_f32_16x16x32_bf16 a[48:51], v[176:179], v[132:135], a[48:51]
ds_read_b64_tr_b16 v[76:77], v29 offset:0
v_mfma_f32_16x16x32_bf16 a[52:55], v[180:183], v[132:135], a[52:55]
buffer_load_dwordx4 v19, s[16:19], s26 offen offset:3072 lds
v_mfma_f32_16x16x32_bf16 a[56:59], v[184:187], v[132:135], a[56:59]
ds_read_b64_tr_b16 v[78:79], v29 offset:1024
v_mfma_f32_16x16x32_bf16 a[60:63], v[188:191], v[132:135], a[60:63]
s_add_u32 s26, s26, 524288
v_mfma_f32_16x16x32_bf16 a[64:67], v[160:163], v[136:139], a[64:67]
s_add_u32 m0, s92, 131072
ds_read_b64_tr_b16 v[80:81], v30 offset:0
v_mfma_f32_16x16x32_bf16 a[68:71], v[164:167], v[136:139], a[68:71]
buffer_load_dwordx4 v20, s[12:15], s24 offen offset:0 lds
v_mfma_f32_16x16x32_bf16 a[72:75], v[168:171], v[136:139], a[72:75]
ds_read_b64_tr_b16 v[82:83], v30 offset:1024
v_mfma_f32_16x16x32_bf16 a[76:79], v[172:175], v[136:139], a[76:79]
v_mfma_f32_16x16x32_bf16 a[80:83], v[176:179], v[136:139], a[80:83]
ds_read_b64_tr_b16 v[84:85], v31 offset:0
v_mfma_f32_16x16x32_bf16 a[84:87], v[180:183], v[136:139], a[84:87]
buffer_load_dwordx4 v21, s[12:15], s24 offen offset:1024 lds
v_mfma_f32_16x16x32_bf16 a[88:91], v[184:187], v[136:139], a[88:91]
ds_read_b64_tr_b16 v[86:87], v31 offset:1024
v_mfma_f32_16x16x32_bf16 a[92:95], v[188:191], v[136:139], a[92:95]
v_mfma_f32_16x16x32_bf16 a[96:99], v[160:163], v[140:143], a[96:99]
ds_read_b64_tr_b16 v[88:89], v32 offset:0
v_mfma_f32_16x16x32_bf16 a[100:103], v[164:167], v[140:143], a[100:103]
buffer_load_dwordx4 v22, s[12:15], s24 offen offset:2048 lds
v_mfma_f32_16x16x32_bf16 a[104:107], v[168:171], v[140:143], a[104:107]
ds_read_b64_tr_b16 v[90:91], v32 offset:1024
v_mfma_f32_16x16x32_bf16 a[108:111], v[172:175], v[140:143], a[108:111]
v_mfma_f32_16x16x32_bf16 a[112:115], v[176:179], v[140:143], a[112:115]
ds_read_b64_tr_b16 v[92:93], v33 offset:0
v_mfma_f32_16x16x32_bf16 a[116:119], v[180:183], v[140:143], a[116:119]
buffer_load_dwordx4 v23, s[12:15], s24 offen offset:3072 lds
v_mfma_f32_16x16x32_bf16 a[120:123], v[184:187], v[140:143], a[120:123]
ds_read_b64_tr_b16 v[94:95], v33 offset:1024
v_mfma_f32_16x16x32_bf16 a[124:127], v[188:191], v[140:143], a[124:127]
v_mfma_f32_16x16x32_bf16 a[128:131], v[160:163], v[144:147], a[128:131]
s_add_u32 m0, s92, 135168
v_mfma_f32_16x16x32_bf16 a[132:135], v[164:167], v[144:147], a[132:135]
buffer_load_dwordx4 v20, s[12:15], s25 offen offset:0 lds
v_mfma_f32_16x16x32_bf16 a[136:139], v[168:171], v[144:147], a[136:139]
ds_read_b128 v[48:51], v24 offset:0
v_mfma_f32_16x16x32_bf16 a[140:143], v[172:175], v[144:147], a[140:143]
v_mfma_f32_16x16x32_bf16 a[144:147], v[176:179], v[144:147], a[144:147]
ds_read_b128 v[52:55], v24 offset:2048
v_mfma_f32_16x16x32_bf16 a[148:151], v[180:183], v[144:147], a[148:151]
buffer_load_dwordx4 v21, s[12:15], s25 offen offset:1024 lds
v_mfma_f32_16x16x32_bf16 a[152:155], v[184:187], v[144:147], a[152:155]
ds_read_b128 v[56:59], v24 offset:4096
v_mfma_f32_16x16x32_bf16 a[156:159], v[188:191], v[144:147], a[156:159]
v_mfma_f32_16x16x32_bf16 a[160:163], v[160:163], v[148:151], a[160:163]
v_mfma_f32_16x16x32_bf16 a[164:167], v[164:167], v[148:151], a[164:167]
buffer_load_dwordx4 v22, s[12:15], s25 offen offset:2048 lds
v_mfma_f32_16x16x32_bf16 a[168:171], v[168:171], v[148:151], a[168:171]
v_mfma_f32_16x16x32_bf16 a[172:175], v[172:175], v[148:151], a[172:175]
v_mfma_f32_16x16x32_bf16 a[176:179], v[176:179], v[148:151], a[176:179]
v_mfma_f32_16x16x32_bf16 a[180:183], v[180:183], v[148:151], a[180:183]
buffer_load_dwordx4 v23, s[12:15], s25 offen offset:3072 lds
v_mfma_f32_16x16x32_bf16 a[184:187], v[184:187], v[148:151], a[184:187]
v_mfma_f32_16x16x32_bf16 a[188:191], v[188:191], v[148:151], a[188:191]
v_mfma_f32_16x16x32_bf16 a[192:195], v[160:163], v[152:155], a[192:195]
v_mfma_f32_16x16x32_bf16 a[196:199], v[164:167], v[152:155], a[196:199]
s_add_u32 s24, s24, 128
s_add_u32 s25, s25, 128
v_mfma_f32_16x16x32_bf16 a[200:203], v[168:171], v[152:155], a[200:203]
v_mfma_f32_16x16x32_bf16 a[204:207], v[172:175], v[152:155], a[204:207]
v_mfma_f32_16x16x32_bf16 a[208:211], v[176:179], v[152:155], a[208:211]
v_mfma_f32_16x16x32_bf16 a[212:215], v[180:183], v[152:155], a[212:215]
v_mfma_f32_16x16x32_bf16 a[216:219], v[184:187], v[152:155], a[216:219]
v_mfma_f32_16x16x32_bf16 a[220:223], v[188:191], v[152:155], a[220:223]
v_mfma_f32_16x16x32_bf16 a[224:227], v[160:163], v[156:159], a[224:227]
v_mfma_f32_16x16x32_bf16 a[228:231], v[164:167], v[156:159], a[228:231]
v_mfma_f32_16x16x32_bf16 a[232:235], v[168:171], v[156:159], a[232:235]
v_mfma_f32_16x16x32_bf16 a[236:239], v[172:175], v[156:159], a[236:239]
v_mfma_f32_16x16x32_bf16 a[240:243], v[176:179], v[156:159], a[240:243]
v_mfma_f32_16x16x32_bf16 a[244:247], v[180:183], v[156:159], a[244:247]
v_mfma_f32_16x16x32_bf16 a[248:251], v[184:187], v[156:159], a[248:251]
v_mfma_f32_16x16x32_bf16 a[252:255], v[188:191], v[156:159], a[252:255]
s_waitcnt lgkmcnt(0)
s_branch .Lp2
.Lp1_v:
v_mfma_f32_16x16x32_bf16 a[0:3], v[64:67], v[96:99], 0
ds_read_b64_tr_b16 v[160:161], v194 offset:8192
v_mfma_f32_16x16x32_bf16 a[4:7], v[64:67], v[100:103], 0
s_add_u32 s37, s92, 98304
s_mov_b32 s40, 128
s_mov_b32 s41, 524416
v_mfma_f32_16x16x32_bf16 a[8:11], v[64:67], v[104:107], 0
s_mov_b32 m0, s37
ds_read_b64_tr_b16 v[162:163], v194 offset:9216
v_mfma_f32_16x16x32_bf16 a[12:15], v[64:67], v[108:111], 0
buffer_load_dwordx4 v202, s[4:7], s40 offen offset:0 lds
v_mfma_f32_16x16x32_bf16 a[16:19], v[64:67], v[112:115], 0
ds_read_b128 v[128:131], v193 offset:0
v_mfma_f32_16x16x32_bf16 a[20:23], v[64:67], v[116:119], 0
buffer_load_dwordx4 v203, s[4:7], s40 offen offset:1024 lds
v_mfma_f32_16x16x32_bf16 a[24:27], v[64:67], v[120:123], 0
ds_read_b64_tr_b16 v[164:165], v195 offset:8192
v_mfma_f32_16x16x32_bf16 a[28:31], v[64:67], v[124:127], 0
buffer_load_dwordx4 v204, s[4:7], s40 offen offset:2048 lds
v_mfma_f32_16x16x32_bf16 a[32:35], v[68:71], v[96:99], 0
ds_read_b64_tr_b16 v[166:167], v195 offset:9216
v_mfma_f32_16x16x32_bf16 a[36:39], v[68:71], v[100:103], 0
buffer_load_dwordx4 v205, s[4:7], s40 offen offset:3072 lds
v_mfma_f32_16x16x32_bf16 a[40:43], v[68:71], v[104:107], 0
s_add_u32 m0, s37, 4096
ds_read_b128 v[132:135], v193 offset:2048
v_mfma_f32_16x16x32_bf16 a[44:47], v[68:71], v[108:111], 0
buffer_load_dwordx4 v202, s[4:7], s41 offen offset:0 lds
v_mfma_f32_16x16x32_bf16 a[48:51], v[68:71], v[112:115], 0
ds_read_b64_tr_b16 v[168:169], v196 offset:8192
v_mfma_f32_16x16x32_bf16 a[52:55], v[68:71], v[116:119], 0
buffer_load_dwordx4 v203, s[4:7], s41 offen offset:1024 lds
v_mfma_f32_16x16x32_bf16 a[56:59], v[68:71], v[120:123], 0
ds_read_b64_tr_b16 v[170:171], v196 offset:9216
v_mfma_f32_16x16x32_bf16 a[60:63], v[68:71], v[124:127], 0
buffer_load_dwordx4 v204, s[4:7], s41 offen offset:2048 lds
v_mfma_f32_16x16x32_bf16 a[64:67], v[72:75], v[96:99], 0
ds_read_b128 v[136:139], v193 offset:4096
v_mfma_f32_16x16x32_bf16 a[68:71], v[72:75], v[100:103], 0
buffer_load_dwordx4 v205, s[4:7], s41 offen offset:3072 lds
v_mfma_f32_16x16x32_bf16 a[72:75], v[72:75], v[104:107], 0
ds_read_b64_tr_b16 v[172:173], v197 offset:8192
v_mfma_f32_16x16x32_bf16 a[76:79], v[72:75], v[108:111], 0
s_add_u32 s37, s92, 131072
s_mov_b32 s42, 1048576
s_mov_b32 s43, 1179648
v_mfma_f32_16x16x32_bf16 a[80:83], v[72:75], v[112:115], 0
s_mov_b32 m0, s37
ds_read_b64_tr_b16 v[174:175], v197 offset:9216
v_mfma_f32_16x16x32_bf16 a[84:87], v[72:75], v[116:119], 0
buffer_load_dwordx4 v16, s[8:11], s42 offen offset:0 nt lds
v_mfma_f32_16x16x32_bf16 a[88:91], v[72:75], v[120:123], 0
ds_read_b128 v[140:143], v193 offset:6144
v_mfma_f32_16x16x32_bf16 a[92:95], v[72:75], v[124:127], 0
buffer_load_dwordx4 v17, s[8:11], s42 offen offset:1024 nt lds
v_mfma_f32_16x16x32_bf16 a[96:99], v[76:79], v[96:99], 0
ds_read_b64_tr_b16 v[176:177], v198 offset:8192
v_mfma_f32_16x16x32_bf16 a[100:103], v[76:79], v[100:103], 0
buffer_load_dwordx4 v18, s[8:11], s42 offen offset:2048 nt lds
v_mfma_f32_16x16x32_bf16 a[104:107], v[76:79], v[104:107], 0
ds_read_b64_tr_b16 v[178:179], v198 offset:9216
v_mfma_f32_16x16x32_bf16 a[108:111], v[76:79], v[108:111], 0
buffer_load_dwordx4 v19, s[8:11], s42 offen offset:3072 nt lds
v_mfma_f32_16x16x32_bf16 a[112:115], v[76:79], v[112:115], 0
s_add_u32 m0, s37, 4096
ds_read_b128 v[144:147], v193 offset:8192
v_mfma_f32_16x16x32_bf16 a[116:119], v[76:79], v[116:119], 0
buffer_load_dwordx4 v16, s[8:11], s43 offen offset:0 nt lds
v_mfma_f32_16x16x32_bf16 a[120:123], v[76:79], v[120:123], 0
ds_read_b64_tr_b16 v[180:181], v199 offset:8192
v_mfma_f32_16x16x32_bf16 a[124:127], v[76:79], v[124:127], 0
buffer_load_dwordx4 v17, s[8:11], s43 offen offset:1024 nt lds
v_mfma_f32_16x16x32_bf16 a[128:131], v[80:83], v[96:99], 0
ds_read_b64_tr_b16 v[182:183], v199 offset:9216
v_mfma_f32_16x16x32_bf16 a[132:135], v[80:83], v[100:103], 0
buffer_load_dwordx4 v18, s[8:11], s43 offen offset:2048 nt lds
v_mfma_f32_16x16x32_bf16 a[136:139], v[80:83], v[104:107], 0
ds_read_b128 v[148:151], v193 offset:10240
v_mfma_f32_16x16x32_bf16 a[140:143], v[80:83], v[108:111], 0
buffer_load_dwordx4 v19, s[8:11], s43 offen offset:3072 nt lds
v_mfma_f32_16x16x32_bf16 a[144:147], v[80:83], v[112:115], 0
ds_read_b64_tr_b16 v[184:185], v200 offset:8192
v_mfma_f32_16x16x32_bf16 a[148:151], v[80:83], v[116:119], 0
v_mfma_f32_16x16x32_bf16 a[152:155], v[80:83], v[120:123], 0
ds_read_b64_tr_b16 v[186:187], v200 offset:9216
v_mfma_f32_16x16x32_bf16 a[156:159], v[80:83], v[124:127], 0
v_mfma_f32_16x16x32_bf16 a[160:163], v[84:87], v[96:99], 0
ds_read_b128 v[152:155], v193 offset:12288
v_mfma_f32_16x16x32_bf16 a[164:167], v[84:87], v[100:103], 0
v_mfma_f32_16x16x32_bf16 a[168:171], v[84:87], v[104:107], 0
ds_read_b64_tr_b16 v[188:189], v201 offset:8192
v_mfma_f32_16x16x32_bf16 a[172:175], v[84:87], v[108:111], 0
v_mfma_f32_16x16x32_bf16 a[176:179], v[84:87], v[112:115], 0
s_add_u32 s44, s35, 65536
ds_read_b64_tr_b16 v[190:191], v201 offset:9216
v_mfma_f32_16x16x32_bf16 a[180:183], v[84:87], v[116:119], 0
s_cmp_ge_u32 s44, s39
s_cselect_b32 s45, s55, s54
v_mfma_f32_16x16x32_bf16 a[184:187], v[84:87], v[120:123], 0
ds_read_b128 v[156:159], v193 offset:14336
v_mfma_f32_16x16x32_bf16 a[188:191], v[84:87], v[124:127], 0
s_add_u32 s44, s36, 65536
v_mfma_f32_16x16x32_bf16 a[192:195], v[88:91], v[96:99], 0
v_mfma_f32_16x16x32_bf16 a[196:199], v[88:91], v[100:103], 0
s_cmp_ge_u32 s44, s39
s_cselect_b32 s34, s55, s54
v_mfma_f32_16x16x32_bf16 a[200:203], v[88:91], v[104:107], 0
v_add_u32 v194, s45, v194
v_mfma_f32_16x16x32_bf16 a[204:207], v[88:91], v[108:111], 0
v_add_u32 v195, s45, v195
v_mfma_f32_16x16x32_bf16 a[208:211], v[88:91], v[112:115], 0
v_add_u32 v196, s45, v196
v_mfma_f32_16x16x32_bf16 a[212:215], v[88:91], v[116:119], 0
v_add_u32 v197, s45, v197
v_mfma_f32_16x16x32_bf16 a[216:219], v[88:91], v[120:123], 0
v_add_u32 v198, s45, v198
v_mfma_f32_16x16x32_bf16 a[220:223], v[88:91], v[124:127], 0
v_add_u32 v199, s45, v199
v_mfma_f32_16x16x32_bf16 a[224:227], v[92:95], v[96:99], 0
v_add_u32 v200, s45, v200
v_mfma_f32_16x16x32_bf16 a[228:231], v[92:95], v[100:103], 0
v_add_u32 v201, s45, v201
v_mfma_f32_16x16x32_bf16 a[232:235], v[92:95], v[104:107], 0
v_add_u32 v192, s34, v192
v_mfma_f32_16x16x32_bf16 a[236:239], v[92:95], v[108:111], 0
v_add_u32 v193, s34, v193
v_mfma_f32_16x16x32_bf16 a[240:243], v[92:95], v[112:115], 0
v_mfma_f32_16x16x32_bf16 a[244:247], v[92:95], v[116:119], 0
s_mov_b32 s40, 256
v_mfma_f32_16x16x32_bf16 a[248:251], v[92:95], v[120:123], 0
s_mov_b32 s41, 524544
v_mfma_f32_16x16x32_bf16 a[252:255], v[92:95], v[124:127], 0
s_waitcnt vmcnt(8) lgkmcnt(0)
s_barrier
v_mfma_f32_16x16x32_bf16 a[0:3], v[128:131], v[160:163], a[0:3]
s_mov_b32 m0, s35
ds_read_b64_tr_b16 v[96:97], v194 offset:0
v_mfma_f32_16x16x32_bf16 a[4:7], v[128:131], v[164:167], a[4:7]
buffer_load_dwordx4 v202, s[4:7], s40 offen offset:0 lds
v_mfma_f32_16x16x32_bf16 a[8:11], v[128:131], v[168:171], a[8:11]
ds_read_b64_tr_b16 v[98:99], v194 offset:1024
v_mfma_f32_16x16x32_bf16 a[12:15], v[128:131], v[172:175], a[12:15]
v_mfma_f32_16x16x32_bf16 a[16:19], v[128:131], v[176:179], a[16:19]
ds_read_b128 v[64:67], v192 offset:0
v_mfma_f32_16x16x32_bf16 a[20:23], v[128:131], v[180:183], a[20:23]
buffer_load_dwordx4 v203, s[4:7], s40 offen offset:1024 lds
v_mfma_f32_16x16x32_bf16 a[24:27], v[128:131], v[184:187], a[24:27]
ds_read_b64_tr_b16 v[100:101], v195 offset:0
v_mfma_f32_16x16x32_bf16 a[28:31], v[128:131], v[188:191], a[28:31]
v_mfma_f32_16x16x32_bf16 a[32:35], v[132:135], v[160:163], a[32:35]
ds_read_b64_tr_b16 v[102:103], v195 offset:1024
v_mfma_f32_16x16x32_bf16 a[36:39], v[132:135], v[164:167], a[36:39]
buffer_load_dwordx4 v204, s[4:7], s40 offen offset:2048 lds
v_mfma_f32_16x16x32_bf16 a[40:43], v[132:135], v[168:171], a[40:43]
ds_read_b128 v[68:71], v192 offset:2048
v_mfma_f32_16x16x32_bf16 a[44:47], v[132:135], v[172:175], a[44:47]
v_mfma_f32_16x16x32_bf16 a[48:51], v[132:135], v[176:179], a[48:51]
ds_read_b64_tr_b16 v[104:105], v196 offset:0
v_mfma_f32_16x16x32_bf16 a[52:55], v[132:135], v[180:183], a[52:55]
buffer_load_dwordx4 v205, s[4:7], s40 offen offset:3072 lds
v_mfma_f32_16x16x32_bf16 a[56:59], v[132:135], v[184:187], a[56:59]
ds_read_b64_tr_b16 v[106:107], v196 offset:1024
v_mfma_f32_16x16x32_bf16 a[60:63], v[132:135], v[188:191], a[60:63]
v_mfma_f32_16x16x32_bf16 a[64:67], v[136:139], v[160:163], a[64:67]
s_add_u32 m0, s35, 4096
ds_read_b128 v[72:75], v192 offset:4096
v_mfma_f32_16x16x32_bf16 a[68:71], v[136:139], v[164:167], a[68:71]
buffer_load_dwordx4 v202, s[4:7], s41 offen offset:0 lds
v_mfma_f32_16x16x32_bf16 a[72:75], v[136:139], v[168:171], a[72:75]
ds_read_b64_tr_b16 v[108:109], v197 offset:0
v_mfma_f32_16x16x32_bf16 a[76:79], v[136:139], v[172:175], a[76:79]
v_mfma_f32_16x16x32_bf16 a[80:83], v[136:139], v[176:179], a[80:83]
ds_read_b64_tr_b16 v[110:111], v197 offset:1024
v_mfma_f32_16x16x32_bf16 a[84:87], v[136:139], v[180:183], a[84:87]
buffer_load_dwordx4 v203, s[4:7], s41 offen offset:1024 lds
v_mfma_f32_16x16x32_bf16 a[88:91], v[136:139], v[184:187], a[88:91]
ds_read_b128 v[76:79], v192 offset:6144
v_mfma_f32_16x16x32_bf16 a[92:95], v[136:139], v[188:191], a[92:95]
v_mfma_f32_16x16x32_bf16 a[96:99], v[140:143], v[160:163], a[96:99]
ds_read_b64_tr_b16 v[112:113], v198 offset:0
v_mfma_f32_16x16x32_bf16 a[100:103], v[140:143], v[164:167], a[100:103]
buffer_load_dwordx4 v204, s[4:7], s41 offen offset:2048 lds
v_mfma_f32_16x16x32_bf16 a[104:107], v[140:143], v[168:171], a[104:107]
ds_read_b64_tr_b16 v[114:115], v198 offset:1024
v_mfma_f32_16x16x32_bf16 a[108:111], v[140:143], v[172:175], a[108:111]
v_mfma_f32_16x16x32_bf16 a[112:115], v[140:143], v[176:179], a[112:115]
ds_read_b128 v[80:83], v192 offset:8192
v_mfma_f32_16x16x32_bf16 a[116:119], v[140:143], v[180:183], a[116:119]
buffer_load_dwordx4 v205, s[4:7], s41 offen offset:3072 lds
v_mfma_f32_16x16x32_bf16 a[120:123], v[140:143], v[184:187], a[120:123]
ds_read_b64_tr_b16 v[116:117], v199 offset:0
v_mfma_f32_16x16x32_bf16 a[124:127], v[140:143], v[188:191], a[124:127]
v_mfma_f32_16x16x32_bf16 a[128:131], v[144:147], v[160:163], a[128:131]
ds_read_b64_tr_b16 v[118:119], v199 offset:1024
v_mfma_f32_16x16x32_bf16 a[132:135], v[144:147], v[164:167], a[132:135]
global_load_dword v35, v[206:207], off
v_mfma_f32_16x16x32_bf16 a[136:139], v[144:147], v[168:171], a[136:139]
ds_read_b128 v[84:87], v192 offset:10240
v_mfma_f32_16x16x32_bf16 a[140:143], v[144:147], v[172:175], a[140:143]
buffer_load_dword v2, v3, s[60:63], s90 offen sc1
v_mfma_f32_16x16x32_bf16 a[144:147], v[144:147], v[176:179], a[144:147]
s_mov_b32 s42, 1572864
ds_read_b64_tr_b16 v[120:121], v200 offset:0
v_mfma_f32_16x16x32_bf16 a[148:151], v[144:147], v[180:183], a[148:151]
s_mov_b32 s43, 1703936
v_mfma_f32_16x16x32_bf16 a[152:155], v[144:147], v[184:187], a[152:155]
ds_read_b64_tr_b16 v[122:123], v200 offset:1024
v_mfma_f32_16x16x32_bf16 a[156:159], v[144:147], v[188:191], a[156:159]
buffer_load_dword v11, v6, s[60:63], 0 offen sc1
v_mfma_f32_16x16x32_bf16 a[160:163], v[148:151], v[160:163], a[160:163]
s_add_u32 s40, s40, 128
ds_read_b128 v[88:91], v192 offset:12288
v_mfma_f32_16x16x32_bf16 a[164:167], v[148:151], v[164:167], a[164:167]
s_add_u32 s41, s41, 128
v_mfma_f32_16x16x32_bf16 a[168:171], v[148:151], v[168:171], a[168:171]
ds_read_b64_tr_b16 v[124:125], v201 offset:0
v_mfma_f32_16x16x32_bf16 a[172:175], v[148:151], v[172:175], a[172:175]
v_add_co_u32 v206, vcc, v208, v206
v_mfma_f32_16x16x32_bf16 a[176:179], v[148:151], v[176:179], a[176:179]
ds_read_b64_tr_b16 v[126:127], v201 offset:1024
v_mfma_f32_16x16x32_bf16 a[180:183], v[148:151], v[180:183], a[180:183]
v_addc_co_u32 v207, vcc, 0, v207, vcc
v_mfma_f32_16x16x32_bf16 a[184:187], v[148:151], v[184:187], a[184:187]
ds_read_b128 v[92:95], v192 offset:14336
v_mfma_f32_16x16x32_bf16 a[188:191], v[148:151], v[188:191], a[188:191]
buffer_store_dword v45, v44, s[60:63], 0 offen sc1
v_mfma_f32_16x16x32_bf16 a[192:195], v[152:155], v[160:163], a[192:195]
v_mfma_f32_16x16x32_bf16 a[196:199], v[152:155], v[164:167], a[196:199]
v_mfma_f32_16x16x32_bf16 a[200:203], v[152:155], v[168:171], a[200:203]
v_mfma_f32_16x16x32_bf16 a[204:207], v[152:155], v[172:175], a[204:207]
v_mfma_f32_16x16x32_bf16 a[208:211], v[152:155], v[176:179], a[208:211]
v_mfma_f32_16x16x32_bf16 a[212:215], v[152:155], v[180:183], a[212:215]
v_mfma_f32_16x16x32_bf16 a[216:219], v[152:155], v[184:187], a[216:219]
v_mfma_f32_16x16x32_bf16 a[220:223], v[152:155], v[188:191], a[220:223]
v_mfma_f32_16x16x32_bf16 a[224:227], v[156:159], v[160:163], a[224:227]
v_mfma_f32_16x16x32_bf16 a[228:231], v[156:159], v[164:167], a[228:231]
s_mov_b32 s37, s36
v_mfma_f32_16x16x32_bf16 a[232:235], v[156:159], v[168:171], a[232:235]
s_add_u32 s35, s35, s45
v_mfma_f32_16x16x32_bf16 a[236:239], v[156:159], v[172:175], a[236:239]
s_add_u32 s36, s36, s34
v_mfma_f32_16x16x32_bf16 a[240:243], v[156:159], v[176:179], a[240:243]
v_mfma_f32_16x16x32_bf16 a[244:247], v[156:159], v[180:183], a[244:247]
v_mfma_f32_16x16x32_bf16 a[248:251], v[156:159], v[184:187], a[248:251]
v_mfma_f32_16x16x32_bf16 a[252:255], v[156:159], v[188:191], a[252:255]
s_waitcnt lgkmcnt(0)
.Lp1_vloop:
v_mfma_f32_16x16x32_bf16 a[0:3], v[64:67], v[96:99], a[0:3]
s_mov_b32 m0, s37
ds_read_b64_tr_b16 v[160:161], v194 offset:8192
v_mfma_f32_16x16x32_bf16 a[4:7], v[64:67], v[100:103], a[4:7]
buffer_load_dwordx4 v16, s[8:11], s42 offen offset:0 nt lds
v_mfma_f32_16x16x32_bf16 a[8:11], v[64:67], v[104:107], a[8:11]
ds_read_b64_tr_b16 v[162:163], v194 offset:9216
v_mfma_f32_16x16x32_bf16 a[12:15], v[64:67], v[108:111], a[12:15]
v_mfma_f32_16x16x32_bf16 a[16:19], v[64:67], v[112:115], a[16:19]
ds_read_b128 v[128:131], v193 offset:0
v_mfma_f32_16x16x32_bf16 a[20:23], v[64:67], v[116:119], a[20:23]
buffer_load_dwordx4 v17, s[8:11], s42 offen offset:1024 nt lds
v_mfma_f32_16x16x32_bf16 a[24:27], v[64:67], v[120:123], a[24:27]
ds_read_b64_tr_b16 v[164:165], v195 offset:8192
v_mfma_f32_16x16x32_bf16 a[28:31], v[64:67], v[124:127], a[28:31]
v_mfma_f32_16x16x32_bf16 a[32:35], v[68:71], v[96:99], a[32:35]
ds_read_b64_tr_b16 v[166:167], v195 offset:9216
v_mfma_f32_16x16x32_bf16 a[36:39], v[68:71], v[100:103], a[36:39]
buffer_load_dwordx4 v18, s[8:11], s42 offen offset:2048 nt lds
v_mfma_f32_16x16x32_bf16 a[40:43], v[68:71], v[104:107], a[40:43]
ds_read_b128 v[132:135], v193 offset:2048
v_mfma_f32_16x16x32_bf16 a[44:47], v[68:71], v[108:111], a[44:47]
v_mfma_f32_16x16x32_bf16 a[48:51], v[68:71], v[112:115], a[48:51]
ds_read_b64_tr_b16 v[168:169], v196 offset:8192
v_mfma_f32_16x16x32_bf16 a[52:55], v[68:71], v[116:119], a[52:55]
buffer_load_dwordx4 v19, s[8:11], s42 offen offset:3072 nt lds
v_mfma_f32_16x16x32_bf16 a[56:59], v[68:71], v[120:123], a[56:59]
ds_read_b64_tr_b16 v[170:171], v196 offset:9216
v_mfma_f32_16x16x32_bf16 a[60:63], v[68:71], v[124:127], a[60:63]
v_mfma_f32_16x16x32_bf16 a[64:67], v[72:75], v[96:99], a[64:67]
s_add_u32 m0, s37, 4096
ds_read_b128 v[136:139], v193 offset:4096
v_mfma_f32_16x16x32_bf16 a[68:71], v[72:75], v[100:103], a[68:71]
buffer_load_dwordx4 v16, s[8:11], s43 offen offset:0 nt lds
v_mfma_f32_16x16x32_bf16 a[72:75], v[72:75], v[104:107], a[72:75]
ds_read_b64_tr_b16 v[172:173], v197 offset:8192
v_mfma_f32_16x16x32_bf16 a[76:79], v[72:75], v[108:111], a[76:79]
v_mfma_f32_16x16x32_bf16 a[80:83], v[72:75], v[112:115], a[80:83]
ds_read_b64_tr_b16 v[174:175], v197 offset:9216
v_mfma_f32_16x16x32_bf16 a[84:87], v[72:75], v[116:119], a[84:87]
buffer_load_dwordx4 v17, s[8:11], s43 offen offset:1024 nt lds
v_mfma_f32_16x16x32_bf16 a[88:91], v[72:75], v[120:123], a[88:91]
ds_read_b128 v[140:143], v193 offset:6144
v_mfma_f32_16x16x32_bf16 a[92:95], v[72:75], v[124:127], a[92:95]
v_mfma_f32_16x16x32_bf16 a[96:99], v[76:79], v[96:99], a[96:99]
ds_read_b64_tr_b16 v[176:177], v198 offset:8192
v_mfma_f32_16x16x32_bf16 a[100:103], v[76:79], v[100:103], a[100:103]
buffer_load_dwordx4 v18, s[8:11], s43 offen offset:2048 nt lds
v_mfma_f32_16x16x32_bf16 a[104:107], v[76:79], v[104:107], a[104:107]
ds_read_b64_tr_b16 v[178:179], v198 offset:9216
v_mfma_f32_16x16x32_bf16 a[108:111], v[76:79], v[108:111], a[108:111]
v_mfma_f32_16x16x32_bf16 a[112:115], v[76:79], v[112:115], a[112:115]
ds_read_b128 v[144:147], v193 offset:8192
v_mfma_f32_16x16x32_bf16 a[116:119], v[76:79], v[116:119], a[116:119]
buffer_load_dwordx4 v19, s[8:11], s43 offen offset:3072 nt lds
v_mfma_f32_16x16x32_bf16 a[120:123], v[76:79], v[120:123], a[120:123]
ds_read_b64_tr_b16 v[180:181], v199 offset:8192
v_mfma_f32_16x16x32_bf16 a[124:127], v[76:79], v[124:127], a[124:127]
v_mfma_f32_16x16x32_bf16 a[128:131], v[80:83], v[96:99], a[128:131]
ds_read_b64_tr_b16 v[182:183], v199 offset:9216
v_mfma_f32_16x16x32_bf16 a[132:135], v[80:83], v[100:103], a[132:135]
v_mfma_f32_16x16x32_bf16 a[136:139], v[80:83], v[104:107], a[136:139]
ds_read_b128 v[148:151], v193 offset:10240
v_mfma_f32_16x16x32_bf16 a[140:143], v[80:83], v[108:111], a[140:143]
v_mfma_f32_16x16x32_bf16 a[144:147], v[80:83], v[112:115], a[144:147]
ds_read_b64_tr_b16 v[184:185], v200 offset:8192
v_mfma_f32_16x16x32_bf16 a[148:151], v[80:83], v[116:119], a[148:151]
v_mfma_f32_16x16x32_bf16 a[152:155], v[80:83], v[120:123], a[152:155]
ds_read_b64_tr_b16 v[186:187], v200 offset:9216
v_mfma_f32_16x16x32_bf16 a[156:159], v[80:83], v[124:127], a[156:159]
v_mfma_f32_16x16x32_bf16 a[160:163], v[84:87], v[96:99], a[160:163]
s_add_u32 s42, s42, 524288
ds_read_b128 v[152:155], v193 offset:12288
v_mfma_f32_16x16x32_bf16 a[164:167], v[84:87], v[100:103], a[164:167]
s_add_u32 s43, s43, 524288
v_mfma_f32_16x16x32_bf16 a[168:171], v[84:87], v[104:107], a[168:171]
ds_read_b64_tr_b16 v[188:189], v201 offset:8192
v_mfma_f32_16x16x32_bf16 a[172:175], v[84:87], v[108:111], a[172:175]
v_mfma_f32_16x16x32_bf16 a[176:179], v[84:87], v[112:115], a[176:179]
s_add_u32 s44, s35, 65536
ds_read_b64_tr_b16 v[190:191], v201 offset:9216
v_mfma_f32_16x16x32_bf16 a[180:183], v[84:87], v[116:119], a[180:183]
s_cmp_ge_u32 s44, s39
s_cselect_b32 s45, s55, s54
v_mfma_f32_16x16x32_bf16 a[184:187], v[84:87], v[120:123], a[184:187]
ds_read_b128 v[156:159], v193 offset:14336
v_mfma_f32_16x16x32_bf16 a[188:191], v[84:87], v[124:127], a[188:191]
s_add_u32 s44, s36, 65536
v_mfma_f32_16x16x32_bf16 a[192:195], v[88:91], v[96:99], a[192:195]
v_mfma_f32_16x16x32_bf16 a[196:199], v[88:91], v[100:103], a[196:199]
s_cmp_ge_u32 s44, s39
s_cselect_b32 s34, s55, s54
v_mfma_f32_16x16x32_bf16 a[200:203], v[88:91], v[104:107], a[200:203]
v_add_u32 v194, s45, v194
v_mfma_f32_16x16x32_bf16 a[204:207], v[88:91], v[108:111], a[204:207]
v_add_u32 v195, s45, v195
v_mfma_f32_16x16x32_bf16 a[208:211], v[88:91], v[112:115], a[208:211]
v_add_u32 v196, s45, v196
v_mfma_f32_16x16x32_bf16 a[212:215], v[88:91], v[116:119], a[212:215]
v_add_u32 v197, s45, v197
v_mfma_f32_16x16x32_bf16 a[216:219], v[88:91], v[120:123], a[216:219]
v_add_u32 v198, s45, v198
v_mfma_f32_16x16x32_bf16 a[220:223], v[88:91], v[124:127], a[220:223]
v_add_u32 v199, s45, v199
v_mfma_f32_16x16x32_bf16 a[224:227], v[92:95], v[96:99], a[224:227]
v_add_u32 v200, s45, v200
v_mfma_f32_16x16x32_bf16 a[228:231], v[92:95], v[100:103], a[228:231]
v_add_u32 v201, s45, v201
v_mfma_f32_16x16x32_bf16 a[232:235], v[92:95], v[104:107], a[232:235]
v_add_u32 v192, s34, v192
v_mfma_f32_16x16x32_bf16 a[236:239], v[92:95], v[108:111], a[236:239]
v_add_u32 v193, s34, v193
v_mfma_f32_16x16x32_bf16 a[240:243], v[92:95], v[112:115], a[240:243]
v_mfma_f32_16x16x32_bf16 a[244:247], v[92:95], v[116:119], a[244:247]
v_mfma_f32_16x16x32_bf16 a[248:251], v[92:95], v[120:123], a[248:251]
v_mfma_f32_16x16x32_bf16 a[252:255], v[92:95], v[124:127], a[252:255]
s_waitcnt vmcnt(9) lgkmcnt(0)
s_barrier
v_mfma_f32_16x16x32_bf16 a[0:3], v[128:131], v[160:163], a[0:3]
s_mov_b32 m0, s35
ds_read_b64_tr_b16 v[96:97], v194 offset:0
v_mfma_f32_16x16x32_bf16 a[4:7], v[128:131], v[164:167], a[4:7]
buffer_load_dwordx4 v202, s[4:7], s40 offen offset:0 lds
v_mfma_f32_16x16x32_bf16 a[8:11], v[128:131], v[168:171], a[8:11]
ds_read_b64_tr_b16 v[98:99], v194 offset:1024
v_mfma_f32_16x16x32_bf16 a[12:15], v[128:131], v[172:175], a[12:15]
v_mfma_f32_16x16x32_bf16 a[16:19], v[128:131], v[176:179], a[16:19]
ds_read_b128 v[64:67], v192 offset:0
v_mfma_f32_16x16x32_bf16 a[20:23], v[128:131], v[180:183], a[20:23]
buffer_load_dwordx4 v203, s[4:7], s40 offen offset:1024 lds
v_mfma_f32_16x16x32_bf16 a[24:27], v[128:131], v[184:187], a[24:27]
ds_read_b64_tr_b16 v[100:101], v195 offset:0
v_mfma_f32_16x16x32_bf16 a[28:31], v[128:131], v[188:191], a[28:31]
v_mfma_f32_16x16x32_bf16 a[32:35], v[132:135], v[160:163], a[32:35]
ds_read_b64_tr_b16 v[102:103], v195 offset:1024
v_mfma_f32_16x16x32_bf16 a[36:39], v[132:135], v[164:167], a[36:39]
buffer_load_dwordx4 v204, s[4:7], s40 offen offset:2048 lds
v_mfma_f32_16x16x32_bf16 a[40:43], v[132:135], v[168:171], a[40:43]
ds_read_b128 v[68:71], v192 offset:2048
v_mfma_f32_16x16x32_bf16 a[44:47], v[132:135], v[172:175], a[44:47]
v_mfma_f32_16x16x32_bf16 a[48:51], v[132:135], v[176:179], a[48:51]
ds_read_b64_tr_b16 v[104:105], v196 offset:0
v_mfma_f32_16x16x32_bf16 a[52:55], v[132:135], v[180:183], a[52:55]
buffer_load_dwordx4 v205, s[4:7], s40 offen offset:3072 lds
v_mfma_f32_16x16x32_bf16 a[56:59], v[132:135], v[184:187], a[56:59]
ds_read_b64_tr_b16 v[106:107], v196 offset:1024
v_mfma_f32_16x16x32_bf16 a[60:63], v[132:135], v[188:191], a[60:63]
v_mfma_f32_16x16x32_bf16 a[64:67], v[136:139], v[160:163], a[64:67]
s_add_u32 m0, s35, 4096
ds_read_b128 v[72:75], v192 offset:4096
v_mfma_f32_16x16x32_bf16 a[68:71], v[136:139], v[164:167], a[68:71]
buffer_load_dwordx4 v202, s[4:7], s41 offen offset:0 lds
v_mfma_f32_16x16x32_bf16 a[72:75], v[136:139], v[168:171], a[72:75]
ds_read_b64_tr_b16 v[108:109], v197 offset:0
v_mfma_f32_16x16x32_bf16 a[76:79], v[136:139], v[172:175], a[76:79]
v_mfma_f32_16x16x32_bf16 a[80:83], v[136:139], v[176:179], a[80:83]
ds_read_b64_tr_b16 v[110:111], v197 offset:1024
v_mfma_f32_16x16x32_bf16 a[84:87], v[136:139], v[180:183], a[84:87]
buffer_load_dwordx4 v203, s[4:7], s41 offen offset:1024 lds
v_mfma_f32_16x16x32_bf16 a[88:91], v[136:139], v[184:187], a[88:91]
ds_read_b128 v[76:79], v192 offset:6144
v_mfma_f32_16x16x32_bf16 a[92:95], v[136:139], v[188:191], a[92:95]
v_mfma_f32_16x16x32_bf16 a[96:99], v[140:143], v[160:163], a[96:99]
ds_read_b64_tr_b16 v[112:113], v198 offset:0
v_mfma_f32_16x16x32_bf16 a[100:103], v[140:143], v[164:167], a[100:103]
buffer_load_dwordx4 v204, s[4:7], s41 offen offset:2048 lds
v_mfma_f32_16x16x32_bf16 a[104:107], v[140:143], v[168:171], a[104:107]
ds_read_b64_tr_b16 v[114:115], v198 offset:1024
v_mfma_f32_16x16x32_bf16 a[108:111], v[140:143], v[172:175], a[108:111]
v_mfma_f32_16x16x32_bf16 a[112:115], v[140:143], v[176:179], a[112:115]
ds_read_b128 v[80:83], v192 offset:8192
v_mfma_f32_16x16x32_bf16 a[116:119], v[140:143], v[180:183], a[116:119]
buffer_load_dwordx4 v205, s[4:7], s41 offen offset:3072 lds
v_mfma_f32_16x16x32_bf16 a[120:123], v[140:143], v[184:187], a[120:123]
ds_read_b64_tr_b16 v[116:117], v199 offset:0
v_mfma_f32_16x16x32_bf16 a[124:127], v[140:143], v[188:191], a[124:127]
v_mfma_f32_16x16x32_bf16 a[128:131], v[144:147], v[160:163], a[128:131]
ds_read_b64_tr_b16 v[118:119], v199 offset:1024
v_mfma_f32_16x16x32_bf16 a[132:135], v[144:147], v[164:167], a[132:135]
global_load_dword v35, v[206:207], off
v_mfma_f32_16x16x32_bf16 a[136:139], v[144:147], v[168:171], a[136:139]
ds_read_b128 v[84:87], v192 offset:10240
v_mfma_f32_16x16x32_bf16 a[140:143], v[144:147], v[172:175], a[140:143]
v_mfma_f32_16x16x32_bf16 a[144:147], v[144:147], v[176:179], a[144:147]
ds_read_b64_tr_b16 v[120:121], v200 offset:0
v_mfma_f32_16x16x32_bf16 a[148:151], v[144:147], v[180:183], a[148:151]
v_mfma_f32_16x16x32_bf16 a[152:155], v[144:147], v[184:187], a[152:155]
ds_read_b64_tr_b16 v[122:123], v200 offset:1024
v_mfma_f32_16x16x32_bf16 a[156:159], v[144:147], v[188:191], a[156:159]
v_mfma_f32_16x16x32_bf16 a[160:163], v[148:151], v[160:163], a[160:163]
s_add_u32 s40, s40, 128
ds_read_b128 v[88:91], v192 offset:12288
v_mfma_f32_16x16x32_bf16 a[164:167], v[148:151], v[164:167], a[164:167]
s_add_u32 s41, s41, 128
v_mfma_f32_16x16x32_bf16 a[168:171], v[148:151], v[168:171], a[168:171]
ds_read_b64_tr_b16 v[124:125], v201 offset:0
v_mfma_f32_16x16x32_bf16 a[172:175], v[148:151], v[172:175], a[172:175]
v_add_co_u32 v206, vcc, v208, v206
v_mfma_f32_16x16x32_bf16 a[176:179], v[148:151], v[176:179], a[176:179]
ds_read_b64_tr_b16 v[126:127], v201 offset:1024
v_mfma_f32_16x16x32_bf16 a[180:183], v[148:151], v[180:183], a[180:183]
v_addc_co_u32 v207, vcc, 0, v207, vcc
v_mfma_f32_16x16x32_bf16 a[184:187], v[148:151], v[184:187], a[184:187]
ds_read_b128 v[92:95], v192 offset:14336
v_mfma_f32_16x16x32_bf16 a[188:191], v[148:151], v[188:191], a[188:191]
v_mfma_f32_16x16x32_bf16 a[192:195], v[152:155], v[160:163], a[192:195]
v_mfma_f32_16x16x32_bf16 a[196:199], v[152:155], v[164:167], a[196:199]
v_mfma_f32_16x16x32_bf16 a[200:203], v[152:155], v[168:171], a[200:203]
v_mfma_f32_16x16x32_bf16 a[204:207], v[152:155], v[172:175], a[204:207]
v_mfma_f32_16x16x32_bf16 a[208:211], v[152:155], v[176:179], a[208:211]
v_mfma_f32_16x16x32_bf16 a[212:215], v[152:155], v[180:183], a[212:215]
v_mfma_f32_16x16x32_bf16 a[216:219], v[152:155], v[184:187], a[216:219]
v_mfma_f32_16x16x32_bf16 a[220:223], v[152:155], v[188:191], a[220:223]
v_mfma_f32_16x16x32_bf16 a[224:227], v[156:159], v[160:163], a[224:227]
v_mfma_f32_16x16x32_bf16 a[228:231], v[156:159], v[164:167], a[228:231]
s_mov_b32 s37, s36
v_mfma_f32_16x16x32_bf16 a[232:235], v[156:159], v[168:171], a[232:235]
s_add_u32 s35, s35, s45
v_mfma_f32_16x16x32_bf16 a[236:239], v[156:159], v[172:175], a[236:239]
s_add_u32 s36, s36, s34
v_mfma_f32_16x16x32_bf16 a[240:243], v[156:159], v[176:179], a[240:243]
s_add_u32 s38, s38, 1
v_mfma_f32_16x16x32_bf16 a[244:247], v[156:159], v[180:183], a[244:247]
s_cmp_lt_u32 s38, 6
v_mfma_f32_16x16x32_bf16 a[248:251], v[156:159], v[184:187], a[248:251]
v_mfma_f32_16x16x32_bf16 a[252:255], v[156:159], v[188:191], a[252:255]
s_waitcnt lgkmcnt(0)
s_cbranch_scc1 .Lp1_vloop
v_mfma_f32_16x16x32_bf16 a[0:3], v[64:67], v[96:99], a[0:3]
s_add_u32 m0, s92, 32768
ds_read_b64_tr_b16 v[160:161], v194 offset:8192
v_mfma_f32_16x16x32_bf16 a[4:7], v[64:67], v[100:103], a[4:7]
buffer_load_dwordx4 v20, s[12:15], s24 offen offset:0 lds
v_mfma_f32_16x16x32_bf16 a[8:11], v[64:67], v[104:107], a[8:11]
ds_read_b64_tr_b16 v[162:163], v194 offset:9216
v_mfma_f32_16x16x32_bf16 a[12:15], v[64:67], v[108:111], a[12:15]
v_mfma_f32_16x16x32_bf16 a[16:19], v[64:67], v[112:115], a[16:19]
ds_read_b128 v[128:131], v193 offset:0
v_mfma_f32_16x16x32_bf16 a[20:23], v[64:67], v[116:119], a[20:23]
buffer_load_dwordx4 v21, s[12:15], s24 offen offset:1024 lds
v_mfma_f32_16x16x32_bf16 a[24:27], v[64:67], v[120:123], a[24:27]
ds_read_b64_tr_b16 v[164:165], v195 offset:8192
v_mfma_f32_16x16x32_bf16 a[28:31], v[64:67], v[124:127], a[28:31]
v_mfma_f32_16x16x32_bf16 a[32:35], v[68:71], v[96:99], a[32:35]
ds_read_b64_tr_b16 v[166:167], v195 offset:9216
v_mfma_f32_16x16x32_bf16 a[36:39], v[68:71], v[100:103], a[36:39]
buffer_load_dwordx4 v22, s[12:15], s24 offen offset:2048 lds
v_mfma_f32_16x16x32_bf16 a[40:43], v[68:71], v[104:107], a[40:43]
ds_read_b128 v[132:135], v193 offset:2048
v_mfma_f32_16x16x32_bf16 a[44:47], v[68:71], v[108:111], a[44:47]
v_mfma_f32_16x16x32_bf16 a[48:51], v[68:71], v[112:115], a[48:51]
ds_read_b64_tr_b16 v[168:169], v196 offset:8192
v_mfma_f32_16x16x32_bf16 a[52:55], v[68:71], v[116:119], a[52:55]
buffer_load_dwordx4 v23, s[12:15], s24 offen offset:3072 lds
v_mfma_f32_16x16x32_bf16 a[56:59], v[68:71], v[120:123], a[56:59]
ds_read_b64_tr_b16 v[170:171], v196 offset:9216
v_mfma_f32_16x16x32_bf16 a[60:63], v[68:71], v[124:127], a[60:63]
v_mfma_f32_16x16x32_bf16 a[64:67], v[72:75], v[96:99], a[64:67]
s_add_u32 m0, s92, 36864
ds_read_b128 v[136:139], v193 offset:4096
v_mfma_f32_16x16x32_bf16 a[68:71], v[72:75], v[100:103], a[68:71]
buffer_load_dwordx4 v20, s[12:15], s25 offen offset:0 lds
v_mfma_f32_16x16x32_bf16 a[72:75], v[72:75], v[104:107], a[72:75]
ds_read_b64_tr_b16 v[172:173], v197 offset:8192
v_mfma_f32_16x16x32_bf16 a[76:79], v[72:75], v[108:111], a[76:79]
v_mfma_f32_16x16x32_bf16 a[80:83], v[72:75], v[112:115], a[80:83]
ds_read_b64_tr_b16 v[174:175], v197 offset:9216
v_mfma_f32_16x16x32_bf16 a[84:87], v[72:75], v[116:119], a[84:87]
buffer_load_dwordx4 v21, s[12:15], s25 offen offset:1024 lds
v_mfma_f32_16x16x32_bf16 a[88:91], v[72:75], v[120:123], a[88:91]
ds_read_b128 v[140:143], v193 offset:6144
v_mfma_f32_16x16x32_bf16 a[92:95], v[72:75], v[124:127], a[92:95]
v_mfma_f32_16x16x32_bf16 a[96:99], v[76:79], v[96:99], a[96:99]
ds_read_b64_tr_b16 v[176:177], v198 offset:8192
v_mfma_f32_16x16x32_bf16 a[100:103], v[76:79], v[100:103], a[100:103]
buffer_load_dwordx4 v22, s[12:15], s25 offen offset:2048 lds
v_mfma_f32_16x16x32_bf16 a[104:107], v[76:79], v[104:107], a[104:107]
ds_read_b64_tr_b16 v[178:179], v198 offset:9216
v_mfma_f32_16x16x32_bf16 a[108:111], v[76:79], v[108:111], a[108:111]
v_mfma_f32_16x16x32_bf16 a[112:115], v[76:79], v[112:115], a[112:115]
ds_read_b128 v[144:147], v193 offset:8192
v_mfma_f32_16x16x32_bf16 a[116:119], v[76:79], v[116:119], a[116:119]
buffer_load_dwordx4 v23, s[12:15], s25 offen offset:3072 lds
v_mfma_f32_16x16x32_bf16 a[120:123], v[76:79], v[120:123], a[120:123]
ds_read_b64_tr_b16 v[180:181], v199 offset:8192
v_mfma_f32_16x16x32_bf16 a[124:127], v[76:79], v[124:127], a[124:127]
v_mfma_f32_16x16x32_bf16 a[128:131], v[80:83], v[96:99], a[128:131]
ds_read_b64_tr_b16 v[182:183], v199 offset:9216
v_mfma_f32_16x16x32_bf16 a[132:135], v[80:83], v[100:103], a[132:135]
v_mfma_f32_16x16x32_bf16 a[136:139], v[80:83], v[104:107], a[136:139]
ds_read_b128 v[148:151], v193 offset:10240
v_mfma_f32_16x16x32_bf16 a[140:143], v[80:83], v[108:111], a[140:143]
v_mfma_f32_16x16x32_bf16 a[144:147], v[80:83], v[112:115], a[144:147]
ds_read_b64_tr_b16 v[184:185], v200 offset:8192
v_mfma_f32_16x16x32_bf16 a[148:151], v[80:83], v[116:119], a[148:151]
v_mfma_f32_16x16x32_bf16 a[152:155], v[80:83], v[120:123], a[152:155]
ds_read_b64_tr_b16 v[186:187], v200 offset:9216
v_mfma_f32_16x16x32_bf16 a[156:159], v[80:83], v[124:127], a[156:159]
v_mfma_f32_16x16x32_bf16 a[160:163], v[84:87], v[96:99], a[160:163]
s_add_u32 s24, s24, 128
s_add_u32 s25, s25, 128
ds_read_b128 v[152:155], v193 offset:12288
v_mfma_f32_16x16x32_bf16 a[164:167], v[84:87], v[100:103], a[164:167]
v_mfma_f32_16x16x32_bf16 a[168:171], v[84:87], v[104:107], a[168:171]
ds_read_b64_tr_b16 v[188:189], v201 offset:8192
v_mfma_f32_16x16x32_bf16 a[172:175], v[84:87], v[108:111], a[172:175]
v_mfma_f32_16x16x32_bf16 a[176:179], v[84:87], v[112:115], a[176:179]
s_add_u32 s44, s35, 65536
ds_read_b64_tr_b16 v[190:191], v201 offset:9216
v_mfma_f32_16x16x32_bf16 a[180:183], v[84:87], v[116:119], a[180:183]
s_cmp_ge_u32 s44, s39
s_cselect_b32 s45, s55, s54
v_mfma_f32_16x16x32_bf16 a[184:187], v[84:87], v[120:123], a[184:187]
ds_read_b128 v[156:159], v193 offset:14336
v_mfma_f32_16x16x32_bf16 a[188:191], v[84:87], v[124:127], a[188:191]
s_add_u32 s44, s36, 65536
v_mfma_f32_16x16x32_bf16 a[192:195], v[88:91], v[96:99], a[192:195]
v_mfma_f32_16x16x32_bf16 a[196:199], v[88:91], v[100:103], a[196:199]
s_cmp_ge_u32 s44, s39
s_cselect_b32 s34, s55, s54
v_mfma_f32_16x16x32_bf16 a[200:203], v[88:91], v[104:107], a[200:203]
v_add_u32 v194, s45, v194
v_mfma_f32_16x16x32_bf16 a[204:207], v[88:91], v[108:111], a[204:207]
v_add_u32 v195, s45, v195
v_mfma_f32_16x16x32_bf16 a[208:211], v[88:91], v[112:115], a[208:211]
v_add_u32 v196, s45, v196
v_mfma_f32_16x16x32_bf16 a[212:215], v[88:91], v[116:119], a[212:215]
v_add_u32 v197, s45, v197
v_mfma_f32_16x16x32_bf16 a[216:219], v[88:91], v[120:123], a[216:219]
v_add_u32 v198, s45, v198
v_mfma_f32_16x16x32_bf16 a[220:223], v[88:91], v[124:127], a[220:223]
v_add_u32 v199, s45, v199
v_mfma_f32_16x16x32_bf16 a[224:227], v[92:95], v[96:99], a[224:227]
v_add_u32 v200, s45, v200
v_mfma_f32_16x16x32_bf16 a[228:231], v[92:95], v[100:103], a[228:231]
v_add_u32 v201, s45, v201
v_mfma_f32_16x16x32_bf16 a[232:235], v[92:95], v[104:107], a[232:235]
v_add_u32 v192, s34, v192
v_mfma_f32_16x16x32_bf16 a[236:239], v[92:95], v[108:111], a[236:239]
v_add_u32 v193, s34, v193
v_mfma_f32_16x16x32_bf16 a[240:243], v[92:95], v[112:115], a[240:243]
v_mfma_f32_16x16x32_bf16 a[244:247], v[92:95], v[116:119], a[244:247]
v_mfma_f32_16x16x32_bf16 a[248:251], v[92:95], v[120:123], a[248:251]
v_mfma_f32_16x16x32_bf16 a[252:255], v[92:95], v[124:127], a[252:255]
s_waitcnt vmcnt(9) lgkmcnt(0)
s_barrier
v_mfma_f32_16x16x32_bf16 a[0:3], v[128:131], v[160:163], a[0:3]
s_add_u32 m0, s91, 65536
ds_read_b64_tr_b16 v[96:97], v194 offset:0
v_mfma_f32_16x16x32_bf16 a[4:7], v[128:131], v[164:167], a[4:7]
buffer_load_dwordx4 v16, s[16:19], s26 offen offset:0 lds
v_mfma_f32_16x16x32_bf16 a[8:11], v[128:131], v[168:171], a[8:11]
ds_read_b64_tr_b16 v[98:99], v194 offset:1024
v_mfma_f32_16x16x32_bf16 a[12:15], v[128:131], v[172:175], a[12:15]
v_mfma_f32_16x16x32_bf16 a[16:19], v[128:131], v[176:179], a[16:19]
ds_read_b128 v[64:67], v192 offset:0
v_mfma_f32_16x16x32_bf16 a[20:23], v[128:131], v[180:183], a[20:23]
buffer_load_dwordx4 v17, s[16:19], s26 offen offset:1024 lds
v_mfma_f32_16x16x32_bf16 a[24:27], v[128:131], v[184:187], a[24:27]
ds_read_b64_tr_b16 v[100:101], v195 offset:0
v_mfma_f32_16x16x32_bf16 a[28:31], v[128:131], v[188:191], a[28:31]
v_mfma_f32_16x16x32_bf16 a[32:35], v[132:135], v[160:163], a[32:35]
ds_read_b64_tr_b16 v[102:103], v195 offset:1024
v_mfma_f32_16x16x32_bf16 a[36:39], v[132:135], v[164:167], a[36:39]
buffer_load_dwordx4 v18, s[16:19], s26 offen offset:2048 lds
v_mfma_f32_16x16x32_bf16 a[40:43], v[132:135], v[168:171], a[40:43]
ds_read_b128 v[68:71], v192 offset:2048
v_mfma_f32_16x16x32_bf16 a[44:47], v[132:135], v[172:175], a[44:47]
v_mfma_f32_16x16x32_bf16 a[48:51], v[132:135], v[176:179], a[48:51]
ds_read_b64_tr_b16 v[104:105], v196 offset:0
v_mfma_f32_16x16x32_bf16 a[52:55], v[132:135], v[180:183], a[52:55]
buffer_load_dwordx4 v19, s[16:19], s26 offen offset:3072 lds
v_mfma_f32_16x16x32_bf16 a[56:59], v[132:135], v[184:187], a[56:59]
ds_read_b64_tr_b16 v[106:107], v196 offset:1024
v_mfma_f32_16x16x32_bf16 a[60:63], v[132:135], v[188:191], a[60:63]
s_add_u32 s26, s26, 524288
v_mfma_f32_16x16x32_bf16 a[64:67], v[136:139], v[160:163], a[64:67]
s_add_u32 m0, s91, 81920
ds_read_b128 v[72:75], v192 offset:4096
v_mfma_f32_16x16x32_bf16 a[68:71], v[136:139], v[164:167], a[68:71]
buffer_load_dwordx4 v16, s[16:19], s26 offen offset:0 lds
v_mfma_f32_16x16x32_bf16 a[72:75], v[136:139], v[168:171], a[72:75]
ds_read_b64_tr_b16 v[108:109], v197 offset:0
v_mfma_f32_16x16x32_bf16 a[76:79], v[136:139], v[172:175], a[76:79]
v_mfma_f32_16x16x32_bf16 a[80:83], v[136:139], v[176:179], a[80:83]
ds_read_b64_tr_b16 v[110:111], v197 offset:1024
v_mfma_f32_16x16x32_bf16 a[84:87], v[136:139], v[180:183], a[84:87]
buffer_load_dwordx4 v17, s[16:19], s26 offen offset:1024 lds
v_mfma_f32_16x16x32_bf16 a[88:91], v[136:139], v[184:187], a[88:91]
ds_read_b128 v[76:79], v192 offset:6144
v_mfma_f32_16x16x32_bf16 a[92:95], v[136:139], v[188:191], a[92:95]
v_mfma_f32_16x16x32_bf16 a[96:99], v[140:143], v[160:163], a[96:99]
ds_read_b64_tr_b16 v[112:113], v198 offset:0
v_mfma_f32_16x16x32_bf16 a[100:103], v[140:143], v[164:167], a[100:103]
buffer_load_dwordx4 v18, s[16:19], s26 offen offset:2048 lds
v_mfma_f32_16x16x32_bf16 a[104:107], v[140:143], v[168:171], a[104:107]
ds_read_b64_tr_b16 v[114:115], v198 offset:1024
v_mfma_f32_16x16x32_bf16 a[108:111], v[140:143], v[172:175], a[108:111]
v_mfma_f32_16x16x32_bf16 a[112:115], v[140:143], v[176:179], a[112:115]
ds_read_b128 v[80:83], v192 offset:8192
v_mfma_f32_16x16x32_bf16 a[116:119], v[140:143], v[180:183], a[116:119]
buffer_load_dwordx4 v19, s[16:19], s26 offen offset:3072 lds
v_mfma_f32_16x16x32_bf16 a[120:123], v[140:143], v[184:187], a[120:123]
ds_read_b64_tr_b16 v[116:117], v199 offset:0
v_mfma_f32_16x16x32_bf16 a[124:127], v[140:143], v[188:191], a[124:127]
s_add_u32 s26, s26, 524288
v_mfma_f32_16x16x32_bf16 a[128:131], v[144:147], v[160:163], a[128:131]
ds_read_b64_tr_b16 v[118:119], v199 offset:1024
v_mfma_f32_16x16x32_bf16 a[132:135], v[144:147], v[164:167], a[132:135]
global_load_dword v35, v[206:207], off
v_mfma_f32_16x16x32_bf16 a[136:139], v[144:147], v[168:171], a[136:139]
ds_read_b128 v[84:87], v192 offset:10240
v_mfma_f32_16x16x32_bf16 a[140:143], v[144:147], v[172:175], a[140:143]
v_mfma_f32_16x16x32_bf16 a[144:147], v[144:147], v[176:179], a[144:147]
ds_read_b64_tr_b16 v[120:121], v200 offset:0
v_mfma_f32_16x16x32_bf16 a[148:151], v[144:147], v[180:183], a[148:151]
v_add_u32 v11, 8, v11
v_and_b32 v11, -8, v11
v_mfma_f32_16x16x32_bf16 a[152:155], v[144:147], v[184:187], a[152:155]
ds_read_b64_tr_b16 v[122:123], v200 offset:1024
v_mfma_f32_16x16x32_bf16 a[156:159], v[144:147], v[188:191], a[156:159]
v_mfma_f32_16x16x32_bf16 a[160:163], v[148:151], v[160:163], a[160:163]
ds_read_b128 v[88:91], v192 offset:12288
v_mfma_f32_16x16x32_bf16 a[164:167], v[148:151], v[164:167], a[164:167]
s_nop 0
v_or_b32 v0, 0, v11
v_mfma_f32_16x16x32_bf16 a[168:171], v[148:151], v[168:171], a[168:171]
ds_read_b64_tr_b16 v[124:125], v201 offset:0
v_mfma_f32_16x16x32_bf16 a[172:175], v[148:151], v[172:175], a[172:175]
v_add_co_u32 v206, vcc, v208, v206
v_mfma_f32_16x16x32_bf16 a[176:179], v[148:151], v[176:179], a[176:179]
ds_read_b64_tr_b16 v[126:127], v201 offset:1024
v_mfma_f32_16x16x32_bf16 a[180:183], v[148:151], v[180:183], a[180:183]
v_addc_co_u32 v207, vcc, 0, v207, vcc
v_mfma_f32_16x16x32_bf16 a[184:187], v[148:151], v[184:187], a[184:187]
ds_read_b128 v[92:95], v192 offset:14336
v_mfma_f32_16x16x32_bf16 a[188:191], v[148:151], v[188:191], a[188:191]
buffer_store_dword v0, v6, s[60:63], 0 offen sc1
v_mfma_f32_16x16x32_bf16 a[192:195], v[152:155], v[160:163], a[192:195]
v_mfma_f32_16x16x32_bf16 a[196:199], v[152:155], v[164:167], a[196:199]
v_mfma_f32_16x16x32_bf16 a[200:203], v[152:155], v[168:171], a[200:203]
v_mfma_f32_16x16x32_bf16 a[204:207], v[152:155], v[172:175], a[204:207]
v_mfma_f32_16x16x32_bf16 a[208:211], v[152:155], v[176:179], a[208:211]
v_mfma_f32_16x16x32_bf16 a[212:215], v[152:155], v[180:183], a[212:215]
v_mfma_f32_16x16x32_bf16 a[216:219], v[152:155], v[184:187], a[216:219]
v_mfma_f32_16x16x32_bf16 a[220:223], v[152:155], v[188:191], a[220:223]
v_mfma_f32_16x16x32_bf16 a[224:227], v[156:159], v[160:163], a[224:227]
v_mfma_f32_16x16x32_bf16 a[228:231], v[156:159], v[164:167], a[228:231]
s_mov_b32 s37, s36
v_mfma_f32_16x16x32_bf16 a[232:235], v[156:159], v[168:171], a[232:235]
s_add_u32 s35, s35, s45
v_mfma_f32_16x16x32_bf16 a[236:239], v[156:159], v[172:175], a[236:239]
s_add_u32 s36, s36, s34
v_mfma_f32_16x16x32_bf16 a[240:243], v[156:159], v[176:179], a[240:243]
v_mfma_f32_16x16x32_bf16 a[244:247], v[156:159], v[180:183], a[244:247]
v_mfma_f32_16x16x32_bf16 a[248:251], v[156:159], v[184:187], a[248:251]
v_mfma_f32_16x16x32_bf16 a[252:255], v[156:159], v[188:191], a[252:255]
s_waitcnt lgkmcnt(0)
v_mfma_f32_16x16x32_bf16 a[0:3], v[64:67], v[96:99], a[0:3]
s_add_u32 m0, s92, 98304
ds_read_b64_tr_b16 v[160:161], v194 offset:8192
v_mfma_f32_16x16x32_bf16 a[4:7], v[64:67], v[100:103], a[4:7]
buffer_load_dwordx4 v20, s[12:15], s24 offen offset:0 lds
v_mfma_f32_16x16x32_bf16 a[8:11], v[64:67], v[104:107], a[8:11]
ds_read_b64_tr_b16 v[162:163], v194 offset:9216
v_mfma_f32_16x16x32_bf16 a[12:15], v[64:67], v[108:111], a[12:15]
v_mfma_f32_16x16x32_bf16 a[16:19], v[64:67], v[112:115], a[16:19]
ds_read_b128 v[128:131], v193 offset:0
v_mfma_f32_16x16x32_bf16 a[20:23], v[64:67], v[116:119], a[20:23]
buffer_load_dwordx4 v21, s[12:15], s24 offen offset:1024 lds
v_mfma_f32_16x16x32_bf16 a[24:27], v[64:67], v[120:123], a[24:27]
ds_read_b64_tr_b16 v[164:165], v195 offset:8192
v_mfma_f32_16x16x32_bf16 a[28:31], v[64:67], v[124:127], a[28:31]
v_mfma_f32_16x16x32_bf16 a[32:35], v[68:71], v[96:99], a[32:35]
ds_read_b64_tr_b16 v[166:167], v195 offset:9216
v_mfma_f32_16x16x32_bf16 a[36:39], v[68:71], v[100:103], a[36:39]
buffer_load_dwordx4 v22, s[12:15], s24 offen offset:2048 lds
v_mfma_f32_16x16x32_bf16 a[40:43], v[68:71], v[104:107], a[40:43]
ds_read_b128 v[132:135], v193 offset:2048
v_mfma_f32_16x16x32_bf16 a[44:47], v[68:71], v[108:111], a[44:47]
v_mfma_f32_16x16x32_bf16 a[48:51], v[68:71], v[112:115], a[48:51]
ds_read_b64_tr_b16 v[168:169], v196 offset:8192
v_mfma_f32_16x16x32_bf16 a[52:55], v[68:71], v[116:119], a[52:55]
buffer_load_dwordx4 v23, s[12:15], s24 offen offset:3072 lds
v_mfma_f32_16x16x32_bf16 a[56:59], v[68:71], v[120:123], a[56:59]
ds_read_b64_tr_b16 v[170:171], v196 offset:9216
v_mfma_f32_16x16x32_bf16 a[60:63], v[68:71], v[124:127], a[60:63]
v_mfma_f32_16x16x32_bf16 a[64:67], v[72:75], v[96:99], a[64:67]
s_add_u32 m0, s92, 102400
ds_read_b128 v[136:139], v193 offset:4096
v_mfma_f32_16x16x32_bf16 a[68:71], v[72:75], v[100:103], a[68:71]
buffer_load_dwordx4 v20, s[12:15], s25 offen offset:0 lds
v_mfma_f32_16x16x32_bf16 a[72:75], v[72:75], v[104:107], a[72:75]
ds_read_b64_tr_b16 v[172:173], v197 offset:8192
v_mfma_f32_16x16x32_bf16 a[76:79], v[72:75], v[108:111], a[76:79]
v_mfma_f32_16x16x32_bf16 a[80:83], v[72:75], v[112:115], a[80:83]
ds_read_b64_tr_b16 v[174:175], v197 offset:9216
v_mfma_f32_16x16x32_bf16 a[84:87], v[72:75], v[116:119], a[84:87]
buffer_load_dwordx4 v21, s[12:15], s25 offen offset:1024 lds
v_mfma_f32_16x16x32_bf16 a[88:91], v[72:75], v[120:123], a[88:91]
ds_read_b128 v[140:143], v193 offset:6144
v_mfma_f32_16x16x32_bf16 a[92:95], v[72:75], v[124:127], a[92:95]
v_mfma_f32_16x16x32_bf16 a[96:99], v[76:79], v[96:99], a[96:99]
ds_read_b64_tr_b16 v[176:177], v198 offset:8192
v_mfma_f32_16x16x32_bf16 a[100:103], v[76:79], v[100:103], a[100:103]
buffer_load_dwordx4 v22, s[12:15], s25 offen offset:2048 lds
v_mfma_f32_16x16x32_bf16 a[104:107], v[76:79], v[104:107], a[104:107]
ds_read_b64_tr_b16 v[178:179], v198 offset:9216
v_mfma_f32_16x16x32_bf16 a[108:111], v[76:79], v[108:111], a[108:111]
v_mfma_f32_16x16x32_bf16 a[112:115], v[76:79], v[112:115], a[112:115]
ds_read_b128 v[144:147], v193 offset:8192
v_mfma_f32_16x16x32_bf16 a[116:119], v[76:79], v[116:119], a[116:119]
buffer_load_dwordx4 v23, s[12:15], s25 offen offset:3072 lds
v_mfma_f32_16x16x32_bf16 a[120:123], v[76:79], v[120:123], a[120:123]
ds_read_b64_tr_b16 v[180:181], v199 offset:8192
v_mfma_f32_16x16x32_bf16 a[124:127], v[76:79], v[124:127], a[124:127]
v_mfma_f32_16x16x32_bf16 a[128:131], v[80:83], v[96:99], a[128:131]
ds_read_b64_tr_b16 v[182:183], v199 offset:9216
v_mfma_f32_16x16x32_bf16 a[132:135], v[80:83], v[100:103], a[132:135]
v_mfma_f32_16x16x32_bf16 a[136:139], v[80:83], v[104:107], a[136:139]
ds_read_b128 v[148:151], v193 offset:10240
v_mfma_f32_16x16x32_bf16 a[140:143], v[80:83], v[108:111], a[140:143]
v_mfma_f32_16x16x32_bf16 a[144:147], v[80:83], v[112:115], a[144:147]
ds_read_b64_tr_b16 v[184:185], v200 offset:8192
v_mfma_f32_16x16x32_bf16 a[148:151], v[80:83], v[116:119], a[148:151]
v_mfma_f32_16x16x32_bf16 a[152:155], v[80:83], v[120:123], a[152:155]
ds_read_b64_tr_b16 v[186:187], v200 offset:9216
v_mfma_f32_16x16x32_bf16 a[156:159], v[80:83], v[124:127], a[156:159]
v_mfma_f32_16x16x32_bf16 a[160:163], v[84:87], v[96:99], a[160:163]
s_add_u32 s24, s24, 128
s_add_u32 s25, s25, 128
ds_read_b128 v[152:155], v193 offset:12288
v_mfma_f32_16x16x32_bf16 a[164:167], v[84:87], v[100:103], a[164:167]
v_mfma_f32_16x16x32_bf16 a[168:171], v[84:87], v[104:107], a[168:171]
ds_read_b64_tr_b16 v[188:189], v201 offset:8192
v_mfma_f32_16x16x32_bf16 a[172:175], v[84:87], v[108:111], a[172:175]
v_mfma_f32_16x16x32_bf16 a[176:179], v[84:87], v[112:115], a[176:179]
ds_read_b64_tr_b16 v[190:191], v201 offset:9216
v_mfma_f32_16x16x32_bf16 a[180:183], v[84:87], v[116:119], a[180:183]
v_mfma_f32_16x16x32_bf16 a[184:187], v[84:87], v[120:123], a[184:187]
ds_read_b128 v[156:159], v193 offset:14336
v_mfma_f32_16x16x32_bf16 a[188:191], v[84:87], v[124:127], a[188:191]
v_mfma_f32_16x16x32_bf16 a[192:195], v[88:91], v[96:99], a[192:195]
v_mfma_f32_16x16x32_bf16 a[196:199], v[88:91], v[100:103], a[196:199]
v_mfma_f32_16x16x32_bf16 a[200:203], v[88:91], v[104:107], a[200:203]
v_mfma_f32_16x16x32_bf16 a[204:207], v[88:91], v[108:111], a[204:207]
v_mfma_f32_16x16x32_bf16 a[208:211], v[88:91], v[112:115], a[208:211]
v_mfma_f32_16x16x32_bf16 a[212:215], v[88:91], v[116:119], a[212:215]
v_mfma_f32_16x16x32_bf16 a[216:219], v[88:91], v[120:123], a[216:219]
v_mfma_f32_16x16x32_bf16 a[220:223], v[88:91], v[124:127], a[220:223]
v_mfma_f32_16x16x32_bf16 a[224:227], v[92:95], v[96:99], a[224:227]
v_mfma_f32_16x16x32_bf16 a[228:231], v[92:95], v[100:103], a[228:231]
v_mfma_f32_16x16x32_bf16 a[232:235], v[92:95], v[104:107], a[232:235]
v_mfma_f32_16x16x32_bf16 a[236:239], v[92:95], v[108:111], a[236:239]
v_mfma_f32_16x16x32_bf16 a[240:243], v[92:95], v[112:115], a[240:243]
v_mfma_f32_16x16x32_bf16 a[244:247], v[92:95], v[116:119], a[244:247]
v_mfma_f32_16x16x32_bf16 a[248:251], v[92:95], v[120:123], a[248:251]
v_mfma_f32_16x16x32_bf16 a[252:255], v[92:95], v[124:127], a[252:255]
s_waitcnt vmcnt(14) lgkmcnt(0)
s_barrier
v_mfma_f32_16x16x32_bf16 a[0:3], v[128:131], v[160:163], a[0:3]
s_add_u32 m0, s91, 0
ds_read_b64_tr_b16 v[64:65], v26 offset:0
v_mfma_f32_16x16x32_bf16 a[4:7], v[128:131], v[164:167], a[4:7]
buffer_load_dwordx4 v16, s[16:19], s26 offen offset:0 lds
v_mfma_f32_16x16x32_bf16 a[8:11], v[128:131], v[168:171], a[8:11]
ds_read_b64_tr_b16 v[66:67], v26 offset:1024
v_mfma_f32_16x16x32_bf16 a[12:15], v[128:131], v[172:175], a[12:15]
v_mfma_f32_16x16x32_bf16 a[16:19], v[128:131], v[176:179], a[16:19]
ds_read_b64_tr_b16 v[68:69], v27 offset:0
v_mfma_f32_16x16x32_bf16 a[20:23], v[128:131], v[180:183], a[20:23]
buffer_load_dwordx4 v17, s[16:19], s26 offen offset:1024 lds
v_mfma_f32_16x16x32_bf16 a[24:27], v[128:131], v[184:187], a[24:27]
ds_read_b64_tr_b16 v[70:71], v27 offset:1024
v_mfma_f32_16x16x32_bf16 a[28:31], v[128:131], v[188:191], a[28:31]
v_mfma_f32_16x16x32_bf16 a[32:35], v[132:135], v[160:163], a[32:35]
ds_read_b64_tr_b16 v[72:73], v28 offset:0
v_mfma_f32_16x16x32_bf16 a[36:39], v[132:135], v[164:167], a[36:39]
buffer_load_dwordx4 v18, s[16:19], s26 offen offset:2048 lds
v_mfma_f32_16x16x32_bf16 a[40:43], v[132:135], v[168:171], a[40:43]
ds_read_b64_tr_b16 v[74:75], v28 offset:1024
v_mfma_f32_16x16x32_bf16 a[44:47], v[132:135], v[172:175], a[44:47]
v_mfma_f32_16x16x32_bf16 a[48:51], v[132:135], v[176:179], a[48:51]
ds_read_b64_tr_b16 v[76:77], v29 offset:0
v_mfma_f32_16x16x32_bf16 a[52:55], v[132:135], v[180:183], a[52:55]
buffer_load_dwordx4 v19, s[16:19], s26 offen offset:3072 lds
v_mfma_f32_16x16x32_bf16 a[56:59], v[132:135], v[184:187], a[56:59]
ds_read_b64_tr_b16 v[78:79], v29 offset:1024
v_mfma_f32_16x16x32_bf16 a[60:63], v[132:135], v[188:191], a[60:63]
s_add_u32 s26, s26, 524288
v_mfma_f32_16x16x32_bf16 a[64:67], v[136:139], v[160:163], a[64:67]
s_add_u32 m0, s92, 131072
ds_read_b64_tr_b16 v[80:81], v30 offset:0
v_mfma_f32_16x16x32_bf16 a[68:71], v[136:139], v[164:167], a[68:71]
buffer_load_dwordx4 v20, s[12:15], s24 offen offset:0 lds
v_mfma_f32_16x16x32_bf16 a[72:75], v[136:139], v[168:171], a[72:75]
ds_read_b64_tr_b16 v[82:83], v30 offset:1024
v_mfma_f32_16x16x32_bf16 a[76:79], v[136:139], v[172:175], a[76:79]
v_mfma_f32_16x16x32_bf16 a[80:83], v[136:139], v[176:179], a[80:83]
ds_read_b64_tr_b16 v[84:85], v31 offset:0
v_mfma_f32_16x16x32_bf16 a[84:87], v[136:139], v[180:183], a[84:87]
buffer_load_dwordx4 v21, s[12:15], s24 offen offset:1024 lds
v_mfma_f32_16x16x32_bf16 a[88:91], v[136:139], v[184:187], a[88:91]
ds_read_b64_tr_b16 v[86:87], v31 offset:1024
v_mfma_f32_16x16x32_bf16 a[92:95], v[136:139], v[188:191], a[92:95]
v_mfma_f32_16x16x32_bf16 a[96:99], v[140:143], v[160:163], a[96:99]
ds_read_b64_tr_b16 v[88:89], v32 offset:0
v_mfma_f32_16x16x32_bf16 a[100:103], v[140:143], v[164:167], a[100:103]
buffer_load_dwordx4 v22, s[12:15], s24 offen offset:2048 lds
v_mfma_f32_16x16x32_bf16 a[104:107], v[140:143], v[168:171], a[104:107]
ds_read_b64_tr_b16 v[90:91], v32 offset:1024
v_mfma_f32_16x16x32_bf16 a[108:111], v[140:143], v[172:175], a[108:111]
v_mfma_f32_16x16x32_bf16 a[112:115], v[140:143], v[176:179], a[112:115]
ds_read_b64_tr_b16 v[92:93], v33 offset:0
v_mfma_f32_16x16x32_bf16 a[116:119], v[140:143], v[180:183], a[116:119]
buffer_load_dwordx4 v23, s[12:15], s24 offen offset:3072 lds
v_mfma_f32_16x16x32_bf16 a[120:123], v[140:143], v[184:187], a[120:123]
ds_read_b64_tr_b16 v[94:95], v33 offset:1024
v_mfma_f32_16x16x32_bf16 a[124:127], v[140:143], v[188:191], a[124:127]
v_mfma_f32_16x16x32_bf16 a[128:131], v[144:147], v[160:163], a[128:131]
s_add_u32 m0, s92, 135168
v_mfma_f32_16x16x32_bf16 a[132:135], v[144:147], v[164:167], a[132:135]
buffer_load_dwordx4 v20, s[12:15], s25 offen offset:0 lds
v_mfma_f32_16x16x32_bf16 a[136:139], v[144:147], v[168:171], a[136:139]
ds_read_b128 v[48:51], v24 offset:0
v_mfma_f32_16x16x32_bf16 a[140:143], v[144:147], v[172:175], a[140:143]
v_mfma_f32_16x16x32_bf16 a[144:147], v[144:147], v[176:179], a[144:147]
ds_read_b128 v[52:55], v24 offset:2048
v_mfma_f32_16x16x32_bf16 a[148:151], v[144:147], v[180:183], a[148:151]
buffer_load_dwordx4 v21, s[12:15], s25 offen offset:1024 lds
v_mfma_f32_16x16x32_bf16 a[152:155], v[144:147], v[184:187], a[152:155]
ds_read_b128 v[56:59], v24 offset:4096
v_mfma_f32_16x16x32_bf16 a[156:159], v[144:147], v[188:191], a[156:159]
v_mfma_f32_16x16x32_bf16 a[160:163], v[148:151], v[160:163], a[160:163]
v_mfma_f32_16x16x32_bf16 a[164:167], v[148:151], v[164:167], a[164:167]
buffer_load_dwordx4 v22, s[12:15], s25 offen offset:2048 lds
v_mfma_f32_16x16x32_bf16 a[168:171], v[148:151], v[168:171], a[168:171]
v_mfma_f32_16x16x32_bf16 a[172:175], v[148:151], v[172:175], a[172:175]
v_mfma_f32_16x16x32_bf16 a[176:179], v[148:151], v[176:179], a[176:179]
v_mfma_f32_16x16x32_bf16 a[180:183], v[148:151], v[180:183], a[180:183]
buffer_load_dwordx4 v23, s[12:15], s25 offen offset:3072 lds
v_mfma_f32_16x16x32_bf16 a[184:187], v[148:151], v[184:187], a[184:187]
v_mfma_f32_16x16x32_bf16 a[188:191], v[148:151], v[188:191], a[188:191]
v_mfma_f32_16x16x32_bf16 a[192:195], v[152:155], v[160:163], a[192:195]
v_mfma_f32_16x16x32_bf16 a[196:199], v[152:155], v[164:167], a[196:199]
s_add_u32 s24, s24, 128
s_add_u32 s25, s25, 128
v_mfma_f32_16x16x32_bf16 a[200:203], v[152:155], v[168:171], a[200:203]
v_mfma_f32_16x16x32_bf16 a[204:207], v[152:155], v[172:175], a[204:207]
v_mfma_f32_16x16x32_bf16 a[208:211], v[152:155], v[176:179], a[208:211]
v_mfma_f32_16x16x32_bf16 a[212:215], v[152:155], v[180:183], a[212:215]
v_mfma_f32_16x16x32_bf16 a[216:219], v[152:155], v[184:187], a[216:219]
v_mfma_f32_16x16x32_bf16 a[220:223], v[152:155], v[188:191], a[220:223]
v_mfma_f32_16x16x32_bf16 a[224:227], v[156:159], v[160:163], a[224:227]
v_mfma_f32_16x16x32_bf16 a[228:231], v[156:159], v[164:167], a[228:231]
v_mfma_f32_16x16x32_bf16 a[232:235], v[156:159], v[168:171], a[232:235]
v_mfma_f32_16x16x32_bf16 a[236:239], v[156:159], v[172:175], a[236:239]
v_mfma_f32_16x16x32_bf16 a[240:243], v[156:159], v[176:179], a[240:243]
v_mfma_f32_16x16x32_bf16 a[244:247], v[156:159], v[180:183], a[244:247]
v_mfma_f32_16x16x32_bf16 a[248:251], v[156:159], v[184:187], a[248:251]
v_mfma_f32_16x16x32_bf16 a[252:255], v[156:159], v[188:191], a[252:255]
s_waitcnt lgkmcnt(0)
.Lp2:
v_mfma_f32_16x16x32_bf16 v[128:131], v[64:67], v[48:51], 0
ds_read_b128 v[60:63], v24 offset:6144
v_mfma_f32_16x16x32_bf16 v[132:135], v[68:71], v[48:51], 0
v_add_u32 v24, 65536, v24
v_accvgpr_read_b32 v36, a0
v_mfma_f32_16x16x32_bf16 v[136:139], v[72:75], v[48:51], 0
ds_read_b64_tr_b16 v[96:97], v26 offset:8192
ds_read_b64_tr_b16 v[98:99], v26 offset:9216
v_mfma_f32_16x16x32_bf16 v[140:143], v[76:79], v[48:51], 0
v_accvgpr_read_b32 v37, a1
v_mul_f32 v36, s47, v36
v_mfma_f32_16x16x32_bf16 v[144:147], v[80:83], v[48:51], 0
ds_read_b64_tr_b16 v[100:101], v27 offset:8192
ds_read_b64_tr_b16 v[102:103], v27 offset:9216
v_mfma_f32_16x16x32_bf16 v[148:151], v[84:87], v[48:51], 0
v_mul_f32 v37, s47, v37
v_cvt_pk_bf16_f32 v40, v36, v37
v_mfma_f32_16x16x32_bf16 v[152:155], v[88:91], v[48:51], 0
ds_read_b64_tr_b16 v[104:105], v28 offset:8192
ds_read_b64_tr_b16 v[106:107], v28 offset:9216
v_mfma_f32_16x16x32_bf16 v[156:159], v[92:95], v[48:51], 0
v_accvgpr_read_b32 v38, a2
v_accvgpr_read_b32 v39, a3
v_mfma_f32_16x16x32_bf16 v[160:163], v[64:67], v[52:55], 0
ds_read_b128 v[48:51], v25 offset:0
v_mul_f32 v38, s47, v38
v_mfma_f32_16x16x32_bf16 v[164:167], v[68:71], v[52:55], 0
v_mul_f32 v39, s47, v39
v_cvt_pk_bf16_f32 v41, v38, v39
v_mfma_f32_16x16x32_bf16 v[168:171], v[72:75], v[52:55], 0
s_add_u32 m0, s91, 16384
ds_read_b64_tr_b16 v[108:109], v29 offset:8192
ds_read_b64_tr_b16 v[110:111], v29 offset:9216
v_mfma_f32_16x16x32_bf16 v[172:175], v[76:79], v[52:55], 0
buffer_load_dwordx4 v16, s[16:19], s26 offen offset:0 lds
v_accvgpr_read_b32 v36, a4
v_mfma_f32_16x16x32_bf16 v[176:179], v[80:83], v[52:55], 0
ds_read_b64_tr_b16 v[112:113], v30 offset:8192
ds_read_b64_tr_b16 v[114:115], v30 offset:9216
v_mfma_f32_16x16x32_bf16 v[180:183], v[84:87], v[52:55], 0
v_accvgpr_read_b32 v37, a5
v_mul_f32 v36, s47, v36
v_mfma_f32_16x16x32_bf16 v[184:187], v[88:91], v[52:55], 0
ds_read_b64_tr_b16 v[116:117], v31 offset:8192
ds_read_b64_tr_b16 v[118:119], v31 offset:9216
v_mfma_f32_16x16x32_bf16 v[188:191], v[92:95], v[52:55], 0
buffer_load_dwordx4 v17, s[16:19], s26 offen offset:1024 lds
v_mul_f32 v37, s47, v37
v_mfma_f32_16x16x32_bf16 v[192:195], v[64:67], v[56:59], 0
ds_read_b128 v[52:55], v25 offset:2048
v_cvt_pk_bf16_f32 v42, v36, v37
v_mfma_f32_16x16x32_bf16 v[196:199], v[68:71], v[56:59], 0
v_accvgpr_read_b32 v38, a6
v_accvgpr_read_b32 v39, a7
v_mfma_f32_16x16x32_bf16 v[200:203], v[72:75], v[56:59], 0
ds_read_b64_tr_b16 v[120:121], v32 offset:8192
ds_read_b64_tr_b16 v[122:123], v32 offset:9216
v_mfma_f32_16x16x32_bf16 v[204:207], v[76:79], v[56:59], 0
buffer_load_dwordx4 v18, s[16:19], s26 offen offset:2048 lds
v_mul_f32 v38, s47, v38
v_mfma_f32_16x16x32_bf16 v[208:211], v[80:83], v[56:59], 0
ds_read_b64_tr_b16 v[124:125], v33 offset:8192
ds_read_b64_tr_b16 v[126:127], v33 offset:9216
v_mfma_f32_16x16x32_bf16 v[212:215], v[84:87], v[56:59], 0
v_mul_f32 v39, s47, v39
v_cvt_pk_bf16_f32 v43, v38, v39
v_mfma_f32_16x16x32_bf16 v[216:219], v[88:91], v[56:59], 0
s_waitcnt lgkmcnt(15)
v_permlane32_swap_b32 v40, v42
v_mfma_f32_16x16x32_bf16 v[220:223], v[92:95], v[56:59], 0
v_permlane32_swap_b32 v41, v43
v_mfma_f32_16x16x32_bf16 v[224:227], v[64:67], v[60:63], 0
s_xor_b32 s48, s95, 0
ds_read_b128 v[56:59], v25 offset:4096
v_mfma_f32_16x16x32_bf16 v[228:231], v[68:71], v[60:63], 0
buffer_load_dwordx4 v19, s[16:19], s26 offen offset:3072 lds
v_accvgpr_read_b32 v36, a128
v_mfma_f32_16x16x32_bf16 v[232:235], v[72:75], v[60:63], 0
buffer_store_dwordx4 v[40:43], v7, s[20:23], s48 offen sc1
v_accvgpr_read_b32 v37, a129
v_mfma_f32_16x16x32_bf16 v[236:239], v[76:79], v[60:63], 0
s_add_u32 s26, s26, 524288
v_mul_f32 v36, s47, v36
v_mul_f32 v37, s47, v37
v_mfma_f32_16x16x32_bf16 v[240:243], v[80:83], v[60:63], 0
v_cvt_pk_bf16_f32 v44, v36, v37
v_accvgpr_read_b32 v38, a130
v_mfma_f32_16x16x32_bf16 v[244:247], v[84:87], v[60:63], 0
v_accvgpr_read_b32 v39, a131
v_mul_f32 v38, s47, v38
v_mfma_f32_16x16x32_bf16 v[248:251], v[88:91], v[60:63], 0
v_mul_f32 v39, s47, v39
v_cvt_pk_bf16_f32 v45, v38, v39
v_mfma_f32_16x16x32_bf16 v[252:255], v[92:95], v[60:63], 0
s_waitcnt vmcnt(17) lgkmcnt(0)
s_barrier
v_accvgpr_read_b32 v36, a132
v_mfma_f32_16x16x32_bf16 v[128:131], v[96:99], v[48:51], v[128:131]
ds_read_b128 v[60:63], v25 offset:6144
v_accvgpr_read_b32 v37, a133
v_mfma_f32_16x16x32_bf16 v[132:135], v[100:103], v[48:51], v[132:135]
v_add_u32 v25, 65536, v25
v_mul_f32 v36, s47, v36
v_mfma_f32_16x16x32_bf16 v[136:139], v[104:107], v[48:51], v[136:139]
s_add_u32 m0, s92, 32768
ds_read_b64_tr_b16 v[64:65], v26 offset:16384
ds_read_b64_tr_b16 v[66:67], v26 offset:17408
v_mfma_f32_16x16x32_bf16 v[140:143], v[108:111], v[48:51], v[140:143]
buffer_load_dwordx4 v20, s[12:15], s24 offen offset:0 lds
v_mul_f32 v37, s47, v37
v_mfma_f32_16x16x32_bf16 v[144:147], v[112:115], v[48:51], v[144:147]
ds_read_b64_tr_b16 v[68:69], v27 offset:16384
ds_read_b64_tr_b16 v[70:71], v27 offset:17408
v_mfma_f32_16x16x32_bf16 v[148:151], v[116:119], v[48:51], v[148:151]
v_cvt_pk_bf16_f32 v46, v36, v37
v_accvgpr_read_b32 v38, a134
v_mfma_f32_16x16x32_bf16 v[152:155], v[120:123], v[48:51], v[152:155]
ds_read_b64_tr_b16 v[72:73], v28 offset:16384
ds_read_b64_tr_b16 v[74:75], v28 offset:17408
v_mfma_f32_16x16x32_bf16 v[156:159], v[124:127], v[48:51], v[156:159]
buffer_load_dwordx4 v21, s[12:15], s24 offen offset:1024 lds
v_accvgpr_read_b32 v39, a135
v_mfma_f32_16x16x32_bf16 v[160:163], v[96:99], v[52:55], v[160:163]
ds_read_b128 v[48:51], v24 offset:0
v_mul_f32 v38, s47, v38
v_mfma_f32_16x16x32_bf16 v[164:167], v[100:103], v[52:55], v[164:167]
v_mul_f32 v39, s47, v39
v_cvt_pk_bf16_f32 v47, v38, v39
v_mfma_f32_16x16x32_bf16 v[168:171], v[104:107], v[52:55], v[168:171]
ds_read_b64_tr_b16 v[76:77], v29 offset:16384
ds_read_b64_tr_b16 v[78:79], v29 offset:17408
v_mfma_f32_16x16x32_bf16 v[172:175], v[108:111], v[52:55], v[172:175]
buffer_load_dwordx4 v22, s[12:15], s24 offen offset:2048 lds
v_mfma_f32_16x16x32_bf16 v[176:179], v[112:115], v[52:55], v[176:179]
ds_read_b64_tr_b16 v[80:81], v30 offset:16384
ds_read_b64_tr_b16 v[82:83], v30 offset:17408
v_mfma_f32_16x16x32_bf16 v[180:183], v[116:119], v[52:55], v[180:183]
v_permlane32_swap_b32 v44, v46
v_mfma_f32_16x16x32_bf16 v[184:187], v[120:123], v[52:55], v[184:187]
ds_read_b64_tr_b16 v[84:85], v31 offset:16384
ds_read_b64_tr_b16 v[86:87], v31 offset:17408
v_mfma_f32_16x16x32_bf16 v[188:191], v[124:127], v[52:55], v[188:191]
buffer_load_dwordx4 v23, s[12:15], s24 offen offset:3072 lds
v_mfma_f32_16x16x32_bf16 v[192:195], v[96:99], v[56:59], v[192:195]
ds_read_b128 v[52:55], v24 offset:2048
v_mfma_f32_16x16x32_bf16 v[196:199], v[100:103], v[56:59], v[196:199]
v_permlane32_swap_b32 v45, v47
v_mfma_f32_16x16x32_bf16 v[200:203], v[104:107], v[56:59], v[200:203]
s_add_u32 m0, s92, 36864
ds_read_b64_tr_b16 v[88:89], v32 offset:16384
ds_read_b64_tr_b16 v[90:91], v32 offset:17408
v_mfma_f32_16x16x32_bf16 v[204:207], v[108:111], v[56:59], v[204:207]
buffer_load_dwordx4 v20, s[12:15], s25 offen offset:0 lds
v_accvgpr_write_b32 a128, v44
v_mfma_f32_16x16x32_bf16 v[208:211], v[112:115], v[56:59], v[208:211]
ds_read_b64_tr_b16 v[92:93], v33 offset:16384
ds_read_b64_tr_b16 v[94:95], v33 offset:17408
v_mfma_f32_16x16x32_bf16 v[212:215], v[116:119], v[56:59], v[212:215]
v_accvgpr_write_b32 a129, v45
v_accvgpr_write_b32 a130, v46
v_mfma_f32_16x16x32_bf16 v[216:219], v[120:123], v[56:59], v[216:219]
s_waitcnt lgkmcnt(15)
v_accvgpr_write_b32 a131, v47
v_accvgpr_read_b32 v36, a8
v_mfma_f32_16x16x32_bf16 v[220:223], v[124:127], v[56:59], v[220:223]
v_accvgpr_read_b32 v37, a9
v_mul_f32 v36, s47, v36
v_mfma_f32_16x16x32_bf16 v[224:227], v[96:99], v[60:63], v[224:227]
ds_read_b128 v[56:59], v24 offset:4096
v_mul_f32 v37, s47, v37
v_mfma_f32_16x16x32_bf16 v[228:231], v[100:103], v[60:63], v[228:231]
buffer_load_dwordx4 v21, s[12:15], s25 offen offset:1024 lds
v_cvt_pk_bf16_f32 v40, v36, v37
v_mfma_f32_16x16x32_bf16 v[232:235], v[104:107], v[60:63], v[232:235]
v_accvgpr_read_b32 v38, a10
v_accvgpr_read_b32 v39, a11
v_mfma_f32_16x16x32_bf16 v[236:239], v[108:111], v[60:63], v[236:239]
v_mul_f32 v38, s47, v38
v_mul_f32 v39, s47, v39
v_mfma_f32_16x16x32_bf16 v[240:243], v[112:115], v[60:63], v[240:243]
v_cvt_pk_bf16_f32 v41, v38, v39
v_accvgpr_read_b32 v36, a12
v_mfma_f32_16x16x32_bf16 v[244:247], v[116:119], v[60:63], v[244:247]
v_accvgpr_read_b32 v37, a13
v_mul_f32 v36, s47, v36
v_mfma_f32_16x16x32_bf16 v[248:251], v[120:123], v[60:63], v[248:251]
v_mul_f32 v37, s47, v37
v_cvt_pk_bf16_f32 v42, v36, v37
v_mfma_f32_16x16x32_bf16 v[252:255], v[124:127], v[60:63], v[252:255]
s_waitcnt lgkmcnt(0)
v_accvgpr_read_b32 v38, a14
v_accvgpr_read_b32 v39, a15
v_mfma_f32_16x16x32_bf16 v[128:131], v[64:67], v[48:51], v[128:131]
ds_read_b128 v[60:63], v24 offset:6144
v_mul_f32 v38, s47, v38
v_mfma_f32_16x16x32_bf16 v[132:135], v[68:71], v[48:51], v[132:135]
v_add_u32 v24, 32768, v24
v_mul_f32 v39, s47, v39
v_mfma_f32_16x16x32_bf16 v[136:139], v[72:75], v[48:51], v[136:139]
ds_read_b64_tr_b16 v[96:97], v26 offset:24576
ds_read_b64_tr_b16 v[98:99], v26 offset:25600
v_mfma_f32_16x16x32_bf16 v[140:143], v[76:79], v[48:51], v[140:143]
buffer_load_dwordx4 v22, s[12:15], s25 offen offset:2048 lds
v_cvt_pk_bf16_f32 v43, v38, v39
v_mfma_f32_16x16x32_bf16 v[144:147], v[80:83], v[48:51], v[144:147]
ds_read_b64_tr_b16 v[100:101], v27 offset:24576
ds_read_b64_tr_b16 v[102:103], v27 offset:25600
v_mfma_f32_16x16x32_bf16 v[148:151], v[84:87], v[48:51], v[148:151]
v_permlane32_swap_b32 v40, v42
v_mfma_f32_16x16x32_bf16 v[152:155], v[88:91], v[48:51], v[152:155]
ds_read_b64_tr_b16 v[104:105], v28 offset:24576
ds_read_b64_tr_b16 v[106:107], v28 offset:25600
v_mfma_f32_16x16x32_bf16 v[156:159], v[92:95], v[48:51], v[156:159]
buffer_load_dwordx4 v23, s[12:15], s25 offen offset:3072 lds
v_accvgpr_read_b32 v36, a136
v_mfma_f32_16x16x32_bf16 v[160:163], v[64:67], v[52:55], v[160:163]
ds_read_b128 v[48:51], v25 offset:0
v_accvgpr_read_b32 v37, a137
v_mfma_f32_16x16x32_bf16 v[164:167], v[68:71], v[52:55], v[164:167]
s_add_u32 s24, s24, 128
s_add_u32 s25, s25, 128
v_mul_f32 v36, s47, v36
v_mfma_f32_16x16x32_bf16 v[168:171], v[72:75], v[52:55], v[168:171]
s_add_u32 m0, s91, 65536
ds_read_b64_tr_b16 v[108:109], v29 offset:24576
ds_read_b64_tr_b16 v[110:111], v29 offset:25600
v_mfma_f32_16x16x32_bf16 v[172:175], v[76:79], v[52:55], v[172:175]
buffer_load_dwordx4 v16, s[16:19], s26 offen offset:0 lds
v_mul_f32 v37, s47, v37
v_mfma_f32_16x16x32_bf16 v[176:179], v[80:83], v[52:55], v[176:179]
ds_read_b64_tr_b16 v[112:113], v30 offset:24576
ds_read_b64_tr_b16 v[114:115], v30 offset:25600
v_mfma_f32_16x16x32_bf16 v[180:183], v[84:87], v[52:55], v[180:183]
v_permlane32_swap_b32 v41, v43
v_mfma_f32_16x16x32_bf16 v[184:187], v[88:91], v[52:55], v[184:187]
ds_read_b64_tr_b16 v[116:117], v31 offset:24576
ds_read_b64_tr_b16 v[118:119], v31 offset:25600
v_mfma_f32_16x16x32_bf16 v[188:191], v[92:95], v[52:55], v[188:191]
s_xor_b32 s48, s95, 1024
buffer_load_dwordx4 v17, s[16:19], s26 offen offset:1024 lds
v_mfma_f32_16x16x32_bf16 v[192:195], v[64:67], v[56:59], v[192:195]
ds_read_b128 v[52:55], v25 offset:2048
v_cvt_pk_bf16_f32 v44, v36, v37
v_mfma_f32_16x16x32_bf16 v[196:199], v[68:71], v[56:59], v[196:199]
buffer_store_dwordx4 v[40:43], v7, s[20:23], s48 offen sc1
v_accvgpr_read_b32 v38, a138
v_mfma_f32_16x16x32_bf16 v[200:203], v[72:75], v[56:59], v[200:203]
ds_read_b64_tr_b16 v[120:121], v32 offset:24576
ds_read_b64_tr_b16 v[122:123], v32 offset:25600
v_mfma_f32_16x16x32_bf16 v[204:207], v[76:79], v[56:59], v[204:207]
buffer_load_dwordx4 v18, s[16:19], s26 offen offset:2048 lds
v_accvgpr_read_b32 v39, a139
v_mfma_f32_16x16x32_bf16 v[208:211], v[80:83], v[56:59], v[208:211]
ds_read_b64_tr_b16 v[124:125], v33 offset:24576
ds_read_b64_tr_b16 v[126:127], v33 offset:25600
v_mfma_f32_16x16x32_bf16 v[212:215], v[84:87], v[56:59], v[212:215]
v_mul_f32 v38, s47, v38
v_mul_f32 v39, s47, v39
v_mfma_f32_16x16x32_bf16 v[216:219], v[88:91], v[56:59], v[216:219]
s_waitcnt lgkmcnt(15)
v_cvt_pk_bf16_f32 v45, v38, v39
v_accvgpr_read_b32 v36, a140
v_mfma_f32_16x16x32_bf16 v[220:223], v[92:95], v[56:59], v[220:223]
v_xor_b32 v26, 0x10000, v26
v_accvgpr_read_b32 v37, a141
v_mfma_f32_16x16x32_bf16 v[224:227], v[64:67], v[60:63], v[224:227]
ds_read_b128 v[56:59], v25 offset:4096
v_xor_b32 v27, 0x10000, v27
v_mfma_f32_16x16x32_bf16 v[228:231], v[68:71], v[60:63], v[228:231]
buffer_load_dwordx4 v19, s[16:19], s26 offen offset:3072 lds
v_xor_b32 v28, 0x10000, v28
v_mfma_f32_16x16x32_bf16 v[232:235], v[72:75], v[60:63], v[232:235]
v_xor_b32 v29, 0x10000, v29
v_mul_f32 v36, s47, v36
v_mfma_f32_16x16x32_bf16 v[236:239], v[76:79], v[60:63], v[236:239]
s_add_u32 s26, s26, 524288
v_xor_b32 v30, 0x10000, v30
v_mul_f32 v37, s47, v37
v_mfma_f32_16x16x32_bf16 v[240:243], v[80:83], v[60:63], v[240:243]
v_xor_b32 v31, 0x10000, v31
v_cvt_pk_bf16_f32 v46, v36, v37
v_mfma_f32_16x16x32_bf16 v[244:247], v[84:87], v[60:63], v[244:247]
v_xor_b32 v32, 0x10000, v32
v_accvgpr_read_b32 v38, a142
v_mfma_f32_16x16x32_bf16 v[248:251], v[88:91], v[60:63], v[248:251]
v_xor_b32 v33, 0x10000, v33
v_accvgpr_read_b32 v39, a143
v_mfma_f32_16x16x32_bf16 v[252:255], v[92:95], v[60:63], v[252:255]
s_waitcnt vmcnt(18) lgkmcnt(0)
s_barrier
v_mul_f32 v38, s47, v38
v_mfma_f32_16x16x32_bf16 v[128:131], v[96:99], v[48:51], v[128:131]
ds_read_b128 v[60:63], v25 offset:6144
v_mul_f32 v39, s47, v39
v_mfma_f32_16x16x32_bf16 v[132:135], v[100:103], v[48:51], v[132:135]
v_add_u32 v25, 32768, v25
v_cvt_pk_bf16_f32 v47, v38, v39
v_mfma_f32_16x16x32_bf16 v[136:139], v[104:107], v[48:51], v[136:139]
s_add_u32 m0, s92, 98304
ds_read_b64_tr_b16 v[64:65], v26 offset:0
ds_read_b64_tr_b16 v[66:67], v26 offset:1024
v_mfma_f32_16x16x32_bf16 v[140:143], v[108:111], v[48:51], v[140:143]
buffer_load_dwordx4 v20, s[12:15], s24 offen offset:0 lds
v_mfma_f32_16x16x32_bf16 v[144:147], v[112:115], v[48:51], v[144:147]
ds_read_b64_tr_b16 v[68:69], v27 offset:0
ds_read_b64_tr_b16 v[70:71], v27 offset:1024
v_mfma_f32_16x16x32_bf16 v[148:151], v[116:119], v[48:51], v[148:151]
v_permlane32_swap_b32 v44, v46
v_mfma_f32_16x16x32_bf16 v[152:155], v[120:123], v[48:51], v[152:155]
ds_read_b64_tr_b16 v[72:73], v28 offset:0
ds_read_b64_tr_b16 v[74:75], v28 offset:1024
v_mfma_f32_16x16x32_bf16 v[156:159], v[124:127], v[48:51], v[156:159]
buffer_load_dwordx4 v21, s[12:15], s24 offen offset:1024 lds
v_mfma_f32_16x16x32_bf16 v[160:163], v[96:99], v[52:55], v[160:163]
ds_read_b128 v[48:51], v24 offset:0
v_mfma_f32_16x16x32_bf16 v[164:167], v[100:103], v[52:55], v[164:167]
v_permlane32_swap_b32 v45, v47
v_mfma_f32_16x16x32_bf16 v[168:171], v[104:107], v[52:55], v[168:171]
ds_read_b64_tr_b16 v[76:77], v29 offset:0
ds_read_b64_tr_b16 v[78:79], v29 offset:1024
v_mfma_f32_16x16x32_bf16 v[172:175], v[108:111], v[52:55], v[172:175]
buffer_load_dwordx4 v22, s[12:15], s24 offen offset:2048 lds
v_accvgpr_write_b32 a132, v44
v_mfma_f32_16x16x32_bf16 v[176:179], v[112:115], v[52:55], v[176:179]
ds_read_b64_tr_b16 v[80:81], v30 offset:0
ds_read_b64_tr_b16 v[82:83], v30 offset:1024
v_mfma_f32_16x16x32_bf16 v[180:183], v[116:119], v[52:55], v[180:183]
v_accvgpr_write_b32 a133, v45
v_accvgpr_write_b32 a134, v46
v_mfma_f32_16x16x32_bf16 v[184:187], v[120:123], v[52:55], v[184:187]
ds_read_b64_tr_b16 v[84:85], v31 offset:0
ds_read_b64_tr_b16 v[86:87], v31 offset:1024
v_mfma_f32_16x16x32_bf16 v[188:191], v[124:127], v[52:55], v[188:191]
buffer_load_dwordx4 v23, s[12:15], s24 offen offset:3072 lds
v_accvgpr_write_b32 a135, v47
v_mfma_f32_16x16x32_bf16 v[192:195], v[96:99], v[56:59], v[192:195]
ds_read_b128 v[52:55], v24 offset:2048
v_accvgpr_read_b32 v36, a16
v_mfma_f32_16x16x32_bf16 v[196:199], v[100:103], v[56:59], v[196:199]
v_accvgpr_read_b32 v37, a17
v_mul_f32 v36, s47, v36
v_mfma_f32_16x16x32_bf16 v[200:203], v[104:107], v[56:59], v[200:203]
s_add_u32 m0, s92, 102400
ds_read_b64_tr_b16 v[88:89], v32 offset:0
ds_read_b64_tr_b16 v[90:91], v32 offset:1024
v_mfma_f32_16x16x32_bf16 v[204:207], v[108:111], v[56:59], v[204:207]
buffer_load_dwordx4 v20, s[12:15], s25 offen offset:0 lds
v_mul_f32 v37, s47, v37
v_mfma_f32_16x16x32_bf16 v[208:211], v[112:115], v[56:59], v[208:211]
ds_read_b64_tr_b16 v[92:93], v33 offset:0
ds_read_b64_tr_b16 v[94:95], v33 offset:1024
v_mfma_f32_16x16x32_bf16 v[212:215], v[116:119], v[56:59], v[212:215]
v_cvt_pk_bf16_f32 v40, v36, v37
v_accvgpr_read_b32 v38, a18
v_mfma_f32_16x16x32_bf16 v[216:219], v[120:123], v[56:59], v[216:219]
s_waitcnt lgkmcnt(15)
v_accvgpr_read_b32 v39, a19
v_mul_f32 v38, s47, v38
v_mfma_f32_16x16x32_bf16 v[220:223], v[124:127], v[56:59], v[220:223]
v_mul_f32 v39, s47, v39
v_cvt_pk_bf16_f32 v41, v38, v39
v_mfma_f32_16x16x32_bf16 v[224:227], v[96:99], v[60:63], v[224:227]
ds_read_b128 v[56:59], v24 offset:4096
v_accvgpr_read_b32 v36, a20
v_mfma_f32_16x16x32_bf16 v[228:231], v[100:103], v[60:63], v[228:231]
buffer_load_dwordx4 v21, s[12:15], s25 offen offset:1024 lds
v_accvgpr_read_b32 v37, a21
v_mfma_f32_16x16x32_bf16 v[232:235], v[104:107], v[60:63], v[232:235]
v_mul_f32 v36, s47, v36
v_mul_f32 v37, s47, v37
v_mfma_f32_16x16x32_bf16 v[236:239], v[108:111], v[60:63], v[236:239]
v_cvt_pk_bf16_f32 v42, v36, v37
v_accvgpr_read_b32 v38, a22
v_mfma_f32_16x16x32_bf16 v[240:243], v[112:115], v[60:63], v[240:243]
v_accvgpr_read_b32 v39, a23
v_mul_f32 v38, s47, v38
v_mfma_f32_16x16x32_bf16 v[244:247], v[116:119], v[60:63], v[244:247]
v_mul_f32 v39, s47, v39
v_cvt_pk_bf16_f32 v43, v38, v39
v_mfma_f32_16x16x32_bf16 v[248:251], v[120:123], v[60:63], v[248:251]
v_permlane32_swap_b32 v40, v42
v_mfma_f32_16x16x32_bf16 v[252:255], v[124:127], v[60:63], v[252:255]
s_waitcnt lgkmcnt(0)
v_permlane32_swap_b32 v41, v43
v_mfma_f32_16x16x32_bf16 v[128:131], v[64:67], v[48:51], v[128:131]
s_xor_b32 s48, s95, 2048
ds_read_b128 v[60:63], v24 offset:6144
v_mfma_f32_16x16x32_bf16 v[132:135], v[68:71], v[48:51], v[132:135]
buffer_store_dwordx4 v[40:43], v7, s[20:23], s48 offen sc1
v_add_u32 v24, 4294868992, v24
v_mfma_f32_16x16x32_bf16 v[136:139], v[72:75], v[48:51], v[136:139]
ds_read_b64_tr_b16 v[96:97], v26 offset:8192
ds_read_b64_tr_b16 v[98:99], v26 offset:9216
v_mfma_f32_16x16x32_bf16 v[140:143], v[76:79], v[48:51], v[140:143]
buffer_load_dwordx4 v22, s[12:15], s25 offen offset:2048 lds
v_accvgpr_read_b32 v36, a144
v_mfma_f32_16x16x32_bf16 v[144:147], v[80:83], v[48:51], v[144:147]
ds_read_b64_tr_b16 v[100:101], v27 offset:8192
ds_read_b64_tr_b16 v[102:103], v27 offset:9216
v_mfma_f32_16x16x32_bf16 v[148:151], v[84:87], v[48:51], v[148:151]
v_accvgpr_read_b32 v37, a145
v_mul_f32 v36, s47, v36
v_mfma_f32_16x16x32_bf16 v[152:155], v[88:91], v[48:51], v[152:155]
ds_read_b64_tr_b16 v[104:105], v28 offset:8192
ds_read_b64_tr_b16 v[106:107], v28 offset:9216
v_mfma_f32_16x16x32_bf16 v[156:159], v[92:95], v[48:51], v[156:159]
buffer_load_dwordx4 v23, s[12:15], s25 offen offset:3072 lds
v_mul_f32 v37, s47, v37
v_mfma_f32_16x16x32_bf16 v[160:163], v[64:67], v[52:55], v[160:163]
ds_read_b128 v[48:51], v25 offset:0
v_cvt_pk_bf16_f32 v44, v36, v37
v_mfma_f32_16x16x32_bf16 v[164:167], v[68:71], v[52:55], v[164:167]
s_add_u32 s24, s24, 128
s_add_u32 s25, s25, 128
v_accvgpr_read_b32 v38, a146
v_mfma_f32_16x16x32_bf16 v[168:171], v[72:75], v[52:55], v[168:171]
s_add_u32 m0, s91, 81920
ds_read_b64_tr_b16 v[108:109], v29 offset:8192
ds_read_b64_tr_b16 v[110:111], v29 offset:9216
v_mfma_f32_16x16x32_bf16 v[172:175], v[76:79], v[52:55], v[172:175]
buffer_load_dwordx4 v16, s[16:19], s26 offen offset:0 lds
v_accvgpr_read_b32 v39, a147
v_mfma_f32_16x16x32_bf16 v[176:179], v[80:83], v[52:55], v[176:179]
ds_read_b64_tr_b16 v[112:113], v30 offset:8192
ds_read_b64_tr_b16 v[114:115], v30 offset:9216
v_mfma_f32_16x16x32_bf16 v[180:183], v[84:87], v[52:55], v[180:183]
v_mul_f32 v38, s47, v38
v_mul_f32 v39, s47, v39
v_mfma_f32_16x16x32_bf16 v[184:187], v[88:91], v[52:55], v[184:187]
ds_read_b64_tr_b16 v[116:117], v31 offset:8192
ds_read_b64_tr_b16 v[118:119], v31 offset:9216
v_mfma_f32_16x16x32_bf16 v[188:191], v[92:95], v[52:55], v[188:191]
buffer_load_dwordx4 v17, s[16:19], s26 offen offset:1024 lds
v_cvt_pk_bf16_f32 v45, v38, v39
v_mfma_f32_16x16x32_bf16 v[192:195], v[64:67], v[56:59], v[192:195]
ds_read_b128 v[52:55], v25 offset:2048
v_accvgpr_read_b32 v36, a148
v_mfma_f32_16x16x32_bf16 v[196:199], v[68:71], v[56:59], v[196:199]
v_accvgpr_read_b32 v37, a149
v_mul_f32 v36, s47, v36
v_mfma_f32_16x16x32_bf16 v[200:203], v[72:75], v[56:59], v[200:203]
ds_read_b64_tr_b16 v[120:121], v32 offset:8192
ds_read_b64_tr_b16 v[122:123], v32 offset:9216
v_mfma_f32_16x16x32_bf16 v[204:207], v[76:79], v[56:59], v[204:207]
buffer_load_dwordx4 v18, s[16:19], s26 offen offset:2048 lds
v_mul_f32 v37, s47, v37
v_mfma_f32_16x16x32_bf16 v[208:211], v[80:83], v[56:59], v[208:211]
ds_read_b64_tr_b16 v[124:125], v33 offset:8192
ds_read_b64_tr_b16 v[126:127], v33 offset:9216
v_mfma_f32_16x16x32_bf16 v[212:215], v[84:87], v[56:59], v[212:215]
v_cvt_pk_bf16_f32 v46, v36, v37
v_accvgpr_read_b32 v38, a150
v_mfma_f32_16x16x32_bf16 v[216:219], v[88:91], v[56:59], v[216:219]
s_waitcnt lgkmcnt(15)
v_accvgpr_read_b32 v39, a151
v_mul_f32 v38, s47, v38
v_mfma_f32_16x16x32_bf16 v[220:223], v[92:95], v[56:59], v[220:223]
v_mul_f32 v39, s47, v39
v_cvt_pk_bf16_f32 v47, v38, v39
v_mfma_f32_16x16x32_bf16 v[224:227], v[64:67], v[60:63], v[224:227]
ds_read_b128 v[56:59], v25 offset:4096
v_mfma_f32_16x16x32_bf16 v[228:231], v[68:71], v[60:63], v[228:231]
buffer_load_dwordx4 v19, s[16:19], s26 offen offset:3072 lds
v_mfma_f32_16x16x32_bf16 v[232:235], v[72:75], v[60:63], v[232:235]
v_permlane32_swap_b32 v44, v46
v_mfma_f32_16x16x32_bf16 v[236:239], v[76:79], v[60:63], v[236:239]
s_add_u32 s26, s26, 524288
v_permlane32_swap_b32 v45, v47
v_mfma_f32_16x16x32_bf16 v[240:243], v[80:83], v[60:63], v[240:243]
v_accvgpr_write_b32 a136, v44
v_accvgpr_write_b32 a137, v45
v_mfma_f32_16x16x32_bf16 v[244:247], v[84:87], v[60:63], v[244:247]
v_accvgpr_write_b32 a138, v46
v_accvgpr_write_b32 a139, v47
v_mfma_f32_16x16x32_bf16 v[248:251], v[88:91], v[60:63], v[248:251]
v_accvgpr_read_b32 v36, a24
v_accvgpr_read_b32 v37, a25
v_mfma_f32_16x16x32_bf16 v[252:255], v[92:95], v[60:63], v[252:255]
s_waitcnt vmcnt(18) lgkmcnt(0)
s_barrier
v_mul_f32 v36, s47, v36
v_mfma_f32_16x16x32_bf16 v[128:131], v[96:99], v[48:51], v[128:131]
ds_read_b128 v[60:63], v25 offset:6144
v_mul_f32 v37, s47, v37
v_mfma_f32_16x16x32_bf16 v[132:135], v[100:103], v[48:51], v[132:135]
v_add_u32 v25, 4294868992, v25
v_cvt_pk_bf16_f32 v40, v36, v37
v_mfma_f32_16x16x32_bf16 v[136:139], v[104:107], v[48:51], v[136:139]
s_add_u32 m0, s92, 131072
ds_read_b64_tr_b16 v[64:65], v26 offset:16384
ds_read_b64_tr_b16 v[66:67], v26 offset:17408
v_mfma_f32_16x16x32_bf16 v[140:143], v[108:111], v[48:51], v[140:143]
buffer_load_dwordx4 v20, s[12:15], s24 offen offset:0 lds
v_accvgpr_read_b32 v38, a26
v_mfma_f32_16x16x32_bf16 v[144:147], v[112:115], v[48:51], v[144:147]
ds_read_b64_tr_b16 v[68:69], v27 offset:16384
ds_read_b64_tr_b16 v[70:71], v27 offset:17408
v_mfma_f32_16x16x32_bf16 v[148:151], v[116:119], v[48:51], v[148:151]
v_accvgpr_read_b32 v39, a27
v_mul_f32 v38, s47, v38
v_mfma_f32_16x16x32_bf16 v[152:155], v[120:123], v[48:51], v[152:155]
ds_read_b64_tr_b16 v[72:73], v28 offset:16384
ds_read_b64_tr_b16 v[74:75], v28 offset:17408
v_mfma_f32_16x16x32_bf16 v[156:159], v[124:127], v[48:51], v[156:159]
buffer_load_dwordx4 v21, s[12:15], s24 offen offset:1024 lds
v_mul_f32 v39, s47, v39
v_mfma_f32_16x16x32_bf16 v[160:163], v[96:99], v[52:55], v[160:163]
ds_read_b128 v[48:51], v24 offset:0
v_cvt_pk_bf16_f32 v41, v38, v39
v_mfma_f32_16x16x32_bf16 v[164:167], v[100:103], v[52:55], v[164:167]
v_accvgpr_read_b32 v36, a28
v_accvgpr_read_b32 v37, a29
v_mfma_f32_16x16x32_bf16 v[168:171], v[104:107], v[52:55], v[168:171]
ds_read_b64_tr_b16 v[76:77], v29 offset:16384
ds_read_b64_tr_b16 v[78:79], v29 offset:17408
v_mfma_f32_16x16x32_bf16 v[172:175], v[108:111], v[52:55], v[172:175]
buffer_load_dwordx4 v22, s[12:15], s24 offen offset:2048 lds
v_mul_f32 v36, s47, v36
v_mfma_f32_16x16x32_bf16 v[176:179], v[112:115], v[52:55], v[176:179]
ds_read_b64_tr_b16 v[80:81], v30 offset:16384
ds_read_b64_tr_b16 v[82:83], v30 offset:17408
v_mfma_f32_16x16x32_bf16 v[180:183], v[116:119], v[52:55], v[180:183]
v_mul_f32 v37, s47, v37
v_cvt_pk_bf16_f32 v42, v36, v37
v_mfma_f32_16x16x32_bf16 v[184:187], v[120:123], v[52:55], v[184:187]
ds_read_b64_tr_b16 v[84:85], v31 offset:16384
ds_read_b64_tr_b16 v[86:87], v31 offset:17408
v_mfma_f32_16x16x32_bf16 v[188:191], v[124:127], v[52:55], v[188:191]
buffer_load_dwordx4 v23, s[12:15], s24 offen offset:3072 lds
v_accvgpr_read_b32 v38, a30
v_mfma_f32_16x16x32_bf16 v[192:195], v[96:99], v[56:59], v[192:195]
ds_read_b128 v[52:55], v24 offset:2048
v_accvgpr_read_b32 v39, a31
v_mfma_f32_16x16x32_bf16 v[196:199], v[100:103], v[56:59], v[196:199]
v_mul_f32 v38, s47, v38
v_mul_f32 v39, s47, v39
v_mfma_f32_16x16x32_bf16 v[200:203], v[104:107], v[56:59], v[200:203]
s_add_u32 m0, s92, 135168
ds_read_b64_tr_b16 v[88:89], v32 offset:16384
ds_read_b64_tr_b16 v[90:91], v32 offset:17408
v_mfma_f32_16x16x32_bf16 v[204:207], v[108:111], v[56:59], v[204:207]
buffer_load_dwordx4 v20, s[12:15], s25 offen offset:0 lds
v_cvt_pk_bf16_f32 v43, v38, v39
v_mfma_f32_16x16x32_bf16 v[208:211], v[112:115], v[56:59], v[208:211]
ds_read_b64_tr_b16 v[92:93], v33 offset:16384
ds_read_b64_tr_b16 v[94:95], v33 offset:17408
v_mfma_f32_16x16x32_bf16 v[212:215], v[116:119], v[56:59], v[212:215]
v_permlane32_swap_b32 v40, v42
v_mfma_f32_16x16x32_bf16 v[216:219], v[120:123], v[56:59], v[216:219]
s_waitcnt lgkmcnt(15)
v_permlane32_swap_b32 v41, v43
v_mfma_f32_16x16x32_bf16 v[220:223], v[124:127], v[56:59], v[220:223]
s_xor_b32 s48, s95, 3072
buffer_store_dwordx4 v[40:43], v7, s[20:23], s48 offen sc1
v_mfma_f32_16x16x32_bf16 v[224:227], v[96:99], v[60:63], v[224:227]
ds_read_b128 v[56:59], v24 offset:4096
v_accvgpr_read_b32 v36, a152
v_mfma_f32_16x16x32_bf16 v[228:231], v[100:103], v[60:63], v[228:231]
buffer_load_dwordx4 v21, s[12:15], s25 offen offset:1024 lds
v_accvgpr_read_b32 v37, a153
v_mfma_f32_16x16x32_bf16 v[232:235], v[104:107], v[60:63], v[232:235]
v_mul_f32 v36, s47, v36
v_mul_f32 v37, s47, v37
v_mfma_f32_16x16x32_bf16 v[236:239], v[108:111], v[60:63], v[236:239]
v_cvt_pk_bf16_f32 v44, v36, v37
v_accvgpr_read_b32 v38, a154
v_mfma_f32_16x16x32_bf16 v[240:243], v[112:115], v[60:63], v[240:243]
v_accvgpr_read_b32 v39, a155
v_mul_f32 v38, s47, v38
v_mfma_f32_16x16x32_bf16 v[244:247], v[116:119], v[60:63], v[244:247]
v_mul_f32 v39, s47, v39
v_cvt_pk_bf16_f32 v45, v38, v39
v_mfma_f32_16x16x32_bf16 v[248:251], v[120:123], v[60:63], v[248:251]
v_accvgpr_read_b32 v36, a156
v_accvgpr_read_b32 v37, a157
v_mfma_f32_16x16x32_bf16 v[252:255], v[124:127], v[60:63], v[252:255]
s_waitcnt lgkmcnt(0)
v_mul_f32 v36, s47, v36
v_mul_f32 v37, s47, v37
v_mfma_f32_16x16x32_bf16 v[128:131], v[64:67], v[48:51], v[128:131]
ds_read_b128 v[60:63], v24 offset:6144
v_cvt_pk_bf16_f32 v46, v36, v37
v_mfma_f32_16x16x32_bf16 v[132:135], v[68:71], v[48:51], v[132:135]
v_add_u32 v24, 65536, v24
v_accvgpr_read_b32 v38, a158
v_mfma_f32_16x16x32_bf16 v[136:139], v[72:75], v[48:51], v[136:139]
ds_read_b64_tr_b16 v[96:97], v26 offset:24576
ds_read_b64_tr_b16 v[98:99], v26 offset:25600
v_mfma_f32_16x16x32_bf16 v[140:143], v[76:79], v[48:51], v[140:143]
buffer_load_dwordx4 v22, s[12:15], s25 offen offset:2048 lds
v_accvgpr_read_b32 v39, a159
v_mfma_f32_16x16x32_bf16 v[144:147], v[80:83], v[48:51], v[144:147]
ds_read_b64_tr_b16 v[100:101], v27 offset:24576
ds_read_b64_tr_b16 v[102:103], v27 offset:25600
v_mfma_f32_16x16x32_bf16 v[148:151], v[84:87], v[48:51], v[148:151]
v_mul_f32 v38, s47, v38
v_mul_f32 v39, s47, v39
v_mfma_f32_16x16x32_bf16 v[152:155], v[88:91], v[48:51], v[152:155]
ds_read_b64_tr_b16 v[104:105], v28 offset:24576
ds_read_b64_tr_b16 v[106:107], v28 offset:25600
v_mfma_f32_16x16x32_bf16 v[156:159], v[92:95], v[48:51], v[156:159]
buffer_load_dwordx4 v23, s[12:15], s25 offen offset:3072 lds
v_cvt_pk_bf16_f32 v47, v38, v39
v_mfma_f32_16x16x32_bf16 v[160:163], v[64:67], v[52:55], v[160:163]
ds_read_b128 v[48:51], v25 offset:0
v_mfma_f32_16x16x32_bf16 v[164:167], v[68:71], v[52:55], v[164:167]
s_add_u32 s24, s24, 128
s_add_u32 s25, s25, 128
v_mfma_f32_16x16x32_bf16 v[168:171], v[72:75], v[52:55], v[168:171]
s_add_u32 m0, s91, 0
ds_read_b64_tr_b16 v[108:109], v29 offset:24576
ds_read_b64_tr_b16 v[110:111], v29 offset:25600
v_mfma_f32_16x16x32_bf16 v[172:175], v[76:79], v[52:55], v[172:175]
buffer_load_dwordx4 v16, s[16:19], s26 offen offset:0 lds
v_mfma_f32_16x16x32_bf16 v[176:179], v[80:83], v[52:55], v[176:179]
ds_read_b64_tr_b16 v[112:113], v30 offset:24576
ds_read_b64_tr_b16 v[114:115], v30 offset:25600
v_mfma_f32_16x16x32_bf16 v[180:183], v[84:87], v[52:55], v[180:183]
v_permlane32_swap_b32 v44, v46
v_mfma_f32_16x16x32_bf16 v[184:187], v[88:91], v[52:55], v[184:187]
ds_read_b64_tr_b16 v[116:117], v31 offset:24576
ds_read_b64_tr_b16 v[118:119], v31 offset:25600
v_mfma_f32_16x16x32_bf16 v[188:191], v[92:95], v[52:55], v[188:191]
buffer_load_dwordx4 v17, s[16:19], s26 offen offset:1024 lds
v_mfma_f32_16x16x32_bf16 v[192:195], v[64:67], v[56:59], v[192:195]
ds_read_b128 v[52:55], v25 offset:2048
v_mfma_f32_16x16x32_bf16 v[196:199], v[68:71], v[56:59], v[196:199]
v_permlane32_swap_b32 v45, v47
v_mfma_f32_16x16x32_bf16 v[200:203], v[72:75], v[56:59], v[200:203]
ds_read_b64_tr_b16 v[120:121], v32 offset:24576
ds_read_b64_tr_b16 v[122:123], v32 offset:25600
v_mfma_f32_16x16x32_bf16 v[204:207], v[76:79], v[56:59], v[204:207]
buffer_load_dwordx4 v18, s[16:19], s26 offen offset:2048 lds
v_accvgpr_write_b32 a140, v44
v_mfma_f32_16x16x32_bf16 v[208:211], v[80:83], v[56:59], v[208:211]
ds_read_b64_tr_b16 v[124:125], v33 offset:24576
ds_read_b64_tr_b16 v[126:127], v33 offset:25600
v_mfma_f32_16x16x32_bf16 v[212:215], v[84:87], v[56:59], v[212:215]
v_accvgpr_write_b32 a141, v45
v_accvgpr_write_b32 a142, v46
v_mfma_f32_16x16x32_bf16 v[216:219], v[88:91], v[56:59], v[216:219]
s_waitcnt lgkmcnt(15)
v_accvgpr_write_b32 a143, v47
v_accvgpr_read_b32 v36, a32
v_mfma_f32_16x16x32_bf16 v[220:223], v[92:95], v[56:59], v[220:223]
v_xor_b32 v26, 0x10000, v26
v_accvgpr_read_b32 v37, a33
v_mfma_f32_16x16x32_bf16 v[224:227], v[64:67], v[60:63], v[224:227]
ds_read_b128 v[56:59], v25 offset:4096
v_xor_b32 v27, 0x10000, v27
v_mfma_f32_16x16x32_bf16 v[228:231], v[68:71], v[60:63], v[228:231]
buffer_load_dwordx4 v19, s[16:19], s26 offen offset:3072 lds
v_xor_b32 v28, 0x10000, v28
v_mfma_f32_16x16x32_bf16 v[232:235], v[72:75], v[60:63], v[232:235]
v_xor_b32 v29, 0x10000, v29
v_mul_f32 v36, s47, v36
v_mfma_f32_16x16x32_bf16 v[236:239], v[76:79], v[60:63], v[236:239]
s_add_u32 s26, s26, 524288
v_xor_b32 v30, 0x10000, v30
v_mul_f32 v37, s47, v37
v_mfma_f32_16x16x32_bf16 v[240:243], v[80:83], v[60:63], v[240:243]
v_xor_b32 v31, 0x10000, v31
v_cvt_pk_bf16_f32 v40, v36, v37
v_mfma_f32_16x16x32_bf16 v[244:247], v[84:87], v[60:63], v[244:247]
v_xor_b32 v32, 0x10000, v32
v_accvgpr_read_b32 v38, a34
v_mfma_f32_16x16x32_bf16 v[248:251], v[88:91], v[60:63], v[248:251]
v_xor_b32 v33, 0x10000, v33
v_accvgpr_read_b32 v39, a35
v_mfma_f32_16x16x32_bf16 v[252:255], v[92:95], v[60:63], v[252:255]
s_waitcnt vmcnt(17) lgkmcnt(0)
s_barrier
v_mul_f32 v38, s47, v38
v_mfma_f32_16x16x32_bf16 v[128:131], v[96:99], v[48:51], v[128:131]
ds_read_b128 v[60:63], v25 offset:6144
v_mul_f32 v39, s47, v39
v_mfma_f32_16x16x32_bf16 v[132:135], v[100:103], v[48:51], v[132:135]
v_add_u32 v25, 65536, v25
v_cvt_pk_bf16_f32 v41, v38, v39
v_mfma_f32_16x16x32_bf16 v[136:139], v[104:107], v[48:51], v[136:139]
s_add_u32 m0, s92, 32768
ds_read_b64_tr_b16 v[64:65], v26 offset:0
ds_read_b64_tr_b16 v[66:67], v26 offset:1024
v_mfma_f32_16x16x32_bf16 v[140:143], v[108:111], v[48:51], v[140:143]
buffer_load_dwordx4 v20, s[12:15], s24 offen offset:0 lds
v_accvgpr_read_b32 v36, a36
v_mfma_f32_16x16x32_bf16 v[144:147], v[112:115], v[48:51], v[144:147]
ds_read_b64_tr_b16 v[68:69], v27 offset:0
ds_read_b64_tr_b16 v[70:71], v27 offset:1024
v_mfma_f32_16x16x32_bf16 v[148:151], v[116:119], v[48:51], v[148:151]
v_accvgpr_read_b32 v37, a37
v_mul_f32 v36, s47, v36
v_mfma_f32_16x16x32_bf16 v[152:155], v[120:123], v[48:51], v[152:155]
ds_read_b64_tr_b16 v[72:73], v28 offset:0
ds_read_b64_tr_b16 v[74:75], v28 offset:1024
v_mfma_f32_16x16x32_bf16 v[156:159], v[124:127], v[48:51], v[156:159]
buffer_load_dwordx4 v21, s[12:15], s24 offen offset:1024 lds
v_mul_f32 v37, s47, v37
v_mfma_f32_16x16x32_bf16 v[160:163], v[96:99], v[52:55], v[160:163]
ds_read_b128 v[48:51], v24 offset:0
v_cvt_pk_bf16_f32 v42, v36, v37
v_mfma_f32_16x16x32_bf16 v[164:167], v[100:103], v[52:55], v[164:167]
v_accvgpr_read_b32 v38, a38
v_accvgpr_read_b32 v39, a39
v_mfma_f32_16x16x32_bf16 v[168:171], v[104:107], v[52:55], v[168:171]
ds_read_b64_tr_b16 v[76:77], v29 offset:0
ds_read_b64_tr_b16 v[78:79], v29 offset:1024
v_mfma_f32_16x16x32_bf16 v[172:175], v[108:111], v[52:55], v[172:175]
buffer_load_dwordx4 v22, s[12:15], s24 offen offset:2048 lds
v_mul_f32 v38, s47, v38
v_mfma_f32_16x16x32_bf16 v[176:179], v[112:115], v[52:55], v[176:179]
ds_read_b64_tr_b16 v[80:81], v30 offset:0
ds_read_b64_tr_b16 v[82:83], v30 offset:1024
v_mfma_f32_16x16x32_bf16 v[180:183], v[116:119], v[52:55], v[180:183]
v_mul_f32 v39, s47, v39
v_cvt_pk_bf16_f32 v43, v38, v39
v_mfma_f32_16x16x32_bf16 v[184:187], v[120:123], v[52:55], v[184:187]
ds_read_b64_tr_b16 v[84:85], v31 offset:0
ds_read_b64_tr_b16 v[86:87], v31 offset:1024
v_mfma_f32_16x16x32_bf16 v[188:191], v[124:127], v[52:55], v[188:191]
buffer_load_dwordx4 v23, s[12:15], s24 offen offset:3072 lds
v_accvgpr_read_b32 v36, a160
v_mfma_f32_16x16x32_bf16 v[192:195], v[96:99], v[56:59], v[192:195]
ds_read_b128 v[52:55], v24 offset:2048
v_accvgpr_read_b32 v37, a161
v_mfma_f32_16x16x32_bf16 v[196:199], v[100:103], v[56:59], v[196:199]
v_permlane32_swap_b32 v40, v42
v_mfma_f32_16x16x32_bf16 v[200:203], v[104:107], v[56:59], v[200:203]
s_add_u32 m0, s92, 36864
ds_read_b64_tr_b16 v[88:89], v32 offset:0
ds_read_b64_tr_b16 v[90:91], v32 offset:1024
v_mfma_f32_16x16x32_bf16 v[204:207], v[108:111], v[56:59], v[204:207]
buffer_load_dwordx4 v20, s[12:15], s25 offen offset:0 lds
v_mul_f32 v36, s47, v36
v_mfma_f32_16x16x32_bf16 v[208:211], v[112:115], v[56:59], v[208:211]
ds_read_b64_tr_b16 v[92:93], v33 offset:0
ds_read_b64_tr_b16 v[94:95], v33 offset:1024
v_mfma_f32_16x16x32_bf16 v[212:215], v[116:119], v[56:59], v[212:215]
v_permlane32_swap_b32 v41, v43
v_mfma_f32_16x16x32_bf16 v[216:219], v[120:123], v[56:59], v[216:219]
s_waitcnt lgkmcnt(15)
s_xor_b32 s48, s95, 65536
v_mul_f32 v37, s47, v37
v_mfma_f32_16x16x32_bf16 v[220:223], v[124:127], v[56:59], v[220:223]
buffer_store_dwordx4 v[40:43], v7, s[20:23], s48 offen sc1
v_cvt_pk_bf16_f32 v44, v36, v37
v_mfma_f32_16x16x32_bf16 v[224:227], v[96:99], v[60:63], v[224:227]
ds_read_b128 v[56:59], v24 offset:4096
v_accvgpr_read_b32 v38, a162
v_mfma_f32_16x16x32_bf16 v[228:231], v[100:103], v[60:63], v[228:231]
buffer_load_dwordx4 v21, s[12:15], s25 offen offset:1024 lds
v_accvgpr_read_b32 v39, a163
v_mfma_f32_16x16x32_bf16 v[232:235], v[104:107], v[60:63], v[232:235]
v_mul_f32 v38, s47, v38
v_mul_f32 v39, s47, v39
v_mfma_f32_16x16x32_bf16 v[236:239], v[108:111], v[60:63], v[236:239]
v_cvt_pk_bf16_f32 v45, v38, v39
v_accvgpr_read_b32 v36, a164
v_mfma_f32_16x16x32_bf16 v[240:243], v[112:115], v[60:63], v[240:243]
v_accvgpr_read_b32 v37, a165
v_mul_f32 v36, s47, v36
v_mfma_f32_16x16x32_bf16 v[244:247], v[116:119], v[60:63], v[244:247]
v_mul_f32 v37, s47, v37
v_cvt_pk_bf16_f32 v46, v36, v37
v_mfma_f32_16x16x32_bf16 v[248:251], v[120:123], v[60:63], v[248:251]
v_accvgpr_read_b32 v38, a166
v_accvgpr_read_b32 v39, a167
v_mfma_f32_16x16x32_bf16 v[252:255], v[124:127], v[60:63], v[252:255]
s_waitcnt lgkmcnt(0)
v_mul_f32 v38, s47, v38
v_mul_f32 v39, s47, v39
v_mfma_f32_16x16x32_bf16 v[128:131], v[64:67], v[48:51], v[128:131]
ds_read_b128 v[60:63], v24 offset:6144
v_cvt_pk_bf16_f32 v47, v38, v39
v_mfma_f32_16x16x32_bf16 v[132:135], v[68:71], v[48:51], v[132:135]
v_add_u32 v24, 32768, v24
v_mfma_f32_16x16x32_bf16 v[136:139], v[72:75], v[48:51], v[136:139]
ds_read_b64_tr_b16 v[96:97], v26 offset:8192
ds_read_b64_tr_b16 v[98:99], v26 offset:9216
v_mfma_f32_16x16x32_bf16 v[140:143], v[76:79], v[48:51], v[140:143]
buffer_load_dwordx4 v22, s[12:15], s25 offen offset:2048 lds
v_mfma_f32_16x16x32_bf16 v[144:147], v[80:83], v[48:51], v[144:147]
ds_read_b64_tr_b16 v[100:101], v27 offset:8192
ds_read_b64_tr_b16 v[102:103], v27 offset:9216
v_mfma_f32_16x16x32_bf16 v[148:151], v[84:87], v[48:51], v[148:151]
v_permlane32_swap_b32 v44, v46
v_mfma_f32_16x16x32_bf16 v[152:155], v[88:91], v[48:51], v[152:155]
ds_read_b64_tr_b16 v[104:105], v28 offset:8192
ds_read_b64_tr_b16 v[106:107], v28 offset:9216
v_mfma_f32_16x16x32_bf16 v[156:159], v[92:95], v[48:51], v[156:159]
buffer_load_dwordx4 v23, s[12:15], s25 offen offset:3072 lds
v_mfma_f32_16x16x32_bf16 v[160:163], v[64:67], v[52:55], v[160:163]
ds_read_b128 v[48:51], v25 offset:0
v_mfma_f32_16x16x32_bf16 v[164:167], v[68:71], v[52:55], v[164:167]
s_add_u32 s24, s24, 128
s_add_u32 s25, s25, 128
v_mfma_f32_16x16x32_bf16 v[168:171], v[72:75], v[52:55], v[168:171]
s_add_u32 m0, s91, 16384
ds_read_b64_tr_b16 v[108:109], v29 offset:8192
ds_read_b64_tr_b16 v[110:111], v29 offset:9216
v_mfma_f32_16x16x32_bf16 v[172:175], v[76:79], v[52:55], v[172:175]
buffer_load_dwordx4 v16, s[16:19], s26 offen offset:0 lds
v_mfma_f32_16x16x32_bf16 v[176:179], v[80:83], v[52:55], v[176:179]
ds_read_b64_tr_b16 v[112:113], v30 offset:8192
ds_read_b64_tr_b16 v[114:115], v30 offset:9216
v_mfma_f32_16x16x32_bf16 v[180:183], v[84:87], v[52:55], v[180:183]
v_permlane32_swap_b32 v45, v47
v_mfma_f32_16x16x32_bf16 v[184:187], v[88:91], v[52:55], v[184:187]
ds_read_b64_tr_b16 v[116:117], v31 offset:8192
ds_read_b64_tr_b16 v[118:119], v31 offset:9216
v_mfma_f32_16x16x32_bf16 v[188:191], v[92:95], v[52:55], v[188:191]
buffer_load_dwordx4 v17, s[16:19], s26 offen offset:1024 lds
v_accvgpr_write_b32 a160, v44
v_mfma_f32_16x16x32_bf16 v[192:195], v[64:67], v[56:59], v[192:195]
ds_read_b128 v[52:55], v25 offset:2048
v_accvgpr_write_b32 a161, v45
v_mfma_f32_16x16x32_bf16 v[196:199], v[68:71], v[56:59], v[196:199]
v_accvgpr_write_b32 a162, v46
v_accvgpr_write_b32 a163, v47
v_mfma_f32_16x16x32_bf16 v[200:203], v[72:75], v[56:59], v[200:203]
ds_read_b64_tr_b16 v[120:121], v32 offset:8192
ds_read_b64_tr_b16 v[122:123], v32 offset:9216
v_mfma_f32_16x16x32_bf16 v[204:207], v[76:79], v[56:59], v[204:207]
buffer_load_dwordx4 v18, s[16:19], s26 offen offset:2048 lds
v_accvgpr_read_b32 v36, a40
v_mfma_f32_16x16x32_bf16 v[208:211], v[80:83], v[56:59], v[208:211]
ds_read_b64_tr_b16 v[124:125], v33 offset:8192
ds_read_b64_tr_b16 v[126:127], v33 offset:9216
v_mfma_f32_16x16x32_bf16 v[212:215], v[84:87], v[56:59], v[212:215]
v_accvgpr_read_b32 v37, a41
v_mul_f32 v36, s47, v36
v_mfma_f32_16x16x32_bf16 v[216:219], v[88:91], v[56:59], v[216:219]
s_waitcnt lgkmcnt(15)
v_mul_f32 v37, s47, v37
v_cvt_pk_bf16_f32 v40, v36, v37
v_mfma_f32_16x16x32_bf16 v[220:223], v[92:95], v[56:59], v[220:223]
v_accvgpr_read_b32 v38, a42
v_accvgpr_read_b32 v39, a43
v_mfma_f32_16x16x32_bf16 v[224:227], v[64:67], v[60:63], v[224:227]
ds_read_b128 v[56:59], v25 offset:4096
v_mul_f32 v38, s47, v38
v_mfma_f32_16x16x32_bf16 v[228:231], v[68:71], v[60:63], v[228:231]
buffer_load_dwordx4 v19, s[16:19], s26 offen offset:3072 lds
v_mul_f32 v39, s47, v39
v_mfma_f32_16x16x32_bf16 v[232:235], v[72:75], v[60:63], v[232:235]
v_cvt_pk_bf16_f32 v41, v38, v39
v_accvgpr_read_b32 v36, a44
v_mfma_f32_16x16x32_bf16 v[236:239], v[76:79], v[60:63], v[236:239]
s_add_u32 s26, s26, 524288
v_accvgpr_read_b32 v37, a45
v_mul_f32 v36, s47, v36
v_mfma_f32_16x16x32_bf16 v[240:243], v[80:83], v[60:63], v[240:243]
v_mul_f32 v37, s47, v37
v_cvt_pk_bf16_f32 v42, v36, v37
v_mfma_f32_16x16x32_bf16 v[244:247], v[84:87], v[60:63], v[244:247]
v_accvgpr_read_b32 v38, a46
v_accvgpr_read_b32 v39, a47
v_mfma_f32_16x16x32_bf16 v[248:251], v[88:91], v[60:63], v[248:251]
v_mul_f32 v38, s47, v38
v_mul_f32 v39, s47, v39
v_mfma_f32_16x16x32_bf16 v[252:255], v[92:95], v[60:63], v[252:255]
s_waitcnt vmcnt(17) lgkmcnt(0)
s_barrier
v_cvt_pk_bf16_f32 v43, v38, v39
v_mfma_f32_16x16x32_bf16 v[128:131], v[96:99], v[48:51], v[128:131]
ds_read_b128 v[60:63], v25 offset:6144
v_accvgpr_read_b32 v36, a168
v_mfma_f32_16x16x32_bf16 v[132:135], v[100:103], v[48:51], v[132:135]
v_add_u32 v25, 32768, v25
v_accvgpr_read_b32 v37, a169
v_mfma_f32_16x16x32_bf16 v[136:139], v[104:107], v[48:51], v[136:139]
s_add_u32 m0, s92, 98304
ds_read_b64_tr_b16 v[64:65], v26 offset:16384
ds_read_b64_tr_b16 v[66:67], v26 offset:17408
v_mfma_f32_16x16x32_bf16 v[140:143], v[108:111], v[48:51], v[140:143]
buffer_load_dwordx4 v20, s[12:15], s24 offen offset:0 lds
v_mul_f32 v36, s47, v36
v_mfma_f32_16x16x32_bf16 v[144:147], v[112:115], v[48:51], v[144:147]
ds_read_b64_tr_b16 v[68:69], v27 offset:16384
ds_read_b64_tr_b16 v[70:71], v27 offset:17408
v_mfma_f32_16x16x32_bf16 v[148:151], v[116:119], v[48:51], v[148:151]
v_permlane32_swap_b32 v40, v42
v_mfma_f32_16x16x32_bf16 v[152:155], v[120:123], v[48:51], v[152:155]
ds_read_b64_tr_b16 v[72:73], v28 offset:16384
ds_read_b64_tr_b16 v[74:75], v28 offset:17408
v_mfma_f32_16x16x32_bf16 v[156:159], v[124:127], v[48:51], v[156:159]
buffer_load_dwordx4 v21, s[12:15], s24 offen offset:1024 lds
v_mul_f32 v37, s47, v37
v_mfma_f32_16x16x32_bf16 v[160:163], v[96:99], v[52:55], v[160:163]
ds_read_b128 v[48:51], v24 offset:0
v_cvt_pk_bf16_f32 v44, v36, v37
v_mfma_f32_16x16x32_bf16 v[164:167], v[100:103], v[52:55], v[164:167]
v_permlane32_swap_b32 v41, v43
v_mfma_f32_16x16x32_bf16 v[168:171], v[104:107], v[52:55], v[168:171]
ds_read_b64_tr_b16 v[76:77], v29 offset:16384
ds_read_b64_tr_b16 v[78:79], v29 offset:17408
v_mfma_f32_16x16x32_bf16 v[172:175], v[108:111], v[52:55], v[172:175]
s_xor_b32 s48, s95, 66560
buffer_load_dwordx4 v22, s[12:15], s24 offen offset:2048 lds
v_mfma_f32_16x16x32_bf16 v[176:179], v[112:115], v[52:55], v[176:179]
ds_read_b64_tr_b16 v[80:81], v30 offset:16384
ds_read_b64_tr_b16 v[82:83], v30 offset:17408
v_mfma_f32_16x16x32_bf16 v[180:183], v[116:119], v[52:55], v[180:183]
buffer_store_dwordx4 v[40:43], v7, s[20:23], s48 offen sc1
v_accvgpr_read_b32 v38, a170
v_mfma_f32_16x16x32_bf16 v[184:187], v[120:123], v[52:55], v[184:187]
ds_read_b64_tr_b16 v[84:85], v31 offset:16384
ds_read_b64_tr_b16 v[86:87], v31 offset:17408
v_mfma_f32_16x16x32_bf16 v[188:191], v[124:127], v[52:55], v[188:191]
buffer_load_dwordx4 v23, s[12:15], s24 offen offset:3072 lds
v_accvgpr_read_b32 v39, a171
v_mfma_f32_16x16x32_bf16 v[192:195], v[96:99], v[56:59], v[192:195]
ds_read_b128 v[52:55], v24 offset:2048
v_mul_f32 v38, s47, v38
v_mfma_f32_16x16x32_bf16 v[196:199], v[100:103], v[56:59], v[196:199]
v_mul_f32 v39, s47, v39
v_cvt_pk_bf16_f32 v45, v38, v39
v_mfma_f32_16x16x32_bf16 v[200:203], v[104:107], v[56:59], v[200:203]
s_add_u32 m0, s92, 102400
ds_read_b64_tr_b16 v[88:89], v32 offset:16384
ds_read_b64_tr_b16 v[90:91], v32 offset:17408
v_mfma_f32_16x16x32_bf16 v[204:207], v[108:111], v[56:59], v[204:207]
buffer_load_dwordx4 v20, s[12:15], s25 offen offset:0 lds
v_accvgpr_read_b32 v36, a172
v_mfma_f32_16x16x32_bf16 v[208:211], v[112:115], v[56:59], v[208:211]
ds_read_b64_tr_b16 v[92:93], v33 offset:16384
ds_read_b64_tr_b16 v[94:95], v33 offset:17408
v_mfma_f32_16x16x32_bf16 v[212:215], v[116:119], v[56:59], v[212:215]
v_accvgpr_read_b32 v37, a173
v_mul_f32 v36, s47, v36
v_mfma_f32_16x16x32_bf16 v[216:219], v[120:123], v[56:59], v[216:219]
s_waitcnt lgkmcnt(15)
v_mul_f32 v37, s47, v37
v_cvt_pk_bf16_f32 v46, v36, v37
v_mfma_f32_16x16x32_bf16 v[220:223], v[124:127], v[56:59], v[220:223]
v_accvgpr_read_b32 v38, a174
v_accvgpr_read_b32 v39, a175
v_mfma_f32_16x16x32_bf16 v[224:227], v[96:99], v[60:63], v[224:227]
ds_read_b128 v[56:59], v24 offset:4096
v_mul_f32 v38, s47, v38
v_mfma_f32_16x16x32_bf16 v[228:231], v[100:103], v[60:63], v[228:231]
buffer_load_dwordx4 v21, s[12:15], s25 offen offset:1024 lds
v_mul_f32 v39, s47, v39
v_mfma_f32_16x16x32_bf16 v[232:235], v[104:107], v[60:63], v[232:235]
v_cvt_pk_bf16_f32 v47, v38, v39
v_mfma_f32_16x16x32_bf16 v[236:239], v[108:111], v[60:63], v[236:239]
v_permlane32_swap_b32 v44, v46
v_mfma_f32_16x16x32_bf16 v[240:243], v[112:115], v[60:63], v[240:243]
v_permlane32_swap_b32 v45, v47
v_mfma_f32_16x16x32_bf16 v[244:247], v[116:119], v[60:63], v[244:247]
v_accvgpr_write_b32 a164, v44
v_accvgpr_write_b32 a165, v45
v_mfma_f32_16x16x32_bf16 v[248:251], v[120:123], v[60:63], v[248:251]
v_accvgpr_write_b32 a166, v46
v_accvgpr_write_b32 a167, v47
v_mfma_f32_16x16x32_bf16 v[252:255], v[124:127], v[60:63], v[252:255]
s_waitcnt lgkmcnt(0)
v_accvgpr_read_b32 v36, a48
v_accvgpr_read_b32 v37, a49
v_mfma_f32_16x16x32_bf16 v[128:131], v[64:67], v[48:51], v[128:131]
ds_read_b128 v[60:63], v24 offset:6144
v_mul_f32 v36, s47, v36
v_mfma_f32_16x16x32_bf16 v[132:135], v[68:71], v[48:51], v[132:135]
v_add_u32 v24, 4294868992, v24
v_mul_f32 v37, s47, v37
v_mfma_f32_16x16x32_bf16 v[136:139], v[72:75], v[48:51], v[136:139]
ds_read_b64_tr_b16 v[96:97], v26 offset:24576
ds_read_b64_tr_b16 v[98:99], v26 offset:25600
v_mfma_f32_16x16x32_bf16 v[140:143], v[76:79], v[48:51], v[140:143]
buffer_load_dwordx4 v22, s[12:15], s25 offen offset:2048 lds
v_cvt_pk_bf16_f32 v40, v36, v37
v_mfma_f32_16x16x32_bf16 v[144:147], v[80:83], v[48:51], v[144:147]
ds_read_b64_tr_b16 v[100:101], v27 offset:24576
ds_read_b64_tr_b16 v[102:103], v27 offset:25600
v_mfma_f32_16x16x32_bf16 v[148:151], v[84:87], v[48:51], v[148:151]
v_accvgpr_read_b32 v38, a50
v_accvgpr_read_b32 v39, a51
v_mfma_f32_16x16x32_bf16 v[152:155], v[88:91], v[48:51], v[152:155]
ds_read_b64_tr_b16 v[104:105], v28 offset:24576
ds_read_b64_tr_b16 v[106:107], v28 offset:25600
v_mfma_f32_16x16x32_bf16 v[156:159], v[92:95], v[48:51], v[156:159]
buffer_load_dwordx4 v23, s[12:15], s25 offen offset:3072 lds
v_mul_f32 v38, s47, v38
v_mfma_f32_16x16x32_bf16 v[160:163], v[64:67], v[52:55], v[160:163]
ds_read_b128 v[48:51], v25 offset:0
v_mul_f32 v39, s47, v39
v_mfma_f32_16x16x32_bf16 v[164:167], v[68:71], v[52:55], v[164:167]
s_add_u32 s24, s24, 128
s_add_u32 s25, s25, 128
v_cvt_pk_bf16_f32 v41, v38, v39
v_mfma_f32_16x16x32_bf16 v[168:171], v[72:75], v[52:55], v[168:171]
s_add_u32 m0, s91, 65536
ds_read_b64_tr_b16 v[108:109], v29 offset:24576
ds_read_b64_tr_b16 v[110:111], v29 offset:25600
v_mfma_f32_16x16x32_bf16 v[172:175], v[76:79], v[52:55], v[172:175]
buffer_load_dwordx4 v16, s[16:19], s26 offen offset:0 lds
v_accvgpr_read_b32 v36, a52
v_mfma_f32_16x16x32_bf16 v[176:179], v[80:83], v[52:55], v[176:179]
ds_read_b64_tr_b16 v[112:113], v30 offset:24576
ds_read_b64_tr_b16 v[114:115], v30 offset:25600
v_mfma_f32_16x16x32_bf16 v[180:183], v[84:87], v[52:55], v[180:183]
v_accvgpr_read_b32 v37, a53
v_mul_f32 v36, s47, v36
v_mfma_f32_16x16x32_bf16 v[184:187], v[88:91], v[52:55], v[184:187]
ds_read_b64_tr_b16 v[116:117], v31 offset:24576
ds_read_b64_tr_b16 v[118:119], v31 offset:25600
v_mfma_f32_16x16x32_bf16 v[188:191], v[92:95], v[52:55], v[188:191]
buffer_load_dwordx4 v17, s[16:19], s26 offen offset:1024 lds
v_mul_f32 v37, s47, v37
v_mfma_f32_16x16x32_bf16 v[192:195], v[64:67], v[56:59], v[192:195]
ds_read_b128 v[52:55], v25 offset:2048
v_cvt_pk_bf16_f32 v42, v36, v37
v_mfma_f32_16x16x32_bf16 v[196:199], v[68:71], v[56:59], v[196:199]
v_accvgpr_read_b32 v38, a54
v_accvgpr_read_b32 v39, a55
v_mfma_f32_16x16x32_bf16 v[200:203], v[72:75], v[56:59], v[200:203]
ds_read_b64_tr_b16 v[120:121], v32 offset:24576
ds_read_b64_tr_b16 v[122:123], v32 offset:25600
v_mfma_f32_16x16x32_bf16 v[204:207], v[76:79], v[56:59], v[204:207]
buffer_load_dwordx4 v18, s[16:19], s26 offen offset:2048 lds
v_mul_f32 v38, s47, v38
v_mfma_f32_16x16x32_bf16 v[208:211], v[80:83], v[56:59], v[208:211]
ds_read_b64_tr_b16 v[124:125], v33 offset:24576
ds_read_b64_tr_b16 v[126:127], v33 offset:25600
v_mfma_f32_16x16x32_bf16 v[212:215], v[84:87], v[56:59], v[212:215]
v_mul_f32 v39, s47, v39
v_cvt_pk_bf16_f32 v43, v38, v39
v_mfma_f32_16x16x32_bf16 v[216:219], v[88:91], v[56:59], v[216:219]
s_waitcnt lgkmcnt(15)
v_permlane32_swap_b32 v40, v42
v_mfma_f32_16x16x32_bf16 v[220:223], v[92:95], v[56:59], v[220:223]
v_xor_b32 v26, 0x10000, v26
v_mfma_f32_16x16x32_bf16 v[224:227], v[64:67], v[60:63], v[224:227]
ds_read_b128 v[56:59], v25 offset:4096
v_xor_b32 v27, 0x10000, v27
v_mfma_f32_16x16x32_bf16 v[228:231], v[68:71], v[60:63], v[228:231]
buffer_load_dwordx4 v19, s[16:19], s26 offen offset:3072 lds
v_xor_b32 v28, 0x10000, v28
v_mfma_f32_16x16x32_bf16 v[232:235], v[72:75], v[60:63], v[232:235]
v_xor_b32 v29, 0x10000, v29
v_mfma_f32_16x16x32_bf16 v[236:239], v[76:79], v[60:63], v[236:239]
s_add_u32 s26, s26, 524288
v_xor_b32 v30, 0x10000, v30
v_mfma_f32_16x16x32_bf16 v[240:243], v[80:83], v[60:63], v[240:243]
v_xor_b32 v31, 0x10000, v31
v_mfma_f32_16x16x32_bf16 v[244:247], v[84:87], v[60:63], v[244:247]
v_xor_b32 v32, 0x10000, v32
v_mfma_f32_16x16x32_bf16 v[248:251], v[88:91], v[60:63], v[248:251]
v_xor_b32 v33, 0x10000, v33
v_mfma_f32_16x16x32_bf16 v[252:255], v[92:95], v[60:63], v[252:255]
s_waitcnt vmcnt(17) lgkmcnt(0)
s_barrier
v_accvgpr_read_b32 v36, a56
v_mfma_f32_16x16x32_bf16 v[128:131], v[96:99], v[48:51], v[128:131]
ds_read_b128 v[60:63], v25 offset:6144
v_accvgpr_read_b32 v37, a57
v_mfma_f32_16x16x32_bf16 v[132:135], v[100:103], v[48:51], v[132:135]
v_add_u32 v25, 4294868992, v25
v_mul_f32 v36, s47, v36
v_mfma_f32_16x16x32_bf16 v[136:139], v[104:107], v[48:51], v[136:139]
s_add_u32 m0, s92, 131072
ds_read_b64_tr_b16 v[64:65], v26 offset:0
ds_read_b64_tr_b16 v[66:67], v26 offset:1024
v_mfma_f32_16x16x32_bf16 v[140:143], v[108:111], v[48:51], v[140:143]
buffer_load_dwordx4 v20, s[12:15], s24 offen offset:0 lds
v_mul_f32 v37, s47, v37
v_mfma_f32_16x16x32_bf16 v[144:147], v[112:115], v[48:51], v[144:147]
ds_read_b64_tr_b16 v[68:69], v27 offset:0
ds_read_b64_tr_b16 v[70:71], v27 offset:1024
v_mfma_f32_16x16x32_bf16 v[148:151], v[116:119], v[48:51], v[148:151]
v_permlane32_swap_b32 v41, v43
v_mfma_f32_16x16x32_bf16 v[152:155], v[120:123], v[48:51], v[152:155]
ds_read_b64_tr_b16 v[72:73], v28 offset:0
ds_read_b64_tr_b16 v[74:75], v28 offset:1024
v_mfma_f32_16x16x32_bf16 v[156:159], v[124:127], v[48:51], v[156:159]
s_xor_b32 s48, s95, 67584
buffer_load_dwordx4 v21, s[12:15], s24 offen offset:1024 lds
v_mfma_f32_16x16x32_bf16 v[160:163], v[96:99], v[52:55], v[160:163]
ds_read_b128 v[48:51], v24 offset:0
v_cvt_pk_bf16_f32 v44, v36, v37
v_mfma_f32_16x16x32_bf16 v[164:167], v[100:103], v[52:55], v[164:167]
buffer_store_dwordx4 v[40:43], v7, s[20:23], s48 offen sc1
v_accvgpr_read_b32 v38, a58
v_mfma_f32_16x16x32_bf16 v[168:171], v[104:107], v[52:55], v[168:171]
ds_read_b64_tr_b16 v[76:77], v29 offset:0
ds_read_b64_tr_b16 v[78:79], v29 offset:1024
v_mfma_f32_16x16x32_bf16 v[172:175], v[108:111], v[52:55], v[172:175]
buffer_load_dwordx4 v22, s[12:15], s24 offen offset:2048 lds
v_accvgpr_read_b32 v39, a59
v_mfma_f32_16x16x32_bf16 v[176:179], v[112:115], v[52:55], v[176:179]
ds_read_b64_tr_b16 v[80:81], v30 offset:0
ds_read_b64_tr_b16 v[82:83], v30 offset:1024
v_mfma_f32_16x16x32_bf16 v[180:183], v[116:119], v[52:55], v[180:183]
v_mul_f32 v38, s47, v38
v_mul_f32 v39, s47, v39
v_mfma_f32_16x16x32_bf16 v[184:187], v[120:123], v[52:55], v[184:187]
ds_read_b64_tr_b16 v[84:85], v31 offset:0
ds_read_b64_tr_b16 v[86:87], v31 offset:1024
v_mfma_f32_16x16x32_bf16 v[188:191], v[124:127], v[52:55], v[188:191]
buffer_load_dwordx4 v23, s[12:15], s24 offen offset:3072 lds
v_cvt_pk_bf16_f32 v45, v38, v39
v_mfma_f32_16x16x32_bf16 v[192:195], v[96:99], v[56:59], v[192:195]
ds_read_b128 v[52:55], v24 offset:2048
v_accvgpr_read_b32 v36, a60
v_mfma_f32_16x16x32_bf16 v[196:199], v[100:103], v[56:59], v[196:199]
v_accvgpr_read_b32 v37, a61
v_mul_f32 v36, s47, v36
v_mfma_f32_16x16x32_bf16 v[200:203], v[104:107], v[56:59], v[200:203]
s_add_u32 m0, s92, 135168
ds_read_b64_tr_b16 v[88:89], v32 offset:0
ds_read_b64_tr_b16 v[90:91], v32 offset:1024
v_mfma_f32_16x16x32_bf16 v[204:207], v[108:111], v[56:59], v[204:207]
buffer_load_dwordx4 v20, s[12:15], s25 offen offset:0 lds
v_mul_f32 v37, s47, v37
v_mfma_f32_16x16x32_bf16 v[208:211], v[112:115], v[56:59], v[208:211]
ds_read_b64_tr_b16 v[92:93], v33 offset:0
ds_read_b64_tr_b16 v[94:95], v33 offset:1024
v_mfma_f32_16x16x32_bf16 v[212:215], v[116:119], v[56:59], v[212:215]
v_cvt_pk_bf16_f32 v46, v36, v37
v_accvgpr_read_b32 v38, a62
v_mfma_f32_16x16x32_bf16 v[216:219], v[120:123], v[56:59], v[216:219]
s_waitcnt lgkmcnt(15)
v_accvgpr_read_b32 v39, a63
v_mul_f32 v38, s47, v38
v_mfma_f32_16x16x32_bf16 v[220:223], v[124:127], v[56:59], v[220:223]
v_mul_f32 v39, s47, v39
v_cvt_pk_bf16_f32 v47, v38, v39
v_mfma_f32_16x16x32_bf16 v[224:227], v[96:99], v[60:63], v[224:227]
ds_read_b128 v[56:59], v24 offset:4096
v_accvgpr_read_b32 v36, a176
v_mfma_f32_16x16x32_bf16 v[228:231], v[100:103], v[60:63], v[228:231]
buffer_load_dwordx4 v21, s[12:15], s25 offen offset:1024 lds
v_accvgpr_read_b32 v37, a177
v_mfma_f32_16x16x32_bf16 v[232:235], v[104:107], v[60:63], v[232:235]
v_permlane32_swap_b32 v44, v46
v_mfma_f32_16x16x32_bf16 v[236:239], v[108:111], v[60:63], v[236:239]
v_permlane32_swap_b32 v45, v47
v_mfma_f32_16x16x32_bf16 v[240:243], v[112:115], v[60:63], v[240:243]
s_xor_b32 s48, s95, 68608
buffer_store_dwordx4 v[44:47], v7, s[20:23], s48 offen sc1
v_mfma_f32_16x16x32_bf16 v[244:247], v[116:119], v[60:63], v[244:247]
v_mul_f32 v36, s47, v36
v_mul_f32 v37, s47, v37
v_mfma_f32_16x16x32_bf16 v[248:251], v[120:123], v[60:63], v[248:251]
v_cvt_pk_bf16_f32 v40, v36, v37
v_accvgpr_read_b32 v38, a178
v_mfma_f32_16x16x32_bf16 v[252:255], v[124:127], v[60:63], v[252:255]
s_waitcnt lgkmcnt(0)
v_accvgpr_read_b32 v39, a179
v_mul_f32 v38, s47, v38
v_mfma_f32_16x16x32_bf16 v[128:131], v[64:67], v[48:51], v[128:131]
ds_read_b128 v[60:63], v24 offset:6144
v_mul_f32 v39, s47, v39
v_mfma_f32_16x16x32_bf16 v[132:135], v[68:71], v[48:51], v[132:135]
v_add_u32 v24, 65536, v24
v_cvt_pk_bf16_f32 v41, v38, v39
v_mfma_f32_16x16x32_bf16 v[136:139], v[72:75], v[48:51], v[136:139]
ds_read_b64_tr_b16 v[96:97], v26 offset:8192
ds_read_b64_tr_b16 v[98:99], v26 offset:9216
v_mfma_f32_16x16x32_bf16 v[140:143], v[76:79], v[48:51], v[140:143]
buffer_load_dwordx4 v22, s[12:15], s25 offen offset:2048 lds
v_accvgpr_read_b32 v36, a180
v_mfma_f32_16x16x32_bf16 v[144:147], v[80:83], v[48:51], v[144:147]
ds_read_b64_tr_b16 v[100:101], v27 offset:8192
ds_read_b64_tr_b16 v[102:103], v27 offset:9216
v_mfma_f32_16x16x32_bf16 v[148:151], v[84:87], v[48:51], v[148:151]
v_accvgpr_read_b32 v37, a181
v_mul_f32 v36, s47, v36
v_mfma_f32_16x16x32_bf16 v[152:155], v[88:91], v[48:51], v[152:155]
ds_read_b64_tr_b16 v[104:105], v28 offset:8192
ds_read_b64_tr_b16 v[106:107], v28 offset:9216
v_mfma_f32_16x16x32_bf16 v[156:159], v[92:95], v[48:51], v[156:159]
buffer_load_dwordx4 v23, s[12:15], s25 offen offset:3072 lds
v_mul_f32 v37, s47, v37
v_mfma_f32_16x16x32_bf16 v[160:163], v[64:67], v[52:55], v[160:163]
ds_read_b128 v[48:51], v25 offset:0
v_cvt_pk_bf16_f32 v42, v36, v37
v_mfma_f32_16x16x32_bf16 v[164:167], v[68:71], v[52:55], v[164:167]
s_add_u32 s24, s24, 128
s_add_u32 s25, s25, 128
v_accvgpr_read_b32 v38, a182
v_mfma_f32_16x16x32_bf16 v[168:171], v[72:75], v[52:55], v[168:171]
s_add_u32 m0, s91, 81920
ds_read_b64_tr_b16 v[108:109], v29 offset:8192
ds_read_b64_tr_b16 v[110:111], v29 offset:9216
v_mfma_f32_16x16x32_bf16 v[172:175], v[76:79], v[52:55], v[172:175]
buffer_load_dwordx4 v16, s[16:19], s26 offen offset:0 lds
v_accvgpr_read_b32 v39, a183
v_mfma_f32_16x16x32_bf16 v[176:179], v[80:83], v[52:55], v[176:179]
ds_read_b64_tr_b16 v[112:113], v30 offset:8192
ds_read_b64_tr_b16 v[114:115], v30 offset:9216
v_mfma_f32_16x16x32_bf16 v[180:183], v[84:87], v[52:55], v[180:183]
v_mul_f32 v38, s47, v38
v_mul_f32 v39, s47, v39
v_mfma_f32_16x16x32_bf16 v[184:187], v[88:91], v[52:55], v[184:187]
ds_read_b64_tr_b16 v[116:117], v31 offset:8192
ds_read_b64_tr_b16 v[118:119], v31 offset:9216
v_mfma_f32_16x16x32_bf16 v[188:191], v[92:95], v[52:55], v[188:191]
buffer_load_dwordx4 v17, s[16:19], s26 offen offset:1024 lds
v_cvt_pk_bf16_f32 v43, v38, v39
v_mfma_f32_16x16x32_bf16 v[192:195], v[64:67], v[56:59], v[192:195]
ds_read_b128 v[52:55], v25 offset:2048
v_mfma_f32_16x16x32_bf16 v[196:199], v[68:71], v[56:59], v[196:199]
v_permlane32_swap_b32 v40, v42
v_mfma_f32_16x16x32_bf16 v[200:203], v[72:75], v[56:59], v[200:203]
ds_read_b64_tr_b16 v[120:121], v32 offset:8192
ds_read_b64_tr_b16 v[122:123], v32 offset:9216
v_mfma_f32_16x16x32_bf16 v[204:207], v[76:79], v[56:59], v[204:207]
buffer_load_dwordx4 v18, s[16:19], s26 offen offset:2048 lds
v_mfma_f32_16x16x32_bf16 v[208:211], v[80:83], v[56:59], v[208:211]
ds_read_b64_tr_b16 v[124:125], v33 offset:8192
ds_read_b64_tr_b16 v[126:127], v33 offset:9216
v_mfma_f32_16x16x32_bf16 v[212:215], v[84:87], v[56:59], v[212:215]
v_permlane32_swap_b32 v41, v43
v_mfma_f32_16x16x32_bf16 v[216:219], v[88:91], v[56:59], v[216:219]
s_waitcnt lgkmcnt(15)
v_accvgpr_write_b32 a168, v40
v_accvgpr_write_b32 a169, v41
v_mfma_f32_16x16x32_bf16 v[220:223], v[92:95], v[56:59], v[220:223]
v_accvgpr_write_b32 a170, v42
v_accvgpr_write_b32 a171, v43
v_mfma_f32_16x16x32_bf16 v[224:227], v[64:67], v[60:63], v[224:227]
ds_read_b128 v[56:59], v25 offset:4096
v_accvgpr_read_b32 v36, a64
v_mfma_f32_16x16x32_bf16 v[228:231], v[68:71], v[60:63], v[228:231]
buffer_load_dwordx4 v19, s[16:19], s26 offen offset:3072 lds
v_accvgpr_read_b32 v37, a65
v_mfma_f32_16x16x32_bf16 v[232:235], v[72:75], v[60:63], v[232:235]
v_mul_f32 v36, s47, v36
v_mul_f32 v37, s47, v37
v_mfma_f32_16x16x32_bf16 v[236:239], v[76:79], v[60:63], v[236:239]
s_add_u32 s26, s26, 524288
v_cvt_pk_bf16_f32 v44, v36, v37
v_accvgpr_read_b32 v38, a66
v_mfma_f32_16x16x32_bf16 v[240:243], v[80:83], v[60:63], v[240:243]
v_accvgpr_read_b32 v39, a67
v_mul_f32 v38, s47, v38
v_mfma_f32_16x16x32_bf16 v[244:247], v[84:87], v[60:63], v[244:247]
v_mul_f32 v39, s47, v39
v_cvt_pk_bf16_f32 v45, v38, v39
v_mfma_f32_16x16x32_bf16 v[248:251], v[88:91], v[60:63], v[248:251]
v_accvgpr_read_b32 v36, a68
v_accvgpr_read_b32 v37, a69
v_mfma_f32_16x16x32_bf16 v[252:255], v[92:95], v[60:63], v[252:255]
s_waitcnt vmcnt(18) lgkmcnt(0)
s_barrier
v_mul_f32 v36, s47, v36
v_mfma_f32_16x16x32_bf16 v[128:131], v[96:99], v[48:51], v[128:131]
ds_read_b128 v[60:63], v25 offset:6144
v_mul_f32 v37, s47, v37
v_mfma_f32_16x16x32_bf16 v[132:135], v[100:103], v[48:51], v[132:135]
v_add_u32 v25, 65536, v25
v_cvt_pk_bf16_f32 v46, v36, v37
v_mfma_f32_16x16x32_bf16 v[136:139], v[104:107], v[48:51], v[136:139]
s_add_u32 m0, s92, 32768
ds_read_b64_tr_b16 v[64:65], v26 offset:16384
ds_read_b64_tr_b16 v[66:67], v26 offset:17408
v_mfma_f32_16x16x32_bf16 v[140:143], v[108:111], v[48:51], v[140:143]
buffer_load_dwordx4 v20, s[12:15], s24 offen offset:0 lds
v_accvgpr_read_b32 v38, a70
v_mfma_f32_16x16x32_bf16 v[144:147], v[112:115], v[48:51], v[144:147]
ds_read_b64_tr_b16 v[68:69], v27 offset:16384
ds_read_b64_tr_b16 v[70:71], v27 offset:17408
v_mfma_f32_16x16x32_bf16 v[148:151], v[116:119], v[48:51], v[148:151]
v_accvgpr_read_b32 v39, a71
v_mul_f32 v38, s47, v38
v_mfma_f32_16x16x32_bf16 v[152:155], v[120:123], v[48:51], v[152:155]
ds_read_b64_tr_b16 v[72:73], v28 offset:16384
ds_read_b64_tr_b16 v[74:75], v28 offset:17408
v_mfma_f32_16x16x32_bf16 v[156:159], v[124:127], v[48:51], v[156:159]
buffer_load_dwordx4 v21, s[12:15], s24 offen offset:1024 lds
v_mul_f32 v39, s47, v39
v_mfma_f32_16x16x32_bf16 v[160:163], v[96:99], v[52:55], v[160:163]
ds_read_b128 v[48:51], v24 offset:0
v_cvt_pk_bf16_f32 v47, v38, v39
v_mfma_f32_16x16x32_bf16 v[164:167], v[100:103], v[52:55], v[164:167]
v_permlane32_swap_b32 v44, v46
v_mfma_f32_16x16x32_bf16 v[168:171], v[104:107], v[52:55], v[168:171]
ds_read_b64_tr_b16 v[76:77], v29 offset:16384
ds_read_b64_tr_b16 v[78:79], v29 offset:17408
v_mfma_f32_16x16x32_bf16 v[172:175], v[108:111], v[52:55], v[172:175]
buffer_load_dwordx4 v22, s[12:15], s24 offen offset:2048 lds
v_accvgpr_read_b32 v36, a184
v_mfma_f32_16x16x32_bf16 v[176:179], v[112:115], v[52:55], v[176:179]
ds_read_b64_tr_b16 v[80:81], v30 offset:16384
ds_read_b64_tr_b16 v[82:83], v30 offset:17408
v_mfma_f32_16x16x32_bf16 v[180:183], v[116:119], v[52:55], v[180:183]
v_permlane32_swap_b32 v45, v47
v_mfma_f32_16x16x32_bf16 v[184:187], v[120:123], v[52:55], v[184:187]
ds_read_b64_tr_b16 v[84:85], v31 offset:16384
ds_read_b64_tr_b16 v[86:87], v31 offset:17408
v_mfma_f32_16x16x32_bf16 v[188:191], v[124:127], v[52:55], v[188:191]
s_xor_b32 s48, s95, 131072
buffer_load_dwordx4 v23, s[12:15], s24 offen offset:3072 lds
v_mfma_f32_16x16x32_bf16 v[192:195], v[96:99], v[56:59], v[192:195]
ds_read_b128 v[52:55], v24 offset:2048
v_accvgpr_read_b32 v37, a185
v_mfma_f32_16x16x32_bf16 v[196:199], v[100:103], v[56:59], v[196:199]
buffer_store_dwordx4 v[44:47], v7, s[20:23], s48 offen sc1
v_mul_f32 v36, s47, v36
v_mfma_f32_16x16x32_bf16 v[200:203], v[104:107], v[56:59], v[200:203]
s_add_u32 m0, s92, 36864
ds_read_b64_tr_b16 v[88:89], v32 offset:16384
ds_read_b64_tr_b16 v[90:91], v32 offset:17408
v_mfma_f32_16x16x32_bf16 v[204:207], v[108:111], v[56:59], v[204:207]
buffer_load_dwordx4 v20, s[12:15], s25 offen offset:0 lds
v_mul_f32 v37, s47, v37
v_mfma_f32_16x16x32_bf16 v[208:211], v[112:115], v[56:59], v[208:211]
ds_read_b64_tr_b16 v[92:93], v33 offset:16384
ds_read_b64_tr_b16 v[94:95], v33 offset:17408
v_mfma_f32_16x16x32_bf16 v[212:215], v[116:119], v[56:59], v[212:215]
v_cvt_pk_bf16_f32 v40, v36, v37
v_accvgpr_read_b32 v38, a186
v_mfma_f32_16x16x32_bf16 v[216:219], v[120:123], v[56:59], v[216:219]
s_waitcnt lgkmcnt(15)
v_accvgpr_read_b32 v39, a187
v_mul_f32 v38, s47, v38
v_mfma_f32_16x16x32_bf16 v[220:223], v[124:127], v[56:59], v[220:223]
v_mul_f32 v39, s47, v39
v_cvt_pk_bf16_f32 v41, v38, v39
v_mfma_f32_16x16x32_bf16 v[224:227], v[96:99], v[60:63], v[224:227]
ds_read_b128 v[56:59], v24 offset:4096
v_accvgpr_read_b32 v36, a188
v_mfma_f32_16x16x32_bf16 v[228:231], v[100:103], v[60:63], v[228:231]
buffer_load_dwordx4 v21, s[12:15], s25 offen offset:1024 lds
v_accvgpr_read_b32 v37, a189
v_mfma_f32_16x16x32_bf16 v[232:235], v[104:107], v[60:63], v[232:235]
v_mul_f32 v36, s47, v36
v_mul_f32 v37, s47, v37
v_mfma_f32_16x16x32_bf16 v[236:239], v[108:111], v[60:63], v[236:239]
v_cvt_pk_bf16_f32 v42, v36, v37
v_accvgpr_read_b32 v38, a190
v_mfma_f32_16x16x32_bf16 v[240:243], v[112:115], v[60:63], v[240:243]
v_accvgpr_read_b32 v39, a191
v_mul_f32 v38, s47, v38
v_mfma_f32_16x16x32_bf16 v[244:247], v[116:119], v[60:63], v[244:247]
v_mul_f32 v39, s47, v39
v_cvt_pk_bf16_f32 v43, v38, v39
v_mfma_f32_16x16x32_bf16 v[248:251], v[120:123], v[60:63], v[248:251]
v_permlane32_swap_b32 v40, v42
v_mfma_f32_16x16x32_bf16 v[252:255], v[124:127], v[60:63], v[252:255]
s_waitcnt lgkmcnt(0)
v_permlane32_swap_b32 v41, v43
v_mfma_f32_16x16x32_bf16 v[128:131], v[64:67], v[48:51], v[128:131]
ds_read_b128 v[60:63], v24 offset:6144
v_accvgpr_write_b32 a172, v40
v_mfma_f32_16x16x32_bf16 v[132:135], v[68:71], v[48:51], v[132:135]
v_add_u32 v24, 32768, v24
v_accvgpr_write_b32 a173, v41
v_mfma_f32_16x16x32_bf16 v[136:139], v[72:75], v[48:51], v[136:139]
ds_read_b64_tr_b16 v[96:97], v26 offset:24576
ds_read_b64_tr_b16 v[98:99], v26 offset:25600
v_mfma_f32_16x16x32_bf16 v[140:143], v[76:79], v[48:51], v[140:143]
buffer_load_dwordx4 v22, s[12:15], s25 offen offset:2048 lds
v_accvgpr_write_b32 a174, v42
v_mfma_f32_16x16x32_bf16 v[144:147], v[80:83], v[48:51], v[144:147]
ds_read_b64_tr_b16 v[100:101], v27 offset:24576
ds_read_b64_tr_b16 v[102:103], v27 offset:25600
v_mfma_f32_16x16x32_bf16 v[148:151], v[84:87], v[48:51], v[148:151]
v_accvgpr_write_b32 a175, v43
v_accvgpr_read_b32 v36, a72
v_mfma_f32_16x16x32_bf16 v[152:155], v[88:91], v[48:51], v[152:155]
ds_read_b64_tr_b16 v[104:105], v28 offset:24576
ds_read_b64_tr_b16 v[106:107], v28 offset:25600
v_mfma_f32_16x16x32_bf16 v[156:159], v[92:95], v[48:51], v[156:159]
buffer_load_dwordx4 v23, s[12:15], s25 offen offset:3072 lds
v_accvgpr_read_b32 v37, a73
v_mfma_f32_16x16x32_bf16 v[160:163], v[64:67], v[52:55], v[160:163]
ds_read_b128 v[48:51], v25 offset:0
v_mul_f32 v36, s47, v36
v_mfma_f32_16x16x32_bf16 v[164:167], v[68:71], v[52:55], v[164:167]
s_add_u32 s24, s24, 128
s_add_u32 s25, s25, 128
v_mul_f32 v37, s47, v37
v_mfma_f32_16x16x32_bf16 v[168:171], v[72:75], v[52:55], v[168:171]
s_add_u32 m0, s91, 0
ds_read_b64_tr_b16 v[108:109], v29 offset:24576
ds_read_b64_tr_b16 v[110:111], v29 offset:25600
v_mfma_f32_16x16x32_bf16 v[172:175], v[76:79], v[52:55], v[172:175]
buffer_load_dwordx4 v16, s[16:19], s26 offen offset:0 lds
v_cvt_pk_bf16_f32 v44, v36, v37
v_mfma_f32_16x16x32_bf16 v[176:179], v[80:83], v[52:55], v[176:179]
ds_read_b64_tr_b16 v[112:113], v30 offset:24576
ds_read_b64_tr_b16 v[114:115], v30 offset:25600
v_mfma_f32_16x16x32_bf16 v[180:183], v[84:87], v[52:55], v[180:183]
v_accvgpr_read_b32 v38, a74
v_accvgpr_read_b32 v39, a75
v_mfma_f32_16x16x32_bf16 v[184:187], v[88:91], v[52:55], v[184:187]
ds_read_b64_tr_b16 v[116:117], v31 offset:24576
ds_read_b64_tr_b16 v[118:119], v31 offset:25600
v_mfma_f32_16x16x32_bf16 v[188:191], v[92:95], v[52:55], v[188:191]
buffer_load_dwordx4 v17, s[16:19], s26 offen offset:1024 lds
v_mul_f32 v38, s47, v38
v_mfma_f32_16x16x32_bf16 v[192:195], v[64:67], v[56:59], v[192:195]
ds_read_b128 v[52:55], v25 offset:2048
v_mul_f32 v39, s47, v39
v_mfma_f32_16x16x32_bf16 v[196:199], v[68:71], v[56:59], v[196:199]
v_cvt_pk_bf16_f32 v45, v38, v39
v_accvgpr_read_b32 v36, a76
v_mfma_f32_16x16x32_bf16 v[200:203], v[72:75], v[56:59], v[200:203]
ds_read_b64_tr_b16 v[120:121], v32 offset:24576
ds_read_b64_tr_b16 v[122:123], v32 offset:25600
v_mfma_f32_16x16x32_bf16 v[204:207], v[76:79], v[56:59], v[204:207]
buffer_load_dwordx4 v18, s[16:19], s26 offen offset:2048 lds
v_accvgpr_read_b32 v37, a77
v_mfma_f32_16x16x32_bf16 v[208:211], v[80:83], v[56:59], v[208:211]
ds_read_b64_tr_b16 v[124:125], v33 offset:24576
ds_read_b64_tr_b16 v[126:127], v33 offset:25600
v_mfma_f32_16x16x32_bf16 v[212:215], v[84:87], v[56:59], v[212:215]
v_mul_f32 v36, s47, v36
v_mul_f32 v37, s47, v37
v_mfma_f32_16x16x32_bf16 v[216:219], v[88:91], v[56:59], v[216:219]
s_waitcnt lgkmcnt(15)
v_cvt_pk_bf16_f32 v46, v36, v37
v_accvgpr_read_b32 v38, a78
v_mfma_f32_16x16x32_bf16 v[220:223], v[92:95], v[56:59], v[220:223]
v_xor_b32 v26, 0x10000, v26
v_accvgpr_read_b32 v39, a79
v_mfma_f32_16x16x32_bf16 v[224:227], v[64:67], v[60:63], v[224:227]
ds_read_b128 v[56:59], v25 offset:4096
v_xor_b32 v27, 0x10000, v27
v_mfma_f32_16x16x32_bf16 v[228:231], v[68:71], v[60:63], v[228:231]
buffer_load_dwordx4 v19, s[16:19], s26 offen offset:3072 lds
v_xor_b32 v28, 0x10000, v28
v_mfma_f32_16x16x32_bf16 v[232:235], v[72:75], v[60:63], v[232:235]
v_xor_b32 v29, 0x10000, v29
v_mul_f32 v38, s47, v38
v_mfma_f32_16x16x32_bf16 v[236:239], v[76:79], v[60:63], v[236:239]
s_add_u32 s26, s26, 524288
v_xor_b32 v30, 0x10000, v30
v_mul_f32 v39, s47, v39
v_mfma_f32_16x16x32_bf16 v[240:243], v[80:83], v[60:63], v[240:243]
v_xor_b32 v31, 0x10000, v31
v_cvt_pk_bf16_f32 v47, v38, v39
v_mfma_f32_16x16x32_bf16 v[244:247], v[84:87], v[60:63], v[244:247]
v_xor_b32 v32, 0x10000, v32
v_accvgpr_read_b32 v36, a192
v_mfma_f32_16x16x32_bf16 v[248:251], v[88:91], v[60:63], v[248:251]
v_xor_b32 v33, 0x10000, v33
v_accvgpr_read_b32 v37, a193
v_mfma_f32_16x16x32_bf16 v[252:255], v[92:95], v[60:63], v[252:255]
s_waitcnt vmcnt(17) lgkmcnt(0)
s_barrier
v_mul_f32 v36, s47, v36
v_mfma_f32_16x16x32_bf16 v[128:131], v[96:99], v[48:51], v[128:131]
ds_read_b128 v[60:63], v25 offset:6144
v_mul_f32 v37, s47, v37
v_mfma_f32_16x16x32_bf16 v[132:135], v[100:103], v[48:51], v[132:135]
v_add_u32 v25, 32768, v25
v_cvt_pk_bf16_f32 v40, v36, v37
v_mfma_f32_16x16x32_bf16 v[136:139], v[104:107], v[48:51], v[136:139]
s_add_u32 m0, s92, 98304
ds_read_b64_tr_b16 v[64:65], v26 offset:0
ds_read_b64_tr_b16 v[66:67], v26 offset:1024
v_mfma_f32_16x16x32_bf16 v[140:143], v[108:111], v[48:51], v[140:143]
buffer_load_dwordx4 v20, s[12:15], s24 offen offset:0 lds
v_accvgpr_read_b32 v38, a194
v_mfma_f32_16x16x32_bf16 v[144:147], v[112:115], v[48:51], v[144:147]
ds_read_b64_tr_b16 v[68:69], v27 offset:0
ds_read_b64_tr_b16 v[70:71], v27 offset:1024
v_mfma_f32_16x16x32_bf16 v[148:151], v[116:119], v[48:51], v[148:151]
v_permlane32_swap_b32 v44, v46
v_mfma_f32_16x16x32_bf16 v[152:155], v[120:123], v[48:51], v[152:155]
ds_read_b64_tr_b16 v[72:73], v28 offset:0
ds_read_b64_tr_b16 v[74:75], v28 offset:1024
v_mfma_f32_16x16x32_bf16 v[156:159], v[124:127], v[48:51], v[156:159]
buffer_load_dwordx4 v21, s[12:15], s24 offen offset:1024 lds
v_accvgpr_read_b32 v39, a195
v_mfma_f32_16x16x32_bf16 v[160:163], v[96:99], v[52:55], v[160:163]
ds_read_b128 v[48:51], v24 offset:0
v_mul_f32 v38, s47, v38
v_mfma_f32_16x16x32_bf16 v[164:167], v[100:103], v[52:55], v[164:167]
v_permlane32_swap_b32 v45, v47
v_mfma_f32_16x16x32_bf16 v[168:171], v[104:107], v[52:55], v[168:171]
ds_read_b64_tr_b16 v[76:77], v29 offset:0
ds_read_b64_tr_b16 v[78:79], v29 offset:1024
v_mfma_f32_16x16x32_bf16 v[172:175], v[108:111], v[52:55], v[172:175]
s_xor_b32 s48, s95, 132096
buffer_load_dwordx4 v22, s[12:15], s24 offen offset:2048 lds
v_mfma_f32_16x16x32_bf16 v[176:179], v[112:115], v[52:55], v[176:179]
ds_read_b64_tr_b16 v[80:81], v30 offset:0
ds_read_b64_tr_b16 v[82:83], v30 offset:1024
v_mfma_f32_16x16x32_bf16 v[180:183], v[116:119], v[52:55], v[180:183]
buffer_store_dwordx4 v[44:47], v7, s[20:23], s48 offen sc1
v_mul_f32 v39, s47, v39
v_mfma_f32_16x16x32_bf16 v[184:187], v[120:123], v[52:55], v[184:187]
ds_read_b64_tr_b16 v[84:85], v31 offset:0
ds_read_b64_tr_b16 v[86:87], v31 offset:1024
v_mfma_f32_16x16x32_bf16 v[188:191], v[124:127], v[52:55], v[188:191]
buffer_load_dwordx4 v23, s[12:15], s24 offen offset:3072 lds
v_cvt_pk_bf16_f32 v41, v38, v39
v_mfma_f32_16x16x32_bf16 v[192:195], v[96:99], v[56:59], v[192:195]
ds_read_b128 v[52:55], v24 offset:2048
v_accvgpr_read_b32 v36, a196
v_mfma_f32_16x16x32_bf16 v[196:199], v[100:103], v[56:59], v[196:199]
v_accvgpr_read_b32 v37, a197
v_mul_f32 v36, s47, v36
v_mfma_f32_16x16x32_bf16 v[200:203], v[104:107], v[56:59], v[200:203]
s_add_u32 m0, s92, 102400
ds_read_b64_tr_b16 v[88:89], v32 offset:0
ds_read_b64_tr_b16 v[90:91], v32 offset:1024
v_mfma_f32_16x16x32_bf16 v[204:207], v[108:111], v[56:59], v[204:207]
buffer_load_dwordx4 v20, s[12:15], s25 offen offset:0 lds
v_mul_f32 v37, s47, v37
v_mfma_f32_16x16x32_bf16 v[208:211], v[112:115], v[56:59], v[208:211]
ds_read_b64_tr_b16 v[92:93], v33 offset:0
ds_read_b64_tr_b16 v[94:95], v33 offset:1024
v_mfma_f32_16x16x32_bf16 v[212:215], v[116:119], v[56:59], v[212:215]
v_cvt_pk_bf16_f32 v42, v36, v37
v_accvgpr_read_b32 v38, a198
v_mfma_f32_16x16x32_bf16 v[216:219], v[120:123], v[56:59], v[216:219]
s_waitcnt lgkmcnt(15)
v_accvgpr_read_b32 v39, a199
v_mul_f32 v38, s47, v38
v_mfma_f32_16x16x32_bf16 v[220:223], v[124:127], v[56:59], v[220:223]
v_mul_f32 v39, s47, v39
v_cvt_pk_bf16_f32 v43, v38, v39
v_mfma_f32_16x16x32_bf16 v[224:227], v[96:99], v[60:63], v[224:227]
ds_read_b128 v[56:59], v24 offset:4096
v_mfma_f32_16x16x32_bf16 v[228:231], v[100:103], v[60:63], v[228:231]
buffer_load_dwordx4 v21, s[12:15], s25 offen offset:1024 lds
v_mfma_f32_16x16x32_bf16 v[232:235], v[104:107], v[60:63], v[232:235]
v_permlane32_swap_b32 v40, v42
v_mfma_f32_16x16x32_bf16 v[236:239], v[108:111], v[60:63], v[236:239]
v_permlane32_swap_b32 v41, v43
v_mfma_f32_16x16x32_bf16 v[240:243], v[112:115], v[60:63], v[240:243]
v_accvgpr_write_b32 a192, v40
v_accvgpr_write_b32 a193, v41
v_mfma_f32_16x16x32_bf16 v[244:247], v[116:119], v[60:63], v[244:247]
v_accvgpr_write_b32 a194, v42
v_accvgpr_write_b32 a195, v43
v_mfma_f32_16x16x32_bf16 v[248:251], v[120:123], v[60:63], v[248:251]
v_accvgpr_read_b32 v36, a80
v_accvgpr_read_b32 v37, a81
v_mfma_f32_16x16x32_bf16 v[252:255], v[124:127], v[60:63], v[252:255]
s_waitcnt lgkmcnt(0)
v_mul_f32 v36, s47, v36
v_mul_f32 v37, s47, v37
v_mfma_f32_16x16x32_bf16 v[128:131], v[64:67], v[48:51], v[128:131]
ds_read_b128 v[60:63], v24 offset:6144
v_cvt_pk_bf16_f32 v44, v36, v37
v_mfma_f32_16x16x32_bf16 v[132:135], v[68:71], v[48:51], v[132:135]
v_add_u32 v24, 4294868992, v24
v_accvgpr_read_b32 v38, a82
v_mfma_f32_16x16x32_bf16 v[136:139], v[72:75], v[48:51], v[136:139]
ds_read_b64_tr_b16 v[96:97], v26 offset:8192
ds_read_b64_tr_b16 v[98:99], v26 offset:9216
v_mfma_f32_16x16x32_bf16 v[140:143], v[76:79], v[48:51], v[140:143]
s_lshl_b32 s28, s88, 19
buffer_load_dwordx4 v22, s[12:15], s25 offen offset:2048 lds
v_mfma_f32_16x16x32_bf16 v[144:147], v[80:83], v[48:51], v[144:147]
ds_read_b64_tr_b16 v[100:101], v27 offset:8192
ds_read_b64_tr_b16 v[102:103], v27 offset:9216
v_mfma_f32_16x16x32_bf16 v[148:151], v[84:87], v[48:51], v[148:151]
s_add_u32 s28, s28, s91
v_accvgpr_read_b32 v39, a83
v_mul_f32 v38, s47, v38
v_mfma_f32_16x16x32_bf16 v[152:155], v[88:91], v[48:51], v[152:155]
ds_read_b64_tr_b16 v[104:105], v28 offset:8192
ds_read_b64_tr_b16 v[106:107], v28 offset:9216
v_mfma_f32_16x16x32_bf16 v[156:159], v[92:95], v[48:51], v[156:159]
s_lshl_b32 s29, s89, 15
buffer_load_dwordx4 v23, s[12:15], s25 offen offset:3072 lds
v_mfma_f32_16x16x32_bf16 v[160:163], v[64:67], v[52:55], v[160:163]
s_sub_u32 s54, 0x80000, s29
ds_read_b128 v[48:51], v25 offset:0
v_mfma_f32_16x16x32_bf16 v[164:167], v[68:71], v[52:55], v[164:167]
s_add_u32 s24, s24, 128
s_add_u32 s25, s25, 128
v_mul_f32 v39, s47, v39
v_mfma_f32_16x16x32_bf16 v[168:171], v[72:75], v[52:55], v[168:171]
s_add_u32 m0, s91, 16384
ds_read_b64_tr_b16 v[108:109], v29 offset:8192
ds_read_b64_tr_b16 v[110:111], v29 offset:9216
v_mfma_f32_16x16x32_bf16 v[172:175], v[76:79], v[52:55], v[172:175]
s_sub_u32 s29, s54, s91
buffer_load_dwordx4 v16, s[16:19], s26 offen offset:0 lds
v_mfma_f32_16x16x32_bf16 v[176:179], v[80:83], v[52:55], v[176:179]
ds_read_b64_tr_b16 v[112:113], v30 offset:8192
ds_read_b64_tr_b16 v[114:115], v30 offset:9216
v_mfma_f32_16x16x32_bf16 v[180:183], v[84:87], v[52:55], v[180:183]
s_add_u32 s4, s84, s28
s_addc_u32 s5, s85, 0
v_cvt_pk_bf16_f32 v45, v38, v39
v_mfma_f32_16x16x32_bf16 v[184:187], v[88:91], v[52:55], v[184:187]
ds_read_b64_tr_b16 v[116:117], v31 offset:8192
ds_read_b64_tr_b16 v[118:119], v31 offset:9216
v_mfma_f32_16x16x32_bf16 v[188:191], v[92:95], v[52:55], v[188:191]
s_and_b32 s5, s5, 0xffff
buffer_load_dwordx4 v17, s[16:19], s26 offen offset:1024 lds
v_mfma_f32_16x16x32_bf16 v[192:195], v[64:67], v[56:59], v[192:195]
s_mov_b32 s6, s29
ds_read_b128 v[52:55], v25 offset:2048
v_mfma_f32_16x16x32_bf16 v[196:199], v[68:71], v[56:59], v[196:199]
s_mov_b32 s7, 0x20000
v_accvgpr_read_b32 v36, a84
v_accvgpr_read_b32 v37, a85
v_mfma_f32_16x16x32_bf16 v[200:203], v[72:75], v[56:59], v[200:203]
ds_read_b64_tr_b16 v[120:121], v32 offset:8192
ds_read_b64_tr_b16 v[122:123], v32 offset:9216
v_mfma_f32_16x16x32_bf16 v[204:207], v[76:79], v[56:59], v[204:207]
s_add_u32 s28, s28, 16777216
buffer_load_dwordx4 v18, s[16:19], s26 offen offset:2048 lds
v_mfma_f32_16x16x32_bf16 v[208:211], v[80:83], v[56:59], v[208:211]
ds_read_b64_tr_b16 v[124:125], v33 offset:8192
ds_read_b64_tr_b16 v[126:127], v33 offset:9216
v_mfma_f32_16x16x32_bf16 v[212:215], v[84:87], v[56:59], v[212:215]
s_add_u32 s8, s84, s28
s_addc_u32 s9, s85, 0
v_mul_f32 v36, s47, v36
v_mfma_f32_16x16x32_bf16 v[216:219], v[88:91], v[56:59], v[216:219]
s_waitcnt lgkmcnt(15)
s_and_b32 s9, s9, 0xffff
v_mul_f32 v37, s47, v37
v_mfma_f32_16x16x32_bf16 v[220:223], v[92:95], v[56:59], v[220:223]
s_mov_b32 s10, s29
v_cvt_pk_bf16_f32 v46, v36, v37
v_accvgpr_read_b32 v38, a86
v_mfma_f32_16x16x32_bf16 v[224:227], v[64:67], v[60:63], v[224:227]
s_mov_b32 s11, 0x20000
ds_read_b128 v[56:59], v25 offset:4096
v_mfma_f32_16x16x32_bf16 v[228:231], v[68:71], v[60:63], v[228:231]
s_and_b32 s46, s89, 1
buffer_load_dwordx4 v19, s[16:19], s26 offen offset:3072 lds
v_mfma_f32_16x16x32_bf16 v[232:235], v[72:75], v[60:63], v[232:235]
s_lshl_b32 s46, s46, 15
v_accvgpr_read_b32 v39, a87
v_mul_f32 v38, s47, v38
v_mfma_f32_16x16x32_bf16 v[236:239], v[76:79], v[60:63], v[236:239]
s_add_u32 s26, s26, 524288
v_mul_f32 v39, s47, v39
v_cvt_pk_bf16_f32 v47, v38, v39
v_mfma_f32_16x16x32_bf16 v[240:243], v[80:83], v[60:63], v[240:243]
s_mov_b32 s39, s46
v_permlane32_swap_b32 v44, v46
v_mfma_f32_16x16x32_bf16 v[244:247], v[84:87], v[60:63], v[244:247]
s_or_b32 s52, s91, 0x18000
v_permlane32_swap_b32 v45, v47
v_mfma_f32_16x16x32_bf16 v[248:251], v[88:91], v[60:63], v[248:251]
s_or_b32 s53, s91, 0x8000
s_xor_b32 s48, s95, 133120
v_accvgpr_read_b32 v36, a200
v_mfma_f32_16x16x32_bf16 v[252:255], v[92:95], v[60:63], v[252:255]
s_waitcnt vmcnt(17) lgkmcnt(0)
s_barrier
s_mov_b32 s27, 0x30000
v_mfma_f32_16x16x32_bf16 v[128:131], v[96:99], v[48:51], v[128:131]
ds_read_b128 v[60:63], v25 offset:6144
v_accvgpr_read_b32 v37, a201
v_mfma_f32_16x16x32_bf16 v[132:135], v[100:103], v[48:51], v[132:135]
buffer_store_dwordx4 v[44:47], v7, s[20:23], s48 offen sc1
v_add_u32 v25, 4294868992, v25
v_mfma_f32_16x16x32_bf16 v[136:139], v[104:107], v[48:51], v[136:139]
s_add_u32 m0, s92, 131072
ds_read_b64_tr_b16 v[64:65], v26 offset:16384
ds_read_b64_tr_b16 v[66:67], v26 offset:17408
v_mfma_f32_16x16x32_bf16 v[140:143], v[108:111], v[48:51], v[140:143]
buffer_load_dwordx4 v20, s[12:15], s24 offen offset:0 lds
v_mul_f32 v36, s47, v36
v_mfma_f32_16x16x32_bf16 v[144:147], v[112:115], v[48:51], v[144:147]
ds_read_b64_tr_b16 v[68:69], v27 offset:16384
ds_read_b64_tr_b16 v[70:71], v27 offset:17408
v_mfma_f32_16x16x32_bf16 v[148:151], v[116:119], v[48:51], v[148:151]
v_mul_f32 v37, s47, v37
v_cvt_pk_bf16_f32 v40, v36, v37
v_mfma_f32_16x16x32_bf16 v[152:155], v[120:123], v[48:51], v[152:155]
ds_read_b64_tr_b16 v[72:73], v28 offset:16384
ds_read_b64_tr_b16 v[74:75], v28 offset:17408
v_mfma_f32_16x16x32_bf16 v[156:159], v[124:127], v[48:51], v[156:159]
buffer_load_dwordx4 v21, s[12:15], s24 offen offset:1024 lds
v_accvgpr_read_b32 v38, a202
v_mfma_f32_16x16x32_bf16 v[160:163], v[96:99], v[52:55], v[160:163]
ds_read_b128 v[48:51], v24 offset:0
v_accvgpr_read_b32 v39, a203
v_mfma_f32_16x16x32_bf16 v[164:167], v[100:103], v[52:55], v[164:167]
v_mul_f32 v38, s47, v38
v_mul_f32 v39, s47, v39
v_mfma_f32_16x16x32_bf16 v[168:171], v[104:107], v[52:55], v[168:171]
ds_read_b64_tr_b16 v[76:77], v29 offset:16384
ds_read_b64_tr_b16 v[78:79], v29 offset:17408
v_mfma_f32_16x16x32_bf16 v[172:175], v[108:111], v[52:55], v[172:175]
buffer_load_dwordx4 v22, s[12:15], s24 offen offset:2048 lds
v_cvt_pk_bf16_f32 v41, v38, v39
v_mfma_f32_16x16x32_bf16 v[176:179], v[112:115], v[52:55], v[176:179]
ds_read_b64_tr_b16 v[80:81], v30 offset:16384
ds_read_b64_tr_b16 v[82:83], v30 offset:17408
v_mfma_f32_16x16x32_bf16 v[180:183], v[116:119], v[52:55], v[180:183]
v_accvgpr_read_b32 v36, a204
v_accvgpr_read_b32 v37, a205
v_mfma_f32_16x16x32_bf16 v[184:187], v[120:123], v[52:55], v[184:187]
ds_read_b64_tr_b16 v[84:85], v31 offset:16384
ds_read_b64_tr_b16 v[86:87], v31 offset:17408
v_mfma_f32_16x16x32_bf16 v[188:191], v[124:127], v[52:55], v[188:191]
buffer_load_dwordx4 v23, s[12:15], s24 offen offset:3072 lds
v_mul_f32 v36, s47, v36
v_mfma_f32_16x16x32_bf16 v[192:195], v[96:99], v[56:59], v[192:195]
ds_read_b128 v[52:55], v24 offset:2048
v_mul_f32 v37, s47, v37
v_mfma_f32_16x16x32_bf16 v[196:199], v[100:103], v[56:59], v[196:199]
v_cvt_pk_bf16_f32 v42, v36, v37
v_accvgpr_read_b32 v38, a206
v_mfma_f32_16x16x32_bf16 v[200:203], v[104:107], v[56:59], v[200:203]
s_add_u32 m0, s92, 135168
ds_read_b64_tr_b16 v[88:89], v32 offset:16384
ds_read_b64_tr_b16 v[90:91], v32 offset:17408
v_mfma_f32_16x16x32_bf16 v[204:207], v[108:111], v[56:59], v[204:207]
buffer_load_dwordx4 v20, s[12:15], s25 offen offset:0 lds
v_accvgpr_read_b32 v39, a207
v_mfma_f32_16x16x32_bf16 v[208:211], v[112:115], v[56:59], v[208:211]
ds_read_b64_tr_b16 v[92:93], v33 offset:16384
ds_read_b64_tr_b16 v[94:95], v33 offset:17408
v_mfma_f32_16x16x32_bf16 v[212:215], v[116:119], v[56:59], v[212:215]
v_mul_f32 v38, s47, v38
v_mul_f32 v39, s47, v39
v_mfma_f32_16x16x32_bf16 v[216:219], v[120:123], v[56:59], v[216:219]
s_waitcnt lgkmcnt(15)
v_cvt_pk_bf16_f32 v43, v38, v39
v_mfma_f32_16x16x32_bf16 v[220:223], v[124:127], v[56:59], v[220:223]
v_permlane32_swap_b32 v40, v42
v_mfma_f32_16x16x32_bf16 v[224:227], v[96:99], v[60:63], v[224:227]
ds_read_b128 v[56:59], v24 offset:4096
v_mfma_f32_16x16x32_bf16 v[228:231], v[100:103], v[60:63], v[228:231]
buffer_load_dwordx4 v21, s[12:15], s25 offen offset:1024 lds
v_mfma_f32_16x16x32_bf16 v[232:235], v[104:107], v[60:63], v[232:235]
v_permlane32_swap_b32 v41, v43
v_mfma_f32_16x16x32_bf16 v[236:239], v[108:111], v[60:63], v[236:239]
v_accvgpr_write_b32 a196, v40
v_accvgpr_write_b32 a197, v41
v_mfma_f32_16x16x32_bf16 v[240:243], v[112:115], v[60:63], v[240:243]
v_accvgpr_write_b32 a198, v42
v_accvgpr_write_b32 a199, v43
v_mfma_f32_16x16x32_bf16 v[244:247], v[116:119], v[60:63], v[244:247]
v_accvgpr_read_b32 v36, a88
v_accvgpr_read_b32 v37, a89
v_mfma_f32_16x16x32_bf16 v[248:251], v[120:123], v[60:63], v[248:251]
v_mul_f32 v36, s47, v36
v_mul_f32 v37, s47, v37
v_mfma_f32_16x16x32_bf16 v[252:255], v[124:127], v[60:63], v[252:255]
s_waitcnt lgkmcnt(0)
v_cvt_pk_bf16_f32 v44, v36, v37
v_accvgpr_read_b32 v38, a90
v_mfma_f32_16x16x32_bf16 v[128:131], v[64:67], v[48:51], v[128:131]
ds_read_b128 v[60:63], v24 offset:6144
v_accvgpr_read_b32 v39, a91
v_mfma_f32_16x16x32_bf16 v[132:135], v[68:71], v[48:51], v[132:135]
v_add_u32 v24, 65536, v24
v_mul_f32 v38, s47, v38
v_mfma_f32_16x16x32_bf16 v[136:139], v[72:75], v[48:51], v[136:139]
ds_read_b64_tr_b16 v[96:97], v26 offset:24576
ds_read_b64_tr_b16 v[98:99], v26 offset:25600
v_mfma_f32_16x16x32_bf16 v[140:143], v[76:79], v[48:51], v[140:143]
buffer_load_dwordx4 v22, s[12:15], s25 offen offset:2048 lds
v_mul_f32 v39, s47, v39
v_mfma_f32_16x16x32_bf16 v[144:147], v[80:83], v[48:51], v[144:147]
ds_read_b64_tr_b16 v[100:101], v27 offset:24576
ds_read_b64_tr_b16 v[102:103], v27 offset:25600
v_mfma_f32_16x16x32_bf16 v[148:151], v[84:87], v[48:51], v[148:151]
v_cvt_pk_bf16_f32 v45, v38, v39
v_accvgpr_read_b32 v36, a92
v_mfma_f32_16x16x32_bf16 v[152:155], v[88:91], v[48:51], v[152:155]
ds_read_b64_tr_b16 v[104:105], v28 offset:24576
ds_read_b64_tr_b16 v[106:107], v28 offset:25600
v_mfma_f32_16x16x32_bf16 v[156:159], v[92:95], v[48:51], v[156:159]
buffer_load_dwordx4 v23, s[12:15], s25 offen offset:3072 lds
v_accvgpr_read_b32 v37, a93
v_mfma_f32_16x16x32_bf16 v[160:163], v[64:67], v[52:55], v[160:163]
ds_read_b128 v[48:51], v25 offset:0
v_mul_f32 v36, s47, v36
v_mfma_f32_16x16x32_bf16 v[164:167], v[68:71], v[52:55], v[164:167]
s_add_u32 s24, s24, 128
s_add_u32 s25, s25, 128
v_mul_f32 v37, s47, v37
v_mfma_f32_16x16x32_bf16 v[168:171], v[72:75], v[52:55], v[168:171]
s_add_u32 m0, s91, 65536
ds_read_b64_tr_b16 v[108:109], v29 offset:24576
ds_read_b64_tr_b16 v[110:111], v29 offset:25600
v_mfma_f32_16x16x32_bf16 v[172:175], v[76:79], v[52:55], v[172:175]
buffer_load_dwordx4 v16, s[16:19], s26 offen offset:0 lds
v_cvt_pk_bf16_f32 v46, v36, v37
v_mfma_f32_16x16x32_bf16 v[176:179], v[80:83], v[52:55], v[176:179]
ds_read_b64_tr_b16 v[112:113], v30 offset:24576
ds_read_b64_tr_b16 v[114:115], v30 offset:25600
v_mfma_f32_16x16x32_bf16 v[180:183], v[84:87], v[52:55], v[180:183]
v_accvgpr_read_b32 v38, a94
v_accvgpr_read_b32 v39, a95
v_mfma_f32_16x16x32_bf16 v[184:187], v[88:91], v[52:55], v[184:187]
ds_read_b64_tr_b16 v[116:117], v31 offset:24576
ds_read_b64_tr_b16 v[118:119], v31 offset:25600
v_mfma_f32_16x16x32_bf16 v[188:191], v[92:95], v[52:55], v[188:191]
buffer_load_dwordx4 v17, s[16:19], s26 offen offset:1024 lds
v_mul_f32 v38, s47, v38
v_mfma_f32_16x16x32_bf16 v[192:195], v[64:67], v[56:59], v[192:195]
ds_read_b128 v[52:55], v25 offset:2048
v_mul_f32 v39, s47, v39
v_mfma_f32_16x16x32_bf16 v[196:199], v[68:71], v[56:59], v[196:199]
v_cvt_pk_bf16_f32 v47, v38, v39
v_mfma_f32_16x16x32_bf16 v[200:203], v[72:75], v[56:59], v[200:203]
ds_read_b64_tr_b16 v[120:121], v32 offset:24576
ds_read_b64_tr_b16 v[122:123], v32 offset:25600
v_mfma_f32_16x16x32_bf16 v[204:207], v[76:79], v[56:59], v[204:207]
buffer_load_dwordx4 v18, s[16:19], s26 offen offset:2048 lds
v_mfma_f32_16x16x32_bf16 v[208:211], v[80:83], v[56:59], v[208:211]
ds_read_b64_tr_b16 v[124:125], v33 offset:24576
ds_read_b64_tr_b16 v[126:127], v33 offset:25600
v_mfma_f32_16x16x32_bf16 v[212:215], v[84:87], v[56:59], v[212:215]
v_permlane32_swap_b32 v44, v46
v_mfma_f32_16x16x32_bf16 v[216:219], v[88:91], v[56:59], v[216:219]
s_waitcnt lgkmcnt(15)
v_permlane32_swap_b32 v45, v47
v_mfma_f32_16x16x32_bf16 v[220:223], v[92:95], v[56:59], v[220:223]
s_xor_b32 s48, s95, 134144
v_xor_b32 v26, 0x10000, v26
v_mfma_f32_16x16x32_bf16 v[224:227], v[64:67], v[60:63], v[224:227]
ds_read_b128 v[56:59], v25 offset:4096
v_xor_b32 v27, 0x10000, v27
v_mfma_f32_16x16x32_bf16 v[228:231], v[68:71], v[60:63], v[228:231]
buffer_load_dwordx4 v19, s[16:19], s26 offen offset:3072 lds
v_xor_b32 v28, 0x10000, v28
v_mfma_f32_16x16x32_bf16 v[232:235], v[72:75], v[60:63], v[232:235]
buffer_store_dwordx4 v[44:47], v7, s[20:23], s48 offen sc1
v_xor_b32 v29, 0x10000, v29
v_mfma_f32_16x16x32_bf16 v[236:239], v[76:79], v[60:63], v[236:239]
s_add_u32 s26, s26, 524288
v_xor_b32 v30, 0x10000, v30
v_accvgpr_read_b32 v36, a96
v_mfma_f32_16x16x32_bf16 v[240:243], v[80:83], v[60:63], v[240:243]
v_xor_b32 v31, 0x10000, v31
v_accvgpr_read_b32 v37, a97
v_mfma_f32_16x16x32_bf16 v[244:247], v[84:87], v[60:63], v[244:247]
v_xor_b32 v32, 0x10000, v32
v_mul_f32 v36, s47, v36
v_mfma_f32_16x16x32_bf16 v[248:251], v[88:91], v[60:63], v[248:251]
v_xor_b32 v33, 0x10000, v33
v_mul_f32 v37, s47, v37
v_mfma_f32_16x16x32_bf16 v[252:255], v[92:95], v[60:63], v[252:255]
s_waitcnt vmcnt(18) lgkmcnt(0)
s_barrier
v_cvt_pk_bf16_f32 v40, v36, v37
v_mfma_f32_16x16x32_bf16 v[128:131], v[96:99], v[48:51], v[128:131]
ds_read_b128 v[60:63], v25 offset:6144
v_accvgpr_read_b32 v38, a98
v_mfma_f32_16x16x32_bf16 v[132:135], v[100:103], v[48:51], v[132:135]
v_add_u32 v25, 65536, v25
v_accvgpr_read_b32 v39, a99
v_mfma_f32_16x16x32_bf16 v[136:139], v[104:107], v[48:51], v[136:139]
s_add_u32 m0, s92, 32768
ds_read_b64_tr_b16 v[64:65], v26 offset:0
ds_read_b64_tr_b16 v[66:67], v26 offset:1024
v_mfma_f32_16x16x32_bf16 v[140:143], v[108:111], v[48:51], v[140:143]
buffer_load_dwordx4 v20, s[12:15], s24 offen offset:0 lds
v_mul_f32 v38, s47, v38
v_mfma_f32_16x16x32_bf16 v[144:147], v[112:115], v[48:51], v[144:147]
ds_read_b64_tr_b16 v[68:69], v27 offset:0
ds_read_b64_tr_b16 v[70:71], v27 offset:1024
v_mfma_f32_16x16x32_bf16 v[148:151], v[116:119], v[48:51], v[148:151]
v_mul_f32 v39, s47, v39
v_cvt_pk_bf16_f32 v41, v38, v39
v_mfma_f32_16x16x32_bf16 v[152:155], v[120:123], v[48:51], v[152:155]
ds_read_b64_tr_b16 v[72:73], v28 offset:0
ds_read_b64_tr_b16 v[74:75], v28 offset:1024
v_mfma_f32_16x16x32_bf16 v[156:159], v[124:127], v[48:51], v[156:159]
buffer_load_dwordx4 v21, s[12:15], s24 offen offset:1024 lds
v_accvgpr_read_b32 v36, a100
v_mfma_f32_16x16x32_bf16 v[160:163], v[96:99], v[52:55], v[160:163]
ds_read_b128 v[48:51], v24 offset:0
v_accvgpr_read_b32 v37, a101
v_mfma_f32_16x16x32_bf16 v[164:167], v[100:103], v[52:55], v[164:167]
v_mul_f32 v36, s47, v36
v_mul_f32 v37, s47, v37
v_mfma_f32_16x16x32_bf16 v[168:171], v[104:107], v[52:55], v[168:171]
ds_read_b64_tr_b16 v[76:77], v29 offset:0
ds_read_b64_tr_b16 v[78:79], v29 offset:1024
v_mfma_f32_16x16x32_bf16 v[172:175], v[108:111], v[52:55], v[172:175]
buffer_load_dwordx4 v22, s[12:15], s24 offen offset:2048 lds
v_cvt_pk_bf16_f32 v42, v36, v37
v_mfma_f32_16x16x32_bf16 v[176:179], v[112:115], v[52:55], v[176:179]
ds_read_b64_tr_b16 v[80:81], v30 offset:0
ds_read_b64_tr_b16 v[82:83], v30 offset:1024
v_mfma_f32_16x16x32_bf16 v[180:183], v[116:119], v[52:55], v[180:183]
v_accvgpr_read_b32 v38, a102
v_accvgpr_read_b32 v39, a103
v_mfma_f32_16x16x32_bf16 v[184:187], v[120:123], v[52:55], v[184:187]
ds_read_b64_tr_b16 v[84:85], v31 offset:0
ds_read_b64_tr_b16 v[86:87], v31 offset:1024
v_mfma_f32_16x16x32_bf16 v[188:191], v[124:127], v[52:55], v[188:191]
buffer_load_dwordx4 v23, s[12:15], s24 offen offset:3072 lds
v_mul_f32 v38, s47, v38
v_mfma_f32_16x16x32_bf16 v[192:195], v[96:99], v[56:59], v[192:195]
ds_read_b128 v[52:55], v24 offset:2048
v_mul_f32 v39, s47, v39
v_mfma_f32_16x16x32_bf16 v[196:199], v[100:103], v[56:59], v[196:199]
v_cvt_pk_bf16_f32 v43, v38, v39
v_mfma_f32_16x16x32_bf16 v[200:203], v[104:107], v[56:59], v[200:203]
s_add_u32 m0, s92, 36864
ds_read_b64_tr_b16 v[88:89], v32 offset:0
ds_read_b64_tr_b16 v[90:91], v32 offset:1024
v_mfma_f32_16x16x32_bf16 v[204:207], v[108:111], v[56:59], v[204:207]
buffer_load_dwordx4 v20, s[12:15], s25 offen offset:0 lds
v_accvgpr_read_b32 v36, a208
v_mfma_f32_16x16x32_bf16 v[208:211], v[112:115], v[56:59], v[208:211]
ds_read_b64_tr_b16 v[92:93], v33 offset:0
ds_read_b64_tr_b16 v[94:95], v33 offset:1024
v_mfma_f32_16x16x32_bf16 v[212:215], v[116:119], v[56:59], v[212:215]
v_permlane32_swap_b32 v40, v42
v_mfma_f32_16x16x32_bf16 v[216:219], v[120:123], v[56:59], v[216:219]
s_waitcnt lgkmcnt(15)
v_permlane32_swap_b32 v41, v43
v_mfma_f32_16x16x32_bf16 v[220:223], v[124:127], v[56:59], v[220:223]
s_xor_b32 s48, s95, 196608
buffer_store_dwordx4 v[40:43], v7, s[20:23], s48 offen sc1
v_mfma_f32_16x16x32_bf16 v[224:227], v[96:99], v[60:63], v[224:227]
ds_read_b128 v[56:59], v24 offset:4096
v_accvgpr_read_b32 v37, a209
v_mfma_f32_16x16x32_bf16 v[228:231], v[100:103], v[60:63], v[228:231]
buffer_load_dwordx4 v21, s[12:15], s25 offen offset:1024 lds
v_mul_f32 v36, s47, v36
v_mfma_f32_16x16x32_bf16 v[232:235], v[104:107], v[60:63], v[232:235]
v_mul_f32 v37, s47, v37
v_cvt_pk_bf16_f32 v44, v36, v37
v_mfma_f32_16x16x32_bf16 v[236:239], v[108:111], v[60:63], v[236:239]
v_accvgpr_read_b32 v38, a210
v_accvgpr_read_b32 v39, a211
v_mfma_f32_16x16x32_bf16 v[240:243], v[112:115], v[60:63], v[240:243]
v_mul_f32 v38, s47, v38
v_mul_f32 v39, s47, v39
v_mfma_f32_16x16x32_bf16 v[244:247], v[116:119], v[60:63], v[244:247]
v_cvt_pk_bf16_f32 v45, v38, v39
v_accvgpr_read_b32 v36, a212
v_mfma_f32_16x16x32_bf16 v[248:251], v[120:123], v[60:63], v[248:251]
v_accvgpr_read_b32 v37, a213
v_mul_f32 v36, s47, v36
v_mfma_f32_16x16x32_bf16 v[252:255], v[124:127], v[60:63], v[252:255]
s_waitcnt lgkmcnt(0)
v_mul_f32 v37, s47, v37
v_cvt_pk_bf16_f32 v46, v36, v37
v_mfma_f32_16x16x32_bf16 v[128:131], v[64:67], v[48:51], v[128:131]
ds_read_b128 v[60:63], v24 offset:6144
v_accvgpr_read_b32 v38, a214
v_mfma_f32_16x16x32_bf16 v[132:135], v[68:71], v[48:51], v[132:135]
v_add_u32 v24, 32768, v24
v_accvgpr_read_b32 v39, a215
v_mfma_f32_16x16x32_bf16 v[136:139], v[72:75], v[48:51], v[136:139]
ds_read_b64_tr_b16 v[96:97], v26 offset:8192
ds_read_b64_tr_b16 v[98:99], v26 offset:9216
v_mfma_f32_16x16x32_bf16 v[140:143], v[76:79], v[48:51], v[140:143]
buffer_load_dwordx4 v22, s[12:15], s25 offen offset:2048 lds
v_mul_f32 v38, s47, v38
v_mfma_f32_16x16x32_bf16 v[144:147], v[80:83], v[48:51], v[144:147]
ds_read_b64_tr_b16 v[100:101], v27 offset:8192
ds_read_b64_tr_b16 v[102:103], v27 offset:9216
v_mfma_f32_16x16x32_bf16 v[148:151], v[84:87], v[48:51], v[148:151]
v_mul_f32 v39, s47, v39
v_cvt_pk_bf16_f32 v47, v38, v39
v_mfma_f32_16x16x32_bf16 v[152:155], v[88:91], v[48:51], v[152:155]
ds_read_b64_tr_b16 v[104:105], v28 offset:8192
ds_read_b64_tr_b16 v[106:107], v28 offset:9216
v_mfma_f32_16x16x32_bf16 v[156:159], v[92:95], v[48:51], v[156:159]
buffer_load_dwordx4 v23, s[12:15], s25 offen offset:3072 lds
v_mfma_f32_16x16x32_bf16 v[160:163], v[64:67], v[52:55], v[160:163]
ds_read_b128 v[48:51], v25 offset:0
v_mfma_f32_16x16x32_bf16 v[164:167], v[68:71], v[52:55], v[164:167]
s_add_u32 s24, s24, 128
s_add_u32 s25, s25, 128
v_mfma_f32_16x16x32_bf16 v[168:171], v[72:75], v[52:55], v[168:171]
s_add_u32 m0, s91, 81920
ds_read_b64_tr_b16 v[108:109], v29 offset:8192
ds_read_b64_tr_b16 v[110:111], v29 offset:9216
v_mfma_f32_16x16x32_bf16 v[172:175], v[76:79], v[52:55], v[172:175]
buffer_load_dwordx4 v16, s[16:19], s26 offen offset:0 lds
v_mfma_f32_16x16x32_bf16 v[176:179], v[80:83], v[52:55], v[176:179]
ds_read_b64_tr_b16 v[112:113], v30 offset:8192
ds_read_b64_tr_b16 v[114:115], v30 offset:9216
v_mfma_f32_16x16x32_bf16 v[180:183], v[84:87], v[52:55], v[180:183]
v_permlane32_swap_b32 v44, v46
v_mfma_f32_16x16x32_bf16 v[184:187], v[88:91], v[52:55], v[184:187]
ds_read_b64_tr_b16 v[116:117], v31 offset:8192
ds_read_b64_tr_b16 v[118:119], v31 offset:9216
v_mfma_f32_16x16x32_bf16 v[188:191], v[92:95], v[52:55], v[188:191]
buffer_load_dwordx4 v17, s[16:19], s26 offen offset:1024 lds
v_mfma_f32_16x16x32_bf16 v[192:195], v[64:67], v[56:59], v[192:195]
ds_read_b128 v[52:55], v25 offset:2048
v_mfma_f32_16x16x32_bf16 v[196:199], v[68:71], v[56:59], v[196:199]
v_permlane32_swap_b32 v45, v47
v_mfma_f32_16x16x32_bf16 v[200:203], v[72:75], v[56:59], v[200:203]
ds_read_b64_tr_b16 v[120:121], v32 offset:8192
ds_read_b64_tr_b16 v[122:123], v32 offset:9216
v_mfma_f32_16x16x32_bf16 v[204:207], v[76:79], v[56:59], v[204:207]
buffer_load_dwordx4 v18, s[16:19], s26 offen offset:2048 lds
v_accvgpr_write_b32 a200, v44
v_mfma_f32_16x16x32_bf16 v[208:211], v[80:83], v[56:59], v[208:211]
ds_read_b64_tr_b16 v[124:125], v33 offset:8192
ds_read_b64_tr_b16 v[126:127], v33 offset:9216
v_mfma_f32_16x16x32_bf16 v[212:215], v[84:87], v[56:59], v[212:215]
v_accvgpr_write_b32 a201, v45
v_accvgpr_write_b32 a202, v46
v_mfma_f32_16x16x32_bf16 v[216:219], v[88:91], v[56:59], v[216:219]
s_waitcnt lgkmcnt(15)
v_accvgpr_write_b32 a203, v47
v_accvgpr_read_b32 v36, a104
v_mfma_f32_16x16x32_bf16 v[220:223], v[92:95], v[56:59], v[220:223]
v_accvgpr_read_b32 v37, a105
v_mul_f32 v36, s47, v36
v_mfma_f32_16x16x32_bf16 v[224:227], v[64:67], v[60:63], v[224:227]
ds_read_b128 v[56:59], v25 offset:4096
v_mul_f32 v37, s47, v37
v_mfma_f32_16x16x32_bf16 v[228:231], v[68:71], v[60:63], v[228:231]
buffer_load_dwordx4 v19, s[16:19], s26 offen offset:3072 lds
v_cvt_pk_bf16_f32 v40, v36, v37
v_mfma_f32_16x16x32_bf16 v[232:235], v[72:75], v[60:63], v[232:235]
v_accvgpr_read_b32 v38, a106
v_accvgpr_read_b32 v39, a107
v_mfma_f32_16x16x32_bf16 v[236:239], v[76:79], v[60:63], v[236:239]
s_add_u32 s26, s26, 524288
v_mul_f32 v38, s47, v38
v_mul_f32 v39, s47, v39
v_mfma_f32_16x16x32_bf16 v[240:243], v[80:83], v[60:63], v[240:243]
v_cvt_pk_bf16_f32 v41, v38, v39
v_accvgpr_read_b32 v36, a108
v_mfma_f32_16x16x32_bf16 v[244:247], v[84:87], v[60:63], v[244:247]
v_accvgpr_read_b32 v37, a109
v_mul_f32 v36, s47, v36
v_mfma_f32_16x16x32_bf16 v[248:251], v[88:91], v[60:63], v[248:251]
v_mul_f32 v37, s47, v37
v_cvt_pk_bf16_f32 v42, v36, v37
v_mfma_f32_16x16x32_bf16 v[252:255], v[92:95], v[60:63], v[252:255]
s_waitcnt vmcnt(18) lgkmcnt(0)
s_barrier
v_accvgpr_read_b32 v38, a110
v_mfma_f32_16x16x32_bf16 v[128:131], v[96:99], v[48:51], v[128:131]
ds_read_b128 v[60:63], v25 offset:6144
v_accvgpr_read_b32 v39, a111
v_mfma_f32_16x16x32_bf16 v[132:135], v[100:103], v[48:51], v[132:135]
v_add_u32 v25, 32768, v25
v_mul_f32 v38, s47, v38
v_mfma_f32_16x16x32_bf16 v[136:139], v[104:107], v[48:51], v[136:139]
s_add_u32 m0, s92, 98304
ds_read_b64_tr_b16 v[64:65], v26 offset:16384
ds_read_b64_tr_b16 v[66:67], v26 offset:17408
v_mfma_f32_16x16x32_bf16 v[140:143], v[108:111], v[48:51], v[140:143]
buffer_load_dwordx4 v20, s[12:15], s24 offen offset:0 lds
v_mul_f32 v39, s47, v39
v_mfma_f32_16x16x32_bf16 v[144:147], v[112:115], v[48:51], v[144:147]
ds_read_b64_tr_b16 v[68:69], v27 offset:16384
ds_read_b64_tr_b16 v[70:71], v27 offset:17408
v_mfma_f32_16x16x32_bf16 v[148:151], v[116:119], v[48:51], v[148:151]
v_cvt_pk_bf16_f32 v43, v38, v39
v_mfma_f32_16x16x32_bf16 v[152:155], v[120:123], v[48:51], v[152:155]
ds_read_b64_tr_b16 v[72:73], v28 offset:16384
ds_read_b64_tr_b16 v[74:75], v28 offset:17408
v_mfma_f32_16x16x32_bf16 v[156:159], v[124:127], v[48:51], v[156:159]
buffer_load_dwordx4 v21, s[12:15], s24 offen offset:1024 lds
v_accvgpr_read_b32 v36, a216
v_mfma_f32_16x16x32_bf16 v[160:163], v[96:99], v[52:55], v[160:163]
ds_read_b128 v[48:51], v24 offset:0
v_accvgpr_read_b32 v37, a217
v_mfma_f32_16x16x32_bf16 v[164:167], v[100:103], v[52:55], v[164:167]
v_permlane32_swap_b32 v40, v42
v_mfma_f32_16x16x32_bf16 v[168:171], v[104:107], v[52:55], v[168:171]
ds_read_b64_tr_b16 v[76:77], v29 offset:16384
ds_read_b64_tr_b16 v[78:79], v29 offset:17408
v_mfma_f32_16x16x32_bf16 v[172:175], v[108:111], v[52:55], v[172:175]
buffer_load_dwordx4 v22, s[12:15], s24 offen offset:2048 lds
v_mul_f32 v36, s47, v36
v_mfma_f32_16x16x32_bf16 v[176:179], v[112:115], v[52:55], v[176:179]
ds_read_b64_tr_b16 v[80:81], v30 offset:16384
ds_read_b64_tr_b16 v[82:83], v30 offset:17408
v_mfma_f32_16x16x32_bf16 v[180:183], v[116:119], v[52:55], v[180:183]
v_permlane32_swap_b32 v41, v43
v_mfma_f32_16x16x32_bf16 v[184:187], v[120:123], v[52:55], v[184:187]
ds_read_b64_tr_b16 v[84:85], v31 offset:16384
ds_read_b64_tr_b16 v[86:87], v31 offset:17408
v_mfma_f32_16x16x32_bf16 v[188:191], v[124:127], v[52:55], v[188:191]
s_xor_b32 s48, s95, 197632
buffer_load_dwordx4 v23, s[12:15], s24 offen offset:3072 lds
v_mfma_f32_16x16x32_bf16 v[192:195], v[96:99], v[56:59], v[192:195]
ds_read_b128 v[52:55], v24 offset:2048
v_mul_f32 v37, s47, v37
v_mfma_f32_16x16x32_bf16 v[196:199], v[100:103], v[56:59], v[196:199]
buffer_store_dwordx4 v[40:43], v7, s[20:23], s48 offen sc1
v_cvt_pk_bf16_f32 v44, v36, v37
v_mfma_f32_16x16x32_bf16 v[200:203], v[104:107], v[56:59], v[200:203]
s_add_u32 m0, s92, 102400
ds_read_b64_tr_b16 v[88:89], v32 offset:16384
ds_read_b64_tr_b16 v[90:91], v32 offset:17408
v_mfma_f32_16x16x32_bf16 v[204:207], v[108:111], v[56:59], v[204:207]
buffer_load_dwordx4 v20, s[12:15], s25 offen offset:0 lds
v_accvgpr_read_b32 v38, a218
v_mfma_f32_16x16x32_bf16 v[208:211], v[112:115], v[56:59], v[208:211]
ds_read_b64_tr_b16 v[92:93], v33 offset:16384
ds_read_b64_tr_b16 v[94:95], v33 offset:17408
v_mfma_f32_16x16x32_bf16 v[212:215], v[116:119], v[56:59], v[212:215]
v_accvgpr_read_b32 v39, a219
v_mul_f32 v38, s47, v38
v_mfma_f32_16x16x32_bf16 v[216:219], v[120:123], v[56:59], v[216:219]
s_waitcnt lgkmcnt(15)
v_mul_f32 v39, s47, v39
v_cvt_pk_bf16_f32 v45, v38, v39
v_mfma_f32_16x16x32_bf16 v[220:223], v[124:127], v[56:59], v[220:223]
v_accvgpr_read_b32 v36, a220
v_accvgpr_read_b32 v37, a221
v_mfma_f32_16x16x32_bf16 v[224:227], v[96:99], v[60:63], v[224:227]
ds_read_b128 v[56:59], v24 offset:4096
v_mul_f32 v36, s47, v36
v_mfma_f32_16x16x32_bf16 v[228:231], v[100:103], v[60:63], v[228:231]
buffer_load_dwordx4 v21, s[12:15], s25 offen offset:1024 lds
v_mul_f32 v37, s47, v37
v_mfma_f32_16x16x32_bf16 v[232:235], v[104:107], v[60:63], v[232:235]
v_cvt_pk_bf16_f32 v46, v36, v37
v_accvgpr_read_b32 v38, a222
v_mfma_f32_16x16x32_bf16 v[236:239], v[108:111], v[60:63], v[236:239]
v_accvgpr_read_b32 v39, a223
v_mul_f32 v38, s47, v38
v_mfma_f32_16x16x32_bf16 v[240:243], v[112:115], v[60:63], v[240:243]
v_mul_f32 v39, s47, v39
v_cvt_pk_bf16_f32 v47, v38, v39
v_mfma_f32_16x16x32_bf16 v[244:247], v[116:119], v[60:63], v[244:247]
v_permlane32_swap_b32 v44, v46
v_mfma_f32_16x16x32_bf16 v[248:251], v[120:123], v[60:63], v[248:251]
v_permlane32_swap_b32 v45, v47
v_mfma_f32_16x16x32_bf16 v[252:255], v[124:127], v[60:63], v[252:255]
s_waitcnt lgkmcnt(0)
v_accvgpr_write_b32 a204, v44
v_accvgpr_write_b32 a205, v45
v_mfma_f32_16x16x32_bf16 v[128:131], v[64:67], v[48:51], v[128:131]
ds_read_b128 v[60:63], v24 offset:6144
v_accvgpr_write_b32 a206, v46
v_mfma_f32_16x16x32_bf16 v[132:135], v[68:71], v[48:51], v[132:135]
v_add_u32 v24, 4294868992, v24
v_accvgpr_write_b32 a207, v47
v_mfma_f32_16x16x32_bf16 v[136:139], v[72:75], v[48:51], v[136:139]
ds_read_b64_tr_b16 v[96:97], v26 offset:24576
ds_read_b64_tr_b16 v[98:99], v26 offset:25600
v_mfma_f32_16x16x32_bf16 v[140:143], v[76:79], v[48:51], v[140:143]
buffer_load_dwordx4 v22, s[12:15], s25 offen offset:2048 lds
v_accvgpr_read_b32 v36, a112
v_mfma_f32_16x16x32_bf16 v[144:147], v[80:83], v[48:51], v[144:147]
ds_read_b64_tr_b16 v[100:101], v27 offset:24576
ds_read_b64_tr_b16 v[102:103], v27 offset:25600
v_mfma_f32_16x16x32_bf16 v[148:151], v[84:87], v[48:51], v[148:151]
v_accvgpr_read_b32 v37, a113
v_mul_f32 v36, s47, v36
v_mfma_f32_16x16x32_bf16 v[152:155], v[88:91], v[48:51], v[152:155]
ds_read_b64_tr_b16 v[104:105], v28 offset:24576
ds_read_b64_tr_b16 v[106:107], v28 offset:25600
v_mfma_f32_16x16x32_bf16 v[156:159], v[92:95], v[48:51], v[156:159]
buffer_load_dwordx4 v23, s[12:15], s25 offen offset:3072 lds
v_mul_f32 v37, s47, v37
v_mfma_f32_16x16x32_bf16 v[160:163], v[64:67], v[52:55], v[160:163]
ds_read_b128 v[48:51], v25 offset:0
v_cvt_pk_bf16_f32 v40, v36, v37
v_mfma_f32_16x16x32_bf16 v[164:167], v[68:71], v[52:55], v[164:167]
s_add_u32 s24, s24, 128
s_add_u32 s25, s25, 128
v_accvgpr_read_b32 v38, a114
v_mfma_f32_16x16x32_bf16 v[168:171], v[72:75], v[52:55], v[168:171]
s_add_u32 m0, s91, 0
ds_read_b64_tr_b16 v[108:109], v29 offset:24576
ds_read_b64_tr_b16 v[110:111], v29 offset:25600
v_mfma_f32_16x16x32_bf16 v[172:175], v[76:79], v[52:55], v[172:175]
buffer_load_dwordx4 v16, s[16:19], s26 offen offset:0 lds
v_accvgpr_read_b32 v39, a115
v_mfma_f32_16x16x32_bf16 v[176:179], v[80:83], v[52:55], v[176:179]
ds_read_b64_tr_b16 v[112:113], v30 offset:24576
ds_read_b64_tr_b16 v[114:115], v30 offset:25600
v_mfma_f32_16x16x32_bf16 v[180:183], v[84:87], v[52:55], v[180:183]
v_mul_f32 v38, s47, v38
v_mul_f32 v39, s47, v39
v_mfma_f32_16x16x32_bf16 v[184:187], v[88:91], v[52:55], v[184:187]
ds_read_b64_tr_b16 v[116:117], v31 offset:24576
ds_read_b64_tr_b16 v[118:119], v31 offset:25600
v_mfma_f32_16x16x32_bf16 v[188:191], v[92:95], v[52:55], v[188:191]
buffer_load_dwordx4 v17, s[16:19], s26 offen offset:1024 lds
v_cvt_pk_bf16_f32 v41, v38, v39
v_mfma_f32_16x16x32_bf16 v[192:195], v[64:67], v[56:59], v[192:195]
ds_read_b128 v[52:55], v25 offset:2048
v_accvgpr_read_b32 v36, a116
v_mfma_f32_16x16x32_bf16 v[196:199], v[68:71], v[56:59], v[196:199]
v_accvgpr_read_b32 v37, a117
v_mul_f32 v36, s47, v36
v_mfma_f32_16x16x32_bf16 v[200:203], v[72:75], v[56:59], v[200:203]
ds_read_b64_tr_b16 v[120:121], v32 offset:24576
ds_read_b64_tr_b16 v[122:123], v32 offset:25600
v_mfma_f32_16x16x32_bf16 v[204:207], v[76:79], v[56:59], v[204:207]
buffer_load_dwordx4 v18, s[16:19], s26 offen offset:2048 lds
v_mul_f32 v37, s47, v37
v_mfma_f32_16x16x32_bf16 v[208:211], v[80:83], v[56:59], v[208:211]
ds_read_b64_tr_b16 v[124:125], v33 offset:24576
ds_read_b64_tr_b16 v[126:127], v33 offset:25600
v_mfma_f32_16x16x32_bf16 v[212:215], v[84:87], v[56:59], v[212:215]
v_cvt_pk_bf16_f32 v42, v36, v37
v_accvgpr_read_b32 v38, a118
v_mfma_f32_16x16x32_bf16 v[216:219], v[88:91], v[56:59], v[216:219]
s_waitcnt lgkmcnt(15)
v_accvgpr_read_b32 v39, a119
v_mul_f32 v38, s47, v38
v_mfma_f32_16x16x32_bf16 v[220:223], v[92:95], v[56:59], v[220:223]
v_xor_b32 v26, 0x10000, v26
v_mul_f32 v39, s47, v39
v_mfma_f32_16x16x32_bf16 v[224:227], v[64:67], v[60:63], v[224:227]
ds_read_b128 v[56:59], v25 offset:4096
v_xor_b32 v27, 0x10000, v27
v_mfma_f32_16x16x32_bf16 v[228:231], v[68:71], v[60:63], v[228:231]
buffer_load_dwordx4 v19, s[16:19], s26 offen offset:3072 lds
v_xor_b32 v28, 0x10000, v28
v_mfma_f32_16x16x32_bf16 v[232:235], v[72:75], v[60:63], v[232:235]
v_xor_b32 v29, 0x10000, v29
v_cvt_pk_bf16_f32 v43, v38, v39
v_mfma_f32_16x16x32_bf16 v[236:239], v[76:79], v[60:63], v[236:239]
s_add_u32 s26, s26, 524288
v_xor_b32 v30, 0x10000, v30
v_accvgpr_read_b32 v36, a224
v_mfma_f32_16x16x32_bf16 v[240:243], v[80:83], v[60:63], v[240:243]
v_xor_b32 v31, 0x10000, v31
v_accvgpr_read_b32 v37, a225
v_mfma_f32_16x16x32_bf16 v[244:247], v[84:87], v[60:63], v[244:247]
v_xor_b32 v32, 0x10000, v32
v_mul_f32 v36, s47, v36
v_mfma_f32_16x16x32_bf16 v[248:251], v[88:91], v[60:63], v[248:251]
v_xor_b32 v33, 0x10000, v33
v_mul_f32 v37, s47, v37
v_mfma_f32_16x16x32_bf16 v[252:255], v[92:95], v[60:63], v[252:255]
s_waitcnt vmcnt(26)
s_waitcnt vmcnt(17) lgkmcnt(0)
s_barrier
v_cvt_pk_bf16_f32 v44, v36, v37
v_mfma_f32_16x16x32_bf16 v[128:131], v[96:99], v[48:51], v[128:131]
ds_read_b128 v[60:63], v25 offset:6144
v_accvgpr_read_b32 v38, a226
v_mfma_f32_16x16x32_bf16 v[132:135], v[100:103], v[48:51], v[132:135]
s_nop 0
v_add_u32 v25, 4294868992, v25
v_accvgpr_read_b32 v39, a227
v_or_b32 v0, 1, v11
v_mfma_f32_16x16x32_bf16 v[136:139], v[104:107], v[48:51], v[136:139]
s_add_u32 m0, s92, 131072
ds_read_b64_tr_b16 v[64:65], v26 offset:0
ds_read_b64_tr_b16 v[66:67], v26 offset:1024
v_mfma_f32_16x16x32_bf16 v[140:143], v[108:111], v[48:51], v[140:143]
buffer_load_dwordx4 v20, s[12:15], s24 offen offset:0 lds
v_mul_f32 v38, s47, v38
v_mfma_f32_16x16x32_bf16 v[144:147], v[112:115], v[48:51], v[144:147]
ds_read_b64_tr_b16 v[68:69], v27 offset:0
ds_read_b64_tr_b16 v[70:71], v27 offset:1024
v_mfma_f32_16x16x32_bf16 v[148:151], v[116:119], v[48:51], v[148:151]
buffer_store_dword v0, v6, s[60:63], 0 offen sc1
v_permlane32_swap_b32 v40, v42
v_mfma_f32_16x16x32_bf16 v[152:155], v[120:123], v[48:51], v[152:155]
ds_read_b64_tr_b16 v[72:73], v28 offset:0
ds_read_b64_tr_b16 v[74:75], v28 offset:1024
v_mfma_f32_16x16x32_bf16 v[156:159], v[124:127], v[48:51], v[156:159]
buffer_load_dwordx4 v21, s[12:15], s24 offen offset:1024 lds
v_mul_f32 v39, s47, v39
v_mfma_f32_16x16x32_bf16 v[160:163], v[96:99], v[52:55], v[160:163]
ds_read_b128 v[48:51], v24 offset:0
v_cvt_pk_bf16_f32 v45, v38, v39
v_mfma_f32_16x16x32_bf16 v[164:167], v[100:103], v[52:55], v[164:167]
v_permlane32_swap_b32 v41, v43
v_mfma_f32_16x16x32_bf16 v[168:171], v[104:107], v[52:55], v[168:171]
ds_read_b64_tr_b16 v[76:77], v29 offset:0
ds_read_b64_tr_b16 v[78:79], v29 offset:1024
v_mfma_f32_16x16x32_bf16 v[172:175], v[108:111], v[52:55], v[172:175]
s_xor_b32 s48, s95, 198656
buffer_load_dwordx4 v22, s[12:15], s24 offen offset:2048 lds
v_mfma_f32_16x16x32_bf16 v[176:179], v[112:115], v[52:55], v[176:179]
ds_read_b64_tr_b16 v[80:81], v30 offset:0
ds_read_b64_tr_b16 v[82:83], v30 offset:1024
v_mfma_f32_16x16x32_bf16 v[180:183], v[116:119], v[52:55], v[180:183]
buffer_store_dwordx4 v[40:43], v7, s[20:23], s48 offen sc1
v_accvgpr_read_b32 v36, a228
v_mfma_f32_16x16x32_bf16 v[184:187], v[120:123], v[52:55], v[184:187]
ds_read_b64_tr_b16 v[84:85], v31 offset:0
ds_read_b64_tr_b16 v[86:87], v31 offset:1024
v_mfma_f32_16x16x32_bf16 v[188:191], v[124:127], v[52:55], v[188:191]
buffer_load_dwordx4 v23, s[12:15], s24 offen offset:3072 lds
v_accvgpr_read_b32 v37, a229
v_mfma_f32_16x16x32_bf16 v[192:195], v[96:99], v[56:59], v[192:195]
ds_read_b128 v[52:55], v24 offset:2048
v_mul_f32 v36, s47, v36
v_mfma_f32_16x16x32_bf16 v[196:199], v[100:103], v[56:59], v[196:199]
v_mul_f32 v37, s47, v37
v_cvt_pk_bf16_f32 v46, v36, v37
v_mfma_f32_16x16x32_bf16 v[200:203], v[104:107], v[56:59], v[200:203]
s_add_u32 m0, s92, 135168
ds_read_b64_tr_b16 v[88:89], v32 offset:0
ds_read_b64_tr_b16 v[90:91], v32 offset:1024
v_mfma_f32_16x16x32_bf16 v[204:207], v[108:111], v[56:59], v[204:207]
buffer_load_dwordx4 v20, s[12:15], s25 offen offset:0 lds
v_accvgpr_read_b32 v38, a230
v_mfma_f32_16x16x32_bf16 v[208:211], v[112:115], v[56:59], v[208:211]
ds_read_b64_tr_b16 v[92:93], v33 offset:0
ds_read_b64_tr_b16 v[94:95], v33 offset:1024
v_mfma_f32_16x16x32_bf16 v[212:215], v[116:119], v[56:59], v[212:215]
v_accvgpr_read_b32 v39, a231
v_mul_f32 v38, s47, v38
v_mfma_f32_16x16x32_bf16 v[216:219], v[120:123], v[56:59], v[216:219]
s_waitcnt lgkmcnt(15)
v_mul_f32 v39, s47, v39
v_cvt_pk_bf16_f32 v47, v38, v39
v_mfma_f32_16x16x32_bf16 v[220:223], v[124:127], v[56:59], v[220:223]
v_permlane32_swap_b32 v44, v46
v_mfma_f32_16x16x32_bf16 v[224:227], v[96:99], v[60:63], v[224:227]
ds_read_b128 v[56:59], v24 offset:4096
v_mfma_f32_16x16x32_bf16 v[228:231], v[100:103], v[60:63], v[228:231]
buffer_load_dwordx4 v21, s[12:15], s25 offen offset:1024 lds
v_mfma_f32_16x16x32_bf16 v[232:235], v[104:107], v[60:63], v[232:235]
v_permlane32_swap_b32 v45, v47
v_mfma_f32_16x16x32_bf16 v[236:239], v[108:111], v[60:63], v[236:239]
v_accvgpr_write_b32 a224, v44
v_accvgpr_write_b32 a225, v45
v_mfma_f32_16x16x32_bf16 v[240:243], v[112:115], v[60:63], v[240:243]
v_accvgpr_write_b32 a226, v46
v_accvgpr_write_b32 a227, v47
v_mfma_f32_16x16x32_bf16 v[244:247], v[116:119], v[60:63], v[244:247]
v_accvgpr_read_b32 v36, a120
v_accvgpr_read_b32 v37, a121
v_mfma_f32_16x16x32_bf16 v[248:251], v[120:123], v[60:63], v[248:251]
v_mul_f32 v36, s47, v36
v_mul_f32 v37, s47, v37
v_mfma_f32_16x16x32_bf16 v[252:255], v[124:127], v[60:63], v[252:255]
s_waitcnt lgkmcnt(0)
v_cvt_pk_bf16_f32 v40, v36, v37
v_accvgpr_read_b32 v38, a122
v_mfma_f32_16x16x32_bf16 v[128:131], v[64:67], v[48:51], v[128:131]
ds_read_b128 v[60:63], v24 offset:6144
v_accvgpr_read_b32 v39, a123
v_mfma_f32_16x16x32_bf16 v[132:135], v[68:71], v[48:51], v[132:135]
v_add_u32 v24, 65536, v24
v_mul_f32 v38, s47, v38
v_mfma_f32_16x16x32_bf16 v[136:139], v[72:75], v[48:51], v[136:139]
ds_read_b64_tr_b16 v[96:97], v26 offset:8192
ds_read_b64_tr_b16 v[98:99], v26 offset:9216
v_mfma_f32_16x16x32_bf16 v[140:143], v[76:79], v[48:51], v[140:143]
buffer_load_dwordx4 v22, s[12:15], s25 offen offset:2048 lds
v_mul_f32 v39, s47, v39
v_mfma_f32_16x16x32_bf16 v[144:147], v[80:83], v[48:51], v[144:147]
ds_read_b64_tr_b16 v[100:101], v27 offset:8192
ds_read_b64_tr_b16 v[102:103], v27 offset:9216
v_mfma_f32_16x16x32_bf16 v[148:151], v[84:87], v[48:51], v[148:151]
v_cvt_pk_bf16_f32 v41, v38, v39
v_accvgpr_read_b32 v36, a124
v_mfma_f32_16x16x32_bf16 v[152:155], v[88:91], v[48:51], v[152:155]
ds_read_b64_tr_b16 v[104:105], v28 offset:8192
ds_read_b64_tr_b16 v[106:107], v28 offset:9216
v_mfma_f32_16x16x32_bf16 v[156:159], v[92:95], v[48:51], v[156:159]
buffer_load_dwordx4 v23, s[12:15], s25 offen offset:3072 lds
v_accvgpr_read_b32 v37, a125
v_mfma_f32_16x16x32_bf16 v[160:163], v[64:67], v[52:55], v[160:163]
ds_read_b128 v[48:51], v25 offset:0
v_mul_f32 v36, s47, v36
v_mfma_f32_16x16x32_bf16 v[164:167], v[68:71], v[52:55], v[164:167]
s_add_u32 s24, s24, 128
s_add_u32 s25, s25, 128
v_mul_f32 v37, s47, v37
v_mfma_f32_16x16x32_bf16 v[168:171], v[72:75], v[52:55], v[168:171]
s_add_u32 m0, s91, 16384
ds_read_b64_tr_b16 v[108:109], v29 offset:8192
ds_read_b64_tr_b16 v[110:111], v29 offset:9216
v_mfma_f32_16x16x32_bf16 v[172:175], v[76:79], v[52:55], v[172:175]
buffer_load_dwordx4 v16, s[16:19], s26 offen offset:0 lds
v_cvt_pk_bf16_f32 v42, v36, v37
v_mfma_f32_16x16x32_bf16 v[176:179], v[80:83], v[52:55], v[176:179]
ds_read_b64_tr_b16 v[112:113], v30 offset:8192
ds_read_b64_tr_b16 v[114:115], v30 offset:9216
v_mfma_f32_16x16x32_bf16 v[180:183], v[84:87], v[52:55], v[180:183]
v_accvgpr_read_b32 v38, a126
v_accvgpr_read_b32 v39, a127
v_mfma_f32_16x16x32_bf16 v[184:187], v[88:91], v[52:55], v[184:187]
ds_read_b64_tr_b16 v[116:117], v31 offset:8192
ds_read_b64_tr_b16 v[118:119], v31 offset:9216
v_mfma_f32_16x16x32_bf16 v[188:191], v[92:95], v[52:55], v[188:191]
buffer_load_dwordx4 v17, s[16:19], s26 offen offset:1024 lds
v_mul_f32 v38, s47, v38
v_mfma_f32_16x16x32_bf16 v[192:195], v[64:67], v[56:59], v[192:195]
ds_read_b128 v[52:55], v25 offset:2048
v_mul_f32 v39, s47, v39
v_mfma_f32_16x16x32_bf16 v[196:199], v[68:71], v[56:59], v[196:199]
v_cvt_pk_bf16_f32 v43, v38, v39
v_mfma_f32_16x16x32_bf16 v[200:203], v[72:75], v[56:59], v[200:203]
ds_read_b64_tr_b16 v[120:121], v32 offset:8192
ds_read_b64_tr_b16 v[122:123], v32 offset:9216
v_mfma_f32_16x16x32_bf16 v[204:207], v[76:79], v[56:59], v[204:207]
buffer_load_dwordx4 v18, s[16:19], s26 offen offset:2048 lds
v_accvgpr_read_b32 v36, a232
v_mfma_f32_16x16x32_bf16 v[208:211], v[80:83], v[56:59], v[208:211]
ds_read_b64_tr_b16 v[124:125], v33 offset:8192
ds_read_b64_tr_b16 v[126:127], v33 offset:9216
v_mfma_f32_16x16x32_bf16 v[212:215], v[84:87], v[56:59], v[212:215]
v_permlane32_swap_b32 v40, v42
v_mfma_f32_16x16x32_bf16 v[216:219], v[88:91], v[56:59], v[216:219]
s_waitcnt lgkmcnt(15)
v_permlane32_swap_b32 v41, v43
v_mfma_f32_16x16x32_bf16 v[220:223], v[92:95], v[56:59], v[220:223]
s_xor_b32 s48, s95, 199680
buffer_store_dwordx4 v[40:43], v7, s[20:23], s48 offen sc1
v_mfma_f32_16x16x32_bf16 v[224:227], v[64:67], v[60:63], v[224:227]
ds_read_b128 v[56:59], v25 offset:4096
v_accvgpr_read_b32 v37, a233
v_mfma_f32_16x16x32_bf16 v[228:231], v[68:71], v[60:63], v[228:231]
buffer_load_dwordx4 v19, s[16:19], s26 offen offset:3072 lds
v_mul_f32 v36, s47, v36
v_mfma_f32_16x16x32_bf16 v[232:235], v[72:75], v[60:63], v[232:235]
v_mul_f32 v37, s47, v37
v_cvt_pk_bf16_f32 v44, v36, v37
v_mfma_f32_16x16x32_bf16 v[236:239], v[76:79], v[60:63], v[236:239]
s_add_u32 s26, s26, 524288
v_accvgpr_read_b32 v38, a234
v_accvgpr_read_b32 v39, a235
v_mfma_f32_16x16x32_bf16 v[240:243], v[80:83], v[60:63], v[240:243]
v_mul_f32 v38, s47, v38
v_mul_f32 v39, s47, v39
v_mfma_f32_16x16x32_bf16 v[244:247], v[84:87], v[60:63], v[244:247]
buffer_load_dword v8, v3, s[60:63], s90 offen sc1
v_cvt_pk_bf16_f32 v45, v38, v39
v_mfma_f32_16x16x32_bf16 v[248:251], v[88:91], v[60:63], v[248:251]
v_accvgpr_read_b32 v36, a236
v_accvgpr_read_b32 v37, a237
v_mfma_f32_16x16x32_bf16 v[252:255], v[92:95], v[60:63], v[252:255]
s_waitcnt vmcnt(20) lgkmcnt(0)
s_barrier
v_mul_f32 v36, s47, v36
v_mfma_f32_16x16x32_bf16 v[128:131], v[96:99], v[48:51], v[128:131]
ds_read_b128 v[60:63], v25 offset:6144
v_mul_f32 v37, s47, v37
v_mfma_f32_16x16x32_bf16 v[132:135], v[100:103], v[48:51], v[132:135]
v_add_u32 v25, 65536, v25
v_cvt_pk_bf16_f32 v46, v36, v37
v_mfma_f32_16x16x32_bf16 v[136:139], v[104:107], v[48:51], v[136:139]
s_add_u32 m0, s92, 32768
ds_read_b64_tr_b16 v[64:65], v26 offset:16384
ds_read_b64_tr_b16 v[66:67], v26 offset:17408
v_mfma_f32_16x16x32_bf16 v[140:143], v[108:111], v[48:51], v[140:143]
buffer_load_dwordx4 v20, s[12:15], s24 offen offset:0 lds
v_accvgpr_read_b32 v38, a238
v_mfma_f32_16x16x32_bf16 v[144:147], v[112:115], v[48:51], v[144:147]
ds_read_b64_tr_b16 v[68:69], v27 offset:16384
ds_read_b64_tr_b16 v[70:71], v27 offset:17408
v_mfma_f32_16x16x32_bf16 v[148:151], v[116:119], v[48:51], v[148:151]
v_accvgpr_read_b32 v39, a239
v_mul_f32 v38, s47, v38
v_mfma_f32_16x16x32_bf16 v[152:155], v[120:123], v[48:51], v[152:155]
ds_read_b64_tr_b16 v[72:73], v28 offset:16384
ds_read_b64_tr_b16 v[74:75], v28 offset:17408
v_mfma_f32_16x16x32_bf16 v[156:159], v[124:127], v[48:51], v[156:159]
buffer_load_dwordx4 v21, s[12:15], s24 offen offset:1024 lds
v_mul_f32 v39, s47, v39
v_mfma_f32_16x16x32_bf16 v[160:163], v[96:99], v[52:55], v[160:163]
ds_read_b128 v[48:51], v24 offset:0
v_cvt_pk_bf16_f32 v47, v38, v39
v_mfma_f32_16x16x32_bf16 v[164:167], v[100:103], v[52:55], v[164:167]
v_permlane32_swap_b32 v44, v46
v_mfma_f32_16x16x32_bf16 v[168:171], v[104:107], v[52:55], v[168:171]
ds_read_b64_tr_b16 v[76:77], v29 offset:16384
ds_read_b64_tr_b16 v[78:79], v29 offset:17408
v_mfma_f32_16x16x32_bf16 v[172:175], v[108:111], v[52:55], v[172:175]
buffer_load_dwordx4 v22, s[12:15], s24 offen offset:2048 lds
v_mfma_f32_16x16x32_bf16 v[176:179], v[112:115], v[52:55], v[176:179]
ds_read_b64_tr_b16 v[80:81], v30 offset:16384
ds_read_b64_tr_b16 v[82:83], v30 offset:17408
v_mfma_f32_16x16x32_bf16 v[180:183], v[116:119], v[52:55], v[180:183]
v_permlane32_swap_b32 v45, v47
v_mfma_f32_16x16x32_bf16 v[184:187], v[120:123], v[52:55], v[184:187]
ds_read_b64_tr_b16 v[84:85], v31 offset:16384
ds_read_b64_tr_b16 v[86:87], v31 offset:17408
v_mfma_f32_16x16x32_bf16 v[188:191], v[124:127], v[52:55], v[188:191]
buffer_load_dwordx4 v23, s[12:15], s24 offen offset:3072 lds
v_accvgpr_write_b32 a228, v44
v_mfma_f32_16x16x32_bf16 v[192:195], v[96:99], v[56:59], v[192:195]
ds_read_b128 v[52:55], v24 offset:2048
v_accvgpr_write_b32 a229, v45
v_mfma_f32_16x16x32_bf16 v[196:199], v[100:103], v[56:59], v[196:199]
v_accvgpr_write_b32 a230, v46
v_accvgpr_write_b32 a231, v47
v_mfma_f32_16x16x32_bf16 v[200:203], v[104:107], v[56:59], v[200:203]
s_add_u32 m0, s92, 36864
ds_read_b64_tr_b16 v[88:89], v32 offset:16384
ds_read_b64_tr_b16 v[90:91], v32 offset:17408
v_mfma_f32_16x16x32_bf16 v[204:207], v[108:111], v[56:59], v[204:207]
buffer_load_dwordx4 v20, s[12:15], s25 offen offset:0 lds
v_accvgpr_read_b32 v36, a240
v_mfma_f32_16x16x32_bf16 v[208:211], v[112:115], v[56:59], v[208:211]
ds_read_b64_tr_b16 v[92:93], v33 offset:16384
ds_read_b64_tr_b16 v[94:95], v33 offset:17408
v_mfma_f32_16x16x32_bf16 v[212:215], v[116:119], v[56:59], v[212:215]
v_accvgpr_read_b32 v37, a241
v_mul_f32 v36, s47, v36
v_mfma_f32_16x16x32_bf16 v[216:219], v[120:123], v[56:59], v[216:219]
s_waitcnt lgkmcnt(15)
v_mul_f32 v37, s47, v37
v_cvt_pk_bf16_f32 v40, v36, v37
v_mfma_f32_16x16x32_bf16 v[220:223], v[124:127], v[56:59], v[220:223]
v_accvgpr_read_b32 v38, a242
v_accvgpr_read_b32 v39, a243
v_mfma_f32_16x16x32_bf16 v[224:227], v[96:99], v[60:63], v[224:227]
ds_read_b128 v[56:59], v24 offset:4096
v_mul_f32 v38, s47, v38
v_mfma_f32_16x16x32_bf16 v[228:231], v[100:103], v[60:63], v[228:231]
buffer_load_dwordx4 v21, s[12:15], s25 offen offset:1024 lds
v_mul_f32 v39, s47, v39
v_mfma_f32_16x16x32_bf16 v[232:235], v[104:107], v[60:63], v[232:235]
v_cvt_pk_bf16_f32 v41, v38, v39
v_accvgpr_read_b32 v36, a244
v_mfma_f32_16x16x32_bf16 v[236:239], v[108:111], v[60:63], v[236:239]
v_accvgpr_read_b32 v37, a245
v_mul_f32 v36, s47, v36
v_mfma_f32_16x16x32_bf16 v[240:243], v[112:115], v[60:63], v[240:243]
v_mul_f32 v37, s47, v37
v_cvt_pk_bf16_f32 v42, v36, v37
v_mfma_f32_16x16x32_bf16 v[244:247], v[116:119], v[60:63], v[244:247]
v_accvgpr_read_b32 v38, a246
v_accvgpr_read_b32 v39, a247
v_mfma_f32_16x16x32_bf16 v[248:251], v[120:123], v[60:63], v[248:251]
v_mul_f32 v38, s47, v38
v_mul_f32 v39, s47, v39
v_mfma_f32_16x16x32_bf16 v[252:255], v[124:127], v[60:63], v[252:255]
s_waitcnt lgkmcnt(0)
v_cvt_pk_bf16_f32 v43, v38, v39
v_mfma_f32_16x16x32_bf16 v[128:131], v[64:67], v[48:51], v[128:131]
ds_read_b128 v[60:63], v24 offset:6144
v_mfma_f32_16x16x32_bf16 v[132:135], v[68:71], v[48:51], v[132:135]
v_add_u32 v24, 32768, v24
v_mfma_f32_16x16x32_bf16 v[136:139], v[72:75], v[48:51], v[136:139]
ds_read_b64_tr_b16 v[96:97], v26 offset:24576
ds_read_b64_tr_b16 v[98:99], v26 offset:25600
v_mfma_f32_16x16x32_bf16 v[140:143], v[76:79], v[48:51], v[140:143]
buffer_load_dwordx4 v22, s[12:15], s25 offen offset:2048 lds
v_mfma_f32_16x16x32_bf16 v[144:147], v[80:83], v[48:51], v[144:147]
ds_read_b64_tr_b16 v[100:101], v27 offset:24576
ds_read_b64_tr_b16 v[102:103], v27 offset:25600
v_mfma_f32_16x16x32_bf16 v[148:151], v[84:87], v[48:51], v[148:151]
v_permlane32_swap_b32 v40, v42
v_mfma_f32_16x16x32_bf16 v[152:155], v[88:91], v[48:51], v[152:155]
ds_read_b64_tr_b16 v[104:105], v28 offset:24576
ds_read_b64_tr_b16 v[106:107], v28 offset:25600
v_mfma_f32_16x16x32_bf16 v[156:159], v[92:95], v[48:51], v[156:159]
buffer_load_dwordx4 v23, s[12:15], s25 offen offset:3072 lds
v_mfma_f32_16x16x32_bf16 v[160:163], v[64:67], v[52:55], v[160:163]
ds_read_b128 v[48:51], v25 offset:0
v_mfma_f32_16x16x32_bf16 v[164:167], v[68:71], v[52:55], v[164:167]
s_add_u32 s24, s24, 128
s_add_u32 s25, s25, 128
v_mfma_f32_16x16x32_bf16 v[168:171], v[72:75], v[52:55], v[168:171]
ds_read_b64_tr_b16 v[108:109], v29 offset:24576
ds_read_b64_tr_b16 v[110:111], v29 offset:25600
v_mfma_f32_16x16x32_bf16 v[172:175], v[76:79], v[52:55], v[172:175]
v_permlane32_swap_b32 v41, v43
v_mfma_f32_16x16x32_bf16 v[176:179], v[80:83], v[52:55], v[176:179]
ds_read_b64_tr_b16 v[112:113], v30 offset:24576
ds_read_b64_tr_b16 v[114:115], v30 offset:25600
v_mfma_f32_16x16x32_bf16 v[180:183], v[84:87], v[52:55], v[180:183]
v_accvgpr_write_b32 a232, v40
v_accvgpr_write_b32 a233, v41
v_mfma_f32_16x16x32_bf16 v[184:187], v[88:91], v[52:55], v[184:187]
ds_read_b64_tr_b16 v[116:117], v31 offset:24576
ds_read_b64_tr_b16 v[118:119], v31 offset:25600
v_mfma_f32_16x16x32_bf16 v[188:191], v[92:95], v[52:55], v[188:191]
v_accvgpr_write_b32 a234, v42
v_accvgpr_write_b32 a235, v43
v_mfma_f32_16x16x32_bf16 v[192:195], v[64:67], v[56:59], v[192:195]
ds_read_b128 v[52:55], v25 offset:2048
v_accvgpr_read_b32 v36, a248
v_mfma_f32_16x16x32_bf16 v[196:199], v[68:71], v[56:59], v[196:199]
v_accvgpr_read_b32 v37, a249
v_mul_f32 v36, s47, v36
v_mfma_f32_16x16x32_bf16 v[200:203], v[72:75], v[56:59], v[200:203]
ds_read_b64_tr_b16 v[120:121], v32 offset:24576
ds_read_b64_tr_b16 v[122:123], v32 offset:25600
v_mfma_f32_16x16x32_bf16 v[204:207], v[76:79], v[56:59], v[204:207]
v_mul_f32 v37, s47, v37
v_cvt_pk_bf16_f32 v44, v36, v37
v_mfma_f32_16x16x32_bf16 v[208:211], v[80:83], v[56:59], v[208:211]
ds_read_b64_tr_b16 v[124:125], v33 offset:24576
ds_read_b64_tr_b16 v[126:127], v33 offset:25600
v_mfma_f32_16x16x32_bf16 v[212:215], v[84:87], v[56:59], v[212:215]
s_waitcnt vmcnt(8)
v_and_b32 v0, 7, v8
v_cmp_lt_u32 s[28:29], v0, v12
v_mfma_f32_16x16x32_bf16 v[216:219], v[88:91], v[56:59], v[216:219]
s_waitcnt lgkmcnt(15)
v_cmp_eq_u32 s[30:31], v8, v2
v_accvgpr_read_b32 v38, a250
v_mfma_f32_16x16x32_bf16 v[220:223], v[92:95], v[56:59], v[220:223]
v_xor_b32 v26, 0x10000, v26
v_accvgpr_read_b32 v39, a251
v_mfma_f32_16x16x32_bf16 v[224:227], v[64:67], v[60:63], v[224:227]
ds_read_b128 v[56:59], v25 offset:4096
v_xor_b32 v27, 0x10000, v27
v_mfma_f32_16x16x32_bf16 v[228:231], v[68:71], v[60:63], v[228:231]
v_xor_b32 v28, 0x10000, v28
v_mul_f32 v38, s47, v38
v_mfma_f32_16x16x32_bf16 v[232:235], v[72:75], v[60:63], v[232:235]
ds_read_b128 v[20:23], v25 offset:6144
v_xor_b32 v29, 0x10000, v29
v_mfma_f32_16x16x32_bf16 v[236:239], v[76:79], v[60:63], v[236:239]
v_xor_b32 v30, 0x10000, v30
v_mul_f32 v39, s47, v39
v_mfma_f32_16x16x32_bf16 v[240:243], v[80:83], v[60:63], v[240:243]
v_xor_b32 v31, 0x10000, v31
v_cvt_pk_bf16_f32 v45, v38, v39
v_mfma_f32_16x16x32_bf16 v[244:247], v[84:87], v[60:63], v[244:247]
v_xor_b32 v32, 0x10000, v32
v_accvgpr_read_b32 v36, a252
v_mfma_f32_16x16x32_bf16 v[248:251], v[88:91], v[60:63], v[248:251]
s_or_b64 s[28:29], s[28:29], s[30:31]
s_cbranch_scc1 .Lspin1
.Lspin1_ret:
s_add_u32 s28, s39, 0
s_and_b32 s28, s28, 0xffff
v_xor_b32 v33, 0x10000, v33
v_mfma_f32_16x16x32_bf16 v[252:255], v[92:95], v[60:63], v[252:255]
s_waitcnt vmcnt(14) lgkmcnt(0)
s_barrier
s_add_u32 s28, s28, s91
s_mov_b32 s40, 0
v_mfma_f32_16x16x32_bf16 v[128:131], v[96:99], v[48:51], v[128:131]
s_add_u32 m0, s28, 65536
v_accvgpr_read_b32 v37, a253
v_mul_f32 v36, s47, v36
v_mfma_f32_16x16x32_bf16 v[132:135], v[100:103], v[48:51], v[132:135]
buffer_load_dwordx4 v7, s[4:7], s40 offen offset:0 sc1 lds
v_add_u32 v25, 32768, v25
v_mfma_f32_16x16x32_bf16 v[136:139], v[104:107], v[48:51], v[136:139]
ds_read_b64_tr_b16 v[64:65], v26 offset:0
ds_read_b64_tr_b16 v[66:67], v26 offset:1024
v_mfma_f32_16x16x32_bf16 v[140:143], v[108:111], v[48:51], v[140:143]
v_mul_f32 v37, s47, v37
v_cvt_pk_bf16_f32 v46, v36, v37
v_mfma_f32_16x16x32_bf16 v[144:147], v[112:115], v[48:51], v[144:147]
ds_read_b64_tr_b16 v[68:69], v27 offset:0
ds_read_b64_tr_b16 v[70:71], v27 offset:1024
v_mfma_f32_16x16x32_bf16 v[148:151], v[116:119], v[48:51], v[148:151]
buffer_load_dwordx4 v7, s[4:7], s40 offen offset:1024 sc1 lds
v_accvgpr_read_b32 v38, a254
v_mfma_f32_16x16x32_bf16 v[152:155], v[120:123], v[48:51], v[152:155]
ds_read_b64_tr_b16 v[72:73], v28 offset:0
ds_read_b64_tr_b16 v[74:75], v28 offset:1024
v_mfma_f32_16x16x32_bf16 v[156:159], v[124:127], v[48:51], v[156:159]
v_accvgpr_read_b32 v39, a255
v_mul_f32 v38, s47, v38
v_mfma_f32_16x16x32_bf16 v[160:163], v[96:99], v[52:55], v[160:163]
ds_read_b128 v[48:51], v24 offset:0
v_mul_f32 v39, s47, v39
v_mfma_f32_16x16x32_bf16 v[164:167], v[100:103], v[52:55], v[164:167]
buffer_load_dwordx4 v7, s[4:7], s40 offen offset:2048 sc1 lds
v_cvt_pk_bf16_f32 v47, v38, v39
v_mfma_f32_16x16x32_bf16 v[168:171], v[104:107], v[52:55], v[168:171]
ds_read_b64_tr_b16 v[76:77], v29 offset:0
ds_read_b64_tr_b16 v[78:79], v29 offset:1024
v_mfma_f32_16x16x32_bf16 v[172:175], v[108:111], v[52:55], v[172:175]
v_permlane32_swap_b32 v44, v46
v_mfma_f32_16x16x32_bf16 v[176:179], v[112:115], v[52:55], v[176:179]
ds_read_b64_tr_b16 v[80:81], v30 offset:0
ds_read_b64_tr_b16 v[82:83], v30 offset:1024
v_mfma_f32_16x16x32_bf16 v[180:183], v[116:119], v[52:55], v[180:183]
buffer_load_dwordx4 v7, s[4:7], s40 offen offset:3072 sc1 lds
v_mfma_f32_16x16x32_bf16 v[184:187], v[120:123], v[52:55], v[184:187]
ds_read_b64_tr_b16 v[84:85], v31 offset:0
ds_read_b64_tr_b16 v[86:87], v31 offset:1024
v_mfma_f32_16x16x32_bf16 v[188:191], v[124:127], v[52:55], v[188:191]
s_add_u32 s28, s39, 16384
s_and_b32 s28, s28, 0xffff
v_mfma_f32_16x16x32_bf16 v[192:195], v[96:99], v[56:59], v[192:195]
s_add_u32 s28, s28, s91
s_mov_b32 s41, 16384
ds_read_b128 v[52:55], v24 offset:2048
v_mfma_f32_16x16x32_bf16 v[196:199], v[100:103], v[56:59], v[196:199]
s_add_u32 m0, s28, 65536
v_permlane32_swap_b32 v45, v47
v_mfma_f32_16x16x32_bf16 v[200:203], v[104:107], v[56:59], v[200:203]
buffer_load_dwordx4 v7, s[4:7], s41 offen offset:0 sc1 lds
ds_read_b64_tr_b16 v[88:89], v32 offset:0
ds_read_b64_tr_b16 v[90:91], v32 offset:1024
v_mfma_f32_16x16x32_bf16 v[204:207], v[108:111], v[56:59], v[204:207]
v_accvgpr_write_b32 a236, v44
v_accvgpr_write_b32 a237, v45
v_mfma_f32_16x16x32_bf16 v[208:211], v[112:115], v[56:59], v[208:211]
ds_read_b64_tr_b16 v[92:93], v33 offset:0
ds_read_b64_tr_b16 v[94:95], v33 offset:1024
v_mfma_f32_16x16x32_bf16 v[212:215], v[116:119], v[56:59], v[212:215]
v_accvgpr_write_b32 a238, v46
v_accvgpr_write_b32 a239, v47
v_mfma_f32_16x16x32_bf16 v[216:219], v[120:123], v[56:59], v[216:219]
buffer_load_dwordx4 v7, s[4:7], s41 offen offset:1024 sc1 lds
v_mfma_f32_16x16x32_bf16 v[220:223], v[124:127], v[56:59], v[220:223]
v_mfma_f32_16x16x32_bf16 v[224:227], v[96:99], v[20:23], v[224:227]
ds_read_b128 v[56:59], v24 offset:4096
v_mfma_f32_16x16x32_bf16 v[228:231], v[100:103], v[20:23], v[228:231]
v_mfma_f32_16x16x32_bf16 v[232:235], v[104:107], v[20:23], v[232:235]
buffer_load_dwordx4 v7, s[4:7], s41 offen offset:2048 sc1 lds
v_mfma_f32_16x16x32_bf16 v[236:239], v[108:111], v[20:23], v[236:239]
v_mfma_f32_16x16x32_bf16 v[240:243], v[112:115], v[20:23], v[240:243]
v_mfma_f32_16x16x32_bf16 v[244:247], v[116:119], v[20:23], v[244:247]
v_mfma_f32_16x16x32_bf16 v[248:251], v[120:123], v[20:23], v[248:251]
buffer_load_dwordx4 v7, s[4:7], s41 offen offset:3072 sc1 lds
v_mfma_f32_16x16x32_bf16 v[252:255], v[124:127], v[20:23], v[252:255]
s_waitcnt lgkmcnt(0)
v_mfma_f32_16x16x32_bf16 v[128:131], v[64:67], v[48:51], v[128:131]
s_add_u32 s28, s39, 32768
s_and_b32 s28, s28, 0xffff
ds_read_b128 v[60:63], v24 offset:6144
v_mfma_f32_16x16x32_bf16 v[132:135], v[68:71], v[48:51], v[132:135]
s_add_u32 s28, s28, s91
s_mov_b32 s40, 32768
v_add_u32 v24, 4294868992, v24
v_mfma_f32_16x16x32_bf16 v[136:139], v[72:75], v[48:51], v[136:139]
s_add_u32 m0, s28, 65536
ds_read_b64_tr_b16 v[96:97], v26 offset:8192
ds_read_b64_tr_b16 v[98:99], v26 offset:9216
v_mfma_f32_16x16x32_bf16 v[140:143], v[76:79], v[48:51], v[140:143]
buffer_load_dwordx4 v7, s[4:7], s40 offen offset:0 sc1 lds
v_mfma_f32_16x16x32_bf16 v[144:147], v[80:83], v[48:51], v[144:147]
ds_read_b64_tr_b16 v[100:101], v27 offset:8192
ds_read_b64_tr_b16 v[102:103], v27 offset:9216
v_mfma_f32_16x16x32_bf16 v[148:151], v[84:87], v[48:51], v[148:151]
s_sub_u32 s32, s66, 4194304
s_subb_u32 s33, s67, 0
v_mfma_f32_16x16x32_bf16 v[152:155], v[88:91], v[48:51], v[152:155]
ds_read_b64_tr_b16 v[104:105], v28 offset:8192
ds_read_b64_tr_b16 v[106:107], v28 offset:9216
v_mfma_f32_16x16x32_bf16 v[156:159], v[92:95], v[48:51], v[156:159]
buffer_load_dwordx4 v7, s[4:7], s40 offen offset:1024 sc1 lds
v_mfma_f32_16x16x32_bf16 v[160:163], v[64:67], v[52:55], v[160:163]
ds_read_b128 v[48:51], v25 offset:0
v_mfma_f32_16x16x32_bf16 v[164:167], v[68:71], v[52:55], v[164:167]
s_sub_u32 s34, s80, 10485760
s_subb_u32 s35, s81, 0
v_mfma_f32_16x16x32_bf16 v[168:171], v[72:75], v[52:55], v[168:171]
ds_read_b64_tr_b16 v[108:109], v29 offset:8192
ds_read_b64_tr_b16 v[110:111], v29 offset:9216
v_mfma_f32_16x16x32_bf16 v[172:175], v[76:79], v[52:55], v[172:175]
buffer_load_dwordx4 v7, s[4:7], s40 offen offset:2048 sc1 lds
v_mfma_f32_16x16x32_bf16 v[176:179], v[80:83], v[52:55], v[176:179]
ds_read_b64_tr_b16 v[112:113], v30 offset:8192
ds_read_b64_tr_b16 v[114:115], v30 offset:9216
v_mfma_f32_16x16x32_bf16 v[180:183], v[84:87], v[52:55], v[180:183]
s_sub_u32 s36, s68, 18874368
s_subb_u32 s37, s69, 0
s_lshl_b32 s38, s2, 2
v_mfma_f32_16x16x32_bf16 v[184:187], v[88:91], v[52:55], v[184:187]
ds_read_b64_tr_b16 v[116:117], v31 offset:8192
ds_read_b64_tr_b16 v[118:119], v31 offset:9216
v_mfma_f32_16x16x32_bf16 v[188:191], v[92:95], v[52:55], v[188:191]
s_add_u32 s38, s38, s3
buffer_load_dwordx4 v7, s[4:7], s40 offen offset:3072 sc1 lds
v_mfma_f32_16x16x32_bf16 v[192:195], v[64:67], v[56:59], v[192:195]
s_lshl_b32 s38, s38, 7
ds_read_b128 v[52:55], v25 offset:2048
v_mfma_f32_16x16x32_bf16 v[196:199], v[68:71], v[56:59], v[196:199]
s_add_u32 s28, s39, 49152
s_and_b32 s28, s28, 0xffff
v_min_u32 v4, 19, v1
v_mfma_f32_16x16x32_bf16 v[200:203], v[72:75], v[56:59], v[200:203]
s_add_u32 s28, s28, s91
s_mov_b32 s41, 49152
ds_read_b64_tr_b16 v[120:121], v32 offset:8192
ds_read_b64_tr_b16 v[122:123], v32 offset:9216
v_mfma_f32_16x16x32_bf16 v[204:207], v[76:79], v[56:59], v[204:207]
s_add_u32 m0, s28, 65536
v_mov_b32 v2, s64
v_mov_b32 v3, s65
v_mfma_f32_16x16x32_bf16 v[208:211], v[80:83], v[56:59], v[208:211]
buffer_load_dwordx4 v7, s[4:7], s41 offen offset:0 sc1 lds
ds_read_b64_tr_b16 v[124:125], v33 offset:8192
ds_read_b64_tr_b16 v[126:127], v33 offset:9216
v_mfma_f32_16x16x32_bf16 v[212:215], v[84:87], v[56:59], v[212:215]
v_cmp_le_u32 vcc, 4, v4
v_mov_b32 v5, s32
v_mfma_f32_16x16x32_bf16 v[216:219], v[88:91], v[56:59], v[216:219]
s_waitcnt lgkmcnt(15)
v_mov_b32 v0, s33
v_cndmask_b32 v2, v2, v5, vcc
v_mfma_f32_16x16x32_bf16 v[220:223], v[92:95], v[56:59], v[220:223]
v_cndmask_b32 v3, v3, v0, vcc
v_cmp_le_u32 vcc, 10, v4
v_mfma_f32_16x16x32_bf16 v[224:227], v[64:67], v[60:63], v[224:227]
buffer_load_dwordx4 v7, s[4:7], s41 offen offset:1024 sc1 lds
ds_read_b128 v[56:59], v25 offset:4096
v_mfma_f32_16x16x32_bf16 v[228:231], v[68:71], v[60:63], v[228:231]
v_mov_b32 v5, s34
v_mov_b32 v0, s35
v_mfma_f32_16x16x32_bf16 v[232:235], v[72:75], v[60:63], v[232:235]
v_cndmask_b32 v2, v2, v5, vcc
v_cndmask_b32 v3, v3, v0, vcc
v_mfma_f32_16x16x32_bf16 v[236:239], v[76:79], v[60:63], v[236:239]
v_cmp_le_u32 vcc, 18, v4
v_mov_b32 v5, s36
v_mfma_f32_16x16x32_bf16 v[240:243], v[80:83], v[60:63], v[240:243]
buffer_load_dwordx4 v7, s[4:7], s41 offen offset:2048 sc1 lds
v_mov_b32 v0, s37
v_mfma_f32_16x16x32_bf16 v[244:247], v[84:87], v[60:63], v[244:247]
v_cndmask_b32 v2, v2, v5, vcc
v_cndmask_b32 v3, v3, v0, vcc
v_mfma_f32_16x16x32_bf16 v[248:251], v[88:91], v[60:63], v[248:251]
v_lshlrev_b32 v4, 20, v4
v_add_u32 v4, s38, v4
v_mfma_f32_16x16x32_bf16 v[252:255], v[92:95], v[60:63], v[252:255]
s_waitcnt vmcnt(15) lgkmcnt(0)
s_barrier
v_add_co_u32 v2, vcc, v2, v4
v_mfma_f32_16x16x32_bf16 v[128:131], v[96:99], v[48:51], v[128:131]
buffer_load_dwordx4 v7, s[4:7], s41 offen offset:3072 sc1 lds
ds_read_b128 v[60:63], v25 offset:6144
v_mfma_f32_16x16x32_bf16 v[132:135], v[100:103], v[48:51], v[132:135]
v_add_u32 v25, 4294868992, v25
v_nop
v_mfma_f32_16x16x32_bf16 v[136:139], v[104:107], v[48:51], v[136:139]
ds_read_b64_tr_b16 v[64:65], v26 offset:16384
ds_read_b64_tr_b16 v[66:67], v26 offset:17408
v_mfma_f32_16x16x32_bf16 v[140:143], v[108:111], v[48:51], v[140:143]
v_nop
v_addc_co_u32 v3, vcc, 0, v3, vcc
v_mfma_f32_16x16x32_bf16 v[144:147], v[112:115], v[48:51], v[144:147]
ds_read_b64_tr_b16 v[68:69], v27 offset:16384
ds_read_b64_tr_b16 v[70:71], v27 offset:17408
v_mfma_f32_16x16x32_bf16 v[148:151], v[116:119], v[48:51], v[148:151]
s_lshl_b32 s93, s2, 4
s_add_u32 s93, s93, s3
s_lshl_b32 s93, s93, 2
v_mfma_f32_16x16x32_bf16 v[152:155], v[120:123], v[48:51], v[152:155]
ds_read_b64_tr_b16 v[72:73], v28 offset:16384
ds_read_b64_tr_b16 v[74:75], v28 offset:17408
v_mfma_f32_16x16x32_bf16 v[156:159], v[124:127], v[48:51], v[156:159]
s_add_u32 s93, s93, 0x10000
v_mfma_f32_16x16x32_bf16 v[160:163], v[96:99], v[52:55], v[160:163]
s_xor_b32 s48, s95, 262144
ds_read_b128 v[48:51], v24 offset:0
v_mfma_f32_16x16x32_bf16 v[164:167], v[100:103], v[52:55], v[164:167]
buffer_store_dwordx4 a[128:131], v7, s[20:23], s48 offen sc1
v_lshlrev_b32 v4, 20, v1
v_mfma_f32_16x16x32_bf16 v[168:171], v[104:107], v[52:55], v[168:171]
ds_read_b64_tr_b16 v[76:77], v29 offset:16384
ds_read_b64_tr_b16 v[78:79], v29 offset:17408
v_mfma_f32_16x16x32_bf16 v[172:175], v[108:111], v[52:55], v[172:175]
v_add_u32 v5, s93, v4
v_mfma_f32_16x16x32_bf16 v[176:179], v[112:115], v[52:55], v[176:179]
ds_read_b64_tr_b16 v[80:81], v30 offset:16384
ds_read_b64_tr_b16 v[82:83], v30 offset:17408
v_mfma_f32_16x16x32_bf16 v[180:183], v[116:119], v[52:55], v[180:183]
v_mfma_f32_16x16x32_bf16 v[184:187], v[120:123], v[52:55], v[184:187]
ds_read_b64_tr_b16 v[84:85], v31 offset:16384
ds_read_b64_tr_b16 v[86:87], v31 offset:17408
v_mfma_f32_16x16x32_bf16 v[188:191], v[124:127], v[52:55], v[188:191]
v_mfma_f32_16x16x32_bf16 v[192:195], v[96:99], v[56:59], v[192:195]
ds_read_b128 v[52:55], v24 offset:2048
v_mfma_f32_16x16x32_bf16 v[196:199], v[100:103], v[56:59], v[196:199]
v_mfma_f32_16x16x32_bf16 v[200:203], v[104:107], v[56:59], v[200:203]
ds_read_b64_tr_b16 v[88:89], v32 offset:16384
ds_read_b64_tr_b16 v[90:91], v32 offset:17408
v_mfma_f32_16x16x32_bf16 v[204:207], v[108:111], v[56:59], v[204:207]
v_mfma_f32_16x16x32_bf16 v[208:211], v[112:115], v[56:59], v[208:211]
ds_read_b64_tr_b16 v[92:93], v33 offset:16384
ds_read_b64_tr_b16 v[94:95], v33 offset:17408
v_mfma_f32_16x16x32_bf16 v[212:215], v[116:119], v[56:59], v[212:215]
v_mfma_f32_16x16x32_bf16 v[216:219], v[120:123], v[56:59], v[216:219]
s_waitcnt lgkmcnt(15)
v_mfma_f32_16x16x32_bf16 v[220:223], v[124:127], v[56:59], v[220:223]
v_mfma_f32_16x16x32_bf16 v[224:227], v[96:99], v[60:63], v[224:227]
ds_read_b128 v[56:59], v24 offset:4096
v_mfma_f32_16x16x32_bf16 v[228:231], v[100:103], v[60:63], v[228:231]
s_xor_b32 s48, s95, 263168
v_mfma_f32_16x16x32_bf16 v[232:235], v[104:107], v[60:63], v[232:235]
buffer_store_dwordx4 a[132:135], v7, s[20:23], s48 offen sc1
v_mfma_f32_16x16x32_bf16 v[236:239], v[108:111], v[60:63], v[236:239]
v_mfma_f32_16x16x32_bf16 v[240:243], v[112:115], v[60:63], v[240:243]
v_mfma_f32_16x16x32_bf16 v[244:247], v[116:119], v[60:63], v[244:247]
v_mfma_f32_16x16x32_bf16 v[248:251], v[120:123], v[60:63], v[248:251]
v_mfma_f32_16x16x32_bf16 v[252:255], v[124:127], v[60:63], v[252:255]
s_waitcnt lgkmcnt(0)
v_mfma_f32_16x16x32_bf16 v[128:131], v[64:67], v[48:51], v[128:131]
ds_read_b128 v[60:63], v24 offset:6144
v_mfma_f32_16x16x32_bf16 v[132:135], v[68:71], v[48:51], v[132:135]
v_mfma_f32_16x16x32_bf16 v[136:139], v[72:75], v[48:51], v[136:139]
ds_read_b64_tr_b16 v[96:97], v26 offset:24576
ds_read_b64_tr_b16 v[98:99], v26 offset:25600
v_mfma_f32_16x16x32_bf16 v[140:143], v[76:79], v[48:51], v[140:143]
v_mfma_f32_16x16x32_bf16 v[144:147], v[80:83], v[48:51], v[144:147]
ds_read_b64_tr_b16 v[100:101], v27 offset:24576
ds_read_b64_tr_b16 v[102:103], v27 offset:25600
v_mfma_f32_16x16x32_bf16 v[148:151], v[84:87], v[48:51], v[148:151]
v_mfma_f32_16x16x32_bf16 v[152:155], v[88:91], v[48:51], v[152:155]
ds_read_b64_tr_b16 v[104:105], v28 offset:24576
ds_read_b64_tr_b16 v[106:107], v28 offset:25600
v_mfma_f32_16x16x32_bf16 v[156:159], v[92:95], v[48:51], v[156:159]
v_mfma_f32_16x16x32_bf16 v[160:163], v[64:67], v[52:55], v[160:163]
ds_read_b128 v[48:51], v25 offset:0
v_mfma_f32_16x16x32_bf16 v[164:167], v[68:71], v[52:55], v[164:167]
v_mfma_f32_16x16x32_bf16 v[168:171], v[72:75], v[52:55], v[168:171]
s_xor_b32 s48, s95, 264192
ds_read_b64_tr_b16 v[108:109], v29 offset:24576
ds_read_b64_tr_b16 v[110:111], v29 offset:25600
v_mfma_f32_16x16x32_bf16 v[172:175], v[76:79], v[52:55], v[172:175]
buffer_store_dwordx4 a[136:139], v7, s[20:23], s48 offen sc1
v_mfma_f32_16x16x32_bf16 v[176:179], v[80:83], v[52:55], v[176:179]
ds_read_b64_tr_b16 v[112:113], v30 offset:24576
ds_read_b64_tr_b16 v[114:115], v30 offset:25600
v_mfma_f32_16x16x32_bf16 v[180:183], v[84:87], v[52:55], v[180:183]
v_mfma_f32_16x16x32_bf16 v[184:187], v[88:91], v[52:55], v[184:187]
ds_read_b64_tr_b16 v[116:117], v31 offset:24576
ds_read_b64_tr_b16 v[118:119], v31 offset:25600
v_mfma_f32_16x16x32_bf16 v[188:191], v[92:95], v[52:55], v[188:191]
v_mfma_f32_16x16x32_bf16 v[192:195], v[64:67], v[56:59], v[192:195]
ds_read_b128 v[52:55], v25 offset:2048
v_mfma_f32_16x16x32_bf16 v[196:199], v[68:71], v[56:59], v[196:199]
v_mfma_f32_16x16x32_bf16 v[200:203], v[72:75], v[56:59], v[200:203]
ds_read_b64_tr_b16 v[120:121], v32 offset:24576
ds_read_b64_tr_b16 v[122:123], v32 offset:25600
v_mfma_f32_16x16x32_bf16 v[204:207], v[76:79], v[56:59], v[204:207]
v_mfma_f32_16x16x32_bf16 v[208:211], v[80:83], v[56:59], v[208:211]
ds_read_b64_tr_b16 v[124:125], v33 offset:24576
ds_read_b64_tr_b16 v[126:127], v33 offset:25600
v_mfma_f32_16x16x32_bf16 v[212:215], v[84:87], v[56:59], v[212:215]
v_mfma_f32_16x16x32_bf16 v[216:219], v[88:91], v[56:59], v[216:219]
s_waitcnt lgkmcnt(15)
v_and_b32 v8, 31, v1
v_lshrrev_b32 v9, 5, v1
v_mfma_f32_16x16x32_bf16 v[220:223], v[92:95], v[56:59], v[220:223]
v_lshrrev_b32 v10, 4, v8
v_and_b32 v13, 15, v1
v_mfma_f32_16x16x32_bf16 v[224:227], v[64:67], v[60:63], v[224:227]
ds_read_b128 v[56:59], v25 offset:4096
v_lshlrev_b32 v13, 4, v13
v_mfma_f32_16x16x32_bf16 v[228:231], v[68:71], v[60:63], v[228:231]
v_lshl_add_u32 v13, v9, 8, v13
v_lshl_add_u32 v32, v10, 12, v13
v_mfma_f32_16x16x32_bf16 v[232:235], v[72:75], v[60:63], v[232:235]
ds_read_b128 v[20:23], v25 offset:6144
v_add_u32 v32, 0x10000, v32
v_mfma_f32_16x16x32_bf16 v[236:239], v[76:79], v[60:63], v[236:239]
v_lshl_add_u32 v33, v10, 9, v13
v_lshlrev_b32 v9, 2, v9
v_mfma_f32_16x16x32_bf16 v[240:243], v[80:83], v[60:63], v[240:243]
v_sub_u32 v8, v8, v9
v_mov_b32 v34, 0xff800000
v_mfma_f32_16x16x32_bf16 v[244:247], v[84:87], v[60:63], v[244:247]
v_add_u32 v12, s46, v32
v_mfma_f32_16x16x32_bf16 v[248:251], v[88:91], v[60:63], v[248:251]
s_add_u32 s28, s39, 0
s_and_b32 s28, s28, 0xffff
v_mfma_f32_16x16x32_bf16 v[252:255], v[92:95], v[60:63], v[252:255]
s_waitcnt  lgkmcnt(0)
s_waitcnt vmcnt(3)
s_barrier
s_add_u32 s28, s28, s91
s_mov_b32 s40, 0
v_mfma_f32_16x16x32_bf16 v[128:131], v[96:99], v[48:51], v[128:131]
s_add_u32 m0, s28, 0
v_mfma_f32_16x16x32_bf16 v[132:135], v[100:103], v[48:51], v[132:135]
buffer_load_dwordx4 v7, s[8:11], s40 offen offset:0 sc1 lds
v_mfma_f32_16x16x32_bf16 v[136:139], v[104:107], v[48:51], v[136:139]
v_mfma_f32_16x16x32_bf16 v[140:143], v[108:111], v[48:51], v[140:143]
v_mfma_f32_16x16x32_bf16 v[144:147], v[112:115], v[48:51], v[144:147]
v_mfma_f32_16x16x32_bf16 v[148:151], v[116:119], v[48:51], v[148:151]
buffer_load_dwordx4 v7, s[8:11], s40 offen offset:1024 sc1 lds
v_mfma_f32_16x16x32_bf16 v[152:155], v[120:123], v[48:51], v[152:155]
v_mfma_f32_16x16x32_bf16 v[156:159], v[124:127], v[48:51], v[156:159]
v_mfma_f32_16x16x32_bf16 v[160:163], v[96:99], v[52:55], v[160:163]
v_mfma_f32_16x16x32_bf16 v[164:167], v[100:103], v[52:55], v[164:167]
buffer_load_dwordx4 v7, s[8:11], s40 offen offset:2048 sc1 lds
v_mfma_f32_16x16x32_bf16 v[168:171], v[104:107], v[52:55], v[168:171]
ds_read_b128 a[0:3], v12 offset:0
v_mfma_f32_16x16x32_bf16 v[172:175], v[108:111], v[52:55], v[172:175]
v_mfma_f32_16x16x32_bf16 v[176:179], v[112:115], v[52:55], v[176:179]
ds_read_b128 a[4:7], v12 offset:512
v_mfma_f32_16x16x32_bf16 v[180:183], v[116:119], v[52:55], v[180:183]
buffer_load_dwordx4 v7, s[8:11], s40 offen offset:3072 sc1 lds
v_mfma_f32_16x16x32_bf16 v[184:187], v[120:123], v[52:55], v[184:187]
ds_read_b128 a[8:11], v12 offset:1024
v_mfma_f32_16x16x32_bf16 v[188:191], v[124:127], v[52:55], v[188:191]
v_mfma_f32_16x16x32_bf16 v[192:195], v[96:99], v[56:59], v[192:195]
ds_read_b128 a[12:15], v12 offset:1536
v_mfma_f32_16x16x32_bf16 v[196:199], v[100:103], v[56:59], v[196:199]
v_mfma_f32_16x16x32_bf16 v[200:203], v[104:107], v[56:59], v[200:203]
ds_read_b128 a[16:19], v12 offset:2048
v_cvt_pk_bf16_f32 v64, v128, v129
v_mfma_f32_16x16x32_bf16 v[204:207], v[108:111], v[56:59], v[204:207]
v_cvt_pk_bf16_f32 v66, v160, v161
v_cvt_pk_bf16_f32 v65, v130, v131
v_mfma_f32_16x16x32_bf16 v[208:211], v[112:115], v[56:59], v[208:211]
ds_read_b128 a[20:23], v12 offset:2560
v_cvt_pk_bf16_f32 v67, v162, v163
v_mfma_f32_16x16x32_bf16 v[212:215], v[116:119], v[56:59], v[212:215]
v_cvt_pk_bf16_f32 v68, v132, v133
v_cvt_pk_bf16_f32 v70, v164, v165
v_mfma_f32_16x16x32_bf16 v[216:219], v[120:123], v[56:59], v[216:219]
ds_read_b128 a[24:27], v12 offset:3072
v_cvt_pk_bf16_f32 v69, v134, v135
v_mfma_f32_16x16x32_bf16 v[220:223], v[124:127], v[56:59], v[220:223]
v_cvt_pk_bf16_f32 v71, v166, v167
v_mfma_f32_16x16x32_bf16 v[224:227], v[96:99], v[20:23], v[224:227]
ds_read_b128 a[28:31], v12 offset:3584
v_mfma_f32_16x16x32_bf16 v[228:231], v[100:103], v[20:23], v[228:231]
v_permlane16_swap_b32 v64, v66
v_mfma_f32_16x16x32_bf16 v[232:235], v[104:107], v[20:23], v[232:235]
v_permlane16_swap_b32 v65, v67
v_mfma_f32_16x16x32_bf16 v[236:239], v[108:111], v[20:23], v[236:239]
v_cvt_pk_bf16_f32 v72, v136, v137
v_cvt_pk_bf16_f32 v74, v168, v169
v_mfma_f32_16x16x32_bf16 v[240:243], v[112:115], v[20:23], v[240:243]
v_cvt_pk_bf16_f32 v73, v138, v139
v_cvt_pk_bf16_f32 v75, v170, v171
v_mfma_f32_16x16x32_bf16 v[244:247], v[116:119], v[20:23], v[244:247]
v_permlane16_swap_b32 v68, v70
v_mfma_f32_16x16x32_bf16 v[248:251], v[120:123], v[20:23], v[248:251]
v_permlane16_swap_b32 v69, v71
v_mfma_f32_16x16x32_bf16 v[252:255], v[124:127], v[20:23], v[252:255]
v_cvt_pk_bf16_f32 v76, v140, v141
v_cvt_pk_bf16_f32 v78, v172, v173
v_cvt_pk_bf16_f32 v77, v142, v143
v_cvt_pk_bf16_f32 v79, v174, v175
v_permlane16_swap_b32 v72, v74
v_permlane16_swap_b32 v73, v75
v_cvt_pk_bf16_f32 v80, v144, v145
v_cvt_pk_bf16_f32 v82, v176, v177
v_cvt_pk_bf16_f32 v81, v146, v147
v_cvt_pk_bf16_f32 v83, v178, v179
v_permlane16_swap_b32 v76, v78
v_permlane16_swap_b32 v77, v79
v_cvt_pk_bf16_f32 v84, v148, v149
v_cvt_pk_bf16_f32 v86, v180, v181
v_cvt_pk_bf16_f32 v85, v150, v151
v_cvt_pk_bf16_f32 v87, v182, v183
v_permlane16_swap_b32 v80, v82
v_permlane16_swap_b32 v81, v83
v_cvt_pk_bf16_f32 v88, v152, v153
v_cvt_pk_bf16_f32 v90, v184, v185
v_cvt_pk_bf16_f32 v89, v154, v155
v_cvt_pk_bf16_f32 v91, v186, v187
v_permlane16_swap_b32 v84, v86
v_permlane16_swap_b32 v85, v87
v_cvt_pk_bf16_f32 v92, v156, v157
v_cvt_pk_bf16_f32 v94, v188, v189
v_cvt_pk_bf16_f32 v93, v158, v159
v_cvt_pk_bf16_f32 v95, v190, v191
v_permlane16_swap_b32 v88, v90
v_permlane16_swap_b32 v89, v91
v_permlane16_swap_b32 v92, v94
v_permlane16_swap_b32 v93, v95
v_mov_b32 v62, v12
v_add_u32 v63, s46, v33
s_waitcnt lgkmcnt(0)
v_mfma_f32_32x32x16_bf16 v[128:143], a[0:3], v[64:67], 0
s_add_u32 s28, s39, 16384
s_and_b32 s28, s28, 0xffff
s_add_u32 s28, s28, s91
s_mov_b32 s41, 16384
s_add_u32 m0, s28, 0
v_cvt_pk_bf16_f32 v96, v192, v193
v_cvt_pk_bf16_f32 v98, v224, v225
v_mfma_f32_32x32x16_bf16 v[128:143], a[4:7], v[68:71], v[128:143]
buffer_load_dwordx4 v7, s[8:11], s41 offen offset:0 sc1 lds
v_cvt_pk_bf16_f32 v97, v194, v195
v_cvt_pk_bf16_f32 v99, v226, v227
v_cvt_pk_bf16_f32 v100, v196, v197
v_cvt_pk_bf16_f32 v102, v228, v229
v_cvt_pk_bf16_f32 v101, v198, v199
v_mfma_f32_32x32x16_bf16 v[128:143], a[8:11], v[72:75], v[128:143]
buffer_load_dwordx4 v7, s[8:11], s41 offen offset:1024 sc1 lds
v_cvt_pk_bf16_f32 v103, v230, v231
v_permlane16_swap_b32 v96, v98
v_permlane16_swap_b32 v97, v99
v_mfma_f32_32x32x16_bf16 v[128:143], a[12:15], v[76:79], v[128:143]
buffer_load_dwordx4 v7, s[8:11], s41 offen offset:2048 sc1 lds
v_cvt_pk_bf16_f32 v104, v200, v201
v_cvt_pk_bf16_f32 v106, v232, v233
v_cvt_pk_bf16_f32 v105, v202, v203
v_cvt_pk_bf16_f32 v107, v234, v235
v_mfma_f32_32x32x16_bf16 v[128:143], a[16:19], v[80:83], v[128:143]
buffer_load_dwordx4 v7, s[8:11], s41 offen offset:3072 sc1 lds
v_permlane16_swap_b32 v100, v102
v_permlane16_swap_b32 v101, v103
v_cvt_pk_bf16_f32 v108, v204, v205
v_mfma_f32_32x32x16_bf16 v[128:143], a[20:23], v[84:87], v[128:143]
s_add_u32 s28, s39, 32768
s_and_b32 s28, s28, 0xffff
v_cvt_pk_bf16_f32 v110, v236, v237
v_cvt_pk_bf16_f32 v109, v206, v207
v_cvt_pk_bf16_f32 v111, v238, v239
v_permlane16_swap_b32 v104, v106
v_mfma_f32_32x32x16_bf16 v[128:143], a[24:27], v[88:91], v[128:143]
s_add_u32 s28, s28, s91
s_mov_b32 s40, 32768
v_permlane16_swap_b32 v105, v107
v_cvt_pk_bf16_f32 v112, v208, v209
v_cvt_pk_bf16_f32 v114, v240, v241
v_cvt_pk_bf16_f32 v113, v210, v211
v_mfma_f32_32x32x16_bf16 v[128:143], a[28:31], v[92:95], v[128:143]
s_add_u32 m0, s28, 0
v_cvt_pk_bf16_f32 v115, v242, v243
v_permlane16_swap_b32 v108, v110
v_permlane16_swap_b32 v109, v111
v_cvt_pk_bf16_f32 v116, v212, v213
v_cvt_pk_bf16_f32 v118, v244, v245
v_cvt_pk_bf16_f32 v117, v214, v215
v_cvt_pk_bf16_f32 v119, v246, v247
v_permlane16_swap_b32 v112, v114
v_permlane16_swap_b32 v113, v115
v_cvt_pk_bf16_f32 v120, v216, v217
v_cvt_pk_bf16_f32 v122, v248, v249
v_cvt_pk_bf16_f32 v121, v218, v219
v_cvt_pk_bf16_f32 v123, v250, v251
v_permlane16_swap_b32 v116, v118
v_permlane16_swap_b32 v117, v119
v_cvt_pk_bf16_f32 v124, v220, v221
v_cvt_pk_bf16_f32 v126, v252, v253
v_cvt_pk_bf16_f32 v125, v222, v223
v_cvt_pk_bf16_f32 v127, v254, v255
v_permlane16_swap_b32 v120, v122
v_permlane16_swap_b32 v121, v123
v_permlane16_swap_b32 v124, v126
v_permlane16_swap_b32 v125, v127
v_mfma_f32_32x32x16_bf16 v[160:175], a[0:3], v[96:99], 0
buffer_load_dwordx4 v7, s[8:11], s40 offen offset:0 sc1 lds
ds_read_b128 a[0:3], v62 offset:8192
v_mfma_f32_32x32x16_bf16 v[160:175], a[4:7], v[100:103], v[160:175]
ds_read_b128 a[4:7], v62 offset:8704
v_mfma_f32_32x32x16_bf16 v[160:175], a[8:11], v[104:107], v[160:175]
buffer_load_dwordx4 v7, s[8:11], s40 offen offset:1024 sc1 lds
ds_read_b128 a[8:11], v62 offset:9216
v_max3_f32 v52, v128, v129, v130
v_max3_f32 v53, v131, v132, v133
v_max3_f32 v52, v52, v134, v135
v_mfma_f32_32x32x16_bf16 v[160:175], a[12:15], v[108:111], v[160:175]
ds_read_b128 a[12:15], v62 offset:9728
v_max3_f32 v53, v53, v136, v137
v_max3_f32 v52, v52, v138, v139
v_max3_f32 v53, v53, v140, v141
v_max3_f32 v52, v52, v142, v143
v_max_f32 v52, v52, v53
v_mfma_f32_32x32x16_bf16 v[160:175], a[16:19], v[112:115], v[160:175]
buffer_load_dwordx4 v7, s[8:11], s40 offen offset:2048 sc1 lds
ds_read_b128 a[16:19], v62 offset:10240
v_mov_b32 v53, v52
v_nop
v_mfma_f32_32x32x16_bf16 v[160:175], a[20:23], v[116:119], v[160:175]
s_add_u32 s46, s46, 16384
s_mov_b32 s49, s46
ds_read_b128 a[20:23], v62 offset:10752
v_permlane32_swap_b32 v52, v53
v_max_f32 v52, v52, v53
v_sub_f32 v192, 0, v52
v_sub_f32 v193, 0, v52
v_mfma_f32_32x32x16_bf16 v[160:175], a[24:27], v[120:123], v[160:175]
buffer_load_dwordx4 v7, s[8:11], s40 offen offset:3072 sc1 lds
ds_read_b128 a[24:27], v62 offset:11264
v_sub_f32 v194, 0, v52
v_sub_f32 v195, 0, v52
v_sub_f32 v196, 0, v52
v_mfma_f32_32x32x16_bf16 v[160:175], a[28:31], v[124:127], v[160:175]
s_xor_b32 s48, s95, 265216
ds_read_b128 a[28:31], v62 offset:11776
v_sub_f32 v197, 0, v52
v_sub_f32 v198, 0, v52
v_sub_f32 v199, 0, v52
v_sub_f32 v200, 0, v52
v_sub_f32 v201, 0, v52
v_sub_f32 v202, 0, v52
v_sub_f32 v203, 0, v52
v_sub_f32 v204, 0, v52
v_sub_f32 v205, 0, v52
v_sub_f32 v206, 0, v52
v_sub_f32 v207, 0, v52
s_waitcnt lgkmcnt(0)
s_cmp_eq_u32 s89, 0
v_mfma_f32_32x32x16_bf16 v[144:159], a[0:3], v[64:67], v[192:207]
buffer_store_dwordx4 a[140:143], v7, s[20:23], s48 offen sc1
v_add_u32 v62, s46, v32
v_add_f32 v128, v128, v192
v_add_f32 v129, v129, v192
v_add_f32 v130, v130, v192
v_add_f32 v131, v131, v192
v_mfma_f32_32x32x16_bf16 v[144:159], a[4:7], v[68:71], v[144:159]
s_xor_b32 s48, s95, 327680
v_add_f32 v132, v132, v192
v_add_f32 v133, v133, v192
v_add_f32 v134, v134, v192
v_add_f32 v135, v135, v192
v_mfma_f32_32x32x16_bf16 v[144:159], a[8:11], v[72:75], v[144:159]
s_cmp_eq_u32 s89, 0
s_cbranch_scc1 .Lj0a
.Lj0a_ret:
buffer_store_dwordx4 a[160:163], v7, s[20:23], s48 offen sc1
v_max3_f32 v56, v160, v161, v162
v_max3_f32 v57, v163, v164, v165
v_mfma_f32_32x32x16_bf16 v[144:159], a[12:15], v[76:79], v[144:159]
v_max3_f32 v56, v56, v166, v167
v_max3_f32 v57, v57, v168, v169
v_max3_f32 v56, v56, v170, v171
v_max3_f32 v57, v57, v172, v173
v_max3_f32 v56, v56, v174, v175
v_max_f32 v56, v56, v57
v_mfma_f32_32x32x16_bf16 v[144:159], a[16:19], v[80:83], v[144:159]
s_xor_b32 s48, s95, 328704
v_mov_b32 v57, v56
v_nop
s_nop 0
v_permlane32_swap_b32 v56, v57
v_max_f32 v56, v56, v57
v_sub_f32 v208, 0, v56
v_mfma_f32_32x32x16_bf16 v[144:159], a[20:23], v[84:87], v[144:159]
buffer_store_dwordx4 a[164:167], v7, s[20:23], s48 offen sc1
v_sub_f32 v209, 0, v56
v_sub_f32 v210, 0, v56
v_sub_f32 v211, 0, v56
v_sub_f32 v212, 0, v56
v_sub_f32 v213, 0, v56
v_mfma_f32_32x32x16_bf16 v[144:159], a[24:27], v[88:91], v[144:159]
v_sub_f32 v214, 0, v56
v_sub_f32 v215, 0, v56
v_sub_f32 v216, 0, v56
v_sub_f32 v217, 0, v56
v_sub_f32 v218, 0, v56
v_sub_f32 v219, 0, v56
v_sub_f32 v220, 0, v56
v_sub_f32 v221, 0, v56
v_sub_f32 v222, 0, v56
v_sub_f32 v223, 0, v56
v_mfma_f32_32x32x16_bf16 v[144:159], a[28:31], v[92:95], v[144:159]
s_xor_b32 s48, s95, 329728
s_cmp_eq_u32 s89, 0
s_cbranch_scc1 .Lj0b
.Lj0b_ret:
v_add_f32 v136, v136, v192
v_add_f32 v137, v137, v192
v_add_f32 v138, v138, v192
v_mfma_f32_32x32x16_bf16 v[176:191], a[0:3], v[96:99], v[208:223]
buffer_store_dwordx4 a[168:171], v7, s[20:23], s48 offen sc1
ds_read_b128 a[0:3], v62 offset:0
v_add_f32 v139, v139, v192
v_add_f32 v140, v140, v192
v_add_f32 v141, v141, v192
v_mfma_f32_32x32x16_bf16 v[176:191], a[4:7], v[100:103], v[176:191]
s_xor_b32 s48, s95, 330752
ds_read_b128 a[4:7], v62 offset:512
v_add_f32 v142, v142, v192
v_add_f32 v143, v143, v192
v_exp_f32 v52, v128
v_mfma_f32_32x32x16_bf16 v[176:191], a[8:11], v[104:107], v[176:191]
buffer_store_dwordx4 a[172:175], v7, s[20:23], s48 offen sc1
ds_read_b128 a[8:11], v62 offset:1024
v_exp_f32 v53, v129
v_mov_b32 v60, v52
v_mfma_f32_32x32x16_bf16 v[176:191], a[12:15], v[108:111], v[176:191]
s_cmp_eq_u32 s89, 0
s_cbranch_scc1 .Lj0c
.Lj0c_ret:
ds_read_b128 a[12:15], v62 offset:1536
v_exp_f32 v54, v130
v_mfma_f32_32x32x16_bf16 v[176:191], a[16:19], v[112:115], v[176:191]
s_xor_b32 s48, s95, 393216
ds_read_b128 a[16:19], v62 offset:2048
v_add_f32 v60, v60, v53
v_cvt_pk_bf16_f32 v224, v52, v53
v_exp_f32 v55, v131
v_mfma_f32_32x32x16_bf16 v[176:191], a[20:23], v[116:119], v[176:191]
buffer_store_dwordx4 a[192:195], v7, s[20:23], s48 offen sc1
ds_read_b128 a[20:23], v62 offset:2560
v_add_f32 v60, v60, v54
v_exp_f32 v52, v132
v_mfma_f32_32x32x16_bf16 v[176:191], a[24:27], v[120:123], v[176:191]
ds_read_b128 a[24:27], v62 offset:3072
v_add_f32 v60, v60, v55
v_cvt_pk_bf16_f32 v225, v54, v55
v_exp_f32 v53, v133
v_add_f32 v60, v60, v52
v_mfma_f32_32x32x16_bf16 v[176:191], a[28:31], v[124:127], v[176:191]
ds_read_b128 a[28:31], v62 offset:3584
v_exp_f32 v54, v134
v_add_f32 v60, v60, v53
v_cvt_pk_bf16_f32 v226, v52, v53
v_exp_f32 v55, v135
v_add_f32 v60, v60, v54
v_exp_f32 v52, v136
v_add_f32 v60, v60, v55
v_cvt_pk_bf16_f32 v227, v54, v55
v_exp_f32 v53, v137
v_add_f32 v60, v60, v52
v_exp_f32 v54, v138
v_add_f32 v60, v60, v53
v_cvt_pk_bf16_f32 v228, v52, v53
v_exp_f32 v55, v139
v_add_f32 v60, v60, v54
v_exp_f32 v52, v140
v_add_f32 v60, v60, v55
v_cvt_pk_bf16_f32 v229, v54, v55
v_exp_f32 v53, v141
v_add_f32 v60, v60, v52
v_exp_f32 v54, v142
v_add_f32 v60, v60, v53
v_cvt_pk_bf16_f32 v230, v52, v53
v_exp_f32 v55, v143
v_add_f32 v60, v60, v54
v_add_f32 v60, v60, v55
v_cvt_pk_bf16_f32 v231, v54, v55
s_cmp_eq_u32 s89, 0
s_cselect_b64 vcc, -1, 0
v_cndmask_b32 v14, v208, v14, vcc
v_cndmask_b32 v13, v8, v13, vcc
v_add_f32 v160, v160, v14
v_add_f32 v161, v161, v14
v_add_f32 v162, v162, v14
v_add_f32 v163, v163, v14
v_add_f32 v164, v164, v14
v_add_f32 v165, v165, v14
v_add_f32 v166, v166, v14
v_add_f32 v167, v167, v14
v_add_f32 v168, v168, v14
v_add_f32 v169, v169, v14
v_add_f32 v170, v170, v14
v_add_f32 v171, v171, v14
v_add_f32 v172, v172, v14
v_add_f32 v173, v173, v14
v_add_f32 v174, v174, v14
v_add_f32 v175, v175, v14
v_exp_f32 v56, v160
v_exp_f32 v57, v161
v_mov_b32 v61, v56
v_exp_f32 v58, v162
v_add_f32 v61, v61, v57
v_cvt_pk_bf16_f32 v240, v56, v57
v_exp_f32 v59, v163
v_add_f32 v61, v61, v58
v_exp_f32 v56, v164
v_add_f32 v61, v61, v59
v_cvt_pk_bf16_f32 v241, v58, v59
v_exp_f32 v57, v165
v_add_f32 v61, v61, v56
v_exp_f32 v58, v166
v_add_f32 v61, v61, v57
v_cvt_pk_bf16_f32 v242, v56, v57
v_exp_f32 v59, v167
v_add_f32 v61, v61, v58
v_exp_f32 v56, v168
v_add_f32 v61, v61, v59
v_cvt_pk_bf16_f32 v243, v58, v59
v_exp_f32 v57, v169
v_add_f32 v61, v61, v56
v_exp_f32 v58, v170
v_add_f32 v61, v61, v57
v_cvt_pk_bf16_f32 v244, v56, v57
v_exp_f32 v59, v171
v_add_f32 v61, v61, v58
v_exp_f32 v56, v172
v_add_f32 v61, v61, v59
v_cvt_pk_bf16_f32 v245, v58, v59
v_exp_f32 v57, v173
v_add_f32 v61, v61, v56
v_exp_f32 v58, v174
v_add_f32 v61, v61, v57
v_cvt_pk_bf16_f32 v246, v56, v57
v_exp_f32 v59, v175
v_add_f32 v61, v61, v58
v_add_f32 v61, v61, v59
v_cvt_pk_bf16_f32 v247, v58, v59
s_add_u32 s46, s46, 16384
s_and_b32 s46, s46, 0xffff
s_mov_b32 s44, 65536
s_mov_b32 s45, 32768
s_xor_b32 s50, s46, s52
s_lshl_b32 s28, s89, 2
s_add_u32 s29, s28, s3
s_max_u32 s29, s29, 3
s_sub_u32 s55, s29, 4
s_sub_u32 s56, 56, s28
s_add_u32 s56, s56, s3
s_sub_u32 s56, s56, s29
s_mov_b32 s57, 0
s_getpc_b64 s[58:59]
.Lpc0:
s_add_u32 s58, s58, .Ljob0-.Lpc0
s_addc_u32 s59, s59, 0
s_waitcnt vmcnt(14) lgkmcnt(0)
s_barrier
v_mfma_f32_32x32x16_bf16 v[128:143], a[0:3], v[64:67], v[192:207]
ds_read_b128 a[32:35], v63 offset:0
v_exp_f32 v52, v144
v_exp_f32 v56, v176
v_add_f32 v60, v60, v52
v_mfma_f32_32x32x16_bf16 v[160:175], a[0:3], v[96:99], v[208:223]
s_xor_b32 s48, s95, 394240
ds_read_b128 a[0:3], v62 offset:8192
v_exp_f32 v53, v145
v_add_f32 v61, v61, v56
v_cmp_gt_i32 s[32:33], 0, v13
v_mfma_f32_32x32x16_bf16 v[128:143], a[4:7], v[68:71], v[128:143]
buffer_store_dwordx4 a[196:199], v7, s[20:23], s48 offen sc1
ds_read_b128 a[36:39], v63 offset:1024
v_exp_f32 v57, v177
v_add_f32 v60, v60, v53
v_cvt_pk_bf16_f32 v232, v52, v53
v_mfma_f32_32x32x16_bf16 v[160:175], a[4:7], v[100:103], v[160:175]
ds_read_b128 a[4:7], v62 offset:8704
v_exp_f32 v54, v146
v_add_f32 v61, v61, v57
v_cvt_pk_bf16_f32 v248, v56, v57
v_nop
v_mfma_f32_32x32x16_bf16 v[128:143], a[8:11], v[72:75], v[128:143]
ds_read_b128 a[40:43], v63 offset:2048
v_exp_f32 v58, v178
v_add_f32 v60, v60, v54
v_cmp_gt_i32 s[34:35], 1, v13
v_cndmask_b32 v36, v14, v34, s[32:33]
v_mfma_f32_32x32x16_bf16 v[160:175], a[8:11], v[104:107], v[160:175]
s_xor_b32 s48, s95, 395264
ds_read_b128 a[8:11], v62 offset:9216
v_exp_f32 v55, v147
v_add_f32 v61, v61, v58
v_cmp_gt_i32 s[32:33], 2, v13
v_mfma_f32_32x32x16_bf16 v[128:143], a[12:15], v[76:79], v[128:143]
buffer_store_dwordx4 a[200:203], v7, s[20:23], s48 offen sc1
ds_read_b128 a[44:47], v63 offset:3072
v_exp_f32 v59, v179
v_add_f32 v60, v60, v55
v_cvt_pk_bf16_f32 v233, v54, v55
v_mfma_f32_32x32x16_bf16 v[160:175], a[12:15], v[108:111], v[160:175]
ds_read_b128 a[12:15], v62 offset:9728
v_exp_f32 v52, v148
v_add_f32 v61, v61, v59
v_cvt_pk_bf16_f32 v249, v58, v59
v_cndmask_b32 v37, v14, v34, s[34:35]
v_mfma_f32_32x32x16_bf16 v[128:143], a[16:19], v[80:83], v[128:143]
ds_read_b128 a[48:51], v63 offset:4096
v_exp_f32 v56, v180
v_add_f32 v60, v60, v52
v_cmp_gt_i32 s[34:35], 3, v13
v_cndmask_b32 v38, v14, v34, s[32:33]
v_mfma_f32_32x32x16_bf16 v[160:175], a[16:19], v[112:115], v[160:175]
s_xor_b32 s48, s95, 396288
ds_read_b128 a[16:19], v62 offset:10240
ds_read_b128 a[56:59], v63 offset:6144
v_exp_f32 v53, v149
v_add_f32 v61, v61, v56
v_mfma_f32_32x32x16_bf16 v[128:143], a[20:23], v[84:87], v[128:143]
buffer_store_dwordx4 a[204:207], v7, s[20:23], s48 offen sc1
ds_read_b128 a[52:55], v63 offset:5120
v_exp_f32 v57, v181
v_add_f32 v60, v60, v53
v_cvt_pk_bf16_f32 v234, v52, v53
v_mfma_f32_32x32x16_bf16 v[160:175], a[20:23], v[116:119], v[160:175]
ds_read_b128 a[20:23], v62 offset:10752
ds_read_b128 a[60:63], v63 offset:7168
v_exp_f32 v54, v150
v_add_f32 v61, v61, v57
v_cvt_pk_bf16_f32 v250, v56, v57
v_mfma_f32_32x32x16_bf16 v[128:143], a[24:27], v[88:91], v[128:143]
v_exp_f32 v58, v182
v_add_f32 v60, v60, v54
v_cmp_gt_i32 s[32:33], 8, v13
v_cndmask_b32 v39, v14, v34, s[34:35]
v_cmp_gt_i32 s[34:35], 9, v13
v_mfma_f32_32x32x16_bf16 v[160:175], a[24:27], v[120:123], v[160:175]
s_xor_b32 s48, s95, 458752
ds_read_b128 a[24:27], v62 offset:11264
v_exp_f32 v55, v151
v_add_f32 v61, v61, v58
v_cndmask_b32 v40, v14, v34, s[32:33]
v_mfma_f32_32x32x16_bf16 v[128:143], a[28:31], v[92:95], v[128:143]
buffer_store_dwordx4 a[224:227], v7, s[20:23], s48 offen sc1
v_exp_f32 v59, v183
v_add_f32 v60, v60, v55
v_cvt_pk_bf16_f32 v235, v54, v55
v_cmp_gt_i32 s[32:33], 10, v13
v_mfma_f32_32x32x16_bf16 v[160:175], a[28:31], v[124:127], v[160:175]
s_waitcnt lgkmcnt(1)
ds_read_b128 a[28:31], v62 offset:11776
v_exp_f32 v52, v152
v_add_f32 v61, v61, v59
v_cvt_pk_bf16_f32 v251, v58, v59
v_mfma_f32_32x32x16_bf16 a[64:79], a[32:35], v[224:227], 0
v_exp_f32 v56, v184
v_add_f32 v60, v60, v52
v_exp_f32 v53, v153
v_add_f32 v61, v61, v56
v_mfma_f32_32x32x16_bf16 a[176:191], a[32:35], v[240:243], 0
s_xor_b32 s48, s95, 459776
ds_read_b128 a[32:35], v63 offset:8192
v_add_u32 v62, s46, v32
v_exp_f32 v57, v185
v_add_f32 v60, v60, v53
v_cvt_pk_bf16_f32 v236, v52, v53
v_mfma_f32_32x32x16_bf16 a[80:95], a[36:39], v[224:227], 0
buffer_store_dwordx4 a[228:231], v7, s[20:23], s48 offen sc1
v_exp_f32 v54, v154
v_add_f32 v61, v61, v57
v_cvt_pk_bf16_f32 v252, v56, v57
v_cndmask_b32 v41, v14, v34, s[34:35]
v_mfma_f32_32x32x16_bf16 a[208:223], a[36:39], v[240:243], 0
ds_read_b128 a[36:39], v63 offset:9216
v_exp_f32 v58, v186
v_add_f32 v60, v60, v54
v_cmp_gt_i32 s[34:35], 11, v13
v_cndmask_b32 v42, v14, v34, s[32:33]
v_mfma_f32_32x32x16_bf16 a[112:127], a[40:43], v[224:227], 0
v_exp_f32 v55, v155
v_add_f32 v61, v61, v58
v_cmp_gt_i32 s[32:33], 16, v13
v_cndmask_b32 v43, v14, v34, s[34:35]
v_cmp_gt_i32 s[34:35], 17, v13
v_mfma_f32_32x32x16_bf16 a[240:255], a[40:43], v[240:243], 0
s_xor_b32 s48, s95, 460800
ds_read_b128 a[40:43], v63 offset:10240
v_exp_f32 v59, v187
v_add_f32 v60, v60, v55
v_cvt_pk_bf16_f32 v237, v54, v55
v_mfma_f32_32x32x16_bf16 a[144:159], a[44:47], v[224:227], 0
buffer_store_dwordx4 a[232:235], v7, s[20:23], s48 offen sc1
v_exp_f32 v52, v156
v_add_f32 v61, v61, v59
v_cvt_pk_bf16_f32 v253, v58, v59
v_cndmask_b32 v44, v14, v34, s[32:33]
v_mfma_f32_32x32x16_bf16 a[96:111], a[44:47], v[240:243], 0
ds_read_b128 a[44:47], v63 offset:11264
v_exp_f32 v56, v188
v_add_f32 v60, v60, v52
v_cmp_gt_i32 s[32:33], 18, v13
v_cndmask_b32 v45, v14, v34, s[34:35]
v_mfma_f32_32x32x16_bf16 a[64:79], a[48:51], v[228:231], a[64:79]
v_exp_f32 v53, v157
v_add_f32 v61, v61, v56
v_cmp_gt_i32 s[34:35], 19, v13
v_cndmask_b32 v46, v14, v34, s[32:33]
v_cmp_gt_i32 s[32:33], 24, v13
v_mfma_f32_32x32x16_bf16 a[176:191], a[48:51], v[244:247], a[176:191]
s_xor_b32 s48, s95, 461824
ds_read_b128 a[48:51], v63 offset:12288
v_exp_f32 v57, v189
v_add_f32 v60, v60, v53
v_cvt_pk_bf16_f32 v238, v52, v53
v_mfma_f32_32x32x16_bf16 a[80:95], a[52:55], v[228:231], a[80:95]
buffer_store_dwordx4 a[236:239], v7, s[20:23], s48 offen sc1
v_exp_f32 v54, v158
v_add_f32 v61, v61, v57
v_cvt_pk_bf16_f32 v254, v56, v57
v_cndmask_b32 v47, v14, v34, s[34:35]
v_mfma_f32_32x32x16_bf16 a[208:223], a[52:55], v[244:247], a[208:223]
ds_read_b128 a[52:55], v63 offset:13312
v_exp_f32 v58, v190
v_add_f32 v60, v60, v54
v_cmp_gt_i32 s[34:35], 25, v13
v_cndmask_b32 v48, v14, v34, s[32:33]
v_mfma_f32_32x32x16_bf16 a[112:127], a[56:59], v[228:231], a[112:127]
v_exp_f32 v55, v159
v_add_f32 v61, v61, v58
v_cmp_gt_i32 s[32:33], 26, v13
v_cndmask_b32 v49, v14, v34, s[34:35]
v_cmp_gt_i32 s[34:35], 27, v13
v_mfma_f32_32x32x16_bf16 a[240:255], a[56:59], v[244:247], a[240:255]
ds_read_b128 a[56:59], v63 offset:14336
v_exp_f32 v59, v191
v_add_f32 v60, v60, v55
v_cvt_pk_bf16_f32 v239, v54, v55
v_cndmask_b32 v50, v14, v34, s[32:33]
v_mfma_f32_32x32x16_bf16 a[144:159], a[60:63], v[228:231], a[144:159]
v_add_f32 v61, v61, v59
v_cvt_pk_bf16_f32 v255, v58, v59
v_nop
v_cndmask_b32 v51, v14, v34, s[34:35]
v_cmp_gt_i32 s[32:33], 0, v8
v_nop
v_mfma_f32_32x32x16_bf16 a[96:111], a[60:63], v[244:247], a[96:111]
s_waitcnt vmcnt(17) lgkmcnt(7)
s_barrier
ds_read_b128 a[60:63], v63 offset:15360
v_add_u32 v63, s49, v33
v_cmp_gt_i32 s[34:35], 1, v8
v_cndmask_b32 v16, v192, v34, s[32:33]
v_cmp_gt_i32 s[32:33], 2, v8
v_cndmask_b32 v17, v193, v34, s[34:35]
v_cmp_gt_i32 s[34:35], 3, v8
v_cndmask_b32 v18, v194, v34, s[32:33]
v_cmp_gt_i32 s[32:33], 8, v8
v_cndmask_b32 v19, v195, v34, s[34:35]
v_cmp_gt_i32 s[34:35], 9, v8
v_cndmask_b32 v20, v196, v34, s[32:33]
v_cmp_gt_i32 s[32:33], 10, v8
v_cndmask_b32 v21, v197, v34, s[34:35]
v_cmp_gt_i32 s[34:35], 11, v8
v_cndmask_b32 v22, v198, v34, s[32:33]
v_cmp_gt_i32 s[32:33], 16, v8
v_cndmask_b32 v23, v199, v34, s[34:35]
v_cmp_gt_i32 s[34:35], 17, v8
v_cndmask_b32 v24, v200, v34, s[32:33]
v_cmp_gt_i32 s[32:33], 18, v8
v_cndmask_b32 v25, v201, v34, s[34:35]
v_cmp_gt_i32 s[34:35], 19, v8
v_cndmask_b32 v26, v202, v34, s[32:33]
v_cmp_gt_i32 s[32:33], 24, v8
v_cndmask_b32 v27, v203, v34, s[34:35]
v_cmp_gt_i32 s[34:35], 25, v8
v_cndmask_b32 v28, v204, v34, s[32:33]
v_cmp_gt_i32 s[32:33], 26, v8
v_cndmask_b32 v29, v205, v34, s[34:35]
v_cmp_gt_i32 s[34:35], 27, v8
v_cndmask_b32 v30, v206, v34, s[32:33]
v_nop
v_cndmask_b32 v31, v207, v34, s[34:35]
s_cmp_eq_u32 s29, 3
s_cbranch_scc1 .LT1
.LD1:
v_mfma_f32_32x32x16_bf16 v[144:159], a[0:3], v[64:67], v[192:207]
s_mov_b32 m0, s50
v_exp_f32 v52, v128
v_exp_f32 v56, v160
v_add_f32 v60, v60, v52
v_mfma_f32_32x32x16_bf16 v[176:191], a[0:3], v[96:99], v[208:223]
ds_read_b128 a[0:3], v62 offset:0
v_exp_f32 v53, v129
v_add_f32 v61, v61, v56
v_mfma_f32_32x32x16_bf16 v[144:159], a[4:7], v[68:71], v[144:159]
s_add_u32 s45, s45, 16384
v_exp_f32 v57, v161
v_add_f32 v60, v60, v53
v_cvt_pk_bf16_f32 v224, v52, v53
v_mfma_f32_32x32x16_bf16 v[176:191], a[4:7], v[100:103], v[176:191]
ds_read_b128 a[4:7], v62 offset:512
v_exp_f32 v54, v130
v_add_f32 v61, v61, v57
v_cvt_pk_bf16_f32 v240, v56, v57
v_mfma_f32_32x32x16_bf16 v[144:159], a[8:11], v[72:75], v[144:159]
buffer_load_dwordx4 v7, s[4:7], s44 offen offset:0 sc1 lds
v_exp_f32 v58, v162
v_add_f32 v60, v60, v54
v_mfma_f32_32x32x16_bf16 v[176:191], a[8:11], v[104:107], v[176:191]
ds_read_b128 a[8:11], v62 offset:1024
v_exp_f32 v55, v131
v_add_f32 v61, v61, v58
v_mfma_f32_32x32x16_bf16 v[144:159], a[12:15], v[76:79], v[144:159]
s_xor_b32 s51, s49, s53
v_exp_f32 v59, v163
v_add_f32 v60, v60, v55
v_cvt_pk_bf16_f32 v225, v54, v55
v_mfma_f32_32x32x16_bf16 v[176:191], a[12:15], v[108:111], v[176:191]
ds_read_b128 a[12:15], v62 offset:1536
v_exp_f32 v52, v132
v_add_f32 v61, v61, v59
v_cvt_pk_bf16_f32 v241, v58, v59
v_mfma_f32_32x32x16_bf16 v[144:159], a[16:19], v[80:83], v[144:159]
s_cmp_lt_u32 s45, s54
s_cselect_b32 s51, s51, s27
v_exp_f32 v56, v164
v_add_f32 v60, v60, v52
v_mfma_f32_32x32x16_bf16 v[176:191], a[16:19], v[112:115], v[176:191]
ds_read_b128 a[16:19], v62 offset:2048
v_exp_f32 v53, v133
v_add_f32 v61, v61, v56
v_mfma_f32_32x32x16_bf16 v[144:159], a[20:23], v[84:87], v[144:159]
s_add_u32 s46, s46, 16384
v_exp_f32 v57, v165
v_add_f32 v60, v60, v53
v_cvt_pk_bf16_f32 v226, v52, v53
v_mfma_f32_32x32x16_bf16 v[176:191], a[20:23], v[116:119], v[176:191]
ds_read_b128 a[20:23], v62 offset:2560
v_exp_f32 v54, v134
v_add_f32 v61, v61, v57
v_cvt_pk_bf16_f32 v242, v56, v57
v_mfma_f32_32x32x16_bf16 v[144:159], a[24:27], v[88:91], v[144:159]
buffer_load_dwordx4 v7, s[4:7], s44 offen offset:1024 sc1 lds
v_exp_f32 v58, v166
v_add_f32 v60, v60, v54
v_mfma_f32_32x32x16_bf16 v[176:191], a[24:27], v[120:123], v[176:191]
ds_read_b128 a[24:27], v62 offset:3072
v_exp_f32 v55, v135
v_add_f32 v61, v61, v58
v_mfma_f32_32x32x16_bf16 v[144:159], a[28:31], v[92:95], v[144:159]
v_exp_f32 v59, v167
v_add_f32 v60, v60, v55
v_cvt_pk_bf16_f32 v227, v54, v55
v_mfma_f32_32x32x16_bf16 v[176:191], a[28:31], v[124:127], v[176:191]
s_waitcnt lgkmcnt(7)
ds_read_b128 a[28:31], v62 offset:3584
v_exp_f32 v52, v136
v_add_f32 v61, v61, v59
v_cvt_pk_bf16_f32 v243, v58, v59
v_mfma_f32_32x32x16_bf16 a[64:79], a[32:35], v[232:235], a[64:79]
s_and_b32 s46, s46, 0xffff
v_exp_f32 v56, v168
v_add_f32 v60, v60, v52
v_exp_f32 v53, v137
v_add_f32 v61, v61, v56
v_mfma_f32_32x32x16_bf16 a[176:191], a[32:35], v[248:251], a[176:191]
ds_read_b128 a[32:35], v63 offset:0
v_exp_f32 v57, v169
v_add_f32 v60, v60, v53
v_cvt_pk_bf16_f32 v228, v52, v53
v_mfma_f32_32x32x16_bf16 a[80:95], a[36:39], v[232:235], a[80:95]
s_add_u32 s49, s49, 16384
v_exp_f32 v54, v138
v_add_f32 v61, v61, v57
v_cvt_pk_bf16_f32 v244, v56, v57
v_mfma_f32_32x32x16_bf16 a[208:223], a[36:39], v[248:251], a[208:223]
ds_read_b128 a[36:39], v63 offset:1024
v_exp_f32 v58, v170
v_add_f32 v60, v60, v54
v_mfma_f32_32x32x16_bf16 a[112:127], a[40:43], v[232:235], a[112:127]
buffer_load_dwordx4 v7, s[4:7], s44 offen offset:2048 sc1 lds
v_exp_f32 v55, v139
v_add_f32 v61, v61, v58
v_mfma_f32_32x32x16_bf16 a[240:255], a[40:43], v[248:251], a[240:255]
ds_read_b128 a[40:43], v63 offset:2048
v_exp_f32 v59, v171
v_add_f32 v60, v60, v55
v_cvt_pk_bf16_f32 v229, v54, v55
v_mfma_f32_32x32x16_bf16 a[144:159], a[44:47], v[232:235], a[144:159]
s_and_b32 s49, s49, 0xffff
v_exp_f32 v52, v140
v_add_f32 v61, v61, v59
v_cvt_pk_bf16_f32 v245, v58, v59
v_mfma_f32_32x32x16_bf16 a[96:111], a[44:47], v[248:251], a[96:111]
ds_read_b128 a[44:47], v63 offset:3072
v_exp_f32 v56, v172
v_add_f32 v60, v60, v52
v_mfma_f32_32x32x16_bf16 a[64:79], a[48:51], v[236:239], a[64:79]
v_exp_f32 v53, v141
v_add_f32 v61, v61, v56
v_mfma_f32_32x32x16_bf16 a[176:191], a[48:51], v[252:255], a[176:191]
ds_read_b128 a[48:51], v63 offset:4096
v_exp_f32 v57, v173
v_add_f32 v60, v60, v53
v_cvt_pk_bf16_f32 v230, v52, v53
v_mfma_f32_32x32x16_bf16 a[80:95], a[52:55], v[236:239], a[80:95]
s_sub_u32 s55, s55, 1
v_exp_f32 v54, v142
v_add_f32 v61, v61, v57
v_cvt_pk_bf16_f32 v246, v56, v57
v_mfma_f32_32x32x16_bf16 a[208:223], a[52:55], v[252:255], a[208:223]
ds_read_b128 a[52:55], v63 offset:5120
v_exp_f32 v58, v174
v_add_f32 v60, v60, v54
v_mfma_f32_32x32x16_bf16 a[112:127], a[56:59], v[236:239], a[112:127]
buffer_load_dwordx4 v7, s[4:7], s44 offen offset:3072 sc1 lds
v_exp_f32 v55, v143
v_add_f32 v61, v61, v58
v_mfma_f32_32x32x16_bf16 a[240:255], a[56:59], v[252:255], a[240:255]
ds_read_b128 a[56:59], v63 offset:6144
v_exp_f32 v59, v175
v_add_f32 v60, v60, v55
v_cvt_pk_bf16_f32 v231, v54, v55
v_mfma_f32_32x32x16_bf16 a[144:159], a[60:63], v[236:239], a[144:159]
v_add_f32 v61, v61, v59
v_cvt_pk_bf16_f32 v247, v58, v59
v_mfma_f32_32x32x16_bf16 a[96:111], a[60:63], v[252:255], a[96:111]
s_waitcnt lgkmcnt(7)
ds_read_b128 a[60:63], v63 offset:7168
s_cbranch_scc1 .LT0x
s_sub_u32 s57, s57, 1
s_cbranch_scc1 .Lh_d1_c
.Lh_d1:
v_mfma_f32_32x32x16_bf16 v[128:143], a[0:3], v[64:67], v[192:207]
s_mov_b32 m0, s51
v_exp_f32 v52, v144
v_exp_f32 v56, v176
v_add_f32 v60, v60, v52
v_mfma_f32_32x32x16_bf16 v[160:175], a[0:3], v[96:99], v[208:223]
ds_read_b128 a[0:3], v62 offset:8192
v_exp_f32 v53, v145
v_add_f32 v61, v61, v56
v_mfma_f32_32x32x16_bf16 v[128:143], a[4:7], v[68:71], v[128:143]
s_add_u32 s44, s44, 16384
v_exp_f32 v57, v177
v_add_f32 v60, v60, v53
v_cvt_pk_bf16_f32 v232, v52, v53
v_mfma_f32_32x32x16_bf16 v[160:175], a[4:7], v[100:103], v[160:175]
ds_read_b128 a[4:7], v62 offset:8704
v_exp_f32 v54, v146
v_add_f32 v61, v61, v57
v_cvt_pk_bf16_f32 v248, v56, v57
v_mfma_f32_32x32x16_bf16 v[128:143], a[8:11], v[72:75], v[128:143]
buffer_load_dwordx4 v7, s[8:11], s45 offen offset:0 sc1 lds
v_exp_f32 v58, v178
v_add_f32 v60, v60, v54
v_mfma_f32_32x32x16_bf16 v[160:175], a[8:11], v[104:107], v[160:175]
ds_read_b128 a[8:11], v62 offset:9216
v_exp_f32 v55, v147
v_add_f32 v61, v61, v58
v_mfma_f32_32x32x16_bf16 v[128:143], a[12:15], v[76:79], v[128:143]
s_xor_b32 s50, s46, s52
v_exp_f32 v59, v179
v_add_f32 v60, v60, v55
v_cvt_pk_bf16_f32 v233, v54, v55
v_mfma_f32_32x32x16_bf16 v[160:175], a[12:15], v[108:111], v[160:175]
ds_read_b128 a[12:15], v62 offset:9728
v_exp_f32 v52, v148
v_add_f32 v61, v61, v59
v_cvt_pk_bf16_f32 v249, v58, v59
v_mfma_f32_32x32x16_bf16 v[128:143], a[16:19], v[80:83], v[128:143]
s_cmp_lt_u32 s44, s54
s_cselect_b32 s50, s50, s27
v_exp_f32 v56, v180
v_add_f32 v60, v60, v52
v_mfma_f32_32x32x16_bf16 v[160:175], a[16:19], v[112:115], v[160:175]
ds_read_b128 a[16:19], v62 offset:10240
v_exp_f32 v53, v149
v_add_f32 v61, v61, v56
v_mfma_f32_32x32x16_bf16 v[128:143], a[20:23], v[84:87], v[128:143]
v_exp_f32 v57, v181
v_add_f32 v60, v60, v53
v_cvt_pk_bf16_f32 v234, v52, v53
v_mfma_f32_32x32x16_bf16 v[160:175], a[20:23], v[116:119], v[160:175]
ds_read_b128 a[20:23], v62 offset:10752
v_exp_f32 v54, v150
v_add_f32 v61, v61, v57
v_cvt_pk_bf16_f32 v250, v56, v57
v_mfma_f32_32x32x16_bf16 v[128:143], a[24:27], v[88:91], v[128:143]
buffer_load_dwordx4 v7, s[8:11], s45 offen offset:1024 sc1 lds
v_exp_f32 v58, v182
v_add_f32 v60, v60, v54
v_mfma_f32_32x32x16_bf16 v[160:175], a[24:27], v[120:123], v[160:175]
ds_read_b128 a[24:27], v62 offset:11264
v_exp_f32 v55, v151
v_add_f32 v61, v61, v58
v_mfma_f32_32x32x16_bf16 v[128:143], a[28:31], v[92:95], v[128:143]
v_exp_f32 v59, v183
v_add_f32 v60, v60, v55
v_cvt_pk_bf16_f32 v235, v54, v55
v_mfma_f32_32x32x16_bf16 v[160:175], a[28:31], v[124:127], v[160:175]
s_waitcnt lgkmcnt(7)
ds_read_b128 a[28:31], v62 offset:11776
v_exp_f32 v52, v152
v_add_f32 v61, v61, v59
v_cvt_pk_bf16_f32 v251, v58, v59
v_mfma_f32_32x32x16_bf16 a[64:79], a[32:35], v[224:227], a[64:79]
v_exp_f32 v56, v184
v_add_f32 v60, v60, v52
v_exp_f32 v53, v153
v_add_f32 v61, v61, v56
v_mfma_f32_32x32x16_bf16 a[176:191], a[32:35], v[240:243], a[176:191]
ds_read_b128 a[32:35], v63 offset:8192
v_add_u32 v62, s46, v32
v_exp_f32 v57, v185
v_add_f32 v60, v60, v53
v_cvt_pk_bf16_f32 v236, v52, v53
v_mfma_f32_32x32x16_bf16 a[80:95], a[36:39], v[224:227], a[80:95]
v_exp_f32 v54, v154
v_add_f32 v61, v61, v57
v_cvt_pk_bf16_f32 v252, v56, v57
v_mfma_f32_32x32x16_bf16 a[208:223], a[36:39], v[240:243], a[208:223]
ds_read_b128 a[36:39], v63 offset:9216
v_exp_f32 v58, v186
v_add_f32 v60, v60, v54
v_mfma_f32_32x32x16_bf16 a[112:127], a[40:43], v[224:227], a[112:127]
buffer_load_dwordx4 v7, s[8:11], s45 offen offset:2048 sc1 lds
v_exp_f32 v55, v155
v_add_f32 v61, v61, v58
v_mfma_f32_32x32x16_bf16 a[240:255], a[40:43], v[240:243], a[240:255]
ds_read_b128 a[40:43], v63 offset:10240
v_exp_f32 v59, v187
v_add_f32 v60, v60, v55
v_cvt_pk_bf16_f32 v237, v54, v55
v_mfma_f32_32x32x16_bf16 a[144:159], a[44:47], v[224:227], a[144:159]
v_exp_f32 v52, v156
v_add_f32 v61, v61, v59
v_cvt_pk_bf16_f32 v253, v58, v59
v_mfma_f32_32x32x16_bf16 a[96:111], a[44:47], v[240:243], a[96:111]
ds_read_b128 a[44:47], v63 offset:11264
v_exp_f32 v56, v188
v_add_f32 v60, v60, v52
v_mfma_f32_32x32x16_bf16 a[64:79], a[48:51], v[228:231], a[64:79]
v_exp_f32 v53, v157
v_add_f32 v61, v61, v56
v_mfma_f32_32x32x16_bf16 a[176:191], a[48:51], v[244:247], a[176:191]
ds_read_b128 a[48:51], v63 offset:12288
v_exp_f32 v57, v189
v_add_f32 v60, v60, v53
v_cvt_pk_bf16_f32 v238, v52, v53
v_mfma_f32_32x32x16_bf16 a[80:95], a[52:55], v[228:231], a[80:95]
s_sub_u32 s55, s55, 1
v_exp_f32 v54, v158
v_add_f32 v61, v61, v57
v_cvt_pk_bf16_f32 v254, v56, v57
v_mfma_f32_32x32x16_bf16 a[208:223], a[52:55], v[244:247], a[208:223]
ds_read_b128 a[52:55], v63 offset:13312
v_exp_f32 v58, v190
v_add_f32 v60, v60, v54
v_mfma_f32_32x32x16_bf16 a[112:127], a[56:59], v[228:231], a[112:127]
buffer_load_dwordx4 v7, s[8:11], s45 offen offset:3072 sc1 lds
v_exp_f32 v55, v159
v_add_f32 v61, v61, v58
v_mfma_f32_32x32x16_bf16 a[240:255], a[56:59], v[244:247], a[240:255]
ds_read_b128 a[56:59], v63 offset:14336
v_exp_f32 v59, v191
v_add_f32 v60, v60, v55
v_cvt_pk_bf16_f32 v239, v54, v55
v_mfma_f32_32x32x16_bf16 a[144:159], a[60:63], v[228:231], a[144:159]
v_add_f32 v61, v61, v59
v_cvt_pk_bf16_f32 v255, v58, v59
v_mfma_f32_32x32x16_bf16 a[96:111], a[60:63], v[244:247], a[96:111]
s_waitcnt vmcnt(8) lgkmcnt(7)
s_barrier
ds_read_b128 a[60:63], v63 offset:15360
v_add_u32 v63, s49, v33
s_cbranch_scc1 .LT1x
s_sub_u32 s57, s57, 1
s_cbranch_scc0 .LD1
s_swappc_b64 s[30:31], s[58:59]
.Lret_d:
s_branch .LD1
.LT0x:
s_sub_u32 s57, s57, 1
s_cbranch_scc0 .LT0
s_swappc_b64 s[30:31], s[58:59]
.LT0:
v_mfma_f32_32x32x16_bf16 v[128:143], a[0:3], v[64:67], v[192:207]
s_mov_b32 m0, s51
v_exp_f32 v52, v144
v_exp_f32 v56, v176
v_add_f32 v60, v60, v52
v_mfma_f32_32x32x16_bf16 v[160:175], a[0:3], v[96:99], v[36:51]
ds_read_b128 a[0:3], v62 offset:8192
v_exp_f32 v53, v145
v_add_f32 v61, v61, v56
v_mfma_f32_32x32x16_bf16 v[128:143], a[4:7], v[68:71], v[128:143]
s_add_u32 s44, s44, 16384
v_exp_f32 v57, v177
v_add_f32 v60, v60, v53
v_cvt_pk_bf16_f32 v232, v52, v53
v_mfma_f32_32x32x16_bf16 v[160:175], a[4:7], v[100:103], v[160:175]
ds_read_b128 a[4:7], v62 offset:8704
v_exp_f32 v54, v146
v_add_f32 v61, v61, v57
v_cvt_pk_bf16_f32 v248, v56, v57
v_mfma_f32_32x32x16_bf16 v[128:143], a[8:11], v[72:75], v[128:143]
buffer_load_dwordx4 v7, s[8:11], s45 offen offset:0 sc1 lds
v_exp_f32 v58, v178
v_add_f32 v60, v60, v54
v_mfma_f32_32x32x16_bf16 v[160:175], a[8:11], v[104:107], v[160:175]
ds_read_b128 a[8:11], v62 offset:9216
v_exp_f32 v55, v147
v_add_f32 v61, v61, v58
v_mfma_f32_32x32x16_bf16 v[128:143], a[12:15], v[76:79], v[128:143]
s_xor_b32 s50, s46, s52
v_exp_f32 v59, v179
v_add_f32 v60, v60, v55
v_cvt_pk_bf16_f32 v233, v54, v55
v_mfma_f32_32x32x16_bf16 v[160:175], a[12:15], v[108:111], v[160:175]
ds_read_b128 a[12:15], v62 offset:9728
v_exp_f32 v52, v148
v_add_f32 v61, v61, v59
v_cvt_pk_bf16_f32 v249, v58, v59
v_mfma_f32_32x32x16_bf16 v[128:143], a[16:19], v[80:83], v[128:143]
s_cmp_lt_u32 s44, s54
s_cselect_b32 s50, s50, s27
v_exp_f32 v56, v180
v_add_f32 v60, v60, v52
v_mfma_f32_32x32x16_bf16 v[160:175], a[16:19], v[112:115], v[160:175]
ds_read_b128 a[16:19], v62 offset:10240
v_exp_f32 v53, v149
v_add_f32 v61, v61, v56
v_mfma_f32_32x32x16_bf16 v[128:143], a[20:23], v[84:87], v[128:143]
v_exp_f32 v57, v181
v_add_f32 v60, v60, v53
v_cvt_pk_bf16_f32 v234, v52, v53
v_mfma_f32_32x32x16_bf16 v[160:175], a[20:23], v[116:119], v[160:175]
ds_read_b128 a[20:23], v62 offset:10752
v_exp_f32 v54, v150
v_add_f32 v61, v61, v57
v_cvt_pk_bf16_f32 v250, v56, v57
v_mfma_f32_32x32x16_bf16 v[128:143], a[24:27], v[88:91], v[128:143]
buffer_load_dwordx4 v7, s[8:11], s45 offen offset:1024 sc1 lds
v_exp_f32 v58, v182
v_add_f32 v60, v60, v54
v_mfma_f32_32x32x16_bf16 v[160:175], a[24:27], v[120:123], v[160:175]
ds_read_b128 a[24:27], v62 offset:11264
v_exp_f32 v55, v151
v_add_f32 v61, v61, v58
v_mfma_f32_32x32x16_bf16 v[128:143], a[28:31], v[92:95], v[128:143]
v_exp_f32 v59, v183
v_add_f32 v60, v60, v55
v_cvt_pk_bf16_f32 v235, v54, v55
v_mfma_f32_32x32x16_bf16 v[160:175], a[28:31], v[124:127], v[160:175]
s_waitcnt lgkmcnt(7)
ds_read_b128 a[28:31], v62 offset:11776
v_exp_f32 v52, v152
v_add_f32 v61, v61, v59
v_cvt_pk_bf16_f32 v251, v58, v59
v_mfma_f32_32x32x16_bf16 a[64:79], a[32:35], v[224:227], a[64:79]
v_exp_f32 v56, v184
v_add_f32 v60, v60, v52
v_exp_f32 v53, v153
v_add_f32 v61, v61, v56
v_mfma_f32_32x32x16_bf16 a[176:191], a[32:35], v[240:243], a[176:191]
ds_read_b128 a[32:35], v63 offset:8192
v_add_u32 v62, s46, v32
v_exp_f32 v57, v185
v_add_f32 v60, v60, v53
v_cvt_pk_bf16_f32 v236, v52, v53
v_mfma_f32_32x32x16_bf16 a[80:95], a[36:39], v[224:227], a[80:95]
v_exp_f32 v54, v154
v_add_f32 v61, v61, v57
v_cvt_pk_bf16_f32 v252, v56, v57
v_mfma_f32_32x32x16_bf16 a[208:223], a[36:39], v[240:243], a[208:223]
ds_read_b128 a[36:39], v63 offset:9216
v_exp_f32 v58, v186
v_add_f32 v60, v60, v54
v_mfma_f32_32x32x16_bf16 a[112:127], a[40:43], v[224:227], a[112:127]
buffer_load_dwordx4 v7, s[8:11], s45 offen offset:2048 sc1 lds
v_exp_f32 v55, v155
v_add_f32 v61, v61, v58
v_mfma_f32_32x32x16_bf16 a[240:255], a[40:43], v[240:243], a[240:255]
ds_read_b128 a[40:43], v63 offset:10240
v_exp_f32 v59, v187
v_add_f32 v60, v60, v55
v_cvt_pk_bf16_f32 v237, v54, v55
v_mfma_f32_32x32x16_bf16 a[144:159], a[44:47], v[224:227], a[144:159]
v_exp_f32 v52, v156
v_add_f32 v61, v61, v59
v_cvt_pk_bf16_f32 v253, v58, v59
v_mfma_f32_32x32x16_bf16 a[96:111], a[44:47], v[240:243], a[96:111]
ds_read_b128 a[44:47], v63 offset:11264
v_exp_f32 v56, v188
v_add_f32 v60, v60, v52
v_mfma_f32_32x32x16_bf16 a[64:79], a[48:51], v[228:231], a[64:79]
v_exp_f32 v53, v157
v_add_f32 v61, v61, v56
v_mfma_f32_32x32x16_bf16 a[176:191], a[48:51], v[244:247], a[176:191]
ds_read_b128 a[48:51], v63 offset:12288
v_exp_f32 v57, v189
v_add_f32 v60, v60, v53
v_cvt_pk_bf16_f32 v238, v52, v53
v_mfma_f32_32x32x16_bf16 a[80:95], a[52:55], v[228:231], a[80:95]
v_exp_f32 v54, v158
v_add_f32 v61, v61, v57
v_cvt_pk_bf16_f32 v254, v56, v57
v_mfma_f32_32x32x16_bf16 a[208:223], a[52:55], v[244:247], a[208:223]
ds_read_b128 a[52:55], v63 offset:13312
v_exp_f32 v58, v190
v_add_f32 v60, v60, v54
v_mfma_f32_32x32x16_bf16 a[112:127], a[56:59], v[228:231], a[112:127]
buffer_load_dwordx4 v7, s[8:11], s45 offen offset:3072 sc1 lds
v_exp_f32 v55, v159
v_add_f32 v61, v61, v58
v_mfma_f32_32x32x16_bf16 a[240:255], a[56:59], v[244:247], a[240:255]
ds_read_b128 a[56:59], v63 offset:14336
v_exp_f32 v59, v191
v_add_f32 v60, v60, v55
v_cvt_pk_bf16_f32 v239, v54, v55
v_mfma_f32_32x32x16_bf16 a[144:159], a[60:63], v[228:231], a[144:159]
v_add_f32 v61, v61, v59
v_cvt_pk_bf16_f32 v255, v58, v59
v_mfma_f32_32x32x16_bf16 a[96:111], a[60:63], v[244:247], a[96:111]
s_waitcnt vmcnt(8) lgkmcnt(7)
s_barrier
ds_read_b128 a[60:63], v63 offset:15360
v_add_u32 v63, s49, v33
s_sub_u32 s57, s57, 1
s_cbranch_scc1 .Lh_t0a_c
.Lh_t0a:
v_mfma_f32_32x32x16_bf16 v[144:159], a[0:3], v[64:67], v[192:207]
s_mov_b32 m0, s50
ds_read_b128 a[0:3], v62 offset:0
v_exp_f32 v52, v128
v_exp_f32 v56, v160
v_mfma_f32_32x32x16_bf16 v[144:159], a[4:7], v[68:71], v[144:159]
s_add_u32 s45, s45, 16384
ds_read_b128 a[4:7], v62 offset:512
v_add_f32 v60, v60, v52
v_exp_f32 v53, v129
v_add_f32 v61, v61, v56
v_mfma_f32_32x32x16_bf16 v[144:159], a[8:11], v[72:75], v[144:159]
s_and_b32 s36, s86, 1
ds_read_b128 a[8:11], v62 offset:1024
v_exp_f32 v57, v161
v_add_f32 v60, v60, v53
v_cvt_pk_bf16_f32 v224, v52, v53
v_mfma_f32_32x32x16_bf16 v[144:159], a[12:15], v[76:79], v[144:159]
buffer_load_dwordx4 v7, s[4:7], s44 offen offset:0 sc1 lds
ds_read_b128 a[12:15], v62 offset:1536
v_exp_f32 v54, v130
v_add_f32 v61, v61, v57
v_mfma_f32_32x32x16_bf16 v[144:159], a[16:19], v[80:83], v[144:159]
s_xor_b32 s51, s49, s53
ds_read_b128 a[16:19], v62 offset:2048
v_cvt_pk_bf16_f32 v240, v56, v57
v_exp_f32 v58, v162
v_add_f32 v60, v60, v54
v_mfma_f32_32x32x16_bf16 v[144:159], a[20:23], v[84:87], v[144:159]
ds_read_b128 a[20:23], v62 offset:2560
v_exp_f32 v55, v131
v_add_f32 v61, v61, v58
v_exp_f32 v59, v163
v_mfma_f32_32x32x16_bf16 v[144:159], a[24:27], v[88:91], v[144:159]
s_cmp_lt_u32 s45, s54
s_cselect_b32 s51, s51, s27
s_lshl_b32 s36, s36, 3
ds_read_b128 a[24:27], v62 offset:3072
v_add_f32 v60, v60, v55
v_cvt_pk_bf16_f32 v225, v54, v55
v_mfma_f32_32x32x16_bf16 v[144:159], a[28:31], v[92:95], v[144:159]
s_waitcnt lgkmcnt(7)
s_add_u32 s46, s46, 16384
ds_read_b128 a[28:31], v62 offset:3584
v_exp_f32 v52, v132
v_add_f32 v61, v61, v59
v_mfma_f32_32x32x16_bf16 a[64:79], a[32:35], v[232:235], a[64:79]
v_cvt_pk_bf16_f32 v241, v58, v59
v_exp_f32 v56, v164
v_add_f32 v60, v60, v52
v_exp_f32 v53, v133
v_mfma_f32_32x32x16_bf16 a[176:191], a[32:35], v[248:251], a[176:191]
buffer_load_dwordx4 v7, s[4:7], s44 offen offset:1024 sc1 lds
ds_read_b128 a[32:35], v63 offset:0
v_add_f32 v61, v61, v56
v_exp_f32 v57, v165
v_mfma_f32_32x32x16_bf16 a[80:95], a[36:39], v[232:235], a[80:95]
v_add_f32 v60, v60, v53
v_cvt_pk_bf16_f32 v226, v52, v53
v_exp_f32 v54, v134
v_add_f32 v61, v61, v57
v_cvt_pk_bf16_f32 v242, v56, v57
v_mfma_f32_32x32x16_bf16 a[208:223], a[36:39], v[248:251], a[208:223]
ds_read_b128 a[36:39], v63 offset:1024
v_exp_f32 v58, v166
v_add_f32 v60, v60, v54
v_exp_f32 v55, v135
v_mfma_f32_32x32x16_bf16 a[112:127], a[40:43], v[232:235], a[112:127]
s_and_b32 s46, s46, 0xffff
s_and_b32 s28, s87, 7
v_add_f32 v61, v61, v58
v_exp_f32 v59, v167
v_add_f32 v60, v60, v55
v_cvt_pk_bf16_f32 v227, v54, v55
v_mfma_f32_32x32x16_bf16 a[240:255], a[40:43], v[248:251], a[240:255]
s_add_u32 s49, s49, 16384
ds_read_b128 a[40:43], v63 offset:2048
v_exp_f32 v52, v136
v_add_f32 v61, v61, v59
v_cvt_pk_bf16_f32 v243, v58, v59
v_mfma_f32_32x32x16_bf16 a[144:159], a[44:47], v[232:235], a[144:159]
v_exp_f32 v56, v168
v_add_f32 v60, v60, v52
v_exp_f32 v53, v137
v_add_f32 v61, v61, v56
v_mfma_f32_32x32x16_bf16 a[96:111], a[44:47], v[248:251], a[96:111]
buffer_load_dwordx4 v7, s[4:7], s44 offen offset:2048 sc1 lds
ds_read_b128 a[44:47], v63 offset:3072
v_exp_f32 v57, v169
v_add_f32 v60, v60, v53
v_mfma_f32_32x32x16_bf16 a[64:79], a[48:51], v[236:239], a[64:79]
s_and_b32 s49, s49, 0xffff
s_or_b32 s36, s36, s28
v_cvt_pk_bf16_f32 v228, v52, v53
v_exp_f32 v54, v138
v_add_f32 v61, v61, v57
v_cvt_pk_bf16_f32 v244, v56, v57
v_mfma_f32_32x32x16_bf16 a[176:191], a[48:51], v[252:255], a[176:191]
ds_read_b128 a[48:51], v63 offset:4096
v_exp_f32 v58, v170
v_add_f32 v60, v60, v54
v_exp_f32 v55, v139
v_mfma_f32_32x32x16_bf16 a[80:95], a[52:55], v[236:239], a[80:95]
s_lshr_b32 s37, s86, 1
s_lshl_b32 s37, s37, 2
v_add_f32 v61, v61, v58
v_exp_f32 v59, v171
v_add_f32 v60, v60, v55
v_cvt_pk_bf16_f32 v229, v54, v55
v_mfma_f32_32x32x16_bf16 a[208:223], a[52:55], v[252:255], a[208:223]
s_lshr_b32 s28, s87, 3
ds_read_b128 a[52:55], v63 offset:5120
v_exp_f32 v52, v140
v_add_f32 v61, v61, v59
v_cvt_pk_bf16_f32 v245, v58, v59
v_mfma_f32_32x32x16_bf16 a[112:127], a[56:59], v[236:239], a[112:127]
v_exp_f32 v56, v172
v_add_f32 v60, v60, v52
v_exp_f32 v53, v141
v_add_f32 v61, v61, v56
v_mfma_f32_32x32x16_bf16 a[240:255], a[56:59], v[252:255], a[240:255]
buffer_load_dwordx4 v7, s[4:7], s44 offen offset:3072 sc1 lds
ds_read_b128 a[56:59], v63 offset:6144
v_exp_f32 v57, v173
v_add_f32 v60, v60, v53
v_mfma_f32_32x32x16_bf16 a[144:159], a[60:63], v[236:239], a[144:159]
s_or_b32 s37, s37, s28
s_lshl_b32 s24, s36, 18
s_lshl_b32 s28, s3, 16
s_add_u32 s24, s24, s28
s_mov_b32 s25, 0
s_mul_i32 s26, s3, 98304
s_lshl_b32 s28, s37, 7
s_add_u32 s26, s26, s28
s_add_u32 s47, s26, 4096
s_mul_i32 s48, s3, 7168
s_mov_b32 s12, s64
s_and_b32 s13, s65, 0xffff
s_mov_b32 s14, 0x400000
s_mov_b32 s16, s66
s_and_b32 s17, s67, 0xffff
s_mov_b32 s18, 0x600000
s_lshl_b32 s29, s36, 18
s_lshl_b32 s28, s3, 13
s_add_u32 s29, s29, s28
s_sub_u32 s29, s29, 34816
s_cmp_eq_u32 s36, 0
s_cselect_b32 s29, 0x7f000000, s29
s_lshl_b32 s32, s3, 2
s_and_b32 s33, s3, 1
s_lshl_b32 s33, s33, 1
s_lshr_b32 s34, s3, 1
s_lshl_b32 s35, s34, 13
s_add_u32 s35, s35, 65536
s_mov_b32 s38, 131072
v_cvt_pk_bf16_f32 v230, v52, v53
v_exp_f32 v54, v142
v_add_f32 v61, v61, v57
v_cvt_pk_bf16_f32 v246, v56, v57
v_exp_f32 v58, v174
v_add_f32 v60, v60, v54
v_exp_f32 v55, v143
v_add_f32 v61, v61, v58
v_exp_f32 v59, v175
v_add_f32 v60, v60, v55
v_cvt_pk_bf16_f32 v231, v54, v55
v_add_f32 v61, v61, v59
v_cvt_pk_bf16_f32 v247, v58, v59
v_mfma_f32_32x32x16_bf16 a[96:111], a[60:63], v[252:255], a[96:111]
s_waitcnt lgkmcnt(7)
ds_read_b128 a[60:63], v63 offset:7168
s_sub_u32 s57, s57, 1
s_cbranch_scc1 .Lh_t0b_c
.Lh_t0b:
v_mfma_f32_32x32x16_bf16 v[128:143], a[0:3], v[64:67], v[192:207]
s_mov_b32 m0, s51
ds_read_b128 a[0:3], v62 offset:8192
v_exp_f32 v52, v144
v_lshrrev_b32 v56, 3, v1
v_and_b32 v57, 7, v1
v_mfma_f32_32x32x16_bf16 v[128:143], a[4:7], v[68:71], v[128:143]
s_add_u32 s44, s44, 16384
ds_read_b128 a[4:7], v62 offset:8704
v_exp_f32 v53, v145
v_add_f32 v60, v60, v52
v_xor_b32 v58, v57, v56
v_mfma_f32_32x32x16_bf16 v[128:143], a[8:11], v[72:75], v[128:143]
ds_read_b128 a[8:11], v62 offset:9216
v_exp_f32 v54, v146
v_add_f32 v60, v60, v53
v_cvt_pk_bf16_f32 v232, v52, v53
v_lshlrev_b32 v58, 4, v58
v_mfma_f32_32x32x16_bf16 v[128:143], a[12:15], v[76:79], v[128:143]
buffer_load_dwordx4 v7, s[8:11], s45 offen offset:0 sc1 lds
ds_read_b128 a[12:15], v62 offset:9728
v_lshl_add_u32 v36, v56, 11, v58
v_add_u32 v37, 15360, v36
v_add_u32 v38, 30720, v36
v_mfma_f32_32x32x16_bf16 v[128:143], a[16:19], v[80:83], v[128:143]
s_xor_b32 s50, s46, s52
ds_read_b128 a[16:19], v62 offset:10240
v_exp_f32 v55, v147
v_add_f32 v60, v60, v54
v_add_u32 v39, 46080, v36
v_mfma_f32_32x32x16_bf16 v[128:143], a[20:23], v[84:87], v[128:143]
ds_read_b128 a[20:23], v62 offset:10752
v_exp_f32 v52, v148
v_add_f32 v60, v60, v55
v_cvt_pk_bf16_f32 v233, v54, v55
v_add_u32 v58, s32, v56
v_mfma_f32_32x32x16_bf16 v[128:143], a[24:27], v[88:91], v[128:143]
s_cmp_lt_u32 s44, s54
s_cselect_b32 s50, s50, s27
ds_read_b128 a[24:27], v62 offset:11264
v_exp_f32 v53, v149
v_add_f32 v60, v60, v52
v_mfma_f32_32x32x16_bf16 v[128:143], a[28:31], v[92:95], v[128:143]
s_waitcnt lgkmcnt(7)
ds_read_b128 a[28:31], v62 offset:11776
v_xor_b32 v58, v58, v57
v_and_b32 v58, 7, v58
v_lshlrev_b32 v58, 4, v58
v_lshl_add_u32 v58, v56, 11, v58
v_mfma_f32_32x32x16_bf16 a[64:79], a[32:35], v[224:227], a[64:79]
v_exp_f32 v54, v150
v_add_f32 v60, v60, v53
v_cvt_pk_bf16_f32 v234, v52, v53
v_add_u32 v58, s29, v58
v_and_b32 v59, 32, v1
v_mfma_f32_32x32x16_bf16 a[176:191], a[32:35], v[240:243], a[176:191]
buffer_load_dwordx4 v7, s[8:11], s45 offen offset:1024 sc1 lds
ds_read_b128 a[32:35], v63 offset:8192
v_add_u32 v62, s46, v32
v_exp_f32 v55, v151
v_mfma_f32_32x32x16_bf16 a[80:95], a[36:39], v[224:227], a[80:95]
v_add_f32 v60, v60, v54
v_exp_f32 v52, v152
v_add_f32 v60, v60, v55
v_cvt_pk_bf16_f32 v235, v54, v55
v_lshlrev_b32 v59, 25, v59
v_mfma_f32_32x32x16_bf16 a[208:223], a[36:39], v[240:243], a[208:223]
ds_read_b128 a[36:39], v63 offset:9216
v_or_b32 v42, v59, v58
v_bfe_u32 v58, v1, 4, 1
v_lshlrev_b32 v58, 1, v58
v_xor_b32 v58, v58, v57
v_lshlrev_b32 v58, 4, v58
v_mfma_f32_32x32x16_bf16 a[112:127], a[40:43], v[224:227], a[112:127]
v_exp_f32 v53, v153
v_add_f32 v60, v60, v52
v_mul_u32_u24 v59, 6144, v56
v_add_u32 v40, v59, v58
v_xor_b32 v41, 64, v40
v_mfma_f32_32x32x16_bf16 a[240:255], a[40:43], v[240:243], a[240:255]
ds_read_b128 a[40:43], v63 offset:10240
v_exp_f32 v54, v154
v_add_f32 v60, v60, v53
v_cvt_pk_bf16_f32 v236, v52, v53
v_add_u32 v41, 48128, v41
v_mfma_f32_32x32x16_bf16 a[144:159], a[44:47], v[224:227], a[144:159]
v_exp_f32 v55, v155
v_add_f32 v60, v60, v54
v_and_b32 v56, 15, v1
v_lshrrev_b32 v57, 4, v1
v_and_b32 v58, 7, v56
v_mfma_f32_32x32x16_bf16 a[96:111], a[44:47], v[240:243], a[96:111]
buffer_load_dwordx4 v7, s[8:11], s45 offen offset:2048 sc1 lds
ds_read_b128 a[44:47], v63 offset:11264
v_xor_b32 v58, v58, v57
v_lshlrev_b32 v58, 4, v58
v_lshl_add_u32 v43, v56, 7, v58
v_mfma_f32_32x32x16_bf16 a[64:79], a[48:51], v[228:231], a[64:79]
v_exp_f32 v52, v156
v_add_f32 v60, v60, v55
v_cvt_pk_bf16_f32 v237, v54, v55
v_add_u32 v43, s35, v43
v_xor_b32 v44, 64, v43
v_mfma_f32_32x32x16_bf16 a[176:191], a[48:51], v[244:247], a[176:191]
ds_read_b128 a[48:51], v63 offset:12288
v_exp_f32 v53, v157
v_add_f32 v60, v60, v52
v_and_b32 v56, 15, v1
v_lshrrev_b32 v57, 2, v56
v_mfma_f32_32x32x16_bf16 a[80:95], a[52:55], v[228:231], a[80:95]
v_exp_f32 v54, v158
v_add_f32 v60, v60, v53
v_cvt_pk_bf16_f32 v238, v52, v53
v_mul_u32_u24 v57, 6656, v57
v_add_u32 v57, 71680, v57
v_mfma_f32_32x32x16_bf16 a[208:223], a[52:55], v[244:247], a[208:223]
ds_read_b128 a[52:55], v63 offset:13312
v_mov_b32 v58, -2048
v_mov_b32 v59, s34
v_cmp_eq_u32 vcc, 0, v59
v_nop
v_nop
v_mfma_f32_32x32x16_bf16 a[112:127], a[56:59], v[228:231], a[112:127]
v_exp_f32 v55, v159
v_add_f32 v60, v60, v54
v_cndmask_b32 v57, v58, v57, vcc
v_add_u32 v45, v57, v43
v_add_u32 v46, v57, v44
v_mfma_f32_32x32x16_bf16 a[240:255], a[56:59], v[244:247], a[240:255]
buffer_load_dwordx4 v7, s[8:11], s45 offen offset:3072 sc1 lds
ds_read_b128 a[56:59], v63 offset:14336
v_add_f32 v60, v60, v55
v_cvt_pk_bf16_f32 v239, v54, v55
v_bfe_u32 v56, v1, 2, 2
v_mfma_f32_32x32x16_bf16 a[144:159], a[60:63], v[228:231], a[144:159]
v_bfe_u32 v57, v1, 4, 1
v_lshl_add_u32 v58, v57, 3, v56
v_lshlrev_b32 v58, 7, v58
v_lshrrev_b32 v59, 5, v1
v_mul_u32_u24 v59, 7168, v59
v_add3_u32 v58, v58, v59, s38
v_lshrrev_b32 v56, 1, v56
v_lshl_or_b32 v56, v57, 1, v56
v_xor_b32 v56, s33, v56
v_lshl_add_u32 v58, v56, 5, v58
v_and_b32 v56, 3, v1
v_lshl_add_u32 v47, v56, 3, v58
v_xor_b32 v48, 32, v47
v_lshrrev_b32 v57, 1, v56
v_and_b32 v56, 1, v56
v_lshl_or_b32 v56, v56, 1, v57
v_lshl_add_u32 v49, v56, 3, v58
v_xor_b32 v50, 32, v49
v_mfma_f32_32x32x16_bf16 a[96:111], a[60:63], v[244:247], a[96:111]
s_waitcnt vmcnt(8) lgkmcnt(7)
s_barrier
ds_read_b128 a[60:63], v63 offset:15360
v_add_u32 v63, s49, v33
s_sub_u32 s57, s57, 1
s_cbranch_scc0 .LS1
s_swappc_b64 s[30:31], s[58:59]
.Lret_t0:
s_branch .LS1
.LT1x:
s_sub_u32 s57, s57, 1
s_cbranch_scc0 .LT1
s_swappc_b64 s[30:31], s[58:59]
.LT1:
v_mfma_f32_32x32x16_bf16 v[144:159], a[0:3], v[64:67], v[192:207]
s_mov_b32 m0, s50
v_exp_f32 v52, v128
v_exp_f32 v56, v160
v_add_f32 v60, v60, v52
v_mfma_f32_32x32x16_bf16 v[176:191], a[0:3], v[96:99], v[36:51]
ds_read_b128 a[0:3], v62 offset:0
v_exp_f32 v53, v129
v_add_f32 v61, v61, v56
v_mfma_f32_32x32x16_bf16 v[144:159], a[4:7], v[68:71], v[144:159]
s_add_u32 s45, s45, 16384
v_exp_f32 v57, v161
v_add_f32 v60, v60, v53
v_cvt_pk_bf16_f32 v224, v52, v53
v_mfma_f32_32x32x16_bf16 v[176:191], a[4:7], v[100:103], v[176:191]
ds_read_b128 a[4:7], v62 offset:512
v_exp_f32 v54, v130
v_add_f32 v61, v61, v57
v_cvt_pk_bf16_f32 v240, v56, v57
v_mfma_f32_32x32x16_bf16 v[144:159], a[8:11], v[72:75], v[144:159]
buffer_load_dwordx4 v7, s[4:7], s44 offen offset:0 sc1 lds
v_exp_f32 v58, v162
v_add_f32 v60, v60, v54
v_mfma_f32_32x32x16_bf16 v[176:191], a[8:11], v[104:107], v[176:191]
ds_read_b128 a[8:11], v62 offset:1024
v_exp_f32 v55, v131
v_add_f32 v61, v61, v58
v_mfma_f32_32x32x16_bf16 v[144:159], a[12:15], v[76:79], v[144:159]
s_xor_b32 s51, s49, s53
v_exp_f32 v59, v163
v_add_f32 v60, v60, v55
v_cvt_pk_bf16_f32 v225, v54, v55
v_mfma_f32_32x32x16_bf16 v[176:191], a[12:15], v[108:111], v[176:191]
ds_read_b128 a[12:15], v62 offset:1536
v_exp_f32 v52, v132
v_add_f32 v61, v61, v59
v_cvt_pk_bf16_f32 v241, v58, v59
v_mfma_f32_32x32x16_bf16 v[144:159], a[16:19], v[80:83], v[144:159]
s_cmp_lt_u32 s45, s54
s_cselect_b32 s51, s51, s27
v_exp_f32 v56, v164
v_add_f32 v60, v60, v52
v_mfma_f32_32x32x16_bf16 v[176:191], a[16:19], v[112:115], v[176:191]
ds_read_b128 a[16:19], v62 offset:2048
v_exp_f32 v53, v133
v_add_f32 v61, v61, v56
v_mfma_f32_32x32x16_bf16 v[144:159], a[20:23], v[84:87], v[144:159]
s_add_u32 s46, s46, 16384
v_exp_f32 v57, v165
v_add_f32 v60, v60, v53
v_cvt_pk_bf16_f32 v226, v52, v53
v_mfma_f32_32x32x16_bf16 v[176:191], a[20:23], v[116:119], v[176:191]
ds_read_b128 a[20:23], v62 offset:2560
v_exp_f32 v54, v134
v_add_f32 v61, v61, v57
v_cvt_pk_bf16_f32 v242, v56, v57
v_mfma_f32_32x32x16_bf16 v[144:159], a[24:27], v[88:91], v[144:159]
buffer_load_dwordx4 v7, s[4:7], s44 offen offset:1024 sc1 lds
v_exp_f32 v58, v166
v_add_f32 v60, v60, v54
v_mfma_f32_32x32x16_bf16 v[176:191], a[24:27], v[120:123], v[176:191]
ds_read_b128 a[24:27], v62 offset:3072
v_exp_f32 v55, v135
v_add_f32 v61, v61, v58
v_mfma_f32_32x32x16_bf16 v[144:159], a[28:31], v[92:95], v[144:159]
v_exp_f32 v59, v167
v_add_f32 v60, v60, v55
v_cvt_pk_bf16_f32 v227, v54, v55
v_mfma_f32_32x32x16_bf16 v[176:191], a[28:31], v[124:127], v[176:191]
s_waitcnt lgkmcnt(7)
ds_read_b128 a[28:31], v62 offset:3584
v_exp_f32 v52, v136
v_add_f32 v61, v61, v59
v_cvt_pk_bf16_f32 v243, v58, v59
v_mfma_f32_32x32x16_bf16 a[64:79], a[32:35], v[232:235], a[64:79]
s_and_b32 s46, s46, 0xffff
v_exp_f32 v56, v168
v_add_f32 v60, v60, v52
v_exp_f32 v53, v137
v_add_f32 v61, v61, v56
v_mfma_f32_32x32x16_bf16 a[176:191], a[32:35], v[248:251], a[176:191]
ds_read_b128 a[32:35], v63 offset:0
v_exp_f32 v57, v169
v_add_f32 v60, v60, v53
v_cvt_pk_bf16_f32 v228, v52, v53
v_mfma_f32_32x32x16_bf16 a[80:95], a[36:39], v[232:235], a[80:95]
s_add_u32 s49, s49, 16384
v_exp_f32 v54, v138
v_add_f32 v61, v61, v57
v_cvt_pk_bf16_f32 v244, v56, v57
v_mfma_f32_32x32x16_bf16 a[208:223], a[36:39], v[248:251], a[208:223]
ds_read_b128 a[36:39], v63 offset:1024
v_exp_f32 v58, v170
v_add_f32 v60, v60, v54
v_mfma_f32_32x32x16_bf16 a[112:127], a[40:43], v[232:235], a[112:127]
buffer_load_dwordx4 v7, s[4:7], s44 offen offset:2048 sc1 lds
v_exp_f32 v55, v139
v_add_f32 v61, v61, v58
v_mfma_f32_32x32x16_bf16 a[240:255], a[40:43], v[248:251], a[240:255]
ds_read_b128 a[40:43], v63 offset:2048
v_exp_f32 v59, v171
v_add_f32 v60, v60, v55
v_cvt_pk_bf16_f32 v229, v54, v55
v_mfma_f32_32x32x16_bf16 a[144:159], a[44:47], v[232:235], a[144:159]
s_and_b32 s49, s49, 0xffff
v_exp_f32 v52, v140
v_add_f32 v61, v61, v59
v_cvt_pk_bf16_f32 v245, v58, v59
v_mfma_f32_32x32x16_bf16 a[96:111], a[44:47], v[248:251], a[96:111]
ds_read_b128 a[44:47], v63 offset:3072
v_exp_f32 v56, v172
v_add_f32 v60, v60, v52
v_mfma_f32_32x32x16_bf16 a[64:79], a[48:51], v[236:239], a[64:79]
v_exp_f32 v53, v141
v_add_f32 v61, v61, v56
v_mfma_f32_32x32x16_bf16 a[176:191], a[48:51], v[252:255], a[176:191]
ds_read_b128 a[48:51], v63 offset:4096
v_exp_f32 v57, v173
v_add_f32 v60, v60, v53
v_cvt_pk_bf16_f32 v230, v52, v53
v_mfma_f32_32x32x16_bf16 a[80:95], a[52:55], v[236:239], a[80:95]
v_exp_f32 v54, v142
v_add_f32 v61, v61, v57
v_cvt_pk_bf16_f32 v246, v56, v57
v_mfma_f32_32x32x16_bf16 a[208:223], a[52:55], v[252:255], a[208:223]
ds_read_b128 a[52:55], v63 offset:5120
v_exp_f32 v58, v174
v_add_f32 v60, v60, v54
v_mfma_f32_32x32x16_bf16 a[112:127], a[56:59], v[236:239], a[112:127]
buffer_load_dwordx4 v7, s[4:7], s44 offen offset:3072 sc1 lds
v_exp_f32 v55, v143
v_add_f32 v61, v61, v58
v_mfma_f32_32x32x16_bf16 a[240:255], a[56:59], v[252:255], a[240:255]
ds_read_b128 a[56:59], v63 offset:6144
v_exp_f32 v59, v175
v_add_f32 v60, v60, v55
v_cvt_pk_bf16_f32 v231, v54, v55
v_mfma_f32_32x32x16_bf16 a[144:159], a[60:63], v[236:239], a[144:159]
v_add_f32 v61, v61, v59
v_cvt_pk_bf16_f32 v247, v58, v59
v_mfma_f32_32x32x16_bf16 a[96:111], a[60:63], v[252:255], a[96:111]
s_waitcnt lgkmcnt(7)
ds_read_b128 a[60:63], v63 offset:7168
s_sub_u32 s57, s57, 1
s_cbranch_scc1 .Lh_t1a_c
.Lh_t1a:
v_mfma_f32_32x32x16_bf16 v[128:143], a[0:3], v[64:67], v[192:207]
s_mov_b32 m0, s51
ds_read_b128 a[0:3], v62 offset:8192
v_exp_f32 v52, v144
v_exp_f32 v56, v176
v_mfma_f32_32x32x16_bf16 v[128:143], a[4:7], v[68:71], v[128:143]
s_add_u32 s44, s44, 16384
ds_read_b128 a[4:7], v62 offset:8704
v_add_f32 v60, v60, v52
v_exp_f32 v53, v145
v_add_f32 v61, v61, v56
v_mfma_f32_32x32x16_bf16 v[128:143], a[8:11], v[72:75], v[128:143]
s_and_b32 s36, s86, 1
ds_read_b128 a[8:11], v62 offset:9216
v_exp_f32 v57, v177
v_add_f32 v60, v60, v53
v_cvt_pk_bf16_f32 v232, v52, v53
v_mfma_f32_32x32x16_bf16 v[128:143], a[12:15], v[76:79], v[128:143]
buffer_load_dwordx4 v7, s[8:11], s45 offen offset:0 sc1 lds
ds_read_b128 a[12:15], v62 offset:9728
v_exp_f32 v54, v146
v_add_f32 v61, v61, v57
v_mfma_f32_32x32x16_bf16 v[128:143], a[16:19], v[80:83], v[128:143]
s_xor_b32 s50, s46, s52
ds_read_b128 a[16:19], v62 offset:10240
v_cvt_pk_bf16_f32 v248, v56, v57
v_exp_f32 v58, v178
v_add_f32 v60, v60, v54
v_mfma_f32_32x32x16_bf16 v[128:143], a[20:23], v[84:87], v[128:143]
ds_read_b128 a[20:23], v62 offset:10752
v_exp_f32 v55, v147
v_add_f32 v61, v61, v58
v_exp_f32 v59, v179
v_mfma_f32_32x32x16_bf16 v[128:143], a[24:27], v[88:91], v[128:143]
s_cmp_lt_u32 s44, s54
s_cselect_b32 s50, s50, s27
s_lshl_b32 s36, s36, 3
ds_read_b128 a[24:27], v62 offset:11264
v_add_f32 v60, v60, v55
v_cvt_pk_bf16_f32 v233, v54, v55
v_mfma_f32_32x32x16_bf16 v[128:143], a[28:31], v[92:95], v[128:143]
s_waitcnt lgkmcnt(7)
ds_read_b128 a[28:31], v62 offset:11776
v_exp_f32 v52, v148
v_add_f32 v61, v61, v59
v_cvt_pk_bf16_f32 v249, v58, v59
v_mfma_f32_32x32x16_bf16 a[64:79], a[32:35], v[224:227], a[64:79]
v_exp_f32 v56, v180
v_add_f32 v60, v60, v52
v_exp_f32 v53, v149
v_add_f32 v61, v61, v56
v_mfma_f32_32x32x16_bf16 a[176:191], a[32:35], v[240:243], a[176:191]
buffer_load_dwordx4 v7, s[8:11], s45 offen offset:1024 sc1 lds
ds_read_b128 a[32:35], v63 offset:8192
v_add_u32 v62, s46, v32
v_exp_f32 v57, v181
v_mfma_f32_32x32x16_bf16 a[80:95], a[36:39], v[224:227], a[80:95]
v_add_f32 v60, v60, v53
v_cvt_pk_bf16_f32 v234, v52, v53
v_exp_f32 v54, v150
v_add_f32 v61, v61, v57
v_cvt_pk_bf16_f32 v250, v56, v57
v_mfma_f32_32x32x16_bf16 a[208:223], a[36:39], v[240:243], a[208:223]
ds_read_b128 a[36:39], v63 offset:9216
v_exp_f32 v58, v182
v_add_f32 v60, v60, v54
v_exp_f32 v55, v151
v_mfma_f32_32x32x16_bf16 a[112:127], a[40:43], v[224:227], a[112:127]
s_and_b32 s28, s87, 7
s_or_b32 s36, s36, s28
v_add_f32 v61, v61, v58
v_exp_f32 v59, v183
v_add_f32 v60, v60, v55
v_cvt_pk_bf16_f32 v235, v54, v55
v_mfma_f32_32x32x16_bf16 a[240:255], a[40:43], v[240:243], a[240:255]
s_lshr_b32 s37, s86, 1
ds_read_b128 a[40:43], v63 offset:10240
v_exp_f32 v52, v152
v_add_f32 v61, v61, v59
v_cvt_pk_bf16_f32 v251, v58, v59
v_mfma_f32_32x32x16_bf16 a[144:159], a[44:47], v[224:227], a[144:159]
v_exp_f32 v56, v184
v_add_f32 v60, v60, v52
v_exp_f32 v53, v153
v_add_f32 v61, v61, v56
v_mfma_f32_32x32x16_bf16 a[96:111], a[44:47], v[240:243], a[96:111]
buffer_load_dwordx4 v7, s[8:11], s45 offen offset:2048 sc1 lds
ds_read_b128 a[44:47], v63 offset:11264
v_exp_f32 v57, v185
v_add_f32 v60, v60, v53
v_mfma_f32_32x32x16_bf16 a[64:79], a[48:51], v[228:231], a[64:79]
s_lshl_b32 s37, s37, 2
s_lshr_b32 s28, s87, 3
v_cvt_pk_bf16_f32 v236, v52, v53
v_exp_f32 v54, v154
v_add_f32 v61, v61, v57
v_cvt_pk_bf16_f32 v252, v56, v57
v_mfma_f32_32x32x16_bf16 a[176:191], a[48:51], v[244:247], a[176:191]
ds_read_b128 a[48:51], v63 offset:12288
v_exp_f32 v58, v186
v_add_f32 v60, v60, v54
v_exp_f32 v55, v155
v_mfma_f32_32x32x16_bf16 a[80:95], a[52:55], v[228:231], a[80:95]
s_or_b32 s37, s37, s28
s_lshl_b32 s24, s36, 18
v_add_f32 v61, v61, v58
v_exp_f32 v59, v187
v_add_f32 v60, v60, v55
v_cvt_pk_bf16_f32 v237, v54, v55
v_mfma_f32_32x32x16_bf16 a[208:223], a[52:55], v[244:247], a[208:223]
s_lshl_b32 s28, s3, 16
ds_read_b128 a[52:55], v63 offset:13312
v_exp_f32 v52, v156
v_add_f32 v61, v61, v59
v_cvt_pk_bf16_f32 v253, v58, v59
v_mfma_f32_32x32x16_bf16 a[112:127], a[56:59], v[228:231], a[112:127]
v_exp_f32 v56, v188
v_add_f32 v60, v60, v52
v_exp_f32 v53, v157
v_add_f32 v61, v61, v56
v_mfma_f32_32x32x16_bf16 a[240:255], a[56:59], v[244:247], a[240:255]
buffer_load_dwordx4 v7, s[8:11], s45 offen offset:3072 sc1 lds
ds_read_b128 a[56:59], v63 offset:14336
v_exp_f32 v57, v189
v_add_f32 v60, v60, v53
v_mfma_f32_32x32x16_bf16 a[144:159], a[60:63], v[228:231], a[144:159]
s_add_u32 s24, s24, s28
s_mov_b32 s25, 0
s_mul_i32 s26, s3, 98304
s_lshl_b32 s28, s37, 7
s_add_u32 s26, s26, s28
s_add_u32 s47, s26, 4096
s_mul_i32 s48, s3, 7168
s_mov_b32 s12, s64
s_and_b32 s13, s65, 0xffff
s_mov_b32 s14, 0x400000
s_mov_b32 s16, s66
s_and_b32 s17, s67, 0xffff
s_mov_b32 s18, 0x600000
s_lshl_b32 s29, s36, 18
s_lshl_b32 s28, s3, 13
s_add_u32 s29, s29, s28
s_sub_u32 s29, s29, 34816
s_cmp_eq_u32 s36, 0
s_cselect_b32 s29, 0x7f000000, s29
s_lshl_b32 s32, s3, 2
s_and_b32 s33, s3, 1
s_lshl_b32 s33, s33, 1
s_lshr_b32 s34, s3, 1
s_lshl_b32 s35, s34, 13
s_add_u32 s35, s35, 65536
s_mov_b32 s38, 131072
v_cvt_pk_bf16_f32 v238, v52, v53
v_exp_f32 v54, v158
v_add_f32 v61, v61, v57
v_cvt_pk_bf16_f32 v254, v56, v57
v_exp_f32 v58, v190
v_add_f32 v60, v60, v54
v_exp_f32 v55, v159
v_add_f32 v61, v61, v58
v_exp_f32 v59, v191
v_add_f32 v60, v60, v55
v_cvt_pk_bf16_f32 v239, v54, v55
v_add_f32 v61, v61, v59
v_cvt_pk_bf16_f32 v255, v58, v59
v_mfma_f32_32x32x16_bf16 a[96:111], a[60:63], v[244:247], a[96:111]
s_waitcnt vmcnt(8) lgkmcnt(7)
s_barrier
ds_read_b128 a[60:63], v63 offset:15360
v_add_u32 v63, s49, v33
s_sub_u32 s57, s57, 1
s_cbranch_scc1 .Lh_t1b_c
.Lh_t1b:
v_mfma_f32_32x32x16_bf16 v[144:159], a[0:3], v[64:67], v[192:207]
s_mov_b32 m0, s50
ds_read_b128 a[0:3], v62 offset:0
v_exp_f32 v52, v128
v_lshrrev_b32 v56, 3, v1
v_and_b32 v57, 7, v1
v_mfma_f32_32x32x16_bf16 v[144:159], a[4:7], v[68:71], v[144:159]
s_add_u32 s45, s45, 16384
ds_read_b128 a[4:7], v62 offset:512
v_exp_f32 v53, v129
v_add_f32 v60, v60, v52
v_xor_b32 v58, v57, v56
v_mfma_f32_32x32x16_bf16 v[144:159], a[8:11], v[72:75], v[144:159]
ds_read_b128 a[8:11], v62 offset:1024
v_exp_f32 v54, v130
v_add_f32 v60, v60, v53
v_cvt_pk_bf16_f32 v224, v52, v53
v_lshlrev_b32 v58, 4, v58
v_mfma_f32_32x32x16_bf16 v[144:159], a[12:15], v[76:79], v[144:159]
buffer_load_dwordx4 v7, s[4:7], s44 offen offset:0 sc1 lds
ds_read_b128 a[12:15], v62 offset:1536
v_lshl_add_u32 v36, v56, 11, v58
v_add_u32 v37, 15360, v36
v_add_u32 v38, 30720, v36
v_mfma_f32_32x32x16_bf16 v[144:159], a[16:19], v[80:83], v[144:159]
s_xor_b32 s51, s49, s53
ds_read_b128 a[16:19], v62 offset:2048
v_exp_f32 v55, v131
v_add_f32 v60, v60, v54
v_add_u32 v39, 46080, v36
v_mfma_f32_32x32x16_bf16 v[144:159], a[20:23], v[84:87], v[144:159]
ds_read_b128 a[20:23], v62 offset:2560
v_exp_f32 v52, v132
v_add_f32 v60, v60, v55
v_cvt_pk_bf16_f32 v225, v54, v55
v_add_u32 v58, s32, v56
v_mfma_f32_32x32x16_bf16 v[144:159], a[24:27], v[88:91], v[144:159]
s_cmp_lt_u32 s45, s54
s_cselect_b32 s51, s51, s27
ds_read_b128 a[24:27], v62 offset:3072
v_exp_f32 v53, v133
v_add_f32 v60, v60, v52
v_mfma_f32_32x32x16_bf16 v[144:159], a[28:31], v[92:95], v[144:159]
s_waitcnt lgkmcnt(7)
s_add_u32 s46, s46, 16384
ds_read_b128 a[28:31], v62 offset:3584
v_xor_b32 v58, v58, v57
v_and_b32 v58, 7, v58
v_lshlrev_b32 v58, 4, v58
v_mfma_f32_32x32x16_bf16 a[64:79], a[32:35], v[232:235], a[64:79]
v_exp_f32 v54, v134
v_add_f32 v60, v60, v53
v_cvt_pk_bf16_f32 v226, v52, v53
v_lshl_add_u32 v58, v56, 11, v58
v_add_u32 v58, s29, v58
v_mfma_f32_32x32x16_bf16 a[176:191], a[32:35], v[248:251], a[176:191]
buffer_load_dwordx4 v7, s[4:7], s44 offen offset:1024 sc1 lds
ds_read_b128 a[32:35], v63 offset:0
v_exp_f32 v55, v135
v_add_f32 v60, v60, v54
v_mfma_f32_32x32x16_bf16 a[80:95], a[36:39], v[232:235], a[80:95]
v_exp_f32 v52, v136
v_add_f32 v60, v60, v55
v_cvt_pk_bf16_f32 v227, v54, v55
v_and_b32 v59, 32, v1
v_lshlrev_b32 v59, 25, v59
v_mfma_f32_32x32x16_bf16 a[208:223], a[36:39], v[248:251], a[208:223]
ds_read_b128 a[36:39], v63 offset:1024
v_or_b32 v42, v59, v58
v_bfe_u32 v58, v1, 4, 1
v_lshlrev_b32 v58, 1, v58
v_xor_b32 v58, v58, v57
v_lshlrev_b32 v58, 4, v58
v_mfma_f32_32x32x16_bf16 a[112:127], a[40:43], v[232:235], a[112:127]
s_and_b32 s46, s46, 0xffff
v_exp_f32 v53, v137
v_add_f32 v60, v60, v52
v_mul_u32_u24 v59, 6144, v56
v_add_u32 v40, v59, v58
v_xor_b32 v41, 64, v40
v_mfma_f32_32x32x16_bf16 a[240:255], a[40:43], v[248:251], a[240:255]
s_add_u32 s49, s49, 16384
ds_read_b128 a[40:43], v63 offset:2048
v_exp_f32 v54, v138
v_add_f32 v60, v60, v53
v_cvt_pk_bf16_f32 v228, v52, v53
v_mfma_f32_32x32x16_bf16 a[144:159], a[44:47], v[232:235], a[144:159]
v_exp_f32 v55, v139
v_add_f32 v60, v60, v54
v_add_u32 v41, 48128, v41
v_and_b32 v56, 15, v1
v_lshrrev_b32 v57, 4, v1
v_mfma_f32_32x32x16_bf16 a[96:111], a[44:47], v[248:251], a[96:111]
buffer_load_dwordx4 v7, s[4:7], s44 offen offset:2048 sc1 lds
ds_read_b128 a[44:47], v63 offset:3072
v_and_b32 v58, 7, v56
v_xor_b32 v58, v58, v57
v_lshlrev_b32 v58, 4, v58
v_mfma_f32_32x32x16_bf16 a[64:79], a[48:51], v[236:239], a[64:79]
s_and_b32 s49, s49, 0xffff
v_exp_f32 v52, v140
v_add_f32 v60, v60, v55
v_cvt_pk_bf16_f32 v229, v54, v55
v_lshl_add_u32 v43, v56, 7, v58
v_add_u32 v43, s35, v43
v_mfma_f32_32x32x16_bf16 a[176:191], a[48:51], v[252:255], a[176:191]
ds_read_b128 a[48:51], v63 offset:4096
v_exp_f32 v53, v141
v_add_f32 v60, v60, v52
v_xor_b32 v44, 64, v43
v_and_b32 v56, 15, v1
v_mfma_f32_32x32x16_bf16 a[80:95], a[52:55], v[236:239], a[80:95]
v_exp_f32 v54, v142
v_add_f32 v60, v60, v53
v_cvt_pk_bf16_f32 v230, v52, v53
v_lshrrev_b32 v57, 2, v56
v_mul_u32_u24 v57, 6656, v57
v_mfma_f32_32x32x16_bf16 a[208:223], a[52:55], v[252:255], a[208:223]
ds_read_b128 a[52:55], v63 offset:5120
v_add_u32 v57, 71680, v57
v_mov_b32 v58, -2048
v_mov_b32 v59, s34
v_cmp_eq_u32 vcc, 0, v59
v_nop
v_mfma_f32_32x32x16_bf16 a[112:127], a[56:59], v[236:239], a[112:127]
v_exp_f32 v55, v143
v_add_f32 v60, v60, v54
v_nop
v_cndmask_b32 v57, v58, v57, vcc
v_add_u32 v45, v57, v43
v_mfma_f32_32x32x16_bf16 a[240:255], a[56:59], v[252:255], a[240:255]
buffer_load_dwordx4 v7, s[4:7], s44 offen offset:3072 sc1 lds
ds_read_b128 a[56:59], v63 offset:6144
v_add_f32 v60, v60, v55
v_cvt_pk_bf16_f32 v231, v54, v55
v_add_u32 v46, v57, v44
v_mfma_f32_32x32x16_bf16 a[144:159], a[60:63], v[236:239], a[144:159]
v_bfe_u32 v56, v1, 2, 2
v_bfe_u32 v57, v1, 4, 1
v_lshl_add_u32 v58, v57, 3, v56
v_lshlrev_b32 v58, 7, v58
v_lshrrev_b32 v59, 5, v1
v_mul_u32_u24 v59, 7168, v59
v_add3_u32 v58, v58, v59, s38
v_lshrrev_b32 v56, 1, v56
v_lshl_or_b32 v56, v57, 1, v56
v_xor_b32 v56, s33, v56
v_lshl_add_u32 v58, v56, 5, v58
v_and_b32 v56, 3, v1
v_lshl_add_u32 v47, v56, 3, v58
v_xor_b32 v48, 32, v47
v_lshrrev_b32 v57, 1, v56
v_and_b32 v56, 1, v56
v_lshl_or_b32 v56, v56, 1, v57
v_lshl_add_u32 v49, v56, 3, v58
v_xor_b32 v50, 32, v49
v_mfma_f32_32x32x16_bf16 a[96:111], a[60:63], v[252:255], a[96:111]
s_waitcnt lgkmcnt(7)
ds_read_b128 a[60:63], v63 offset:7168
s_sub_u32 s57, s57, 1
s_cbranch_scc0 .LS0
s_swappc_b64 s[30:31], s[58:59]
.Lret_t1:
s_branch .LS0
.LS1:
v_mfma_f32_32x32x16_bf16 v[144:159], a[0:3], v[64:67], v[192:207]
s_mov_b32 m0, s50
ds_read_b128 a[0:3], v62 offset:0
v_exp_f32 v52, v128
v_exp_f32 v53, v129
v_mfma_f32_32x32x16_bf16 v[144:159], a[4:7], v[68:71], v[144:159]
s_add_u32 s45, s45, 16384
ds_read_b128 a[4:7], v62 offset:512
v_add_f32 v60, v60, v52
v_exp_f32 v54, v130
v_add_f32 v60, v60, v53
v_mfma_f32_32x32x16_bf16 v[144:159], a[8:11], v[72:75], v[144:159]
buffer_load_dwordx4 v7, s[4:7], s44 offen offset:0 sc1 lds
ds_read_b128 a[8:11], v62 offset:1024
v_cvt_pk_bf16_f32 v224, v52, v53
v_exp_f32 v55, v131
v_mfma_f32_32x32x16_bf16 v[144:159], a[12:15], v[76:79], v[144:159]
s_xor_b32 s51, s49, s53
ds_read_b128 a[12:15], v62 offset:1536
v_add_f32 v60, v60, v54
v_exp_f32 v52, v132
v_add_f32 v60, v60, v55
v_mfma_f32_32x32x16_bf16 v[144:159], a[16:19], v[80:83], v[144:159]
s_cmp_lt_u32 s45, s54
s_cselect_b32 s51, s51, s27
ds_read_b128 a[16:19], v62 offset:2048
v_cvt_pk_bf16_f32 v225, v54, v55
v_exp_f32 v53, v133
v_mfma_f32_32x32x16_bf16 v[144:159], a[20:23], v[84:87], v[144:159]
s_add_u32 s46, s46, 16384
ds_read_b128 a[20:23], v62 offset:2560
v_add_f32 v60, v60, v52
v_exp_f32 v54, v134
v_add_f32 v60, v60, v53
v_mfma_f32_32x32x16_bf16 v[144:159], a[24:27], v[88:91], v[144:159]
buffer_load_dwordx4 v7, s[4:7], s44 offen offset:1024 sc1 lds
ds_read_b128 a[24:27], v62 offset:3072
v_cvt_pk_bf16_f32 v226, v52, v53
v_exp_f32 v55, v135
v_mfma_f32_32x32x16_bf16 v[144:159], a[28:31], v[92:95], v[144:159]
s_waitcnt lgkmcnt(7)
ds_read_b128 a[28:31], v62 offset:3584
v_add_f32 v60, v60, v54
v_exp_f32 v52, v136
v_add_f32 v60, v60, v55
v_mfma_f32_32x32x16_bf16 a[64:79], a[32:35], v[232:235], a[64:79]
s_and_b32 s46, s46, 0xffff
ds_read_b128 a[32:35], v63 offset:0
v_cvt_pk_bf16_f32 v227, v54, v55
v_exp_f32 v53, v137
v_add_f32 v60, v60, v52
v_mfma_f32_32x32x16_bf16 a[80:95], a[36:39], v[232:235], a[80:95]
s_add_u32 s49, s49, 16384
ds_read_b128 a[36:39], v63 offset:1024
v_exp_f32 v54, v138
v_add_f32 v60, v60, v53
v_cvt_pk_bf16_f32 v228, v52, v53
v_mfma_f32_32x32x16_bf16 a[112:127], a[40:43], v[232:235], a[112:127]
buffer_load_dwordx4 v7, s[4:7], s44 offen offset:2048 sc1 lds
ds_read_b128 a[40:43], v63 offset:2048
v_exp_f32 v55, v139
v_add_f32 v60, v60, v54
v_mfma_f32_32x32x16_bf16 a[144:159], a[44:47], v[232:235], a[144:159]
s_and_b32 s49, s49, 0xffff
ds_read_b128 a[44:47], v63 offset:3072
v_exp_f32 v52, v140
v_add_f32 v60, v60, v55
v_cvt_pk_bf16_f32 v229, v54, v55
v_mfma_f32_32x32x16_bf16 a[64:79], a[48:51], v[236:239], a[64:79]
ds_read_b128 a[48:51], v63 offset:4096
v_exp_f32 v53, v141
v_add_f32 v60, v60, v52
v_exp_f32 v54, v142
v_mfma_f32_32x32x16_bf16 a[80:95], a[52:55], v[236:239], a[80:95]
s_sub_u32 s56, s56, 1
ds_read_b128 a[52:55], v63 offset:5120
v_add_f32 v60, v60, v53
v_cvt_pk_bf16_f32 v230, v52, v53
v_exp_f32 v55, v143
v_mfma_f32_32x32x16_bf16 a[112:127], a[56:59], v[236:239], a[112:127]
buffer_load_dwordx4 v7, s[4:7], s44 offen offset:3072 sc1 lds
ds_read_b128 a[56:59], v63 offset:6144
v_add_f32 v60, v60, v54
v_add_f32 v60, v60, v55
v_cvt_pk_bf16_f32 v231, v54, v55
v_mfma_f32_32x32x16_bf16 a[144:159], a[60:63], v[236:239], a[144:159]
s_waitcnt lgkmcnt(7)
ds_read_b128 a[60:63], v63 offset:7168
s_cbranch_scc1 .LE0
.LS0:
v_mfma_f32_32x32x16_bf16 v[128:143], a[0:3], v[64:67], v[192:207]
s_mov_b32 m0, s51
ds_read_b128 a[0:3], v62 offset:8192
v_exp_f32 v52, v144
v_exp_f32 v53, v145
v_mfma_f32_32x32x16_bf16 v[128:143], a[4:7], v[68:71], v[128:143]
s_add_u32 s44, s44, 16384
ds_read_b128 a[4:7], v62 offset:8704
v_add_f32 v60, v60, v52
v_exp_f32 v54, v146
v_add_f32 v60, v60, v53
v_mfma_f32_32x32x16_bf16 v[128:143], a[8:11], v[72:75], v[128:143]
buffer_load_dwordx4 v7, s[8:11], s45 offen offset:0 sc1 lds
ds_read_b128 a[8:11], v62 offset:9216
v_cvt_pk_bf16_f32 v232, v52, v53
v_exp_f32 v55, v147
v_mfma_f32_32x32x16_bf16 v[128:143], a[12:15], v[76:79], v[128:143]
s_xor_b32 s50, s46, s52
ds_read_b128 a[12:15], v62 offset:9728
v_add_f32 v60, v60, v54
v_exp_f32 v52, v148
v_add_f32 v60, v60, v55
v_mfma_f32_32x32x16_bf16 v[128:143], a[16:19], v[80:83], v[128:143]
s_cmp_lt_u32 s44, s54
s_cselect_b32 s50, s50, s27
ds_read_b128 a[16:19], v62 offset:10240
v_cvt_pk_bf16_f32 v233, v54, v55
v_exp_f32 v53, v149
v_mfma_f32_32x32x16_bf16 v[128:143], a[20:23], v[84:87], v[128:143]
ds_read_b128 a[20:23], v62 offset:10752
v_add_f32 v60, v60, v52
v_exp_f32 v54, v150
v_add_f32 v60, v60, v53
v_cvt_pk_bf16_f32 v234, v52, v53
v_mfma_f32_32x32x16_bf16 v[128:143], a[24:27], v[88:91], v[128:143]
buffer_load_dwordx4 v7, s[8:11], s45 offen offset:1024 sc1 lds
ds_read_b128 a[24:27], v62 offset:11264
v_exp_f32 v55, v151
v_add_f32 v60, v60, v54
v_mfma_f32_32x32x16_bf16 v[128:143], a[28:31], v[92:95], v[128:143]
s_waitcnt lgkmcnt(7)
ds_read_b128 a[28:31], v62 offset:11776
v_exp_f32 v52, v152
v_add_f32 v60, v60, v55
v_cvt_pk_bf16_f32 v235, v54, v55
v_mfma_f32_32x32x16_bf16 a[64:79], a[32:35], v[224:227], a[64:79]
ds_read_b128 a[32:35], v63 offset:8192
v_exp_f32 v53, v153
v_add_f32 v60, v60, v52
v_exp_f32 v54, v154
v_mfma_f32_32x32x16_bf16 a[80:95], a[36:39], v[224:227], a[80:95]
ds_read_b128 a[36:39], v63 offset:9216
v_add_u32 v62, s46, v32
v_add_f32 v60, v60, v53
v_cvt_pk_bf16_f32 v236, v52, v53
v_exp_f32 v55, v155
v_mfma_f32_32x32x16_bf16 a[112:127], a[40:43], v[224:227], a[112:127]
buffer_load_dwordx4 v7, s[8:11], s45 offen offset:2048 sc1 lds
ds_read_b128 a[40:43], v63 offset:10240
v_add_f32 v60, v60, v54
v_exp_f32 v52, v156
v_mfma_f32_32x32x16_bf16 a[144:159], a[44:47], v[224:227], a[144:159]
ds_read_b128 a[44:47], v63 offset:11264
v_add_f32 v60, v60, v55
v_cvt_pk_bf16_f32 v237, v54, v55
v_exp_f32 v53, v157
v_add_f32 v60, v60, v52
v_mfma_f32_32x32x16_bf16 a[64:79], a[48:51], v[228:231], a[64:79]
ds_read_b128 a[48:51], v63 offset:12288
v_exp_f32 v54, v158
v_add_f32 v60, v60, v53
v_cvt_pk_bf16_f32 v238, v52, v53
v_mfma_f32_32x32x16_bf16 a[80:95], a[52:55], v[228:231], a[80:95]
s_sub_u32 s56, s56, 1
ds_read_b128 a[52:55], v63 offset:13312
v_exp_f32 v55, v159
v_add_f32 v60, v60, v54
v_mfma_f32_32x32x16_bf16 a[112:127], a[56:59], v[228:231], a[112:127]
buffer_load_dwordx4 v7, s[8:11], s45 offen offset:3072 sc1 lds
ds_read_b128 a[56:59], v63 offset:14336
v_add_f32 v60, v60, v55
v_cvt_pk_bf16_f32 v239, v54, v55
v_mfma_f32_32x32x16_bf16 a[144:159], a[60:63], v[228:231], a[144:159]
s_waitcnt vmcnt(8) lgkmcnt(7)
s_barrier
ds_read_b128 a[60:63], v63 offset:15360
v_add_u32 v63, s49, v33
s_cbranch_scc1 .LE1
s_sub_u32 s57, s57, 1
s_cbranch_scc0 .LS1
s_swappc_b64 s[30:31], s[58:59]
.Lret_s:
s_branch .LS1
.LE0:
v_mfma_f32_32x32x16_bf16 v[128:143], a[0:3], v[64:67], v[16:31]
s_mov_b32 m0, s51
v_exp_f32 v52, v144
v_exp_f32 v53, v145
v_add_f32 v60, v60, v52
v_mfma_f32_32x32x16_bf16 v[128:143], a[4:7], v[68:71], v[128:143]
s_add_u32 s44, s44, 16384
buffer_load_dwordx4 v7, s[8:11], s45 offen offset:0 sc1 lds
buffer_load_dwordx4 v7, s[8:11], s45 offen offset:1024 sc1 lds
v_exp_f32 v54, v146
v_add_f32 v60, v60, v53
v_cvt_pk_bf16_f32 v232, v52, v53
v_mfma_f32_32x32x16_bf16 v[128:143], a[8:11], v[72:75], v[128:143]
buffer_load_dwordx4 v7, s[8:11], s45 offen offset:2048 sc1 lds
buffer_load_dwordx4 v7, s[8:11], s45 offen offset:3072 sc1 lds
v_exp_f32 v55, v147
v_add_f32 v60, v60, v54
v_mfma_f32_32x32x16_bf16 v[128:143], a[12:15], v[76:79], v[128:143]
s_xor_b32 s50, s46, s52
s_add_u32 m0, s48, 131072
v_exp_f32 v52, v148
v_add_f32 v60, v60, v55
v_cvt_pk_bf16_f32 v233, v54, v55
v_mfma_f32_32x32x16_bf16 v[128:143], a[16:19], v[80:83], v[128:143]
s_cmp_lt_u32 s44, s54
s_cselect_b32 s50, s50, s27
buffer_load_dwordx4 v40, s[16:19], s26 offen offset:0 lds
v_exp_f32 v53, v149
v_add_f32 v60, v60, v52
v_mfma_f32_32x32x16_bf16 v[128:143], a[20:23], v[84:87], v[128:143]
buffer_load_dwordx4 v41, s[16:19], s26 offen offset:1024 lds
v_exp_f32 v54, v150
v_add_f32 v60, v60, v53
v_cvt_pk_bf16_f32 v234, v52, v53
v_mfma_f32_32x32x16_bf16 v[128:143], a[24:27], v[88:91], v[128:143]
buffer_load_dwordx4 v40, s[16:19], s26 offen offset:2048 lds
v_exp_f32 v55, v151
v_add_f32 v60, v60, v54
v_mfma_f32_32x32x16_bf16 v[128:143], a[28:31], v[92:95], v[128:143]
s_waitcnt lgkmcnt(0)
buffer_load_dwordx4 v41, s[16:19], s26 offen offset:3072 lds
v_exp_f32 v52, v152
v_add_f32 v60, v60, v55
v_cvt_pk_bf16_f32 v235, v54, v55
v_exp_f32 v53, v153
v_mfma_f32_32x32x16_bf16 a[64:79], a[32:35], v[224:227], a[64:79]
s_add_u32 m0, s48, 135168
ds_read_b128 a[32:35], v63 offset:8192
v_add_f32 v60, v60, v52
v_exp_f32 v54, v154
v_add_f32 v60, v60, v53
v_cvt_pk_bf16_f32 v236, v52, v53
v_mfma_f32_32x32x16_bf16 a[80:95], a[36:39], v[224:227], a[80:95]
buffer_load_dwordx4 v40, s[16:19], s47 offen offset:0 lds
ds_read_b128 a[36:39], v63 offset:9216
v_exp_f32 v55, v155
v_add_f32 v60, v60, v54
v_mfma_f32_32x32x16_bf16 a[112:127], a[40:43], v[224:227], a[112:127]
buffer_load_dwordx4 v41, s[16:19], s47 offen offset:1024 lds
ds_read_b128 a[40:43], v63 offset:10240
v_exp_f32 v52, v156
v_add_f32 v60, v60, v55
v_mfma_f32_32x32x16_bf16 a[144:159], a[44:47], v[224:227], a[144:159]
buffer_load_dwordx4 v42, s[12:15], s25 offen offset:2048 lds
ds_read_b128 a[44:47], v63 offset:11264
v_cvt_pk_bf16_f32 v237, v54, v55
v_exp_f32 v53, v157
v_add_f32 v60, v60, v52
v_mfma_f32_32x32x16_bf16 a[64:79], a[48:51], v[228:231], a[64:79]
s_add_u32 m0, s91, 65536
ds_read_b128 a[48:51], v63 offset:12288
v_exp_f32 v54, v158
v_add_f32 v60, v60, v53
v_cvt_pk_bf16_f32 v238, v52, v53
v_mfma_f32_32x32x16_bf16 a[80:95], a[52:55], v[228:231], a[80:95]
buffer_load_dwordx4 v36, s[12:15], s24 offen offset:0 lds
buffer_load_dwordx4 v37, s[12:15], s24 offen offset:1024 lds
ds_read_b128 a[52:55], v63 offset:13312
v_exp_f32 v55, v159
v_add_f32 v60, v60, v54
v_mfma_f32_32x32x16_bf16 a[112:127], a[56:59], v[228:231], a[112:127]
buffer_load_dwordx4 v38, s[12:15], s24 offen offset:2048 lds
buffer_load_dwordx4 v39, s[12:15], s24 offen offset:3072 lds
ds_read_b128 a[56:59], v63 offset:14336
v_add_f32 v60, v60, v55
v_cvt_pk_bf16_f32 v239, v54, v55
v_mfma_f32_32x32x16_bf16 a[144:159], a[60:63], v[228:231], a[144:159]
s_waitcnt vmcnt(19)
s_barrier
s_add_u32 s26, s26, 393216
s_add_u32 s47, s47, 393216
s_add_u32 s25, s25, 128
s_add_u32 s24, s24, 128
ds_read_b128 a[60:63], v63 offset:15360
v_add_u32 v63, s49, v33
s_waitcnt lgkmcnt(4)
v_mfma_f32_32x32x16_bf16 a[64:79], a[32:35], v[232:235], a[64:79]
s_add_u32 m0, s91, 81920
ds_read_b128 a[32:35], v63 offset:0
v_exp_f32 v52, v128
v_exp_f32 v53, v129
v_add_f32 v60, v60, v52
v_mfma_f32_32x32x16_bf16 a[80:95], a[36:39], v[232:235], a[80:95]
buffer_load_dwordx4 v36, s[12:15], s24 offen offset:0 lds
ds_read_b128 a[36:39], v63 offset:1024
v_exp_f32 v54, v130
v_add_f32 v60, v60, v53
v_cvt_pk_bf16_f32 v224, v52, v53
v_mfma_f32_32x32x16_bf16 a[112:127], a[40:43], v[232:235], a[112:127]
buffer_load_dwordx4 v37, s[12:15], s24 offen offset:1024 lds
ds_read_b128 a[40:43], v63 offset:2048
v_exp_f32 v55, v131
v_add_f32 v60, v60, v54
v_exp_f32 v52, v132
v_mfma_f32_32x32x16_bf16 a[144:159], a[44:47], v[232:235], a[144:159]
s_waitcnt lgkmcnt(3)
ds_read_b128 a[44:47], v63 offset:3072
v_add_f32 v60, v60, v55
v_cvt_pk_bf16_f32 v225, v54, v55
v_exp_f32 v53, v133
v_add_f32 v60, v60, v52
v_mfma_f32_32x32x16_bf16 a[64:79], a[48:51], v[236:239], a[64:79]
buffer_load_dwordx4 v38, s[12:15], s24 offen offset:2048 lds
ds_read_b128 a[48:51], v63 offset:4096
v_exp_f32 v54, v134
v_add_f32 v60, v60, v53
v_cvt_pk_bf16_f32 v226, v52, v53
v_mfma_f32_32x32x16_bf16 a[80:95], a[52:55], v[236:239], a[80:95]
buffer_load_dwordx4 v39, s[12:15], s24 offen offset:3072 lds
ds_read_b128 a[52:55], v63 offset:5120
v_exp_f32 v55, v135
v_add_f32 v60, v60, v54
v_exp_f32 v52, v136
v_mfma_f32_32x32x16_bf16 a[112:127], a[56:59], v[236:239], a[112:127]
s_add_u32 s24, s24, 128
ds_read_b128 a[56:59], v63 offset:6144
v_add_f32 v60, v60, v55
v_cvt_pk_bf16_f32 v227, v54, v55
v_exp_f32 v53, v137
v_add_f32 v60, v60, v52
v_mfma_f32_32x32x16_bf16 a[144:159], a[60:63], v[236:239], a[144:159]
ds_read_b128 a[60:63], v63 offset:7168
v_exp_f32 v54, v138
v_add_f32 v60, v60, v53
v_cvt_pk_bf16_f32 v228, v52, v53
v_exp_f32 v55, v139
v_add_f32 v60, v60, v54
v_exp_f32 v52, v140
v_add_f32 v60, v60, v55
v_cvt_pk_bf16_f32 v229, v54, v55
v_exp_f32 v53, v141
v_add_f32 v60, v60, v52
v_exp_f32 v54, v142
v_add_f32 v60, v60, v53
v_cvt_pk_bf16_f32 v230, v52, v53
v_exp_f32 v55, v143
v_add_f32 v60, v60, v54
v_add_f32 v60, v60, v55
v_cvt_pk_bf16_f32 v231, v54, v55
s_waitcnt lgkmcnt(4)
v_mfma_f32_32x32x16_bf16 a[64:79], a[32:35], v[224:227], a[64:79]
v_mfma_f32_32x32x16_bf16 a[80:95], a[36:39], v[224:227], a[80:95]
v_mfma_f32_32x32x16_bf16 a[112:127], a[40:43], v[224:227], a[112:127]
v_mfma_f32_32x32x16_bf16 a[144:159], a[44:47], v[224:227], a[144:159]
s_waitcnt lgkmcnt(0)
v_mfma_f32_32x32x16_bf16 a[64:79], a[48:51], v[228:231], a[64:79]
v_mfma_f32_32x32x16_bf16 a[80:95], a[52:55], v[228:231], a[80:95]
v_mfma_f32_32x32x16_bf16 a[112:127], a[56:59], v[228:231], a[112:127]
v_mfma_f32_32x32x16_bf16 a[144:159], a[60:63], v[228:231], a[144:159]
s_waitcnt vmcnt(15)
s_barrier
s_branch .LEend
.LE1:
v_mfma_f32_32x32x16_bf16 v[144:159], a[0:3], v[64:67], v[16:31]
s_add_u32 m0, s48, 131072
v_exp_f32 v52, v128
v_exp_f32 v53, v129
v_add_f32 v60, v60, v52
v_mfma_f32_32x32x16_bf16 v[144:159], a[4:7], v[68:71], v[144:159]
buffer_load_dwordx4 v40, s[16:19], s26 offen offset:0 lds
v_exp_f32 v54, v130
v_add_f32 v60, v60, v53
v_cvt_pk_bf16_f32 v224, v52, v53
v_mfma_f32_32x32x16_bf16 v[144:159], a[8:11], v[72:75], v[144:159]
buffer_load_dwordx4 v41, s[16:19], s26 offen offset:1024 lds
v_exp_f32 v55, v131
v_add_f32 v60, v60, v54
v_mfma_f32_32x32x16_bf16 v[144:159], a[12:15], v[76:79], v[144:159]
buffer_load_dwordx4 v40, s[16:19], s26 offen offset:2048 lds
v_exp_f32 v52, v132
v_add_f32 v60, v60, v55
v_cvt_pk_bf16_f32 v225, v54, v55
v_mfma_f32_32x32x16_bf16 v[144:159], a[16:19], v[80:83], v[144:159]
buffer_load_dwordx4 v41, s[16:19], s26 offen offset:3072 lds
v_exp_f32 v53, v133
v_add_f32 v60, v60, v52
v_mfma_f32_32x32x16_bf16 v[144:159], a[20:23], v[84:87], v[144:159]
s_add_u32 m0, s48, 135168
v_exp_f32 v54, v134
v_add_f32 v60, v60, v53
v_cvt_pk_bf16_f32 v226, v52, v53
v_mfma_f32_32x32x16_bf16 v[144:159], a[24:27], v[88:91], v[144:159]
buffer_load_dwordx4 v40, s[16:19], s47 offen offset:0 lds
v_exp_f32 v55, v135
v_add_f32 v60, v60, v54
v_mfma_f32_32x32x16_bf16 v[144:159], a[28:31], v[92:95], v[144:159]
s_waitcnt lgkmcnt(0)
buffer_load_dwordx4 v41, s[16:19], s47 offen offset:1024 lds
v_exp_f32 v52, v136
v_add_f32 v60, v60, v55
v_cvt_pk_bf16_f32 v227, v54, v55
v_exp_f32 v53, v137
v_mfma_f32_32x32x16_bf16 a[64:79], a[32:35], v[232:235], a[64:79]
buffer_load_dwordx4 v42, s[12:15], s25 offen offset:2048 lds
ds_read_b128 a[32:35], v63 offset:0
v_add_f32 v60, v60, v52
v_exp_f32 v54, v138
v_add_f32 v60, v60, v53
v_cvt_pk_bf16_f32 v228, v52, v53
v_mfma_f32_32x32x16_bf16 a[80:95], a[36:39], v[232:235], a[80:95]
s_add_u32 m0, s91, 65536
ds_read_b128 a[36:39], v63 offset:1024
v_exp_f32 v55, v139
v_add_f32 v60, v60, v54
v_mfma_f32_32x32x16_bf16 a[112:127], a[40:43], v[232:235], a[112:127]
buffer_load_dwordx4 v36, s[12:15], s24 offen offset:0 lds
ds_read_b128 a[40:43], v63 offset:2048
v_exp_f32 v52, v140
v_add_f32 v60, v60, v55
v_cvt_pk_bf16_f32 v229, v54, v55
v_mfma_f32_32x32x16_bf16 a[144:159], a[44:47], v[232:235], a[144:159]
buffer_load_dwordx4 v37, s[12:15], s24 offen offset:1024 lds
ds_read_b128 a[44:47], v63 offset:3072
v_exp_f32 v53, v141
v_add_f32 v60, v60, v52
v_mfma_f32_32x32x16_bf16 a[64:79], a[48:51], v[236:239], a[64:79]
buffer_load_dwordx4 v38, s[12:15], s24 offen offset:2048 lds
ds_read_b128 a[48:51], v63 offset:4096
v_exp_f32 v54, v142
v_add_f32 v60, v60, v53
v_cvt_pk_bf16_f32 v230, v52, v53
v_mfma_f32_32x32x16_bf16 a[80:95], a[52:55], v[236:239], a[80:95]
buffer_load_dwordx4 v39, s[12:15], s24 offen offset:3072 lds
ds_read_b128 a[52:55], v63 offset:5120
v_exp_f32 v55, v143
v_add_f32 v60, v60, v54
v_mfma_f32_32x32x16_bf16 a[112:127], a[56:59], v[236:239], a[112:127]
ds_read_b128 a[56:59], v63 offset:6144
v_add_f32 v60, v60, v55
v_cvt_pk_bf16_f32 v231, v54, v55
v_mfma_f32_32x32x16_bf16 a[144:159], a[60:63], v[236:239], a[144:159]
s_add_u32 s26, s26, 393216
s_add_u32 s47, s47, 393216
s_add_u32 s25, s25, 128
s_add_u32 s24, s24, 128
ds_read_b128 a[60:63], v63 offset:7168
s_waitcnt lgkmcnt(4)
v_mfma_f32_32x32x16_bf16 a[64:79], a[32:35], v[224:227], a[64:79]
s_add_u32 m0, s91, 81920
ds_read_b128 a[32:35], v63 offset:8192
v_exp_f32 v52, v144
v_exp_f32 v53, v145
v_add_f32 v60, v60, v52
v_mfma_f32_32x32x16_bf16 a[80:95], a[36:39], v[224:227], a[80:95]
buffer_load_dwordx4 v36, s[12:15], s24 offen offset:0 lds
ds_read_b128 a[36:39], v63 offset:9216
v_exp_f32 v54, v146
v_add_f32 v60, v60, v53
v_cvt_pk_bf16_f32 v232, v52, v53
v_mfma_f32_32x32x16_bf16 a[112:127], a[40:43], v[224:227], a[112:127]
buffer_load_dwordx4 v37, s[12:15], s24 offen offset:1024 lds
ds_read_b128 a[40:43], v63 offset:10240
v_exp_f32 v55, v147
v_add_f32 v60, v60, v54
v_exp_f32 v52, v148
v_mfma_f32_32x32x16_bf16 a[144:159], a[44:47], v[224:227], a[144:159]
s_waitcnt lgkmcnt(3)
ds_read_b128 a[44:47], v63 offset:11264
v_add_f32 v60, v60, v55
v_cvt_pk_bf16_f32 v233, v54, v55
v_exp_f32 v53, v149
v_add_f32 v60, v60, v52
v_mfma_f32_32x32x16_bf16 a[64:79], a[48:51], v[228:231], a[64:79]
buffer_load_dwordx4 v38, s[12:15], s24 offen offset:2048 lds
ds_read_b128 a[48:51], v63 offset:12288
v_exp_f32 v54, v150
v_add_f32 v60, v60, v53
v_cvt_pk_bf16_f32 v234, v52, v53
v_mfma_f32_32x32x16_bf16 a[80:95], a[52:55], v[228:231], a[80:95]
buffer_load_dwordx4 v39, s[12:15], s24 offen offset:3072 lds
ds_read_b128 a[52:55], v63 offset:13312
v_exp_f32 v55, v151
v_add_f32 v60, v60, v54
v_exp_f32 v52, v152
v_mfma_f32_32x32x16_bf16 a[112:127], a[56:59], v[228:231], a[112:127]
s_add_u32 s24, s24, 128
ds_read_b128 a[56:59], v63 offset:14336
v_add_f32 v60, v60, v55
v_cvt_pk_bf16_f32 v235, v54, v55
v_exp_f32 v53, v153
v_add_f32 v60, v60, v52
v_mfma_f32_32x32x16_bf16 a[144:159], a[60:63], v[228:231], a[144:159]
s_waitcnt vmcnt(15)
s_barrier
ds_read_b128 a[60:63], v63 offset:15360
v_add_u32 v63, s49, v33
v_exp_f32 v54, v154
v_add_f32 v60, v60, v53
v_cvt_pk_bf16_f32 v236, v52, v53
v_exp_f32 v55, v155
v_add_f32 v60, v60, v54
v_exp_f32 v52, v156
v_add_f32 v60, v60, v55
v_cvt_pk_bf16_f32 v237, v54, v55
v_exp_f32 v53, v157
v_add_f32 v60, v60, v52
v_exp_f32 v54, v158
v_add_f32 v60, v60, v53
v_cvt_pk_bf16_f32 v238, v52, v53
v_exp_f32 v55, v159
v_add_f32 v60, v60, v54
v_add_f32 v60, v60, v55
v_cvt_pk_bf16_f32 v239, v54, v55
s_waitcnt lgkmcnt(4)
v_mfma_f32_32x32x16_bf16 a[64:79], a[32:35], v[232:235], a[64:79]
v_mfma_f32_32x32x16_bf16 a[80:95], a[36:39], v[232:235], a[80:95]
v_mfma_f32_32x32x16_bf16 a[112:127], a[40:43], v[232:235], a[112:127]
v_mfma_f32_32x32x16_bf16 a[144:159], a[44:47], v[232:235], a[144:159]
s_waitcnt lgkmcnt(0)
v_mfma_f32_32x32x16_bf16 a[64:79], a[48:51], v[236:239], a[64:79]
v_mfma_f32_32x32x16_bf16 a[80:95], a[52:55], v[236:239], a[80:95]
v_mfma_f32_32x32x16_bf16 a[112:127], a[56:59], v[236:239], a[112:127]
v_mfma_f32_32x32x16_bf16 a[144:159], a[60:63], v[236:239], a[144:159]
.LEend:
s_cmp_lt_u32 s3, 2
s_cbranch_scc0 .LEend2
s_barrier
.LEend2:
v_mov_b32 v8, v60
v_mov_b32 v9, v61
s_add_u32 m0, s48, 98304
s_nop 0
buffer_load_dwordx4 v40, s[16:19], s26 offen offset:0 lds
buffer_load_dwordx4 v41, s[16:19], s26 offen offset:1024 lds
buffer_load_dwordx4 v40, s[16:19], s26 offen offset:2048 lds
buffer_load_dwordx4 v41, s[16:19], s26 offen offset:3072 lds
v_permlane32_swap_b32 v60, v8
v_permlane32_swap_b32 v61, v9
v_add_f32 v60, v60, v8
v_add_f32 v61, v61, v9
s_add_u32 m0, s48, 102400
s_nop 0
buffer_load_dwordx4 v40, s[16:19], s47 offen offset:0 lds
buffer_load_dwordx4 v41, s[16:19], s47 offen offset:1024 lds
buffer_load_dwordx4 v42, s[12:15], s25 offen offset:2048 lds
s_add_u32 m0, s91, 0
s_nop 0
buffer_load_dwordx4 v36, s[12:15], s24 offen offset:0 lds
buffer_load_dwordx4 v37, s[12:15], s24 offen offset:1024 lds
buffer_load_dwordx4 v38, s[12:15], s24 offen offset:2048 lds
buffer_load_dwordx4 v39, s[12:15], s24 offen offset:3072 lds
s_add_u32 s26, s26, 393216
s_add_u32 s47, s47, 393216
s_add_u32 s25, s25, 128
s_add_u32 s24, s24, 128
s_mov_b32 s28, 1602224128
v_cmp_nge_f32 s[30:31], s28, v60
v_cmp_nge_f32 s[32:33], s28, v61
s_or_b64 s[34:35], s[30:31], s[32:33]
s_cbranch_scc1 .Lx
.Lx_ret:
v_rcp_f32 v52, v60
v_rcp_f32 v53, v61
s_lshl_b32 s28, s88, 13
s_lshl_b32 s29, s3, 18
s_add_u32 s28, s28, s29
s_add_u32 s28, s28, 33554432
s_add_u32 s20, s84, s28
s_addc_u32 s21, s85, 0
s_and_b32 s21, s21, 0xffff
s_mov_b32 s22, 16777216
s_mov_b32 s23, 0x20000
s_sub_u32 s40, 15, s89
s_lshl_b32 s40, s40, 20
s_add_u32 s41, s40, 4096
s_lshl_b32 s42, s89, 20
s_add_u32 s43, s42, 4096
s_cmp_lt_u32 s3, 2
s_waitcnt vmcnt(15)
s_barrier
ds_read_b128 a[128:131], v45
ds_read_b128 a[132:135], v43 offset:0
ds_read_b64_tr_b16 a[164:165], v47 offset:0
ds_read_b64_tr_b16 a[166:167], v47 offset:512
ds_read_b64_tr_b16 a[168:169], v48 offset:0
ds_read_b64_tr_b16 a[170:171], v48 offset:512
ds_read_b128 a[136:139], v43 offset:2048
ds_read_b128 a[140:143], v43 offset:4096
ds_read_b128 a[160:163], v43 offset:6144
ds_read_b64_tr_b16 a[172:173], v47 offset:2048
ds_read_b64_tr_b16 a[174:175], v47 offset:2560
ds_read_b64_tr_b16 a[192:193], v48 offset:2048
ds_read_b64_tr_b16 a[194:195], v48 offset:2560
s_cselect_b32 s39, 1, 0
s_waitcnt lgkmcnt(8)
ds_read_b64_tr_b16 a[196:197], v49 offset:4096
ds_read_b64_tr_b16 a[198:199], v49 offset:4608
ds_read_b64_tr_b16 a[200:201], v50 offset:4096
ds_read_b64_tr_b16 a[202:203], v50 offset:4608
s_waitcnt lgkmcnt(0)
v_mfma_f32_16x16x32_bf16 v[160:163], a[172:175], a[128:131], 0
v_mfma_f32_16x16x32_bf16 v[164:167], a[192:195], a[128:131], 0
s_add_u32 m0, s48, 32768
ds_read_b64_tr_b16 a[20:21], v47 offset:14336
ds_read_b64_tr_b16 a[22:23], v47 offset:14848
v_mfma_f32_16x16x32_bf16 v[200:203], a[128:131], a[196:199], 0
buffer_load_dwordx4 v40, s[16:19], s26 offen offset:0 lds
v_accvgpr_read_b32 v72, a64
v_mfma_f32_16x16x32_bf16 v[204:207], a[128:131], a[200:203], 0
ds_read_b128 a[0:3], v46
v_accvgpr_read_b32 v73, a65
v_mfma_f32_16x16x32_bf16 v[128:131], a[164:167], a[132:135], 0
ds_read_b64_tr_b16 a[24:25], v48 offset:14336
ds_read_b64_tr_b16 a[26:27], v48 offset:14848
v_mfma_f32_16x16x32_bf16 v[132:135], a[168:171], a[132:135], 0
buffer_load_dwordx4 v41, s[16:19], s26 offen offset:1024 lds
v_mul_f32 v72, v52, v72
v_mfma_f32_16x16x32_bf16 v[168:171], a[172:175], a[132:135], 0
ds_read_b128 a[4:7], v44 offset:0
v_mul_f32 v73, v52, v73
v_mfma_f32_16x16x32_bf16 v[172:175], a[192:195], a[132:135], 0
ds_read_b64_tr_b16 a[28:29], v47 offset:16384
ds_read_b64_tr_b16 a[30:31], v47 offset:16896
v_mfma_f32_16x16x32_bf16 v[208:211], a[132:135], a[196:199], 0
buffer_load_dwordx4 v40, s[16:19], s26 offen offset:2048 lds
v_cvt_pk_bf16_f32 v64, v72, v73
v_mfma_f32_16x16x32_bf16 v[212:215], a[132:135], a[200:203], 0
ds_read_b128 a[8:11], v44 offset:2048
v_accvgpr_read_b32 v74, a66
v_mfma_f32_16x16x32_bf16 v[136:139], a[164:167], a[136:139], 0
ds_read_b64_tr_b16 a[32:33], v48 offset:16384
ds_read_b64_tr_b16 a[34:35], v48 offset:16896
v_mfma_f32_16x16x32_bf16 v[140:143], a[168:171], a[136:139], 0
buffer_load_dwordx4 v41, s[16:19], s26 offen offset:3072 lds
v_accvgpr_read_b32 v75, a67
v_mfma_f32_16x16x32_bf16 v[176:179], a[172:175], a[136:139], 0
ds_read_b128 a[12:15], v44 offset:4096
v_mul_f32 v74, v52, v74
v_mfma_f32_16x16x32_bf16 v[180:183], a[192:195], a[136:139], 0
s_add_u32 m0, s48, 36864
ds_read_b64_tr_b16 a[36:37], v49 offset:18432
ds_read_b64_tr_b16 a[38:39], v49 offset:18944
v_mfma_f32_16x16x32_bf16 v[216:219], a[136:139], a[196:199], 0
buffer_load_dwordx4 v40, s[16:19], s47 offen offset:0 lds
v_mul_f32 v75, v52, v75
v_mfma_f32_16x16x32_bf16 v[220:223], a[136:139], a[200:203], 0
ds_read_b128 a[16:19], v44 offset:6144
v_cvt_pk_bf16_f32 v65, v74, v75
v_mfma_f32_16x16x32_bf16 v[144:147], a[164:167], a[140:143], 0
ds_read_b64_tr_b16 a[40:41], v50 offset:18432
ds_read_b64_tr_b16 a[42:43], v50 offset:18944
v_mfma_f32_16x16x32_bf16 v[148:151], a[168:171], a[140:143], 0
s_cmp_eq_u32 s39, 1
s_mov_b32 s28, 16384
s_cselect_b32 s28, 4294934528, s28
buffer_load_dwordx4 v41, s[16:19], s47 offen offset:1024 lds
v_mfma_f32_16x16x32_bf16 v[184:187], a[172:175], a[140:143], 0
v_add_u32 v43, 16384, v43
v_add_u32 v44, 16384, v44
v_mfma_f32_16x16x32_bf16 v[188:191], a[192:195], a[140:143], 0
v_add_u32 v47, 4294934528, v47
v_add_u32 v48, 4294934528, v48
v_mfma_f32_16x16x32_bf16 v[224:227], a[140:143], a[196:199], 0
buffer_load_dwordx4 v42, s[12:15], s25 offen offset:2048 lds
v_add_u32 v49, 4294934528, v49
v_add_u32 v50, 4294934528, v50
v_mfma_f32_16x16x32_bf16 v[228:231], a[140:143], a[200:203], 0
s_add_u32 s26, s26, 393216
s_add_u32 s47, s47, 393216
s_add_u32 s25, s25, 128
v_add_u32 v45, s28, v45
v_add_u32 v46, s28, v46
v_mfma_f32_16x16x32_bf16 v[152:155], a[164:167], a[160:163], 0
s_add_u32 m0, s91, 16384
v_accvgpr_read_b32 v72, a68
v_accvgpr_read_b32 v73, a69
v_mfma_f32_16x16x32_bf16 v[156:159], a[168:171], a[160:163], 0
buffer_load_dwordx4 v36, s[12:15], s24 offen offset:0 lds
v_mul_f32 v72, v52, v72
v_mfma_f32_16x16x32_bf16 v[192:195], a[172:175], a[160:163], 0
buffer_load_dwordx4 v37, s[12:15], s24 offen offset:1024 lds
v_mul_f32 v73, v52, v73
v_mfma_f32_16x16x32_bf16 v[196:199], a[192:195], a[160:163], 0
buffer_load_dwordx4 v38, s[12:15], s24 offen offset:2048 lds
v_cvt_pk_bf16_f32 v66, v72, v73
v_mfma_f32_16x16x32_bf16 v[232:235], a[160:163], a[196:199], 0
buffer_load_dwordx4 v39, s[12:15], s24 offen offset:3072 lds
v_accvgpr_read_b32 v74, a70
v_mfma_f32_16x16x32_bf16 v[236:239], a[160:163], a[200:203], 0
s_waitcnt vmcnt(15) lgkmcnt(0)
s_barrier
v_accvgpr_read_b32 v75, a71
v_mfma_f32_16x16x32_bf16 v[160:163], a[28:31], a[0:3], v[160:163]
s_add_u32 s24, s24, 128
v_mul_f32 v74, v52, v74
v_mul_f32 v75, v52, v75
v_mfma_f32_16x16x32_bf16 v[164:167], a[32:35], a[0:3], v[164:167]
s_add_u32 m0, s48, 131072
ds_read_b64_tr_b16 a[164:165], v47 offset:0
ds_read_b64_tr_b16 a[166:167], v47 offset:512
v_mfma_f32_16x16x32_bf16 v[200:203], a[0:3], a[36:39], v[200:203]
buffer_load_dwordx4 v40, s[16:19], s26 offen offset:0 lds
v_cvt_pk_bf16_f32 v67, v74, v75
v_mfma_f32_16x16x32_bf16 v[204:207], a[0:3], a[40:43], v[204:207]
ds_read_b128 a[128:131], v45
v_accvgpr_read_b32 v72, a72
v_mfma_f32_16x16x32_bf16 v[128:131], a[20:23], a[4:7], v[128:131]
ds_read_b64_tr_b16 a[168:169], v48 offset:0
ds_read_b64_tr_b16 a[170:171], v48 offset:512
v_mfma_f32_16x16x32_bf16 v[132:135], a[24:27], a[4:7], v[132:135]
buffer_load_dwordx4 v41, s[16:19], s26 offen offset:1024 lds
v_accvgpr_read_b32 v73, a73
v_mfma_f32_16x16x32_bf16 v[168:171], a[28:31], a[4:7], v[168:171]
ds_read_b128 a[132:135], v43 offset:0
v_mul_f32 v72, v52, v72
v_mfma_f32_16x16x32_bf16 v[172:175], a[32:35], a[4:7], v[172:175]
ds_read_b64_tr_b16 a[172:173], v47 offset:2048
ds_read_b64_tr_b16 a[174:175], v47 offset:2560
v_mfma_f32_16x16x32_bf16 v[208:211], a[4:7], a[36:39], v[208:211]
buffer_load_dwordx4 v40, s[16:19], s26 offen offset:2048 lds
v_mul_f32 v73, v52, v73
v_mfma_f32_16x16x32_bf16 v[212:215], a[4:7], a[40:43], v[212:215]
ds_read_b128 a[136:139], v43 offset:2048
v_cvt_pk_bf16_f32 v68, v72, v73
v_mfma_f32_16x16x32_bf16 v[136:139], a[20:23], a[8:11], v[136:139]
ds_read_b64_tr_b16 a[192:193], v48 offset:2048
ds_read_b64_tr_b16 a[194:195], v48 offset:2560
v_mfma_f32_16x16x32_bf16 v[140:143], a[24:27], a[8:11], v[140:143]
buffer_load_dwordx4 v41, s[16:19], s26 offen offset:3072 lds
v_accvgpr_read_b32 v74, a74
v_mfma_f32_16x16x32_bf16 v[176:179], a[28:31], a[8:11], v[176:179]
ds_read_b128 a[140:143], v43 offset:4096
v_accvgpr_read_b32 v75, a75
v_mfma_f32_16x16x32_bf16 v[180:183], a[32:35], a[8:11], v[180:183]
s_add_u32 m0, s48, 135168
ds_read_b64_tr_b16 a[196:197], v49 offset:4096
ds_read_b64_tr_b16 a[198:199], v49 offset:4608
v_mfma_f32_16x16x32_bf16 v[216:219], a[8:11], a[36:39], v[216:219]
buffer_load_dwordx4 v40, s[16:19], s47 offen offset:0 lds
v_mul_f32 v74, v52, v74
v_mfma_f32_16x16x32_bf16 v[220:223], a[8:11], a[40:43], v[220:223]
ds_read_b128 a[160:163], v43 offset:6144
v_mul_f32 v75, v52, v75
v_mfma_f32_16x16x32_bf16 v[144:147], a[20:23], a[12:15], v[144:147]
ds_read_b64_tr_b16 a[200:201], v50 offset:4096
ds_read_b64_tr_b16 a[202:203], v50 offset:4608
v_mfma_f32_16x16x32_bf16 v[148:151], a[24:27], a[12:15], v[148:151]
buffer_load_dwordx4 v41, s[16:19], s47 offen offset:1024 lds
v_cvt_pk_bf16_f32 v69, v74, v75
v_mfma_f32_16x16x32_bf16 v[184:187], a[28:31], a[12:15], v[184:187]
buffer_store_dwordx4 v[64:67], v7, s[20:23], s40 offen offset:0 sc1
v_accvgpr_read_b32 v72, a76
v_mfma_f32_16x16x32_bf16 v[188:191], a[32:35], a[12:15], v[188:191]
v_accvgpr_read_b32 v73, a77
v_mul_f32 v72, v52, v72
v_mfma_f32_16x16x32_bf16 v[224:227], a[12:15], a[36:39], v[224:227]
buffer_load_dwordx4 v42, s[12:15], s25 offen offset:2048 lds
v_mul_f32 v73, v52, v73
v_mfma_f32_16x16x32_bf16 v[228:231], a[12:15], a[40:43], v[228:231]
v_cvt_pk_bf16_f32 v70, v72, v73
v_accvgpr_read_b32 v74, a78
v_mfma_f32_16x16x32_bf16 v[152:155], a[20:23], a[16:19], v[152:155]
s_add_u32 s26, s26, 393216
s_add_u32 s47, s47, 393216
s_add_u32 s25, s25, 128
v_mfma_f32_16x16x32_bf16 v[156:159], a[24:27], a[16:19], v[156:159]
v_accvgpr_read_b32 v75, a79
v_mul_f32 v74, v52, v74
v_mfma_f32_16x16x32_bf16 v[192:195], a[28:31], a[16:19], v[192:195]
v_mul_f32 v75, v52, v75
v_cvt_pk_bf16_f32 v71, v74, v75
v_mfma_f32_16x16x32_bf16 v[196:199], a[32:35], a[16:19], v[196:199]
buffer_store_dwordx4 v[68:71], v7, s[20:23], s40 offen offset:1024 sc1
v_mfma_f32_16x16x32_bf16 v[232:235], a[16:19], a[36:39], v[232:235]
v_mfma_f32_16x16x32_bf16 v[236:239], a[16:19], a[40:43], v[236:239]
s_waitcnt lgkmcnt(0)
v_mfma_f32_16x16x32_bf16 v[160:163], a[172:175], a[128:131], v[160:163]
v_mfma_f32_16x16x32_bf16 v[164:167], a[192:195], a[128:131], v[164:167]
s_add_u32 m0, s91, 65536
ds_read_b64_tr_b16 a[20:21], v47 offset:14336
ds_read_b64_tr_b16 a[22:23], v47 offset:14848
v_mfma_f32_16x16x32_bf16 v[200:203], a[128:131], a[196:199], v[200:203]
buffer_load_dwordx4 v36, s[12:15], s24 offen offset:0 lds
v_accvgpr_read_b32 v72, a80
v_mfma_f32_16x16x32_bf16 v[204:207], a[128:131], a[200:203], v[204:207]
ds_read_b128 a[0:3], v46
v_accvgpr_read_b32 v73, a81
v_mfma_f32_16x16x32_bf16 v[128:131], a[164:167], a[132:135], v[128:131]
ds_read_b64_tr_b16 a[24:25], v48 offset:14336
ds_read_b64_tr_b16 a[26:27], v48 offset:14848
v_mfma_f32_16x16x32_bf16 v[132:135], a[168:171], a[132:135], v[132:135]
v_mul_f32 v72, v52, v72
v_mul_f32 v73, v52, v73
v_mfma_f32_16x16x32_bf16 v[168:171], a[172:175], a[132:135], v[168:171]
ds_read_b128 a[4:7], v44 offset:0
v_cvt_pk_bf16_f32 v64, v72, v73
v_mfma_f32_16x16x32_bf16 v[172:175], a[192:195], a[132:135], v[172:175]
ds_read_b64_tr_b16 a[28:29], v47 offset:16384
ds_read_b64_tr_b16 a[30:31], v47 offset:16896
v_mfma_f32_16x16x32_bf16 v[208:211], a[132:135], a[196:199], v[208:211]
buffer_load_dwordx4 v37, s[12:15], s24 offen offset:1024 lds
v_accvgpr_read_b32 v74, a82
v_mfma_f32_16x16x32_bf16 v[212:215], a[132:135], a[200:203], v[212:215]
ds_read_b128 a[8:11], v44 offset:2048
v_accvgpr_read_b32 v75, a83
v_mfma_f32_16x16x32_bf16 v[136:139], a[164:167], a[136:139], v[136:139]
ds_read_b64_tr_b16 a[32:33], v48 offset:16384
ds_read_b64_tr_b16 a[34:35], v48 offset:16896
v_mfma_f32_16x16x32_bf16 v[140:143], a[168:171], a[136:139], v[140:143]
v_mul_f32 v74, v52, v74
v_mul_f32 v75, v52, v75
v_mfma_f32_16x16x32_bf16 v[176:179], a[172:175], a[136:139], v[176:179]
ds_read_b128 a[12:15], v44 offset:4096
v_cvt_pk_bf16_f32 v65, v74, v75
v_mfma_f32_16x16x32_bf16 v[180:183], a[192:195], a[136:139], v[180:183]
ds_read_b64_tr_b16 a[36:37], v49 offset:18432
ds_read_b64_tr_b16 a[38:39], v49 offset:18944
v_mfma_f32_16x16x32_bf16 v[216:219], a[136:139], a[196:199], v[216:219]
buffer_load_dwordx4 v38, s[12:15], s24 offen offset:2048 lds
v_accvgpr_read_b32 v72, a84
v_mfma_f32_16x16x32_bf16 v[220:223], a[136:139], a[200:203], v[220:223]
ds_read_b128 a[16:19], v44 offset:6144
v_accvgpr_read_b32 v73, a85
v_mfma_f32_16x16x32_bf16 v[144:147], a[164:167], a[140:143], v[144:147]
ds_read_b64_tr_b16 a[40:41], v50 offset:18432
ds_read_b64_tr_b16 a[42:43], v50 offset:18944
v_mfma_f32_16x16x32_bf16 v[148:151], a[168:171], a[140:143], v[148:151]
s_cmp_eq_u32 s39, 1
s_mov_b32 s28, 4294885376
s_cselect_b32 s28, 4294901760, s28
v_mfma_f32_16x16x32_bf16 v[184:187], a[172:175], a[140:143], v[184:187]
v_add_u32 v43, 4294885376, v43
v_add_u32 v44, 4294885376, v44
v_mfma_f32_16x16x32_bf16 v[188:191], a[192:195], a[140:143], v[188:191]
v_add_u32 v47, 4294901760, v47
v_add_u32 v48, 4294901760, v48
v_mfma_f32_16x16x32_bf16 v[224:227], a[140:143], a[196:199], v[224:227]
buffer_load_dwordx4 v39, s[12:15], s24 offen offset:3072 lds
v_add_u32 v49, 4294901760, v49
v_add_u32 v50, 4294901760, v50
v_mfma_f32_16x16x32_bf16 v[228:231], a[140:143], a[200:203], v[228:231]
v_add_u32 v45, s28, v45
v_add_u32 v46, s28, v46
v_mfma_f32_16x16x32_bf16 v[152:155], a[164:167], a[160:163], v[152:155]
v_mul_f32 v72, v52, v72
v_mul_f32 v73, v52, v73
v_mfma_f32_16x16x32_bf16 v[156:159], a[168:171], a[160:163], v[156:159]
s_add_u32 s24, s24, 128
v_cvt_pk_bf16_f32 v66, v72, v73
v_accvgpr_read_b32 v74, a86
v_mfma_f32_16x16x32_bf16 v[192:195], a[172:175], a[160:163], v[192:195]
v_accvgpr_read_b32 v75, a87
v_mul_f32 v74, v52, v74
v_mfma_f32_16x16x32_bf16 v[196:199], a[192:195], a[160:163], v[196:199]
v_mul_f32 v75, v52, v75
v_cvt_pk_bf16_f32 v67, v74, v75
v_mfma_f32_16x16x32_bf16 v[232:235], a[160:163], a[196:199], v[232:235]
buffer_store_dwordx4 v[64:67], v7, s[20:23], s40 offen offset:2048 sc1
v_mfma_f32_16x16x32_bf16 v[236:239], a[160:163], a[200:203], v[236:239]
s_waitcnt vmcnt(18) lgkmcnt(0)
s_barrier
v_mfma_f32_16x16x32_bf16 v[160:163], a[28:31], a[0:3], v[160:163]
v_mfma_f32_16x16x32_bf16 v[164:167], a[32:35], a[0:3], v[164:167]
s_add_u32 m0, s48, 98304
ds_read_b64_tr_b16 a[164:165], v47 offset:0
ds_read_b64_tr_b16 a[166:167], v47 offset:512
v_mfma_f32_16x16x32_bf16 v[200:203], a[0:3], a[36:39], v[200:203]
buffer_load_dwordx4 v40, s[16:19], s26 offen offset:0 lds
v_accvgpr_read_b32 v72, a88
v_mfma_f32_16x16x32_bf16 v[204:207], a[0:3], a[40:43], v[204:207]
ds_read_b128 a[128:131], v45
v_accvgpr_read_b32 v73, a89
v_mfma_f32_16x16x32_bf16 v[128:131], a[20:23], a[4:7], v[128:131]
ds_read_b64_tr_b16 a[168:169], v48 offset:0
ds_read_b64_tr_b16 a[170:171], v48 offset:512
v_mfma_f32_16x16x32_bf16 v[132:135], a[24:27], a[4:7], v[132:135]
buffer_load_dwordx4 v41, s[16:19], s26 offen offset:1024 lds
v_mul_f32 v72, v52, v72
v_mfma_f32_16x16x32_bf16 v[168:171], a[28:31], a[4:7], v[168:171]
ds_read_b128 a[132:135], v43 offset:0
v_mul_f32 v73, v52, v73
v_mfma_f32_16x16x32_bf16 v[172:175], a[32:35], a[4:7], v[172:175]
ds_read_b64_tr_b16 a[172:173], v47 offset:2048
ds_read_b64_tr_b16 a[174:175], v47 offset:2560
v_mfma_f32_16x16x32_bf16 v[208:211], a[4:7], a[36:39], v[208:211]
buffer_load_dwordx4 v40, s[16:19], s26 offen offset:2048 lds
v_cvt_pk_bf16_f32 v68, v72, v73
v_mfma_f32_16x16x32_bf16 v[212:215], a[4:7], a[40:43], v[212:215]
ds_read_b128 a[136:139], v43 offset:2048
v_accvgpr_read_b32 v74, a90
v_mfma_f32_16x16x32_bf16 v[136:139], a[20:23], a[8:11], v[136:139]
ds_read_b64_tr_b16 a[192:193], v48 offset:2048
ds_read_b64_tr_b16 a[194:195], v48 offset:2560
v_mfma_f32_16x16x32_bf16 v[140:143], a[24:27], a[8:11], v[140:143]
buffer_load_dwordx4 v41, s[16:19], s26 offen offset:3072 lds
v_accvgpr_read_b32 v75, a91
v_mfma_f32_16x16x32_bf16 v[176:179], a[28:31], a[8:11], v[176:179]
ds_read_b128 a[140:143], v43 offset:4096
v_mul_f32 v74, v52, v74
v_mfma_f32_16x16x32_bf16 v[180:183], a[32:35], a[8:11], v[180:183]
s_add_u32 m0, s48, 102400
ds_read_b64_tr_b16 a[196:197], v49 offset:4096
ds_read_b64_tr_b16 a[198:199], v49 offset:4608
v_mfma_f32_16x16x32_bf16 v[216:219], a[8:11], a[36:39], v[216:219]
buffer_load_dwordx4 v40, s[16:19], s47 offen offset:0 lds
v_mul_f32 v75, v52, v75
v_mfma_f32_16x16x32_bf16 v[220:223], a[8:11], a[40:43], v[220:223]
ds_read_b128 a[160:163], v43 offset:6144
v_cvt_pk_bf16_f32 v69, v74, v75
v_mfma_f32_16x16x32_bf16 v[144:147], a[20:23], a[12:15], v[144:147]
ds_read_b64_tr_b16 a[200:201], v50 offset:4096
ds_read_b64_tr_b16 a[202:203], v50 offset:4608
v_mfma_f32_16x16x32_bf16 v[148:151], a[24:27], a[12:15], v[148:151]
buffer_load_dwordx4 v41, s[16:19], s47 offen offset:1024 lds
v_accvgpr_read_b32 v72, a92
v_mfma_f32_16x16x32_bf16 v[184:187], a[28:31], a[12:15], v[184:187]
v_accvgpr_read_b32 v73, a93
v_mul_f32 v72, v52, v72
v_mfma_f32_16x16x32_bf16 v[188:191], a[32:35], a[12:15], v[188:191]
v_mul_f32 v73, v52, v73
v_cvt_pk_bf16_f32 v70, v72, v73
v_mfma_f32_16x16x32_bf16 v[224:227], a[12:15], a[36:39], v[224:227]
buffer_load_dwordx4 v42, s[12:15], s25 offen offset:2048 lds
v_accvgpr_read_b32 v74, a94
v_mfma_f32_16x16x32_bf16 v[228:231], a[12:15], a[40:43], v[228:231]
v_accvgpr_read_b32 v75, a95
v_mul_f32 v74, v52, v74
v_mfma_f32_16x16x32_bf16 v[152:155], a[20:23], a[16:19], v[152:155]
s_add_u32 s26, s26, 393216
s_add_u32 s47, s47, 393216
s_add_u32 s25, s25, 128
v_mfma_f32_16x16x32_bf16 v[156:159], a[24:27], a[16:19], v[156:159]
v_mul_f32 v75, v52, v75
v_cvt_pk_bf16_f32 v71, v74, v75
v_mfma_f32_16x16x32_bf16 v[192:195], a[28:31], a[16:19], v[192:195]
buffer_store_dwordx4 v[68:71], v7, s[20:23], s40 offen offset:3072 sc1
v_mfma_f32_16x16x32_bf16 v[196:199], a[32:35], a[16:19], v[196:199]
v_mfma_f32_16x16x32_bf16 v[232:235], a[16:19], a[36:39], v[232:235]
v_mfma_f32_16x16x32_bf16 v[236:239], a[16:19], a[40:43], v[236:239]
s_waitcnt lgkmcnt(0)
v_mfma_f32_16x16x32_bf16 v[160:163], a[172:175], a[128:131], v[160:163]
v_mfma_f32_16x16x32_bf16 v[164:167], a[192:195], a[128:131], v[164:167]
s_add_u32 m0, s91, 81920
ds_read_b64_tr_b16 a[20:21], v47 offset:14336
ds_read_b64_tr_b16 a[22:23], v47 offset:14848
v_mfma_f32_16x16x32_bf16 v[200:203], a[128:131], a[196:199], v[200:203]
buffer_load_dwordx4 v36, s[12:15], s24 offen offset:0 lds
v_accvgpr_read_b32 v72, a112
v_mfma_f32_16x16x32_bf16 v[204:207], a[128:131], a[200:203], v[204:207]
ds_read_b128 a[0:3], v46
v_accvgpr_read_b32 v73, a113
v_mfma_f32_16x16x32_bf16 v[128:131], a[164:167], a[132:135], v[128:131]
ds_read_b64_tr_b16 a[24:25], v48 offset:14336
ds_read_b64_tr_b16 a[26:27], v48 offset:14848
v_mfma_f32_16x16x32_bf16 v[132:135], a[168:171], a[132:135], v[132:135]
v_mul_f32 v72, v52, v72
v_mul_f32 v73, v52, v73
v_mfma_f32_16x16x32_bf16 v[168:171], a[172:175], a[132:135], v[168:171]
ds_read_b128 a[4:7], v44 offset:0
v_cvt_pk_bf16_f32 v64, v72, v73
v_mfma_f32_16x16x32_bf16 v[172:175], a[192:195], a[132:135], v[172:175]
ds_read_b64_tr_b16 a[28:29], v47 offset:16384
ds_read_b64_tr_b16 a[30:31], v47 offset:16896
v_mfma_f32_16x16x32_bf16 v[208:211], a[132:135], a[196:199], v[208:211]
buffer_load_dwordx4 v37, s[12:15], s24 offen offset:1024 lds
v_accvgpr_read_b32 v74, a114
v_mfma_f32_16x16x32_bf16 v[212:215], a[132:135], a[200:203], v[212:215]
ds_read_b128 a[8:11], v44 offset:2048
v_accvgpr_read_b32 v75, a115
v_mfma_f32_16x16x32_bf16 v[136:139], a[164:167], a[136:139], v[136:139]
ds_read_b64_tr_b16 a[32:33], v48 offset:16384
ds_read_b64_tr_b16 a[34:35], v48 offset:16896
v_mfma_f32_16x16x32_bf16 v[140:143], a[168:171], a[136:139], v[140:143]
v_mul_f32 v74, v52, v74
v_mul_f32 v75, v52, v75
v_mfma_f32_16x16x32_bf16 v[176:179], a[172:175], a[136:139], v[176:179]
ds_read_b128 a[12:15], v44 offset:4096
v_cvt_pk_bf16_f32 v65, v74, v75
v_mfma_f32_16x16x32_bf16 v[180:183], a[192:195], a[136:139], v[180:183]
ds_read_b64_tr_b16 a[36:37], v49 offset:18432
ds_read_b64_tr_b16 a[38:39], v49 offset:18944
v_mfma_f32_16x16x32_bf16 v[216:219], a[136:139], a[196:199], v[216:219]
buffer_load_dwordx4 v38, s[12:15], s24 offen offset:2048 lds
v_accvgpr_read_b32 v72, a116
v_mfma_f32_16x16x32_bf16 v[220:223], a[136:139], a[200:203], v[220:223]
ds_read_b128 a[16:19], v44 offset:6144
v_accvgpr_read_b32 v73, a117
v_mfma_f32_16x16x32_bf16 v[144:147], a[164:167], a[140:143], v[144:147]
ds_read_b64_tr_b16 a[40:41], v50 offset:18432
ds_read_b64_tr_b16 a[42:43], v50 offset:18944
v_mfma_f32_16x16x32_bf16 v[148:151], a[168:171], a[140:143], v[148:151]
s_cmp_eq_u32 s39, 1
s_mov_b32 s28, 16384
s_cselect_b32 s28, 98304, s28
v_mfma_f32_16x16x32_bf16 v[184:187], a[172:175], a[140:143], v[184:187]
v_add_u32 v43, 16384, v43
v_add_u32 v44, 16384, v44
v_mfma_f32_16x16x32_bf16 v[188:191], a[192:195], a[140:143], v[188:191]
v_add_u32 v47, 98304, v47
v_add_u32 v48, 98304, v48
v_mfma_f32_16x16x32_bf16 v[224:227], a[140:143], a[196:199], v[224:227]
buffer_load_dwordx4 v39, s[12:15], s24 offen offset:3072 lds
v_add_u32 v49, 98304, v49
v_add_u32 v50, 98304, v50
v_mfma_f32_16x16x32_bf16 v[228:231], a[140:143], a[200:203], v[228:231]
v_add_u32 v45, s28, v45
v_add_u32 v46, s28, v46
v_mfma_f32_16x16x32_bf16 v[152:155], a[164:167], a[160:163], v[152:155]
v_mul_f32 v72, v52, v72
v_mul_f32 v73, v52, v73
v_mfma_f32_16x16x32_bf16 v[156:159], a[168:171], a[160:163], v[156:159]
s_add_u32 s24, s24, 128
v_cvt_pk_bf16_f32 v66, v72, v73
v_accvgpr_read_b32 v74, a118
v_mfma_f32_16x16x32_bf16 v[192:195], a[172:175], a[160:163], v[192:195]
v_accvgpr_read_b32 v75, a119
v_mul_f32 v74, v52, v74
v_mfma_f32_16x16x32_bf16 v[196:199], a[192:195], a[160:163], v[196:199]
v_mul_f32 v75, v52, v75
v_cvt_pk_bf16_f32 v67, v74, v75
v_mfma_f32_16x16x32_bf16 v[232:235], a[160:163], a[196:199], v[232:235]
buffer_store_dwordx4 v[64:67], v7, s[20:23], s41 offen offset:0 sc1
v_mfma_f32_16x16x32_bf16 v[236:239], a[160:163], a[200:203], v[236:239]
s_waitcnt vmcnt(19) lgkmcnt(0)
s_barrier
v_mfma_f32_16x16x32_bf16 v[160:163], a[28:31], a[0:3], v[160:163]
v_mfma_f32_16x16x32_bf16 v[164:167], a[32:35], a[0:3], v[164:167]
s_add_u32 m0, s48, 32768
ds_read_b64_tr_b16 a[164:165], v47 offset:0
ds_read_b64_tr_b16 a[166:167], v47 offset:512
v_mfma_f32_16x16x32_bf16 v[200:203], a[0:3], a[36:39], v[200:203]
buffer_load_dwordx4 v40, s[16:19], s26 offen offset:0 lds
v_accvgpr_read_b32 v72, a120
v_mfma_f32_16x16x32_bf16 v[204:207], a[0:3], a[40:43], v[204:207]
ds_read_b128 a[128:131], v45
v_accvgpr_read_b32 v73, a121
v_mfma_f32_16x16x32_bf16 v[128:131], a[20:23], a[4:7], v[128:131]
ds_read_b64_tr_b16 a[168:169], v48 offset:0
ds_read_b64_tr_b16 a[170:171], v48 offset:512
v_mfma_f32_16x16x32_bf16 v[132:135], a[24:27], a[4:7], v[132:135]
buffer_load_dwordx4 v41, s[16:19], s26 offen offset:1024 lds
v_mul_f32 v72, v52, v72
v_mfma_f32_16x16x32_bf16 v[168:171], a[28:31], a[4:7], v[168:171]
ds_read_b128 a[132:135], v43 offset:0
v_mul_f32 v73, v52, v73
v_mfma_f32_16x16x32_bf16 v[172:175], a[32:35], a[4:7], v[172:175]
ds_read_b64_tr_b16 a[172:173], v47 offset:2048
ds_read_b64_tr_b16 a[174:175], v47 offset:2560
v_mfma_f32_16x16x32_bf16 v[208:211], a[4:7], a[36:39], v[208:211]
buffer_load_dwordx4 v40, s[16:19], s26 offen offset:2048 lds
v_cvt_pk_bf16_f32 v68, v72, v73
v_mfma_f32_16x16x32_bf16 v[212:215], a[4:7], a[40:43], v[212:215]
ds_read_b128 a[136:139], v43 offset:2048
v_accvgpr_read_b32 v74, a122
v_mfma_f32_16x16x32_bf16 v[136:139], a[20:23], a[8:11], v[136:139]
ds_read_b64_tr_b16 a[192:193], v48 offset:2048
ds_read_b64_tr_b16 a[194:195], v48 offset:2560
v_mfma_f32_16x16x32_bf16 v[140:143], a[24:27], a[8:11], v[140:143]
buffer_load_dwordx4 v41, s[16:19], s26 offen offset:3072 lds
v_accvgpr_read_b32 v75, a123
v_mfma_f32_16x16x32_bf16 v[176:179], a[28:31], a[8:11], v[176:179]
ds_read_b128 a[140:143], v43 offset:4096
v_mul_f32 v74, v52, v74
v_mfma_f32_16x16x32_bf16 v[180:183], a[32:35], a[8:11], v[180:183]
s_add_u32 m0, s48, 36864
ds_read_b64_tr_b16 a[196:197], v49 offset:4096
ds_read_b64_tr_b16 a[198:199], v49 offset:4608
v_mfma_f32_16x16x32_bf16 v[216:219], a[8:11], a[36:39], v[216:219]
buffer_load_dwordx4 v40, s[16:19], s47 offen offset:0 lds
v_mul_f32 v75, v52, v75
v_mfma_f32_16x16x32_bf16 v[220:223], a[8:11], a[40:43], v[220:223]
ds_read_b128 a[160:163], v43 offset:6144
v_cvt_pk_bf16_f32 v69, v74, v75
v_mfma_f32_16x16x32_bf16 v[144:147], a[20:23], a[12:15], v[144:147]
ds_read_b64_tr_b16 a[200:201], v50 offset:4096
ds_read_b64_tr_b16 a[202:203], v50 offset:4608
v_mfma_f32_16x16x32_bf16 v[148:151], a[24:27], a[12:15], v[148:151]
buffer_load_dwordx4 v41, s[16:19], s47 offen offset:1024 lds
v_accvgpr_read_b32 v72, a124
v_mfma_f32_16x16x32_bf16 v[184:187], a[28:31], a[12:15], v[184:187]
v_accvgpr_read_b32 v73, a125
v_mul_f32 v72, v52, v72
v_mfma_f32_16x16x32_bf16 v[188:191], a[32:35], a[12:15], v[188:191]
v_mul_f32 v73, v52, v73
v_cvt_pk_bf16_f32 v70, v72, v73
v_mfma_f32_16x16x32_bf16 v[224:227], a[12:15], a[36:39], v[224:227]
buffer_load_dwordx4 v42, s[12:15], s25 offen offset:2048 lds
v_accvgpr_read_b32 v74, a126
v_mfma_f32_16x16x32_bf16 v[228:231], a[12:15], a[40:43], v[228:231]
v_accvgpr_read_b32 v75, a127
v_mul_f32 v74, v52, v74
v_mfma_f32_16x16x32_bf16 v[152:155], a[20:23], a[16:19], v[152:155]
s_add_u32 s26, s26, 393216
s_add_u32 s47, s47, 393216
s_add_u32 s25, s25, 128
v_mfma_f32_16x16x32_bf16 v[156:159], a[24:27], a[16:19], v[156:159]
v_mul_f32 v75, v52, v75
v_cvt_pk_bf16_f32 v71, v74, v75
v_mfma_f32_16x16x32_bf16 v[192:195], a[28:31], a[16:19], v[192:195]
buffer_store_dwordx4 v[68:71], v7, s[20:23], s41 offen offset:1024 sc1
v_mfma_f32_16x16x32_bf16 v[196:199], a[32:35], a[16:19], v[196:199]
v_mfma_f32_16x16x32_bf16 v[232:235], a[16:19], a[36:39], v[232:235]
v_mfma_f32_16x16x32_bf16 v[236:239], a[16:19], a[40:43], v[236:239]
s_waitcnt lgkmcnt(0)
v_mfma_f32_16x16x32_bf16 v[160:163], a[172:175], a[128:131], v[160:163]
v_mfma_f32_16x16x32_bf16 v[164:167], a[192:195], a[128:131], v[164:167]
s_add_u32 m0, s91, 0
ds_read_b64_tr_b16 a[20:21], v47 offset:14336
ds_read_b64_tr_b16 a[22:23], v47 offset:14848
v_mfma_f32_16x16x32_bf16 v[200:203], a[128:131], a[196:199], v[200:203]
buffer_load_dwordx4 v36, s[12:15], s24 offen offset:0 lds
v_accvgpr_read_b32 v72, a144
v_mfma_f32_16x16x32_bf16 v[204:207], a[128:131], a[200:203], v[204:207]
ds_read_b128 a[0:3], v46
v_accvgpr_read_b32 v73, a145
v_mfma_f32_16x16x32_bf16 v[128:131], a[164:167], a[132:135], v[128:131]
ds_read_b64_tr_b16 a[24:25], v48 offset:14336
ds_read_b64_tr_b16 a[26:27], v48 offset:14848
v_mfma_f32_16x16x32_bf16 v[132:135], a[168:171], a[132:135], v[132:135]
v_mul_f32 v72, v52, v72
v_mul_f32 v73, v52, v73
v_mfma_f32_16x16x32_bf16 v[168:171], a[172:175], a[132:135], v[168:171]
ds_read_b128 a[4:7], v44 offset:0
v_cvt_pk_bf16_f32 v64, v72, v73
v_mfma_f32_16x16x32_bf16 v[172:175], a[192:195], a[132:135], v[172:175]
ds_read_b64_tr_b16 a[28:29], v47 offset:16384
ds_read_b64_tr_b16 a[30:31], v47 offset:16896
v_mfma_f32_16x16x32_bf16 v[208:211], a[132:135], a[196:199], v[208:211]
buffer_load_dwordx4 v37, s[12:15], s24 offen offset:1024 lds
v_accvgpr_read_b32 v74, a146
v_mfma_f32_16x16x32_bf16 v[212:215], a[132:135], a[200:203], v[212:215]
ds_read_b128 a[8:11], v44 offset:2048
v_accvgpr_read_b32 v75, a147
v_mfma_f32_16x16x32_bf16 v[136:139], a[164:167], a[136:139], v[136:139]
ds_read_b64_tr_b16 a[32:33], v48 offset:16384
ds_read_b64_tr_b16 a[34:35], v48 offset:16896
v_mfma_f32_16x16x32_bf16 v[140:143], a[168:171], a[136:139], v[140:143]
v_mul_f32 v74, v52, v74
v_mul_f32 v75, v52, v75
v_mfma_f32_16x16x32_bf16 v[176:179], a[172:175], a[136:139], v[176:179]
ds_read_b128 a[12:15], v44 offset:4096
v_cvt_pk_bf16_f32 v65, v74, v75
v_mfma_f32_16x16x32_bf16 v[180:183], a[192:195], a[136:139], v[180:183]
ds_read_b64_tr_b16 a[36:37], v49 offset:18432
ds_read_b64_tr_b16 a[38:39], v49 offset:18944
v_mfma_f32_16x16x32_bf16 v[216:219], a[136:139], a[196:199], v[216:219]
buffer_load_dwordx4 v38, s[12:15], s24 offen offset:2048 lds
v_accvgpr_read_b32 v72, a148
v_mfma_f32_16x16x32_bf16 v[220:223], a[136:139], a[200:203], v[220:223]
ds_read_b128 a[16:19], v44 offset:6144
v_accvgpr_read_b32 v73, a149
v_mfma_f32_16x16x32_bf16 v[144:147], a[164:167], a[140:143], v[144:147]
ds_read_b64_tr_b16 a[40:41], v50 offset:18432
ds_read_b64_tr_b16 a[42:43], v50 offset:18944
v_mfma_f32_16x16x32_bf16 v[148:151], a[168:171], a[140:143], v[148:151]
s_cmp_eq_u32 s39, 1
s_mov_b32 s28, 49152
s_cselect_b32 s28, 4294934528, s28
v_mfma_f32_16x16x32_bf16 v[184:187], a[172:175], a[140:143], v[184:187]
v_add_u32 v43, 49152, v43
v_add_u32 v44, 49152, v44
v_mfma_f32_16x16x32_bf16 v[188:191], a[192:195], a[140:143], v[188:191]
v_add_u32 v47, 4294934528, v47
v_add_u32 v48, 4294934528, v48
v_mfma_f32_16x16x32_bf16 v[224:227], a[140:143], a[196:199], v[224:227]
buffer_load_dwordx4 v39, s[12:15], s24 offen offset:3072 lds
v_add_u32 v49, 4294934528, v49
v_add_u32 v50, 4294934528, v50
v_mfma_f32_16x16x32_bf16 v[228:231], a[140:143], a[200:203], v[228:231]
v_add_u32 v45, s28, v45
v_add_u32 v46, s28, v46
v_mfma_f32_16x16x32_bf16 v[152:155], a[164:167], a[160:163], v[152:155]
v_mul_f32 v72, v52, v72
v_mul_f32 v73, v52, v73
v_mfma_f32_16x16x32_bf16 v[156:159], a[168:171], a[160:163], v[156:159]
s_add_u32 s24, s24, 128
v_cvt_pk_bf16_f32 v66, v72, v73
v_accvgpr_read_b32 v74, a150
v_mfma_f32_16x16x32_bf16 v[192:195], a[172:175], a[160:163], v[192:195]
v_accvgpr_read_b32 v75, a151
v_mul_f32 v74, v52, v74
v_mfma_f32_16x16x32_bf16 v[196:199], a[192:195], a[160:163], v[196:199]
v_mul_f32 v75, v52, v75
v_cvt_pk_bf16_f32 v67, v74, v75
v_mfma_f32_16x16x32_bf16 v[232:235], a[160:163], a[196:199], v[232:235]
buffer_store_dwordx4 v[64:67], v7, s[20:23], s41 offen offset:2048 sc1
v_mfma_f32_16x16x32_bf16 v[236:239], a[160:163], a[200:203], v[236:239]
s_waitcnt vmcnt(19) lgkmcnt(0)
s_barrier
v_mfma_f32_16x16x32_bf16 v[160:163], a[28:31], a[0:3], v[160:163]
v_mfma_f32_16x16x32_bf16 v[164:167], a[32:35], a[0:3], v[164:167]
s_add_u32 m0, s48, 131072
ds_read_b64_tr_b16 a[164:165], v47 offset:0
ds_read_b64_tr_b16 a[166:167], v47 offset:512
v_mfma_f32_16x16x32_bf16 v[200:203], a[0:3], a[36:39], v[200:203]
buffer_load_dwordx4 v40, s[16:19], s26 offen offset:0 lds
v_accvgpr_read_b32 v72, a152
v_mfma_f32_16x16x32_bf16 v[204:207], a[0:3], a[40:43], v[204:207]
ds_read_b128 a[128:131], v45
v_accvgpr_read_b32 v73, a153
v_mfma_f32_16x16x32_bf16 v[128:131], a[20:23], a[4:7], v[128:131]
ds_read_b64_tr_b16 a[168:169], v48 offset:0
ds_read_b64_tr_b16 a[170:171], v48 offset:512
v_mfma_f32_16x16x32_bf16 v[132:135], a[24:27], a[4:7], v[132:135]
buffer_load_dwordx4 v41, s[16:19], s26 offen offset:1024 lds
v_mul_f32 v72, v52, v72
v_mfma_f32_16x16x32_bf16 v[168:171], a[28:31], a[4:7], v[168:171]
ds_read_b128 a[132:135], v43 offset:0
v_mul_f32 v73, v52, v73
v_mfma_f32_16x16x32_bf16 v[172:175], a[32:35], a[4:7], v[172:175]
ds_read_b64_tr_b16 a[172:173], v47 offset:2048
ds_read_b64_tr_b16 a[174:175], v47 offset:2560
v_mfma_f32_16x16x32_bf16 v[208:211], a[4:7], a[36:39], v[208:211]
buffer_load_dwordx4 v40, s[16:19], s26 offen offset:2048 lds
v_cvt_pk_bf16_f32 v68, v72, v73
v_mfma_f32_16x16x32_bf16 v[212:215], a[4:7], a[40:43], v[212:215]
ds_read_b128 a[136:139], v43 offset:2048
v_accvgpr_read_b32 v74, a154
v_mfma_f32_16x16x32_bf16 v[136:139], a[20:23], a[8:11], v[136:139]
ds_read_b64_tr_b16 a[192:193], v48 offset:2048
ds_read_b64_tr_b16 a[194:195], v48 offset:2560
v_mfma_f32_16x16x32_bf16 v[140:143], a[24:27], a[8:11], v[140:143]
buffer_load_dwordx4 v41, s[16:19], s26 offen offset:3072 lds
v_accvgpr_read_b32 v75, a155
v_mfma_f32_16x16x32_bf16 v[176:179], a[28:31], a[8:11], v[176:179]
ds_read_b128 a[140:143], v43 offset:4096
v_mul_f32 v74, v52, v74
v_mfma_f32_16x16x32_bf16 v[180:183], a[32:35], a[8:11], v[180:183]
s_add_u32 m0, s48, 135168
ds_read_b64_tr_b16 a[196:197], v49 offset:4096
ds_read_b64_tr_b16 a[198:199], v49 offset:4608
v_mfma_f32_16x16x32_bf16 v[216:219], a[8:11], a[36:39], v[216:219]
buffer_load_dwordx4 v40, s[16:19], s47 offen offset:0 lds
v_mul_f32 v75, v52, v75
v_mfma_f32_16x16x32_bf16 v[220:223], a[8:11], a[40:43], v[220:223]
ds_read_b128 a[160:163], v43 offset:6144
v_cvt_pk_bf16_f32 v69, v74, v75
v_mfma_f32_16x16x32_bf16 v[144:147], a[20:23], a[12:15], v[144:147]
ds_read_b64_tr_b16 a[200:201], v50 offset:4096
ds_read_b64_tr_b16 a[202:203], v50 offset:4608
v_mfma_f32_16x16x32_bf16 v[148:151], a[24:27], a[12:15], v[148:151]
buffer_load_dwordx4 v41, s[16:19], s47 offen offset:1024 lds
v_accvgpr_read_b32 v72, a156
v_mfma_f32_16x16x32_bf16 v[184:187], a[28:31], a[12:15], v[184:187]
v_accvgpr_read_b32 v73, a157
v_mul_f32 v72, v52, v72
v_mfma_f32_16x16x32_bf16 v[188:191], a[32:35], a[12:15], v[188:191]
v_mul_f32 v73, v52, v73
v_cvt_pk_bf16_f32 v70, v72, v73
v_mfma_f32_16x16x32_bf16 v[224:227], a[12:15], a[36:39], v[224:227]
buffer_load_dwordx4 v42, s[12:15], s25 offen offset:2048 lds
v_accvgpr_read_b32 v74, a158
v_mfma_f32_16x16x32_bf16 v[228:231], a[12:15], a[40:43], v[228:231]
v_accvgpr_read_b32 v75, a159
v_mul_f32 v74, v52, v74
v_mfma_f32_16x16x32_bf16 v[152:155], a[20:23], a[16:19], v[152:155]
s_add_u32 s26, s26, 393216
s_add_u32 s47, s47, 393216
s_add_u32 s25, s25, 128
v_mfma_f32_16x16x32_bf16 v[156:159], a[24:27], a[16:19], v[156:159]
v_mul_f32 v75, v52, v75
v_cvt_pk_bf16_f32 v71, v74, v75
v_mfma_f32_16x16x32_bf16 v[192:195], a[28:31], a[16:19], v[192:195]
buffer_store_dwordx4 v[68:71], v7, s[20:23], s41 offen offset:3072 sc1
v_mfma_f32_16x16x32_bf16 v[196:199], a[32:35], a[16:19], v[196:199]
v_mfma_f32_16x16x32_bf16 v[232:235], a[16:19], a[36:39], v[232:235]
v_mfma_f32_16x16x32_bf16 v[236:239], a[16:19], a[40:43], v[236:239]
s_waitcnt lgkmcnt(0)
v_mfma_f32_16x16x32_bf16 v[160:163], a[172:175], a[128:131], v[160:163]
v_mfma_f32_16x16x32_bf16 v[164:167], a[192:195], a[128:131], v[164:167]
s_add_u32 m0, s91, 16384
ds_read_b64_tr_b16 a[20:21], v47 offset:14336
ds_read_b64_tr_b16 a[22:23], v47 offset:14848
v_mfma_f32_16x16x32_bf16 v[200:203], a[128:131], a[196:199], v[200:203]
buffer_load_dwordx4 v36, s[12:15], s24 offen offset:0 lds
v_accvgpr_read_b32 v72, a176
v_mfma_f32_16x16x32_bf16 v[204:207], a[128:131], a[200:203], v[204:207]
ds_read_b128 a[0:3], v46
v_accvgpr_read_b32 v73, a177
v_mfma_f32_16x16x32_bf16 v[128:131], a[164:167], a[132:135], v[128:131]
ds_read_b64_tr_b16 a[24:25], v48 offset:14336
ds_read_b64_tr_b16 a[26:27], v48 offset:14848
v_mfma_f32_16x16x32_bf16 v[132:135], a[168:171], a[132:135], v[132:135]
v_mul_f32 v72, v53, v72
v_mul_f32 v73, v53, v73
v_mfma_f32_16x16x32_bf16 v[168:171], a[172:175], a[132:135], v[168:171]
ds_read_b128 a[4:7], v44 offset:0
v_cvt_pk_bf16_f32 v64, v72, v73
v_mfma_f32_16x16x32_bf16 v[172:175], a[192:195], a[132:135], v[172:175]
ds_read_b64_tr_b16 a[28:29], v47 offset:16384
ds_read_b64_tr_b16 a[30:31], v47 offset:16896
v_mfma_f32_16x16x32_bf16 v[208:211], a[132:135], a[196:199], v[208:211]
buffer_load_dwordx4 v37, s[12:15], s24 offen offset:1024 lds
v_accvgpr_read_b32 v74, a178
v_mfma_f32_16x16x32_bf16 v[212:215], a[132:135], a[200:203], v[212:215]
ds_read_b128 a[8:11], v44 offset:2048
v_accvgpr_read_b32 v75, a179
v_mfma_f32_16x16x32_bf16 v[136:139], a[164:167], a[136:139], v[136:139]
ds_read_b64_tr_b16 a[32:33], v48 offset:16384
ds_read_b64_tr_b16 a[34:35], v48 offset:16896
v_mfma_f32_16x16x32_bf16 v[140:143], a[168:171], a[136:139], v[140:143]
v_mul_f32 v74, v53, v74
v_mul_f32 v75, v53, v75
v_mfma_f32_16x16x32_bf16 v[176:179], a[172:175], a[136:139], v[176:179]
ds_read_b128 a[12:15], v44 offset:4096
v_cvt_pk_bf16_f32 v65, v74, v75
v_mfma_f32_16x16x32_bf16 v[180:183], a[192:195], a[136:139], v[180:183]
ds_read_b64_tr_b16 a[36:37], v49 offset:18432
ds_read_b64_tr_b16 a[38:39], v49 offset:18944
v_mfma_f32_16x16x32_bf16 v[216:219], a[136:139], a[196:199], v[216:219]
buffer_load_dwordx4 v38, s[12:15], s24 offen offset:2048 lds
v_accvgpr_read_b32 v72, a180
v_mfma_f32_16x16x32_bf16 v[220:223], a[136:139], a[200:203], v[220:223]
ds_read_b128 a[16:19], v44 offset:6144
v_accvgpr_read_b32 v73, a181
v_mfma_f32_16x16x32_bf16 v[144:147], a[164:167], a[140:143], v[144:147]
ds_read_b64_tr_b16 a[40:41], v50 offset:18432
ds_read_b64_tr_b16 a[42:43], v50 offset:18944
v_mfma_f32_16x16x32_bf16 v[148:151], a[168:171], a[140:143], v[148:151]
s_cmp_eq_u32 s39, 1
s_mov_b32 s28, 16384
s_cselect_b32 s28, 4294901760, s28
v_mfma_f32_16x16x32_bf16 v[184:187], a[172:175], a[140:143], v[184:187]
v_add_u32 v43, 16384, v43
v_add_u32 v44, 16384, v44
v_mfma_f32_16x16x32_bf16 v[188:191], a[192:195], a[140:143], v[188:191]
v_add_u32 v47, 4294901760, v47
v_add_u32 v48, 4294901760, v48
v_mfma_f32_16x16x32_bf16 v[224:227], a[140:143], a[196:199], v[224:227]
buffer_load_dwordx4 v39, s[12:15], s24 offen offset:3072 lds
v_add_u32 v49, 4294901760, v49
v_add_u32 v50, 4294901760, v50
v_mfma_f32_16x16x32_bf16 v[228:231], a[140:143], a[200:203], v[228:231]
v_add_u32 v45, s28, v45
v_add_u32 v46, s28, v46
v_mfma_f32_16x16x32_bf16 v[152:155], a[164:167], a[160:163], v[152:155]
v_mul_f32 v72, v53, v72
v_mul_f32 v73, v53, v73
v_mfma_f32_16x16x32_bf16 v[156:159], a[168:171], a[160:163], v[156:159]
s_add_u32 s24, s24, 128
v_cvt_pk_bf16_f32 v66, v72, v73
v_accvgpr_read_b32 v74, a182
v_mfma_f32_16x16x32_bf16 v[192:195], a[172:175], a[160:163], v[192:195]
v_accvgpr_read_b32 v75, a183
v_mul_f32 v74, v53, v74
v_mfma_f32_16x16x32_bf16 v[196:199], a[192:195], a[160:163], v[196:199]
v_mul_f32 v75, v53, v75
v_cvt_pk_bf16_f32 v67, v74, v75
v_mfma_f32_16x16x32_bf16 v[232:235], a[160:163], a[196:199], v[232:235]
buffer_store_dwordx4 v[64:67], v7, s[20:23], s42 offen offset:0 sc1
v_mfma_f32_16x16x32_bf16 v[236:239], a[160:163], a[200:203], v[236:239]
s_waitcnt vmcnt(19) lgkmcnt(0)
s_barrier
v_mfma_f32_16x16x32_bf16 v[160:163], a[28:31], a[0:3], v[160:163]
v_mfma_f32_16x16x32_bf16 v[164:167], a[32:35], a[0:3], v[164:167]
s_add_u32 m0, s48, 98304
ds_read_b64_tr_b16 a[164:165], v47 offset:0
ds_read_b64_tr_b16 a[166:167], v47 offset:512
v_mfma_f32_16x16x32_bf16 v[200:203], a[0:3], a[36:39], v[200:203]
buffer_load_dwordx4 v40, s[16:19], s26 offen offset:0 lds
v_accvgpr_read_b32 v72, a184
v_mfma_f32_16x16x32_bf16 v[204:207], a[0:3], a[40:43], v[204:207]
ds_read_b128 a[128:131], v45
v_accvgpr_read_b32 v73, a185
v_mfma_f32_16x16x32_bf16 v[128:131], a[20:23], a[4:7], v[128:131]
ds_read_b64_tr_b16 a[168:169], v48 offset:0
ds_read_b64_tr_b16 a[170:171], v48 offset:512
v_mfma_f32_16x16x32_bf16 v[132:135], a[24:27], a[4:7], v[132:135]
buffer_load_dwordx4 v41, s[16:19], s26 offen offset:1024 lds
v_mul_f32 v72, v53, v72
v_mfma_f32_16x16x32_bf16 v[168:171], a[28:31], a[4:7], v[168:171]
ds_read_b128 a[132:135], v43 offset:0
v_mul_f32 v73, v53, v73
v_mfma_f32_16x16x32_bf16 v[172:175], a[32:35], a[4:7], v[172:175]
ds_read_b64_tr_b16 a[172:173], v47 offset:2048
ds_read_b64_tr_b16 a[174:175], v47 offset:2560
v_mfma_f32_16x16x32_bf16 v[208:211], a[4:7], a[36:39], v[208:211]
buffer_load_dwordx4 v40, s[16:19], s26 offen offset:2048 lds
v_cvt_pk_bf16_f32 v68, v72, v73
v_mfma_f32_16x16x32_bf16 v[212:215], a[4:7], a[40:43], v[212:215]
ds_read_b128 a[136:139], v43 offset:2048
v_accvgpr_read_b32 v74, a186
v_mfma_f32_16x16x32_bf16 v[136:139], a[20:23], a[8:11], v[136:139]
ds_read_b64_tr_b16 a[192:193], v48 offset:2048
ds_read_b64_tr_b16 a[194:195], v48 offset:2560
v_mfma_f32_16x16x32_bf16 v[140:143], a[24:27], a[8:11], v[140:143]
buffer_load_dwordx4 v41, s[16:19], s26 offen offset:3072 lds
v_accvgpr_read_b32 v75, a187
v_mfma_f32_16x16x32_bf16 v[176:179], a[28:31], a[8:11], v[176:179]
ds_read_b128 a[140:143], v43 offset:4096
v_mul_f32 v74, v53, v74
v_mfma_f32_16x16x32_bf16 v[180:183], a[32:35], a[8:11], v[180:183]
s_add_u32 m0, s48, 102400
ds_read_b64_tr_b16 a[196:197], v49 offset:4096
ds_read_b64_tr_b16 a[198:199], v49 offset:4608
v_mfma_f32_16x16x32_bf16 v[216:219], a[8:11], a[36:39], v[216:219]
buffer_load_dwordx4 v40, s[16:19], s47 offen offset:0 lds
v_mul_f32 v75, v53, v75
v_mfma_f32_16x16x32_bf16 v[220:223], a[8:11], a[40:43], v[220:223]
ds_read_b128 a[160:163], v43 offset:6144
v_cvt_pk_bf16_f32 v69, v74, v75
v_mfma_f32_16x16x32_bf16 v[144:147], a[20:23], a[12:15], v[144:147]
ds_read_b64_tr_b16 a[200:201], v50 offset:4096
ds_read_b64_tr_b16 a[202:203], v50 offset:4608
v_mfma_f32_16x16x32_bf16 v[148:151], a[24:27], a[12:15], v[148:151]
buffer_load_dwordx4 v41, s[16:19], s47 offen offset:1024 lds
v_accvgpr_read_b32 v72, a188
v_mfma_f32_16x16x32_bf16 v[184:187], a[28:31], a[12:15], v[184:187]
v_accvgpr_read_b32 v73, a189
v_mul_f32 v72, v53, v72
v_mfma_f32_16x16x32_bf16 v[188:191], a[32:35], a[12:15], v[188:191]
v_mul_f32 v73, v53, v73
v_cvt_pk_bf16_f32 v70, v72, v73
v_mfma_f32_16x16x32_bf16 v[224:227], a[12:15], a[36:39], v[224:227]
buffer_load_dwordx4 v42, s[12:15], s25 offen offset:2048 lds
v_accvgpr_read_b32 v74, a190
v_mfma_f32_16x16x32_bf16 v[228:231], a[12:15], a[40:43], v[228:231]
v_accvgpr_read_b32 v75, a191
v_mul_f32 v74, v53, v74
v_mfma_f32_16x16x32_bf16 v[152:155], a[20:23], a[16:19], v[152:155]
s_add_u32 s26, s26, 393216
s_add_u32 s47, s47, 393216
s_add_u32 s25, s25, 128
v_mfma_f32_16x16x32_bf16 v[156:159], a[24:27], a[16:19], v[156:159]
v_mul_f32 v75, v53, v75
v_cvt_pk_bf16_f32 v71, v74, v75
v_mfma_f32_16x16x32_bf16 v[192:195], a[28:31], a[16:19], v[192:195]
buffer_store_dwordx4 v[68:71], v7, s[20:23], s42 offen offset:1024 sc1
v_mfma_f32_16x16x32_bf16 v[196:199], a[32:35], a[16:19], v[196:199]
v_mfma_f32_16x16x32_bf16 v[232:235], a[16:19], a[36:39], v[232:235]
v_mfma_f32_16x16x32_bf16 v[236:239], a[16:19], a[40:43], v[236:239]
s_waitcnt lgkmcnt(0)
v_mfma_f32_16x16x32_bf16 v[160:163], a[172:175], a[128:131], v[160:163]
v_mfma_f32_16x16x32_bf16 v[164:167], a[192:195], a[128:131], v[164:167]
s_add_u32 m0, s91, 65536
ds_read_b64_tr_b16 a[20:21], v47 offset:14336
ds_read_b64_tr_b16 a[22:23], v47 offset:14848
v_mfma_f32_16x16x32_bf16 v[200:203], a[128:131], a[196:199], v[200:203]
buffer_load_dwordx4 v36, s[12:15], s24 offen offset:0 lds
v_accvgpr_read_b32 v72, a208
v_mfma_f32_16x16x32_bf16 v[204:207], a[128:131], a[200:203], v[204:207]
ds_read_b128 a[0:3], v46
v_accvgpr_read_b32 v73, a209
v_mfma_f32_16x16x32_bf16 v[128:131], a[164:167], a[132:135], v[128:131]
ds_read_b64_tr_b16 a[24:25], v48 offset:14336
ds_read_b64_tr_b16 a[26:27], v48 offset:14848
v_mfma_f32_16x16x32_bf16 v[132:135], a[168:171], a[132:135], v[132:135]
v_mul_f32 v72, v53, v72
v_mul_f32 v73, v53, v73
v_mfma_f32_16x16x32_bf16 v[168:171], a[172:175], a[132:135], v[168:171]
ds_read_b128 a[4:7], v44 offset:0
v_cvt_pk_bf16_f32 v64, v72, v73
v_mfma_f32_16x16x32_bf16 v[172:175], a[192:195], a[132:135], v[172:175]
ds_read_b64_tr_b16 a[28:29], v47 offset:16384
ds_read_b64_tr_b16 a[30:31], v47 offset:16896
v_mfma_f32_16x16x32_bf16 v[208:211], a[132:135], a[196:199], v[208:211]
buffer_load_dwordx4 v37, s[12:15], s24 offen offset:1024 lds
v_accvgpr_read_b32 v74, a210
v_mfma_f32_16x16x32_bf16 v[212:215], a[132:135], a[200:203], v[212:215]
ds_read_b128 a[8:11], v44 offset:2048
v_accvgpr_read_b32 v75, a211
v_mfma_f32_16x16x32_bf16 v[136:139], a[164:167], a[136:139], v[136:139]
ds_read_b64_tr_b16 a[32:33], v48 offset:16384
ds_read_b64_tr_b16 a[34:35], v48 offset:16896
v_mfma_f32_16x16x32_bf16 v[140:143], a[168:171], a[136:139], v[140:143]
v_mul_f32 v74, v53, v74
v_mul_f32 v75, v53, v75
v_mfma_f32_16x16x32_bf16 v[176:179], a[172:175], a[136:139], v[176:179]
ds_read_b128 a[12:15], v44 offset:4096
v_cvt_pk_bf16_f32 v65, v74, v75
v_mfma_f32_16x16x32_bf16 v[180:183], a[192:195], a[136:139], v[180:183]
ds_read_b64_tr_b16 a[36:37], v49 offset:18432
ds_read_b64_tr_b16 a[38:39], v49 offset:18944
v_mfma_f32_16x16x32_bf16 v[216:219], a[136:139], a[196:199], v[216:219]
buffer_load_dwordx4 v38, s[12:15], s24 offen offset:2048 lds
v_accvgpr_read_b32 v72, a212
v_mfma_f32_16x16x32_bf16 v[220:223], a[136:139], a[200:203], v[220:223]
ds_read_b128 a[16:19], v44 offset:6144
v_accvgpr_read_b32 v73, a213
v_mfma_f32_16x16x32_bf16 v[144:147], a[164:167], a[140:143], v[144:147]
ds_read_b64_tr_b16 a[40:41], v50 offset:18432
ds_read_b64_tr_b16 a[42:43], v50 offset:18944
v_mfma_f32_16x16x32_bf16 v[148:151], a[168:171], a[140:143], v[148:151]
s_cmp_eq_u32 s39, 1
s_mov_b32 s28, 4294885376
s_cselect_b32 s28, 98304, s28
v_mfma_f32_16x16x32_bf16 v[184:187], a[172:175], a[140:143], v[184:187]
v_add_u32 v43, 4294885376, v43
v_add_u32 v44, 4294885376, v44
v_mfma_f32_16x16x32_bf16 v[188:191], a[192:195], a[140:143], v[188:191]
v_add_u32 v47, 98304, v47
v_add_u32 v48, 98304, v48
v_mfma_f32_16x16x32_bf16 v[224:227], a[140:143], a[196:199], v[224:227]
buffer_load_dwordx4 v39, s[12:15], s24 offen offset:3072 lds
v_add_u32 v49, 98304, v49
v_add_u32 v50, 98304, v50
v_mfma_f32_16x16x32_bf16 v[228:231], a[140:143], a[200:203], v[228:231]
v_add_u32 v45, s28, v45
v_add_u32 v46, s28, v46
v_mfma_f32_16x16x32_bf16 v[152:155], a[164:167], a[160:163], v[152:155]
v_mul_f32 v72, v53, v72
v_mul_f32 v73, v53, v73
v_mfma_f32_16x16x32_bf16 v[156:159], a[168:171], a[160:163], v[156:159]
s_add_u32 s24, s24, 128
v_cvt_pk_bf16_f32 v66, v72, v73
v_accvgpr_read_b32 v74, a214
v_mfma_f32_16x16x32_bf16 v[192:195], a[172:175], a[160:163], v[192:195]
v_accvgpr_read_b32 v75, a215
v_mul_f32 v74, v53, v74
v_mfma_f32_16x16x32_bf16 v[196:199], a[192:195], a[160:163], v[196:199]
v_mul_f32 v75, v53, v75
v_cvt_pk_bf16_f32 v67, v74, v75
v_mfma_f32_16x16x32_bf16 v[232:235], a[160:163], a[196:199], v[232:235]
buffer_store_dwordx4 v[64:67], v7, s[20:23], s42 offen offset:2048 sc1
v_mfma_f32_16x16x32_bf16 v[236:239], a[160:163], a[200:203], v[236:239]
s_waitcnt vmcnt(19) lgkmcnt(0)
s_barrier
v_mfma_f32_16x16x32_bf16 v[160:163], a[28:31], a[0:3], v[160:163]
v_mfma_f32_16x16x32_bf16 v[164:167], a[32:35], a[0:3], v[164:167]
s_add_u32 m0, s48, 32768
ds_read_b64_tr_b16 a[164:165], v47 offset:0
ds_read_b64_tr_b16 a[166:167], v47 offset:512
v_mfma_f32_16x16x32_bf16 v[200:203], a[0:3], a[36:39], v[200:203]
buffer_load_dwordx4 v40, s[16:19], s26 offen offset:0 lds
v_accvgpr_read_b32 v72, a216
v_mfma_f32_16x16x32_bf16 v[204:207], a[0:3], a[40:43], v[204:207]
ds_read_b128 a[128:131], v45
v_accvgpr_read_b32 v73, a217
v_mfma_f32_16x16x32_bf16 v[128:131], a[20:23], a[4:7], v[128:131]
ds_read_b64_tr_b16 a[168:169], v48 offset:0
ds_read_b64_tr_b16 a[170:171], v48 offset:512
v_mfma_f32_16x16x32_bf16 v[132:135], a[24:27], a[4:7], v[132:135]
buffer_load_dwordx4 v41, s[16:19], s26 offen offset:1024 lds
v_mul_f32 v72, v53, v72
v_mfma_f32_16x16x32_bf16 v[168:171], a[28:31], a[4:7], v[168:171]
ds_read_b128 a[132:135], v43 offset:0
v_mul_f32 v73, v53, v73
v_mfma_f32_16x16x32_bf16 v[172:175], a[32:35], a[4:7], v[172:175]
ds_read_b64_tr_b16 a[172:173], v47 offset:2048
ds_read_b64_tr_b16 a[174:175], v47 offset:2560
v_mfma_f32_16x16x32_bf16 v[208:211], a[4:7], a[36:39], v[208:211]
buffer_load_dwordx4 v40, s[16:19], s26 offen offset:2048 lds
v_cvt_pk_bf16_f32 v68, v72, v73
v_mfma_f32_16x16x32_bf16 v[212:215], a[4:7], a[40:43], v[212:215]
ds_read_b128 a[136:139], v43 offset:2048
v_accvgpr_read_b32 v74, a218
v_mfma_f32_16x16x32_bf16 v[136:139], a[20:23], a[8:11], v[136:139]
ds_read_b64_tr_b16 a[192:193], v48 offset:2048
ds_read_b64_tr_b16 a[194:195], v48 offset:2560
v_mfma_f32_16x16x32_bf16 v[140:143], a[24:27], a[8:11], v[140:143]
buffer_load_dwordx4 v41, s[16:19], s26 offen offset:3072 lds
v_accvgpr_read_b32 v75, a219
v_mfma_f32_16x16x32_bf16 v[176:179], a[28:31], a[8:11], v[176:179]
ds_read_b128 a[140:143], v43 offset:4096
v_mul_f32 v74, v53, v74
v_mfma_f32_16x16x32_bf16 v[180:183], a[32:35], a[8:11], v[180:183]
s_add_u32 m0, s48, 36864
ds_read_b64_tr_b16 a[196:197], v49 offset:4096
ds_read_b64_tr_b16 a[198:199], v49 offset:4608
v_mfma_f32_16x16x32_bf16 v[216:219], a[8:11], a[36:39], v[216:219]
buffer_load_dwordx4 v40, s[16:19], s47 offen offset:0 lds
v_mul_f32 v75, v53, v75
v_mfma_f32_16x16x32_bf16 v[220:223], a[8:11], a[40:43], v[220:223]
ds_read_b128 a[160:163], v43 offset:6144
v_cvt_pk_bf16_f32 v69, v74, v75
v_mfma_f32_16x16x32_bf16 v[144:147], a[20:23], a[12:15], v[144:147]
ds_read_b64_tr_b16 a[200:201], v50 offset:4096
ds_read_b64_tr_b16 a[202:203], v50 offset:4608
v_mfma_f32_16x16x32_bf16 v[148:151], a[24:27], a[12:15], v[148:151]
buffer_load_dwordx4 v41, s[16:19], s47 offen offset:1024 lds
v_accvgpr_read_b32 v72, a220
v_mfma_f32_16x16x32_bf16 v[184:187], a[28:31], a[12:15], v[184:187]
v_accvgpr_read_b32 v73, a221
v_mul_f32 v72, v53, v72
v_mfma_f32_16x16x32_bf16 v[188:191], a[32:35], a[12:15], v[188:191]
v_mul_f32 v73, v53, v73
v_cvt_pk_bf16_f32 v70, v72, v73
v_mfma_f32_16x16x32_bf16 v[224:227], a[12:15], a[36:39], v[224:227]
buffer_load_dwordx4 v42, s[12:15], s25 offen offset:2048 lds
v_accvgpr_read_b32 v74, a222
v_mfma_f32_16x16x32_bf16 v[228:231], a[12:15], a[40:43], v[228:231]
v_accvgpr_read_b32 v75, a223
v_mul_f32 v74, v53, v74
v_mfma_f32_16x16x32_bf16 v[152:155], a[20:23], a[16:19], v[152:155]
s_add_u32 s26, s26, 393216
s_add_u32 s47, s47, 393216
s_add_u32 s25, s25, 128
v_mfma_f32_16x16x32_bf16 v[156:159], a[24:27], a[16:19], v[156:159]
v_mul_f32 v75, v53, v75
v_cvt_pk_bf16_f32 v71, v74, v75
v_mfma_f32_16x16x32_bf16 v[192:195], a[28:31], a[16:19], v[192:195]
buffer_store_dwordx4 v[68:71], v7, s[20:23], s42 offen offset:3072 sc1
v_mfma_f32_16x16x32_bf16 v[196:199], a[32:35], a[16:19], v[196:199]
v_mfma_f32_16x16x32_bf16 v[232:235], a[16:19], a[36:39], v[232:235]
v_mfma_f32_16x16x32_bf16 v[236:239], a[16:19], a[40:43], v[236:239]
s_waitcnt lgkmcnt(0)
v_mfma_f32_16x16x32_bf16 v[160:163], a[172:175], a[128:131], v[160:163]
v_mfma_f32_16x16x32_bf16 v[164:167], a[192:195], a[128:131], v[164:167]
s_add_u32 m0, s91, 81920
ds_read_b64_tr_b16 a[20:21], v47 offset:14336
ds_read_b64_tr_b16 a[22:23], v47 offset:14848
v_mfma_f32_16x16x32_bf16 v[200:203], a[128:131], a[196:199], v[200:203]
buffer_load_dwordx4 v36, s[12:15], s24 offen offset:0 lds
v_accvgpr_read_b32 v72, a240
v_mfma_f32_16x16x32_bf16 v[204:207], a[128:131], a[200:203], v[204:207]
ds_read_b128 a[0:3], v46
v_accvgpr_read_b32 v73, a241
v_mfma_f32_16x16x32_bf16 v[128:131], a[164:167], a[132:135], v[128:131]
ds_read_b64_tr_b16 a[24:25], v48 offset:14336
ds_read_b64_tr_b16 a[26:27], v48 offset:14848
v_mfma_f32_16x16x32_bf16 v[132:135], a[168:171], a[132:135], v[132:135]
v_mul_f32 v72, v53, v72
v_mul_f32 v73, v53, v73
v_mfma_f32_16x16x32_bf16 v[168:171], a[172:175], a[132:135], v[168:171]
ds_read_b128 a[4:7], v44 offset:0
v_cvt_pk_bf16_f32 v64, v72, v73
v_mfma_f32_16x16x32_bf16 v[172:175], a[192:195], a[132:135], v[172:175]
ds_read_b64_tr_b16 a[28:29], v47 offset:16384
ds_read_b64_tr_b16 a[30:31], v47 offset:16896
v_mfma_f32_16x16x32_bf16 v[208:211], a[132:135], a[196:199], v[208:211]
buffer_load_dwordx4 v37, s[12:15], s24 offen offset:1024 lds
v_accvgpr_read_b32 v74, a242
v_mfma_f32_16x16x32_bf16 v[212:215], a[132:135], a[200:203], v[212:215]
ds_read_b128 a[8:11], v44 offset:2048
v_accvgpr_read_b32 v75, a243
v_mfma_f32_16x16x32_bf16 v[136:139], a[164:167], a[136:139], v[136:139]
ds_read_b64_tr_b16 a[32:33], v48 offset:16384
ds_read_b64_tr_b16 a[34:35], v48 offset:16896
v_mfma_f32_16x16x32_bf16 v[140:143], a[168:171], a[136:139], v[140:143]
v_mul_f32 v74, v53, v74
v_mul_f32 v75, v53, v75
v_mfma_f32_16x16x32_bf16 v[176:179], a[172:175], a[136:139], v[176:179]
ds_read_b128 a[12:15], v44 offset:4096
v_cvt_pk_bf16_f32 v65, v74, v75
v_mfma_f32_16x16x32_bf16 v[180:183], a[192:195], a[136:139], v[180:183]
ds_read_b64_tr_b16 a[36:37], v49 offset:18432
ds_read_b64_tr_b16 a[38:39], v49 offset:18944
v_mfma_f32_16x16x32_bf16 v[216:219], a[136:139], a[196:199], v[216:219]
buffer_load_dwordx4 v38, s[12:15], s24 offen offset:2048 lds
v_accvgpr_read_b32 v72, a244
v_mfma_f32_16x16x32_bf16 v[220:223], a[136:139], a[200:203], v[220:223]
ds_read_b128 a[16:19], v44 offset:6144
v_accvgpr_read_b32 v73, a245
v_mfma_f32_16x16x32_bf16 v[144:147], a[164:167], a[140:143], v[144:147]
ds_read_b64_tr_b16 a[40:41], v50 offset:18432
ds_read_b64_tr_b16 a[42:43], v50 offset:18944
v_mfma_f32_16x16x32_bf16 v[148:151], a[168:171], a[140:143], v[148:151]
s_cmp_eq_u32 s39, 1
s_mov_b32 s28, 16384
s_cselect_b32 s28, 4294934528, s28
v_mfma_f32_16x16x32_bf16 v[184:187], a[172:175], a[140:143], v[184:187]
v_add_u32 v43, 16384, v43
v_add_u32 v44, 16384, v44
v_mfma_f32_16x16x32_bf16 v[188:191], a[192:195], a[140:143], v[188:191]
v_add_u32 v47, 4294934528, v47
v_add_u32 v48, 4294934528, v48
v_mfma_f32_16x16x32_bf16 v[224:227], a[140:143], a[196:199], v[224:227]
buffer_load_dwordx4 v39, s[12:15], s24 offen offset:3072 lds
v_add_u32 v49, 4294934528, v49
v_add_u32 v50, 4294934528, v50
v_mfma_f32_16x16x32_bf16 v[228:231], a[140:143], a[200:203], v[228:231]
v_add_u32 v45, s28, v45
v_add_u32 v46, s28, v46
v_mfma_f32_16x16x32_bf16 v[152:155], a[164:167], a[160:163], v[152:155]
v_mul_f32 v72, v53, v72
v_mul_f32 v73, v53, v73
v_mfma_f32_16x16x32_bf16 v[156:159], a[168:171], a[160:163], v[156:159]
s_add_u32 s24, s24, 128
v_cvt_pk_bf16_f32 v66, v72, v73
v_accvgpr_read_b32 v74, a246
v_mfma_f32_16x16x32_bf16 v[192:195], a[172:175], a[160:163], v[192:195]
v_accvgpr_read_b32 v75, a247
v_mul_f32 v74, v53, v74
v_mfma_f32_16x16x32_bf16 v[196:199], a[192:195], a[160:163], v[196:199]
v_mul_f32 v75, v53, v75
v_cvt_pk_bf16_f32 v67, v74, v75
v_mfma_f32_16x16x32_bf16 v[232:235], a[160:163], a[196:199], v[232:235]
buffer_store_dwordx4 v[64:67], v7, s[20:23], s43 offen offset:0 sc1
v_mfma_f32_16x16x32_bf16 v[236:239], a[160:163], a[200:203], v[236:239]
s_waitcnt vmcnt(19) lgkmcnt(0)
s_barrier
v_mfma_f32_16x16x32_bf16 v[160:163], a[28:31], a[0:3], v[160:163]
v_mfma_f32_16x16x32_bf16 v[164:167], a[32:35], a[0:3], v[164:167]
s_add_u32 m0, s48, 131072
ds_read_b64_tr_b16 a[164:165], v47 offset:0
ds_read_b64_tr_b16 a[166:167], v47 offset:512
v_mfma_f32_16x16x32_bf16 v[200:203], a[0:3], a[36:39], v[200:203]
buffer_load_dwordx4 v40, s[16:19], s26 offen offset:0 lds
v_accvgpr_read_b32 v72, a248
v_mfma_f32_16x16x32_bf16 v[204:207], a[0:3], a[40:43], v[204:207]
ds_read_b128 a[128:131], v45
v_accvgpr_read_b32 v73, a249
v_mfma_f32_16x16x32_bf16 v[128:131], a[20:23], a[4:7], v[128:131]
ds_read_b64_tr_b16 a[168:169], v48 offset:0
ds_read_b64_tr_b16 a[170:171], v48 offset:512
v_mfma_f32_16x16x32_bf16 v[132:135], a[24:27], a[4:7], v[132:135]
buffer_load_dwordx4 v41, s[16:19], s26 offen offset:1024 lds
v_mul_f32 v72, v53, v72
v_mfma_f32_16x16x32_bf16 v[168:171], a[28:31], a[4:7], v[168:171]
ds_read_b128 a[132:135], v43 offset:0
v_mul_f32 v73, v53, v73
v_mfma_f32_16x16x32_bf16 v[172:175], a[32:35], a[4:7], v[172:175]
ds_read_b64_tr_b16 a[172:173], v47 offset:2048
ds_read_b64_tr_b16 a[174:175], v47 offset:2560
v_mfma_f32_16x16x32_bf16 v[208:211], a[4:7], a[36:39], v[208:211]
buffer_load_dwordx4 v40, s[16:19], s26 offen offset:2048 lds
v_cvt_pk_bf16_f32 v68, v72, v73
v_mfma_f32_16x16x32_bf16 v[212:215], a[4:7], a[40:43], v[212:215]
ds_read_b128 a[136:139], v43 offset:2048
v_accvgpr_read_b32 v74, a250
v_mfma_f32_16x16x32_bf16 v[136:139], a[20:23], a[8:11], v[136:139]
ds_read_b64_tr_b16 a[192:193], v48 offset:2048
ds_read_b64_tr_b16 a[194:195], v48 offset:2560
v_mfma_f32_16x16x32_bf16 v[140:143], a[24:27], a[8:11], v[140:143]
buffer_load_dwordx4 v41, s[16:19], s26 offen offset:3072 lds
v_accvgpr_read_b32 v75, a251
v_mfma_f32_16x16x32_bf16 v[176:179], a[28:31], a[8:11], v[176:179]
ds_read_b128 a[140:143], v43 offset:4096
v_mul_f32 v74, v53, v74
v_mfma_f32_16x16x32_bf16 v[180:183], a[32:35], a[8:11], v[180:183]
s_add_u32 m0, s48, 135168
ds_read_b64_tr_b16 a[196:197], v49 offset:4096
ds_read_b64_tr_b16 a[198:199], v49 offset:4608
v_mfma_f32_16x16x32_bf16 v[216:219], a[8:11], a[36:39], v[216:219]
buffer_load_dwordx4 v40, s[16:19], s47 offen offset:0 lds
v_mul_f32 v75, v53, v75
v_mfma_f32_16x16x32_bf16 v[220:223], a[8:11], a[40:43], v[220:223]
ds_read_b128 a[160:163], v43 offset:6144
v_cvt_pk_bf16_f32 v69, v74, v75
v_mfma_f32_16x16x32_bf16 v[144:147], a[20:23], a[12:15], v[144:147]
ds_read_b64_tr_b16 a[200:201], v50 offset:4096
ds_read_b64_tr_b16 a[202:203], v50 offset:4608
v_mfma_f32_16x16x32_bf16 v[148:151], a[24:27], a[12:15], v[148:151]
buffer_load_dwordx4 v41, s[16:19], s47 offen offset:1024 lds
v_accvgpr_read_b32 v72, a252
v_mfma_f32_16x16x32_bf16 v[184:187], a[28:31], a[12:15], v[184:187]
v_accvgpr_read_b32 v73, a253
v_mul_f32 v72, v53, v72
v_mfma_f32_16x16x32_bf16 v[188:191], a[32:35], a[12:15], v[188:191]
v_mul_f32 v73, v53, v73
v_cvt_pk_bf16_f32 v70, v72, v73
v_mfma_f32_16x16x32_bf16 v[224:227], a[12:15], a[36:39], v[224:227]
buffer_load_dwordx4 v42, s[12:15], s25 offen offset:2048 lds
v_accvgpr_read_b32 v74, a254
v_mfma_f32_16x16x32_bf16 v[228:231], a[12:15], a[40:43], v[228:231]
v_accvgpr_read_b32 v75, a255
v_mul_f32 v74, v53, v74
v_mfma_f32_16x16x32_bf16 v[152:155], a[20:23], a[16:19], v[152:155]
s_add_u32 s26, s26, 393216
s_add_u32 s47, s47, 393216
s_add_u32 s25, s25, 128
v_mfma_f32_16x16x32_bf16 v[156:159], a[24:27], a[16:19], v[156:159]
v_mul_f32 v75, v53, v75
v_cvt_pk_bf16_f32 v71, v74, v75
v_mfma_f32_16x16x32_bf16 v[192:195], a[28:31], a[16:19], v[192:195]
buffer_store_dwordx4 v[68:71], v7, s[20:23], s43 offen offset:1024 sc1
v_mfma_f32_16x16x32_bf16 v[196:199], a[32:35], a[16:19], v[196:199]
v_mfma_f32_16x16x32_bf16 v[232:235], a[16:19], a[36:39], v[232:235]
v_mfma_f32_16x16x32_bf16 v[236:239], a[16:19], a[40:43], v[236:239]
s_waitcnt lgkmcnt(0)
v_mfma_f32_16x16x32_bf16 v[160:163], a[172:175], a[128:131], v[160:163]
v_mfma_f32_16x16x32_bf16 v[164:167], a[192:195], a[128:131], v[164:167]
s_add_u32 m0, s91, 0
ds_read_b64_tr_b16 a[20:21], v47 offset:14336
ds_read_b64_tr_b16 a[22:23], v47 offset:14848
v_mfma_f32_16x16x32_bf16 v[200:203], a[128:131], a[196:199], v[200:203]
buffer_load_dwordx4 v36, s[12:15], s24 offen offset:0 lds
v_accvgpr_read_b32 v72, a96
v_mfma_f32_16x16x32_bf16 v[204:207], a[128:131], a[200:203], v[204:207]
ds_read_b128 a[0:3], v46
v_accvgpr_read_b32 v73, a97
v_mfma_f32_16x16x32_bf16 v[128:131], a[164:167], a[132:135], v[128:131]
ds_read_b64_tr_b16 a[24:25], v48 offset:14336
ds_read_b64_tr_b16 a[26:27], v48 offset:14848
v_mfma_f32_16x16x32_bf16 v[132:135], a[168:171], a[132:135], v[132:135]
v_mul_f32 v72, v53, v72
v_mul_f32 v73, v53, v73
v_mfma_f32_16x16x32_bf16 v[168:171], a[172:175], a[132:135], v[168:171]
ds_read_b128 a[4:7], v44 offset:0
v_cvt_pk_bf16_f32 v64, v72, v73
v_mfma_f32_16x16x32_bf16 v[172:175], a[192:195], a[132:135], v[172:175]
ds_read_b64_tr_b16 a[28:29], v47 offset:16384
ds_read_b64_tr_b16 a[30:31], v47 offset:16896
v_mfma_f32_16x16x32_bf16 v[208:211], a[132:135], a[196:199], v[208:211]
buffer_load_dwordx4 v37, s[12:15], s24 offen offset:1024 lds
v_accvgpr_read_b32 v74, a98
v_mfma_f32_16x16x32_bf16 v[212:215], a[132:135], a[200:203], v[212:215]
ds_read_b128 a[8:11], v44 offset:2048
v_accvgpr_read_b32 v75, a99
v_mfma_f32_16x16x32_bf16 v[136:139], a[164:167], a[136:139], v[136:139]
ds_read_b64_tr_b16 a[32:33], v48 offset:16384
ds_read_b64_tr_b16 a[34:35], v48 offset:16896
v_mfma_f32_16x16x32_bf16 v[140:143], a[168:171], a[136:139], v[140:143]
v_mul_f32 v74, v53, v74
v_mul_f32 v75, v53, v75
v_mfma_f32_16x16x32_bf16 v[176:179], a[172:175], a[136:139], v[176:179]
ds_read_b128 a[12:15], v44 offset:4096
v_cvt_pk_bf16_f32 v65, v74, v75
v_mfma_f32_16x16x32_bf16 v[180:183], a[192:195], a[136:139], v[180:183]
ds_read_b64_tr_b16 a[36:37], v49 offset:18432
ds_read_b64_tr_b16 a[38:39], v49 offset:18944
v_mfma_f32_16x16x32_bf16 v[216:219], a[136:139], a[196:199], v[216:219]
buffer_load_dwordx4 v38, s[12:15], s24 offen offset:2048 lds
v_accvgpr_read_b32 v72, a100
v_mfma_f32_16x16x32_bf16 v[220:223], a[136:139], a[200:203], v[220:223]
ds_read_b128 a[16:19], v44 offset:6144
v_accvgpr_read_b32 v73, a101
v_mfma_f32_16x16x32_bf16 v[144:147], a[164:167], a[140:143], v[144:147]
ds_read_b64_tr_b16 a[40:41], v50 offset:18432
ds_read_b64_tr_b16 a[42:43], v50 offset:18944
v_mfma_f32_16x16x32_bf16 v[148:151], a[168:171], a[140:143], v[148:151]
s_cmp_eq_u32 s39, 1
s_mov_b32 s28, 49152
s_cselect_b32 s28, 4294901760, s28
v_mfma_f32_16x16x32_bf16 v[184:187], a[172:175], a[140:143], v[184:187]
v_add_u32 v43, 49152, v43
v_add_u32 v44, 49152, v44
v_mfma_f32_16x16x32_bf16 v[188:191], a[192:195], a[140:143], v[188:191]
v_add_u32 v47, 4294901760, v47
v_add_u32 v48, 4294901760, v48
v_mfma_f32_16x16x32_bf16 v[224:227], a[140:143], a[196:199], v[224:227]
buffer_load_dwordx4 v39, s[12:15], s24 offen offset:3072 lds
v_add_u32 v49, 4294901760, v49
v_add_u32 v50, 4294901760, v50
v_mfma_f32_16x16x32_bf16 v[228:231], a[140:143], a[200:203], v[228:231]
v_add_u32 v45, s28, v45
v_add_u32 v46, s28, v46
v_mfma_f32_16x16x32_bf16 v[152:155], a[164:167], a[160:163], v[152:155]
v_mul_f32 v72, v53, v72
v_mul_f32 v73, v53, v73
v_mfma_f32_16x16x32_bf16 v[156:159], a[168:171], a[160:163], v[156:159]
s_add_u32 s24, s24, 128
v_cvt_pk_bf16_f32 v66, v72, v73
v_accvgpr_read_b32 v74, a102
v_mfma_f32_16x16x32_bf16 v[192:195], a[172:175], a[160:163], v[192:195]
v_accvgpr_read_b32 v75, a103
v_mul_f32 v74, v53, v74
v_mfma_f32_16x16x32_bf16 v[196:199], a[192:195], a[160:163], v[196:199]
v_mul_f32 v75, v53, v75
v_cvt_pk_bf16_f32 v67, v74, v75
v_mfma_f32_16x16x32_bf16 v[232:235], a[160:163], a[196:199], v[232:235]
buffer_store_dwordx4 v[64:67], v7, s[20:23], s43 offen offset:2048 sc1
v_mfma_f32_16x16x32_bf16 v[236:239], a[160:163], a[200:203], v[236:239]
s_waitcnt vmcnt(19) lgkmcnt(0)
s_barrier
v_mfma_f32_16x16x32_bf16 v[160:163], a[28:31], a[0:3], v[160:163]
v_mfma_f32_16x16x32_bf16 v[164:167], a[32:35], a[0:3], v[164:167]
s_add_u32 m0, s48, 98304
ds_read_b64_tr_b16 a[164:165], v47 offset:0
ds_read_b64_tr_b16 a[166:167], v47 offset:512
v_mfma_f32_16x16x32_bf16 v[200:203], a[0:3], a[36:39], v[200:203]
buffer_load_dwordx4 v40, s[16:19], s26 offen offset:0 lds
v_accvgpr_read_b32 v72, a104
v_mfma_f32_16x16x32_bf16 v[204:207], a[0:3], a[40:43], v[204:207]
ds_read_b128 a[128:131], v45
v_accvgpr_read_b32 v73, a105
v_mfma_f32_16x16x32_bf16 v[128:131], a[20:23], a[4:7], v[128:131]
ds_read_b64_tr_b16 a[168:169], v48 offset:0
ds_read_b64_tr_b16 a[170:171], v48 offset:512
v_mfma_f32_16x16x32_bf16 v[132:135], a[24:27], a[4:7], v[132:135]
buffer_load_dwordx4 v41, s[16:19], s26 offen offset:1024 lds
v_mul_f32 v72, v53, v72
v_mfma_f32_16x16x32_bf16 v[168:171], a[28:31], a[4:7], v[168:171]
ds_read_b128 a[132:135], v43 offset:0
v_mul_f32 v73, v53, v73
v_mfma_f32_16x16x32_bf16 v[172:175], a[32:35], a[4:7], v[172:175]
ds_read_b64_tr_b16 a[172:173], v47 offset:2048
ds_read_b64_tr_b16 a[174:175], v47 offset:2560
v_mfma_f32_16x16x32_bf16 v[208:211], a[4:7], a[36:39], v[208:211]
buffer_load_dwordx4 v40, s[16:19], s26 offen offset:2048 lds
v_cvt_pk_bf16_f32 v68, v72, v73
v_mfma_f32_16x16x32_bf16 v[212:215], a[4:7], a[40:43], v[212:215]
ds_read_b128 a[136:139], v43 offset:2048
v_accvgpr_read_b32 v74, a106
v_mfma_f32_16x16x32_bf16 v[136:139], a[20:23], a[8:11], v[136:139]
ds_read_b64_tr_b16 a[192:193], v48 offset:2048
ds_read_b64_tr_b16 a[194:195], v48 offset:2560
v_mfma_f32_16x16x32_bf16 v[140:143], a[24:27], a[8:11], v[140:143]
buffer_load_dwordx4 v41, s[16:19], s26 offen offset:3072 lds
v_accvgpr_read_b32 v75, a107
v_mfma_f32_16x16x32_bf16 v[176:179], a[28:31], a[8:11], v[176:179]
ds_read_b128 a[140:143], v43 offset:4096
v_mul_f32 v74, v53, v74
v_mfma_f32_16x16x32_bf16 v[180:183], a[32:35], a[8:11], v[180:183]
s_add_u32 m0, s48, 102400
ds_read_b64_tr_b16 a[196:197], v49 offset:4096
ds_read_b64_tr_b16 a[198:199], v49 offset:4608
v_mfma_f32_16x16x32_bf16 v[216:219], a[8:11], a[36:39], v[216:219]
buffer_load_dwordx4 v40, s[16:19], s47 offen offset:0 lds
v_mul_f32 v75, v53, v75
v_mfma_f32_16x16x32_bf16 v[220:223], a[8:11], a[40:43], v[220:223]
ds_read_b128 a[160:163], v43 offset:6144
v_cvt_pk_bf16_f32 v69, v74, v75
v_mfma_f32_16x16x32_bf16 v[144:147], a[20:23], a[12:15], v[144:147]
ds_read_b64_tr_b16 a[200:201], v50 offset:4096
ds_read_b64_tr_b16 a[202:203], v50 offset:4608
v_mfma_f32_16x16x32_bf16 v[148:151], a[24:27], a[12:15], v[148:151]
buffer_load_dwordx4 v41, s[16:19], s47 offen offset:1024 lds
v_accvgpr_read_b32 v72, a108
v_mfma_f32_16x16x32_bf16 v[184:187], a[28:31], a[12:15], v[184:187]
v_accvgpr_read_b32 v73, a109
v_mul_f32 v72, v53, v72
v_mfma_f32_16x16x32_bf16 v[188:191], a[32:35], a[12:15], v[188:191]
v_mul_f32 v73, v53, v73
v_cvt_pk_bf16_f32 v70, v72, v73
v_mfma_f32_16x16x32_bf16 v[224:227], a[12:15], a[36:39], v[224:227]
buffer_load_dwordx4 v42, s[12:15], s25 offen offset:2048 lds
v_accvgpr_read_b32 v74, a110
v_mfma_f32_16x16x32_bf16 v[228:231], a[12:15], a[40:43], v[228:231]
v_accvgpr_read_b32 v75, a111
v_mul_f32 v74, v53, v74
v_mfma_f32_16x16x32_bf16 v[152:155], a[20:23], a[16:19], v[152:155]
s_add_u32 s26, s26, 393216
s_add_u32 s47, s47, 393216
s_add_u32 s25, s25, 128
v_mfma_f32_16x16x32_bf16 v[156:159], a[24:27], a[16:19], v[156:159]
v_mul_f32 v75, v53, v75
v_cvt_pk_bf16_f32 v71, v74, v75
v_mfma_f32_16x16x32_bf16 v[192:195], a[28:31], a[16:19], v[192:195]
buffer_store_dwordx4 v[68:71], v7, s[20:23], s43 offen offset:3072 sc1
v_mfma_f32_16x16x32_bf16 v[196:199], a[32:35], a[16:19], v[196:199]
v_mfma_f32_16x16x32_bf16 v[232:235], a[16:19], a[36:39], v[232:235]
v_mfma_f32_16x16x32_bf16 v[236:239], a[16:19], a[40:43], v[236:239]
s_waitcnt lgkmcnt(0)
v_mfma_f32_16x16x32_bf16 v[160:163], a[172:175], a[128:131], v[160:163]
v_mfma_f32_16x16x32_bf16 v[164:167], a[192:195], a[128:131], v[164:167]
s_add_u32 m0, s91, 16384
ds_read_b64_tr_b16 a[20:21], v47 offset:14336
ds_read_b64_tr_b16 a[22:23], v47 offset:14848
v_mfma_f32_16x16x32_bf16 v[200:203], a[128:131], a[196:199], v[200:203]
buffer_load_dwordx4 v36, s[12:15], s24 offen offset:0 lds
v_mfma_f32_16x16x32_bf16 v[204:207], a[128:131], a[200:203], v[204:207]
ds_read_b128 a[0:3], v46
v_mfma_f32_16x16x32_bf16 v[128:131], a[164:167], a[132:135], v[128:131]
ds_read_b64_tr_b16 a[24:25], v48 offset:14336
ds_read_b64_tr_b16 a[26:27], v48 offset:14848
v_mfma_f32_16x16x32_bf16 v[132:135], a[168:171], a[132:135], v[132:135]
v_mfma_f32_16x16x32_bf16 v[168:171], a[172:175], a[132:135], v[168:171]
ds_read_b128 a[4:7], v44 offset:0
v_mfma_f32_16x16x32_bf16 v[172:175], a[192:195], a[132:135], v[172:175]
ds_read_b64_tr_b16 a[28:29], v47 offset:16384
ds_read_b64_tr_b16 a[30:31], v47 offset:16896
v_mfma_f32_16x16x32_bf16 v[208:211], a[132:135], a[196:199], v[208:211]
buffer_load_dwordx4 v37, s[12:15], s24 offen offset:1024 lds
v_mfma_f32_16x16x32_bf16 v[212:215], a[132:135], a[200:203], v[212:215]
ds_read_b128 a[8:11], v44 offset:2048
v_mfma_f32_16x16x32_bf16 v[136:139], a[164:167], a[136:139], v[136:139]
ds_read_b64_tr_b16 a[32:33], v48 offset:16384
ds_read_b64_tr_b16 a[34:35], v48 offset:16896
v_mfma_f32_16x16x32_bf16 v[140:143], a[168:171], a[136:139], v[140:143]
v_mfma_f32_16x16x32_bf16 v[176:179], a[172:175], a[136:139], v[176:179]
ds_read_b128 a[12:15], v44 offset:4096
v_mfma_f32_16x16x32_bf16 v[180:183], a[192:195], a[136:139], v[180:183]
ds_read_b64_tr_b16 a[36:37], v49 offset:18432
ds_read_b64_tr_b16 a[38:39], v49 offset:18944
v_mfma_f32_16x16x32_bf16 v[216:219], a[136:139], a[196:199], v[216:219]
buffer_load_dwordx4 v38, s[12:15], s24 offen offset:2048 lds
v_mfma_f32_16x16x32_bf16 v[220:223], a[136:139], a[200:203], v[220:223]
ds_read_b128 a[16:19], v44 offset:6144
v_mfma_f32_16x16x32_bf16 v[144:147], a[164:167], a[140:143], v[144:147]
ds_read_b64_tr_b16 a[40:41], v50 offset:18432
ds_read_b64_tr_b16 a[42:43], v50 offset:18944
v_mfma_f32_16x16x32_bf16 v[148:151], a[168:171], a[140:143], v[148:151]
s_cmp_eq_u32 s39, 1
s_mov_b32 s28, 16384
s_cselect_b32 s28, 98304, s28
v_mfma_f32_16x16x32_bf16 v[184:187], a[172:175], a[140:143], v[184:187]
v_add_u32 v43, 16384, v43
v_add_u32 v44, 16384, v44
v_mfma_f32_16x16x32_bf16 v[188:191], a[192:195], a[140:143], v[188:191]
v_add_u32 v47, 98304, v47
v_add_u32 v48, 98304, v48
v_mfma_f32_16x16x32_bf16 v[224:227], a[140:143], a[196:199], v[224:227]
buffer_load_dwordx4 v39, s[12:15], s24 offen offset:3072 lds
v_add_u32 v49, 98304, v49
v_add_u32 v50, 98304, v50
v_mfma_f32_16x16x32_bf16 v[228:231], a[140:143], a[200:203], v[228:231]
v_add_u32 v45, s28, v45
v_add_u32 v46, s28, v46
v_mfma_f32_16x16x32_bf16 v[152:155], a[164:167], a[160:163], v[152:155]
v_mfma_f32_16x16x32_bf16 v[156:159], a[168:171], a[160:163], v[156:159]
s_add_u32 s24, s24, 128
v_mfma_f32_16x16x32_bf16 v[192:195], a[172:175], a[160:163], v[192:195]
v_mfma_f32_16x16x32_bf16 v[196:199], a[192:195], a[160:163], v[196:199]
v_mfma_f32_16x16x32_bf16 v[232:235], a[160:163], a[196:199], v[232:235]
v_mfma_f32_16x16x32_bf16 v[236:239], a[160:163], a[200:203], v[236:239]
s_waitcnt vmcnt(18) lgkmcnt(0)
s_barrier
v_mfma_f32_16x16x32_bf16 v[160:163], a[28:31], a[0:3], v[160:163]
v_mfma_f32_16x16x32_bf16 v[164:167], a[32:35], a[0:3], v[164:167]
s_add_u32 m0, s48, 32768
ds_read_b64_tr_b16 a[164:165], v47 offset:0
ds_read_b64_tr_b16 a[166:167], v47 offset:512
v_mfma_f32_16x16x32_bf16 v[200:203], a[0:3], a[36:39], v[200:203]
buffer_load_dwordx4 v40, s[16:19], s26 offen offset:0 lds
v_mfma_f32_16x16x32_bf16 v[204:207], a[0:3], a[40:43], v[204:207]
ds_read_b128 a[128:131], v45
v_mfma_f32_16x16x32_bf16 v[128:131], a[20:23], a[4:7], v[128:131]
ds_read_b64_tr_b16 a[168:169], v48 offset:0
ds_read_b64_tr_b16 a[170:171], v48 offset:512
v_mfma_f32_16x16x32_bf16 v[132:135], a[24:27], a[4:7], v[132:135]
buffer_load_dwordx4 v41, s[16:19], s26 offen offset:1024 lds
v_mfma_f32_16x16x32_bf16 v[168:171], a[28:31], a[4:7], v[168:171]
ds_read_b128 a[132:135], v43 offset:0
v_mfma_f32_16x16x32_bf16 v[172:175], a[32:35], a[4:7], v[172:175]
ds_read_b64_tr_b16 a[172:173], v47 offset:2048
ds_read_b64_tr_b16 a[174:175], v47 offset:2560
v_mfma_f32_16x16x32_bf16 v[208:211], a[4:7], a[36:39], v[208:211]
buffer_load_dwordx4 v40, s[16:19], s26 offen offset:2048 lds
v_mfma_f32_16x16x32_bf16 v[212:215], a[4:7], a[40:43], v[212:215]
ds_read_b128 a[136:139], v43 offset:2048
v_mfma_f32_16x16x32_bf16 v[136:139], a[20:23], a[8:11], v[136:139]
ds_read_b64_tr_b16 a[192:193], v48 offset:2048
ds_read_b64_tr_b16 a[194:195], v48 offset:2560
v_mfma_f32_16x16x32_bf16 v[140:143], a[24:27], a[8:11], v[140:143]
buffer_load_dwordx4 v41, s[16:19], s26 offen offset:3072 lds
v_mfma_f32_16x16x32_bf16 v[176:179], a[28:31], a[8:11], v[176:179]
ds_read_b128 a[140:143], v43 offset:4096
v_mfma_f32_16x16x32_bf16 v[180:183], a[32:35], a[8:11], v[180:183]
s_add_u32 m0, s48, 36864
ds_read_b64_tr_b16 a[196:197], v49 offset:4096
ds_read_b64_tr_b16 a[198:199], v49 offset:4608
v_mfma_f32_16x16x32_bf16 v[216:219], a[8:11], a[36:39], v[216:219]
buffer_load_dwordx4 v40, s[16:19], s47 offen offset:0 lds
v_mfma_f32_16x16x32_bf16 v[220:223], a[8:11], a[40:43], v[220:223]
ds_read_b128 a[160:163], v43 offset:6144
v_mfma_f32_16x16x32_bf16 v[144:147], a[20:23], a[12:15], v[144:147]
ds_read_b64_tr_b16 a[200:201], v50 offset:4096
ds_read_b64_tr_b16 a[202:203], v50 offset:4608
v_mfma_f32_16x16x32_bf16 v[148:151], a[24:27], a[12:15], v[148:151]
buffer_load_dwordx4 v41, s[16:19], s47 offen offset:1024 lds
v_mfma_f32_16x16x32_bf16 v[184:187], a[28:31], a[12:15], v[184:187]
v_mfma_f32_16x16x32_bf16 v[188:191], a[32:35], a[12:15], v[188:191]
v_mfma_f32_16x16x32_bf16 v[224:227], a[12:15], a[36:39], v[224:227]
buffer_load_dwordx4 v42, s[12:15], s25 offen offset:2048 lds
v_mfma_f32_16x16x32_bf16 v[228:231], a[12:15], a[40:43], v[228:231]
v_mfma_f32_16x16x32_bf16 v[152:155], a[20:23], a[16:19], v[152:155]
s_add_u32 s26, s26, 393216
s_add_u32 s47, s47, 393216
s_add_u32 s25, s25, 128
v_mfma_f32_16x16x32_bf16 v[156:159], a[24:27], a[16:19], v[156:159]
v_mfma_f32_16x16x32_bf16 v[192:195], a[28:31], a[16:19], v[192:195]
v_mfma_f32_16x16x32_bf16 v[196:199], a[32:35], a[16:19], v[196:199]
v_mfma_f32_16x16x32_bf16 v[232:235], a[16:19], a[36:39], v[232:235]
v_mfma_f32_16x16x32_bf16 v[236:239], a[16:19], a[40:43], v[236:239]
s_waitcnt lgkmcnt(0)
v_mfma_f32_16x16x32_bf16 v[160:163], a[172:175], a[128:131], v[160:163]
s_bfe_u32 s51, s86, 0x10001
s_lshl_b32 s51, s51, 3
s_and_b32 s54, s87, 7
v_mfma_f32_16x16x32_bf16 v[164:167], a[192:195], a[128:131], v[164:167]
s_add_u32 m0, s91, 65536
ds_read_b64_tr_b16 a[20:21], v47 offset:14336
ds_read_b64_tr_b16 a[22:23], v47 offset:14848
v_mfma_f32_16x16x32_bf16 v[200:203], a[128:131], a[196:199], v[200:203]
s_or_b32 s51, s51, s54
buffer_load_dwordx4 v36, s[12:15], s24 offen offset:0 lds
v_mfma_f32_16x16x32_bf16 v[204:207], a[128:131], a[200:203], v[204:207]
s_lshr_b32 s52, s86, 2
ds_read_b128 a[0:3], v46
v_mfma_f32_16x16x32_bf16 v[128:131], a[164:167], a[132:135], v[128:131]
ds_read_b64_tr_b16 a[24:25], v48 offset:14336
ds_read_b64_tr_b16 a[26:27], v48 offset:14848
v_mfma_f32_16x16x32_bf16 v[132:135], a[168:171], a[132:135], v[132:135]
s_lshl_b32 s52, s52, 2
s_lshr_b32 s54, s87, 3
s_or_b32 s52, s52, s54
v_mfma_f32_16x16x32_bf16 v[168:171], a[172:175], a[132:135], v[168:171]
s_lshl_b32 s52, s52, 8
ds_read_b128 a[4:7], v44 offset:0
v_mfma_f32_16x16x32_bf16 v[172:175], a[192:195], a[132:135], v[172:175]
ds_read_b64_tr_b16 a[28:29], v47 offset:16384
ds_read_b64_tr_b16 a[30:31], v47 offset:16896
v_mfma_f32_16x16x32_bf16 v[208:211], a[132:135], a[196:199], v[208:211]
s_and_b32 s55, s86, 1
buffer_load_dwordx4 v37, s[12:15], s24 offen offset:1024 lds
v_mfma_f32_16x16x32_bf16 v[212:215], a[132:135], a[200:203], v[212:215]
s_lshl_b32 s54, s51, 2
ds_read_b128 a[8:11], v44 offset:2048
v_mfma_f32_16x16x32_bf16 v[136:139], a[164:167], a[136:139], v[136:139]
ds_read_b64_tr_b16 a[32:33], v48 offset:16384
ds_read_b64_tr_b16 a[34:35], v48 offset:16896
v_mfma_f32_16x16x32_bf16 v[140:143], a[168:171], a[136:139], v[140:143]
s_add_u32 s54, s54, s3
s_lshl_b32 s54, s54, 18
s_add_u32 s54, s54, 33554432
v_mfma_f32_16x16x32_bf16 v[176:179], a[172:175], a[136:139], v[176:179]
ds_read_b128 a[12:15], v44 offset:4096
v_mfma_f32_16x16x32_bf16 v[180:183], a[192:195], a[136:139], v[180:183]
ds_read_b64_tr_b16 a[36:37], v49 offset:18432
ds_read_b64_tr_b16 a[38:39], v49 offset:18944
v_mfma_f32_16x16x32_bf16 v[216:219], a[136:139], a[196:199], v[216:219]
buffer_load_dwordx4 v38, s[12:15], s24 offen offset:2048 lds
v_mfma_f32_16x16x32_bf16 v[220:223], a[136:139], a[200:203], v[220:223]
ds_read_b128 a[16:19], v44 offset:6144
v_mfma_f32_16x16x32_bf16 v[144:147], a[164:167], a[140:143], v[144:147]
ds_read_b64_tr_b16 a[40:41], v50 offset:18432
ds_read_b64_tr_b16 a[42:43], v50 offset:18944
v_mfma_f32_16x16x32_bf16 v[148:151], a[168:171], a[140:143], v[148:151]
s_cmp_eq_u32 s39, 1
s_mov_b32 s28, 4294885376
s_cselect_b32 s28, 4294934528, s28
v_mfma_f32_16x16x32_bf16 v[184:187], a[172:175], a[140:143], v[184:187]
v_add_u32 v43, 4294885376, v43
v_add_u32 v44, 4294885376, v44
v_mfma_f32_16x16x32_bf16 v[188:191], a[192:195], a[140:143], v[188:191]
v_add_u32 v47, 4294934528, v47
v_add_u32 v48, 4294934528, v48
v_mfma_f32_16x16x32_bf16 v[224:227], a[140:143], a[196:199], v[224:227]
buffer_load_dwordx4 v39, s[12:15], s24 offen offset:3072 lds
v_add_u32 v49, 4294934528, v49
v_add_u32 v50, 4294934528, v50
v_mfma_f32_16x16x32_bf16 v[228:231], a[140:143], a[200:203], v[228:231]
v_add_u32 v45, s28, v45
v_add_u32 v46, s28, v46
v_mfma_f32_16x16x32_bf16 v[152:155], a[164:167], a[160:163], v[152:155]
s_add_u32 s4, s84, s54
s_addc_u32 s5, s85, 0
s_and_b32 s5, s5, 0xffff
v_mfma_f32_16x16x32_bf16 v[156:159], a[168:171], a[160:163], v[156:159]
s_add_u32 s24, s24, 128
s_lshl_b32 s54, s3, 15
s_add_u32 s54, s54, s52
v_mfma_f32_16x16x32_bf16 v[192:195], a[172:175], a[160:163], v[192:195]
s_add_u32 s8, s80, s54
s_addc_u32 s9, s81, 0
s_and_b32 s9, s9, 0xffff
v_mfma_f32_16x16x32_bf16 v[196:199], a[192:195], a[160:163], v[196:199]
s_mul_i32 s44, s55, 172032
s_mul_i32 s45, s55, 5505024
s_mov_b32 s56, 172032
v_mfma_f32_16x16x32_bf16 v[232:235], a[160:163], a[196:199], v[232:235]
s_mov_b32 s57, 5505024
v_mfma_f32_16x16x32_bf16 v[236:239], a[160:163], a[200:203], v[236:239]
s_waitcnt vmcnt(16) lgkmcnt(0)
s_barrier
v_mfma_f32_16x16x32_bf16 v[160:163], a[28:31], a[0:3], v[160:163]
s_cmp_eq_u32 s55, 0
s_cselect_b32 s6, s56, 0x40000
s_cselect_b32 s57, s57, 0x800000
v_mfma_f32_16x16x32_bf16 v[164:167], a[32:35], a[0:3], v[164:167]
s_add_u32 m0, s48, 131072
ds_read_b64_tr_b16 a[164:165], v47 offset:0
ds_read_b64_tr_b16 a[166:167], v47 offset:512
v_mfma_f32_16x16x32_bf16 v[200:203], a[0:3], a[36:39], v[200:203]
s_sub_u32 s10, s57, s54
buffer_load_dwordx4 v40, s[16:19], s26 offen offset:0 lds
v_mfma_f32_16x16x32_bf16 v[204:207], a[0:3], a[40:43], v[204:207]
s_and_b32 s54, s3, 1
ds_read_b128 a[128:131], v45
v_mfma_f32_16x16x32_bf16 v[128:131], a[20:23], a[4:7], v[128:131]
ds_read_b64_tr_b16 a[168:169], v48 offset:0
ds_read_b64_tr_b16 a[170:171], v48 offset:512
v_mfma_f32_16x16x32_bf16 v[132:135], a[24:27], a[4:7], v[132:135]
s_lshl_b32 s54, s54, 13
buffer_load_dwordx4 v41, s[16:19], s26 offen offset:1024 lds
v_mfma_f32_16x16x32_bf16 v[168:171], a[28:31], a[4:7], v[168:171]
s_add_u32 s54, s54, 98304
ds_read_b128 a[132:135], v43 offset:0
v_mfma_f32_16x16x32_bf16 v[172:175], a[32:35], a[4:7], v[172:175]
ds_read_b64_tr_b16 a[172:173], v47 offset:2048
ds_read_b64_tr_b16 a[174:175], v47 offset:2560
v_mfma_f32_16x16x32_bf16 v[208:211], a[4:7], a[36:39], v[208:211]
s_lshr_b32 s56, s3, 1
buffer_load_dwordx4 v40, s[16:19], s26 offen offset:2048 lds
v_mfma_f32_16x16x32_bf16 v[212:215], a[4:7], a[40:43], v[212:215]
s_lshl_b32 s56, s56, 2
ds_read_b128 a[136:139], v43 offset:2048
v_mfma_f32_16x16x32_bf16 v[136:139], a[20:23], a[8:11], v[136:139]
ds_read_b64_tr_b16 a[192:193], v48 offset:2048
ds_read_b64_tr_b16 a[194:195], v48 offset:2560
v_mfma_f32_16x16x32_bf16 v[140:143], a[24:27], a[8:11], v[140:143]
s_sub_u32 s57, 15, s51
buffer_load_dwordx4 v41, s[16:19], s26 offen offset:3072 lds
v_mfma_f32_16x16x32_bf16 v[176:179], a[28:31], a[8:11], v[176:179]
s_min_u32 s57, s57, s51
ds_read_b128 a[140:143], v43 offset:4096
v_mfma_f32_16x16x32_bf16 v[180:183], a[32:35], a[8:11], v[180:183]
s_add_u32 m0, s48, 135168
ds_read_b64_tr_b16 a[196:197], v49 offset:4096
ds_read_b64_tr_b16 a[198:199], v49 offset:4608
v_mfma_f32_16x16x32_bf16 v[216:219], a[8:11], a[36:39], v[216:219]
s_lshl_b32 s57, s57, 10
buffer_load_dwordx4 v40, s[16:19], s47 offen offset:0 lds
v_mfma_f32_16x16x32_bf16 v[220:223], a[8:11], a[40:43], v[220:223]
ds_read_b128 a[160:163], v43 offset:6144
v_lshrrev_b32 v252, 4, v1
v_mfma_f32_16x16x32_bf16 v[144:147], a[20:23], a[12:15], v[144:147]
ds_read_b64_tr_b16 a[200:201], v50 offset:4096
ds_read_b64_tr_b16 a[202:203], v50 offset:4608
v_mfma_f32_16x16x32_bf16 v[148:151], a[24:27], a[12:15], v[148:151]
buffer_load_dwordx4 v41, s[16:19], s47 offen offset:1024 lds
v_and_b32 v253, 15, v1
v_mfma_f32_16x16x32_bf16 v[184:187], a[28:31], a[12:15], v[184:187]
v_lshlrev_b32 v254, 1, v252
v_xor_b32 v254, v254, v253
v_mfma_f32_16x16x32_bf16 v[188:191], a[32:35], a[12:15], v[188:191]
v_lshlrev_b32 v254, 4, v254
v_lshl_add_u32 v240, v252, 11, v254
v_mfma_f32_16x16x32_bf16 v[224:227], a[12:15], a[36:39], v[224:227]
buffer_load_dwordx4 v42, s[12:15], s25 offen offset:2048 lds
v_xor_b32 v241, 0x80, v240
v_mfma_f32_16x16x32_bf16 v[228:231], a[12:15], a[40:43], v[228:231]
v_add_u32 v241, 7168, v241
v_add_u32 v242, 14336, v240
v_mfma_f32_16x16x32_bf16 v[152:155], a[20:23], a[16:19], v[152:155]
s_add_u32 s26, s26, 393216
s_add_u32 s47, s47, 393216
s_add_u32 s25, s25, 128
v_mfma_f32_16x16x32_bf16 v[156:159], a[24:27], a[16:19], v[156:159]
v_add_u32 v243, 14336, v241
v_lshlrev_b32 v254, 9, v252
v_mfma_f32_16x16x32_bf16 v[192:195], a[28:31], a[16:19], v[192:195]
v_lshl_add_u32 v254, v253, 4, v254
v_add_u32 v244, s54, v254
v_mfma_f32_16x16x32_bf16 v[196:199], a[32:35], a[16:19], v[196:199]
v_bfe_u32 v254, v1, 2, 3
v_lshrrev_b32 v255, 5, v1
v_mfma_f32_16x16x32_bf16 v[232:235], a[16:19], a[36:39], v[232:235]
v_lshl_add_u32 v255, v255, 4, v254
v_lshlrev_b32 v255, 8, v255
v_mfma_f32_16x16x32_bf16 v[236:239], a[16:19], a[40:43], v[236:239]
s_waitcnt lgkmcnt(0)
v_and_b32 v253, 3, v1
v_lshl_add_u32 v255, v253, 3, v255
v_mfma_f32_16x16x32_bf16 v[160:163], a[172:175], a[128:131], v[160:163]
v_add_u32 v255, 114688, v255
v_xor_b32 v254, s56, v254
v_mfma_f32_16x16x32_bf16 v[164:167], a[192:195], a[128:131], v[164:167]
s_add_u32 m0, s91, 81920
ds_read_b64_tr_b16 a[20:21], v47 offset:14336
ds_read_b64_tr_b16 a[22:23], v47 offset:14848
v_mfma_f32_16x16x32_bf16 v[200:203], a[128:131], a[196:199], v[200:203]
buffer_load_dwordx4 v36, s[12:15], s24 offen offset:0 lds
v_xor_b32 v253, 0, v254
v_mfma_f32_16x16x32_bf16 v[204:207], a[128:131], a[200:203], v[204:207]
ds_read_b128 a[0:3], v46
v_lshl_add_u32 v245, v253, 5, v255
v_mfma_f32_16x16x32_bf16 v[128:131], a[164:167], a[132:135], v[128:131]
ds_read_b64_tr_b16 a[24:25], v48 offset:14336
ds_read_b64_tr_b16 a[26:27], v48 offset:14848
v_mfma_f32_16x16x32_bf16 v[132:135], a[168:171], a[132:135], v[132:135]
v_xor_b32 v253, 1, v254
v_lshl_add_u32 v246, v253, 5, v255
v_mfma_f32_16x16x32_bf16 v[168:171], a[172:175], a[132:135], v[168:171]
ds_read_b128 a[4:7], v44 offset:0
v_xor_b32 v253, 2, v254
v_mfma_f32_16x16x32_bf16 v[172:175], a[192:195], a[132:135], v[172:175]
ds_read_b64_tr_b16 a[28:29], v47 offset:16384
ds_read_b64_tr_b16 a[30:31], v47 offset:16896
v_mfma_f32_16x16x32_bf16 v[208:211], a[132:135], a[196:199], v[208:211]
buffer_load_dwordx4 v37, s[12:15], s24 offen offset:1024 lds
v_lshl_add_u32 v247, v253, 5, v255
v_mfma_f32_16x16x32_bf16 v[212:215], a[132:135], a[200:203], v[212:215]
ds_read_b128 a[8:11], v44 offset:2048
v_xor_b32 v253, 3, v254
v_mfma_f32_16x16x32_bf16 v[136:139], a[164:167], a[136:139], v[136:139]
ds_read_b64_tr_b16 a[32:33], v48 offset:16384
ds_read_b64_tr_b16 a[34:35], v48 offset:16896
v_mfma_f32_16x16x32_bf16 v[140:143], a[168:171], a[136:139], v[140:143]
v_lshl_add_u32 v248, v253, 5, v255
v_and_b32 v253, 31, v1
v_mfma_f32_16x16x32_bf16 v[176:179], a[172:175], a[136:139], v[176:179]
ds_read_b128 a[12:15], v44 offset:4096
v_lshl_add_u32 v249, v253, 5, s57
v_mfma_f32_16x16x32_bf16 v[180:183], a[192:195], a[136:139], v[180:183]
ds_read_b64_tr_b16 a[36:37], v49 offset:18432
ds_read_b64_tr_b16 a[38:39], v49 offset:18944
v_mfma_f32_16x16x32_bf16 v[216:219], a[136:139], a[196:199], v[216:219]
buffer_load_dwordx4 v38, s[12:15], s24 offen offset:2048 lds
v_mfma_f32_16x16x32_bf16 v[220:223], a[136:139], a[200:203], v[220:223]
ds_read_b128 a[16:19], v44 offset:6144
v_mfma_f32_16x16x32_bf16 v[144:147], a[164:167], a[140:143], v[144:147]
ds_read_b64_tr_b16 a[40:41], v50 offset:18432
ds_read_b64_tr_b16 a[42:43], v50 offset:18944
v_mfma_f32_16x16x32_bf16 v[148:151], a[168:171], a[140:143], v[148:151]
s_cmp_eq_u32 s39, 1
s_mov_b32 s28, 16384
s_cselect_b32 s28, 4294901760, s28
v_mfma_f32_16x16x32_bf16 v[184:187], a[172:175], a[140:143], v[184:187]
v_add_u32 v43, 16384, v43
v_add_u32 v44, 16384, v44
v_mfma_f32_16x16x32_bf16 v[188:191], a[192:195], a[140:143], v[188:191]
v_add_u32 v47, 4294901760, v47
v_add_u32 v48, 4294901760, v48
v_mfma_f32_16x16x32_bf16 v[224:227], a[140:143], a[196:199], v[224:227]
buffer_load_dwordx4 v39, s[12:15], s24 offen offset:3072 lds
v_add_u32 v49, 4294901760, v49
v_add_u32 v50, 4294901760, v50
v_mfma_f32_16x16x32_bf16 v[228:231], a[140:143], a[200:203], v[228:231]
v_add_u32 v45, s28, v45
v_add_u32 v46, s28, v46
v_mfma_f32_16x16x32_bf16 v[152:155], a[164:167], a[160:163], v[152:155]
v_mfma_f32_16x16x32_bf16 v[156:159], a[168:171], a[160:163], v[156:159]
s_add_u32 s24, s24, 128
v_mfma_f32_16x16x32_bf16 v[192:195], a[172:175], a[160:163], v[192:195]
v_mfma_f32_16x16x32_bf16 v[196:199], a[192:195], a[160:163], v[196:199]
v_mfma_f32_16x16x32_bf16 v[232:235], a[160:163], a[196:199], v[232:235]
v_mfma_f32_16x16x32_bf16 v[236:239], a[160:163], a[200:203], v[236:239]
s_waitcnt vmcnt(26)
s_waitcnt vmcnt(15) lgkmcnt(0)
s_barrier
v_mfma_f32_16x16x32_bf16 v[160:163], a[28:31], a[0:3], v[160:163]
s_nop 0
v_or_b32 v0, 3, v11
v_mfma_f32_16x16x32_bf16 v[164:167], a[32:35], a[0:3], v[164:167]
s_add_u32 m0, s48, 98304
ds_read_b64_tr_b16 a[164:165], v47 offset:0
ds_read_b64_tr_b16 a[166:167], v47 offset:512
v_mfma_f32_16x16x32_bf16 v[200:203], a[0:3], a[36:39], v[200:203]
buffer_load_dwordx4 v40, s[16:19], s26 offen offset:0 lds
v_mfma_f32_16x16x32_bf16 v[204:207], a[0:3], a[40:43], v[204:207]
ds_read_b128 a[128:131], v45
v_mfma_f32_16x16x32_bf16 v[128:131], a[20:23], a[4:7], v[128:131]
ds_read_b64_tr_b16 a[168:169], v48 offset:0
ds_read_b64_tr_b16 a[170:171], v48 offset:512
v_mfma_f32_16x16x32_bf16 v[132:135], a[24:27], a[4:7], v[132:135]
buffer_load_dwordx4 v41, s[16:19], s26 offen offset:1024 lds
v_mfma_f32_16x16x32_bf16 v[168:171], a[28:31], a[4:7], v[168:171]
ds_read_b128 a[132:135], v43 offset:0
v_mfma_f32_16x16x32_bf16 v[172:175], a[32:35], a[4:7], v[172:175]
ds_read_b64_tr_b16 a[172:173], v47 offset:2048
ds_read_b64_tr_b16 a[174:175], v47 offset:2560
v_mfma_f32_16x16x32_bf16 v[208:211], a[4:7], a[36:39], v[208:211]
buffer_load_dwordx4 v40, s[16:19], s26 offen offset:2048 lds
v_mfma_f32_16x16x32_bf16 v[212:215], a[4:7], a[40:43], v[212:215]
ds_read_b128 a[136:139], v43 offset:2048
v_mfma_f32_16x16x32_bf16 v[136:139], a[20:23], a[8:11], v[136:139]
ds_read_b64_tr_b16 a[192:193], v48 offset:2048
ds_read_b64_tr_b16 a[194:195], v48 offset:2560
v_mfma_f32_16x16x32_bf16 v[140:143], a[24:27], a[8:11], v[140:143]
buffer_load_dwordx4 v41, s[16:19], s26 offen offset:3072 lds
v_mfma_f32_16x16x32_bf16 v[176:179], a[28:31], a[8:11], v[176:179]
ds_read_b128 a[140:143], v43 offset:4096
v_mfma_f32_16x16x32_bf16 v[180:183], a[32:35], a[8:11], v[180:183]
s_add_u32 m0, s48, 102400
ds_read_b64_tr_b16 a[196:197], v49 offset:4096
ds_read_b64_tr_b16 a[198:199], v49 offset:4608
v_mfma_f32_16x16x32_bf16 v[216:219], a[8:11], a[36:39], v[216:219]
buffer_load_dwordx4 v40, s[16:19], s47 offen offset:0 lds
v_mfma_f32_16x16x32_bf16 v[220:223], a[8:11], a[40:43], v[220:223]
ds_read_b128 a[160:163], v43 offset:6144
v_mfma_f32_16x16x32_bf16 v[144:147], a[20:23], a[12:15], v[144:147]
ds_read_b64_tr_b16 a[200:201], v50 offset:4096
ds_read_b64_tr_b16 a[202:203], v50 offset:4608
v_mfma_f32_16x16x32_bf16 v[148:151], a[24:27], a[12:15], v[148:151]
buffer_load_dwordx4 v41, s[16:19], s47 offen offset:1024 lds
v_mfma_f32_16x16x32_bf16 v[184:187], a[28:31], a[12:15], v[184:187]
v_mfma_f32_16x16x32_bf16 v[188:191], a[32:35], a[12:15], v[188:191]
v_mfma_f32_16x16x32_bf16 v[224:227], a[12:15], a[36:39], v[224:227]
buffer_load_dwordx4 v42, s[12:15], s25 offen offset:2048 lds
v_mfma_f32_16x16x32_bf16 v[228:231], a[12:15], a[40:43], v[228:231]
v_mfma_f32_16x16x32_bf16 v[152:155], a[20:23], a[16:19], v[152:155]
s_add_u32 s26, s26, 393216
s_add_u32 s47, s47, 393216
s_add_u32 s25, s25, 128
v_mfma_f32_16x16x32_bf16 v[156:159], a[24:27], a[16:19], v[156:159]
v_mfma_f32_16x16x32_bf16 v[192:195], a[28:31], a[16:19], v[192:195]
buffer_store_dword v0, v6, s[60:63], 0 offen sc1
v_mfma_f32_16x16x32_bf16 v[196:199], a[32:35], a[16:19], v[196:199]
v_mfma_f32_16x16x32_bf16 v[232:235], a[16:19], a[36:39], v[232:235]
v_mfma_f32_16x16x32_bf16 v[236:239], a[16:19], a[40:43], v[236:239]
s_waitcnt lgkmcnt(0)
v_mfma_f32_16x16x32_bf16 v[160:163], a[172:175], a[128:131], v[160:163]
v_mfma_f32_16x16x32_bf16 v[164:167], a[192:195], a[128:131], v[164:167]
s_add_u32 m0, s91, 0
ds_read_b64_tr_b16 a[20:21], v47 offset:14336
ds_read_b64_tr_b16 a[22:23], v47 offset:14848
v_mfma_f32_16x16x32_bf16 v[200:203], a[128:131], a[196:199], v[200:203]
buffer_load_dwordx4 v36, s[12:15], s24 offen offset:0 lds
v_mfma_f32_16x16x32_bf16 v[204:207], a[128:131], a[200:203], v[204:207]
ds_read_b128 a[0:3], v46
v_mfma_f32_16x16x32_bf16 v[128:131], a[164:167], a[132:135], v[128:131]
ds_read_b64_tr_b16 a[24:25], v48 offset:14336
ds_read_b64_tr_b16 a[26:27], v48 offset:14848
v_mfma_f32_16x16x32_bf16 v[132:135], a[168:171], a[132:135], v[132:135]
v_mfma_f32_16x16x32_bf16 v[168:171], a[172:175], a[132:135], v[168:171]
ds_read_b128 a[4:7], v44 offset:0
v_mfma_f32_16x16x32_bf16 v[172:175], a[192:195], a[132:135], v[172:175]
ds_read_b64_tr_b16 a[28:29], v47 offset:16384
ds_read_b64_tr_b16 a[30:31], v47 offset:16896
v_mfma_f32_16x16x32_bf16 v[208:211], a[132:135], a[196:199], v[208:211]
buffer_load_dwordx4 v37, s[12:15], s24 offen offset:1024 lds
v_mfma_f32_16x16x32_bf16 v[212:215], a[132:135], a[200:203], v[212:215]
ds_read_b128 a[8:11], v44 offset:2048
v_mfma_f32_16x16x32_bf16 v[136:139], a[164:167], a[136:139], v[136:139]
ds_read_b64_tr_b16 a[32:33], v48 offset:16384
ds_read_b64_tr_b16 a[34:35], v48 offset:16896
v_mfma_f32_16x16x32_bf16 v[140:143], a[168:171], a[136:139], v[140:143]
v_mfma_f32_16x16x32_bf16 v[176:179], a[172:175], a[136:139], v[176:179]
ds_read_b128 a[12:15], v44 offset:4096
v_mfma_f32_16x16x32_bf16 v[180:183], a[192:195], a[136:139], v[180:183]
ds_read_b64_tr_b16 a[36:37], v49 offset:18432
ds_read_b64_tr_b16 a[38:39], v49 offset:18944
v_mfma_f32_16x16x32_bf16 v[216:219], a[136:139], a[196:199], v[216:219]
buffer_load_dwordx4 v38, s[12:15], s24 offen offset:2048 lds
v_mfma_f32_16x16x32_bf16 v[220:223], a[136:139], a[200:203], v[220:223]
ds_read_b128 a[16:19], v44 offset:6144
v_mfma_f32_16x16x32_bf16 v[144:147], a[164:167], a[140:143], v[144:147]
ds_read_b64_tr_b16 a[40:41], v50 offset:18432
ds_read_b64_tr_b16 a[42:43], v50 offset:18944
v_mfma_f32_16x16x32_bf16 v[148:151], a[168:171], a[140:143], v[148:151]
s_cmp_eq_u32 s39, 1
s_mov_b32 s28, 49152
s_cselect_b32 s28, 98304, s28
v_mfma_f32_16x16x32_bf16 v[184:187], a[172:175], a[140:143], v[184:187]
v_add_u32 v43, 49152, v43
v_add_u32 v44, 49152, v44
v_mfma_f32_16x16x32_bf16 v[188:191], a[192:195], a[140:143], v[188:191]
v_add_u32 v47, 98304, v47
v_add_u32 v48, 98304, v48
v_mfma_f32_16x16x32_bf16 v[224:227], a[140:143], a[196:199], v[224:227]
buffer_load_dwordx4 v39, s[12:15], s24 offen offset:3072 lds
v_add_u32 v49, 98304, v49
v_add_u32 v50, 98304, v50
v_mfma_f32_16x16x32_bf16 v[228:231], a[140:143], a[200:203], v[228:231]
v_add_u32 v45, s28, v45
v_add_u32 v46, s28, v46
v_mfma_f32_16x16x32_bf16 v[152:155], a[164:167], a[160:163], v[152:155]
v_mfma_f32_16x16x32_bf16 v[156:159], a[168:171], a[160:163], v[156:159]
s_add_u32 s24, s24, 128
v_mfma_f32_16x16x32_bf16 v[192:195], a[172:175], a[160:163], v[192:195]
v_mfma_f32_16x16x32_bf16 v[196:199], a[192:195], a[160:163], v[196:199]
v_mfma_f32_16x16x32_bf16 v[232:235], a[160:163], a[196:199], v[232:235]
v_mfma_f32_16x16x32_bf16 v[236:239], a[160:163], a[200:203], v[236:239]
s_waitcnt vmcnt(16) lgkmcnt(0)
s_barrier
v_mfma_f32_16x16x32_bf16 v[160:163], a[28:31], a[0:3], v[160:163]
v_mfma_f32_16x16x32_bf16 v[164:167], a[32:35], a[0:3], v[164:167]
s_add_u32 m0, s48, 32768
ds_read_b64_tr_b16 a[164:165], v47 offset:0
ds_read_b64_tr_b16 a[166:167], v47 offset:512
v_mfma_f32_16x16x32_bf16 v[200:203], a[0:3], a[36:39], v[200:203]
buffer_load_dwordx4 v40, s[16:19], s26 offen offset:0 lds
v_mfma_f32_16x16x32_bf16 v[204:207], a[0:3], a[40:43], v[204:207]
ds_read_b128 a[128:131], v45
v_mfma_f32_16x16x32_bf16 v[128:131], a[20:23], a[4:7], v[128:131]
ds_read_b64_tr_b16 a[168:169], v48 offset:0
ds_read_b64_tr_b16 a[170:171], v48 offset:512
v_mfma_f32_16x16x32_bf16 v[132:135], a[24:27], a[4:7], v[132:135]
buffer_load_dwordx4 v41, s[16:19], s26 offen offset:1024 lds
v_mfma_f32_16x16x32_bf16 v[168:171], a[28:31], a[4:7], v[168:171]
ds_read_b128 a[132:135], v43 offset:0
v_mfma_f32_16x16x32_bf16 v[172:175], a[32:35], a[4:7], v[172:175]
ds_read_b64_tr_b16 a[172:173], v47 offset:2048
ds_read_b64_tr_b16 a[174:175], v47 offset:2560
v_mfma_f32_16x16x32_bf16 v[208:211], a[4:7], a[36:39], v[208:211]
buffer_load_dwordx4 v40, s[16:19], s26 offen offset:2048 lds
v_mfma_f32_16x16x32_bf16 v[212:215], a[4:7], a[40:43], v[212:215]
ds_read_b128 a[136:139], v43 offset:2048
v_mfma_f32_16x16x32_bf16 v[136:139], a[20:23], a[8:11], v[136:139]
ds_read_b64_tr_b16 a[192:193], v48 offset:2048
ds_read_b64_tr_b16 a[194:195], v48 offset:2560
v_mfma_f32_16x16x32_bf16 v[140:143], a[24:27], a[8:11], v[140:143]
buffer_load_dwordx4 v41, s[16:19], s26 offen offset:3072 lds
v_mfma_f32_16x16x32_bf16 v[176:179], a[28:31], a[8:11], v[176:179]
ds_read_b128 a[140:143], v43 offset:4096
v_mfma_f32_16x16x32_bf16 v[180:183], a[32:35], a[8:11], v[180:183]
s_add_u32 m0, s48, 36864
ds_read_b64_tr_b16 a[196:197], v49 offset:4096
ds_read_b64_tr_b16 a[198:199], v49 offset:4608
v_mfma_f32_16x16x32_bf16 v[216:219], a[8:11], a[36:39], v[216:219]
buffer_load_dwordx4 v40, s[16:19], s47 offen offset:0 lds
v_mfma_f32_16x16x32_bf16 v[220:223], a[8:11], a[40:43], v[220:223]
ds_read_b128 a[160:163], v43 offset:6144
v_mfma_f32_16x16x32_bf16 v[144:147], a[20:23], a[12:15], v[144:147]
ds_read_b64_tr_b16 a[200:201], v50 offset:4096
ds_read_b64_tr_b16 a[202:203], v50 offset:4608
v_mfma_f32_16x16x32_bf16 v[148:151], a[24:27], a[12:15], v[148:151]
buffer_load_dwordx4 v41, s[16:19], s47 offen offset:1024 lds
v_mfma_f32_16x16x32_bf16 v[184:187], a[28:31], a[12:15], v[184:187]
v_mfma_f32_16x16x32_bf16 v[188:191], a[32:35], a[12:15], v[188:191]
v_mfma_f32_16x16x32_bf16 v[224:227], a[12:15], a[36:39], v[224:227]
buffer_load_dwordx4 v42, s[12:15], s25 offen offset:2048 lds
v_mfma_f32_16x16x32_bf16 v[228:231], a[12:15], a[40:43], v[228:231]
v_mfma_f32_16x16x32_bf16 v[152:155], a[20:23], a[16:19], v[152:155]
s_add_u32 s26, s26, 393216
s_add_u32 s47, s47, 393216
s_add_u32 s25, s25, 128
v_mfma_f32_16x16x32_bf16 v[156:159], a[24:27], a[16:19], v[156:159]
v_mfma_f32_16x16x32_bf16 v[192:195], a[28:31], a[16:19], v[192:195]
v_mfma_f32_16x16x32_bf16 v[196:199], a[32:35], a[16:19], v[196:199]
v_mfma_f32_16x16x32_bf16 v[232:235], a[16:19], a[36:39], v[232:235]
v_mfma_f32_16x16x32_bf16 v[236:239], a[16:19], a[40:43], v[236:239]
s_waitcnt lgkmcnt(0)
v_mfma_f32_16x16x32_bf16 v[160:163], a[172:175], a[128:131], v[160:163]
v_and_b32 v28, 15, v1
v_lshrrev_b32 v29, 4, v1
v_mfma_f32_16x16x32_bf16 v[164:167], a[192:195], a[128:131], v[164:167]
s_add_u32 m0, s91, 16384
ds_read_b64_tr_b16 a[20:21], v47 offset:14336
ds_read_b64_tr_b16 a[22:23], v47 offset:14848
v_mfma_f32_16x16x32_bf16 v[200:203], a[128:131], a[196:199], v[200:203]
buffer_load_dwordx4 v36, s[12:15], s24 offen offset:0 lds
v_lshlrev_b32 v29, 2, v29
v_mfma_f32_16x16x32_bf16 v[204:207], a[128:131], a[200:203], v[204:207]
ds_read_b128 a[0:3], v46
v_sub_u32 v28, v28, v29
v_mfma_f32_16x16x32_bf16 v[128:131], a[164:167], a[132:135], v[128:131]
ds_read_b64_tr_b16 a[24:25], v48 offset:14336
ds_read_b64_tr_b16 a[26:27], v48 offset:14848
v_mfma_f32_16x16x32_bf16 v[132:135], a[168:171], a[132:135], v[132:135]
v_mov_b32 v30, 0
v_add_u32 v29, 16, v28
v_mfma_f32_16x16x32_bf16 v[168:171], a[172:175], a[132:135], v[168:171]
ds_read_b128 a[4:7], v44 offset:0
v_and_b32 v29, 0xfffffff1, v29
v_mfma_f32_16x16x32_bf16 v[172:175], a[192:195], a[132:135], v[172:175]
ds_read_b64_tr_b16 a[28:29], v47 offset:16384
ds_read_b64_tr_b16 a[30:31], v47 offset:16896
v_mfma_f32_16x16x32_bf16 v[208:211], a[132:135], a[196:199], v[208:211]
buffer_load_dwordx4 v37, s[12:15], s24 offen offset:1024 lds
v_cmp_eq_u32 vcc, 0, v29
v_mfma_f32_16x16x32_bf16 v[212:215], a[132:135], a[200:203], v[212:215]
ds_read_b128 a[8:11], v44 offset:2048
v_nop
v_mfma_f32_16x16x32_bf16 v[136:139], a[164:167], a[136:139], v[136:139]
ds_read_b64_tr_b16 a[32:33], v48 offset:16384
ds_read_b64_tr_b16 a[34:35], v48 offset:16896
v_mfma_f32_16x16x32_bf16 v[140:143], a[168:171], a[136:139], v[140:143]
v_nop
v_cndmask_b32 v16, v34, v30, vcc
v_mfma_f32_16x16x32_bf16 v[176:179], a[172:175], a[136:139], v[176:179]
ds_read_b128 a[12:15], v44 offset:4096
v_add_u32 v29, 15, v28
v_mfma_f32_16x16x32_bf16 v[180:183], a[192:195], a[136:139], v[180:183]
ds_read_b64_tr_b16 a[36:37], v49 offset:18432
ds_read_b64_tr_b16 a[38:39], v49 offset:18944
v_mfma_f32_16x16x32_bf16 v[216:219], a[136:139], a[196:199], v[216:219]
buffer_load_dwordx4 v38, s[12:15], s24 offen offset:2048 lds
v_and_b32 v29, 0xfffffff1, v29
v_mfma_f32_16x16x32_bf16 v[220:223], a[136:139], a[200:203], v[220:223]
ds_read_b128 a[16:19], v44 offset:6144
v_cmp_eq_u32 vcc, 0, v29
v_mfma_f32_16x16x32_bf16 v[144:147], a[164:167], a[140:143], v[144:147]
ds_read_b64_tr_b16 a[40:41], v50 offset:18432
ds_read_b64_tr_b16 a[42:43], v50 offset:18944
v_mfma_f32_16x16x32_bf16 v[148:151], a[168:171], a[140:143], v[148:151]
s_cmp_eq_u32 s39, 1
s_mov_b32 s28, 16384
s_cselect_b32 s28, 4294934528, s28
v_mfma_f32_16x16x32_bf16 v[184:187], a[172:175], a[140:143], v[184:187]
v_add_u32 v43, 16384, v43
v_add_u32 v44, 16384, v44
v_mfma_f32_16x16x32_bf16 v[188:191], a[192:195], a[140:143], v[188:191]
v_add_u32 v47, 4294934528, v47
v_add_u32 v48, 4294934528, v48
v_mfma_f32_16x16x32_bf16 v[224:227], a[140:143], a[196:199], v[224:227]
buffer_load_dwordx4 v39, s[12:15], s24 offen offset:3072 lds
v_add_u32 v49, 4294934528, v49
v_add_u32 v50, 4294934528, v50
v_mfma_f32_16x16x32_bf16 v[228:231], a[140:143], a[200:203], v[228:231]
v_add_u32 v45, s28, v45
v_add_u32 v46, s28, v46
v_mfma_f32_16x16x32_bf16 v[152:155], a[164:167], a[160:163], v[152:155]
v_nop
v_nop
v_mfma_f32_16x16x32_bf16 v[156:159], a[168:171], a[160:163], v[156:159]
s_add_u32 s24, s24, 128
v_cndmask_b32 v17, v34, v30, vcc
v_add_u32 v29, 14, v28
v_mfma_f32_16x16x32_bf16 v[192:195], a[172:175], a[160:163], v[192:195]
v_and_b32 v29, 0xfffffff1, v29
v_cmp_eq_u32 vcc, 0, v29
v_mfma_f32_16x16x32_bf16 v[196:199], a[192:195], a[160:163], v[196:199]
v_nop
v_nop
v_mfma_f32_16x16x32_bf16 v[232:235], a[160:163], a[196:199], v[232:235]
v_cndmask_b32 v18, v34, v30, vcc
v_add_u32 v29, 13, v28
v_mfma_f32_16x16x32_bf16 v[236:239], a[160:163], a[200:203], v[236:239]
s_waitcnt vmcnt(16) lgkmcnt(0)
s_barrier
v_and_b32 v29, 0xfffffff1, v29
v_mfma_f32_16x16x32_bf16 v[160:163], a[28:31], a[0:3], v[160:163]
v_cmp_eq_u32 vcc, 0, v29
v_nop
v_mfma_f32_16x16x32_bf16 v[164:167], a[32:35], a[0:3], v[164:167]
s_add_u32 m0, s48, 131072
ds_read_b64_tr_b16 a[164:165], v47 offset:0
ds_read_b64_tr_b16 a[166:167], v47 offset:512
v_mfma_f32_16x16x32_bf16 v[200:203], a[0:3], a[36:39], v[200:203]
buffer_load_dwordx4 v40, s[16:19], s26 offen offset:0 lds
v_nop
v_mfma_f32_16x16x32_bf16 v[204:207], a[0:3], a[40:43], v[204:207]
ds_read_b128 a[128:131], v45
v_cndmask_b32 v19, v34, v30, vcc
v_mfma_f32_16x16x32_bf16 v[128:131], a[20:23], a[4:7], v[128:131]
ds_read_b64_tr_b16 a[168:169], v48 offset:0
ds_read_b64_tr_b16 a[170:171], v48 offset:512
v_mfma_f32_16x16x32_bf16 v[132:135], a[24:27], a[4:7], v[132:135]
buffer_load_dwordx4 v41, s[16:19], s26 offen offset:1024 lds
v_add_u32 v29, 0, v28
v_mfma_f32_16x16x32_bf16 v[168:171], a[28:31], a[4:7], v[168:171]
ds_read_b128 a[132:135], v43 offset:0
v_and_b32 v29, 0xfffffff1, v29
v_mfma_f32_16x16x32_bf16 v[172:175], a[32:35], a[4:7], v[172:175]
ds_read_b64_tr_b16 a[172:173], v47 offset:2048
ds_read_b64_tr_b16 a[174:175], v47 offset:2560
v_mfma_f32_16x16x32_bf16 v[208:211], a[4:7], a[36:39], v[208:211]
buffer_load_dwordx4 v40, s[16:19], s26 offen offset:2048 lds
v_cmp_eq_u32 vcc, 0, v29
v_mfma_f32_16x16x32_bf16 v[212:215], a[4:7], a[40:43], v[212:215]
ds_read_b128 a[136:139], v43 offset:2048
v_nop
v_mfma_f32_16x16x32_bf16 v[136:139], a[20:23], a[8:11], v[136:139]
ds_read_b64_tr_b16 a[192:193], v48 offset:2048
ds_read_b64_tr_b16 a[194:195], v48 offset:2560
v_mfma_f32_16x16x32_bf16 v[140:143], a[24:27], a[8:11], v[140:143]
buffer_load_dwordx4 v41, s[16:19], s26 offen offset:3072 lds
v_nop
v_mfma_f32_16x16x32_bf16 v[176:179], a[28:31], a[8:11], v[176:179]
ds_read_b128 a[140:143], v43 offset:4096
v_cndmask_b32 v20, v34, v30, vcc
v_mfma_f32_16x16x32_bf16 v[180:183], a[32:35], a[8:11], v[180:183]
s_add_u32 m0, s48, 135168
ds_read_b64_tr_b16 a[196:197], v49 offset:4096
ds_read_b64_tr_b16 a[198:199], v49 offset:4608
v_mfma_f32_16x16x32_bf16 v[216:219], a[8:11], a[36:39], v[216:219]
buffer_load_dwordx4 v40, s[16:19], s47 offen offset:0 lds
v_add_u32 v29, -1, v28
v_mfma_f32_16x16x32_bf16 v[220:223], a[8:11], a[40:43], v[220:223]
ds_read_b128 a[160:163], v43 offset:6144
v_and_b32 v29, 0xfffffff1, v29
v_mfma_f32_16x16x32_bf16 v[144:147], a[20:23], a[12:15], v[144:147]
ds_read_b64_tr_b16 a[200:201], v50 offset:4096
ds_read_b64_tr_b16 a[202:203], v50 offset:4608
v_mfma_f32_16x16x32_bf16 v[148:151], a[24:27], a[12:15], v[148:151]
buffer_load_dwordx4 v41, s[16:19], s47 offen offset:1024 lds
v_cmp_eq_u32 vcc, 0, v29
v_mfma_f32_16x16x32_bf16 v[184:187], a[28:31], a[12:15], v[184:187]
v_nop
v_nop
v_mfma_f32_16x16x32_bf16 v[188:191], a[32:35], a[12:15], v[188:191]
v_cndmask_b32 v21, v34, v30, vcc
v_add_u32 v29, -2, v28
v_mfma_f32_16x16x32_bf16 v[224:227], a[12:15], a[36:39], v[224:227]
buffer_load_dwordx4 v42, s[12:15], s25 offen offset:2048 lds
v_and_b32 v29, 0xfffffff1, v29
v_mfma_f32_16x16x32_bf16 v[228:231], a[12:15], a[40:43], v[228:231]
v_cmp_eq_u32 vcc, 0, v29
v_nop
v_mfma_f32_16x16x32_bf16 v[152:155], a[20:23], a[16:19], v[152:155]
s_add_u32 s26, s26, 393216
s_add_u32 s47, s47, 393216
s_add_u32 s25, s25, 128
v_mfma_f32_16x16x32_bf16 v[156:159], a[24:27], a[16:19], v[156:159]
v_nop
v_cndmask_b32 v22, v34, v30, vcc
v_mfma_f32_16x16x32_bf16 v[192:195], a[28:31], a[16:19], v[192:195]
v_add_u32 v29, -3, v28
v_and_b32 v29, 0xfffffff1, v29
v_mfma_f32_16x16x32_bf16 v[196:199], a[32:35], a[16:19], v[196:199]
v_cmp_eq_u32 vcc, 0, v29
v_nop
v_mfma_f32_16x16x32_bf16 v[232:235], a[16:19], a[36:39], v[232:235]
v_nop
v_cndmask_b32 v23, v34, v30, vcc
v_mfma_f32_16x16x32_bf16 v[236:239], a[16:19], a[40:43], v[236:239]
s_waitcnt lgkmcnt(0)
s_lshr_b32 s29, s3, 1
s_or_b32 s29, s29, s36
v_mfma_f32_16x16x32_bf16 v[160:163], a[172:175], a[128:131], v[160:163]
s_cmp_eq_u32 s29, 0
s_cselect_b64 s[32:33], -1, 0
v_mfma_f32_16x16x32_bf16 v[164:167], a[192:195], a[128:131], v[164:167]
ds_read_b64_tr_b16 a[20:21], v47 offset:14336
ds_read_b64_tr_b16 a[22:23], v47 offset:14848
v_mfma_f32_16x16x32_bf16 v[200:203], a[128:131], a[196:199], v[200:203]
v_cndmask_b32 v24, v16, v34, s[32:33]
v_cndmask_b32 v25, v17, v34, s[32:33]
v_mfma_f32_16x16x32_bf16 v[204:207], a[128:131], a[200:203], v[204:207]
ds_read_b128 a[0:3], v46
v_cndmask_b32 v26, v18, v34, s[32:33]
v_mfma_f32_16x16x32_bf16 v[128:131], a[164:167], a[132:135], v[128:131]
ds_read_b64_tr_b16 a[24:25], v48 offset:14336
ds_read_b64_tr_b16 a[26:27], v48 offset:14848
v_mfma_f32_16x16x32_bf16 v[132:135], a[168:171], a[132:135], v[132:135]
v_cndmask_b32 v27, v19, v34, s[32:33]
v_mfma_f32_16x16x32_bf16 v[168:171], a[172:175], a[132:135], v[168:171]
ds_read_b128 a[4:7], v44 offset:0
v_mfma_f32_16x16x32_bf16 v[172:175], a[192:195], a[132:135], v[172:175]
ds_read_b64_tr_b16 a[28:29], v47 offset:16384
ds_read_b64_tr_b16 a[30:31], v47 offset:16896
v_mfma_f32_16x16x32_bf16 v[208:211], a[132:135], a[196:199], v[208:211]
v_mfma_f32_16x16x32_bf16 v[212:215], a[132:135], a[200:203], v[212:215]
ds_read_b128 a[8:11], v44 offset:2048
v_mfma_f32_16x16x32_bf16 v[136:139], a[164:167], a[136:139], v[136:139]
ds_read_b64_tr_b16 a[32:33], v48 offset:16384
ds_read_b64_tr_b16 a[34:35], v48 offset:16896
v_mfma_f32_16x16x32_bf16 v[140:143], a[168:171], a[136:139], v[140:143]
v_mfma_f32_16x16x32_bf16 v[176:179], a[172:175], a[136:139], v[176:179]
ds_read_b128 a[12:15], v44 offset:4096
v_mfma_f32_16x16x32_bf16 v[180:183], a[192:195], a[136:139], v[180:183]
ds_read_b64_tr_b16 a[36:37], v49 offset:18432
ds_read_b64_tr_b16 a[38:39], v49 offset:18944
v_mfma_f32_16x16x32_bf16 v[216:219], a[136:139], a[196:199], v[216:219]
v_mfma_f32_16x16x32_bf16 v[220:223], a[136:139], a[200:203], v[220:223]
ds_read_b128 a[16:19], v44 offset:6144
v_mfma_f32_16x16x32_bf16 v[144:147], a[164:167], a[140:143], v[144:147]
ds_read_b64_tr_b16 a[40:41], v50 offset:18432
ds_read_b64_tr_b16 a[42:43], v50 offset:18944
v_mfma_f32_16x16x32_bf16 v[148:151], a[168:171], a[140:143], v[148:151]
s_cmp_eq_u32 s39, 1
s_mov_b32 s28, 4294885376
s_cselect_b32 s28, 4294901760, s28
v_mfma_f32_16x16x32_bf16 v[184:187], a[172:175], a[140:143], v[184:187]
v_add_u32 v43, 4294885376, v43
v_add_u32 v44, 4294885376, v44
v_mfma_f32_16x16x32_bf16 v[188:191], a[192:195], a[140:143], v[188:191]
v_add_u32 v47, 4294901760, v47
v_add_u32 v48, 4294901760, v48
v_mfma_f32_16x16x32_bf16 v[224:227], a[140:143], a[196:199], v[224:227]
v_add_u32 v49, 4294901760, v49
v_add_u32 v50, 4294901760, v50
v_mfma_f32_16x16x32_bf16 v[228:231], a[140:143], a[200:203], v[228:231]
v_add_u32 v45, s28, v45
v_add_u32 v46, s28, v46
v_mfma_f32_16x16x32_bf16 v[152:155], a[164:167], a[160:163], v[152:155]
v_mfma_f32_16x16x32_bf16 v[156:159], a[168:171], a[160:163], v[156:159]
v_mfma_f32_16x16x32_bf16 v[192:195], a[172:175], a[160:163], v[192:195]
v_mfma_f32_16x16x32_bf16 v[196:199], a[192:195], a[160:163], v[196:199]
v_mfma_f32_16x16x32_bf16 v[232:235], a[160:163], a[196:199], v[232:235]
v_mfma_f32_16x16x32_bf16 v[236:239], a[160:163], a[200:203], v[236:239]
s_waitcnt vmcnt(11) lgkmcnt(0)
s_barrier
v_mfma_f32_16x16x32_bf16 v[160:163], a[28:31], a[0:3], v[160:163]
v_mfma_f32_16x16x32_bf16 v[164:167], a[32:35], a[0:3], v[164:167]
s_add_u32 m0, s91, 114688
ds_read_b64_tr_b16 a[164:165], v47 offset:0
ds_read_b64_tr_b16 a[166:167], v47 offset:512
v_mfma_f32_16x16x32_bf16 v[200:203], a[0:3], a[36:39], v[200:203]
buffer_load_dwordx4 v240, s[8:11], s45 offen offset:0 lds
v_mfma_f32_16x16x32_bf16 v[204:207], a[0:3], a[40:43], v[204:207]
ds_read_b128 a[128:131], v45
v_mfma_f32_16x16x32_bf16 v[128:131], a[20:23], a[4:7], v[128:131]
ds_read_b64_tr_b16 a[168:169], v48 offset:0
ds_read_b64_tr_b16 a[170:171], v48 offset:512
v_mfma_f32_16x16x32_bf16 v[132:135], a[24:27], a[4:7], v[132:135]
buffer_load_dwordx4 v241, s[8:11], s45 offen offset:1024 lds
v_mfma_f32_16x16x32_bf16 v[168:171], a[28:31], a[4:7], v[168:171]
ds_read_b128 a[132:135], v43 offset:0
v_mfma_f32_16x16x32_bf16 v[172:175], a[32:35], a[4:7], v[172:175]
ds_read_b64_tr_b16 a[172:173], v47 offset:2048
ds_read_b64_tr_b16 a[174:175], v47 offset:2560
v_mfma_f32_16x16x32_bf16 v[208:211], a[4:7], a[36:39], v[208:211]
buffer_load_dwordx4 v242, s[8:11], s45 offen offset:2048 lds
v_mfma_f32_16x16x32_bf16 v[212:215], a[4:7], a[40:43], v[212:215]
ds_read_b128 a[136:139], v43 offset:2048
v_mfma_f32_16x16x32_bf16 v[136:139], a[20:23], a[8:11], v[136:139]
ds_read_b64_tr_b16 a[192:193], v48 offset:2048
ds_read_b64_tr_b16 a[194:195], v48 offset:2560
v_mfma_f32_16x16x32_bf16 v[140:143], a[24:27], a[8:11], v[140:143]
buffer_load_dwordx4 v243, s[8:11], s45 offen offset:3072 lds
v_mfma_f32_16x16x32_bf16 v[176:179], a[28:31], a[8:11], v[176:179]
s_add_u32 s45, s45, 131072
ds_read_b128 a[140:143], v43 offset:4096
v_mfma_f32_16x16x32_bf16 v[180:183], a[32:35], a[8:11], v[180:183]
ds_read_b64_tr_b16 a[196:197], v49 offset:4096
ds_read_b64_tr_b16 a[198:199], v49 offset:4608
v_mfma_f32_16x16x32_bf16 v[216:219], a[8:11], a[36:39], v[216:219]
s_add_u32 m0, s91, 81920
v_mfma_f32_16x16x32_bf16 v[220:223], a[8:11], a[40:43], v[220:223]
buffer_load_dwordx4 v240, s[8:11], s45 offen offset:0 lds
ds_read_b128 a[160:163], v43 offset:6144
v_mfma_f32_16x16x32_bf16 v[144:147], a[20:23], a[12:15], v[144:147]
ds_read_b64_tr_b16 a[200:201], v50 offset:4096
ds_read_b64_tr_b16 a[202:203], v50 offset:4608
v_mfma_f32_16x16x32_bf16 v[148:151], a[24:27], a[12:15], v[148:151]
v_mfma_f32_16x16x32_bf16 v[184:187], a[28:31], a[12:15], v[184:187]
buffer_load_dwordx4 v241, s[8:11], s45 offen offset:1024 lds
v_mfma_f32_16x16x32_bf16 v[188:191], a[32:35], a[12:15], v[188:191]
v_mfma_f32_16x16x32_bf16 v[224:227], a[12:15], a[36:39], v[224:227]
v_mfma_f32_16x16x32_bf16 v[228:231], a[12:15], a[40:43], v[228:231]
buffer_load_dwordx4 v242, s[8:11], s45 offen offset:2048 lds
v_mfma_f32_16x16x32_bf16 v[152:155], a[20:23], a[16:19], v[152:155]
v_mfma_f32_16x16x32_bf16 v[156:159], a[24:27], a[16:19], v[156:159]
v_mfma_f32_16x16x32_bf16 v[192:195], a[28:31], a[16:19], v[192:195]
buffer_load_dwordx4 v243, s[8:11], s45 offen offset:3072 lds
v_mfma_f32_16x16x32_bf16 v[196:199], a[32:35], a[16:19], v[196:199]
s_add_u32 s45, s45, 131072
v_mfma_f32_16x16x32_bf16 v[232:235], a[16:19], a[36:39], v[232:235]
v_mfma_f32_16x16x32_bf16 v[236:239], a[16:19], a[40:43], v[236:239]
s_waitcnt lgkmcnt(0)
v_mfma_f32_16x16x32_bf16 v[160:163], a[172:175], a[128:131], v[160:163]
s_lshl_b32 s34, s36, 18
s_lshr_b32 s35, s3, 1
s_lshl_b32 s35, s35, 17
v_mfma_f32_16x16x32_bf16 v[164:167], a[192:195], a[128:131], v[164:167]
ds_read_b64_tr_b16 a[20:21], v47 offset:14336
ds_read_b64_tr_b16 a[22:23], v47 offset:14848
v_mfma_f32_16x16x32_bf16 v[200:203], a[128:131], a[196:199], v[200:203]
s_add_u32 s34, s34, s35
s_lshl_b32 s35, s37, 12
s_add_u32 s34, s34, s35
v_mfma_f32_16x16x32_bf16 v[204:207], a[128:131], a[200:203], v[204:207]
s_and_b32 s35, s3, 1
ds_read_b128 a[0:3], v46
v_mfma_f32_16x16x32_bf16 v[128:131], a[164:167], a[132:135], v[128:131]
ds_read_b64_tr_b16 a[24:25], v48 offset:14336
ds_read_b64_tr_b16 a[26:27], v48 offset:14848
v_mfma_f32_16x16x32_bf16 v[132:135], a[168:171], a[132:135], v[132:135]
s_lshl_b32 s35, s35, 11
s_add_u32 s34, s34, s35
s_add_u32 s34, s34, 50331648
v_mfma_f32_16x16x32_bf16 v[168:171], a[172:175], a[132:135], v[168:171]
ds_read_b128 a[4:7], v44 offset:0
v_mfma_f32_16x16x32_bf16 v[172:175], a[192:195], a[132:135], v[172:175]
ds_read_b64_tr_b16 a[28:29], v47 offset:16384
ds_read_b64_tr_b16 a[30:31], v47 offset:16896
v_mfma_f32_16x16x32_bf16 v[208:211], a[132:135], a[196:199], v[208:211]
s_add_u32 s20, s84, s34
s_addc_u32 s21, s85, 0
s_and_b32 s21, s21, 0xffff
v_mfma_f32_16x16x32_bf16 v[212:215], a[132:135], a[200:203], v[212:215]
s_mov_b32 s22, 131072
ds_read_b128 a[8:11], v44 offset:2048
v_mfma_f32_16x16x32_bf16 v[136:139], a[164:167], a[136:139], v[136:139]
ds_read_b64_tr_b16 a[32:33], v48 offset:16384
ds_read_b64_tr_b16 a[34:35], v48 offset:16896
v_mfma_f32_16x16x32_bf16 v[140:143], a[168:171], a[136:139], v[140:143]
s_mov_b32 s40, 0
s_mov_b32 s41, 65536
v_mfma_f32_16x16x32_bf16 v[176:179], a[172:175], a[136:139], v[176:179]
ds_read_b128 a[12:15], v44 offset:4096
v_mfma_f32_16x16x32_bf16 v[180:183], a[192:195], a[136:139], v[180:183]
ds_read_b64_tr_b16 a[36:37], v49 offset:18432
ds_read_b64_tr_b16 a[38:39], v49 offset:18944
v_mfma_f32_16x16x32_bf16 v[216:219], a[136:139], a[196:199], v[216:219]
v_mfma_f32_16x16x32_bf16 v[220:223], a[136:139], a[200:203], v[220:223]
ds_read_b128 a[16:19], v44 offset:6144
v_mfma_f32_16x16x32_bf16 v[144:147], a[164:167], a[140:143], v[144:147]
ds_read_b64_tr_b16 a[40:41], v50 offset:18432
ds_read_b64_tr_b16 a[42:43], v50 offset:18944
v_mfma_f32_16x16x32_bf16 v[148:151], a[168:171], a[140:143], v[148:151]
s_cmp_eq_u32 s39, 1
s_mov_b32 s28, 16384
s_cselect_b32 s28, 98304, s28
v_mfma_f32_16x16x32_bf16 v[184:187], a[172:175], a[140:143], v[184:187]
v_add_u32 v43, 16384, v43
v_add_u32 v44, 16384, v44
v_mfma_f32_16x16x32_bf16 v[188:191], a[192:195], a[140:143], v[188:191]
v_add_u32 v47, 98304, v47
v_add_u32 v48, 98304, v48
v_mfma_f32_16x16x32_bf16 v[224:227], a[140:143], a[196:199], v[224:227]
v_add_u32 v49, 98304, v49
v_add_u32 v50, 98304, v50
v_mfma_f32_16x16x32_bf16 v[228:231], a[140:143], a[200:203], v[228:231]
v_add_u32 v45, s28, v45
v_add_u32 v46, s28, v46
v_mfma_f32_16x16x32_bf16 v[152:155], a[164:167], a[160:163], v[152:155]
v_mfma_f32_16x16x32_bf16 v[156:159], a[168:171], a[160:163], v[156:159]
v_mfma_f32_16x16x32_bf16 v[192:195], a[172:175], a[160:163], v[192:195]
v_mfma_f32_16x16x32_bf16 v[196:199], a[192:195], a[160:163], v[196:199]
v_mfma_f32_16x16x32_bf16 v[232:235], a[160:163], a[196:199], v[232:235]
v_mfma_f32_16x16x32_bf16 v[236:239], a[160:163], a[200:203], v[236:239]
s_waitcnt vmcnt(8) lgkmcnt(0)
s_barrier
v_mfma_f32_16x16x32_bf16 v[160:163], a[28:31], a[0:3], v[160:163]
v_mfma_f32_16x16x32_bf16 v[164:167], a[32:35], a[0:3], v[164:167]
s_add_u32 m0, s91, 49152
ds_read_b64_tr_b16 a[164:165], v47 offset:0
ds_read_b64_tr_b16 a[166:167], v47 offset:512
v_mfma_f32_16x16x32_bf16 v[200:203], a[0:3], a[36:39], v[200:203]
buffer_load_dwordx4 v240, s[8:11], s45 offen offset:0 lds
v_mfma_f32_16x16x32_bf16 v[204:207], a[0:3], a[40:43], v[204:207]
ds_read_b128 a[128:131], v45
v_mfma_f32_16x16x32_bf16 v[128:131], a[20:23], a[4:7], v[128:131]
ds_read_b64_tr_b16 a[168:169], v48 offset:0
ds_read_b64_tr_b16 a[170:171], v48 offset:512
v_mfma_f32_16x16x32_bf16 v[132:135], a[24:27], a[4:7], v[132:135]
buffer_load_dwordx4 v241, s[8:11], s45 offen offset:1024 lds
v_mfma_f32_16x16x32_bf16 v[168:171], a[28:31], a[4:7], v[168:171]
ds_read_b128 a[132:135], v43 offset:0
v_mfma_f32_16x16x32_bf16 v[172:175], a[32:35], a[4:7], v[172:175]
ds_read_b64_tr_b16 a[172:173], v47 offset:2048
ds_read_b64_tr_b16 a[174:175], v47 offset:2560
v_mfma_f32_16x16x32_bf16 v[208:211], a[4:7], a[36:39], v[208:211]
buffer_load_dwordx4 v242, s[8:11], s45 offen offset:2048 lds
v_mfma_f32_16x16x32_bf16 v[212:215], a[4:7], a[40:43], v[212:215]
ds_read_b128 a[136:139], v43 offset:2048
v_mfma_f32_16x16x32_bf16 v[136:139], a[20:23], a[8:11], v[136:139]
ds_read_b64_tr_b16 a[192:193], v48 offset:2048
ds_read_b64_tr_b16 a[194:195], v48 offset:2560
v_mfma_f32_16x16x32_bf16 v[140:143], a[24:27], a[8:11], v[140:143]
buffer_load_dwordx4 v243, s[8:11], s45 offen offset:3072 lds
v_mfma_f32_16x16x32_bf16 v[176:179], a[28:31], a[8:11], v[176:179]
s_add_u32 s45, s45, 131072
ds_read_b128 a[140:143], v43 offset:4096
v_mfma_f32_16x16x32_bf16 v[180:183], a[32:35], a[8:11], v[180:183]
ds_read_b64_tr_b16 a[196:197], v49 offset:4096
ds_read_b64_tr_b16 a[198:199], v49 offset:4608
v_mfma_f32_16x16x32_bf16 v[216:219], a[8:11], a[36:39], v[216:219]
v_mfma_f32_16x16x32_bf16 v[220:223], a[8:11], a[40:43], v[220:223]
ds_read_b128 a[160:163], v43 offset:6144
v_mfma_f32_16x16x32_bf16 v[144:147], a[20:23], a[12:15], v[144:147]
ds_read_b64_tr_b16 a[200:201], v50 offset:4096
ds_read_b64_tr_b16 a[202:203], v50 offset:4608
v_mfma_f32_16x16x32_bf16 v[148:151], a[24:27], a[12:15], v[148:151]
v_mfma_f32_16x16x32_bf16 v[184:187], a[28:31], a[12:15], v[184:187]
v_mfma_f32_16x16x32_bf16 v[188:191], a[32:35], a[12:15], v[188:191]
v_mfma_f32_16x16x32_bf16 v[224:227], a[12:15], a[36:39], v[224:227]
v_mfma_f32_16x16x32_bf16 v[228:231], a[12:15], a[40:43], v[228:231]
v_mfma_f32_16x16x32_bf16 v[152:155], a[20:23], a[16:19], v[152:155]
v_mfma_f32_16x16x32_bf16 v[156:159], a[24:27], a[16:19], v[156:159]
v_mfma_f32_16x16x32_bf16 v[192:195], a[28:31], a[16:19], v[192:195]
v_mfma_f32_16x16x32_bf16 v[196:199], a[32:35], a[16:19], v[196:199]
v_mfma_f32_16x16x32_bf16 v[232:235], a[16:19], a[36:39], v[232:235]
v_mfma_f32_16x16x32_bf16 v[236:239], a[16:19], a[40:43], v[236:239]
s_waitcnt lgkmcnt(0)
v_mfma_f32_16x16x32_bf16 v[160:163], a[172:175], a[128:131], v[160:163]
v_mfma_f32_16x16x32_bf16 v[164:167], a[192:195], a[128:131], v[164:167]
ds_read_b64_tr_b16 a[20:21], v47 offset:14336
ds_read_b64_tr_b16 a[22:23], v47 offset:14848
v_mfma_f32_16x16x32_bf16 v[200:203], a[128:131], a[196:199], v[200:203]
buffer_load_dword v250, v249, s[60:63], s90 offen sc1
v_mfma_f32_16x16x32_bf16 v[204:207], a[128:131], a[200:203], v[204:207]
ds_read_b128 a[0:3], v46
v_mfma_f32_16x16x32_bf16 v[128:131], a[164:167], a[132:135], v[128:131]
ds_read_b64_tr_b16 a[24:25], v48 offset:14336
ds_read_b64_tr_b16 a[26:27], v48 offset:14848
v_mfma_f32_16x16x32_bf16 v[132:135], a[168:171], a[132:135], v[132:135]
v_mfma_f32_16x16x32_bf16 v[168:171], a[172:175], a[132:135], v[168:171]
ds_read_b128 a[4:7], v44 offset:0
v_mfma_f32_16x16x32_bf16 v[172:175], a[192:195], a[132:135], v[172:175]
ds_read_b64_tr_b16 a[28:29], v47 offset:16384
ds_read_b64_tr_b16 a[30:31], v47 offset:16896
v_mfma_f32_16x16x32_bf16 v[208:211], a[132:135], a[196:199], v[208:211]
v_mfma_f32_16x16x32_bf16 v[212:215], a[132:135], a[200:203], v[212:215]
ds_read_b128 a[8:11], v44 offset:2048
v_mfma_f32_16x16x32_bf16 v[136:139], a[164:167], a[136:139], v[136:139]
ds_read_b64_tr_b16 a[32:33], v48 offset:16384
ds_read_b64_tr_b16 a[34:35], v48 offset:16896
v_mfma_f32_16x16x32_bf16 v[140:143], a[168:171], a[136:139], v[140:143]
v_mfma_f32_16x16x32_bf16 v[176:179], a[172:175], a[136:139], v[176:179]
ds_read_b128 a[12:15], v44 offset:4096
v_mfma_f32_16x16x32_bf16 v[180:183], a[192:195], a[136:139], v[180:183]
ds_read_b64_tr_b16 a[36:37], v49 offset:18432
ds_read_b64_tr_b16 a[38:39], v49 offset:18944
v_mfma_f32_16x16x32_bf16 v[216:219], a[136:139], a[196:199], v[216:219]
v_mfma_f32_16x16x32_bf16 v[220:223], a[136:139], a[200:203], v[220:223]
ds_read_b128 a[16:19], v44 offset:6144
v_mfma_f32_16x16x32_bf16 v[144:147], a[164:167], a[140:143], v[144:147]
ds_read_b64_tr_b16 a[40:41], v50 offset:18432
ds_read_b64_tr_b16 a[42:43], v50 offset:18944
v_mfma_f32_16x16x32_bf16 v[148:151], a[168:171], a[140:143], v[148:151]
v_mfma_f32_16x16x32_bf16 v[184:187], a[172:175], a[140:143], v[184:187]
v_mfma_f32_16x16x32_bf16 v[188:191], a[192:195], a[140:143], v[188:191]
v_mfma_f32_16x16x32_bf16 v[224:227], a[140:143], a[196:199], v[224:227]
v_mfma_f32_16x16x32_bf16 v[228:231], a[140:143], a[200:203], v[228:231]
v_mfma_f32_16x16x32_bf16 v[152:155], a[164:167], a[160:163], v[152:155]
v_mfma_f32_16x16x32_bf16 v[156:159], a[168:171], a[160:163], v[156:159]
v_mfma_f32_16x16x32_bf16 v[192:195], a[172:175], a[160:163], v[192:195]
v_mfma_f32_16x16x32_bf16 v[196:199], a[192:195], a[160:163], v[196:199]
v_mfma_f32_16x16x32_bf16 v[232:235], a[160:163], a[196:199], v[232:235]
v_mfma_f32_16x16x32_bf16 v[236:239], a[160:163], a[200:203], v[236:239]
s_waitcnt lgkmcnt(0)
s_barrier
v_mfma_f32_16x16x32_bf16 v[160:163], a[28:31], a[0:3], v[160:163]
v_mfma_f32_16x16x32_bf16 v[164:167], a[32:35], a[0:3], v[164:167]
s_add_u32 m0, s91, 16384
v_mfma_f32_16x16x32_bf16 v[200:203], a[0:3], a[36:39], v[200:203]
buffer_load_dwordx4 v240, s[8:11], s45 offen offset:0 lds
v_mfma_f32_16x16x32_bf16 v[204:207], a[0:3], a[40:43], v[204:207]
v_mfma_f32_16x16x32_bf16 v[128:131], a[20:23], a[4:7], v[128:131]
v_mfma_f32_16x16x32_bf16 v[132:135], a[24:27], a[4:7], v[132:135]
buffer_load_dwordx4 v241, s[8:11], s45 offen offset:1024 lds
v_mfma_f32_16x16x32_bf16 v[168:171], a[28:31], a[4:7], v[168:171]
v_mfma_f32_16x16x32_bf16 v[172:175], a[32:35], a[4:7], v[172:175]
v_mfma_f32_16x16x32_bf16 v[208:211], a[4:7], a[36:39], v[208:211]
buffer_load_dwordx4 v242, s[8:11], s45 offen offset:2048 lds
v_mfma_f32_16x16x32_bf16 v[212:215], a[4:7], a[40:43], v[212:215]
v_mfma_f32_16x16x32_bf16 v[136:139], a[20:23], a[8:11], v[136:139]
v_mfma_f32_16x16x32_bf16 v[140:143], a[24:27], a[8:11], v[140:143]
buffer_load_dwordx4 v243, s[8:11], s45 offen offset:3072 lds
v_mfma_f32_16x16x32_bf16 v[176:179], a[28:31], a[8:11], v[176:179]
s_add_u32 s45, s45, 131072
v_mfma_f32_16x16x32_bf16 v[180:183], a[32:35], a[8:11], v[180:183]
s_add_u32 m0, s91, 147456
v_mfma_f32_16x16x32_bf16 v[216:219], a[8:11], a[36:39], v[216:219]
buffer_load_dwordx4 v240, s[8:11], s45 offen offset:0 lds
v_mfma_f32_16x16x32_bf16 v[220:223], a[8:11], a[40:43], v[220:223]
v_mfma_f32_16x16x32_bf16 v[144:147], a[20:23], a[12:15], v[144:147]
v_mfma_f32_16x16x32_bf16 v[148:151], a[24:27], a[12:15], v[148:151]
buffer_load_dwordx4 v241, s[8:11], s45 offen offset:1024 lds
v_mfma_f32_16x16x32_bf16 v[184:187], a[28:31], a[12:15], v[184:187]
v_mfma_f32_16x16x32_bf16 v[188:191], a[32:35], a[12:15], v[188:191]
v_mfma_f32_16x16x32_bf16 v[224:227], a[12:15], a[36:39], v[224:227]
buffer_load_dwordx4 v242, s[8:11], s45 offen offset:2048 lds
v_mfma_f32_16x16x32_bf16 v[228:231], a[12:15], a[40:43], v[228:231]
v_mfma_f32_16x16x32_bf16 v[152:155], a[20:23], a[16:19], v[152:155]
v_mfma_f32_16x16x32_bf16 v[156:159], a[24:27], a[16:19], v[156:159]
buffer_load_dwordx4 v243, s[8:11], s45 offen offset:3072 lds
v_mfma_f32_16x16x32_bf16 v[192:195], a[28:31], a[16:19], v[192:195]
s_add_u32 s45, s45, 131072
v_mfma_f32_16x16x32_bf16 v[196:199], a[32:35], a[16:19], v[196:199]
v_mfma_f32_16x16x32_bf16 v[232:235], a[16:19], a[36:39], v[232:235]
v_mfma_f32_16x16x32_bf16 v[236:239], a[16:19], a[40:43], v[236:239]
s_mov_b32 s28, 1048744942
s_waitcnt vmcnt(8)
v_and_b32 v250, 7, v250
v_cmp_gt_u32 vcc, 3, v250
s_cmp_lg_u64 vcc, 0
s_cbranch_scc1 .Lspin3
.Lspin3_ret:
v_cvt_pk_bf16_f32 v80, v160, v161
v_cvt_pk_bf16_f32 v81, v162, v163
v_cvt_pk_bf16_f32 v82, v164, v165
v_cvt_pk_bf16_f32 v83, v166, v167
v_cvt_pk_bf16_f32 v84, v168, v169
v_cvt_pk_bf16_f32 v85, v170, v171
v_cvt_pk_bf16_f32 v86, v172, v173
v_cvt_pk_bf16_f32 v87, v174, v175
v_cvt_pk_bf16_f32 v64, v128, v129
v_cvt_pk_bf16_f32 v65, v130, v131
s_add_u32 m0, s91, 98304
v_cvt_pk_bf16_f32 v66, v132, v133
v_cvt_pk_bf16_f32 v67, v134, v135
v_cvt_pk_bf16_f32 v88, v176, v177
v_cvt_pk_bf16_f32 v89, v178, v179
v_cvt_pk_bf16_f32 v90, v180, v181
v_cvt_pk_bf16_f32 v91, v182, v183
v_cvt_pk_bf16_f32 v68, v136, v137
v_cvt_pk_bf16_f32 v69, v138, v139
v_cvt_pk_bf16_f32 v70, v140, v141
buffer_load_dwordx4 v7, s[4:7], s44 offen offset:0 sc1 lds
v_cvt_pk_bf16_f32 v71, v142, v143
v_cvt_pk_bf16_f32 v92, v184, v185
v_cvt_pk_bf16_f32 v93, v186, v187
v_cvt_pk_bf16_f32 v94, v188, v189
v_cvt_pk_bf16_f32 v95, v190, v191
v_cvt_pk_bf16_f32 v72, v144, v145
v_cvt_pk_bf16_f32 v73, v146, v147
v_cvt_pk_bf16_f32 v74, v148, v149
v_cvt_pk_bf16_f32 v75, v150, v151
buffer_load_dwordx4 v7, s[4:7], s44 offen offset:1024 sc1 lds
v_cvt_pk_bf16_f32 v96, v192, v193
v_cvt_pk_bf16_f32 v97, v194, v195
v_cvt_pk_bf16_f32 v98, v196, v197
v_cvt_pk_bf16_f32 v99, v198, v199
v_cvt_pk_bf16_f32 v76, v152, v153
v_cvt_pk_bf16_f32 v77, v154, v155
v_cvt_pk_bf16_f32 v78, v156, v157
v_cvt_pk_bf16_f32 v79, v158, v159
v_mfma_f32_16x16x32_bf16 v[128:131], v[80:83], v[64:67], v[24:27]
buffer_load_dwordx4 v7, s[4:7], s44 offen offset:2048 sc1 lds
v_mfma_f32_16x16x32_bf16 v[132:135], v[84:87], v[64:67], v[20:23]
v_cvt_pk_bf16_f32 v100, v200, v201
v_cvt_pk_bf16_f32 v101, v202, v203
v_cvt_pk_bf16_f32 v110, v204, v205
v_cvt_pk_bf16_f32 v111, v206, v207
v_cvt_pk_bf16_f32 v102, v208, v209
v_cvt_pk_bf16_f32 v103, v210, v211
v_cvt_pk_bf16_f32 v112, v212, v213
v_cvt_pk_bf16_f32 v113, v214, v215
v_mfma_f32_16x16x32_bf16 v[136:139], v[84:87], v[68:71], v[16:19]
buffer_load_dwordx4 v7, s[4:7], s44 offen offset:3072 sc1 lds
v_mfma_f32_16x16x32_bf16 v[140:143], v[88:91], v[68:71], v[20:23]
v_cvt_pk_bf16_f32 v104, v216, v217
v_cvt_pk_bf16_f32 v105, v218, v219
v_cvt_pk_bf16_f32 v114, v220, v221
v_cvt_pk_bf16_f32 v115, v222, v223
v_mfma_f32_16x16x32_bf16 v[144:147], v[88:91], v[72:75], v[16:19]
v_mfma_f32_16x16x32_bf16 v[148:151], v[92:95], v[72:75], v[20:23]
v_cvt_pk_bf16_f32 v106, v224, v225
v_cvt_pk_bf16_f32 v107, v226, v227
s_add_u32 s44, s44, 4096
v_cvt_pk_bf16_f32 v116, v228, v229
v_cvt_pk_bf16_f32 v117, v230, v231
v_mfma_f32_16x16x32_bf16 v[152:155], v[92:95], v[76:79], v[16:19]
v_mfma_f32_16x16x32_bf16 v[156:159], v[96:99], v[76:79], v[20:23]
v_cvt_pk_bf16_f32 v108, v232, v233
v_cvt_pk_bf16_f32 v109, v234, v235
v_cvt_pk_bf16_f32 v118, v236, v237
v_cvt_pk_bf16_f32 v119, v238, v239
v_max3_f32 v160, v128, v129, v130
s_add_u32 m0, s91, 65536
v_max3_f32 v162, v136, v137, v138
v_max3_f32 v164, v144, v145, v146
v_max3_f32 v166, v152, v153, v154
v_max3_f32 v161, v131, v132, v133
v_max3_f32 v163, v139, v140, v141
v_max3_f32 v165, v147, v148, v149
v_max3_f32 v167, v155, v156, v157
v_max3_f32 v160, v160, v134, v135
v_max3_f32 v162, v162, v142, v143
buffer_load_dwordx4 v7, s[4:7], s44 offen offset:0 sc1 lds
v_max3_f32 v164, v164, v150, v151
v_max3_f32 v166, v166, v158, v159
v_max_f32 v160, v160, v161
v_max_f32 v162, v162, v163
v_max_f32 v164, v164, v165
v_max_f32 v166, v166, v167
v_mov_b32 v161, v160
v_mov_b32 v163, v162
v_mov_b32 v165, v164
buffer_load_dwordx4 v7, s[4:7], s44 offen offset:1024 sc1 lds
v_mov_b32 v167, v166
v_permlane16_swap_b32 v160, v161
v_permlane16_swap_b32 v162, v163
v_permlane16_swap_b32 v164, v165
v_permlane16_swap_b32 v166, v167
v_max_f32 v160, v160, v161
v_max_f32 v162, v162, v163
v_max_f32 v164, v164, v165
v_max_f32 v166, v166, v167
v_mov_b32 v161, v160
buffer_load_dwordx4 v7, s[4:7], s44 offen offset:2048 sc1 lds
v_mov_b32 v163, v162
v_mov_b32 v165, v164
v_mov_b32 v167, v166
v_permlane32_swap_b32 v160, v161
v_permlane32_swap_b32 v162, v163
v_permlane32_swap_b32 v164, v165
v_permlane32_swap_b32 v166, v167
v_max_f32 v160, v160, v161
v_max_f32 v162, v162, v163
buffer_load_dwordx4 v7, s[4:7], s44 offen offset:3072 sc1 lds
v_max_f32 v164, v164, v165
v_max_f32 v166, v166, v167
v_mul_f32 v160, s28, v160
v_mul_f32 v162, s28, v162
v_mul_f32 v164, s28, v164
v_mul_f32 v166, s28, v166
v_fma_f32 v128, v128, s28, -v160
v_fma_f32 v136, v136, s28, -v162
v_fma_f32 v144, v144, s28, -v164
s_add_u32 s44, s44, 4096
v_fma_f32 v152, v152, s28, -v166
v_fma_f32 v129, v129, s28, -v160
v_fma_f32 v137, v137, s28, -v162
v_fma_f32 v145, v145, s28, -v164
v_fma_f32 v153, v153, s28, -v166
v_fma_f32 v130, v130, s28, -v160
v_fma_f32 v138, v138, s28, -v162
v_fma_f32 v146, v146, s28, -v164
v_fma_f32 v154, v154, s28, -v166
s_add_u32 m0, s91, 32768
v_fma_f32 v131, v131, s28, -v160
v_fma_f32 v139, v139, s28, -v162
v_fma_f32 v147, v147, s28, -v164
v_fma_f32 v155, v155, s28, -v166
v_fma_f32 v132, v132, s28, -v160
v_fma_f32 v140, v140, s28, -v162
v_fma_f32 v148, v148, s28, -v164
v_fma_f32 v156, v156, s28, -v166
v_fma_f32 v133, v133, s28, -v160
buffer_load_dwordx4 v7, s[4:7], s44 offen offset:0 sc1 lds
v_fma_f32 v141, v141, s28, -v162
v_fma_f32 v149, v149, s28, -v164
v_fma_f32 v157, v157, s28, -v166
v_fma_f32 v134, v134, s28, -v160
v_fma_f32 v142, v142, s28, -v162
v_fma_f32 v150, v150, s28, -v164
v_fma_f32 v158, v158, s28, -v166
v_fma_f32 v135, v135, s28, -v160
v_fma_f32 v143, v143, s28, -v162
v_fma_f32 v151, v151, s28, -v164
buffer_load_dwordx4 v7, s[4:7], s44 offen offset:1024 sc1 lds
v_fma_f32 v159, v159, s28, -v166
v_exp_f32 v128, v128
v_exp_f32 v136, v136
v_exp_f32 v144, v144
v_exp_f32 v152, v152
v_exp_f32 v129, v129
v_exp_f32 v137, v137
v_exp_f32 v145, v145
v_exp_f32 v153, v153
buffer_load_dwordx4 v7, s[4:7], s44 offen offset:2048 sc1 lds
v_exp_f32 v130, v130
v_exp_f32 v138, v138
v_exp_f32 v146, v146
v_exp_f32 v154, v154
v_exp_f32 v131, v131
v_exp_f32 v139, v139
v_exp_f32 v147, v147
v_exp_f32 v155, v155
v_exp_f32 v132, v132
buffer_load_dwordx4 v7, s[4:7], s44 offen offset:3072 sc1 lds
v_exp_f32 v140, v140
v_exp_f32 v148, v148
v_exp_f32 v156, v156
v_exp_f32 v133, v133
v_exp_f32 v141, v141
v_exp_f32 v149, v149
v_exp_f32 v157, v157
v_exp_f32 v134, v134
v_exp_f32 v142, v142
s_add_u32 s44, s44, 4096
v_exp_f32 v150, v150
v_exp_f32 v158, v158
v_exp_f32 v135, v135
v_exp_f32 v143, v143
v_exp_f32 v151, v151
v_exp_f32 v159, v159
v_cvt_pk_bf16_f32 v168, v128, v129
v_cvt_pk_bf16_f32 v172, v136, v137
v_cvt_pk_bf16_f32 v176, v144, v145
v_cvt_pk_bf16_f32 v180, v152, v153
s_add_u32 m0, s91, 0
v_cvt_pk_bf16_f32 v169, v130, v131
v_cvt_pk_bf16_f32 v173, v138, v139
v_cvt_pk_bf16_f32 v177, v146, v147
v_cvt_pk_bf16_f32 v181, v154, v155
v_cvt_pk_bf16_f32 v170, v132, v133
v_cvt_pk_bf16_f32 v174, v140, v141
v_cvt_pk_bf16_f32 v178, v148, v149
v_cvt_pk_bf16_f32 v182, v156, v157
v_cvt_pk_bf16_f32 v171, v134, v135
buffer_load_dwordx4 v7, s[4:7], s44 offen offset:0 sc1 lds
v_cvt_pk_bf16_f32 v175, v142, v143
v_cvt_pk_bf16_f32 v179, v150, v151
v_cvt_pk_bf16_f32 v183, v158, v159
v_mfma_f32_16x16x32_bf16 v[200:203], v[100:103], v[168:171], 0
v_mfma_f32_16x16x32_bf16 v[204:207], v[110:113], v[168:171], 0
v_mfma_f32_16x16x32_bf16 v[208:211], v[102:105], v[172:175], 0
v_mfma_f32_16x16x32_bf16 v[212:215], v[112:115], v[172:175], 0
v_mfma_f32_16x16x32_bf16 v[216:219], v[104:107], v[176:179], 0
v_mfma_f32_16x16x32_bf16 v[220:223], v[114:117], v[176:179], 0
buffer_load_dwordx4 v7, s[4:7], s44 offen offset:1024 sc1 lds
v_mfma_f32_16x16x32_bf16 v[224:227], v[106:109], v[180:183], 0
v_mfma_f32_16x16x32_bf16 v[228:231], v[116:119], v[180:183], 0
v_add_f32 v160, v128, v129
v_add_f32 v162, v136, v137
v_add_f32 v164, v144, v145
v_add_f32 v166, v152, v153
v_add_f32 v161, v130, v131
v_add_f32 v163, v138, v139
v_add_f32 v165, v146, v147
buffer_load_dwordx4 v7, s[4:7], s44 offen offset:2048 sc1 lds
v_add_f32 v167, v154, v155
v_add_f32 v160, v160, v132
v_add_f32 v162, v162, v140
v_add_f32 v164, v164, v148
v_add_f32 v166, v166, v156
v_add_f32 v161, v161, v133
v_add_f32 v163, v163, v141
v_add_f32 v165, v165, v149
v_add_f32 v167, v167, v157
buffer_load_dwordx4 v7, s[4:7], s44 offen offset:3072 sc1 lds
v_add_f32 v160, v160, v134
v_add_f32 v162, v162, v142
v_add_f32 v164, v164, v150
v_add_f32 v166, v166, v158
v_add_f32 v161, v161, v135
v_add_f32 v163, v163, v143
v_add_f32 v165, v165, v151
v_add_f32 v167, v167, v159
v_add_f32 v160, v160, v161
v_add_f32 v162, v162, v163
s_add_u32 s44, s44, 4096
v_add_f32 v164, v164, v165
v_add_f32 v166, v166, v167
v_mov_b32 v161, v160
v_mov_b32 v163, v162
v_mov_b32 v165, v164
v_mov_b32 v167, v166
v_permlane16_swap_b32 v160, v161
v_permlane16_swap_b32 v162, v163
v_permlane16_swap_b32 v164, v165
s_add_u32 m0, s91, 131072
v_permlane16_swap_b32 v166, v167
v_add_f32 v160, v160, v161
v_add_f32 v162, v162, v163
v_add_f32 v164, v164, v165
v_add_f32 v166, v166, v167
v_mov_b32 v161, v160
v_mov_b32 v163, v162
v_mov_b32 v165, v164
v_mov_b32 v167, v166
buffer_load_dwordx4 v7, s[4:7], s44 offen offset:0 sc1 lds
v_permlane32_swap_b32 v160, v161
v_permlane32_swap_b32 v162, v163
v_permlane32_swap_b32 v164, v165
v_permlane32_swap_b32 v166, v167
v_add_f32 v160, v160, v161
v_add_f32 v162, v162, v163
v_add_f32 v164, v164, v165
v_add_f32 v166, v166, v167
v_rcp_f32 v160, v160
buffer_load_dwordx4 v7, s[4:7], s44 offen offset:1024 sc1 lds
v_rcp_f32 v162, v162
v_rcp_f32 v164, v164
v_rcp_f32 v166, v166
v_mul_f32 v200, v160, v200
v_mul_f32 v208, v162, v208
v_mul_f32 v216, v164, v216
v_mul_f32 v224, v166, v224
v_mul_f32 v201, v160, v201
v_mul_f32 v209, v162, v209
buffer_load_dwordx4 v7, s[4:7], s44 offen offset:2048 sc1 lds
v_mul_f32 v217, v164, v217
v_mul_f32 v225, v166, v225
v_mul_f32 v202, v160, v202
v_mul_f32 v210, v162, v210
v_mul_f32 v218, v164, v218
v_mul_f32 v226, v166, v226
v_mul_f32 v203, v160, v203
v_mul_f32 v211, v162, v211
v_mul_f32 v219, v164, v219
v_mul_f32 v227, v166, v227
buffer_load_dwordx4 v7, s[4:7], s44 offen offset:3072 sc1 lds
v_mul_f32 v204, v160, v204
v_mul_f32 v212, v162, v212
v_mul_f32 v220, v164, v220
v_mul_f32 v228, v166, v228
v_mul_f32 v205, v160, v205
v_mul_f32 v213, v162, v213
v_mul_f32 v221, v164, v221
v_mul_f32 v229, v166, v229
v_mul_f32 v206, v160, v206
s_lshr_b32 s54, s2, 1
s_lshl_b32 s54, s54, 16
s_lshl_b32 s56, s3, 14
s_add_u32 s54, s54, s56
s_add_u32 s54, s54, 54525952
s_add_u32 s12, s84, s54
s_addc_u32 s13, s85, 0
s_and_b32 s13, s13, 0xffff
s_mov_b32 s14, 16384
s_mov_b32 s15, 0x20000
v_mul_f32 v214, v162, v214
v_mul_f32 v222, v164, v222
v_mul_f32 v230, v166, v230
v_mul_f32 v207, v160, v207
v_mul_f32 v215, v162, v215
v_mul_f32 v223, v164, v223
v_mul_f32 v231, v166, v231
v_cvt_pk_bf16_f32 v184, v200, v201
v_cvt_pk_bf16_f32 v185, v202, v203
s_lshl_b32 s54, s51, 19
s_and_b32 s56, s3, 1
s_lshl_b32 s56, s56, 18
s_add_u32 s54, s54, s56
s_add_u32 s54, s54, s52
s_lshr_b32 s56, s3, 1
s_lshl_b32 s56, s56, 7
s_add_u32 s54, s54, s56
s_xor_b32 s56, s55, 1
s_lshl_b32 s56, s56, 11
s_add_u32 s54, s54, s56
s_sub_u32 s56, 0x800000, s54
s_add_u32 s16, s82, s54
s_addc_u32 s17, s83, 0
s_and_b32 s17, s17, 0xffff
s_mov_b32 s18, s56
s_mov_b32 s19, 0x20000
v_cvt_pk_bf16_f32 v186, v208, v209
v_cvt_pk_bf16_f32 v187, v210, v211
v_cvt_pk_bf16_f32 v188, v204, v205
v_cvt_pk_bf16_f32 v189, v206, v207
v_cvt_pk_bf16_f32 v190, v212, v213
v_cvt_pk_bf16_f32 v191, v214, v215
v_cvt_pk_bf16_f32 v192, v216, v217
v_cvt_pk_bf16_f32 v193, v218, v219
v_cvt_pk_bf16_f32 v194, v224, v225
v_and_b32 v48, 7, v1
v_bfe_u32 v49, v1, 3, 1
v_bfe_u32 v50, v1, 4, 1
v_lshrrev_b32 v51, 5, v1
v_lshlrev_b32 v48, 12, v48
v_lshl_add_u32 v48, v49, 6, v48
v_lshl_add_u32 v48, v50, 5, v48
v_lshl_add_u32 v251, v51, 4, v48
v_cvt_pk_bf16_f32 v195, v226, v227
v_cvt_pk_bf16_f32 v196, v220, v221
v_cvt_pk_bf16_f32 v197, v222, v223
v_cvt_pk_bf16_f32 v198, v228, v229
v_cvt_pk_bf16_f32 v199, v230, v231
v_permlane16_swap_b32 v184, v186
v_permlane16_swap_b32 v185, v187
v_permlane16_swap_b32 v188, v190
v_permlane16_swap_b32 v189, v191
s_mov_b32 s56, 131072
s_mov_b32 s57, -32768
s_mov_b32 s46, 98304
v_permlane16_swap_b32 v192, v194
v_permlane16_swap_b32 v193, v195
v_permlane16_swap_b32 v196, v198
v_permlane16_swap_b32 v197, v199
buffer_store_dwordx4 v[184:187], v7, s[20:23], s40 offen offset:0 sc1
buffer_store_dwordx4 v[188:191], v7, s[20:23], s40 offen offset:1024 sc1
buffer_store_dwordx4 v[192:195], v7, s[20:23], s41 offen offset:0 sc1
buffer_store_dwordx4 v[196:199], v7, s[20:23], s41 offen offset:1024 sc1
s_waitcnt vmcnt(20)
s_barrier
ds_read_b64_tr_b16 a[144:145], v245 offset:0
ds_read_b64_tr_b16 a[146:147], v245 offset:2048
ds_read_b128 a[128:131], v244 offset:0
ds_read_b128 a[132:135], v244 offset:256
ds_read_b128 a[136:139], v244 offset:4096
ds_read_b128 a[140:143], v244 offset:4352
ds_read_b64_tr_b16 a[148:149], v246 offset:0
ds_read_b64_tr_b16 a[150:151], v246 offset:2048
ds_read_b64_tr_b16 a[152:153], v247 offset:0
ds_read_b64_tr_b16 a[154:155], v247 offset:2048
ds_read_b64_tr_b16 a[156:157], v248 offset:0
ds_read_b64_tr_b16 a[158:159], v248 offset:2048
s_lshr_b32 s28, s51, 3
s_and_b32 s29, s51, 7
s_lshl_b32 s29, s29, 3
s_or_b32 s28, s28, s29
v_bfe_u32 v48, v1, 2, 2
v_and_b32 v49, 3, v1
v_lshlrev_b32 v48, 1, v48
v_lshl_or_b32 v48, v49, 6, v48
v_or_b32 v48, s28, v48
s_xor_b32 s28, s2, 1
v_mov_b32 v49, s28
s_cmp_eq_u32 s55, 0
s_cselect_b64 vcc, -1, 0
s_cselect_b32 s50, 33, 11
v_cndmask_b32 v48, v48, v49, vcc
v_lshlrev_b32 v249, 5, v48
s_waitcnt lgkmcnt(0)
v_mfma_f32_16x16x32_bf16 v[64:67], a[144:147], a[128:131], 0
ds_read_b64_tr_b16 a[176:177], v245 offset:8192
ds_read_b64_tr_b16 a[178:179], v245 offset:10240
v_mfma_f32_16x16x32_bf16 v[68:71], a[148:151], a[128:131], 0
v_mfma_f32_16x16x32_bf16 v[72:75], a[152:155], a[128:131], 0
ds_read_b128 a[160:163], v244 offset:2048
ds_read_b128 a[164:167], v244 offset:2304
v_mfma_f32_16x16x32_bf16 v[76:79], a[156:159], a[128:131], 0
ds_read_b64_tr_b16 a[180:181], v246 offset:8192
ds_read_b64_tr_b16 a[182:183], v246 offset:10240
v_mfma_f32_16x16x32_bf16 v[80:83], a[144:147], a[132:135], 0
v_mfma_f32_16x16x32_bf16 v[84:87], a[148:151], a[132:135], 0
ds_read_b64_tr_b16 a[184:185], v247 offset:8192
ds_read_b64_tr_b16 a[186:187], v247 offset:10240
v_mfma_f32_16x16x32_bf16 v[88:91], a[152:155], a[132:135], 0
ds_read_b128 a[168:171], v244 offset:6144
ds_read_b128 a[172:175], v244 offset:6400
v_mfma_f32_16x16x32_bf16 v[92:95], a[156:159], a[132:135], 0
v_mfma_f32_16x16x32_bf16 v[96:99], a[144:147], a[136:139], 0
s_cmp_eq_u32 s46, 0
s_cselect_b32 s53, s56, s57
ds_read_b64_tr_b16 a[188:189], v248 offset:8192
ds_read_b64_tr_b16 a[190:191], v248 offset:10240
v_mfma_f32_16x16x32_bf16 v[100:103], a[148:151], a[136:139], 0
v_mfma_f32_16x16x32_bf16 v[104:107], a[152:155], a[136:139], 0
s_add_u32 s49, s46, s91
s_add_u32 s46, s46, s53
v_add_u32 v244, s53, v244
v_mfma_f32_16x16x32_bf16 v[108:111], a[156:159], a[136:139], 0
v_mfma_f32_16x16x32_bf16 v[112:115], a[144:147], a[140:143], 0
v_add_u32 v245, s53, v245
v_add_u32 v246, s53, v246
v_mfma_f32_16x16x32_bf16 v[116:119], a[148:151], a[140:143], 0
v_mfma_f32_16x16x32_bf16 v[120:123], a[152:155], a[140:143], 0
v_add_u32 v247, s53, v247
v_add_u32 v248, s53, v248
v_mfma_f32_16x16x32_bf16 v[124:127], a[156:159], a[140:143], 0
s_waitcnt vmcnt(16) lgkmcnt(0)
s_barrier
v_mfma_f32_16x16x32_bf16 v[64:67], a[176:179], a[160:163], v[64:67]
s_add_u32 m0, s49, 16384
s_add_u32 s44, s44, 4096
ds_read_b64_tr_b16 a[144:145], v245 offset:0
ds_read_b64_tr_b16 a[146:147], v245 offset:2048
v_mfma_f32_16x16x32_bf16 v[68:71], a[180:183], a[160:163], v[68:71]
buffer_load_dwordx4 v240, s[8:11], s45 offen offset:0 lds
v_mfma_f32_16x16x32_bf16 v[72:75], a[184:187], a[160:163], v[72:75]
ds_read_b128 a[128:131], v244 offset:0
ds_read_b128 a[132:135], v244 offset:256
v_mfma_f32_16x16x32_bf16 v[76:79], a[188:191], a[160:163], v[76:79]
ds_read_b64_tr_b16 a[148:149], v246 offset:0
ds_read_b64_tr_b16 a[150:151], v246 offset:2048
v_mfma_f32_16x16x32_bf16 v[80:83], a[176:179], a[164:167], v[80:83]
buffer_load_dwordx4 v241, s[8:11], s45 offen offset:1024 lds
v_mfma_f32_16x16x32_bf16 v[84:87], a[180:183], a[164:167], v[84:87]
ds_read_b64_tr_b16 a[152:153], v247 offset:0
ds_read_b64_tr_b16 a[154:155], v247 offset:2048
v_mfma_f32_16x16x32_bf16 v[88:91], a[184:187], a[164:167], v[88:91]
ds_read_b128 a[136:139], v244 offset:4096
ds_read_b128 a[140:143], v244 offset:4352
v_mfma_f32_16x16x32_bf16 v[92:95], a[188:191], a[164:167], v[92:95]
v_mfma_f32_16x16x32_bf16 v[96:99], a[176:179], a[168:171], v[96:99]
ds_read_b64_tr_b16 a[156:157], v248 offset:0
ds_read_b64_tr_b16 a[158:159], v248 offset:2048
v_mfma_f32_16x16x32_bf16 v[100:103], a[180:183], a[168:171], v[100:103]
buffer_load_dwordx4 v242, s[8:11], s45 offen offset:2048 lds
v_mfma_f32_16x16x32_bf16 v[104:107], a[184:187], a[168:171], v[104:107]
v_mfma_f32_16x16x32_bf16 v[108:111], a[188:191], a[168:171], v[108:111]
v_mfma_f32_16x16x32_bf16 v[112:115], a[176:179], a[172:175], v[112:115]
buffer_load_dwordx4 v243, s[8:11], s45 offen offset:3072 lds
v_mfma_f32_16x16x32_bf16 v[116:119], a[180:183], a[172:175], v[116:119]
s_add_u32 s45, s45, 131072
v_mfma_f32_16x16x32_bf16 v[120:123], a[184:187], a[172:175], v[120:123]
v_mfma_f32_16x16x32_bf16 v[124:127], a[188:191], a[172:175], v[124:127]
s_waitcnt lgkmcnt(0)
v_mfma_f32_16x16x32_bf16 v[64:67], a[144:147], a[128:131], v[64:67]
s_mov_b32 m0, s49
ds_read_b64_tr_b16 a[176:177], v245 offset:8192
ds_read_b64_tr_b16 a[178:179], v245 offset:10240
v_mfma_f32_16x16x32_bf16 v[68:71], a[148:151], a[128:131], v[68:71]
buffer_load_dwordx4 v7, s[4:7], s44 offen offset:0 sc1 lds
v_mfma_f32_16x16x32_bf16 v[72:75], a[152:155], a[128:131], v[72:75]
ds_read_b128 a[160:163], v244 offset:2048
ds_read_b128 a[164:167], v244 offset:2304
v_mfma_f32_16x16x32_bf16 v[76:79], a[156:159], a[128:131], v[76:79]
ds_read_b64_tr_b16 a[180:181], v246 offset:8192
ds_read_b64_tr_b16 a[182:183], v246 offset:10240
v_mfma_f32_16x16x32_bf16 v[80:83], a[144:147], a[132:135], v[80:83]
buffer_load_dwordx4 v7, s[4:7], s44 offen offset:1024 sc1 lds
v_mfma_f32_16x16x32_bf16 v[84:87], a[148:151], a[132:135], v[84:87]
ds_read_b64_tr_b16 a[184:185], v247 offset:8192
ds_read_b64_tr_b16 a[186:187], v247 offset:10240
v_mfma_f32_16x16x32_bf16 v[88:91], a[152:155], a[132:135], v[88:91]
ds_read_b128 a[168:171], v244 offset:6144
ds_read_b128 a[172:175], v244 offset:6400
v_mfma_f32_16x16x32_bf16 v[92:95], a[156:159], a[132:135], v[92:95]
v_mfma_f32_16x16x32_bf16 v[96:99], a[144:147], a[136:139], v[96:99]
s_cmp_eq_u32 s46, 0
s_cselect_b32 s53, s56, s57
ds_read_b64_tr_b16 a[188:189], v248 offset:8192
ds_read_b64_tr_b16 a[190:191], v248 offset:10240
v_mfma_f32_16x16x32_bf16 v[100:103], a[148:151], a[136:139], v[100:103]
v_mfma_f32_16x16x32_bf16 v[104:107], a[152:155], a[136:139], v[104:107]
s_add_u32 s49, s46, s91
s_add_u32 s46, s46, s53
v_add_u32 v244, s53, v244
v_mfma_f32_16x16x32_bf16 v[108:111], a[156:159], a[136:139], v[108:111]
buffer_load_dwordx4 v7, s[4:7], s44 offen offset:2048 sc1 lds
v_mfma_f32_16x16x32_bf16 v[112:115], a[144:147], a[140:143], v[112:115]
v_add_u32 v245, s53, v245
v_add_u32 v246, s53, v246
v_mfma_f32_16x16x32_bf16 v[116:119], a[148:151], a[140:143], v[116:119]
buffer_load_dwordx4 v7, s[4:7], s44 offen offset:3072 sc1 lds
v_mfma_f32_16x16x32_bf16 v[120:123], a[152:155], a[140:143], v[120:123]
v_add_u32 v247, s53, v247
v_add_u32 v248, s53, v248
v_mfma_f32_16x16x32_bf16 v[124:127], a[156:159], a[140:143], v[124:127]
s_waitcnt vmcnt(20) lgkmcnt(0)
s_barrier
v_mfma_f32_16x16x32_bf16 v[64:67], a[176:179], a[160:163], v[64:67]
s_add_u32 m0, s49, 16384
s_add_u32 s44, s44, 4096
ds_read_b64_tr_b16 a[144:145], v245 offset:0
ds_read_b64_tr_b16 a[146:147], v245 offset:2048
v_mfma_f32_16x16x32_bf16 v[68:71], a[180:183], a[160:163], v[68:71]
buffer_load_dwordx4 v240, s[8:11], s45 offen offset:0 lds
v_mfma_f32_16x16x32_bf16 v[72:75], a[184:187], a[160:163], v[72:75]
ds_read_b128 a[128:131], v244 offset:0
ds_read_b128 a[132:135], v244 offset:256
v_mfma_f32_16x16x32_bf16 v[76:79], a[188:191], a[160:163], v[76:79]
ds_read_b64_tr_b16 a[148:149], v246 offset:0
ds_read_b64_tr_b16 a[150:151], v246 offset:2048
v_mfma_f32_16x16x32_bf16 v[80:83], a[176:179], a[164:167], v[80:83]
buffer_load_dwordx4 v241, s[8:11], s45 offen offset:1024 lds
v_mfma_f32_16x16x32_bf16 v[84:87], a[180:183], a[164:167], v[84:87]
ds_read_b64_tr_b16 a[152:153], v247 offset:0
ds_read_b64_tr_b16 a[154:155], v247 offset:2048
v_mfma_f32_16x16x32_bf16 v[88:91], a[184:187], a[164:167], v[88:91]
ds_read_b128 a[136:139], v244 offset:4096
ds_read_b128 a[140:143], v244 offset:4352
v_mfma_f32_16x16x32_bf16 v[92:95], a[188:191], a[164:167], v[92:95]
v_mfma_f32_16x16x32_bf16 v[96:99], a[176:179], a[168:171], v[96:99]
ds_read_b64_tr_b16 a[156:157], v248 offset:0
ds_read_b64_tr_b16 a[158:159], v248 offset:2048
v_mfma_f32_16x16x32_bf16 v[100:103], a[180:183], a[168:171], v[100:103]
buffer_load_dwordx4 v242, s[8:11], s45 offen offset:2048 lds
v_mfma_f32_16x16x32_bf16 v[104:107], a[184:187], a[168:171], v[104:107]
v_mfma_f32_16x16x32_bf16 v[108:111], a[188:191], a[168:171], v[108:111]
v_mfma_f32_16x16x32_bf16 v[112:115], a[176:179], a[172:175], v[112:115]
buffer_load_dwordx4 v243, s[8:11], s45 offen offset:3072 lds
v_mfma_f32_16x16x32_bf16 v[116:119], a[180:183], a[172:175], v[116:119]
s_add_u32 s45, s45, 131072
v_mfma_f32_16x16x32_bf16 v[120:123], a[184:187], a[172:175], v[120:123]
v_mfma_f32_16x16x32_bf16 v[124:127], a[188:191], a[172:175], v[124:127]
s_waitcnt lgkmcnt(0)
v_mfma_f32_16x16x32_bf16 v[64:67], a[144:147], a[128:131], v[64:67]
s_mov_b32 m0, s49
ds_read_b64_tr_b16 a[176:177], v245 offset:8192
ds_read_b64_tr_b16 a[178:179], v245 offset:10240
v_mfma_f32_16x16x32_bf16 v[68:71], a[148:151], a[128:131], v[68:71]
buffer_load_dwordx4 v7, s[4:7], s44 offen offset:0 sc1 lds
v_mfma_f32_16x16x32_bf16 v[72:75], a[152:155], a[128:131], v[72:75]
ds_read_b128 a[160:163], v244 offset:2048
ds_read_b128 a[164:167], v244 offset:2304
v_mfma_f32_16x16x32_bf16 v[76:79], a[156:159], a[128:131], v[76:79]
ds_read_b64_tr_b16 a[180:181], v246 offset:8192
ds_read_b64_tr_b16 a[182:183], v246 offset:10240
v_mfma_f32_16x16x32_bf16 v[80:83], a[144:147], a[132:135], v[80:83]
buffer_load_dwordx4 v7, s[4:7], s44 offen offset:1024 sc1 lds
v_mfma_f32_16x16x32_bf16 v[84:87], a[148:151], a[132:135], v[84:87]
ds_read_b64_tr_b16 a[184:185], v247 offset:8192
ds_read_b64_tr_b16 a[186:187], v247 offset:10240
v_mfma_f32_16x16x32_bf16 v[88:91], a[152:155], a[132:135], v[88:91]
ds_read_b128 a[168:171], v244 offset:6144
ds_read_b128 a[172:175], v244 offset:6400
v_mfma_f32_16x16x32_bf16 v[92:95], a[156:159], a[132:135], v[92:95]
v_mfma_f32_16x16x32_bf16 v[96:99], a[144:147], a[136:139], v[96:99]
s_cmp_eq_u32 s46, 0
s_cselect_b32 s53, s56, s57
ds_read_b64_tr_b16 a[188:189], v248 offset:8192
ds_read_b64_tr_b16 a[190:191], v248 offset:10240
v_mfma_f32_16x16x32_bf16 v[100:103], a[148:151], a[136:139], v[100:103]
v_mfma_f32_16x16x32_bf16 v[104:107], a[152:155], a[136:139], v[104:107]
s_add_u32 s49, s46, s91
s_add_u32 s46, s46, s53
v_add_u32 v244, s53, v244
v_mfma_f32_16x16x32_bf16 v[108:111], a[156:159], a[136:139], v[108:111]
buffer_load_dwordx4 v7, s[4:7], s44 offen offset:2048 sc1 lds
v_mfma_f32_16x16x32_bf16 v[112:115], a[144:147], a[140:143], v[112:115]
v_add_u32 v245, s53, v245
v_add_u32 v246, s53, v246
v_mfma_f32_16x16x32_bf16 v[116:119], a[148:151], a[140:143], v[116:119]
buffer_load_dwordx4 v7, s[4:7], s44 offen offset:3072 sc1 lds
v_mfma_f32_16x16x32_bf16 v[120:123], a[152:155], a[140:143], v[120:123]
v_add_u32 v247, s53, v247
v_add_u32 v248, s53, v248
v_mfma_f32_16x16x32_bf16 v[124:127], a[156:159], a[140:143], v[124:127]
s_waitcnt vmcnt(16) lgkmcnt(0)
s_barrier
v_mfma_f32_16x16x32_bf16 v[64:67], a[176:179], a[160:163], v[64:67]
s_add_u32 m0, s49, 16384
s_add_u32 s44, s44, 4096
ds_read_b64_tr_b16 a[144:145], v245 offset:0
ds_read_b64_tr_b16 a[146:147], v245 offset:2048
v_mfma_f32_16x16x32_bf16 v[68:71], a[180:183], a[160:163], v[68:71]
buffer_load_dwordx4 v240, s[8:11], s45 offen offset:0 lds
v_mfma_f32_16x16x32_bf16 v[72:75], a[184:187], a[160:163], v[72:75]
ds_read_b128 a[128:131], v244 offset:0
ds_read_b128 a[132:135], v244 offset:256
v_mfma_f32_16x16x32_bf16 v[76:79], a[188:191], a[160:163], v[76:79]
ds_read_b64_tr_b16 a[148:149], v246 offset:0
ds_read_b64_tr_b16 a[150:151], v246 offset:2048
v_mfma_f32_16x16x32_bf16 v[80:83], a[176:179], a[164:167], v[80:83]
buffer_load_dwordx4 v241, s[8:11], s45 offen offset:1024 lds
v_mfma_f32_16x16x32_bf16 v[84:87], a[180:183], a[164:167], v[84:87]
ds_read_b64_tr_b16 a[152:153], v247 offset:0
ds_read_b64_tr_b16 a[154:155], v247 offset:2048
v_mfma_f32_16x16x32_bf16 v[88:91], a[184:187], a[164:167], v[88:91]
ds_read_b128 a[136:139], v244 offset:4096
ds_read_b128 a[140:143], v244 offset:4352
v_mfma_f32_16x16x32_bf16 v[92:95], a[188:191], a[164:167], v[92:95]
v_mfma_f32_16x16x32_bf16 v[96:99], a[176:179], a[168:171], v[96:99]
ds_read_b64_tr_b16 a[156:157], v248 offset:0
ds_read_b64_tr_b16 a[158:159], v248 offset:2048
v_mfma_f32_16x16x32_bf16 v[100:103], a[180:183], a[168:171], v[100:103]
buffer_load_dwordx4 v242, s[8:11], s45 offen offset:2048 lds
v_mfma_f32_16x16x32_bf16 v[104:107], a[184:187], a[168:171], v[104:107]
s_nop 0
v_or_b32 v0, 4, v11
v_mfma_f32_16x16x32_bf16 v[108:111], a[188:191], a[168:171], v[108:111]
v_mfma_f32_16x16x32_bf16 v[112:115], a[176:179], a[172:175], v[112:115]
buffer_load_dwordx4 v243, s[8:11], s45 offen offset:3072 lds
v_mfma_f32_16x16x32_bf16 v[116:119], a[180:183], a[172:175], v[116:119]
s_add_u32 s45, s45, 131072
v_mfma_f32_16x16x32_bf16 v[120:123], a[184:187], a[172:175], v[120:123]
buffer_store_dword v0, v6, s[60:63], 0 offen sc1
v_mfma_f32_16x16x32_bf16 v[124:127], a[188:191], a[172:175], v[124:127]
s_waitcnt lgkmcnt(0)
v_mfma_f32_16x16x32_bf16 v[64:67], a[144:147], a[128:131], v[64:67]
s_mov_b32 m0, s49
ds_read_b64_tr_b16 a[176:177], v245 offset:8192
ds_read_b64_tr_b16 a[178:179], v245 offset:10240
v_mfma_f32_16x16x32_bf16 v[68:71], a[148:151], a[128:131], v[68:71]
buffer_load_dwordx4 v7, s[4:7], s44 offen offset:0 sc1 lds
v_mfma_f32_16x16x32_bf16 v[72:75], a[152:155], a[128:131], v[72:75]
ds_read_b128 a[160:163], v244 offset:2048
ds_read_b128 a[164:167], v244 offset:2304
v_mfma_f32_16x16x32_bf16 v[76:79], a[156:159], a[128:131], v[76:79]
ds_read_b64_tr_b16 a[180:181], v246 offset:8192
ds_read_b64_tr_b16 a[182:183], v246 offset:10240
v_mfma_f32_16x16x32_bf16 v[80:83], a[144:147], a[132:135], v[80:83]
buffer_load_dwordx4 v7, s[4:7], s44 offen offset:1024 sc1 lds
v_mfma_f32_16x16x32_bf16 v[84:87], a[148:151], a[132:135], v[84:87]
ds_read_b64_tr_b16 a[184:185], v247 offset:8192
ds_read_b64_tr_b16 a[186:187], v247 offset:10240
v_mfma_f32_16x16x32_bf16 v[88:91], a[152:155], a[132:135], v[88:91]
ds_read_b128 a[168:171], v244 offset:6144
ds_read_b128 a[172:175], v244 offset:6400
v_mfma_f32_16x16x32_bf16 v[92:95], a[156:159], a[132:135], v[92:95]
v_mfma_f32_16x16x32_bf16 v[96:99], a[144:147], a[136:139], v[96:99]
s_cmp_eq_u32 s46, 0
s_cselect_b32 s53, s56, s57
ds_read_b64_tr_b16 a[188:189], v248 offset:8192
ds_read_b64_tr_b16 a[190:191], v248 offset:10240
v_mfma_f32_16x16x32_bf16 v[100:103], a[148:151], a[136:139], v[100:103]
v_mfma_f32_16x16x32_bf16 v[104:107], a[152:155], a[136:139], v[104:107]
s_add_u32 s49, s46, s91
s_add_u32 s46, s46, s53
v_add_u32 v244, s53, v244
v_mfma_f32_16x16x32_bf16 v[108:111], a[156:159], a[136:139], v[108:111]
buffer_load_dwordx4 v7, s[4:7], s44 offen offset:2048 sc1 lds
v_mfma_f32_16x16x32_bf16 v[112:115], a[144:147], a[140:143], v[112:115]
v_add_u32 v245, s53, v245
v_add_u32 v246, s53, v246
v_mfma_f32_16x16x32_bf16 v[116:119], a[148:151], a[140:143], v[116:119]
buffer_load_dwordx4 v7, s[4:7], s44 offen offset:3072 sc1 lds
v_mfma_f32_16x16x32_bf16 v[120:123], a[152:155], a[140:143], v[120:123]
v_add_u32 v247, s53, v247
v_add_u32 v248, s53, v248
v_mfma_f32_16x16x32_bf16 v[124:127], a[156:159], a[140:143], v[124:127]
s_waitcnt vmcnt(29) lgkmcnt(0)
s_barrier
.L5b:
v_mfma_f32_16x16x32_bf16 v[64:67], a[176:179], a[160:163], v[64:67]
s_add_u32 m0, s49, 16384
s_add_u32 s44, s44, 4096
ds_read_b64_tr_b16 a[144:145], v245 offset:0
ds_read_b64_tr_b16 a[146:147], v245 offset:2048
v_mfma_f32_16x16x32_bf16 v[68:71], a[180:183], a[160:163], v[68:71]
buffer_load_dwordx4 v240, s[8:11], s45 offen offset:0 lds
v_mfma_f32_16x16x32_bf16 v[72:75], a[184:187], a[160:163], v[72:75]
ds_read_b128 a[128:131], v244 offset:0
ds_read_b128 a[132:135], v244 offset:256
v_mfma_f32_16x16x32_bf16 v[76:79], a[188:191], a[160:163], v[76:79]
ds_read_b64_tr_b16 a[148:149], v246 offset:0
ds_read_b64_tr_b16 a[150:151], v246 offset:2048
v_mfma_f32_16x16x32_bf16 v[80:83], a[176:179], a[164:167], v[80:83]
buffer_load_dwordx4 v241, s[8:11], s45 offen offset:1024 lds
v_mfma_f32_16x16x32_bf16 v[84:87], a[180:183], a[164:167], v[84:87]
ds_read_b64_tr_b16 a[152:153], v247 offset:0
ds_read_b64_tr_b16 a[154:155], v247 offset:2048
v_mfma_f32_16x16x32_bf16 v[88:91], a[184:187], a[164:167], v[88:91]
ds_read_b128 a[136:139], v244 offset:4096
ds_read_b128 a[140:143], v244 offset:4352
v_mfma_f32_16x16x32_bf16 v[92:95], a[188:191], a[164:167], v[92:95]
v_mfma_f32_16x16x32_bf16 v[96:99], a[176:179], a[168:171], v[96:99]
ds_read_b64_tr_b16 a[156:157], v248 offset:0
ds_read_b64_tr_b16 a[158:159], v248 offset:2048
v_mfma_f32_16x16x32_bf16 v[100:103], a[180:183], a[168:171], v[100:103]
buffer_load_dwordx4 v242, s[8:11], s45 offen offset:2048 lds
v_mfma_f32_16x16x32_bf16 v[104:107], a[184:187], a[168:171], v[104:107]
v_mfma_f32_16x16x32_bf16 v[108:111], a[188:191], a[168:171], v[108:111]
v_mfma_f32_16x16x32_bf16 v[112:115], a[176:179], a[172:175], v[112:115]
buffer_load_dwordx4 v243, s[8:11], s45 offen offset:3072 lds
v_mfma_f32_16x16x32_bf16 v[116:119], a[180:183], a[172:175], v[116:119]
s_add_u32 s45, s45, 131072
v_mfma_f32_16x16x32_bf16 v[120:123], a[184:187], a[172:175], v[120:123]
v_mfma_f32_16x16x32_bf16 v[124:127], a[188:191], a[172:175], v[124:127]
s_waitcnt lgkmcnt(0)
v_mfma_f32_16x16x32_bf16 v[64:67], a[144:147], a[128:131], v[64:67]
s_mov_b32 m0, s49
ds_read_b64_tr_b16 a[176:177], v245 offset:8192
ds_read_b64_tr_b16 a[178:179], v245 offset:10240
v_mfma_f32_16x16x32_bf16 v[68:71], a[148:151], a[128:131], v[68:71]
buffer_load_dwordx4 v7, s[4:7], s44 offen offset:0 sc1 lds
v_mfma_f32_16x16x32_bf16 v[72:75], a[152:155], a[128:131], v[72:75]
ds_read_b128 a[160:163], v244 offset:2048
ds_read_b128 a[164:167], v244 offset:2304
v_mfma_f32_16x16x32_bf16 v[76:79], a[156:159], a[128:131], v[76:79]
ds_read_b64_tr_b16 a[180:181], v246 offset:8192
ds_read_b64_tr_b16 a[182:183], v246 offset:10240
v_mfma_f32_16x16x32_bf16 v[80:83], a[144:147], a[132:135], v[80:83]
buffer_load_dwordx4 v7, s[4:7], s44 offen offset:1024 sc1 lds
v_mfma_f32_16x16x32_bf16 v[84:87], a[148:151], a[132:135], v[84:87]
ds_read_b64_tr_b16 a[184:185], v247 offset:8192
ds_read_b64_tr_b16 a[186:187], v247 offset:10240
v_mfma_f32_16x16x32_bf16 v[88:91], a[152:155], a[132:135], v[88:91]
ds_read_b128 a[168:171], v244 offset:6144
ds_read_b128 a[172:175], v244 offset:6400
v_mfma_f32_16x16x32_bf16 v[92:95], a[156:159], a[132:135], v[92:95]
v_mfma_f32_16x16x32_bf16 v[96:99], a[144:147], a[136:139], v[96:99]
s_cmp_eq_u32 s46, 0
s_cselect_b32 s53, s56, s57
ds_read_b64_tr_b16 a[188:189], v248 offset:8192
ds_read_b64_tr_b16 a[190:191], v248 offset:10240
v_mfma_f32_16x16x32_bf16 v[100:103], a[148:151], a[136:139], v[100:103]
v_mfma_f32_16x16x32_bf16 v[104:107], a[152:155], a[136:139], v[104:107]
s_add_u32 s49, s46, s91
s_add_u32 s46, s46, s53
v_add_u32 v244, s53, v244
v_mfma_f32_16x16x32_bf16 v[108:111], a[156:159], a[136:139], v[108:111]
buffer_load_dwordx4 v7, s[4:7], s44 offen offset:2048 sc1 lds
v_mfma_f32_16x16x32_bf16 v[112:115], a[144:147], a[140:143], v[112:115]
s_sub_u32 s50, s50, 1
v_add_u32 v245, s53, v245
v_add_u32 v246, s53, v246
v_mfma_f32_16x16x32_bf16 v[116:119], a[148:151], a[140:143], v[116:119]
buffer_load_dwordx4 v7, s[4:7], s44 offen offset:3072 sc1 lds
v_mfma_f32_16x16x32_bf16 v[120:123], a[152:155], a[140:143], v[120:123]
v_add_u32 v247, s53, v247
v_add_u32 v248, s53, v248
v_mfma_f32_16x16x32_bf16 v[124:127], a[156:159], a[140:143], v[124:127]
s_waitcnt vmcnt(24) lgkmcnt(0)
s_barrier
s_cbranch_scc0 .L5b
s_cmp_eq_u32 s55, 0
s_cbranch_scc0 .L5odd
s_mov_b32 s28, 0
s_mov_b32 s29, 4096
s_mov_b32 s30, 8192
s_mov_b32 s31, 12288
v_mfma_f32_16x16x32_bf16 v[64:67], a[176:179], a[160:163], v[64:67]
ds_read_b64_tr_b16 a[144:145], v245 offset:0
ds_read_b64_tr_b16 a[146:147], v245 offset:2048
v_mfma_f32_16x16x32_bf16 v[68:71], a[180:183], a[160:163], v[68:71]
buffer_load_dword v250, v249, s[60:63], s90 offen sc1
v_mfma_f32_16x16x32_bf16 v[72:75], a[184:187], a[160:163], v[72:75]
ds_read_b128 a[128:131], v244 offset:0
ds_read_b128 a[132:135], v244 offset:256
v_mfma_f32_16x16x32_bf16 v[76:79], a[188:191], a[160:163], v[76:79]
ds_read_b64_tr_b16 a[148:149], v246 offset:0
ds_read_b64_tr_b16 a[150:151], v246 offset:2048
v_mfma_f32_16x16x32_bf16 v[80:83], a[176:179], a[164:167], v[80:83]
v_mfma_f32_16x16x32_bf16 v[84:87], a[180:183], a[164:167], v[84:87]
ds_read_b64_tr_b16 a[152:153], v247 offset:0
ds_read_b64_tr_b16 a[154:155], v247 offset:2048
v_mfma_f32_16x16x32_bf16 v[88:91], a[184:187], a[164:167], v[88:91]
ds_read_b128 a[136:139], v244 offset:4096
ds_read_b128 a[140:143], v244 offset:4352
v_mfma_f32_16x16x32_bf16 v[92:95], a[188:191], a[164:167], v[92:95]
v_mfma_f32_16x16x32_bf16 v[96:99], a[176:179], a[168:171], v[96:99]
ds_read_b64_tr_b16 a[156:157], v248 offset:0
ds_read_b64_tr_b16 a[158:159], v248 offset:2048
v_mfma_f32_16x16x32_bf16 v[100:103], a[180:183], a[168:171], v[100:103]
v_mfma_f32_16x16x32_bf16 v[104:107], a[184:187], a[168:171], v[104:107]
v_mfma_f32_16x16x32_bf16 v[108:111], a[188:191], a[168:171], v[108:111]
v_mfma_f32_16x16x32_bf16 v[112:115], a[176:179], a[172:175], v[112:115]
v_mfma_f32_16x16x32_bf16 v[116:119], a[180:183], a[172:175], v[116:119]
v_mfma_f32_16x16x32_bf16 v[120:123], a[184:187], a[172:175], v[120:123]
v_mfma_f32_16x16x32_bf16 v[124:127], a[188:191], a[172:175], v[124:127]
s_waitcnt lgkmcnt(0)
v_mfma_f32_16x16x32_bf16 v[64:67], a[144:147], a[128:131], v[64:67]
ds_read_b64_tr_b16 a[176:177], v245 offset:8192
ds_read_b64_tr_b16 a[178:179], v245 offset:10240
v_mfma_f32_16x16x32_bf16 v[68:71], a[148:151], a[128:131], v[68:71]
v_mfma_f32_16x16x32_bf16 v[72:75], a[152:155], a[128:131], v[72:75]
ds_read_b128 a[160:163], v244 offset:2048
ds_read_b128 a[164:167], v244 offset:2304
v_mfma_f32_16x16x32_bf16 v[76:79], a[156:159], a[128:131], v[76:79]
ds_read_b64_tr_b16 a[180:181], v246 offset:8192
ds_read_b64_tr_b16 a[182:183], v246 offset:10240
v_mfma_f32_16x16x32_bf16 v[80:83], a[144:147], a[132:135], v[80:83]
v_mfma_f32_16x16x32_bf16 v[84:87], a[148:151], a[132:135], v[84:87]
ds_read_b64_tr_b16 a[184:185], v247 offset:8192
ds_read_b64_tr_b16 a[186:187], v247 offset:10240
v_mfma_f32_16x16x32_bf16 v[88:91], a[152:155], a[132:135], v[88:91]
ds_read_b128 a[168:171], v244 offset:6144
ds_read_b128 a[172:175], v244 offset:6400
v_mfma_f32_16x16x32_bf16 v[92:95], a[156:159], a[132:135], v[92:95]
v_mfma_f32_16x16x32_bf16 v[96:99], a[144:147], a[136:139], v[96:99]
s_cmp_eq_u32 s46, 0
s_cselect_b32 s53, s56, s57
ds_read_b64_tr_b16 a[188:189], v248 offset:8192
ds_read_b64_tr_b16 a[190:191], v248 offset:10240
v_mfma_f32_16x16x32_bf16 v[100:103], a[148:151], a[136:139], v[100:103]
v_mfma_f32_16x16x32_bf16 v[104:107], a[152:155], a[136:139], v[104:107]
s_add_u32 s49, s46, s91
s_add_u32 s46, s46, s53
v_add_u32 v244, s53, v244
v_mfma_f32_16x16x32_bf16 v[108:111], a[156:159], a[136:139], v[108:111]
v_mfma_f32_16x16x32_bf16 v[112:115], a[144:147], a[140:143], v[112:115]
v_add_u32 v245, s53, v245
v_add_u32 v246, s53, v246
v_mfma_f32_16x16x32_bf16 v[116:119], a[148:151], a[140:143], v[116:119]
v_mfma_f32_16x16x32_bf16 v[120:123], a[152:155], a[140:143], v[120:123]
v_add_u32 v247, s53, v247
v_add_u32 v248, s53, v248
v_mfma_f32_16x16x32_bf16 v[124:127], a[156:159], a[140:143], v[124:127]
s_waitcnt vmcnt(17) lgkmcnt(0)
s_barrier
v_mfma_f32_16x16x32_bf16 v[64:67], a[176:179], a[160:163], v[64:67]
ds_read_b64_tr_b16 a[144:145], v245 offset:0
ds_read_b64_tr_b16 a[146:147], v245 offset:2048
v_mfma_f32_16x16x32_bf16 v[68:71], a[180:183], a[160:163], v[68:71]
v_mfma_f32_16x16x32_bf16 v[72:75], a[184:187], a[160:163], v[72:75]
ds_read_b128 a[128:131], v244 offset:0
ds_read_b128 a[132:135], v244 offset:256
v_mfma_f32_16x16x32_bf16 v[76:79], a[188:191], a[160:163], v[76:79]
ds_read_b64_tr_b16 a[148:149], v246 offset:0
ds_read_b64_tr_b16 a[150:151], v246 offset:2048
v_mfma_f32_16x16x32_bf16 v[80:83], a[176:179], a[164:167], v[80:83]
v_mfma_f32_16x16x32_bf16 v[84:87], a[180:183], a[164:167], v[84:87]
ds_read_b64_tr_b16 a[152:153], v247 offset:0
ds_read_b64_tr_b16 a[154:155], v247 offset:2048
v_mfma_f32_16x16x32_bf16 v[88:91], a[184:187], a[164:167], v[88:91]
ds_read_b128 a[136:139], v244 offset:4096
ds_read_b128 a[140:143], v244 offset:4352
v_mfma_f32_16x16x32_bf16 v[92:95], a[188:191], a[164:167], v[92:95]
v_mfma_f32_16x16x32_bf16 v[96:99], a[176:179], a[168:171], v[96:99]
ds_read_b64_tr_b16 a[156:157], v248 offset:0
ds_read_b64_tr_b16 a[158:159], v248 offset:2048
v_mfma_f32_16x16x32_bf16 v[100:103], a[180:183], a[168:171], v[100:103]
v_mfma_f32_16x16x32_bf16 v[104:107], a[184:187], a[168:171], v[104:107]
s_waitcnt vmcnt(0)
v_and_b32 v250, 7, v250
v_mfma_f32_16x16x32_bf16 v[108:111], a[188:191], a[168:171], v[108:111]
v_cmp_gt_u32 vcc, 5, v250
v_mfma_f32_16x16x32_bf16 v[112:115], a[176:179], a[172:175], v[112:115]
v_mfma_f32_16x16x32_bf16 v[116:119], a[180:183], a[172:175], v[116:119]
s_cmp_lg_u64 vcc, 0
s_cbranch_scc1 .Lspin5
.Lspin5_ret:
v_mfma_f32_16x16x32_bf16 v[120:123], a[184:187], a[172:175], v[120:123]
v_mfma_f32_16x16x32_bf16 v[124:127], a[188:191], a[172:175], v[124:127]
s_waitcnt lgkmcnt(0)
v_mfma_f32_16x16x32_bf16 v[64:67], a[144:147], a[128:131], v[64:67]
ds_read_b64_tr_b16 a[176:177], v245 offset:8192
ds_read_b64_tr_b16 a[178:179], v245 offset:10240
v_mfma_f32_16x16x32_bf16 v[68:71], a[148:151], a[128:131], v[68:71]
buffer_load_dwordx4 v[128:131], v7, s[12:15], s28 offen offset:0 sc1
v_mfma_f32_16x16x32_bf16 v[72:75], a[152:155], a[128:131], v[72:75]
ds_read_b128 a[160:163], v244 offset:2048
ds_read_b128 a[164:167], v244 offset:2304
v_mfma_f32_16x16x32_bf16 v[76:79], a[156:159], a[128:131], v[76:79]
ds_read_b64_tr_b16 a[180:181], v246 offset:8192
ds_read_b64_tr_b16 a[182:183], v246 offset:10240
v_mfma_f32_16x16x32_bf16 v[80:83], a[144:147], a[132:135], v[80:83]
buffer_load_dwordx4 v[132:135], v7, s[12:15], s28 offen offset:1024 sc1
v_mfma_f32_16x16x32_bf16 v[84:87], a[148:151], a[132:135], v[84:87]
ds_read_b64_tr_b16 a[184:185], v247 offset:8192
ds_read_b64_tr_b16 a[186:187], v247 offset:10240
v_mfma_f32_16x16x32_bf16 v[88:91], a[152:155], a[132:135], v[88:91]
ds_read_b128 a[168:171], v244 offset:6144
ds_read_b128 a[172:175], v244 offset:6400
v_mfma_f32_16x16x32_bf16 v[92:95], a[156:159], a[132:135], v[92:95]
buffer_load_dwordx4 v[136:139], v7, s[12:15], s28 offen offset:2048 sc1
v_mfma_f32_16x16x32_bf16 v[96:99], a[144:147], a[136:139], v[96:99]
s_cmp_eq_u32 s46, 0
s_cselect_b32 s53, s56, s57
ds_read_b64_tr_b16 a[188:189], v248 offset:8192
ds_read_b64_tr_b16 a[190:191], v248 offset:10240
v_mfma_f32_16x16x32_bf16 v[100:103], a[148:151], a[136:139], v[100:103]
v_mfma_f32_16x16x32_bf16 v[104:107], a[152:155], a[136:139], v[104:107]
s_add_u32 s49, s46, s91
s_add_u32 s46, s46, s53
buffer_load_dwordx4 v[140:143], v7, s[12:15], s28 offen offset:3072 sc1
v_add_u32 v244, s53, v244
v_mfma_f32_16x16x32_bf16 v[108:111], a[156:159], a[136:139], v[108:111]
v_mfma_f32_16x16x32_bf16 v[112:115], a[144:147], a[140:143], v[112:115]
v_add_u32 v245, s53, v245
v_add_u32 v246, s53, v246
v_mfma_f32_16x16x32_bf16 v[116:119], a[148:151], a[140:143], v[116:119]
v_mfma_f32_16x16x32_bf16 v[120:123], a[152:155], a[140:143], v[120:123]
v_add_u32 v247, s53, v247
v_add_u32 v248, s53, v248
v_mfma_f32_16x16x32_bf16 v[124:127], a[156:159], a[140:143], v[124:127]
s_waitcnt vmcnt(13) lgkmcnt(0)
s_barrier
v_mfma_f32_16x16x32_bf16 v[64:67], a[176:179], a[160:163], v[64:67]
ds_read_b64_tr_b16 a[144:145], v245 offset:0
ds_read_b64_tr_b16 a[146:147], v245 offset:2048
v_mfma_f32_16x16x32_bf16 v[68:71], a[180:183], a[160:163], v[68:71]
buffer_load_dwordx4 v[144:147], v7, s[12:15], s29 offen offset:0 sc1
v_mfma_f32_16x16x32_bf16 v[72:75], a[184:187], a[160:163], v[72:75]
ds_read_b128 a[128:131], v244 offset:0
ds_read_b128 a[132:135], v244 offset:256
v_mfma_f32_16x16x32_bf16 v[76:79], a[188:191], a[160:163], v[76:79]
ds_read_b64_tr_b16 a[148:149], v246 offset:0
ds_read_b64_tr_b16 a[150:151], v246 offset:2048
v_mfma_f32_16x16x32_bf16 v[80:83], a[176:179], a[164:167], v[80:83]
buffer_load_dwordx4 v[148:151], v7, s[12:15], s29 offen offset:1024 sc1
v_mfma_f32_16x16x32_bf16 v[84:87], a[180:183], a[164:167], v[84:87]
ds_read_b64_tr_b16 a[152:153], v247 offset:0
ds_read_b64_tr_b16 a[154:155], v247 offset:2048
v_mfma_f32_16x16x32_bf16 v[88:91], a[184:187], a[164:167], v[88:91]
ds_read_b128 a[136:139], v244 offset:4096
ds_read_b128 a[140:143], v244 offset:4352
v_mfma_f32_16x16x32_bf16 v[92:95], a[188:191], a[164:167], v[92:95]
v_mfma_f32_16x16x32_bf16 v[96:99], a[176:179], a[168:171], v[96:99]
ds_read_b64_tr_b16 a[156:157], v248 offset:0
ds_read_b64_tr_b16 a[158:159], v248 offset:2048
v_mfma_f32_16x16x32_bf16 v[100:103], a[180:183], a[168:171], v[100:103]
buffer_load_dwordx4 v[152:155], v7, s[12:15], s29 offen offset:2048 sc1
v_mfma_f32_16x16x32_bf16 v[104:107], a[184:187], a[168:171], v[104:107]
v_mfma_f32_16x16x32_bf16 v[108:111], a[188:191], a[168:171], v[108:111]
v_mfma_f32_16x16x32_bf16 v[112:115], a[176:179], a[172:175], v[112:115]
buffer_load_dwordx4 v[156:159], v7, s[12:15], s29 offen offset:3072 sc1
v_mfma_f32_16x16x32_bf16 v[116:119], a[180:183], a[172:175], v[116:119]
v_mfma_f32_16x16x32_bf16 v[120:123], a[184:187], a[172:175], v[120:123]
v_mfma_f32_16x16x32_bf16 v[124:127], a[188:191], a[172:175], v[124:127]
s_waitcnt lgkmcnt(0)
v_mfma_f32_16x16x32_bf16 v[64:67], a[144:147], a[128:131], v[64:67]
ds_read_b64_tr_b16 a[176:177], v245 offset:8192
ds_read_b64_tr_b16 a[178:179], v245 offset:10240
v_mfma_f32_16x16x32_bf16 v[68:71], a[148:151], a[128:131], v[68:71]
buffer_load_dwordx4 v[160:163], v7, s[12:15], s30 offen offset:0 sc1
v_mfma_f32_16x16x32_bf16 v[72:75], a[152:155], a[128:131], v[72:75]
ds_read_b128 a[160:163], v244 offset:2048
ds_read_b128 a[164:167], v244 offset:2304
v_mfma_f32_16x16x32_bf16 v[76:79], a[156:159], a[128:131], v[76:79]
ds_read_b64_tr_b16 a[180:181], v246 offset:8192
ds_read_b64_tr_b16 a[182:183], v246 offset:10240
v_mfma_f32_16x16x32_bf16 v[80:83], a[144:147], a[132:135], v[80:83]
buffer_load_dwordx4 v[164:167], v7, s[12:15], s30 offen offset:1024 sc1
v_mfma_f32_16x16x32_bf16 v[84:87], a[148:151], a[132:135], v[84:87]
ds_read_b64_tr_b16 a[184:185], v247 offset:8192
ds_read_b64_tr_b16 a[186:187], v247 offset:10240
v_mfma_f32_16x16x32_bf16 v[88:91], a[152:155], a[132:135], v[88:91]
ds_read_b128 a[168:171], v244 offset:6144
ds_read_b128 a[172:175], v244 offset:6400
v_mfma_f32_16x16x32_bf16 v[92:95], a[156:159], a[132:135], v[92:95]
v_mfma_f32_16x16x32_bf16 v[96:99], a[144:147], a[136:139], v[96:99]
s_cmp_eq_u32 s46, 0
s_cselect_b32 s53, s56, s57
ds_read_b64_tr_b16 a[188:189], v248 offset:8192
ds_read_b64_tr_b16 a[190:191], v248 offset:10240
v_mfma_f32_16x16x32_bf16 v[100:103], a[148:151], a[136:139], v[100:103]
v_mfma_f32_16x16x32_bf16 v[104:107], a[152:155], a[136:139], v[104:107]
s_add_u32 s49, s46, s91
s_add_u32 s46, s46, s53
buffer_load_dwordx4 v[168:171], v7, s[12:15], s30 offen offset:2048 sc1
v_add_u32 v244, s53, v244
v_mfma_f32_16x16x32_bf16 v[108:111], a[156:159], a[136:139], v[108:111]
v_mfma_f32_16x16x32_bf16 v[112:115], a[144:147], a[140:143], v[112:115]
v_add_u32 v245, s53, v245
v_add_u32 v246, s53, v246
v_mfma_f32_16x16x32_bf16 v[116:119], a[148:151], a[140:143], v[116:119]
buffer_load_dwordx4 v[172:175], v7, s[12:15], s30 offen offset:3072 sc1
v_mfma_f32_16x16x32_bf16 v[120:123], a[152:155], a[140:143], v[120:123]
v_add_u32 v247, s53, v247
v_add_u32 v248, s53, v248
v_mfma_f32_16x16x32_bf16 v[124:127], a[156:159], a[140:143], v[124:127]
s_waitcnt vmcnt(13) lgkmcnt(0)
s_barrier
v_mfma_f32_16x16x32_bf16 v[64:67], a[176:179], a[160:163], v[64:67]
ds_read_b64_tr_b16 a[144:145], v245 offset:0
ds_read_b64_tr_b16 a[146:147], v245 offset:2048
v_mfma_f32_16x16x32_bf16 v[68:71], a[180:183], a[160:163], v[68:71]
buffer_load_dwordx4 v[176:179], v7, s[12:15], s31 offen offset:0 sc1
v_mfma_f32_16x16x32_bf16 v[72:75], a[184:187], a[160:163], v[72:75]
ds_read_b128 a[128:131], v244 offset:0
ds_read_b128 a[132:135], v244 offset:256
v_mfma_f32_16x16x32_bf16 v[76:79], a[188:191], a[160:163], v[76:79]
ds_read_b64_tr_b16 a[148:149], v246 offset:0
ds_read_b64_tr_b16 a[150:151], v246 offset:2048
v_mfma_f32_16x16x32_bf16 v[80:83], a[176:179], a[164:167], v[80:83]
buffer_load_dwordx4 v[180:183], v7, s[12:15], s31 offen offset:1024 sc1
v_mfma_f32_16x16x32_bf16 v[84:87], a[180:183], a[164:167], v[84:87]
ds_read_b64_tr_b16 a[152:153], v247 offset:0
ds_read_b64_tr_b16 a[154:155], v247 offset:2048
v_mfma_f32_16x16x32_bf16 v[88:91], a[184:187], a[164:167], v[88:91]
ds_read_b128 a[136:139], v244 offset:4096
ds_read_b128 a[140:143], v244 offset:4352
v_mfma_f32_16x16x32_bf16 v[92:95], a[188:191], a[164:167], v[92:95]
v_mfma_f32_16x16x32_bf16 v[96:99], a[176:179], a[168:171], v[96:99]
ds_read_b64_tr_b16 a[156:157], v248 offset:0
ds_read_b64_tr_b16 a[158:159], v248 offset:2048
v_mfma_f32_16x16x32_bf16 v[100:103], a[180:183], a[168:171], v[100:103]
buffer_load_dwordx4 v[184:187], v7, s[12:15], s31 offen offset:2048 sc1
v_mfma_f32_16x16x32_bf16 v[104:107], a[184:187], a[168:171], v[104:107]
v_mfma_f32_16x16x32_bf16 v[108:111], a[188:191], a[168:171], v[108:111]
v_mfma_f32_16x16x32_bf16 v[112:115], a[176:179], a[172:175], v[112:115]
buffer_load_dwordx4 v[188:191], v7, s[12:15], s31 offen offset:3072 sc1
v_mfma_f32_16x16x32_bf16 v[116:119], a[180:183], a[172:175], v[116:119]
v_mfma_f32_16x16x32_bf16 v[120:123], a[184:187], a[172:175], v[120:123]
v_mfma_f32_16x16x32_bf16 v[124:127], a[188:191], a[172:175], v[124:127]
s_waitcnt lgkmcnt(0)
v_mfma_f32_16x16x32_bf16 v[64:67], a[144:147], a[128:131], v[64:67]
ds_read_b64_tr_b16 a[176:177], v245 offset:8192
ds_read_b64_tr_b16 a[178:179], v245 offset:10240
v_mfma_f32_16x16x32_bf16 v[68:71], a[148:151], a[128:131], v[68:71]
v_mfma_f32_16x16x32_bf16 v[72:75], a[152:155], a[128:131], v[72:75]
ds_read_b128 a[160:163], v244 offset:2048
ds_read_b128 a[164:167], v244 offset:2304
v_mfma_f32_16x16x32_bf16 v[76:79], a[156:159], a[128:131], v[76:79]
ds_read_b64_tr_b16 a[180:181], v246 offset:8192
ds_read_b64_tr_b16 a[182:183], v246 offset:10240
v_mfma_f32_16x16x32_bf16 v[80:83], a[144:147], a[132:135], v[80:83]
v_mfma_f32_16x16x32_bf16 v[84:87], a[148:151], a[132:135], v[84:87]
ds_read_b64_tr_b16 a[184:185], v247 offset:8192
ds_read_b64_tr_b16 a[186:187], v247 offset:10240
v_mfma_f32_16x16x32_bf16 v[88:91], a[152:155], a[132:135], v[88:91]
ds_read_b128 a[168:171], v244 offset:6144
ds_read_b128 a[172:175], v244 offset:6400
v_mfma_f32_16x16x32_bf16 v[92:95], a[156:159], a[132:135], v[92:95]
v_mfma_f32_16x16x32_bf16 v[96:99], a[144:147], a[136:139], v[96:99]
s_cmp_eq_u32 s46, 0
s_cselect_b32 s53, s56, s57
ds_read_b64_tr_b16 a[188:189], v248 offset:8192
ds_read_b64_tr_b16 a[190:191], v248 offset:10240
v_mfma_f32_16x16x32_bf16 v[100:103], a[148:151], a[136:139], v[100:103]
v_mfma_f32_16x16x32_bf16 v[104:107], a[152:155], a[136:139], v[104:107]
s_add_u32 s49, s46, s91
s_add_u32 s46, s46, s53
v_add_u32 v244, s53, v244
v_mfma_f32_16x16x32_bf16 v[108:111], a[156:159], a[136:139], v[108:111]
v_mfma_f32_16x16x32_bf16 v[112:115], a[144:147], a[140:143], v[112:115]
v_add_u32 v245, s53, v245
v_add_u32 v246, s53, v246
v_mfma_f32_16x16x32_bf16 v[116:119], a[148:151], a[140:143], v[116:119]
v_mfma_f32_16x16x32_bf16 v[120:123], a[152:155], a[140:143], v[120:123]
v_add_u32 v247, s53, v247
v_add_u32 v248, s53, v248
v_mfma_f32_16x16x32_bf16 v[124:127], a[156:159], a[140:143], v[124:127]
s_waitcnt  lgkmcnt(0)
s_barrier
v_mfma_f32_16x16x32_bf16 v[64:67], a[176:179], a[160:163], v[64:67]
v_mfma_f32_16x16x32_bf16 v[68:71], a[180:183], a[160:163], v[68:71]
v_mfma_f32_16x16x32_bf16 v[72:75], a[184:187], a[160:163], v[72:75]
v_mfma_f32_16x16x32_bf16 v[76:79], a[188:191], a[160:163], v[76:79]
v_mfma_f32_16x16x32_bf16 v[80:83], a[176:179], a[164:167], v[80:83]
v_mfma_f32_16x16x32_bf16 v[84:87], a[180:183], a[164:167], v[84:87]
v_mfma_f32_16x16x32_bf16 v[88:91], a[184:187], a[164:167], v[88:91]
v_mfma_f32_16x16x32_bf16 v[92:95], a[188:191], a[164:167], v[92:95]
v_mfma_f32_16x16x32_bf16 v[96:99], a[176:179], a[168:171], v[96:99]
v_mfma_f32_16x16x32_bf16 v[100:103], a[180:183], a[168:171], v[100:103]
v_mfma_f32_16x16x32_bf16 v[104:107], a[184:187], a[168:171], v[104:107]
v_mfma_f32_16x16x32_bf16 v[108:111], a[188:191], a[168:171], v[108:111]
v_mfma_f32_16x16x32_bf16 v[112:115], a[176:179], a[172:175], v[112:115]
v_mfma_f32_16x16x32_bf16 v[116:119], a[180:183], a[172:175], v[116:119]
v_mfma_f32_16x16x32_bf16 v[120:123], a[184:187], a[172:175], v[120:123]
v_mfma_f32_16x16x32_bf16 v[124:127], a[188:191], a[172:175], v[124:127]
s_waitcnt vmcnt(12)
v_add_f32 v64, v64, v128
v_add_f32 v65, v65, v129
v_add_f32 v66, v66, v130
v_add_f32 v67, v67, v131
v_add_f32 v68, v68, v132
v_add_f32 v69, v69, v133
v_add_f32 v70, v70, v134
v_add_f32 v71, v71, v135
v_add_f32 v72, v72, v136
v_add_f32 v73, v73, v137
v_add_f32 v74, v74, v138
v_add_f32 v75, v75, v139
v_add_f32 v76, v76, v140
v_add_f32 v77, v77, v141
v_add_f32 v78, v78, v142
v_add_f32 v79, v79, v143
v_cvt_pk_bf16_f32 v192, v64, v65
v_cvt_pk_bf16_f32 v194, v68, v69
v_cvt_pk_bf16_f32 v193, v66, v67
v_cvt_pk_bf16_f32 v195, v70, v71
v_cvt_pk_bf16_f32 v196, v72, v73
v_cvt_pk_bf16_f32 v198, v76, v77
v_cvt_pk_bf16_f32 v197, v74, v75
v_cvt_pk_bf16_f32 v199, v78, v79
v_permlane16_swap_b32 v192, v194
v_permlane16_swap_b32 v193, v195
v_permlane16_swap_b32 v196, v198
v_permlane16_swap_b32 v197, v199
v_mov_b32 v200, v192
v_mov_b32 v201, v193
v_mov_b32 v202, v194
v_mov_b32 v203, v195
v_mov_b32_dpp v192, v196 row_ror:8 row_mask:0xf bank_mask:0xc
v_mov_b32_dpp v193, v197 row_ror:8 row_mask:0xf bank_mask:0xc
v_mov_b32_dpp v194, v198 row_ror:8 row_mask:0xf bank_mask:0xc
v_mov_b32_dpp v195, v199 row_ror:8 row_mask:0xf bank_mask:0xc
v_mov_b32_dpp v196, v200 row_ror:8 row_mask:0xf bank_mask:0x3
v_mov_b32_dpp v197, v201 row_ror:8 row_mask:0xf bank_mask:0x3
v_mov_b32_dpp v198, v202 row_ror:8 row_mask:0xf bank_mask:0x3
v_mov_b32_dpp v199, v203 row_ror:8 row_mask:0xf bank_mask:0x3
s_mov_b32 s28, 0
s_mov_b32 s29, 32768
buffer_store_dwordx4 v[192:195], v251, s[16:19], s28 offen
buffer_store_dwordx4 v[196:199], v251, s[16:19], s29 offen
s_waitcnt vmcnt(10)
v_add_f32 v80, v80, v144
v_add_f32 v81, v81, v145
v_add_f32 v82, v82, v146
v_add_f32 v83, v83, v147
v_add_f32 v84, v84, v148
v_add_f32 v85, v85, v149
v_add_f32 v86, v86, v150
v_add_f32 v87, v87, v151
v_add_f32 v88, v88, v152
v_add_f32 v89, v89, v153
v_add_f32 v90, v90, v154
v_add_f32 v91, v91, v155
v_add_f32 v92, v92, v156
v_add_f32 v93, v93, v157
v_add_f32 v94, v94, v158
v_add_f32 v95, v95, v159
v_cvt_pk_bf16_f32 v204, v80, v81
v_cvt_pk_bf16_f32 v206, v84, v85
v_cvt_pk_bf16_f32 v205, v82, v83
v_cvt_pk_bf16_f32 v207, v86, v87
v_cvt_pk_bf16_f32 v208, v88, v89
v_cvt_pk_bf16_f32 v210, v92, v93
v_cvt_pk_bf16_f32 v209, v90, v91
v_cvt_pk_bf16_f32 v211, v94, v95
v_permlane16_swap_b32 v204, v206
v_permlane16_swap_b32 v205, v207
v_permlane16_swap_b32 v208, v210
v_permlane16_swap_b32 v209, v211
v_mov_b32 v212, v204
v_mov_b32 v213, v205
v_mov_b32 v214, v206
v_mov_b32 v215, v207
v_mov_b32_dpp v204, v208 row_ror:8 row_mask:0xf bank_mask:0xc
v_mov_b32_dpp v205, v209 row_ror:8 row_mask:0xf bank_mask:0xc
v_mov_b32_dpp v206, v210 row_ror:8 row_mask:0xf bank_mask:0xc
v_mov_b32_dpp v207, v211 row_ror:8 row_mask:0xf bank_mask:0xc
v_mov_b32_dpp v208, v212 row_ror:8 row_mask:0xf bank_mask:0x3
v_mov_b32_dpp v209, v213 row_ror:8 row_mask:0xf bank_mask:0x3
v_mov_b32_dpp v210, v214 row_ror:8 row_mask:0xf bank_mask:0x3
v_mov_b32_dpp v211, v215 row_ror:8 row_mask:0xf bank_mask:0x3
s_mov_b32 s28, 65536
s_mov_b32 s29, 98304
buffer_store_dwordx4 v[204:207], v251, s[16:19], s28 offen
buffer_store_dwordx4 v[208:211], v251, s[16:19], s29 offen
s_waitcnt vmcnt(8)
v_add_f32 v96, v96, v160
v_add_f32 v97, v97, v161
v_add_f32 v98, v98, v162
v_add_f32 v99, v99, v163
v_add_f32 v100, v100, v164
v_add_f32 v101, v101, v165
v_add_f32 v102, v102, v166
v_add_f32 v103, v103, v167
v_add_f32 v104, v104, v168
v_add_f32 v105, v105, v169
v_add_f32 v106, v106, v170
v_add_f32 v107, v107, v171
v_add_f32 v108, v108, v172
v_add_f32 v109, v109, v173
v_add_f32 v110, v110, v174
v_add_f32 v111, v111, v175
v_cvt_pk_bf16_f32 v192, v96, v97
v_cvt_pk_bf16_f32 v194, v100, v101
v_cvt_pk_bf16_f32 v193, v98, v99
v_cvt_pk_bf16_f32 v195, v102, v103
v_cvt_pk_bf16_f32 v196, v104, v105
v_cvt_pk_bf16_f32 v198, v108, v109
v_cvt_pk_bf16_f32 v197, v106, v107
v_cvt_pk_bf16_f32 v199, v110, v111
v_permlane16_swap_b32 v192, v194
v_permlane16_swap_b32 v193, v195
v_permlane16_swap_b32 v196, v198
v_permlane16_swap_b32 v197, v199
v_mov_b32 v200, v192
v_mov_b32 v201, v193
v_mov_b32 v202, v194
v_mov_b32 v203, v195
v_mov_b32_dpp v192, v196 row_ror:8 row_mask:0xf bank_mask:0xc
v_mov_b32_dpp v193, v197 row_ror:8 row_mask:0xf bank_mask:0xc
v_mov_b32_dpp v194, v198 row_ror:8 row_mask:0xf bank_mask:0xc
v_mov_b32_dpp v195, v199 row_ror:8 row_mask:0xf bank_mask:0xc
v_mov_b32_dpp v196, v200 row_ror:8 row_mask:0xf bank_mask:0x3
v_mov_b32_dpp v197, v201 row_ror:8 row_mask:0xf bank_mask:0x3
v_mov_b32_dpp v198, v202 row_ror:8 row_mask:0xf bank_mask:0x3
v_mov_b32_dpp v199, v203 row_ror:8 row_mask:0xf bank_mask:0x3
s_mov_b32 s28, 131072
s_mov_b32 s29, 163840
buffer_store_dwordx4 v[192:195], v251, s[16:19], s28 offen
buffer_store_dwordx4 v[196:199], v251, s[16:19], s29 offen
s_waitcnt vmcnt(6)
v_add_f32 v112, v112, v176
v_add_f32 v113, v113, v177
v_add_f32 v114, v114, v178
v_add_f32 v115, v115, v179
v_add_f32 v116, v116, v180
v_add_f32 v117, v117, v181
v_add_f32 v118, v118, v182
v_add_f32 v119, v119, v183
v_add_f32 v120, v120, v184
v_add_f32 v121, v121, v185
v_add_f32 v122, v122, v186
v_add_f32 v123, v123, v187
v_add_f32 v124, v124, v188
v_add_f32 v125, v125, v189
v_add_f32 v126, v126, v190
v_add_f32 v127, v127, v191
v_cvt_pk_bf16_f32 v204, v112, v113
v_cvt_pk_bf16_f32 v206, v116, v117
v_cvt_pk_bf16_f32 v205, v114, v115
v_cvt_pk_bf16_f32 v207, v118, v119
v_cvt_pk_bf16_f32 v208, v120, v121
v_cvt_pk_bf16_f32 v210, v124, v125
v_cvt_pk_bf16_f32 v209, v122, v123
v_cvt_pk_bf16_f32 v211, v126, v127
v_permlane16_swap_b32 v204, v206
v_permlane16_swap_b32 v205, v207
v_permlane16_swap_b32 v208, v210
v_permlane16_swap_b32 v209, v211
v_mov_b32 v212, v204
v_mov_b32 v213, v205
v_mov_b32 v214, v206
v_mov_b32 v215, v207
v_mov_b32_dpp v204, v208 row_ror:8 row_mask:0xf bank_mask:0xc
v_mov_b32_dpp v205, v209 row_ror:8 row_mask:0xf bank_mask:0xc
v_mov_b32_dpp v206, v210 row_ror:8 row_mask:0xf bank_mask:0xc
v_mov_b32_dpp v207, v211 row_ror:8 row_mask:0xf bank_mask:0xc
v_mov_b32_dpp v208, v212 row_ror:8 row_mask:0xf bank_mask:0x3
v_mov_b32_dpp v209, v213 row_ror:8 row_mask:0xf bank_mask:0x3
v_mov_b32_dpp v210, v214 row_ror:8 row_mask:0xf bank_mask:0x3
v_mov_b32_dpp v211, v215 row_ror:8 row_mask:0xf bank_mask:0x3
s_mov_b32 s28, 196608
s_mov_b32 s29, 229376
buffer_store_dwordx4 v[204:207], v251, s[16:19], s28 offen
buffer_store_dwordx4 v[208:211], v251, s[16:19], s29 offen
s_branch .Lend
.L5odd:
v_mfma_f32_16x16x32_bf16 v[64:67], a[176:179], a[160:163], v[64:67]
s_add_u32 m0, s49, 16384
s_add_u32 s44, s44, 4096
ds_read_b64_tr_b16 a[144:145], v245 offset:0
ds_read_b64_tr_b16 a[146:147], v245 offset:2048
v_mfma_f32_16x16x32_bf16 v[68:71], a[180:183], a[160:163], v[68:71]
buffer_load_dwordx4 v240, s[8:11], s45 offen offset:0 lds
v_mfma_f32_16x16x32_bf16 v[72:75], a[184:187], a[160:163], v[72:75]
ds_read_b128 a[128:131], v244 offset:0
ds_read_b128 a[132:135], v244 offset:256
v_mfma_f32_16x16x32_bf16 v[76:79], a[188:191], a[160:163], v[76:79]
ds_read_b64_tr_b16 a[148:149], v246 offset:0
ds_read_b64_tr_b16 a[150:151], v246 offset:2048
v_mfma_f32_16x16x32_bf16 v[80:83], a[176:179], a[164:167], v[80:83]
buffer_load_dwordx4 v241, s[8:11], s45 offen offset:1024 lds
v_mfma_f32_16x16x32_bf16 v[84:87], a[180:183], a[164:167], v[84:87]
ds_read_b64_tr_b16 a[152:153], v247 offset:0
ds_read_b64_tr_b16 a[154:155], v247 offset:2048
v_mfma_f32_16x16x32_bf16 v[88:91], a[184:187], a[164:167], v[88:91]
ds_read_b128 a[136:139], v244 offset:4096
ds_read_b128 a[140:143], v244 offset:4352
v_mfma_f32_16x16x32_bf16 v[92:95], a[188:191], a[164:167], v[92:95]
buffer_load_dword v250, v249, s[60:63], s90 offen sc1
v_mfma_f32_16x16x32_bf16 v[96:99], a[176:179], a[168:171], v[96:99]
ds_read_b64_tr_b16 a[156:157], v248 offset:0
ds_read_b64_tr_b16 a[158:159], v248 offset:2048
v_mfma_f32_16x16x32_bf16 v[100:103], a[180:183], a[168:171], v[100:103]
buffer_load_dwordx4 v242, s[8:11], s45 offen offset:2048 lds
v_mfma_f32_16x16x32_bf16 v[104:107], a[184:187], a[168:171], v[104:107]
v_mfma_f32_16x16x32_bf16 v[108:111], a[188:191], a[168:171], v[108:111]
v_mfma_f32_16x16x32_bf16 v[112:115], a[176:179], a[172:175], v[112:115]
buffer_load_dwordx4 v243, s[8:11], s45 offen offset:3072 lds
v_mfma_f32_16x16x32_bf16 v[116:119], a[180:183], a[172:175], v[116:119]
s_add_u32 s45, s45, 131072
v_mfma_f32_16x16x32_bf16 v[120:123], a[184:187], a[172:175], v[120:123]
v_mfma_f32_16x16x32_bf16 v[124:127], a[188:191], a[172:175], v[124:127]
s_waitcnt lgkmcnt(0)
v_mfma_f32_16x16x32_bf16 v[64:67], a[144:147], a[128:131], v[64:67]
s_mov_b32 m0, s49
ds_read_b64_tr_b16 a[176:177], v245 offset:8192
ds_read_b64_tr_b16 a[178:179], v245 offset:10240
v_mfma_f32_16x16x32_bf16 v[68:71], a[148:151], a[128:131], v[68:71]
buffer_load_dwordx4 v7, s[4:7], s44 offen offset:0 sc1 lds
v_mfma_f32_16x16x32_bf16 v[72:75], a[152:155], a[128:131], v[72:75]
ds_read_b128 a[160:163], v244 offset:2048
ds_read_b128 a[164:167], v244 offset:2304
v_mfma_f32_16x16x32_bf16 v[76:79], a[156:159], a[128:131], v[76:79]
ds_read_b64_tr_b16 a[180:181], v246 offset:8192
ds_read_b64_tr_b16 a[182:183], v246 offset:10240
v_mfma_f32_16x16x32_bf16 v[80:83], a[144:147], a[132:135], v[80:83]
buffer_load_dwordx4 v7, s[4:7], s44 offen offset:1024 sc1 lds
v_mfma_f32_16x16x32_bf16 v[84:87], a[148:151], a[132:135], v[84:87]
ds_read_b64_tr_b16 a[184:185], v247 offset:8192
ds_read_b64_tr_b16 a[186:187], v247 offset:10240
v_mfma_f32_16x16x32_bf16 v[88:91], a[152:155], a[132:135], v[88:91]
ds_read_b128 a[168:171], v244 offset:6144
ds_read_b128 a[172:175], v244 offset:6400
v_mfma_f32_16x16x32_bf16 v[92:95], a[156:159], a[132:135], v[92:95]
v_mfma_f32_16x16x32_bf16 v[96:99], a[144:147], a[136:139], v[96:99]
s_cmp_eq_u32 s46, 0
s_cselect_b32 s53, s56, s57
ds_read_b64_tr_b16 a[188:189], v248 offset:8192
ds_read_b64_tr_b16 a[190:191], v248 offset:10240
v_mfma_f32_16x16x32_bf16 v[100:103], a[148:151], a[136:139], v[100:103]
v_mfma_f32_16x16x32_bf16 v[104:107], a[152:155], a[136:139], v[104:107]
s_add_u32 s49, s46, s91
s_add_u32 s46, s46, s53
v_add_u32 v244, s53, v244
v_mfma_f32_16x16x32_bf16 v[108:111], a[156:159], a[136:139], v[108:111]
buffer_load_dwordx4 v7, s[4:7], s44 offen offset:2048 sc1 lds
v_mfma_f32_16x16x32_bf16 v[112:115], a[144:147], a[140:143], v[112:115]
v_add_u32 v245, s53, v245
v_add_u32 v246, s53, v246
v_mfma_f32_16x16x32_bf16 v[116:119], a[148:151], a[140:143], v[116:119]
buffer_load_dwordx4 v7, s[4:7], s44 offen offset:3072 sc1 lds
v_mfma_f32_16x16x32_bf16 v[120:123], a[152:155], a[140:143], v[120:123]
v_add_u32 v247, s53, v247
v_add_u32 v248, s53, v248
v_mfma_f32_16x16x32_bf16 v[124:127], a[156:159], a[140:143], v[124:127]
s_waitcnt vmcnt(25) lgkmcnt(0)
s_barrier
v_mfma_f32_16x16x32_bf16 v[64:67], a[176:179], a[160:163], v[64:67]
s_add_u32 m0, s49, 16384
s_add_u32 s44, s44, 4096
ds_read_b64_tr_b16 a[144:145], v245 offset:0
ds_read_b64_tr_b16 a[146:147], v245 offset:2048
v_mfma_f32_16x16x32_bf16 v[68:71], a[180:183], a[160:163], v[68:71]
buffer_load_dwordx4 v240, s[8:11], s45 offen offset:0 lds
v_mfma_f32_16x16x32_bf16 v[72:75], a[184:187], a[160:163], v[72:75]
ds_read_b128 a[128:131], v244 offset:0
ds_read_b128 a[132:135], v244 offset:256
v_mfma_f32_16x16x32_bf16 v[76:79], a[188:191], a[160:163], v[76:79]
ds_read_b64_tr_b16 a[148:149], v246 offset:0
ds_read_b64_tr_b16 a[150:151], v246 offset:2048
v_mfma_f32_16x16x32_bf16 v[80:83], a[176:179], a[164:167], v[80:83]
buffer_load_dwordx4 v241, s[8:11], s45 offen offset:1024 lds
v_mfma_f32_16x16x32_bf16 v[84:87], a[180:183], a[164:167], v[84:87]
ds_read_b64_tr_b16 a[152:153], v247 offset:0
ds_read_b64_tr_b16 a[154:155], v247 offset:2048
v_mfma_f32_16x16x32_bf16 v[88:91], a[184:187], a[164:167], v[88:91]
ds_read_b128 a[136:139], v244 offset:4096
ds_read_b128 a[140:143], v244 offset:4352
v_mfma_f32_16x16x32_bf16 v[92:95], a[188:191], a[164:167], v[92:95]
v_mfma_f32_16x16x32_bf16 v[96:99], a[176:179], a[168:171], v[96:99]
ds_read_b64_tr_b16 a[156:157], v248 offset:0
ds_read_b64_tr_b16 a[158:159], v248 offset:2048
v_mfma_f32_16x16x32_bf16 v[100:103], a[180:183], a[168:171], v[100:103]
buffer_load_dwordx4 v242, s[8:11], s45 offen offset:2048 lds
v_mfma_f32_16x16x32_bf16 v[104:107], a[184:187], a[168:171], v[104:107]
v_mfma_f32_16x16x32_bf16 v[108:111], a[188:191], a[168:171], v[108:111]
v_mfma_f32_16x16x32_bf16 v[112:115], a[176:179], a[172:175], v[112:115]
buffer_load_dwordx4 v243, s[8:11], s45 offen offset:3072 lds
v_mfma_f32_16x16x32_bf16 v[116:119], a[180:183], a[172:175], v[116:119]
s_add_u32 s45, s45, 131072
v_mfma_f32_16x16x32_bf16 v[120:123], a[184:187], a[172:175], v[120:123]
v_mfma_f32_16x16x32_bf16 v[124:127], a[188:191], a[172:175], v[124:127]
s_waitcnt lgkmcnt(0)
v_mfma_f32_16x16x32_bf16 v[64:67], a[144:147], a[128:131], v[64:67]
s_mov_b32 m0, s49
ds_read_b64_tr_b16 a[176:177], v245 offset:8192
ds_read_b64_tr_b16 a[178:179], v245 offset:10240
v_mfma_f32_16x16x32_bf16 v[68:71], a[148:151], a[128:131], v[68:71]
buffer_load_dwordx4 v7, s[4:7], s44 offen offset:0 sc1 lds
v_mfma_f32_16x16x32_bf16 v[72:75], a[152:155], a[128:131], v[72:75]
ds_read_b128 a[160:163], v244 offset:2048
ds_read_b128 a[164:167], v244 offset:2304
v_mfma_f32_16x16x32_bf16 v[76:79], a[156:159], a[128:131], v[76:79]
ds_read_b64_tr_b16 a[180:181], v246 offset:8192
ds_read_b64_tr_b16 a[182:183], v246 offset:10240
v_mfma_f32_16x16x32_bf16 v[80:83], a[144:147], a[132:135], v[80:83]
buffer_load_dwordx4 v7, s[4:7], s44 offen offset:1024 sc1 lds
v_mfma_f32_16x16x32_bf16 v[84:87], a[148:151], a[132:135], v[84:87]
ds_read_b64_tr_b16 a[184:185], v247 offset:8192
ds_read_b64_tr_b16 a[186:187], v247 offset:10240
v_mfma_f32_16x16x32_bf16 v[88:91], a[152:155], a[132:135], v[88:91]
ds_read_b128 a[168:171], v244 offset:6144
ds_read_b128 a[172:175], v244 offset:6400
v_mfma_f32_16x16x32_bf16 v[92:95], a[156:159], a[132:135], v[92:95]
v_mfma_f32_16x16x32_bf16 v[96:99], a[144:147], a[136:139], v[96:99]
s_cmp_eq_u32 s46, 0
s_cselect_b32 s53, s56, s57
ds_read_b64_tr_b16 a[188:189], v248 offset:8192
ds_read_b64_tr_b16 a[190:191], v248 offset:10240
v_mfma_f32_16x16x32_bf16 v[100:103], a[148:151], a[136:139], v[100:103]
v_mfma_f32_16x16x32_bf16 v[104:107], a[152:155], a[136:139], v[104:107]
s_add_u32 s49, s46, s91
s_add_u32 s46, s46, s53
v_add_u32 v244, s53, v244
v_mfma_f32_16x16x32_bf16 v[108:111], a[156:159], a[136:139], v[108:111]
buffer_load_dwordx4 v7, s[4:7], s44 offen offset:2048 sc1 lds
v_mfma_f32_16x16x32_bf16 v[112:115], a[144:147], a[140:143], v[112:115]
v_add_u32 v245, s53, v245
v_add_u32 v246, s53, v246
v_mfma_f32_16x16x32_bf16 v[116:119], a[148:151], a[140:143], v[116:119]
buffer_load_dwordx4 v7, s[4:7], s44 offen offset:3072 sc1 lds
v_mfma_f32_16x16x32_bf16 v[120:123], a[152:155], a[140:143], v[120:123]
v_add_u32 v247, s53, v247
v_add_u32 v248, s53, v248
v_mfma_f32_16x16x32_bf16 v[124:127], a[156:159], a[140:143], v[124:127]
s_waitcnt vmcnt(25) lgkmcnt(0)
s_barrier
s_lshl_b32 s54, s51, 2
s_add_u32 s54, s54, s3
s_lshl_b32 s54, s54, 16
s_add_u32 s54, s54, 50331648
s_add_u32 s4, s84, s54
s_addc_u32 s5, s85, 0
s_and_b32 s5, s5, 0xffff
s_mov_b32 s6, 65536
s_lshl_b32 s54, s3, 15
s_add_u32 s54, s54, s52
s_sub_u32 s28, 0x200000, s54
s_add_u32 s8, s68, s54
s_addc_u32 s9, s69, 0
s_and_b32 s9, s9, 0xffff
s_mov_b32 s10, s28
s_mov_b32 s44, -4096
s_mov_b32 s45, 0
v_mfma_f32_16x16x32_bf16 v[64:67], a[176:179], a[160:163], v[64:67]
s_add_u32 m0, s49, 16384
s_add_u32 s44, s44, 4096
ds_read_b64_tr_b16 a[144:145], v245 offset:0
ds_read_b64_tr_b16 a[146:147], v245 offset:2048
v_mfma_f32_16x16x32_bf16 v[68:71], a[180:183], a[160:163], v[68:71]
buffer_load_dwordx4 v240, s[8:11], s45 offen offset:0 lds
v_mfma_f32_16x16x32_bf16 v[72:75], a[184:187], a[160:163], v[72:75]
ds_read_b128 a[128:131], v244 offset:0
ds_read_b128 a[132:135], v244 offset:256
v_mfma_f32_16x16x32_bf16 v[76:79], a[188:191], a[160:163], v[76:79]
ds_read_b64_tr_b16 a[148:149], v246 offset:0
ds_read_b64_tr_b16 a[150:151], v246 offset:2048
v_mfma_f32_16x16x32_bf16 v[80:83], a[176:179], a[164:167], v[80:83]
buffer_load_dwordx4 v241, s[8:11], s45 offen offset:1024 lds
v_mfma_f32_16x16x32_bf16 v[84:87], a[180:183], a[164:167], v[84:87]
ds_read_b64_tr_b16 a[152:153], v247 offset:0
ds_read_b64_tr_b16 a[154:155], v247 offset:2048
v_mfma_f32_16x16x32_bf16 v[88:91], a[184:187], a[164:167], v[88:91]
ds_read_b128 a[136:139], v244 offset:4096
ds_read_b128 a[140:143], v244 offset:4352
v_mfma_f32_16x16x32_bf16 v[92:95], a[188:191], a[164:167], v[92:95]
v_mfma_f32_16x16x32_bf16 v[96:99], a[176:179], a[168:171], v[96:99]
ds_read_b64_tr_b16 a[156:157], v248 offset:0
ds_read_b64_tr_b16 a[158:159], v248 offset:2048
v_mfma_f32_16x16x32_bf16 v[100:103], a[180:183], a[168:171], v[100:103]
buffer_load_dwordx4 v242, s[8:11], s45 offen offset:2048 lds
v_mfma_f32_16x16x32_bf16 v[104:107], a[184:187], a[168:171], v[104:107]
s_waitcnt vmcnt(17)
v_and_b32 v250, 7, v250
v_mfma_f32_16x16x32_bf16 v[108:111], a[188:191], a[168:171], v[108:111]
v_cmp_gt_u32 vcc, 4, v250
v_mfma_f32_16x16x32_bf16 v[112:115], a[176:179], a[172:175], v[112:115]
buffer_load_dwordx4 v243, s[8:11], s45 offen offset:3072 lds
v_mfma_f32_16x16x32_bf16 v[116:119], a[180:183], a[172:175], v[116:119]
s_add_u32 s45, s45, 131072
s_cmp_lg_u64 vcc, 0
s_cbranch_scc1 .Lspin4
.Lspin4_ret:
v_mfma_f32_16x16x32_bf16 v[120:123], a[184:187], a[172:175], v[120:123]
v_mfma_f32_16x16x32_bf16 v[124:127], a[188:191], a[172:175], v[124:127]
s_waitcnt lgkmcnt(0)
v_mfma_f32_16x16x32_bf16 v[64:67], a[144:147], a[128:131], v[64:67]
s_mov_b32 m0, s49
ds_read_b64_tr_b16 a[176:177], v245 offset:8192
ds_read_b64_tr_b16 a[178:179], v245 offset:10240
v_mfma_f32_16x16x32_bf16 v[68:71], a[148:151], a[128:131], v[68:71]
buffer_load_dwordx4 v7, s[4:7], s44 offen offset:0 sc1 lds
v_mfma_f32_16x16x32_bf16 v[72:75], a[152:155], a[128:131], v[72:75]
ds_read_b128 a[160:163], v244 offset:2048
ds_read_b128 a[164:167], v244 offset:2304
v_mfma_f32_16x16x32_bf16 v[76:79], a[156:159], a[128:131], v[76:79]
ds_read_b64_tr_b16 a[180:181], v246 offset:8192
ds_read_b64_tr_b16 a[182:183], v246 offset:10240
v_mfma_f32_16x16x32_bf16 v[80:83], a[144:147], a[132:135], v[80:83]
buffer_load_dwordx4 v7, s[4:7], s44 offen offset:1024 sc1 lds
v_mfma_f32_16x16x32_bf16 v[84:87], a[148:151], a[132:135], v[84:87]
ds_read_b64_tr_b16 a[184:185], v247 offset:8192
ds_read_b64_tr_b16 a[186:187], v247 offset:10240
v_mfma_f32_16x16x32_bf16 v[88:91], a[152:155], a[132:135], v[88:91]
ds_read_b128 a[168:171], v244 offset:6144
ds_read_b128 a[172:175], v244 offset:6400
v_mfma_f32_16x16x32_bf16 v[92:95], a[156:159], a[132:135], v[92:95]
v_mfma_f32_16x16x32_bf16 v[96:99], a[144:147], a[136:139], v[96:99]
s_cmp_eq_u32 s46, 0
s_cselect_b32 s53, s56, s57
ds_read_b64_tr_b16 a[188:189], v248 offset:8192
ds_read_b64_tr_b16 a[190:191], v248 offset:10240
v_mfma_f32_16x16x32_bf16 v[100:103], a[148:151], a[136:139], v[100:103]
v_mfma_f32_16x16x32_bf16 v[104:107], a[152:155], a[136:139], v[104:107]
s_add_u32 s49, s46, s91
s_add_u32 s46, s46, s53
v_add_u32 v244, s53, v244
v_mfma_f32_16x16x32_bf16 v[108:111], a[156:159], a[136:139], v[108:111]
buffer_load_dwordx4 v7, s[4:7], s44 offen offset:2048 sc1 lds
v_mfma_f32_16x16x32_bf16 v[112:115], a[144:147], a[140:143], v[112:115]
v_add_u32 v245, s53, v245
v_add_u32 v246, s53, v246
v_mfma_f32_16x16x32_bf16 v[116:119], a[148:151], a[140:143], v[116:119]
buffer_load_dwordx4 v7, s[4:7], s44 offen offset:3072 sc1 lds
v_mfma_f32_16x16x32_bf16 v[120:123], a[152:155], a[140:143], v[120:123]
v_add_u32 v247, s53, v247
v_add_u32 v248, s53, v248
v_mfma_f32_16x16x32_bf16 v[124:127], a[156:159], a[140:143], v[124:127]
s_waitcnt vmcnt(25) lgkmcnt(0)
s_barrier
v_mfma_f32_16x16x32_bf16 v[64:67], a[176:179], a[160:163], v[64:67]
s_add_u32 m0, s49, 16384
s_add_u32 s44, s44, 4096
ds_read_b64_tr_b16 a[144:145], v245 offset:0
ds_read_b64_tr_b16 a[146:147], v245 offset:2048
v_mfma_f32_16x16x32_bf16 v[68:71], a[180:183], a[160:163], v[68:71]
buffer_load_dwordx4 v240, s[8:11], s45 offen offset:0 lds
v_mfma_f32_16x16x32_bf16 v[72:75], a[184:187], a[160:163], v[72:75]
ds_read_b128 a[128:131], v244 offset:0
ds_read_b128 a[132:135], v244 offset:256
v_mfma_f32_16x16x32_bf16 v[76:79], a[188:191], a[160:163], v[76:79]
ds_read_b64_tr_b16 a[148:149], v246 offset:0
ds_read_b64_tr_b16 a[150:151], v246 offset:2048
v_mfma_f32_16x16x32_bf16 v[80:83], a[176:179], a[164:167], v[80:83]
buffer_load_dwordx4 v241, s[8:11], s45 offen offset:1024 lds
v_mfma_f32_16x16x32_bf16 v[84:87], a[180:183], a[164:167], v[84:87]
ds_read_b64_tr_b16 a[152:153], v247 offset:0
ds_read_b64_tr_b16 a[154:155], v247 offset:2048
v_mfma_f32_16x16x32_bf16 v[88:91], a[184:187], a[164:167], v[88:91]
ds_read_b128 a[136:139], v244 offset:4096
ds_read_b128 a[140:143], v244 offset:4352
v_mfma_f32_16x16x32_bf16 v[92:95], a[188:191], a[164:167], v[92:95]
v_mfma_f32_16x16x32_bf16 v[96:99], a[176:179], a[168:171], v[96:99]
ds_read_b64_tr_b16 a[156:157], v248 offset:0
ds_read_b64_tr_b16 a[158:159], v248 offset:2048
v_mfma_f32_16x16x32_bf16 v[100:103], a[180:183], a[168:171], v[100:103]
buffer_load_dwordx4 v242, s[8:11], s45 offen offset:2048 lds
v_mfma_f32_16x16x32_bf16 v[104:107], a[184:187], a[168:171], v[104:107]
v_mfma_f32_16x16x32_bf16 v[108:111], a[188:191], a[168:171], v[108:111]
v_mfma_f32_16x16x32_bf16 v[112:115], a[176:179], a[172:175], v[112:115]
buffer_load_dwordx4 v243, s[8:11], s45 offen offset:3072 lds
v_mfma_f32_16x16x32_bf16 v[116:119], a[180:183], a[172:175], v[116:119]
s_add_u32 s45, s45, 131072
v_mfma_f32_16x16x32_bf16 v[120:123], a[184:187], a[172:175], v[120:123]
v_mfma_f32_16x16x32_bf16 v[124:127], a[188:191], a[172:175], v[124:127]
s_waitcnt lgkmcnt(0)
v_mfma_f32_16x16x32_bf16 v[64:67], a[144:147], a[128:131], v[64:67]
s_mov_b32 m0, s49
ds_read_b64_tr_b16 a[176:177], v245 offset:8192
ds_read_b64_tr_b16 a[178:179], v245 offset:10240
v_mfma_f32_16x16x32_bf16 v[68:71], a[148:151], a[128:131], v[68:71]
buffer_load_dwordx4 v7, s[4:7], s44 offen offset:0 sc1 lds
v_mfma_f32_16x16x32_bf16 v[72:75], a[152:155], a[128:131], v[72:75]
ds_read_b128 a[160:163], v244 offset:2048
ds_read_b128 a[164:167], v244 offset:2304
v_mfma_f32_16x16x32_bf16 v[76:79], a[156:159], a[128:131], v[76:79]
ds_read_b64_tr_b16 a[180:181], v246 offset:8192
ds_read_b64_tr_b16 a[182:183], v246 offset:10240
v_mfma_f32_16x16x32_bf16 v[80:83], a[144:147], a[132:135], v[80:83]
buffer_load_dwordx4 v7, s[4:7], s44 offen offset:1024 sc1 lds
v_mfma_f32_16x16x32_bf16 v[84:87], a[148:151], a[132:135], v[84:87]
ds_read_b64_tr_b16 a[184:185], v247 offset:8192
ds_read_b64_tr_b16 a[186:187], v247 offset:10240
v_mfma_f32_16x16x32_bf16 v[88:91], a[152:155], a[132:135], v[88:91]
ds_read_b128 a[168:171], v244 offset:6144
ds_read_b128 a[172:175], v244 offset:6400
v_mfma_f32_16x16x32_bf16 v[92:95], a[156:159], a[132:135], v[92:95]
v_mfma_f32_16x16x32_bf16 v[96:99], a[144:147], a[136:139], v[96:99]
s_cmp_eq_u32 s46, 0
s_cselect_b32 s53, s56, s57
ds_read_b64_tr_b16 a[188:189], v248 offset:8192
ds_read_b64_tr_b16 a[190:191], v248 offset:10240
v_mfma_f32_16x16x32_bf16 v[100:103], a[148:151], a[136:139], v[100:103]
v_mfma_f32_16x16x32_bf16 v[104:107], a[152:155], a[136:139], v[104:107]
s_add_u32 s49, s46, s91
s_add_u32 s46, s46, s53
v_add_u32 v244, s53, v244
v_mfma_f32_16x16x32_bf16 v[108:111], a[156:159], a[136:139], v[108:111]
buffer_load_dwordx4 v7, s[4:7], s44 offen offset:2048 sc1 lds
v_mfma_f32_16x16x32_bf16 v[112:115], a[144:147], a[140:143], v[112:115]
v_add_u32 v245, s53, v245
v_add_u32 v246, s53, v246
v_mfma_f32_16x16x32_bf16 v[116:119], a[148:151], a[140:143], v[116:119]
buffer_load_dwordx4 v7, s[4:7], s44 offen offset:3072 sc1 lds
v_mfma_f32_16x16x32_bf16 v[120:123], a[152:155], a[140:143], v[120:123]
v_add_u32 v247, s53, v247
v_add_u32 v248, s53, v248
v_mfma_f32_16x16x32_bf16 v[124:127], a[156:159], a[140:143], v[124:127]
s_waitcnt vmcnt(24) lgkmcnt(0)
s_barrier
v_mfma_f32_16x16x32_bf16 v[64:67], a[176:179], a[160:163], v[64:67]
s_add_u32 m0, s49, 16384
s_add_u32 s44, s44, 4096
ds_read_b64_tr_b16 a[144:145], v245 offset:0
ds_read_b64_tr_b16 a[146:147], v245 offset:2048
v_mfma_f32_16x16x32_bf16 v[68:71], a[180:183], a[160:163], v[68:71]
buffer_load_dwordx4 v240, s[8:11], s45 offen offset:0 lds
v_mfma_f32_16x16x32_bf16 v[72:75], a[184:187], a[160:163], v[72:75]
ds_read_b128 a[128:131], v244 offset:0
ds_read_b128 a[132:135], v244 offset:256
v_mfma_f32_16x16x32_bf16 v[76:79], a[188:191], a[160:163], v[76:79]
ds_read_b64_tr_b16 a[148:149], v246 offset:0
ds_read_b64_tr_b16 a[150:151], v246 offset:2048
v_mfma_f32_16x16x32_bf16 v[80:83], a[176:179], a[164:167], v[80:83]
buffer_load_dwordx4 v241, s[8:11], s45 offen offset:1024 lds
v_mfma_f32_16x16x32_bf16 v[84:87], a[180:183], a[164:167], v[84:87]
ds_read_b64_tr_b16 a[152:153], v247 offset:0
ds_read_b64_tr_b16 a[154:155], v247 offset:2048
v_mfma_f32_16x16x32_bf16 v[88:91], a[184:187], a[164:167], v[88:91]
ds_read_b128 a[136:139], v244 offset:4096
ds_read_b128 a[140:143], v244 offset:4352
v_mfma_f32_16x16x32_bf16 v[92:95], a[188:191], a[164:167], v[92:95]
v_mfma_f32_16x16x32_bf16 v[96:99], a[176:179], a[168:171], v[96:99]
ds_read_b64_tr_b16 a[156:157], v248 offset:0
ds_read_b64_tr_b16 a[158:159], v248 offset:2048
v_mfma_f32_16x16x32_bf16 v[100:103], a[180:183], a[168:171], v[100:103]
buffer_load_dwordx4 v242, s[8:11], s45 offen offset:2048 lds
v_mfma_f32_16x16x32_bf16 v[104:107], a[184:187], a[168:171], v[104:107]
v_mfma_f32_16x16x32_bf16 v[108:111], a[188:191], a[168:171], v[108:111]
v_mfma_f32_16x16x32_bf16 v[112:115], a[176:179], a[172:175], v[112:115]
buffer_load_dwordx4 v243, s[8:11], s45 offen offset:3072 lds
v_mfma_f32_16x16x32_bf16 v[116:119], a[180:183], a[172:175], v[116:119]
s_add_u32 s45, s45, 131072
v_mfma_f32_16x16x32_bf16 v[120:123], a[184:187], a[172:175], v[120:123]
v_mfma_f32_16x16x32_bf16 v[124:127], a[188:191], a[172:175], v[124:127]
s_waitcnt lgkmcnt(0)
v_mfma_f32_16x16x32_bf16 v[64:67], a[144:147], a[128:131], v[64:67]
s_mov_b32 m0, s49
ds_read_b64_tr_b16 a[176:177], v245 offset:8192
ds_read_b64_tr_b16 a[178:179], v245 offset:10240
v_mfma_f32_16x16x32_bf16 v[68:71], a[148:151], a[128:131], v[68:71]
buffer_load_dwordx4 v7, s[4:7], s44 offen offset:0 sc1 lds
v_mfma_f32_16x16x32_bf16 v[72:75], a[152:155], a[128:131], v[72:75]
ds_read_b128 a[160:163], v244 offset:2048
ds_read_b128 a[164:167], v244 offset:2304
v_mfma_f32_16x16x32_bf16 v[76:79], a[156:159], a[128:131], v[76:79]
ds_read_b64_tr_b16 a[180:181], v246 offset:8192
ds_read_b64_tr_b16 a[182:183], v246 offset:10240
v_mfma_f32_16x16x32_bf16 v[80:83], a[144:147], a[132:135], v[80:83]
buffer_load_dwordx4 v7, s[4:7], s44 offen offset:1024 sc1 lds
v_mfma_f32_16x16x32_bf16 v[84:87], a[148:151], a[132:135], v[84:87]
ds_read_b64_tr_b16 a[184:185], v247 offset:8192
ds_read_b64_tr_b16 a[186:187], v247 offset:10240
v_mfma_f32_16x16x32_bf16 v[88:91], a[152:155], a[132:135], v[88:91]
ds_read_b128 a[168:171], v244 offset:6144
ds_read_b128 a[172:175], v244 offset:6400
v_mfma_f32_16x16x32_bf16 v[92:95], a[156:159], a[132:135], v[92:95]
v_mfma_f32_16x16x32_bf16 v[96:99], a[144:147], a[136:139], v[96:99]
s_cmp_eq_u32 s46, 0
s_cselect_b32 s53, s56, s57
ds_read_b64_tr_b16 a[188:189], v248 offset:8192
ds_read_b64_tr_b16 a[190:191], v248 offset:10240
v_mfma_f32_16x16x32_bf16 v[100:103], a[148:151], a[136:139], v[100:103]
v_mfma_f32_16x16x32_bf16 v[104:107], a[152:155], a[136:139], v[104:107]
s_add_u32 s49, s46, s91
s_add_u32 s46, s46, s53
v_add_u32 v244, s53, v244
v_mfma_f32_16x16x32_bf16 v[108:111], a[156:159], a[136:139], v[108:111]
buffer_load_dwordx4 v7, s[4:7], s44 offen offset:2048 sc1 lds
v_mfma_f32_16x16x32_bf16 v[112:115], a[144:147], a[140:143], v[112:115]
v_add_u32 v245, s53, v245
v_add_u32 v246, s53, v246
v_mfma_f32_16x16x32_bf16 v[116:119], a[148:151], a[140:143], v[116:119]
buffer_load_dwordx4 v7, s[4:7], s44 offen offset:3072 sc1 lds
v_mfma_f32_16x16x32_bf16 v[120:123], a[152:155], a[140:143], v[120:123]
v_add_u32 v247, s53, v247
v_add_u32 v248, s53, v248
v_mfma_f32_16x16x32_bf16 v[124:127], a[156:159], a[140:143], v[124:127]
s_waitcnt vmcnt(24) lgkmcnt(0)
s_barrier
v_mfma_f32_16x16x32_bf16 v[64:67], a[176:179], a[160:163], v[64:67]
s_add_u32 m0, s49, 16384
s_add_u32 s44, s44, 4096
ds_read_b64_tr_b16 a[144:145], v245 offset:0
ds_read_b64_tr_b16 a[146:147], v245 offset:2048
v_mfma_f32_16x16x32_bf16 v[68:71], a[180:183], a[160:163], v[68:71]
buffer_load_dwordx4 v240, s[8:11], s45 offen offset:0 lds
v_mfma_f32_16x16x32_bf16 v[72:75], a[184:187], a[160:163], v[72:75]
ds_read_b128 a[128:131], v244 offset:0
ds_read_b128 a[132:135], v244 offset:256
v_mfma_f32_16x16x32_bf16 v[76:79], a[188:191], a[160:163], v[76:79]
ds_read_b64_tr_b16 a[148:149], v246 offset:0
ds_read_b64_tr_b16 a[150:151], v246 offset:2048
v_mfma_f32_16x16x32_bf16 v[80:83], a[176:179], a[164:167], v[80:83]
buffer_load_dwordx4 v241, s[8:11], s45 offen offset:1024 lds
v_mfma_f32_16x16x32_bf16 v[84:87], a[180:183], a[164:167], v[84:87]
ds_read_b64_tr_b16 a[152:153], v247 offset:0
ds_read_b64_tr_b16 a[154:155], v247 offset:2048
v_mfma_f32_16x16x32_bf16 v[88:91], a[184:187], a[164:167], v[88:91]
ds_read_b128 a[136:139], v244 offset:4096
ds_read_b128 a[140:143], v244 offset:4352
v_mfma_f32_16x16x32_bf16 v[92:95], a[188:191], a[164:167], v[92:95]
v_mfma_f32_16x16x32_bf16 v[96:99], a[176:179], a[168:171], v[96:99]
ds_read_b64_tr_b16 a[156:157], v248 offset:0
ds_read_b64_tr_b16 a[158:159], v248 offset:2048
v_mfma_f32_16x16x32_bf16 v[100:103], a[180:183], a[168:171], v[100:103]
buffer_load_dwordx4 v242, s[8:11], s45 offen offset:2048 lds
v_mfma_f32_16x16x32_bf16 v[104:107], a[184:187], a[168:171], v[104:107]
v_mfma_f32_16x16x32_bf16 v[108:111], a[188:191], a[168:171], v[108:111]
v_mfma_f32_16x16x32_bf16 v[112:115], a[176:179], a[172:175], v[112:115]
buffer_load_dwordx4 v243, s[8:11], s45 offen offset:3072 lds
v_mfma_f32_16x16x32_bf16 v[116:119], a[180:183], a[172:175], v[116:119]
s_add_u32 s45, s45, 131072
v_mfma_f32_16x16x32_bf16 v[120:123], a[184:187], a[172:175], v[120:123]
v_mfma_f32_16x16x32_bf16 v[124:127], a[188:191], a[172:175], v[124:127]
s_waitcnt lgkmcnt(0)
v_mfma_f32_16x16x32_bf16 v[64:67], a[144:147], a[128:131], v[64:67]
s_mov_b32 m0, s49
ds_read_b64_tr_b16 a[176:177], v245 offset:8192
ds_read_b64_tr_b16 a[178:179], v245 offset:10240
v_mfma_f32_16x16x32_bf16 v[68:71], a[148:151], a[128:131], v[68:71]
buffer_load_dwordx4 v7, s[4:7], s44 offen offset:0 sc1 lds
v_mfma_f32_16x16x32_bf16 v[72:75], a[152:155], a[128:131], v[72:75]
ds_read_b128 a[160:163], v244 offset:2048
ds_read_b128 a[164:167], v244 offset:2304
v_mfma_f32_16x16x32_bf16 v[76:79], a[156:159], a[128:131], v[76:79]
ds_read_b64_tr_b16 a[180:181], v246 offset:8192
ds_read_b64_tr_b16 a[182:183], v246 offset:10240
v_mfma_f32_16x16x32_bf16 v[80:83], a[144:147], a[132:135], v[80:83]
buffer_load_dwordx4 v7, s[4:7], s44 offen offset:1024 sc1 lds
v_mfma_f32_16x16x32_bf16 v[84:87], a[148:151], a[132:135], v[84:87]
ds_read_b64_tr_b16 a[184:185], v247 offset:8192
ds_read_b64_tr_b16 a[186:187], v247 offset:10240
v_mfma_f32_16x16x32_bf16 v[88:91], a[152:155], a[132:135], v[88:91]
ds_read_b128 a[168:171], v244 offset:6144
ds_read_b128 a[172:175], v244 offset:6400
v_mfma_f32_16x16x32_bf16 v[92:95], a[156:159], a[132:135], v[92:95]
v_mfma_f32_16x16x32_bf16 v[96:99], a[144:147], a[136:139], v[96:99]
s_cmp_eq_u32 s46, 0
s_cselect_b32 s53, s56, s57
ds_read_b64_tr_b16 a[188:189], v248 offset:8192
ds_read_b64_tr_b16 a[190:191], v248 offset:10240
v_mfma_f32_16x16x32_bf16 v[100:103], a[148:151], a[136:139], v[100:103]
v_mfma_f32_16x16x32_bf16 v[104:107], a[152:155], a[136:139], v[104:107]
s_add_u32 s49, s46, s91
s_add_u32 s46, s46, s53
v_add_u32 v244, s53, v244
v_mfma_f32_16x16x32_bf16 v[108:111], a[156:159], a[136:139], v[108:111]
buffer_load_dwordx4 v7, s[4:7], s44 offen offset:2048 sc1 lds
v_mfma_f32_16x16x32_bf16 v[112:115], a[144:147], a[140:143], v[112:115]
v_add_u32 v245, s53, v245
v_add_u32 v246, s53, v246
v_mfma_f32_16x16x32_bf16 v[116:119], a[148:151], a[140:143], v[116:119]
buffer_load_dwordx4 v7, s[4:7], s44 offen offset:3072 sc1 lds
v_mfma_f32_16x16x32_bf16 v[120:123], a[152:155], a[140:143], v[120:123]
v_add_u32 v247, s53, v247
v_add_u32 v248, s53, v248
v_mfma_f32_16x16x32_bf16 v[124:127], a[156:159], a[140:143], v[124:127]
s_waitcnt vmcnt(24) lgkmcnt(0)
s_barrier
v_mfma_f32_16x16x32_bf16 v[64:67], a[176:179], a[160:163], v[64:67]
s_add_u32 m0, s49, 16384
s_add_u32 s44, s44, 4096
ds_read_b64_tr_b16 a[144:145], v245 offset:0
ds_read_b64_tr_b16 a[146:147], v245 offset:2048
v_mfma_f32_16x16x32_bf16 v[68:71], a[180:183], a[160:163], v[68:71]
buffer_load_dwordx4 v240, s[8:11], s45 offen offset:0 lds
v_mfma_f32_16x16x32_bf16 v[72:75], a[184:187], a[160:163], v[72:75]
ds_read_b128 a[128:131], v244 offset:0
ds_read_b128 a[132:135], v244 offset:256
v_mfma_f32_16x16x32_bf16 v[76:79], a[188:191], a[160:163], v[76:79]
ds_read_b64_tr_b16 a[148:149], v246 offset:0
ds_read_b64_tr_b16 a[150:151], v246 offset:2048
v_mfma_f32_16x16x32_bf16 v[80:83], a[176:179], a[164:167], v[80:83]
buffer_load_dwordx4 v241, s[8:11], s45 offen offset:1024 lds
v_mfma_f32_16x16x32_bf16 v[84:87], a[180:183], a[164:167], v[84:87]
ds_read_b64_tr_b16 a[152:153], v247 offset:0
ds_read_b64_tr_b16 a[154:155], v247 offset:2048
v_mfma_f32_16x16x32_bf16 v[88:91], a[184:187], a[164:167], v[88:91]
ds_read_b128 a[136:139], v244 offset:4096
ds_read_b128 a[140:143], v244 offset:4352
v_mfma_f32_16x16x32_bf16 v[92:95], a[188:191], a[164:167], v[92:95]
v_mfma_f32_16x16x32_bf16 v[96:99], a[176:179], a[168:171], v[96:99]
ds_read_b64_tr_b16 a[156:157], v248 offset:0
ds_read_b64_tr_b16 a[158:159], v248 offset:2048
v_mfma_f32_16x16x32_bf16 v[100:103], a[180:183], a[168:171], v[100:103]
buffer_load_dwordx4 v242, s[8:11], s45 offen offset:2048 lds
v_mfma_f32_16x16x32_bf16 v[104:107], a[184:187], a[168:171], v[104:107]
v_mfma_f32_16x16x32_bf16 v[108:111], a[188:191], a[168:171], v[108:111]
v_mfma_f32_16x16x32_bf16 v[112:115], a[176:179], a[172:175], v[112:115]
buffer_load_dwordx4 v243, s[8:11], s45 offen offset:3072 lds
v_mfma_f32_16x16x32_bf16 v[116:119], a[180:183], a[172:175], v[116:119]
s_add_u32 s45, s45, 131072
v_mfma_f32_16x16x32_bf16 v[120:123], a[184:187], a[172:175], v[120:123]
v_mfma_f32_16x16x32_bf16 v[124:127], a[188:191], a[172:175], v[124:127]
s_waitcnt lgkmcnt(0)
v_mfma_f32_16x16x32_bf16 v[128:131], a[144:147], a[128:131], 0
s_mov_b32 m0, s49
ds_read_b64_tr_b16 a[176:177], v245 offset:8192
ds_read_b64_tr_b16 a[178:179], v245 offset:10240
v_mfma_f32_16x16x32_bf16 v[132:135], a[148:151], a[128:131], 0
buffer_load_dwordx4 v7, s[4:7], s44 offen offset:0 sc1 lds
v_mfma_f32_16x16x32_bf16 v[136:139], a[152:155], a[128:131], 0
ds_read_b128 a[160:163], v244 offset:2048
ds_read_b128 a[164:167], v244 offset:2304
v_mfma_f32_16x16x32_bf16 v[140:143], a[156:159], a[128:131], 0
ds_read_b64_tr_b16 a[180:181], v246 offset:8192
ds_read_b64_tr_b16 a[182:183], v246 offset:10240
v_mfma_f32_16x16x32_bf16 v[144:147], a[144:147], a[132:135], 0
buffer_load_dwordx4 v7, s[4:7], s44 offen offset:1024 sc1 lds
v_mfma_f32_16x16x32_bf16 v[148:151], a[148:151], a[132:135], 0
ds_read_b64_tr_b16 a[184:185], v247 offset:8192
ds_read_b64_tr_b16 a[186:187], v247 offset:10240
v_mfma_f32_16x16x32_bf16 v[152:155], a[152:155], a[132:135], 0
ds_read_b128 a[168:171], v244 offset:6144
ds_read_b128 a[172:175], v244 offset:6400
v_mfma_f32_16x16x32_bf16 v[156:159], a[156:159], a[132:135], 0
v_mfma_f32_16x16x32_bf16 v[160:163], a[144:147], a[136:139], 0
s_cmp_eq_u32 s46, 0
s_cselect_b32 s53, s56, s57
ds_read_b64_tr_b16 a[188:189], v248 offset:8192
ds_read_b64_tr_b16 a[190:191], v248 offset:10240
v_mfma_f32_16x16x32_bf16 v[164:167], a[148:151], a[136:139], 0
v_mfma_f32_16x16x32_bf16 v[168:171], a[152:155], a[136:139], 0
s_add_u32 s49, s46, s91
s_add_u32 s46, s46, s53
v_add_u32 v244, s53, v244
v_mfma_f32_16x16x32_bf16 v[172:175], a[156:159], a[136:139], 0
buffer_load_dwordx4 v7, s[4:7], s44 offen offset:2048 sc1 lds
v_mfma_f32_16x16x32_bf16 v[176:179], a[144:147], a[140:143], 0
v_add_u32 v245, s53, v245
v_add_u32 v246, s53, v246
v_mfma_f32_16x16x32_bf16 v[180:183], a[148:151], a[140:143], 0
buffer_load_dwordx4 v7, s[4:7], s44 offen offset:3072 sc1 lds
v_mfma_f32_16x16x32_bf16 v[184:187], a[152:155], a[140:143], 0
v_add_u32 v247, s53, v247
v_add_u32 v248, s53, v248
v_mfma_f32_16x16x32_bf16 v[188:191], a[156:159], a[140:143], 0
s_waitcnt vmcnt(24) lgkmcnt(0)
s_barrier
s_mov_b32 s28, 0
s_mov_b32 s29, 4096
s_mov_b32 s30, 8192
s_mov_b32 s31, 12288
v_mfma_f32_16x16x32_bf16 v[128:131], a[176:179], a[160:163], v[128:131]
s_add_u32 m0, s49, 16384
s_add_u32 s44, s44, 4096
ds_read_b64_tr_b16 a[144:145], v245 offset:0
ds_read_b64_tr_b16 a[146:147], v245 offset:2048
v_mfma_f32_16x16x32_bf16 v[132:135], a[180:183], a[160:163], v[132:135]
buffer_load_dwordx4 v240, s[8:11], s45 offen offset:0 lds
v_mfma_f32_16x16x32_bf16 v[136:139], a[184:187], a[160:163], v[136:139]
ds_read_b128 a[128:131], v244 offset:0
ds_read_b128 a[132:135], v244 offset:256
v_mfma_f32_16x16x32_bf16 v[140:143], a[188:191], a[160:163], v[140:143]
ds_read_b64_tr_b16 a[148:149], v246 offset:0
ds_read_b64_tr_b16 a[150:151], v246 offset:2048
v_mfma_f32_16x16x32_bf16 v[144:147], a[176:179], a[164:167], v[144:147]
buffer_load_dwordx4 v241, s[8:11], s45 offen offset:1024 lds
v_mfma_f32_16x16x32_bf16 v[148:151], a[180:183], a[164:167], v[148:151]
ds_read_b64_tr_b16 a[152:153], v247 offset:0
ds_read_b64_tr_b16 a[154:155], v247 offset:2048
v_mfma_f32_16x16x32_bf16 v[152:155], a[184:187], a[164:167], v[152:155]
ds_read_b128 a[136:139], v244 offset:4096
ds_read_b128 a[140:143], v244 offset:4352
v_mfma_f32_16x16x32_bf16 v[156:159], a[188:191], a[164:167], v[156:159]
buffer_store_dwordx4 v[64:67], v7, s[12:15], s28 offen offset:0 sc1
v_mfma_f32_16x16x32_bf16 v[160:163], a[176:179], a[168:171], v[160:163]
ds_read_b64_tr_b16 a[156:157], v248 offset:0
ds_read_b64_tr_b16 a[158:159], v248 offset:2048
v_mfma_f32_16x16x32_bf16 v[164:167], a[180:183], a[168:171], v[164:167]
buffer_load_dwordx4 v242, s[8:11], s45 offen offset:2048 lds
v_mfma_f32_16x16x32_bf16 v[168:171], a[184:187], a[168:171], v[168:171]
v_mfma_f32_16x16x32_bf16 v[172:175], a[188:191], a[168:171], v[172:175]
v_mfma_f32_16x16x32_bf16 v[176:179], a[176:179], a[172:175], v[176:179]
buffer_load_dwordx4 v243, s[8:11], s45 offen offset:3072 lds
v_mfma_f32_16x16x32_bf16 v[180:183], a[180:183], a[172:175], v[180:183]
s_add_u32 s45, s45, 131072
v_mfma_f32_16x16x32_bf16 v[184:187], a[184:187], a[172:175], v[184:187]
buffer_store_dwordx4 v[72:75], v7, s[12:15], s28 offen offset:2048 sc1
v_mfma_f32_16x16x32_bf16 v[188:191], a[188:191], a[172:175], v[188:191]
s_waitcnt lgkmcnt(0)
v_mfma_f32_16x16x32_bf16 v[128:131], a[144:147], a[128:131], v[128:131]
s_mov_b32 m0, s49
ds_read_b64_tr_b16 a[176:177], v245 offset:8192
ds_read_b64_tr_b16 a[178:179], v245 offset:10240
v_mfma_f32_16x16x32_bf16 v[132:135], a[148:151], a[128:131], v[132:135]
buffer_load_dwordx4 v7, s[4:7], s44 offen offset:0 sc1 lds
v_mfma_f32_16x16x32_bf16 v[136:139], a[152:155], a[128:131], v[136:139]
ds_read_b128 a[160:163], v244 offset:2048
ds_read_b128 a[164:167], v244 offset:2304
v_mfma_f32_16x16x32_bf16 v[140:143], a[156:159], a[128:131], v[140:143]
ds_read_b64_tr_b16 a[180:181], v246 offset:8192
ds_read_b64_tr_b16 a[182:183], v246 offset:10240
v_mfma_f32_16x16x32_bf16 v[144:147], a[144:147], a[132:135], v[144:147]
buffer_load_dwordx4 v7, s[4:7], s44 offen offset:1024 sc1 lds
v_mfma_f32_16x16x32_bf16 v[148:151], a[148:151], a[132:135], v[148:151]
ds_read_b64_tr_b16 a[184:185], v247 offset:8192
ds_read_b64_tr_b16 a[186:187], v247 offset:10240
v_mfma_f32_16x16x32_bf16 v[152:155], a[152:155], a[132:135], v[152:155]
ds_read_b128 a[168:171], v244 offset:6144
ds_read_b128 a[172:175], v244 offset:6400
v_mfma_f32_16x16x32_bf16 v[156:159], a[156:159], a[132:135], v[156:159]
buffer_store_dwordx4 v[68:71], v7, s[12:15], s28 offen offset:1024 sc1
v_mfma_f32_16x16x32_bf16 v[160:163], a[144:147], a[136:139], v[160:163]
s_cmp_eq_u32 s46, 0
s_cselect_b32 s53, s56, s57
ds_read_b64_tr_b16 a[188:189], v248 offset:8192
ds_read_b64_tr_b16 a[190:191], v248 offset:10240
v_mfma_f32_16x16x32_bf16 v[164:167], a[148:151], a[136:139], v[164:167]
v_mfma_f32_16x16x32_bf16 v[168:171], a[152:155], a[136:139], v[168:171]
s_add_u32 s49, s46, s91
s_add_u32 s46, s46, s53
v_add_u32 v244, s53, v244
v_mfma_f32_16x16x32_bf16 v[172:175], a[156:159], a[136:139], v[172:175]
buffer_load_dwordx4 v7, s[4:7], s44 offen offset:2048 sc1 lds
v_mfma_f32_16x16x32_bf16 v[176:179], a[144:147], a[140:143], v[176:179]
v_add_u32 v245, s53, v245
v_add_u32 v246, s53, v246
v_mfma_f32_16x16x32_bf16 v[180:183], a[148:151], a[140:143], v[180:183]
buffer_load_dwordx4 v7, s[4:7], s44 offen offset:3072 sc1 lds
v_mfma_f32_16x16x32_bf16 v[184:187], a[152:155], a[140:143], v[184:187]
buffer_store_dwordx4 v[76:79], v7, s[12:15], s28 offen offset:3072 sc1
v_add_u32 v247, s53, v247
v_add_u32 v248, s53, v248
v_mfma_f32_16x16x32_bf16 v[188:191], a[156:159], a[140:143], v[188:191]
s_waitcnt vmcnt(28) lgkmcnt(0)
s_barrier
v_mfma_f32_16x16x32_bf16 v[128:131], a[176:179], a[160:163], v[128:131]
s_add_u32 m0, s49, 16384
s_add_u32 s44, s44, 4096
ds_read_b64_tr_b16 a[144:145], v245 offset:0
ds_read_b64_tr_b16 a[146:147], v245 offset:2048
v_mfma_f32_16x16x32_bf16 v[132:135], a[180:183], a[160:163], v[132:135]
buffer_load_dwordx4 v240, s[8:11], s45 offen offset:0 lds
v_mfma_f32_16x16x32_bf16 v[136:139], a[184:187], a[160:163], v[136:139]
ds_read_b128 a[128:131], v244 offset:0
ds_read_b128 a[132:135], v244 offset:256
v_mfma_f32_16x16x32_bf16 v[140:143], a[188:191], a[160:163], v[140:143]
ds_read_b64_tr_b16 a[148:149], v246 offset:0
ds_read_b64_tr_b16 a[150:151], v246 offset:2048
v_mfma_f32_16x16x32_bf16 v[144:147], a[176:179], a[164:167], v[144:147]
buffer_load_dwordx4 v241, s[8:11], s45 offen offset:1024 lds
v_mfma_f32_16x16x32_bf16 v[148:151], a[180:183], a[164:167], v[148:151]
ds_read_b64_tr_b16 a[152:153], v247 offset:0
ds_read_b64_tr_b16 a[154:155], v247 offset:2048
v_mfma_f32_16x16x32_bf16 v[152:155], a[184:187], a[164:167], v[152:155]
ds_read_b128 a[136:139], v244 offset:4096
ds_read_b128 a[140:143], v244 offset:4352
v_mfma_f32_16x16x32_bf16 v[156:159], a[188:191], a[164:167], v[156:159]
buffer_store_dwordx4 v[80:83], v7, s[12:15], s29 offen offset:0 sc1
v_mfma_f32_16x16x32_bf16 v[160:163], a[176:179], a[168:171], v[160:163]
ds_read_b64_tr_b16 a[156:157], v248 offset:0
ds_read_b64_tr_b16 a[158:159], v248 offset:2048
v_mfma_f32_16x16x32_bf16 v[164:167], a[180:183], a[168:171], v[164:167]
buffer_load_dwordx4 v242, s[8:11], s45 offen offset:2048 lds
v_mfma_f32_16x16x32_bf16 v[168:171], a[184:187], a[168:171], v[168:171]
v_mfma_f32_16x16x32_bf16 v[172:175], a[188:191], a[168:171], v[172:175]
v_mfma_f32_16x16x32_bf16 v[176:179], a[176:179], a[172:175], v[176:179]
buffer_load_dwordx4 v243, s[8:11], s45 offen offset:3072 lds
v_mfma_f32_16x16x32_bf16 v[180:183], a[180:183], a[172:175], v[180:183]
s_add_u32 s45, s45, 131072
v_mfma_f32_16x16x32_bf16 v[184:187], a[184:187], a[172:175], v[184:187]
buffer_store_dwordx4 v[88:91], v7, s[12:15], s29 offen offset:2048 sc1
v_mfma_f32_16x16x32_bf16 v[188:191], a[188:191], a[172:175], v[188:191]
s_waitcnt lgkmcnt(0)
v_mfma_f32_16x16x32_bf16 v[128:131], a[144:147], a[128:131], v[128:131]
s_mov_b32 m0, s49
ds_read_b64_tr_b16 a[176:177], v245 offset:8192
ds_read_b64_tr_b16 a[178:179], v245 offset:10240
v_mfma_f32_16x16x32_bf16 v[132:135], a[148:151], a[128:131], v[132:135]
buffer_load_dwordx4 v7, s[4:7], s44 offen offset:0 sc1 lds
v_mfma_f32_16x16x32_bf16 v[136:139], a[152:155], a[128:131], v[136:139]
ds_read_b128 a[160:163], v244 offset:2048
ds_read_b128 a[164:167], v244 offset:2304
v_mfma_f32_16x16x32_bf16 v[140:143], a[156:159], a[128:131], v[140:143]
ds_read_b64_tr_b16 a[180:181], v246 offset:8192
ds_read_b64_tr_b16 a[182:183], v246 offset:10240
v_mfma_f32_16x16x32_bf16 v[144:147], a[144:147], a[132:135], v[144:147]
buffer_load_dwordx4 v7, s[4:7], s44 offen offset:1024 sc1 lds
v_mfma_f32_16x16x32_bf16 v[148:151], a[148:151], a[132:135], v[148:151]
ds_read_b64_tr_b16 a[184:185], v247 offset:8192
ds_read_b64_tr_b16 a[186:187], v247 offset:10240
v_mfma_f32_16x16x32_bf16 v[152:155], a[152:155], a[132:135], v[152:155]
ds_read_b128 a[168:171], v244 offset:6144
ds_read_b128 a[172:175], v244 offset:6400
v_mfma_f32_16x16x32_bf16 v[156:159], a[156:159], a[132:135], v[156:159]
buffer_store_dwordx4 v[84:87], v7, s[12:15], s29 offen offset:1024 sc1
v_mfma_f32_16x16x32_bf16 v[160:163], a[144:147], a[136:139], v[160:163]
s_cmp_eq_u32 s46, 0
s_cselect_b32 s53, s56, s57
ds_read_b64_tr_b16 a[188:189], v248 offset:8192
ds_read_b64_tr_b16 a[190:191], v248 offset:10240
v_mfma_f32_16x16x32_bf16 v[164:167], a[148:151], a[136:139], v[164:167]
v_mfma_f32_16x16x32_bf16 v[168:171], a[152:155], a[136:139], v[168:171]
s_add_u32 s49, s46, s91
s_add_u32 s46, s46, s53
v_add_u32 v244, s53, v244
v_mfma_f32_16x16x32_bf16 v[172:175], a[156:159], a[136:139], v[172:175]
buffer_load_dwordx4 v7, s[4:7], s44 offen offset:2048 sc1 lds
v_mfma_f32_16x16x32_bf16 v[176:179], a[144:147], a[140:143], v[176:179]
v_add_u32 v245, s53, v245
v_add_u32 v246, s53, v246
v_mfma_f32_16x16x32_bf16 v[180:183], a[148:151], a[140:143], v[180:183]
buffer_load_dwordx4 v7, s[4:7], s44 offen offset:3072 sc1 lds
v_mfma_f32_16x16x32_bf16 v[184:187], a[152:155], a[140:143], v[184:187]
buffer_store_dwordx4 v[92:95], v7, s[12:15], s29 offen offset:3072 sc1
v_add_u32 v247, s53, v247
v_add_u32 v248, s53, v248
v_mfma_f32_16x16x32_bf16 v[188:191], a[156:159], a[140:143], v[188:191]
s_waitcnt vmcnt(32) lgkmcnt(0)
s_barrier
v_mfma_f32_16x16x32_bf16 v[128:131], a[176:179], a[160:163], v[128:131]
s_add_u32 m0, s49, 16384
s_add_u32 s44, s44, 4096
ds_read_b64_tr_b16 a[144:145], v245 offset:0
ds_read_b64_tr_b16 a[146:147], v245 offset:2048
v_mfma_f32_16x16x32_bf16 v[132:135], a[180:183], a[160:163], v[132:135]
buffer_load_dwordx4 v240, s[8:11], s45 offen offset:0 lds
v_mfma_f32_16x16x32_bf16 v[136:139], a[184:187], a[160:163], v[136:139]
ds_read_b128 a[128:131], v244 offset:0
ds_read_b128 a[132:135], v244 offset:256
v_mfma_f32_16x16x32_bf16 v[140:143], a[188:191], a[160:163], v[140:143]
ds_read_b64_tr_b16 a[148:149], v246 offset:0
ds_read_b64_tr_b16 a[150:151], v246 offset:2048
v_mfma_f32_16x16x32_bf16 v[144:147], a[176:179], a[164:167], v[144:147]
buffer_load_dwordx4 v241, s[8:11], s45 offen offset:1024 lds
v_mfma_f32_16x16x32_bf16 v[148:151], a[180:183], a[164:167], v[148:151]
ds_read_b64_tr_b16 a[152:153], v247 offset:0
ds_read_b64_tr_b16 a[154:155], v247 offset:2048
v_mfma_f32_16x16x32_bf16 v[152:155], a[184:187], a[164:167], v[152:155]
ds_read_b128 a[136:139], v244 offset:4096
ds_read_b128 a[140:143], v244 offset:4352
v_mfma_f32_16x16x32_bf16 v[156:159], a[188:191], a[164:167], v[156:159]
buffer_store_dwordx4 v[96:99], v7, s[12:15], s30 offen offset:0 sc1
v_mfma_f32_16x16x32_bf16 v[160:163], a[176:179], a[168:171], v[160:163]
ds_read_b64_tr_b16 a[156:157], v248 offset:0
ds_read_b64_tr_b16 a[158:159], v248 offset:2048
v_mfma_f32_16x16x32_bf16 v[164:167], a[180:183], a[168:171], v[164:167]
buffer_load_dwordx4 v242, s[8:11], s45 offen offset:2048 lds
v_mfma_f32_16x16x32_bf16 v[168:171], a[184:187], a[168:171], v[168:171]
v_mfma_f32_16x16x32_bf16 v[172:175], a[188:191], a[168:171], v[172:175]
v_mfma_f32_16x16x32_bf16 v[176:179], a[176:179], a[172:175], v[176:179]
buffer_load_dwordx4 v243, s[8:11], s45 offen offset:3072 lds
v_mfma_f32_16x16x32_bf16 v[180:183], a[180:183], a[172:175], v[180:183]
s_add_u32 s45, s45, 131072
v_mfma_f32_16x16x32_bf16 v[184:187], a[184:187], a[172:175], v[184:187]
buffer_store_dwordx4 v[104:107], v7, s[12:15], s30 offen offset:2048 sc1
v_mfma_f32_16x16x32_bf16 v[188:191], a[188:191], a[172:175], v[188:191]
s_waitcnt lgkmcnt(0)
v_mfma_f32_16x16x32_bf16 v[128:131], a[144:147], a[128:131], v[128:131]
s_mov_b32 m0, s49
ds_read_b64_tr_b16 a[176:177], v245 offset:8192
ds_read_b64_tr_b16 a[178:179], v245 offset:10240
v_mfma_f32_16x16x32_bf16 v[132:135], a[148:151], a[128:131], v[132:135]
buffer_load_dwordx4 v7, s[4:7], s44 offen offset:0 sc1 lds
v_mfma_f32_16x16x32_bf16 v[136:139], a[152:155], a[128:131], v[136:139]
ds_read_b128 a[160:163], v244 offset:2048
ds_read_b128 a[164:167], v244 offset:2304
v_mfma_f32_16x16x32_bf16 v[140:143], a[156:159], a[128:131], v[140:143]
ds_read_b64_tr_b16 a[180:181], v246 offset:8192
ds_read_b64_tr_b16 a[182:183], v246 offset:10240
v_mfma_f32_16x16x32_bf16 v[144:147], a[144:147], a[132:135], v[144:147]
buffer_load_dwordx4 v7, s[4:7], s44 offen offset:1024 sc1 lds
v_mfma_f32_16x16x32_bf16 v[148:151], a[148:151], a[132:135], v[148:151]
ds_read_b64_tr_b16 a[184:185], v247 offset:8192
ds_read_b64_tr_b16 a[186:187], v247 offset:10240
v_mfma_f32_16x16x32_bf16 v[152:155], a[152:155], a[132:135], v[152:155]
ds_read_b128 a[168:171], v244 offset:6144
ds_read_b128 a[172:175], v244 offset:6400
v_mfma_f32_16x16x32_bf16 v[156:159], a[156:159], a[132:135], v[156:159]
buffer_store_dwordx4 v[100:103], v7, s[12:15], s30 offen offset:1024 sc1
v_mfma_f32_16x16x32_bf16 v[160:163], a[144:147], a[136:139], v[160:163]
s_cmp_eq_u32 s46, 0
s_cselect_b32 s53, s56, s57
ds_read_b64_tr_b16 a[188:189], v248 offset:8192
ds_read_b64_tr_b16 a[190:191], v248 offset:10240
v_mfma_f32_16x16x32_bf16 v[164:167], a[148:151], a[136:139], v[164:167]
v_mfma_f32_16x16x32_bf16 v[168:171], a[152:155], a[136:139], v[168:171]
s_add_u32 s49, s46, s91
s_add_u32 s46, s46, s53
v_add_u32 v244, s53, v244
v_mfma_f32_16x16x32_bf16 v[172:175], a[156:159], a[136:139], v[172:175]
buffer_load_dwordx4 v7, s[4:7], s44 offen offset:2048 sc1 lds
v_mfma_f32_16x16x32_bf16 v[176:179], a[144:147], a[140:143], v[176:179]
v_add_u32 v245, s53, v245
v_add_u32 v246, s53, v246
v_mfma_f32_16x16x32_bf16 v[180:183], a[148:151], a[140:143], v[180:183]
buffer_load_dwordx4 v7, s[4:7], s44 offen offset:3072 sc1 lds
v_mfma_f32_16x16x32_bf16 v[184:187], a[152:155], a[140:143], v[184:187]
buffer_store_dwordx4 v[108:111], v7, s[12:15], s30 offen offset:3072 sc1
v_add_u32 v247, s53, v247
v_add_u32 v248, s53, v248
v_mfma_f32_16x16x32_bf16 v[188:191], a[156:159], a[140:143], v[188:191]
s_waitcnt vmcnt(36) lgkmcnt(0)
s_barrier
v_mfma_f32_16x16x32_bf16 v[128:131], a[176:179], a[160:163], v[128:131]
s_add_u32 m0, s49, 16384
s_add_u32 s44, s44, 4096
ds_read_b64_tr_b16 a[144:145], v245 offset:0
ds_read_b64_tr_b16 a[146:147], v245 offset:2048
v_mfma_f32_16x16x32_bf16 v[132:135], a[180:183], a[160:163], v[132:135]
buffer_load_dwordx4 v240, s[8:11], s45 offen offset:0 lds
v_mfma_f32_16x16x32_bf16 v[136:139], a[184:187], a[160:163], v[136:139]
ds_read_b128 a[128:131], v244 offset:0
ds_read_b128 a[132:135], v244 offset:256
v_mfma_f32_16x16x32_bf16 v[140:143], a[188:191], a[160:163], v[140:143]
ds_read_b64_tr_b16 a[148:149], v246 offset:0
ds_read_b64_tr_b16 a[150:151], v246 offset:2048
v_mfma_f32_16x16x32_bf16 v[144:147], a[176:179], a[164:167], v[144:147]
buffer_load_dwordx4 v241, s[8:11], s45 offen offset:1024 lds
v_mfma_f32_16x16x32_bf16 v[148:151], a[180:183], a[164:167], v[148:151]
ds_read_b64_tr_b16 a[152:153], v247 offset:0
ds_read_b64_tr_b16 a[154:155], v247 offset:2048
v_mfma_f32_16x16x32_bf16 v[152:155], a[184:187], a[164:167], v[152:155]
ds_read_b128 a[136:139], v244 offset:4096
ds_read_b128 a[140:143], v244 offset:4352
v_mfma_f32_16x16x32_bf16 v[156:159], a[188:191], a[164:167], v[156:159]
buffer_store_dwordx4 v[112:115], v7, s[12:15], s31 offen offset:0 sc1
v_mfma_f32_16x16x32_bf16 v[160:163], a[176:179], a[168:171], v[160:163]
ds_read_b64_tr_b16 a[156:157], v248 offset:0
ds_read_b64_tr_b16 a[158:159], v248 offset:2048
v_mfma_f32_16x16x32_bf16 v[164:167], a[180:183], a[168:171], v[164:167]
buffer_load_dwordx4 v242, s[8:11], s45 offen offset:2048 lds
v_mfma_f32_16x16x32_bf16 v[168:171], a[184:187], a[168:171], v[168:171]
v_mfma_f32_16x16x32_bf16 v[172:175], a[188:191], a[168:171], v[172:175]
v_mfma_f32_16x16x32_bf16 v[176:179], a[176:179], a[172:175], v[176:179]
buffer_load_dwordx4 v243, s[8:11], s45 offen offset:3072 lds
v_mfma_f32_16x16x32_bf16 v[180:183], a[180:183], a[172:175], v[180:183]
s_add_u32 s45, s45, 131072
v_mfma_f32_16x16x32_bf16 v[184:187], a[184:187], a[172:175], v[184:187]
buffer_store_dwordx4 v[120:123], v7, s[12:15], s31 offen offset:2048 sc1
v_mfma_f32_16x16x32_bf16 v[188:191], a[188:191], a[172:175], v[188:191]
s_waitcnt lgkmcnt(0)
v_mfma_f32_16x16x32_bf16 v[128:131], a[144:147], a[128:131], v[128:131]
s_mov_b32 m0, s49
ds_read_b64_tr_b16 a[176:177], v245 offset:8192
ds_read_b64_tr_b16 a[178:179], v245 offset:10240
v_mfma_f32_16x16x32_bf16 v[132:135], a[148:151], a[128:131], v[132:135]
buffer_load_dwordx4 v7, s[4:7], s44 offen offset:0 sc1 lds
v_mfma_f32_16x16x32_bf16 v[136:139], a[152:155], a[128:131], v[136:139]
ds_read_b128 a[160:163], v244 offset:2048
ds_read_b128 a[164:167], v244 offset:2304
v_mfma_f32_16x16x32_bf16 v[140:143], a[156:159], a[128:131], v[140:143]
ds_read_b64_tr_b16 a[180:181], v246 offset:8192
ds_read_b64_tr_b16 a[182:183], v246 offset:10240
v_mfma_f32_16x16x32_bf16 v[144:147], a[144:147], a[132:135], v[144:147]
buffer_load_dwordx4 v7, s[4:7], s44 offen offset:1024 sc1 lds
v_mfma_f32_16x16x32_bf16 v[148:151], a[148:151], a[132:135], v[148:151]
ds_read_b64_tr_b16 a[184:185], v247 offset:8192
ds_read_b64_tr_b16 a[186:187], v247 offset:10240
v_mfma_f32_16x16x32_bf16 v[152:155], a[152:155], a[132:135], v[152:155]
ds_read_b128 a[168:171], v244 offset:6144
ds_read_b128 a[172:175], v244 offset:6400
v_mfma_f32_16x16x32_bf16 v[156:159], a[156:159], a[132:135], v[156:159]
buffer_store_dwordx4 v[116:119], v7, s[12:15], s31 offen offset:1024 sc1
v_mfma_f32_16x16x32_bf16 v[160:163], a[144:147], a[136:139], v[160:163]
s_cmp_eq_u32 s46, 0
s_cselect_b32 s53, s56, s57
ds_read_b64_tr_b16 a[188:189], v248 offset:8192
ds_read_b64_tr_b16 a[190:191], v248 offset:10240
v_mfma_f32_16x16x32_bf16 v[164:167], a[148:151], a[136:139], v[164:167]
v_mfma_f32_16x16x32_bf16 v[168:171], a[152:155], a[136:139], v[168:171]
s_add_u32 s49, s46, s91
s_add_u32 s46, s46, s53
v_add_u32 v244, s53, v244
v_mfma_f32_16x16x32_bf16 v[172:175], a[156:159], a[136:139], v[172:175]
buffer_load_dwordx4 v7, s[4:7], s44 offen offset:2048 sc1 lds
v_mfma_f32_16x16x32_bf16 v[176:179], a[144:147], a[140:143], v[176:179]
v_add_u32 v245, s53, v245
v_add_u32 v246, s53, v246
v_mfma_f32_16x16x32_bf16 v[180:183], a[148:151], a[140:143], v[180:183]
buffer_load_dwordx4 v7, s[4:7], s44 offen offset:3072 sc1 lds
v_mfma_f32_16x16x32_bf16 v[184:187], a[152:155], a[140:143], v[184:187]
buffer_store_dwordx4 v[124:127], v7, s[12:15], s31 offen offset:3072 sc1
v_add_u32 v247, s53, v247
v_add_u32 v248, s53, v248
v_mfma_f32_16x16x32_bf16 v[188:191], a[156:159], a[140:143], v[188:191]
s_waitcnt vmcnt(37) lgkmcnt(0)
s_barrier
v_mfma_f32_16x16x32_bf16 v[128:131], a[176:179], a[160:163], v[128:131]
s_add_u32 m0, s49, 16384
s_add_u32 s44, s44, 4096
ds_read_b64_tr_b16 a[144:145], v245 offset:0
ds_read_b64_tr_b16 a[146:147], v245 offset:2048
v_mfma_f32_16x16x32_bf16 v[132:135], a[180:183], a[160:163], v[132:135]
buffer_load_dwordx4 v240, s[8:11], s45 offen offset:0 lds
v_mfma_f32_16x16x32_bf16 v[136:139], a[184:187], a[160:163], v[136:139]
ds_read_b128 a[128:131], v244 offset:0
ds_read_b128 a[132:135], v244 offset:256
v_mfma_f32_16x16x32_bf16 v[140:143], a[188:191], a[160:163], v[140:143]
ds_read_b64_tr_b16 a[148:149], v246 offset:0
ds_read_b64_tr_b16 a[150:151], v246 offset:2048
v_mfma_f32_16x16x32_bf16 v[144:147], a[176:179], a[164:167], v[144:147]
buffer_load_dwordx4 v241, s[8:11], s45 offen offset:1024 lds
v_mfma_f32_16x16x32_bf16 v[148:151], a[180:183], a[164:167], v[148:151]
ds_read_b64_tr_b16 a[152:153], v247 offset:0
ds_read_b64_tr_b16 a[154:155], v247 offset:2048
v_mfma_f32_16x16x32_bf16 v[152:155], a[184:187], a[164:167], v[152:155]
ds_read_b128 a[136:139], v244 offset:4096
ds_read_b128 a[140:143], v244 offset:4352
v_mfma_f32_16x16x32_bf16 v[156:159], a[188:191], a[164:167], v[156:159]
v_mfma_f32_16x16x32_bf16 v[160:163], a[176:179], a[168:171], v[160:163]
ds_read_b64_tr_b16 a[156:157], v248 offset:0
ds_read_b64_tr_b16 a[158:159], v248 offset:2048
v_mfma_f32_16x16x32_bf16 v[164:167], a[180:183], a[168:171], v[164:167]
buffer_load_dwordx4 v242, s[8:11], s45 offen offset:2048 lds
v_mfma_f32_16x16x32_bf16 v[168:171], a[184:187], a[168:171], v[168:171]
v_mfma_f32_16x16x32_bf16 v[172:175], a[188:191], a[168:171], v[172:175]
v_mfma_f32_16x16x32_bf16 v[176:179], a[176:179], a[172:175], v[176:179]
buffer_load_dwordx4 v243, s[8:11], s45 offen offset:3072 lds
v_mfma_f32_16x16x32_bf16 v[180:183], a[180:183], a[172:175], v[180:183]
s_add_u32 s45, s45, 131072
v_mfma_f32_16x16x32_bf16 v[184:187], a[184:187], a[172:175], v[184:187]
v_mfma_f32_16x16x32_bf16 v[188:191], a[188:191], a[172:175], v[188:191]
s_waitcnt lgkmcnt(0)
v_mfma_f32_16x16x32_bf16 v[128:131], a[144:147], a[128:131], v[128:131]
s_mov_b32 m0, s49
ds_read_b64_tr_b16 a[176:177], v245 offset:8192
ds_read_b64_tr_b16 a[178:179], v245 offset:10240
v_mfma_f32_16x16x32_bf16 v[132:135], a[148:151], a[128:131], v[132:135]
buffer_load_dwordx4 v7, s[4:7], s44 offen offset:0 sc1 lds
v_mfma_f32_16x16x32_bf16 v[136:139], a[152:155], a[128:131], v[136:139]
ds_read_b128 a[160:163], v244 offset:2048
ds_read_b128 a[164:167], v244 offset:2304
v_mfma_f32_16x16x32_bf16 v[140:143], a[156:159], a[128:131], v[140:143]
ds_read_b64_tr_b16 a[180:181], v246 offset:8192
ds_read_b64_tr_b16 a[182:183], v246 offset:10240
v_mfma_f32_16x16x32_bf16 v[144:147], a[144:147], a[132:135], v[144:147]
buffer_load_dwordx4 v7, s[4:7], s44 offen offset:1024 sc1 lds
v_mfma_f32_16x16x32_bf16 v[148:151], a[148:151], a[132:135], v[148:151]
ds_read_b64_tr_b16 a[184:185], v247 offset:8192
ds_read_b64_tr_b16 a[186:187], v247 offset:10240
v_mfma_f32_16x16x32_bf16 v[152:155], a[152:155], a[132:135], v[152:155]
ds_read_b128 a[168:171], v244 offset:6144
ds_read_b128 a[172:175], v244 offset:6400
v_mfma_f32_16x16x32_bf16 v[156:159], a[156:159], a[132:135], v[156:159]
v_mfma_f32_16x16x32_bf16 v[160:163], a[144:147], a[136:139], v[160:163]
s_cmp_eq_u32 s46, 0
s_cselect_b32 s53, s56, s57
ds_read_b64_tr_b16 a[188:189], v248 offset:8192
ds_read_b64_tr_b16 a[190:191], v248 offset:10240
v_mfma_f32_16x16x32_bf16 v[164:167], a[148:151], a[136:139], v[164:167]
v_mfma_f32_16x16x32_bf16 v[168:171], a[152:155], a[136:139], v[168:171]
s_add_u32 s49, s46, s91
s_add_u32 s46, s46, s53
v_add_u32 v244, s53, v244
v_mfma_f32_16x16x32_bf16 v[172:175], a[156:159], a[136:139], v[172:175]
buffer_load_dwordx4 v7, s[4:7], s44 offen offset:2048 sc1 lds
v_mfma_f32_16x16x32_bf16 v[176:179], a[144:147], a[140:143], v[176:179]
v_add_u32 v245, s53, v245
v_add_u32 v246, s53, v246
v_mfma_f32_16x16x32_bf16 v[180:183], a[148:151], a[140:143], v[180:183]
buffer_load_dwordx4 v7, s[4:7], s44 offen offset:3072 sc1 lds
v_mfma_f32_16x16x32_bf16 v[184:187], a[152:155], a[140:143], v[184:187]
v_add_u32 v247, s53, v247
v_add_u32 v248, s53, v248
v_mfma_f32_16x16x32_bf16 v[188:191], a[156:159], a[140:143], v[188:191]
s_waitcnt vmcnt(33) lgkmcnt(0)
s_barrier
v_mfma_f32_16x16x32_bf16 v[128:131], a[176:179], a[160:163], v[128:131]
s_add_u32 m0, s49, 16384
s_add_u32 s44, s44, 4096
ds_read_b64_tr_b16 a[144:145], v245 offset:0
ds_read_b64_tr_b16 a[146:147], v245 offset:2048
v_mfma_f32_16x16x32_bf16 v[132:135], a[180:183], a[160:163], v[132:135]
buffer_load_dwordx4 v240, s[8:11], s45 offen offset:0 lds
v_mfma_f32_16x16x32_bf16 v[136:139], a[184:187], a[160:163], v[136:139]
ds_read_b128 a[128:131], v244 offset:0
ds_read_b128 a[132:135], v244 offset:256
v_mfma_f32_16x16x32_bf16 v[140:143], a[188:191], a[160:163], v[140:143]
ds_read_b64_tr_b16 a[148:149], v246 offset:0
ds_read_b64_tr_b16 a[150:151], v246 offset:2048
v_mfma_f32_16x16x32_bf16 v[144:147], a[176:179], a[164:167], v[144:147]
buffer_load_dwordx4 v241, s[8:11], s45 offen offset:1024 lds
v_mfma_f32_16x16x32_bf16 v[148:151], a[180:183], a[164:167], v[148:151]
ds_read_b64_tr_b16 a[152:153], v247 offset:0
ds_read_b64_tr_b16 a[154:155], v247 offset:2048
v_mfma_f32_16x16x32_bf16 v[152:155], a[184:187], a[164:167], v[152:155]
ds_read_b128 a[136:139], v244 offset:4096
ds_read_b128 a[140:143], v244 offset:4352
v_mfma_f32_16x16x32_bf16 v[156:159], a[188:191], a[164:167], v[156:159]
v_mfma_f32_16x16x32_bf16 v[160:163], a[176:179], a[168:171], v[160:163]
ds_read_b64_tr_b16 a[156:157], v248 offset:0
ds_read_b64_tr_b16 a[158:159], v248 offset:2048
v_mfma_f32_16x16x32_bf16 v[164:167], a[180:183], a[168:171], v[164:167]
buffer_load_dwordx4 v242, s[8:11], s45 offen offset:2048 lds
v_mfma_f32_16x16x32_bf16 v[168:171], a[184:187], a[168:171], v[168:171]
v_mfma_f32_16x16x32_bf16 v[172:175], a[188:191], a[168:171], v[172:175]
v_mfma_f32_16x16x32_bf16 v[176:179], a[176:179], a[172:175], v[176:179]
buffer_load_dwordx4 v243, s[8:11], s45 offen offset:3072 lds
v_mfma_f32_16x16x32_bf16 v[180:183], a[180:183], a[172:175], v[180:183]
s_add_u32 s45, s45, 131072
v_mfma_f32_16x16x32_bf16 v[184:187], a[184:187], a[172:175], v[184:187]
v_mfma_f32_16x16x32_bf16 v[188:191], a[188:191], a[172:175], v[188:191]
s_waitcnt lgkmcnt(0)
v_mfma_f32_16x16x32_bf16 v[128:131], a[144:147], a[128:131], v[128:131]
s_mov_b32 m0, s49
ds_read_b64_tr_b16 a[176:177], v245 offset:8192
ds_read_b64_tr_b16 a[178:179], v245 offset:10240
v_mfma_f32_16x16x32_bf16 v[132:135], a[148:151], a[128:131], v[132:135]
buffer_load_dwordx4 v7, s[4:7], s44 offen offset:0 sc1 lds
v_mfma_f32_16x16x32_bf16 v[136:139], a[152:155], a[128:131], v[136:139]
ds_read_b128 a[160:163], v244 offset:2048
ds_read_b128 a[164:167], v244 offset:2304
v_mfma_f32_16x16x32_bf16 v[140:143], a[156:159], a[128:131], v[140:143]
ds_read_b64_tr_b16 a[180:181], v246 offset:8192
ds_read_b64_tr_b16 a[182:183], v246 offset:10240
v_mfma_f32_16x16x32_bf16 v[144:147], a[144:147], a[132:135], v[144:147]
buffer_load_dwordx4 v7, s[4:7], s44 offen offset:1024 sc1 lds
v_mfma_f32_16x16x32_bf16 v[148:151], a[148:151], a[132:135], v[148:151]
ds_read_b64_tr_b16 a[184:185], v247 offset:8192
ds_read_b64_tr_b16 a[186:187], v247 offset:10240
v_mfma_f32_16x16x32_bf16 v[152:155], a[152:155], a[132:135], v[152:155]
ds_read_b128 a[168:171], v244 offset:6144
ds_read_b128 a[172:175], v244 offset:6400
v_mfma_f32_16x16x32_bf16 v[156:159], a[156:159], a[132:135], v[156:159]
v_mfma_f32_16x16x32_bf16 v[160:163], a[144:147], a[136:139], v[160:163]
s_cmp_eq_u32 s46, 0
s_cselect_b32 s53, s56, s57
ds_read_b64_tr_b16 a[188:189], v248 offset:8192
ds_read_b64_tr_b16 a[190:191], v248 offset:10240
v_mfma_f32_16x16x32_bf16 v[164:167], a[148:151], a[136:139], v[164:167]
v_mfma_f32_16x16x32_bf16 v[168:171], a[152:155], a[136:139], v[168:171]
s_add_u32 s49, s46, s91
s_add_u32 s46, s46, s53
v_add_u32 v244, s53, v244
v_mfma_f32_16x16x32_bf16 v[172:175], a[156:159], a[136:139], v[172:175]
buffer_load_dwordx4 v7, s[4:7], s44 offen offset:2048 sc1 lds
v_mfma_f32_16x16x32_bf16 v[176:179], a[144:147], a[140:143], v[176:179]
v_add_u32 v245, s53, v245
v_add_u32 v246, s53, v246
v_mfma_f32_16x16x32_bf16 v[180:183], a[148:151], a[140:143], v[180:183]
buffer_load_dwordx4 v7, s[4:7], s44 offen offset:3072 sc1 lds
v_mfma_f32_16x16x32_bf16 v[184:187], a[152:155], a[140:143], v[184:187]
v_add_u32 v247, s53, v247
v_add_u32 v248, s53, v248
v_mfma_f32_16x16x32_bf16 v[188:191], a[156:159], a[140:143], v[188:191]
s_waitcnt vmcnt(16)
s_waitcnt vmcnt(29) lgkmcnt(0)
s_barrier
v_mfma_f32_16x16x32_bf16 v[128:131], a[176:179], a[160:163], v[128:131]
s_add_u32 m0, s49, 16384
s_add_u32 s44, s44, 4096
ds_read_b64_tr_b16 a[144:145], v245 offset:0
ds_read_b64_tr_b16 a[146:147], v245 offset:2048
v_mfma_f32_16x16x32_bf16 v[132:135], a[180:183], a[160:163], v[132:135]
buffer_load_dwordx4 v240, s[8:11], s45 offen offset:0 lds
v_mfma_f32_16x16x32_bf16 v[136:139], a[184:187], a[160:163], v[136:139]
ds_read_b128 a[128:131], v244 offset:0
ds_read_b128 a[132:135], v244 offset:256
v_mfma_f32_16x16x32_bf16 v[140:143], a[188:191], a[160:163], v[140:143]
ds_read_b64_tr_b16 a[148:149], v246 offset:0
ds_read_b64_tr_b16 a[150:151], v246 offset:2048
v_mfma_f32_16x16x32_bf16 v[144:147], a[176:179], a[164:167], v[144:147]
buffer_load_dwordx4 v241, s[8:11], s45 offen offset:1024 lds
v_mfma_f32_16x16x32_bf16 v[148:151], a[180:183], a[164:167], v[148:151]
ds_read_b64_tr_b16 a[152:153], v247 offset:0
ds_read_b64_tr_b16 a[154:155], v247 offset:2048
v_mfma_f32_16x16x32_bf16 v[152:155], a[184:187], a[164:167], v[152:155]
ds_read_b128 a[136:139], v244 offset:4096
ds_read_b128 a[140:143], v244 offset:4352
v_mfma_f32_16x16x32_bf16 v[156:159], a[188:191], a[164:167], v[156:159]
v_mfma_f32_16x16x32_bf16 v[160:163], a[176:179], a[168:171], v[160:163]
ds_read_b64_tr_b16 a[156:157], v248 offset:0
ds_read_b64_tr_b16 a[158:159], v248 offset:2048
v_mfma_f32_16x16x32_bf16 v[164:167], a[180:183], a[168:171], v[164:167]
buffer_load_dwordx4 v242, s[8:11], s45 offen offset:2048 lds
v_mfma_f32_16x16x32_bf16 v[168:171], a[184:187], a[168:171], v[168:171]
s_nop 0
v_or_b32 v0, 5, v11
v_mfma_f32_16x16x32_bf16 v[172:175], a[188:191], a[168:171], v[172:175]
v_mfma_f32_16x16x32_bf16 v[176:179], a[176:179], a[172:175], v[176:179]
buffer_load_dwordx4 v243, s[8:11], s45 offen offset:3072 lds
v_mfma_f32_16x16x32_bf16 v[180:183], a[180:183], a[172:175], v[180:183]
s_add_u32 s45, s45, 131072
v_mfma_f32_16x16x32_bf16 v[184:187], a[184:187], a[172:175], v[184:187]
buffer_store_dword v0, v6, s[60:63], 0 offen sc1
v_mfma_f32_16x16x32_bf16 v[188:191], a[188:191], a[172:175], v[188:191]
s_waitcnt lgkmcnt(0)
v_mfma_f32_16x16x32_bf16 v[128:131], a[144:147], a[128:131], v[128:131]
s_mov_b32 m0, s49
ds_read_b64_tr_b16 a[176:177], v245 offset:8192
ds_read_b64_tr_b16 a[178:179], v245 offset:10240
v_mfma_f32_16x16x32_bf16 v[132:135], a[148:151], a[128:131], v[132:135]
buffer_load_dwordx4 v7, s[4:7], s44 offen offset:0 sc1 lds
v_mfma_f32_16x16x32_bf16 v[136:139], a[152:155], a[128:131], v[136:139]
ds_read_b128 a[160:163], v244 offset:2048
ds_read_b128 a[164:167], v244 offset:2304
v_mfma_f32_16x16x32_bf16 v[140:143], a[156:159], a[128:131], v[140:143]
ds_read_b64_tr_b16 a[180:181], v246 offset:8192
ds_read_b64_tr_b16 a[182:183], v246 offset:10240
v_mfma_f32_16x16x32_bf16 v[144:147], a[144:147], a[132:135], v[144:147]
buffer_load_dwordx4 v7, s[4:7], s44 offen offset:1024 sc1 lds
v_mfma_f32_16x16x32_bf16 v[148:151], a[148:151], a[132:135], v[148:151]
ds_read_b64_tr_b16 a[184:185], v247 offset:8192
ds_read_b64_tr_b16 a[186:187], v247 offset:10240
v_mfma_f32_16x16x32_bf16 v[152:155], a[152:155], a[132:135], v[152:155]
ds_read_b128 a[168:171], v244 offset:6144
ds_read_b128 a[172:175], v244 offset:6400
v_mfma_f32_16x16x32_bf16 v[156:159], a[156:159], a[132:135], v[156:159]
v_mfma_f32_16x16x32_bf16 v[160:163], a[144:147], a[136:139], v[160:163]
s_cmp_eq_u32 s46, 0
s_cselect_b32 s53, s56, s57
ds_read_b64_tr_b16 a[188:189], v248 offset:8192
ds_read_b64_tr_b16 a[190:191], v248 offset:10240
v_mfma_f32_16x16x32_bf16 v[164:167], a[148:151], a[136:139], v[164:167]
v_mfma_f32_16x16x32_bf16 v[168:171], a[152:155], a[136:139], v[168:171]
s_add_u32 s49, s46, s91
s_add_u32 s46, s46, s53
v_add_u32 v244, s53, v244
v_mfma_f32_16x16x32_bf16 v[172:175], a[156:159], a[136:139], v[172:175]
buffer_load_dwordx4 v7, s[4:7], s44 offen offset:2048 sc1 lds
v_mfma_f32_16x16x32_bf16 v[176:179], a[144:147], a[140:143], v[176:179]
v_add_u32 v245, s53, v245
v_add_u32 v246, s53, v246
v_mfma_f32_16x16x32_bf16 v[180:183], a[148:151], a[140:143], v[180:183]
buffer_load_dwordx4 v7, s[4:7], s44 offen offset:3072 sc1 lds
v_mfma_f32_16x16x32_bf16 v[184:187], a[152:155], a[140:143], v[184:187]
v_add_u32 v247, s53, v247
v_add_u32 v248, s53, v248
v_mfma_f32_16x16x32_bf16 v[188:191], a[156:159], a[140:143], v[188:191]
s_waitcnt vmcnt(26) lgkmcnt(0)
s_barrier
v_mfma_f32_16x16x32_bf16 v[128:131], a[176:179], a[160:163], v[128:131]
s_add_u32 m0, s49, 16384
s_add_u32 s44, s44, 4096
ds_read_b64_tr_b16 a[144:145], v245 offset:0
ds_read_b64_tr_b16 a[146:147], v245 offset:2048
v_mfma_f32_16x16x32_bf16 v[132:135], a[180:183], a[160:163], v[132:135]
buffer_load_dwordx4 v240, s[8:11], s45 offen offset:0 lds
v_mfma_f32_16x16x32_bf16 v[136:139], a[184:187], a[160:163], v[136:139]
ds_read_b128 a[128:131], v244 offset:0
ds_read_b128 a[132:135], v244 offset:256
v_mfma_f32_16x16x32_bf16 v[140:143], a[188:191], a[160:163], v[140:143]
ds_read_b64_tr_b16 a[148:149], v246 offset:0
ds_read_b64_tr_b16 a[150:151], v246 offset:2048
v_mfma_f32_16x16x32_bf16 v[144:147], a[176:179], a[164:167], v[144:147]
buffer_load_dwordx4 v241, s[8:11], s45 offen offset:1024 lds
v_mfma_f32_16x16x32_bf16 v[148:151], a[180:183], a[164:167], v[148:151]
ds_read_b64_tr_b16 a[152:153], v247 offset:0
ds_read_b64_tr_b16 a[154:155], v247 offset:2048
v_mfma_f32_16x16x32_bf16 v[152:155], a[184:187], a[164:167], v[152:155]
ds_read_b128 a[136:139], v244 offset:4096
ds_read_b128 a[140:143], v244 offset:4352
v_mfma_f32_16x16x32_bf16 v[156:159], a[188:191], a[164:167], v[156:159]
v_mfma_f32_16x16x32_bf16 v[160:163], a[176:179], a[168:171], v[160:163]
ds_read_b64_tr_b16 a[156:157], v248 offset:0
ds_read_b64_tr_b16 a[158:159], v248 offset:2048
v_mfma_f32_16x16x32_bf16 v[164:167], a[180:183], a[168:171], v[164:167]
buffer_load_dwordx4 v242, s[8:11], s45 offen offset:2048 lds
v_mfma_f32_16x16x32_bf16 v[168:171], a[184:187], a[168:171], v[168:171]
v_mfma_f32_16x16x32_bf16 v[172:175], a[188:191], a[168:171], v[172:175]
v_mfma_f32_16x16x32_bf16 v[176:179], a[176:179], a[172:175], v[176:179]
buffer_load_dwordx4 v243, s[8:11], s45 offen offset:3072 lds
v_mfma_f32_16x16x32_bf16 v[180:183], a[180:183], a[172:175], v[180:183]
s_add_u32 s45, s45, 131072
v_mfma_f32_16x16x32_bf16 v[184:187], a[184:187], a[172:175], v[184:187]
v_mfma_f32_16x16x32_bf16 v[188:191], a[188:191], a[172:175], v[188:191]
s_waitcnt lgkmcnt(0)
v_mfma_f32_16x16x32_bf16 v[128:131], a[144:147], a[128:131], v[128:131]
s_mov_b32 m0, s49
ds_read_b64_tr_b16 a[176:177], v245 offset:8192
ds_read_b64_tr_b16 a[178:179], v245 offset:10240
v_mfma_f32_16x16x32_bf16 v[132:135], a[148:151], a[128:131], v[132:135]
buffer_load_dwordx4 v7, s[4:7], s44 offen offset:0 sc1 lds
v_mfma_f32_16x16x32_bf16 v[136:139], a[152:155], a[128:131], v[136:139]
ds_read_b128 a[160:163], v244 offset:2048
ds_read_b128 a[164:167], v244 offset:2304
v_mfma_f32_16x16x32_bf16 v[140:143], a[156:159], a[128:131], v[140:143]
ds_read_b64_tr_b16 a[180:181], v246 offset:8192
ds_read_b64_tr_b16 a[182:183], v246 offset:10240
v_mfma_f32_16x16x32_bf16 v[144:147], a[144:147], a[132:135], v[144:147]
buffer_load_dwordx4 v7, s[4:7], s44 offen offset:1024 sc1 lds
v_mfma_f32_16x16x32_bf16 v[148:151], a[148:151], a[132:135], v[148:151]
ds_read_b64_tr_b16 a[184:185], v247 offset:8192
ds_read_b64_tr_b16 a[186:187], v247 offset:10240
v_mfma_f32_16x16x32_bf16 v[152:155], a[152:155], a[132:135], v[152:155]
ds_read_b128 a[168:171], v244 offset:6144
ds_read_b128 a[172:175], v244 offset:6400
v_mfma_f32_16x16x32_bf16 v[156:159], a[156:159], a[132:135], v[156:159]
v_mfma_f32_16x16x32_bf16 v[160:163], a[144:147], a[136:139], v[160:163]
s_cmp_eq_u32 s46, 0
s_cselect_b32 s53, s56, s57
ds_read_b64_tr_b16 a[188:189], v248 offset:8192
ds_read_b64_tr_b16 a[190:191], v248 offset:10240
v_mfma_f32_16x16x32_bf16 v[164:167], a[148:151], a[136:139], v[164:167]
v_mfma_f32_16x16x32_bf16 v[168:171], a[152:155], a[136:139], v[168:171]
s_add_u32 s49, s46, s91
s_add_u32 s46, s46, s53
v_add_u32 v244, s53, v244
v_mfma_f32_16x16x32_bf16 v[172:175], a[156:159], a[136:139], v[172:175]
buffer_load_dwordx4 v7, s[4:7], s44 offen offset:2048 sc1 lds
v_mfma_f32_16x16x32_bf16 v[176:179], a[144:147], a[140:143], v[176:179]
v_add_u32 v245, s53, v245
v_add_u32 v246, s53, v246
v_mfma_f32_16x16x32_bf16 v[180:183], a[148:151], a[140:143], v[180:183]
buffer_load_dwordx4 v7, s[4:7], s44 offen offset:3072 sc1 lds
v_mfma_f32_16x16x32_bf16 v[184:187], a[152:155], a[140:143], v[184:187]
v_add_u32 v247, s53, v247
v_add_u32 v248, s53, v248
v_mfma_f32_16x16x32_bf16 v[188:191], a[156:159], a[140:143], v[188:191]
s_waitcnt vmcnt(25) lgkmcnt(0)
s_barrier
v_mfma_f32_16x16x32_bf16 v[128:131], a[176:179], a[160:163], v[128:131]
s_add_u32 m0, s49, 16384
s_add_u32 s44, s44, 4096
ds_read_b64_tr_b16 a[144:145], v245 offset:0
ds_read_b64_tr_b16 a[146:147], v245 offset:2048
v_mfma_f32_16x16x32_bf16 v[132:135], a[180:183], a[160:163], v[132:135]
buffer_load_dwordx4 v240, s[8:11], s45 offen offset:0 lds
v_mfma_f32_16x16x32_bf16 v[136:139], a[184:187], a[160:163], v[136:139]
ds_read_b128 a[128:131], v244 offset:0
ds_read_b128 a[132:135], v244 offset:256
v_mfma_f32_16x16x32_bf16 v[140:143], a[188:191], a[160:163], v[140:143]
ds_read_b64_tr_b16 a[148:149], v246 offset:0
ds_read_b64_tr_b16 a[150:151], v246 offset:2048
v_mfma_f32_16x16x32_bf16 v[144:147], a[176:179], a[164:167], v[144:147]
buffer_load_dwordx4 v241, s[8:11], s45 offen offset:1024 lds
v_mfma_f32_16x16x32_bf16 v[148:151], a[180:183], a[164:167], v[148:151]
ds_read_b64_tr_b16 a[152:153], v247 offset:0
ds_read_b64_tr_b16 a[154:155], v247 offset:2048
v_mfma_f32_16x16x32_bf16 v[152:155], a[184:187], a[164:167], v[152:155]
ds_read_b128 a[136:139], v244 offset:4096
ds_read_b128 a[140:143], v244 offset:4352
v_mfma_f32_16x16x32_bf16 v[156:159], a[188:191], a[164:167], v[156:159]
v_mfma_f32_16x16x32_bf16 v[160:163], a[176:179], a[168:171], v[160:163]
ds_read_b64_tr_b16 a[156:157], v248 offset:0
ds_read_b64_tr_b16 a[158:159], v248 offset:2048
v_mfma_f32_16x16x32_bf16 v[164:167], a[180:183], a[168:171], v[164:167]
buffer_load_dwordx4 v242, s[8:11], s45 offen offset:2048 lds
v_mfma_f32_16x16x32_bf16 v[168:171], a[184:187], a[168:171], v[168:171]
v_mfma_f32_16x16x32_bf16 v[172:175], a[188:191], a[168:171], v[172:175]
v_mfma_f32_16x16x32_bf16 v[176:179], a[176:179], a[172:175], v[176:179]
buffer_load_dwordx4 v243, s[8:11], s45 offen offset:3072 lds
v_mfma_f32_16x16x32_bf16 v[180:183], a[180:183], a[172:175], v[180:183]
s_add_u32 s45, s45, 131072
v_mfma_f32_16x16x32_bf16 v[184:187], a[184:187], a[172:175], v[184:187]
v_mfma_f32_16x16x32_bf16 v[188:191], a[188:191], a[172:175], v[188:191]
s_waitcnt lgkmcnt(0)
v_mfma_f32_16x16x32_bf16 v[128:131], a[144:147], a[128:131], v[128:131]
s_mov_b32 m0, s49
ds_read_b64_tr_b16 a[176:177], v245 offset:8192
ds_read_b64_tr_b16 a[178:179], v245 offset:10240
v_mfma_f32_16x16x32_bf16 v[132:135], a[148:151], a[128:131], v[132:135]
buffer_load_dwordx4 v7, s[4:7], s44 offen offset:0 sc1 lds
v_mfma_f32_16x16x32_bf16 v[136:139], a[152:155], a[128:131], v[136:139]
ds_read_b128 a[160:163], v244 offset:2048
ds_read_b128 a[164:167], v244 offset:2304
v_mfma_f32_16x16x32_bf16 v[140:143], a[156:159], a[128:131], v[140:143]
ds_read_b64_tr_b16 a[180:181], v246 offset:8192
ds_read_b64_tr_b16 a[182:183], v246 offset:10240
v_mfma_f32_16x16x32_bf16 v[144:147], a[144:147], a[132:135], v[144:147]
buffer_load_dwordx4 v7, s[4:7], s44 offen offset:1024 sc1 lds
v_mfma_f32_16x16x32_bf16 v[148:151], a[148:151], a[132:135], v[148:151]
ds_read_b64_tr_b16 a[184:185], v247 offset:8192
ds_read_b64_tr_b16 a[186:187], v247 offset:10240
v_mfma_f32_16x16x32_bf16 v[152:155], a[152:155], a[132:135], v[152:155]
ds_read_b128 a[168:171], v244 offset:6144
ds_read_b128 a[172:175], v244 offset:6400
v_mfma_f32_16x16x32_bf16 v[156:159], a[156:159], a[132:135], v[156:159]
v_mfma_f32_16x16x32_bf16 v[160:163], a[144:147], a[136:139], v[160:163]
s_cmp_eq_u32 s46, 0
s_cselect_b32 s53, s56, s57
ds_read_b64_tr_b16 a[188:189], v248 offset:8192
ds_read_b64_tr_b16 a[190:191], v248 offset:10240
v_mfma_f32_16x16x32_bf16 v[164:167], a[148:151], a[136:139], v[164:167]
v_mfma_f32_16x16x32_bf16 v[168:171], a[152:155], a[136:139], v[168:171]
s_add_u32 s49, s46, s91
s_add_u32 s46, s46, s53
v_add_u32 v244, s53, v244
v_mfma_f32_16x16x32_bf16 v[172:175], a[156:159], a[136:139], v[172:175]
buffer_load_dwordx4 v7, s[4:7], s44 offen offset:2048 sc1 lds
v_mfma_f32_16x16x32_bf16 v[176:179], a[144:147], a[140:143], v[176:179]
v_add_u32 v245, s53, v245
v_add_u32 v246, s53, v246
v_mfma_f32_16x16x32_bf16 v[180:183], a[148:151], a[140:143], v[180:183]
buffer_load_dwordx4 v7, s[4:7], s44 offen offset:3072 sc1 lds
v_mfma_f32_16x16x32_bf16 v[184:187], a[152:155], a[140:143], v[184:187]
v_add_u32 v247, s53, v247
v_add_u32 v248, s53, v248
v_mfma_f32_16x16x32_bf16 v[188:191], a[156:159], a[140:143], v[188:191]
s_waitcnt vmcnt(25) lgkmcnt(0)
s_barrier
v_mfma_f32_16x16x32_bf16 v[128:131], a[176:179], a[160:163], v[128:131]
s_add_u32 m0, s49, 16384
s_add_u32 s44, s44, 4096
ds_read_b64_tr_b16 a[144:145], v245 offset:0
ds_read_b64_tr_b16 a[146:147], v245 offset:2048
v_mfma_f32_16x16x32_bf16 v[132:135], a[180:183], a[160:163], v[132:135]
buffer_load_dwordx4 v240, s[8:11], s45 offen offset:0 lds
v_mfma_f32_16x16x32_bf16 v[136:139], a[184:187], a[160:163], v[136:139]
ds_read_b128 a[128:131], v244 offset:0
ds_read_b128 a[132:135], v244 offset:256
v_mfma_f32_16x16x32_bf16 v[140:143], a[188:191], a[160:163], v[140:143]
ds_read_b64_tr_b16 a[148:149], v246 offset:0
ds_read_b64_tr_b16 a[150:151], v246 offset:2048
v_mfma_f32_16x16x32_bf16 v[144:147], a[176:179], a[164:167], v[144:147]
buffer_load_dwordx4 v241, s[8:11], s45 offen offset:1024 lds
v_mfma_f32_16x16x32_bf16 v[148:151], a[180:183], a[164:167], v[148:151]
ds_read_b64_tr_b16 a[152:153], v247 offset:0
ds_read_b64_tr_b16 a[154:155], v247 offset:2048
v_mfma_f32_16x16x32_bf16 v[152:155], a[184:187], a[164:167], v[152:155]
ds_read_b128 a[136:139], v244 offset:4096
ds_read_b128 a[140:143], v244 offset:4352
v_mfma_f32_16x16x32_bf16 v[156:159], a[188:191], a[164:167], v[156:159]
v_mfma_f32_16x16x32_bf16 v[160:163], a[176:179], a[168:171], v[160:163]
ds_read_b64_tr_b16 a[156:157], v248 offset:0
ds_read_b64_tr_b16 a[158:159], v248 offset:2048
v_mfma_f32_16x16x32_bf16 v[164:167], a[180:183], a[168:171], v[164:167]
buffer_load_dwordx4 v242, s[8:11], s45 offen offset:2048 lds
v_mfma_f32_16x16x32_bf16 v[168:171], a[184:187], a[168:171], v[168:171]
v_mfma_f32_16x16x32_bf16 v[172:175], a[188:191], a[168:171], v[172:175]
v_mfma_f32_16x16x32_bf16 v[176:179], a[176:179], a[172:175], v[176:179]
buffer_load_dwordx4 v243, s[8:11], s45 offen offset:3072 lds
v_mfma_f32_16x16x32_bf16 v[180:183], a[180:183], a[172:175], v[180:183]
s_add_u32 s45, s45, 131072
v_mfma_f32_16x16x32_bf16 v[184:187], a[184:187], a[172:175], v[184:187]
v_mfma_f32_16x16x32_bf16 v[188:191], a[188:191], a[172:175], v[188:191]
s_waitcnt lgkmcnt(0)
v_mfma_f32_16x16x32_bf16 v[128:131], a[144:147], a[128:131], v[128:131]
s_mov_b32 m0, s49
ds_read_b64_tr_b16 a[176:177], v245 offset:8192
ds_read_b64_tr_b16 a[178:179], v245 offset:10240
v_mfma_f32_16x16x32_bf16 v[132:135], a[148:151], a[128:131], v[132:135]
buffer_load_dwordx4 v7, s[4:7], s44 offen offset:0 sc1 lds
v_mfma_f32_16x16x32_bf16 v[136:139], a[152:155], a[128:131], v[136:139]
ds_read_b128 a[160:163], v244 offset:2048
ds_read_b128 a[164:167], v244 offset:2304
v_mfma_f32_16x16x32_bf16 v[140:143], a[156:159], a[128:131], v[140:143]
ds_read_b64_tr_b16 a[180:181], v246 offset:8192
ds_read_b64_tr_b16 a[182:183], v246 offset:10240
v_mfma_f32_16x16x32_bf16 v[144:147], a[144:147], a[132:135], v[144:147]
buffer_load_dwordx4 v7, s[4:7], s44 offen offset:1024 sc1 lds
v_mfma_f32_16x16x32_bf16 v[148:151], a[148:151], a[132:135], v[148:151]
ds_read_b64_tr_b16 a[184:185], v247 offset:8192
ds_read_b64_tr_b16 a[186:187], v247 offset:10240
v_mfma_f32_16x16x32_bf16 v[152:155], a[152:155], a[132:135], v[152:155]
ds_read_b128 a[168:171], v244 offset:6144
ds_read_b128 a[172:175], v244 offset:6400
v_mfma_f32_16x16x32_bf16 v[156:159], a[156:159], a[132:135], v[156:159]
v_mfma_f32_16x16x32_bf16 v[160:163], a[144:147], a[136:139], v[160:163]
s_cmp_eq_u32 s46, 0
s_cselect_b32 s53, s56, s57
ds_read_b64_tr_b16 a[188:189], v248 offset:8192
ds_read_b64_tr_b16 a[190:191], v248 offset:10240
v_mfma_f32_16x16x32_bf16 v[164:167], a[148:151], a[136:139], v[164:167]
v_mfma_f32_16x16x32_bf16 v[168:171], a[152:155], a[136:139], v[168:171]
s_add_u32 s49, s46, s91
s_add_u32 s46, s46, s53
v_add_u32 v244, s53, v244
v_mfma_f32_16x16x32_bf16 v[172:175], a[156:159], a[136:139], v[172:175]
buffer_load_dwordx4 v7, s[4:7], s44 offen offset:2048 sc1 lds
v_mfma_f32_16x16x32_bf16 v[176:179], a[144:147], a[140:143], v[176:179]
v_add_u32 v245, s53, v245
v_add_u32 v246, s53, v246
v_mfma_f32_16x16x32_bf16 v[180:183], a[148:151], a[140:143], v[180:183]
buffer_load_dwordx4 v7, s[4:7], s44 offen offset:3072 sc1 lds
v_mfma_f32_16x16x32_bf16 v[184:187], a[152:155], a[140:143], v[184:187]
v_add_u32 v247, s53, v247
v_add_u32 v248, s53, v248
v_mfma_f32_16x16x32_bf16 v[188:191], a[156:159], a[140:143], v[188:191]
s_waitcnt vmcnt(24) lgkmcnt(0)
s_barrier
v_mfma_f32_16x16x32_bf16 v[128:131], a[176:179], a[160:163], v[128:131]
s_add_u32 m0, s49, 16384
s_add_u32 s44, s44, 4096
ds_read_b64_tr_b16 a[144:145], v245 offset:0
ds_read_b64_tr_b16 a[146:147], v245 offset:2048
v_mfma_f32_16x16x32_bf16 v[132:135], a[180:183], a[160:163], v[132:135]
buffer_load_dwordx4 v240, s[8:11], s45 offen offset:0 lds
v_mfma_f32_16x16x32_bf16 v[136:139], a[184:187], a[160:163], v[136:139]
ds_read_b128 a[128:131], v244 offset:0
ds_read_b128 a[132:135], v244 offset:256
v_mfma_f32_16x16x32_bf16 v[140:143], a[188:191], a[160:163], v[140:143]
ds_read_b64_tr_b16 a[148:149], v246 offset:0
ds_read_b64_tr_b16 a[150:151], v246 offset:2048
v_mfma_f32_16x16x32_bf16 v[144:147], a[176:179], a[164:167], v[144:147]
buffer_load_dwordx4 v241, s[8:11], s45 offen offset:1024 lds
v_mfma_f32_16x16x32_bf16 v[148:151], a[180:183], a[164:167], v[148:151]
ds_read_b64_tr_b16 a[152:153], v247 offset:0
ds_read_b64_tr_b16 a[154:155], v247 offset:2048
v_mfma_f32_16x16x32_bf16 v[152:155], a[184:187], a[164:167], v[152:155]
ds_read_b128 a[136:139], v244 offset:4096
ds_read_b128 a[140:143], v244 offset:4352
v_mfma_f32_16x16x32_bf16 v[156:159], a[188:191], a[164:167], v[156:159]
v_mfma_f32_16x16x32_bf16 v[160:163], a[176:179], a[168:171], v[160:163]
ds_read_b64_tr_b16 a[156:157], v248 offset:0
ds_read_b64_tr_b16 a[158:159], v248 offset:2048
v_mfma_f32_16x16x32_bf16 v[164:167], a[180:183], a[168:171], v[164:167]
buffer_load_dwordx4 v242, s[8:11], s45 offen offset:2048 lds
v_mfma_f32_16x16x32_bf16 v[168:171], a[184:187], a[168:171], v[168:171]
v_mfma_f32_16x16x32_bf16 v[172:175], a[188:191], a[168:171], v[172:175]
v_mfma_f32_16x16x32_bf16 v[176:179], a[176:179], a[172:175], v[176:179]
buffer_load_dwordx4 v243, s[8:11], s45 offen offset:3072 lds
v_mfma_f32_16x16x32_bf16 v[180:183], a[180:183], a[172:175], v[180:183]
s_add_u32 s45, s45, 131072
v_mfma_f32_16x16x32_bf16 v[184:187], a[184:187], a[172:175], v[184:187]
v_mfma_f32_16x16x32_bf16 v[188:191], a[188:191], a[172:175], v[188:191]
s_waitcnt lgkmcnt(0)
v_mfma_f32_16x16x32_bf16 v[128:131], a[144:147], a[128:131], v[128:131]
s_mov_b32 m0, s49
ds_read_b64_tr_b16 a[176:177], v245 offset:8192
ds_read_b64_tr_b16 a[178:179], v245 offset:10240
v_mfma_f32_16x16x32_bf16 v[132:135], a[148:151], a[128:131], v[132:135]
buffer_load_dwordx4 v7, s[4:7], s44 offen offset:0 sc1 lds
v_mfma_f32_16x16x32_bf16 v[136:139], a[152:155], a[128:131], v[136:139]
ds_read_b128 a[160:163], v244 offset:2048
ds_read_b128 a[164:167], v244 offset:2304
v_mfma_f32_16x16x32_bf16 v[140:143], a[156:159], a[128:131], v[140:143]
ds_read_b64_tr_b16 a[180:181], v246 offset:8192
ds_read_b64_tr_b16 a[182:183], v246 offset:10240
v_mfma_f32_16x16x32_bf16 v[144:147], a[144:147], a[132:135], v[144:147]
buffer_load_dwordx4 v7, s[4:7], s44 offen offset:1024 sc1 lds
v_mfma_f32_16x16x32_bf16 v[148:151], a[148:151], a[132:135], v[148:151]
ds_read_b64_tr_b16 a[184:185], v247 offset:8192
ds_read_b64_tr_b16 a[186:187], v247 offset:10240
v_mfma_f32_16x16x32_bf16 v[152:155], a[152:155], a[132:135], v[152:155]
ds_read_b128 a[168:171], v244 offset:6144
ds_read_b128 a[172:175], v244 offset:6400
v_mfma_f32_16x16x32_bf16 v[156:159], a[156:159], a[132:135], v[156:159]
v_mfma_f32_16x16x32_bf16 v[160:163], a[144:147], a[136:139], v[160:163]
s_cmp_eq_u32 s46, 0
s_cselect_b32 s53, s56, s57
ds_read_b64_tr_b16 a[188:189], v248 offset:8192
ds_read_b64_tr_b16 a[190:191], v248 offset:10240
v_mfma_f32_16x16x32_bf16 v[164:167], a[148:151], a[136:139], v[164:167]
v_mfma_f32_16x16x32_bf16 v[168:171], a[152:155], a[136:139], v[168:171]
s_add_u32 s49, s46, s91
s_add_u32 s46, s46, s53
v_add_u32 v244, s53, v244
v_mfma_f32_16x16x32_bf16 v[172:175], a[156:159], a[136:139], v[172:175]
buffer_load_dwordx4 v7, s[4:7], s44 offen offset:2048 sc1 lds
v_mfma_f32_16x16x32_bf16 v[176:179], a[144:147], a[140:143], v[176:179]
v_add_u32 v245, s53, v245
v_add_u32 v246, s53, v246
v_mfma_f32_16x16x32_bf16 v[180:183], a[148:151], a[140:143], v[180:183]
buffer_load_dwordx4 v7, s[4:7], s44 offen offset:3072 sc1 lds
v_mfma_f32_16x16x32_bf16 v[184:187], a[152:155], a[140:143], v[184:187]
v_add_u32 v247, s53, v247
v_add_u32 v248, s53, v248
v_mfma_f32_16x16x32_bf16 v[188:191], a[156:159], a[140:143], v[188:191]
s_waitcnt vmcnt(24) lgkmcnt(0)
s_barrier
v_mfma_f32_16x16x32_bf16 v[128:131], a[176:179], a[160:163], v[128:131]
ds_read_b64_tr_b16 a[144:145], v245 offset:0
ds_read_b64_tr_b16 a[146:147], v245 offset:2048
v_mfma_f32_16x16x32_bf16 v[132:135], a[180:183], a[160:163], v[132:135]
v_mfma_f32_16x16x32_bf16 v[136:139], a[184:187], a[160:163], v[136:139]
ds_read_b128 a[128:131], v244 offset:0
ds_read_b128 a[132:135], v244 offset:256
v_mfma_f32_16x16x32_bf16 v[140:143], a[188:191], a[160:163], v[140:143]
ds_read_b64_tr_b16 a[148:149], v246 offset:0
ds_read_b64_tr_b16 a[150:151], v246 offset:2048
v_mfma_f32_16x16x32_bf16 v[144:147], a[176:179], a[164:167], v[144:147]
v_mfma_f32_16x16x32_bf16 v[148:151], a[180:183], a[164:167], v[148:151]
ds_read_b64_tr_b16 a[152:153], v247 offset:0
ds_read_b64_tr_b16 a[154:155], v247 offset:2048
v_mfma_f32_16x16x32_bf16 v[152:155], a[184:187], a[164:167], v[152:155]
ds_read_b128 a[136:139], v244 offset:4096
ds_read_b128 a[140:143], v244 offset:4352
v_mfma_f32_16x16x32_bf16 v[156:159], a[188:191], a[164:167], v[156:159]
v_mfma_f32_16x16x32_bf16 v[160:163], a[176:179], a[168:171], v[160:163]
ds_read_b64_tr_b16 a[156:157], v248 offset:0
ds_read_b64_tr_b16 a[158:159], v248 offset:2048
v_mfma_f32_16x16x32_bf16 v[164:167], a[180:183], a[168:171], v[164:167]
v_mfma_f32_16x16x32_bf16 v[168:171], a[184:187], a[168:171], v[168:171]
v_mfma_f32_16x16x32_bf16 v[172:175], a[188:191], a[168:171], v[172:175]
v_mfma_f32_16x16x32_bf16 v[176:179], a[176:179], a[172:175], v[176:179]
v_mfma_f32_16x16x32_bf16 v[180:183], a[180:183], a[172:175], v[180:183]
v_mfma_f32_16x16x32_bf16 v[184:187], a[184:187], a[172:175], v[184:187]
v_mfma_f32_16x16x32_bf16 v[188:191], a[188:191], a[172:175], v[188:191]
s_waitcnt lgkmcnt(0)
v_mfma_f32_16x16x32_bf16 v[128:131], a[144:147], a[128:131], v[128:131]
ds_read_b64_tr_b16 a[176:177], v245 offset:8192
ds_read_b64_tr_b16 a[178:179], v245 offset:10240
v_mfma_f32_16x16x32_bf16 v[132:135], a[148:151], a[128:131], v[132:135]
v_mfma_f32_16x16x32_bf16 v[136:139], a[152:155], a[128:131], v[136:139]
ds_read_b128 a[160:163], v244 offset:2048
ds_read_b128 a[164:167], v244 offset:2304
v_mfma_f32_16x16x32_bf16 v[140:143], a[156:159], a[128:131], v[140:143]
ds_read_b64_tr_b16 a[180:181], v246 offset:8192
ds_read_b64_tr_b16 a[182:183], v246 offset:10240
v_mfma_f32_16x16x32_bf16 v[144:147], a[144:147], a[132:135], v[144:147]
v_mfma_f32_16x16x32_bf16 v[148:151], a[148:151], a[132:135], v[148:151]
ds_read_b64_tr_b16 a[184:185], v247 offset:8192
ds_read_b64_tr_b16 a[186:187], v247 offset:10240
v_mfma_f32_16x16x32_bf16 v[152:155], a[152:155], a[132:135], v[152:155]
ds_read_b128 a[168:171], v244 offset:6144
ds_read_b128 a[172:175], v244 offset:6400
v_mfma_f32_16x16x32_bf16 v[156:159], a[156:159], a[132:135], v[156:159]
v_mfma_f32_16x16x32_bf16 v[160:163], a[144:147], a[136:139], v[160:163]
s_cmp_eq_u32 s46, 0
s_cselect_b32 s53, s56, s57
ds_read_b64_tr_b16 a[188:189], v248 offset:8192
ds_read_b64_tr_b16 a[190:191], v248 offset:10240
v_mfma_f32_16x16x32_bf16 v[164:167], a[148:151], a[136:139], v[164:167]
v_mfma_f32_16x16x32_bf16 v[168:171], a[152:155], a[136:139], v[168:171]
s_add_u32 s49, s46, s91
s_add_u32 s46, s46, s53
v_add_u32 v244, s53, v244
v_mfma_f32_16x16x32_bf16 v[172:175], a[156:159], a[136:139], v[172:175]
v_mfma_f32_16x16x32_bf16 v[176:179], a[144:147], a[140:143], v[176:179]
v_add_u32 v245, s53, v245
v_add_u32 v246, s53, v246
v_mfma_f32_16x16x32_bf16 v[180:183], a[148:151], a[140:143], v[180:183]
v_mfma_f32_16x16x32_bf16 v[184:187], a[152:155], a[140:143], v[184:187]
v_add_u32 v247, s53, v247
v_add_u32 v248, s53, v248
v_mfma_f32_16x16x32_bf16 v[188:191], a[156:159], a[140:143], v[188:191]
s_waitcnt vmcnt(16) lgkmcnt(0)
s_barrier
v_mfma_f32_16x16x32_bf16 v[128:131], a[176:179], a[160:163], v[128:131]
ds_read_b64_tr_b16 a[144:145], v245 offset:0
ds_read_b64_tr_b16 a[146:147], v245 offset:2048
v_mfma_f32_16x16x32_bf16 v[132:135], a[180:183], a[160:163], v[132:135]
v_mfma_f32_16x16x32_bf16 v[136:139], a[184:187], a[160:163], v[136:139]
ds_read_b128 a[128:131], v244 offset:0
ds_read_b128 a[132:135], v244 offset:256
v_mfma_f32_16x16x32_bf16 v[140:143], a[188:191], a[160:163], v[140:143]
ds_read_b64_tr_b16 a[148:149], v246 offset:0
ds_read_b64_tr_b16 a[150:151], v246 offset:2048
v_mfma_f32_16x16x32_bf16 v[144:147], a[176:179], a[164:167], v[144:147]
v_mfma_f32_16x16x32_bf16 v[148:151], a[180:183], a[164:167], v[148:151]
ds_read_b64_tr_b16 a[152:153], v247 offset:0
ds_read_b64_tr_b16 a[154:155], v247 offset:2048
v_mfma_f32_16x16x32_bf16 v[152:155], a[184:187], a[164:167], v[152:155]
ds_read_b128 a[136:139], v244 offset:4096
ds_read_b128 a[140:143], v244 offset:4352
v_mfma_f32_16x16x32_bf16 v[156:159], a[188:191], a[164:167], v[156:159]
v_mfma_f32_16x16x32_bf16 v[160:163], a[176:179], a[168:171], v[160:163]
ds_read_b64_tr_b16 a[156:157], v248 offset:0
ds_read_b64_tr_b16 a[158:159], v248 offset:2048
v_mfma_f32_16x16x32_bf16 v[164:167], a[180:183], a[168:171], v[164:167]
v_mfma_f32_16x16x32_bf16 v[168:171], a[184:187], a[168:171], v[168:171]
v_mfma_f32_16x16x32_bf16 v[172:175], a[188:191], a[168:171], v[172:175]
v_mfma_f32_16x16x32_bf16 v[176:179], a[176:179], a[172:175], v[176:179]
v_mfma_f32_16x16x32_bf16 v[180:183], a[180:183], a[172:175], v[180:183]
v_mfma_f32_16x16x32_bf16 v[184:187], a[184:187], a[172:175], v[184:187]
v_mfma_f32_16x16x32_bf16 v[188:191], a[188:191], a[172:175], v[188:191]
s_waitcnt lgkmcnt(0)
v_mfma_f32_16x16x32_bf16 v[128:131], a[144:147], a[128:131], v[128:131]
ds_read_b64_tr_b16 a[176:177], v245 offset:8192
ds_read_b64_tr_b16 a[178:179], v245 offset:10240
v_mfma_f32_16x16x32_bf16 v[132:135], a[148:151], a[128:131], v[132:135]
v_mfma_f32_16x16x32_bf16 v[136:139], a[152:155], a[128:131], v[136:139]
ds_read_b128 a[160:163], v244 offset:2048
ds_read_b128 a[164:167], v244 offset:2304
v_mfma_f32_16x16x32_bf16 v[140:143], a[156:159], a[128:131], v[140:143]
ds_read_b64_tr_b16 a[180:181], v246 offset:8192
ds_read_b64_tr_b16 a[182:183], v246 offset:10240
v_mfma_f32_16x16x32_bf16 v[144:147], a[144:147], a[132:135], v[144:147]
v_mfma_f32_16x16x32_bf16 v[148:151], a[148:151], a[132:135], v[148:151]
ds_read_b64_tr_b16 a[184:185], v247 offset:8192
ds_read_b64_tr_b16 a[186:187], v247 offset:10240
v_mfma_f32_16x16x32_bf16 v[152:155], a[152:155], a[132:135], v[152:155]
ds_read_b128 a[168:171], v244 offset:6144
ds_read_b128 a[172:175], v244 offset:6400
v_mfma_f32_16x16x32_bf16 v[156:159], a[156:159], a[132:135], v[156:159]
v_mfma_f32_16x16x32_bf16 v[160:163], a[144:147], a[136:139], v[160:163]
s_cmp_eq_u32 s46, 0
s_cselect_b32 s53, s56, s57
ds_read_b64_tr_b16 a[188:189], v248 offset:8192
ds_read_b64_tr_b16 a[190:191], v248 offset:10240
v_mfma_f32_16x16x32_bf16 v[164:167], a[148:151], a[136:139], v[164:167]
v_mfma_f32_16x16x32_bf16 v[168:171], a[152:155], a[136:139], v[168:171]
s_add_u32 s49, s46, s91
s_add_u32 s46, s46, s53
v_add_u32 v244, s53, v244
v_mfma_f32_16x16x32_bf16 v[172:175], a[156:159], a[136:139], v[172:175]
v_mfma_f32_16x16x32_bf16 v[176:179], a[144:147], a[140:143], v[176:179]
v_add_u32 v245, s53, v245
v_add_u32 v246, s53, v246
v_mfma_f32_16x16x32_bf16 v[180:183], a[148:151], a[140:143], v[180:183]
v_mfma_f32_16x16x32_bf16 v[184:187], a[152:155], a[140:143], v[184:187]
v_add_u32 v247, s53, v247
v_add_u32 v248, s53, v248
v_mfma_f32_16x16x32_bf16 v[188:191], a[156:159], a[140:143], v[188:191]
s_waitcnt vmcnt(8) lgkmcnt(0)
s_barrier
v_mfma_f32_16x16x32_bf16 v[128:131], a[176:179], a[160:163], v[128:131]
ds_read_b64_tr_b16 a[144:145], v245 offset:0
ds_read_b64_tr_b16 a[146:147], v245 offset:2048
v_mfma_f32_16x16x32_bf16 v[132:135], a[180:183], a[160:163], v[132:135]
v_mfma_f32_16x16x32_bf16 v[136:139], a[184:187], a[160:163], v[136:139]
ds_read_b128 a[128:131], v244 offset:0
ds_read_b128 a[132:135], v244 offset:256
v_mfma_f32_16x16x32_bf16 v[140:143], a[188:191], a[160:163], v[140:143]
ds_read_b64_tr_b16 a[148:149], v246 offset:0
ds_read_b64_tr_b16 a[150:151], v246 offset:2048
v_mfma_f32_16x16x32_bf16 v[144:147], a[176:179], a[164:167], v[144:147]
v_mfma_f32_16x16x32_bf16 v[148:151], a[180:183], a[164:167], v[148:151]
ds_read_b64_tr_b16 a[152:153], v247 offset:0
ds_read_b64_tr_b16 a[154:155], v247 offset:2048
v_mfma_f32_16x16x32_bf16 v[152:155], a[184:187], a[164:167], v[152:155]
ds_read_b128 a[136:139], v244 offset:4096
ds_read_b128 a[140:143], v244 offset:4352
v_mfma_f32_16x16x32_bf16 v[156:159], a[188:191], a[164:167], v[156:159]
v_mfma_f32_16x16x32_bf16 v[160:163], a[176:179], a[168:171], v[160:163]
ds_read_b64_tr_b16 a[156:157], v248 offset:0
ds_read_b64_tr_b16 a[158:159], v248 offset:2048
v_mfma_f32_16x16x32_bf16 v[164:167], a[180:183], a[168:171], v[164:167]
v_mfma_f32_16x16x32_bf16 v[168:171], a[184:187], a[168:171], v[168:171]
v_mfma_f32_16x16x32_bf16 v[172:175], a[188:191], a[168:171], v[172:175]
v_mfma_f32_16x16x32_bf16 v[176:179], a[176:179], a[172:175], v[176:179]
v_mfma_f32_16x16x32_bf16 v[180:183], a[180:183], a[172:175], v[180:183]
v_mfma_f32_16x16x32_bf16 v[184:187], a[184:187], a[172:175], v[184:187]
v_mfma_f32_16x16x32_bf16 v[188:191], a[188:191], a[172:175], v[188:191]
s_waitcnt lgkmcnt(0)
v_mfma_f32_16x16x32_bf16 v[128:131], a[144:147], a[128:131], v[128:131]
ds_read_b64_tr_b16 a[176:177], v245 offset:8192
ds_read_b64_tr_b16 a[178:179], v245 offset:10240
v_mfma_f32_16x16x32_bf16 v[132:135], a[148:151], a[128:131], v[132:135]
v_mfma_f32_16x16x32_bf16 v[136:139], a[152:155], a[128:131], v[136:139]
ds_read_b128 a[160:163], v244 offset:2048
ds_read_b128 a[164:167], v244 offset:2304
v_mfma_f32_16x16x32_bf16 v[140:143], a[156:159], a[128:131], v[140:143]
ds_read_b64_tr_b16 a[180:181], v246 offset:8192
ds_read_b64_tr_b16 a[182:183], v246 offset:10240
v_mfma_f32_16x16x32_bf16 v[144:147], a[144:147], a[132:135], v[144:147]
v_mfma_f32_16x16x32_bf16 v[148:151], a[148:151], a[132:135], v[148:151]
ds_read_b64_tr_b16 a[184:185], v247 offset:8192
ds_read_b64_tr_b16 a[186:187], v247 offset:10240
v_mfma_f32_16x16x32_bf16 v[152:155], a[152:155], a[132:135], v[152:155]
ds_read_b128 a[168:171], v244 offset:6144
ds_read_b128 a[172:175], v244 offset:6400
v_mfma_f32_16x16x32_bf16 v[156:159], a[156:159], a[132:135], v[156:159]
v_mfma_f32_16x16x32_bf16 v[160:163], a[144:147], a[136:139], v[160:163]
s_cmp_eq_u32 s46, 0
s_cselect_b32 s53, s56, s57
ds_read_b64_tr_b16 a[188:189], v248 offset:8192
ds_read_b64_tr_b16 a[190:191], v248 offset:10240
v_mfma_f32_16x16x32_bf16 v[164:167], a[148:151], a[136:139], v[164:167]
v_mfma_f32_16x16x32_bf16 v[168:171], a[152:155], a[136:139], v[168:171]
s_add_u32 s49, s46, s91
s_add_u32 s46, s46, s53
v_add_u32 v244, s53, v244
v_mfma_f32_16x16x32_bf16 v[172:175], a[156:159], a[136:139], v[172:175]
v_mfma_f32_16x16x32_bf16 v[176:179], a[144:147], a[140:143], v[176:179]
v_add_u32 v245, s53, v245
v_add_u32 v246, s53, v246
v_mfma_f32_16x16x32_bf16 v[180:183], a[148:151], a[140:143], v[180:183]
v_mfma_f32_16x16x32_bf16 v[184:187], a[152:155], a[140:143], v[184:187]
v_add_u32 v247, s53, v247
v_add_u32 v248, s53, v248
v_mfma_f32_16x16x32_bf16 v[188:191], a[156:159], a[140:143], v[188:191]
s_waitcnt vmcnt(0) lgkmcnt(0)
s_barrier
v_mfma_f32_16x16x32_bf16 v[128:131], a[176:179], a[160:163], v[128:131]
ds_read_b64_tr_b16 a[144:145], v245 offset:0
ds_read_b64_tr_b16 a[146:147], v245 offset:2048
v_mfma_f32_16x16x32_bf16 v[132:135], a[180:183], a[160:163], v[132:135]
v_mfma_f32_16x16x32_bf16 v[136:139], a[184:187], a[160:163], v[136:139]
ds_read_b128 a[128:131], v244 offset:0
ds_read_b128 a[132:135], v244 offset:256
v_mfma_f32_16x16x32_bf16 v[140:143], a[188:191], a[160:163], v[140:143]
ds_read_b64_tr_b16 a[148:149], v246 offset:0
ds_read_b64_tr_b16 a[150:151], v246 offset:2048
v_mfma_f32_16x16x32_bf16 v[144:147], a[176:179], a[164:167], v[144:147]
v_mfma_f32_16x16x32_bf16 v[148:151], a[180:183], a[164:167], v[148:151]
ds_read_b64_tr_b16 a[152:153], v247 offset:0
ds_read_b64_tr_b16 a[154:155], v247 offset:2048
v_mfma_f32_16x16x32_bf16 v[152:155], a[184:187], a[164:167], v[152:155]
ds_read_b128 a[136:139], v244 offset:4096
ds_read_b128 a[140:143], v244 offset:4352
v_mfma_f32_16x16x32_bf16 v[156:159], a[188:191], a[164:167], v[156:159]
v_mfma_f32_16x16x32_bf16 v[160:163], a[176:179], a[168:171], v[160:163]
ds_read_b64_tr_b16 a[156:157], v248 offset:0
ds_read_b64_tr_b16 a[158:159], v248 offset:2048
v_mfma_f32_16x16x32_bf16 v[164:167], a[180:183], a[168:171], v[164:167]
v_mfma_f32_16x16x32_bf16 v[168:171], a[184:187], a[168:171], v[168:171]
v_mfma_f32_16x16x32_bf16 v[172:175], a[188:191], a[168:171], v[172:175]
v_mfma_f32_16x16x32_bf16 v[176:179], a[176:179], a[172:175], v[176:179]
v_mfma_f32_16x16x32_bf16 v[180:183], a[180:183], a[172:175], v[180:183]
v_mfma_f32_16x16x32_bf16 v[184:187], a[184:187], a[172:175], v[184:187]
v_mfma_f32_16x16x32_bf16 v[188:191], a[188:191], a[172:175], v[188:191]
s_waitcnt lgkmcnt(0)
v_mfma_f32_16x16x32_bf16 v[128:131], a[144:147], a[128:131], v[128:131]
ds_read_b64_tr_b16 a[176:177], v245 offset:8192
ds_read_b64_tr_b16 a[178:179], v245 offset:10240
v_mfma_f32_16x16x32_bf16 v[132:135], a[148:151], a[128:131], v[132:135]
v_mfma_f32_16x16x32_bf16 v[136:139], a[152:155], a[128:131], v[136:139]
ds_read_b128 a[160:163], v244 offset:2048
ds_read_b128 a[164:167], v244 offset:2304
v_mfma_f32_16x16x32_bf16 v[140:143], a[156:159], a[128:131], v[140:143]
ds_read_b64_tr_b16 a[180:181], v246 offset:8192
ds_read_b64_tr_b16 a[182:183], v246 offset:10240
v_mfma_f32_16x16x32_bf16 v[144:147], a[144:147], a[132:135], v[144:147]
v_mfma_f32_16x16x32_bf16 v[148:151], a[148:151], a[132:135], v[148:151]
ds_read_b64_tr_b16 a[184:185], v247 offset:8192
ds_read_b64_tr_b16 a[186:187], v247 offset:10240
v_mfma_f32_16x16x32_bf16 v[152:155], a[152:155], a[132:135], v[152:155]
ds_read_b128 a[168:171], v244 offset:6144
ds_read_b128 a[172:175], v244 offset:6400
v_mfma_f32_16x16x32_bf16 v[156:159], a[156:159], a[132:135], v[156:159]
v_mfma_f32_16x16x32_bf16 v[160:163], a[144:147], a[136:139], v[160:163]
s_cmp_eq_u32 s46, 0
s_cselect_b32 s53, s56, s57
ds_read_b64_tr_b16 a[188:189], v248 offset:8192
ds_read_b64_tr_b16 a[190:191], v248 offset:10240
v_mfma_f32_16x16x32_bf16 v[164:167], a[148:151], a[136:139], v[164:167]
v_mfma_f32_16x16x32_bf16 v[168:171], a[152:155], a[136:139], v[168:171]
s_add_u32 s49, s46, s91
s_add_u32 s46, s46, s53
v_add_u32 v244, s53, v244
v_mfma_f32_16x16x32_bf16 v[172:175], a[156:159], a[136:139], v[172:175]
v_mfma_f32_16x16x32_bf16 v[176:179], a[144:147], a[140:143], v[176:179]
v_add_u32 v245, s53, v245
v_add_u32 v246, s53, v246
v_mfma_f32_16x16x32_bf16 v[180:183], a[148:151], a[140:143], v[180:183]
v_mfma_f32_16x16x32_bf16 v[184:187], a[152:155], a[140:143], v[184:187]
v_add_u32 v247, s53, v247
v_add_u32 v248, s53, v248
v_mfma_f32_16x16x32_bf16 v[188:191], a[156:159], a[140:143], v[188:191]
s_waitcnt  lgkmcnt(0)
s_barrier
v_mfma_f32_16x16x32_bf16 v[128:131], a[176:179], a[160:163], v[128:131]
v_mfma_f32_16x16x32_bf16 v[132:135], a[180:183], a[160:163], v[132:135]
v_mfma_f32_16x16x32_bf16 v[136:139], a[184:187], a[160:163], v[136:139]
v_mfma_f32_16x16x32_bf16 v[140:143], a[188:191], a[160:163], v[140:143]
v_mfma_f32_16x16x32_bf16 v[144:147], a[176:179], a[164:167], v[144:147]
v_mfma_f32_16x16x32_bf16 v[148:151], a[180:183], a[164:167], v[148:151]
v_mfma_f32_16x16x32_bf16 v[152:155], a[184:187], a[164:167], v[152:155]
v_mfma_f32_16x16x32_bf16 v[156:159], a[188:191], a[164:167], v[156:159]
v_mfma_f32_16x16x32_bf16 v[160:163], a[176:179], a[168:171], v[160:163]
v_mfma_f32_16x16x32_bf16 v[164:167], a[180:183], a[168:171], v[164:167]
v_mfma_f32_16x16x32_bf16 v[168:171], a[184:187], a[168:171], v[168:171]
v_mfma_f32_16x16x32_bf16 v[172:175], a[188:191], a[168:171], v[172:175]
v_mfma_f32_16x16x32_bf16 v[176:179], a[176:179], a[172:175], v[176:179]
v_mfma_f32_16x16x32_bf16 v[180:183], a[180:183], a[172:175], v[180:183]
v_mfma_f32_16x16x32_bf16 v[184:187], a[184:187], a[172:175], v[184:187]
v_mfma_f32_16x16x32_bf16 v[188:191], a[188:191], a[172:175], v[188:191]
v_cvt_pk_bf16_f32 v192, v128, v129
v_cvt_pk_bf16_f32 v194, v132, v133
v_cvt_pk_bf16_f32 v193, v130, v131
v_cvt_pk_bf16_f32 v195, v134, v135
v_cvt_pk_bf16_f32 v196, v136, v137
v_cvt_pk_bf16_f32 v198, v140, v141
v_cvt_pk_bf16_f32 v197, v138, v139
v_cvt_pk_bf16_f32 v199, v142, v143
v_permlane16_swap_b32 v192, v194
v_permlane16_swap_b32 v193, v195
v_permlane16_swap_b32 v196, v198
v_permlane16_swap_b32 v197, v199
v_mov_b32 v200, v192
v_mov_b32 v201, v193
v_mov_b32 v202, v194
v_mov_b32 v203, v195
v_mov_b32_dpp v192, v196 row_ror:8 row_mask:0xf bank_mask:0xc
v_mov_b32_dpp v193, v197 row_ror:8 row_mask:0xf bank_mask:0xc
v_mov_b32_dpp v194, v198 row_ror:8 row_mask:0xf bank_mask:0xc
v_mov_b32_dpp v195, v199 row_ror:8 row_mask:0xf bank_mask:0xc
v_mov_b32_dpp v196, v200 row_ror:8 row_mask:0xf bank_mask:0x3
v_mov_b32_dpp v197, v201 row_ror:8 row_mask:0xf bank_mask:0x3
v_mov_b32_dpp v198, v202 row_ror:8 row_mask:0xf bank_mask:0x3
v_mov_b32_dpp v199, v203 row_ror:8 row_mask:0xf bank_mask:0x3
s_mov_b32 s28, 0
s_mov_b32 s29, 32768
buffer_store_dwordx4 v[192:195], v251, s[16:19], s28 offen
buffer_store_dwordx4 v[196:199], v251, s[16:19], s29 offen
v_cvt_pk_bf16_f32 v204, v144, v145
v_cvt_pk_bf16_f32 v206, v148, v149
v_cvt_pk_bf16_f32 v205, v146, v147
v_cvt_pk_bf16_f32 v207, v150, v151
v_cvt_pk_bf16_f32 v208, v152, v153
v_cvt_pk_bf16_f32 v210, v156, v157
v_cvt_pk_bf16_f32 v209, v154, v155
v_cvt_pk_bf16_f32 v211, v158, v159
v_permlane16_swap_b32 v204, v206
v_permlane16_swap_b32 v205, v207
v_permlane16_swap_b32 v208, v210
v_permlane16_swap_b32 v209, v211
v_mov_b32 v212, v204
v_mov_b32 v213, v205
v_mov_b32 v214, v206
v_mov_b32 v215, v207
v_mov_b32_dpp v204, v208 row_ror:8 row_mask:0xf bank_mask:0xc
v_mov_b32_dpp v205, v209 row_ror:8 row_mask:0xf bank_mask:0xc
v_mov_b32_dpp v206, v210 row_ror:8 row_mask:0xf bank_mask:0xc
v_mov_b32_dpp v207, v211 row_ror:8 row_mask:0xf bank_mask:0xc
v_mov_b32_dpp v208, v212 row_ror:8 row_mask:0xf bank_mask:0x3
v_mov_b32_dpp v209, v213 row_ror:8 row_mask:0xf bank_mask:0x3
v_mov_b32_dpp v210, v214 row_ror:8 row_mask:0xf bank_mask:0x3
v_mov_b32_dpp v211, v215 row_ror:8 row_mask:0xf bank_mask:0x3
s_mov_b32 s28, 65536
s_mov_b32 s29, 98304
buffer_store_dwordx4 v[204:207], v251, s[16:19], s28 offen
buffer_store_dwordx4 v[208:211], v251, s[16:19], s29 offen
v_cvt_pk_bf16_f32 v192, v160, v161
v_cvt_pk_bf16_f32 v194, v164, v165
v_cvt_pk_bf16_f32 v193, v162, v163
v_cvt_pk_bf16_f32 v195, v166, v167
v_cvt_pk_bf16_f32 v196, v168, v169
v_cvt_pk_bf16_f32 v198, v172, v173
v_cvt_pk_bf16_f32 v197, v170, v171
v_cvt_pk_bf16_f32 v199, v174, v175
v_permlane16_swap_b32 v192, v194
v_permlane16_swap_b32 v193, v195
v_permlane16_swap_b32 v196, v198
v_permlane16_swap_b32 v197, v199
v_mov_b32 v200, v192
v_mov_b32 v201, v193
v_mov_b32 v202, v194
v_mov_b32 v203, v195
v_mov_b32_dpp v192, v196 row_ror:8 row_mask:0xf bank_mask:0xc
v_mov_b32_dpp v193, v197 row_ror:8 row_mask:0xf bank_mask:0xc
v_mov_b32_dpp v194, v198 row_ror:8 row_mask:0xf bank_mask:0xc
v_mov_b32_dpp v195, v199 row_ror:8 row_mask:0xf bank_mask:0xc
v_mov_b32_dpp v196, v200 row_ror:8 row_mask:0xf bank_mask:0x3
v_mov_b32_dpp v197, v201 row_ror:8 row_mask:0xf bank_mask:0x3
v_mov_b32_dpp v198, v202 row_ror:8 row_mask:0xf bank_mask:0x3
v_mov_b32_dpp v199, v203 row_ror:8 row_mask:0xf bank_mask:0x3
s_mov_b32 s28, 131072
s_mov_b32 s29, 163840
buffer_store_dwordx4 v[192:195], v251, s[16:19], s28 offen
buffer_store_dwordx4 v[196:199], v251, s[16:19], s29 offen
v_cvt_pk_bf16_f32 v204, v176, v177
v_cvt_pk_bf16_f32 v206, v180, v181
v_cvt_pk_bf16_f32 v205, v178, v179
v_cvt_pk_bf16_f32 v207, v182, v183
v_cvt_pk_bf16_f32 v208, v184, v185
v_cvt_pk_bf16_f32 v210, v188, v189
v_cvt_pk_bf16_f32 v209, v186, v187
v_cvt_pk_bf16_f32 v211, v190, v191
v_permlane16_swap_b32 v204, v206
v_permlane16_swap_b32 v205, v207
v_permlane16_swap_b32 v208, v210
v_permlane16_swap_b32 v209, v211
v_mov_b32 v212, v204
v_mov_b32 v213, v205
v_mov_b32 v214, v206
v_mov_b32 v215, v207
v_mov_b32_dpp v204, v208 row_ror:8 row_mask:0xf bank_mask:0xc
v_mov_b32_dpp v205, v209 row_ror:8 row_mask:0xf bank_mask:0xc
v_mov_b32_dpp v206, v210 row_ror:8 row_mask:0xf bank_mask:0xc
v_mov_b32_dpp v207, v211 row_ror:8 row_mask:0xf bank_mask:0xc
v_mov_b32_dpp v208, v212 row_ror:8 row_mask:0xf bank_mask:0x3
v_mov_b32_dpp v209, v213 row_ror:8 row_mask:0xf bank_mask:0x3
v_mov_b32_dpp v210, v214 row_ror:8 row_mask:0xf bank_mask:0x3
v_mov_b32_dpp v211, v215 row_ror:8 row_mask:0xf bank_mask:0x3
s_mov_b32 s28, 196608
s_mov_b32 s29, 229376
buffer_store_dwordx4 v[204:207], v251, s[16:19], s28 offen
buffer_store_dwordx4 v[208:211], v251, s[16:19], s29 offen
.Lend:
s_endpgm
.Lspin1:
s_mov_b32 s93, 0
.Lspin1_l:
s_sleep 4
buffer_load_dword v8, v3, s[60:63], s90 offen sc1
s_waitcnt vmcnt(0)
v_and_b32 v0, 7, v8
v_cmp_lt_u32 s[28:29], v0, v12
v_cmp_eq_u32 s[30:31], v8, v2
s_nop 0
s_or_b64 s[28:29], s[28:29], s[30:31]
s_cbranch_scc0 .Lspin1_ret
s_add_u32 s93, s93, 1
s_cmp_lt_u32 s93, 0x100000
s_cbranch_scc1 .Lspin1_l
s_branch .Lspin1_ret
.Lj0a:
s_lshl_b32 s28, s3, 5
v_add_u32 v13, s28, v8
v_cmp_gt_i32 s[32:33], 0, v13
v_nop
v_cmp_gt_i32 s[34:35], 1, v13
v_cndmask_b32 v160, v160, v34, s[32:33]
v_cmp_gt_i32 s[32:33], 2, v13
v_cndmask_b32 v161, v161, v34, s[34:35]
v_cmp_gt_i32 s[34:35], 3, v13
v_cndmask_b32 v162, v162, v34, s[32:33]
v_cmp_gt_i32 s[32:33], 8, v13
v_cndmask_b32 v163, v163, v34, s[34:35]
v_cmp_gt_i32 s[34:35], 9, v13
v_cndmask_b32 v164, v164, v34, s[32:33]
v_cmp_gt_i32 s[32:33], 10, v13
v_cndmask_b32 v165, v165, v34, s[34:35]
v_cmp_gt_i32 s[34:35], 11, v13
v_cndmask_b32 v166, v166, v34, s[32:33]
v_cmp_gt_i32 s[32:33], 16, v13
v_cndmask_b32 v167, v167, v34, s[34:35]
v_cmp_gt_i32 s[34:35], 17, v13
v_cndmask_b32 v168, v168, v34, s[32:33]
v_cmp_gt_i32 s[32:33], 18, v13
v_cndmask_b32 v169, v169, v34, s[34:35]
v_cmp_gt_i32 s[34:35], 19, v13
v_cndmask_b32 v170, v170, v34, s[32:33]
v_cmp_gt_i32 s[32:33], 24, v13
v_cndmask_b32 v171, v171, v34, s[34:35]
v_cmp_gt_i32 s[34:35], 25, v13
v_cndmask_b32 v172, v172, v34, s[32:33]
v_cmp_gt_i32 s[32:33], 26, v13
v_cndmask_b32 v173, v173, v34, s[34:35]
v_cmp_gt_i32 s[34:35], 27, v13
v_cndmask_b32 v174, v174, v34, s[32:33]
v_nop
v_cndmask_b32 v175, v175, v34, s[34:35]
s_cmp_eq_u32 s89, 0
s_branch .Lj0a_ret
.Lj0b:
v_mov_b32 v14, v208
v_subrev_u32 v13, 32, v13
v_cmp_gt_i32 s[32:33], 0, v13
v_nop
v_cmp_gt_i32 s[34:35], 1, v13
v_cndmask_b32 v208, v208, v34, s[32:33]
v_cmp_gt_i32 s[32:33], 2, v13
v_cndmask_b32 v209, v209, v34, s[34:35]
v_cmp_gt_i32 s[34:35], 3, v13
v_cndmask_b32 v210, v210, v34, s[32:33]
v_cmp_gt_i32 s[32:33], 8, v13
v_cndmask_b32 v211, v211, v34, s[34:35]
v_cmp_gt_i32 s[34:35], 9, v13
v_cndmask_b32 v212, v212, v34, s[32:33]
v_cmp_gt_i32 s[32:33], 10, v13
v_cndmask_b32 v213, v213, v34, s[34:35]
v_cmp_gt_i32 s[34:35], 11, v13
v_cndmask_b32 v214, v214, v34, s[32:33]
v_cmp_gt_i32 s[32:33], 16, v13
v_cndmask_b32 v215, v215, v34, s[34:35]
v_cmp_gt_i32 s[34:35], 17, v13
v_cndmask_b32 v216, v216, v34, s[32:33]
v_cmp_gt_i32 s[32:33], 18, v13
v_cndmask_b32 v217, v217, v34, s[34:35]
v_cmp_gt_i32 s[34:35], 19, v13
v_cndmask_b32 v218, v218, v34, s[32:33]
v_cmp_gt_i32 s[32:33], 24, v13
v_cndmask_b32 v219, v219, v34, s[34:35]
v_cmp_gt_i32 s[34:35], 25, v13
v_cndmask_b32 v220, v220, v34, s[32:33]
v_cmp_gt_i32 s[32:33], 26, v13
v_cndmask_b32 v221, v221, v34, s[34:35]
v_cmp_gt_i32 s[34:35], 27, v13
v_cndmask_b32 v222, v222, v34, s[32:33]
v_nop
v_cndmask_b32 v223, v223, v34, s[34:35]
s_branch .Lj0b_ret
.Lj0c:
v_subrev_u32 v13, 32, v13
v_cmp_gt_i32 s[32:33], 0, v13
v_nop
v_cmp_gt_i32 s[34:35], 1, v13
v_cndmask_b32 v208, v208, v34, s[32:33]
v_cmp_gt_i32 s[32:33], 2, v13
v_cndmask_b32 v209, v209, v34, s[34:35]
v_cmp_gt_i32 s[34:35], 3, v13
v_cndmask_b32 v210, v210, v34, s[32:33]
v_cmp_gt_i32 s[32:33], 8, v13
v_cndmask_b32 v211, v211, v34, s[34:35]
v_cmp_gt_i32 s[34:35], 9, v13
v_cndmask_b32 v212, v212, v34, s[32:33]
v_cmp_gt_i32 s[32:33], 10, v13
v_cndmask_b32 v213, v213, v34, s[34:35]
v_cmp_gt_i32 s[34:35], 11, v13
v_cndmask_b32 v214, v214, v34, s[32:33]
v_cmp_gt_i32 s[32:33], 16, v13
v_cndmask_b32 v215, v215, v34, s[34:35]
v_cmp_gt_i32 s[34:35], 17, v13
v_cndmask_b32 v216, v216, v34, s[32:33]
v_cmp_gt_i32 s[32:33], 18, v13
v_cndmask_b32 v217, v217, v34, s[34:35]
v_cmp_gt_i32 s[34:35], 19, v13
v_cndmask_b32 v218, v218, v34, s[32:33]
v_cmp_gt_i32 s[32:33], 24, v13
v_cndmask_b32 v219, v219, v34, s[34:35]
v_cmp_gt_i32 s[34:35], 25, v13
v_cndmask_b32 v220, v220, v34, s[32:33]
v_cmp_gt_i32 s[32:33], 26, v13
v_cndmask_b32 v221, v221, v34, s[34:35]
v_cmp_gt_i32 s[34:35], 27, v13
v_cndmask_b32 v222, v222, v34, s[32:33]
v_nop
v_cndmask_b32 v223, v223, v34, s[34:35]
v_subrev_u32 v13, 32, v13
s_branch .Lj0c_ret
.Lh_d1_c:
s_swappc_b64 s[30:31], s[58:59]
.Lh_d1_r:
s_branch .Lh_d1
.Lh_t0a_c:
s_swappc_b64 s[30:31], s[58:59]
.Lh_t0a_r:
s_branch .Lh_t0a
.Lh_t0b_c:
s_swappc_b64 s[30:31], s[58:59]
.Lh_t0b_r:
s_branch .Lh_t0b
.Lh_t1a_c:
s_swappc_b64 s[30:31], s[58:59]
.Lh_t1a_r:
s_branch .Lh_t1a
.Lh_t1b_c:
s_swappc_b64 s[30:31], s[58:59]
.Lh_t1b_r:
s_branch .Lh_t1b
.Lspin21:
s_mov_b32 s93, 0
.Lspin21_l:
s_sleep 4
buffer_load_dword v9, v10, s[60:63], 0 offen sc1
s_waitcnt vmcnt(0)
v_cmp_gt_u32 vcc, 1, v9
s_cmp_lg_u64 vcc, 0
s_cbranch_scc0 .Lspin21_ret
s_add_u32 s93, s93, 1
s_cmp_lt_u32 s93, 0x100000
s_cbranch_scc1 .Lspin21_l
s_branch .Lspin21_ret
.Ljob0:
global_load_dword v35, v[2:3], off
v_add_co_u32 v2, vcc, 0x20000, v2
s_nop 1
v_addc_co_u32 v3, vcc, 0, v3, vcc
s_mov_b32 s57, 0
s_swappc_b64 s[58:59], s[30:31]
.Ljob1:
global_load_dword v35, v[2:3], off
v_add_co_u32 v2, vcc, 0x20000, v2
s_nop 1
v_addc_co_u32 v3, vcc, 0, v3, vcc
s_waitcnt vmcnt(10)
v_mov_b32 v0, 1
buffer_store_dword v0, v5, s[60:63], 0 offen sc1
s_mov_b32 s57, 0
s_swappc_b64 s[58:59], s[30:31]
.Ljob2:
global_load_dword v35, v[2:3], off
v_add_co_u32 v2, vcc, 0x20000, v2
s_nop 1
v_addc_co_u32 v3, vcc, 0, v3, vcc
s_mov_b32 s57, 0
s_swappc_b64 s[58:59], s[30:31]
.Ljob3:
global_load_dword v35, v[2:3], off
v_add_co_u32 v2, vcc, 0x20000, v2
s_nop 1
v_addc_co_u32 v3, vcc, 0, v3, vcc
s_mov_b32 s57, 0
s_swappc_b64 s[58:59], s[30:31]
.Ljob4:
global_load_dword v35, v[2:3], off
v_add_co_u32 v2, vcc, 0x20000, v2
s_nop 1
v_addc_co_u32 v3, vcc, 0, v3, vcc
s_mov_b32 s57, 0
s_swappc_b64 s[58:59], s[30:31]
.Ljob5:
global_load_dword v35, v[2:3], off
v_add_co_u32 v2, vcc, 0x20000, v2
s_nop 1
v_addc_co_u32 v3, vcc, 0, v3, vcc
s_mov_b32 s57, 0
s_swappc_b64 s[58:59], s[30:31]
.Ljob6:
v_and_b32 v10, 7, v1
v_and_b32 v9, 8, v1
v_lshl_or_b32 v10, v9, 1, v10
s_and_b32 s93, s87, 2
s_lshl_b32 s93, s93, 2
v_or_b32 v10, s93, v10
v_lshlrev_b32 v10, 9, v10
v_bfe_u32 v9, v1, 4, 1
v_lshl_or_b32 v10, v9, 2, v10
s_lshl_b32 s93, s86, 6
v_add_u32 v10, s93, v10
s_and_b32 s93, s87, 1
s_lshl_b32 s93, s93, 3
s_add_u32 s93, s93, 0x10000
v_add_u32 v10, s93, v10
buffer_load_dword v9, v10, s[60:63], 0 offen sc1
global_load_dword v35, v[2:3], off
v_add_co_u32 v2, vcc, 0x20000, v2
s_nop 1
v_addc_co_u32 v3, vcc, 0, v3, vcc
s_mov_b32 s57, 0
s_swappc_b64 s[58:59], s[30:31]
.Ljob7:
s_waitcnt vmcnt(5)
v_cmp_gt_u32 vcc, 1, v9
s_cmp_lg_u64 vcc, 0
s_cbranch_scc1 .Lspin21
.Lspin21_ret:
global_load_dword v35, v[2:3], off
v_add_co_u32 v2, vcc, 0x20000, v2
s_nop 1
v_addc_co_u32 v3, vcc, 0, v3, vcc
s_mov_b32 s57, 2147483647
s_swappc_b64 s[58:59], s[30:31]
.Lx:
s_sub_u32 s4, s4, s91
s_subb_u32 s5, s5, 0
s_mov_b32 s6, 0x80000
s_sub_u32 s8, s8, s91
s_subb_u32 s9, s9, 0
s_mov_b32 s10, 0x80000
s_mov_b64 s[38:39], s[32:33]
v_add_u32 v222, 0xffff0000, v32
s_cmp_eq_u64 s[30:31], 0
s_cbranch_scc1 .Lx_s0
v_mov_b32 v224, v64
v_mov_b32 v225, v65
v_mov_b32 v226, v66
v_mov_b32 v227, v67
v_mov_b32 v228, v68
v_mov_b32 v229, v69
v_mov_b32 v230, v70
v_mov_b32 v231, v71
v_mov_b32 v232, v72
v_mov_b32 v233, v73
v_mov_b32 v234, v74
v_mov_b32 v235, v75
v_mov_b32 v236, v76
v_mov_b32 v237, v77
v_mov_b32 v238, v78
v_mov_b32 v239, v79
v_mov_b32 v240, v80
v_mov_b32 v241, v81
v_mov_b32 v242, v82
v_mov_b32 v243, v83
v_mov_b32 v244, v84
v_mov_b32 v245, v85
v_mov_b32 v246, v86
v_mov_b32 v247, v87
v_mov_b32 v248, v88
v_mov_b32 v249, v89
v_mov_b32 v250, v90
v_mov_b32 v251, v91
v_mov_b32 v252, v92
v_mov_b32 v253, v93
v_mov_b32 v254, v94
v_mov_b32 v255, v95
s_sub_u32 s40, 15, s89
s_lshl_b32 s40, s40, 2
s_add_u32 s40, s40, s3
s_getpc_b64 s[42:43]
.Lx_p0:
s_add_u32 s42, s42, .Lx_r0-.Lx_p0
s_addc_u32 s43, s43, 0
s_branch .Lx_core
.Lx_r0:
v_accvgpr_write_b32 a64, v128
v_accvgpr_write_b32 a65, v129
v_accvgpr_write_b32 a66, v130
v_accvgpr_write_b32 a67, v131
v_accvgpr_write_b32 a68, v132
v_accvgpr_write_b32 a69, v133
v_accvgpr_write_b32 a70, v134
v_accvgpr_write_b32 a71, v135
v_accvgpr_write_b32 a72, v136
v_accvgpr_write_b32 a73, v137
v_accvgpr_write_b32 a74, v138
v_accvgpr_write_b32 a75, v139
v_accvgpr_write_b32 a76, v140
v_accvgpr_write_b32 a77, v141
v_accvgpr_write_b32 a78, v142
v_accvgpr_write_b32 a79, v143
v_accvgpr_write_b32 a80, v144
v_accvgpr_write_b32 a81, v145
v_accvgpr_write_b32 a82, v146
v_accvgpr_write_b32 a83, v147
v_accvgpr_write_b32 a84, v148
v_accvgpr_write_b32 a85, v149
v_accvgpr_write_b32 a86, v150
v_accvgpr_write_b32 a87, v151
v_accvgpr_write_b32 a88, v152
v_accvgpr_write_b32 a89, v153
v_accvgpr_write_b32 a90, v154
v_accvgpr_write_b32 a91, v155
v_accvgpr_write_b32 a92, v156
v_accvgpr_write_b32 a93, v157
v_accvgpr_write_b32 a94, v158
v_accvgpr_write_b32 a95, v159
v_accvgpr_write_b32 a112, v160
v_accvgpr_write_b32 a113, v161
v_accvgpr_write_b32 a114, v162
v_accvgpr_write_b32 a115, v163
v_accvgpr_write_b32 a116, v164
v_accvgpr_write_b32 a117, v165
v_accvgpr_write_b32 a118, v166
v_accvgpr_write_b32 a119, v167
v_accvgpr_write_b32 a120, v168
v_accvgpr_write_b32 a121, v169
v_accvgpr_write_b32 a122, v170
v_accvgpr_write_b32 a123, v171
v_accvgpr_write_b32 a124, v172
v_accvgpr_write_b32 a125, v173
v_accvgpr_write_b32 a126, v174
v_accvgpr_write_b32 a127, v175
v_accvgpr_write_b32 a144, v176
v_accvgpr_write_b32 a145, v177
v_accvgpr_write_b32 a146, v178
v_accvgpr_write_b32 a147, v179
v_accvgpr_write_b32 a148, v180
v_accvgpr_write_b32 a149, v181
v_accvgpr_write_b32 a150, v182
v_accvgpr_write_b32 a151, v183
v_accvgpr_write_b32 a152, v184
v_accvgpr_write_b32 a153, v185
v_accvgpr_write_b32 a154, v186
v_accvgpr_write_b32 a155, v187
v_accvgpr_write_b32 a156, v188
v_accvgpr_write_b32 a157, v189
v_accvgpr_write_b32 a158, v190
v_accvgpr_write_b32 a159, v191
v_mov_b32 v60, v220
.Lx_s0:
s_cmp_eq_u64 s[38:39], 0
s_cbranch_scc1 .Lx_s1
v_mov_b32 v224, v96
v_mov_b32 v225, v97
v_mov_b32 v226, v98
v_mov_b32 v227, v99
v_mov_b32 v228, v100
v_mov_b32 v229, v101
v_mov_b32 v230, v102
v_mov_b32 v231, v103
v_mov_b32 v232, v104
v_mov_b32 v233, v105
v_mov_b32 v234, v106
v_mov_b32 v235, v107
v_mov_b32 v236, v108
v_mov_b32 v237, v109
v_mov_b32 v238, v110
v_mov_b32 v239, v111
v_mov_b32 v240, v112
v_mov_b32 v241, v113
v_mov_b32 v242, v114
v_mov_b32 v243, v115
v_mov_b32 v244, v116
v_mov_b32 v245, v117
v_mov_b32 v246, v118
v_mov_b32 v247, v119
v_mov_b32 v248, v120
v_mov_b32 v249, v121
v_mov_b32 v250, v122
v_mov_b32 v251, v123
v_mov_b32 v252, v124
v_mov_b32 v253, v125
v_mov_b32 v254, v126
v_mov_b32 v255, v127
s_mov_b32 s40, s89
s_lshl_b32 s40, s40, 2
s_add_u32 s40, s40, s3
s_getpc_b64 s[42:43]
.Lx_p1:
s_add_u32 s42, s42, .Lx_r1-.Lx_p1
s_addc_u32 s43, s43, 0
s_branch .Lx_core
.Lx_r1:
v_accvgpr_write_b32 a176, v128
v_accvgpr_write_b32 a177, v129
v_accvgpr_write_b32 a178, v130
v_accvgpr_write_b32 a179, v131
v_accvgpr_write_b32 a180, v132
v_accvgpr_write_b32 a181, v133
v_accvgpr_write_b32 a182, v134
v_accvgpr_write_b32 a183, v135
v_accvgpr_write_b32 a184, v136
v_accvgpr_write_b32 a185, v137
v_accvgpr_write_b32 a186, v138
v_accvgpr_write_b32 a187, v139
v_accvgpr_write_b32 a188, v140
v_accvgpr_write_b32 a189, v141
v_accvgpr_write_b32 a190, v142
v_accvgpr_write_b32 a191, v143
v_accvgpr_write_b32 a208, v144
v_accvgpr_write_b32 a209, v145
v_accvgpr_write_b32 a210, v146
v_accvgpr_write_b32 a211, v147
v_accvgpr_write_b32 a212, v148
v_accvgpr_write_b32 a213, v149
v_accvgpr_write_b32 a214, v150
v_accvgpr_write_b32 a215, v151
v_accvgpr_write_b32 a216, v152
v_accvgpr_write_b32 a217, v153
v_accvgpr_write_b32 a218, v154
v_accvgpr_write_b32 a219, v155
v_accvgpr_write_b32 a220, v156
v_accvgpr_write_b32 a221, v157
v_accvgpr_write_b32 a222, v158
v_accvgpr_write_b32 a223, v159
v_accvgpr_write_b32 a240, v160
v_accvgpr_write_b32 a241, v161
v_accvgpr_write_b32 a242, v162
v_accvgpr_write_b32 a243, v163
v_accvgpr_write_b32 a244, v164
v_accvgpr_write_b32 a245, v165
v_accvgpr_write_b32 a246, v166
v_accvgpr_write_b32 a247, v167
v_accvgpr_write_b32 a248, v168
v_accvgpr_write_b32 a249, v169
v_accvgpr_write_b32 a250, v170
v_accvgpr_write_b32 a251, v171
v_accvgpr_write_b32 a252, v172
v_accvgpr_write_b32 a253, v173
v_accvgpr_write_b32 a254, v174
v_accvgpr_write_b32 a255, v175
v_accvgpr_write_b32 a96, v176
v_accvgpr_write_b32 a97, v177
v_accvgpr_write_b32 a98, v178
v_accvgpr_write_b32 a99, v179
v_accvgpr_write_b32 a100, v180
v_accvgpr_write_b32 a101, v181
v_accvgpr_write_b32 a102, v182
v_accvgpr_write_b32 a103, v183
v_accvgpr_write_b32 a104, v184
v_accvgpr_write_b32 a105, v185
v_accvgpr_write_b32 a106, v186
v_accvgpr_write_b32 a107, v187
v_accvgpr_write_b32 a108, v188
v_accvgpr_write_b32 a109, v189
v_accvgpr_write_b32 a110, v190
v_accvgpr_write_b32 a111, v191
v_mov_b32 v61, v220
.Lx_s1:
s_branch .Lx_ret
.Lx_core:
s_lshl_b32 s41, s40, 5
v_and_b32 v221, 31, v1
v_lshrrev_b32 v218, 5, v1
v_lshlrev_b32 v218, 2, v218
v_sub_u32 v221, v221, v218
v_add_u32 v221, s41, v221
v_mov_b32 v216, v34
v_mov_b32 v220, 0
s_mov_b32 s44, 0
s_mov_b32 s45, 4096
s_mov_b32 s46, 0
v_mov_b32 v128, 0
v_mov_b32 v129, 0
v_mov_b32 v130, 0
v_mov_b32 v131, 0
v_mov_b32 v132, 0
v_mov_b32 v133, 0
v_mov_b32 v134, 0
v_mov_b32 v135, 0
v_mov_b32 v136, 0
v_mov_b32 v137, 0
v_mov_b32 v138, 0
v_mov_b32 v139, 0
v_mov_b32 v140, 0
v_mov_b32 v141, 0
v_mov_b32 v142, 0
v_mov_b32 v143, 0
v_mov_b32 v144, 0
v_mov_b32 v145, 0
v_mov_b32 v146, 0
v_mov_b32 v147, 0
v_mov_b32 v148, 0
v_mov_b32 v149, 0
v_mov_b32 v150, 0
v_mov_b32 v151, 0
v_mov_b32 v152, 0
v_mov_b32 v153, 0
v_mov_b32 v154, 0
v_mov_b32 v155, 0
v_mov_b32 v156, 0
v_mov_b32 v157, 0
v_mov_b32 v158, 0
v_mov_b32 v159, 0
v_mov_b32 v160, 0
v_mov_b32 v161, 0
v_mov_b32 v162, 0
v_mov_b32 v163, 0
v_mov_b32 v164, 0
v_mov_b32 v165, 0
v_mov_b32 v166, 0
v_mov_b32 v167, 0
v_mov_b32 v168, 0
v_mov_b32 v169, 0
v_mov_b32 v170, 0
v_mov_b32 v171, 0
v_mov_b32 v172, 0
v_mov_b32 v173, 0
v_mov_b32 v174, 0
v_mov_b32 v175, 0
v_mov_b32 v176, 0
v_mov_b32 v177, 0
v_mov_b32 v178, 0
v_mov_b32 v179, 0
v_mov_b32 v180, 0
v_mov_b32 v181, 0
v_mov_b32 v182, 0
v_mov_b32 v183, 0
v_mov_b32 v184, 0
v_mov_b32 v185, 0
v_mov_b32 v186, 0
v_mov_b32 v187, 0
v_mov_b32 v188, 0
v_mov_b32 v189, 0
v_mov_b32 v190, 0
v_mov_b32 v191, 0
.Lx_loop:
buffer_load_dwordx4 a[0:3], v222, s[4:7], s44 offen offset:0 sc1
buffer_load_dwordx4 a[4:7], v222, s[4:7], s44 offen offset:512 sc1
buffer_load_dwordx4 a[8:11], v222, s[4:7], s44 offen offset:1024 sc1
buffer_load_dwordx4 a[12:15], v222, s[4:7], s44 offen offset:1536 sc1
buffer_load_dwordx4 a[16:19], v222, s[4:7], s44 offen offset:2048 sc1
buffer_load_dwordx4 a[20:23], v222, s[4:7], s44 offen offset:2560 sc1
buffer_load_dwordx4 a[24:27], v222, s[4:7], s44 offen offset:3072 sc1
buffer_load_dwordx4 a[28:31], v222, s[4:7], s44 offen offset:3584 sc1
buffer_load_dwordx4 a[32:35], v33, s[8:11], s44 offen offset:0 sc1
buffer_load_dwordx4 a[36:39], v33, s[8:11], s44 offen offset:1024 sc1
buffer_load_dwordx4 a[40:43], v33, s[8:11], s44 offen offset:2048 sc1
buffer_load_dwordx4 a[44:47], v33, s[8:11], s44 offen offset:3072 sc1
buffer_load_dwordx4 a[48:51], v33, s[8:11], s45 offen offset:0 sc1
buffer_load_dwordx4 a[52:55], v33, s[8:11], s45 offen offset:1024 sc1
buffer_load_dwordx4 a[56:59], v33, s[8:11], s45 offen offset:2048 sc1
buffer_load_dwordx4 a[60:63], v33, s[8:11], s45 offen offset:3072 sc1
s_waitcnt vmcnt(8)
v_mfma_f32_32x32x16_bf16 v[192:207], a[0:3], v[224:227], 0
v_mfma_f32_32x32x16_bf16 v[192:207], a[4:7], v[228:231], v[192:207]
v_mfma_f32_32x32x16_bf16 v[192:207], a[8:11], v[232:235], v[192:207]
v_mfma_f32_32x32x16_bf16 v[192:207], a[12:15], v[236:239], v[192:207]
v_mfma_f32_32x32x16_bf16 v[192:207], a[16:19], v[240:243], v[192:207]
v_mfma_f32_32x32x16_bf16 v[192:207], a[20:23], v[244:247], v[192:207]
v_mfma_f32_32x32x16_bf16 v[192:207], a[24:27], v[248:251], v[192:207]
v_mfma_f32_32x32x16_bf16 v[192:207], a[28:31], v[252:255], v[192:207]
v_cmp_gt_i32 s[32:33], 0, v221
v_nop
v_cmp_gt_i32 s[34:35], 1, v221
s_nop 8
v_cndmask_b32 v192, v192, v34, s[32:33]
v_cmp_gt_i32 s[32:33], 2, v221
v_cndmask_b32 v193, v193, v34, s[34:35]
v_cmp_gt_i32 s[34:35], 3, v221
v_cndmask_b32 v194, v194, v34, s[32:33]
v_cmp_gt_i32 s[32:33], 8, v221
v_cndmask_b32 v195, v195, v34, s[34:35]
v_cmp_gt_i32 s[34:35], 9, v221
v_cndmask_b32 v196, v196, v34, s[32:33]
v_cmp_gt_i32 s[32:33], 10, v221
v_cndmask_b32 v197, v197, v34, s[34:35]
v_cmp_gt_i32 s[34:35], 11, v221
v_cndmask_b32 v198, v198, v34, s[32:33]
v_cmp_gt_i32 s[32:33], 16, v221
v_cndmask_b32 v199, v199, v34, s[34:35]
v_cmp_gt_i32 s[34:35], 17, v221
v_cndmask_b32 v200, v200, v34, s[32:33]
v_cmp_gt_i32 s[32:33], 18, v221
v_cndmask_b32 v201, v201, v34, s[34:35]
v_cmp_gt_i32 s[34:35], 19, v221
v_cndmask_b32 v202, v202, v34, s[32:33]
v_cmp_gt_i32 s[32:33], 24, v221
v_cndmask_b32 v203, v203, v34, s[34:35]
v_cmp_gt_i32 s[34:35], 25, v221
v_cndmask_b32 v204, v204, v34, s[32:33]
v_cmp_gt_i32 s[32:33], 26, v221
v_cndmask_b32 v205, v205, v34, s[34:35]
v_cmp_gt_i32 s[34:35], 27, v221
v_cndmask_b32 v206, v206, v34, s[32:33]
v_nop
v_cndmask_b32 v207, v207, v34, s[34:35]
v_max3_f32 v217, v192, v193, v216
v_max3_f32 v217, v217, v194, v195
v_max3_f32 v217, v217, v196, v197
v_max3_f32 v217, v217, v198, v199
v_max3_f32 v217, v217, v200, v201
v_max3_f32 v217, v217, v202, v203
v_max3_f32 v217, v217, v204, v205
v_max3_f32 v217, v217, v206, v207
v_mov_b32 v218, v217
s_nop 1
v_permlane32_swap_b32 v217, v218
v_max_f32 v217, v217, v218
v_sub_f32 v219, v216, v217
v_exp_f32 v219, v219
v_mov_b32 v216, v217
v_sub_f32 v192, v192, v217
v_sub_f32 v193, v193, v217
v_sub_f32 v194, v194, v217
v_sub_f32 v195, v195, v217
v_sub_f32 v196, v196, v217
v_sub_f32 v197, v197, v217
v_sub_f32 v198, v198, v217
v_sub_f32 v199, v199, v217
v_sub_f32 v200, v200, v217
v_sub_f32 v201, v201, v217
v_sub_f32 v202, v202, v217
v_sub_f32 v203, v203, v217
v_sub_f32 v204, v204, v217
v_sub_f32 v205, v205, v217
v_sub_f32 v206, v206, v217
v_sub_f32 v207, v207, v217
v_exp_f32 v192, v192
v_exp_f32 v193, v193
v_exp_f32 v194, v194
v_exp_f32 v195, v195
v_exp_f32 v196, v196
v_exp_f32 v197, v197
v_exp_f32 v198, v198
v_exp_f32 v199, v199
v_exp_f32 v200, v200
v_exp_f32 v201, v201
v_exp_f32 v202, v202
v_exp_f32 v203, v203
v_exp_f32 v204, v204
v_exp_f32 v205, v205
v_exp_f32 v206, v206
v_exp_f32 v207, v207
v_mul_f32 v220, v220, v219
v_add_f32 v220, v220, v192
v_add_f32 v220, v220, v193
v_add_f32 v220, v220, v194
v_add_f32 v220, v220, v195
v_add_f32 v220, v220, v196
v_add_f32 v220, v220, v197
v_add_f32 v220, v220, v198
v_add_f32 v220, v220, v199
v_add_f32 v220, v220, v200
v_add_f32 v220, v220, v201
v_add_f32 v220, v220, v202
v_add_f32 v220, v220, v203
v_add_f32 v220, v220, v204
v_add_f32 v220, v220, v205
v_add_f32 v220, v220, v206
v_add_f32 v220, v220, v207
v_cvt_pk_bf16_f32 v208, v192, v193
v_cvt_pk_bf16_f32 v209, v194, v195
v_cvt_pk_bf16_f32 v210, v196, v197
v_cvt_pk_bf16_f32 v211, v198, v199
v_cvt_pk_bf16_f32 v212, v200, v201
v_cvt_pk_bf16_f32 v213, v202, v203
v_cvt_pk_bf16_f32 v214, v204, v205
v_cvt_pk_bf16_f32 v215, v206, v207
v_mul_f32 v128, v219, v128
v_mul_f32 v129, v219, v129
v_mul_f32 v130, v219, v130
v_mul_f32 v131, v219, v131
v_mul_f32 v132, v219, v132
v_mul_f32 v133, v219, v133
v_mul_f32 v134, v219, v134
v_mul_f32 v135, v219, v135
v_mul_f32 v136, v219, v136
v_mul_f32 v137, v219, v137
v_mul_f32 v138, v219, v138
v_mul_f32 v139, v219, v139
v_mul_f32 v140, v219, v140
v_mul_f32 v141, v219, v141
v_mul_f32 v142, v219, v142
v_mul_f32 v143, v219, v143
v_mul_f32 v144, v219, v144
v_mul_f32 v145, v219, v145
v_mul_f32 v146, v219, v146
v_mul_f32 v147, v219, v147
v_mul_f32 v148, v219, v148
v_mul_f32 v149, v219, v149
v_mul_f32 v150, v219, v150
v_mul_f32 v151, v219, v151
v_mul_f32 v152, v219, v152
v_mul_f32 v153, v219, v153
v_mul_f32 v154, v219, v154
v_mul_f32 v155, v219, v155
v_mul_f32 v156, v219, v156
v_mul_f32 v157, v219, v157
v_mul_f32 v158, v219, v158
v_mul_f32 v159, v219, v159
v_mul_f32 v160, v219, v160
v_mul_f32 v161, v219, v161
v_mul_f32 v162, v219, v162
v_mul_f32 v163, v219, v163
v_mul_f32 v164, v219, v164
v_mul_f32 v165, v219, v165
v_mul_f32 v166, v219, v166
v_mul_f32 v167, v219, v167
v_mul_f32 v168, v219, v168
v_mul_f32 v169, v219, v169
v_mul_f32 v170, v219, v170
v_mul_f32 v171, v219, v171
v_mul_f32 v172, v219, v172
v_mul_f32 v173, v219, v173
v_mul_f32 v174, v219, v174
v_mul_f32 v175, v219, v175
v_mul_f32 v176, v219, v176
v_mul_f32 v177, v219, v177
v_mul_f32 v178, v219, v178
v_mul_f32 v179, v219, v179
v_mul_f32 v180, v219, v180
v_mul_f32 v181, v219, v181
v_mul_f32 v182, v219, v182
v_mul_f32 v183, v219, v183
v_mul_f32 v184, v219, v184
v_mul_f32 v185, v219, v185
v_mul_f32 v186, v219, v186
v_mul_f32 v187, v219, v187
v_mul_f32 v188, v219, v188
v_mul_f32 v189, v219, v189
v_mul_f32 v190, v219, v190
v_mul_f32 v191, v219, v191
s_waitcnt vmcnt(0)
v_mfma_f32_32x32x16_bf16 v[128:143], a[32:35], v[208:211], v[128:143]
v_mfma_f32_32x32x16_bf16 v[144:159], a[36:39], v[208:211], v[144:159]
v_mfma_f32_32x32x16_bf16 v[160:175], a[40:43], v[208:211], v[160:175]
v_mfma_f32_32x32x16_bf16 v[176:191], a[44:47], v[208:211], v[176:191]
v_mfma_f32_32x32x16_bf16 v[128:143], a[48:51], v[212:215], v[128:143]
v_mfma_f32_32x32x16_bf16 v[144:159], a[52:55], v[212:215], v[144:159]
v_mfma_f32_32x32x16_bf16 v[160:175], a[56:59], v[212:215], v[160:175]
v_mfma_f32_32x32x16_bf16 v[176:191], a[60:63], v[212:215], v[176:191]
v_subrev_u32 v221, 32, v221
s_add_u32 s44, s44, 8192
s_add_u32 s45, s45, 8192
s_add_u32 s46, s46, 1
s_cmp_le_u32 s46, s40
s_cbranch_scc1 .Lx_loop
v_mov_b32 v218, v220
s_nop 1
v_permlane32_swap_b32 v220, v218
v_add_f32 v220, v220, v218
s_setpc_b64 s[42:43]
.Lspin3:
s_mov_b32 s93, 0
.Lspin3_l:
s_sleep 4
buffer_load_dword v250, v249, s[60:63], s90 offen sc1
s_waitcnt vmcnt(0)
v_and_b32 v250, 7, v250
v_cmp_gt_u32 vcc, 3, v250
s_cmp_lg_u64 vcc, 0
s_cbranch_scc0 .Lspin3_ret
s_add_u32 s93, s93, 1
s_cmp_lt_u32 s93, 0x100000
s_cbranch_scc1 .Lspin3_l
s_branch .Lspin3_ret
.Lspin5:
s_mov_b32 s93, 0
.Lspin5_l:
s_sleep 4
buffer_load_dword v250, v249, s[60:63], s90 offen sc1
s_waitcnt vmcnt(0)
v_and_b32 v250, 7, v250
v_cmp_gt_u32 vcc, 5, v250
s_cmp_lg_u64 vcc, 0
s_cbranch_scc0 .Lspin5_ret
s_add_u32 s93, s93, 1
s_cmp_lt_u32 s93, 0x100000
s_cbranch_scc1 .Lspin5_l
s_branch .Lspin5_ret
.Lspin4:
s_mov_b32 s93, 0
.Lspin4_l:
s_sleep 4
buffer_load_dword v250, v249, s[60:63], s90 offen sc1
s_waitcnt vmcnt(0)
v_and_b32 v250, 7, v250
v_cmp_gt_u32 vcc, 4, v250
s_cmp_lg_u64 vcc, 0
s_cbranch_scc0 .Lspin4_ret
s_add_u32 s93, s93, 1
s_cmp_lt_u32 s93, 0x100000
s_cbranch_scc1 .Lspin4_l
s_branch .Lspin4_ret
.Lcode1:
)ASM"
    :: "{s[0:1]}"(__builtin_amdgcn_kernarg_segment_ptr()), "{s2}"(blockIdx.x), "{v0}"(threadIdx.x),
       "{s3}"((unsigned)(size_t)(__attribute__((address_space(3))) char*)smem)
    : "v0", "v1", "v2", "v3", "v4", "v5", "v6", "v7", "v8", "v9", "v10", "v11", "v12", "v13", "v14", "v15", "v16", "v17", "v18", "v19", "v20", "v21", "v22", "v23", "v24", "v25", "v26", "v27", "v28", "v29", "v30", "v31", "v32", "v33", "v34", "v35", "v36", "v37", "v38", "v39", "v40", "v41", "v42", "v43", "v44", "v45", "v46", "v47", "v48", "v49", "v50", "v51", "v52", "v53", "v54", "v55", "v56", "v57", "v58", "v59", "v60", "v61", "v62", "v63", "v64", "v65", "v66", "v67", "v68", "v69", "v70", "v71", "v72", "v73", "v74", "v75", "v76", "v77", "v78", "v79", "v80", "v81", "v82", "v83", "v84", "v85", "v86", "v87", "v88", "v89", "v90", "v91", "v92", "v93", "v94", "v95", "v96", "v97", "v98", "v99", "v100", "v101", "v102", "v103", "v104", "v105", "v106", "v107", "v108", "v109", "v110", "v111", "v112", "v113", "v114", "v115", "v116", "v117", "v118", "v119", "v120", "v121", "v122", "v123", "v124", "v125", "v126", "v127", "v128", "v129", "v130", "v131", "v132", "v133", "v134", "v135", "v136", "v137", "v138", "v139", "v140", "v141", "v142", "v143", "v144", "v145", "v146", "v147", "v148", "v149", "v150", "v151", "v152", "v153", "v154", "v155", "v156", "v157", "v158", "v159", "v160", "v161", "v162", "v163", "v164", "v165", "v166", "v167", "v168", "v169", "v170", "v171", "v172", "v173", "v174", "v175", "v176", "v177", "v178", "v179", "v180", "v181", "v182", "v183", "v184", "v185", "v186", "v187", "v188", "v189", "v190", "v191", "v192", "v193", "v194", "v195", "v196", "v197", "v198", "v199", "v200", "v201", "v202", "v203", "v204", "v205", "v206", "v207", "v208", "v209", "v210", "v211", "v212", "v213", "v214", "v215", "v216", "v217", "v218", "v219", "v220", "v221", "v222", "v223", "v224", "v225", "v226", "v227", "v228", "v229", "v230", "v231", "v232", "v233", "v234", "v235", "v236", "v237", "v238", "v239", "v240", "v241", "v242", "v243", "v244", "v245", "v246", "v247", "v248", "v249", "v250", "v251", "v252", "v253", "v254", "v255", "a0", "a1", "a2", "a3", "a4", "a5", "a6", "a7", "a8", "a9", "a10", "a11", "a12", "a13", "a14", "a15", "a16", "a17", "a18", "a19", "a20", "a21", "a22", "a23", "a24", "a25", "a26", "a27", "a28", "a29", "a30", "a31", "a32", "a33", "a34", "a35", "a36", "a37", "a38", "a39", "a40", "a41", "a42", "a43", "a44", "a45", "a46", "a47", "a48", "a49", "a50", "a51", "a52", "a53", "a54", "a55", "a56", "a57", "a58", "a59", "a60", "a61", "a62", "a63", "a64", "a65", "a66", "a67", "a68", "a69", "a70", "a71", "a72", "a73", "a74", "a75", "a76", "a77", "a78", "a79", "a80", "a81", "a82", "a83", "a84", "a85", "a86", "a87", "a88", "a89", "a90", "a91", "a92", "a93", "a94", "a95", "a96", "a97", "a98", "a99", "a100", "a101", "a102", "a103", "a104", "a105", "a106", "a107", "a108", "a109", "a110", "a111", "a112", "a113", "a114", "a115", "a116", "a117", "a118", "a119", "a120", "a121", "a122", "a123", "a124", "a125", "a126", "a127", "a128", "a129", "a130", "a131", "a132", "a133", "a134", "a135", "a136", "a137", "a138", "a139", "a140", "a141", "a142", "a143", "a144", "a145", "a146", "a147", "a148", "a149", "a150", "a151", "a152", "a153", "a154", "a155", "a156", "a157", "a158", "a159", "a160", "a161", "a162", "a163", "a164", "a165", "a166", "a167", "a168", "a169", "a170", "a171", "a172", "a173", "a174", "a175", "a176", "a177", "a178", "a179", "a180", "a181", "a182", "a183", "a184", "a185", "a186", "a187", "a188", "a189", "a190", "a191", "a192", "a193", "a194", "a195", "a196", "a197", "a198", "a199", "a200", "a201", "a202", "a203", "a204", "a205", "a206", "a207", "a208", "a209", "a210", "a211", "a212", "a213", "a214", "a215", "a216", "a217", "a218", "a219", "a220", "a221", "a222", "a223", "a224", "a225", "a226", "a227", "a228", "a229", "a230", "a231", "a232", "a233", "a234", "a235", "a236", "a237", "a238", "a239", "a240", "a241", "a242", "a243", "a244", "a245", "a246", "a247", "a248", "a249", "a250", "a251", "a252", "a253", "a254", "a255", "s3", "s4", "s5", "s6", "s7", "s8", "s9", "s10", "s11", "s12", "s13", "s14", "s15", "s16", "s17", "s18", "s19", "s20", "s21", "s22", "s23", "s24", "s25", "s26", "s27", "s28", "s29", "s30", "s31", "s32", "s33", "s34", "s35", "s36", "s37", "s38", "s39", "s40", "s41", "s42", "s43", "s44", "s45", "s46", "s47", "s48", "s49", "s50", "s51", "s52", "s53", "s54", "s55", "s56", "s57", "s58", "s59", "s60", "s61", "s62", "s63", "s64", "s65", "s66", "s67", "s68", "s69", "s70", "s71", "s72", "s73", "s74", "s75", "s76", "s77", "s78", "s79", "s80", "s81", "s82", "s83", "s84", "s85", "s86", "s87", "s88", "s89", "s90", "s91", "s92", "s93", "s94", "s95", "s96", "s97", "s98", "s99", "s100", "s101", "vcc", "m0", "scc", "memory");
}
extern "C" void kernel_launch(void* const* d_in, const int* in_sizes, int n_in, void* d_out, int out_size,
                              void* d_ws, size_t ws_size, hipStream_t stream) {
  Args a;
  for (int i = 0; i < 9; ++i) a.p[i] = d_in[i];
  a.out = d_out; a.ws = (char*)d_ws + (0 ? ((ws_size - (72u << 20)) & ~(size_t)0xfffff) : 0);
  mega_kernel<<<256, 256, 0, stream>>>(a);
}
```

```cpp
#include <hip/hip_runtime.h>
#include <stdint.h>
struct Args { const void* p[9]; void* out; void* ws; };
__global__ __launch_bounds__(256, 1) __attribute__((aligned(4096))) void mega_kernel(Args args) {
  __shared__ __attribute__((aligned(16))) char smem[163840];
  asm volatile(R"ASM(
s_getpc_b64 s[96:97]
.Lcode0:
s_load_dwordx16 s[64:79], s[0:1], 0
s_load_dwordx4 s[80:83], s[0:1], 64
s_load_dwordx2 s[84:85], s[0:1], 80
v_and_b32 v1, 63, v0
v_lshrrev_b32 v16, 6, v0
v_lshlrev_b32 v7, 4, v1
v_readfirstlane_b32 s3, v16
s_and_b32 s86, s2, 7
s_lshr_b32 s87, s2, 3
s_and_b32 s4, s87, 3
s_lshl_b32 s88, s86, 2
s_add_u32 s88, s88, s4
s_lshr_b32 s89, s2, 5
s_lshl_b32 s91, s3, 12
s_lshl_b32 s92, s3, 13
s_and_b32 s97, s97, 0xffff
s_mov_b32 s98, .Lcode1-.Lcode0
s_mov_b32 s99, 0x20000
s_lshl_b32 s33, s87, 2
s_add_u32 s33, s33, s3
s_lshl_b32 s33, s33, 10
v_and_b32 v4, 7, v1
v_lshlrev_b32 v4, 7, v4
v_add_u32 v4, s33, v4
buffer_load_dword v35, v4, s[96:99], 0 offen
s_and_b32 s27, s87, 7
s_bfe_u32 s28, s87, 0x10003
s_lshl_b32 s29, s86, 1
s_add_u32 s28, s28, s29
v_lshrrev_b32 v40, 3, v1
v_and_b32 v41, 7, v1
v_xor_b32 v41, v41, v40
v_lshlrev_b32 v41, 4, v41
v_lshl_add_u32 v202, v40, 10, v41
v_add_u32 v203, 7168, v202
v_add_u32 v204, 260096, v202
v_add_u32 v205, 267264, v202
v_lshl_add_u32 v20, v40, 11, v41
v_add_u32 v21, 15360, v20
v_add_u32 v22, 30720, v20
v_add_u32 v23, 46080, v20
v_lshrrev_b32 v40, 4, v1
v_and_b32 v41, 15, v1
v_lshlrev_b32 v42, 1, v40
v_xor_b32 v41, v41, v42
v_lshlrev_b32 v42, 4, v41
v_lshl_add_u32 v16, v40, 13, v42
v_add_u32 v17, 31744, v16
v_xor_b32 v18, 0x80, v16
v_add_u32 v18, 63488, v18
v_add_u32 v19, 31744, v18
s_and_b32 s30, s3, 1
s_lshr_b32 s31, s3, 1
s_lshl_b32 s32, s30, 20
s_lshl_b32 s33, s27, 15
s_add_u32 s32, s32, s33
s_lshl_b32 s33, s31, 14
s_add_u32 s32, s32, s33
s_lshl_b32 s34, s28, 9
s_lshl_b32 s33, s31, 8
s_add_u32 s34, s34, s33
s_lshl_b32 s33, s30, 18
s_add_u32 s34, s34, s33
s_sub_u32 s35, 0x200000, s32
s_sub_u32 s36, 0x400000, s34
s_cmp_lt_u32 s87, 16
s_cselect_b32 s46, 1, 0
s_waitcnt lgkmcnt(0)
s_cselect_b32 s38, s76, s78
s_cselect_b32 s39, s77, s79
s_add_u32 s8, s38, s34
s_addc_u32 s9, s39, 0
s_and_b32 s9, s9, 0xffff
s_mov_b32 s10, s36
s_mov_b32 s11, 0x20000
s_add_u32 s4, s72, s32
s_addc_u32 s5, s73, 0
s_and_b32 s5, s5, 0xffff
s_mov_b32 s6, s35
s_mov_b32 s7, 0x20000
s_add_u32 s37, s92, 0
s_mov_b32 s42, 0
s_mov_b32 s43, 131072
s_mov_b32 m0, s37
s_nop 0
buffer_load_dwordx4 v16, s[8:11], s42 offen offset:0 nt lds
buffer_load_dwordx4 v17, s[8:11], s42 offen offset:1024 nt lds
buffer_load_dwordx4 v18, s[8:11], s42 offen offset:2048 nt lds
buffer_load_dwordx4 v19, s[8:11], s42 offen offset:3072 nt lds
s_add_u32 m0, s37, 4096
s_nop 0
buffer_load_dwordx4 v16, s[8:11], s43 offen offset:0 nt lds
buffer_load_dwordx4 v17, s[8:11], s43 offen offset:1024 nt lds
buffer_load_dwordx4 v18, s[8:11], s43 offen offset:2048 nt lds
buffer_load_dwordx4 v19, s[8:11], s43 offen offset:3072 nt lds
s_add_u32 s60, s84, 62914560
s_addc_u32 s61, s85, 0
s_and_b32 s61, s61, 0xffff
s_mov_b32 s62, 81920
s_mov_b32 s63, 0x20000
s_lshl_b32 s90, s86, 13
v_lshlrev_b32 v3, 5, v0
s_lshl_b32 s93, s2, 4
s_add_u32 s93, s93, s3
s_lshl_b32 s93, s93, 2
s_add_u32 s37, s92, 32768
s_mov_b32 s40, 0
s_mov_b32 s41, 524288
s_mov_b32 m0, s37
s_nop 0
buffer_load_dwordx4 v202, s[4:7], s40 offen offset:0 lds
s_add_u32 s93, s93, 0x10000
v_lshlrev_b32 v45, 20, v1
v_add_u32 v44, s93, v45
v_mov_b32 v45, 0
s_lshl_b32 s33, s2, 5
v_lshlrev_b32 v41, 13, v0
v_lshrrev_b32 v42, 3, v0
v_lshl_add_u32 v41, v42, 20, v41
v_add_u32 v6, s33, v41
s_and_b32 s33, s87, 2
s_lshl_b32 s33, s33, 5
buffer_load_dwordx4 v203, s[4:7], s40 offen offset:1024 lds
s_or_b32 s33, s33, s86
v_and_b32 v41, 0x47, v0
v_cmp_eq_u32 vcc, s33, v41
s_nop 1
v_cndmask_b32 v14, 0, 1, vcc
s_lshl_b32 s33, s30, 14
s_add_u32 s33, s33, 32768
v_and_b32 v36, 15, v1
v_lshrrev_b32 v37, 4, v1
v_and_b32 v38, 7, v36
v_xor_b32 v38, v38, v37
v_lshlrev_b32 v38, 4, v38
buffer_load_dwordx4 v204, s[4:7], s40 offen offset:2048 lds
v_lshl_add_u32 v192, v36, 7, v38
v_add_u32 v192, s33, v192
v_xor_b32 v193, 64, v192
s_lshl_b32 s33, s31, 14
v_and_b32 v36, 15, v1
v_lshrrev_b32 v37, 4, v1
v_lshrrev_b32 v38, 2, v36
v_and_b32 v39, 3, v36
v_lshrrev_b32 v40, 1, v39
v_xor_b32 v40, v40, v39
v_and_b32 v40, s46, v40
buffer_load_dwordx4 v205, s[4:7], s40 offen offset:3072 lds
s_mov_b64 exec, 0
s_cmp_eq_u32 s3, 0
s_cbranch_scc1 .Lpad1
s_cmp_eq_u32 s3, 1
s_cbranch_scc1 .Lp1_v_pad
s_cmp_eq_u32 s3, 2
s_cbranch_scc1 .Lpad2
s_branch .Lpad3
.Ltch0:
s_mov_b64 exec, -1
v_mul_u32_u24 v40, 3, v40
v_xor_b32 v39, v39, v40
v_lshl_add_u32 v40, v37, 3, v38
v_and_b32 v41, 1, v37
v_lshl_or_b32 v41, v41, 2, v38
v_lshlrev_b32 v40, 8, v40
v_lshl_add_u32 v40, v39, 3, v40
v_add_u32 v40, s33, v40
v_xor_b32 v36, 0, v41
v_lshl_add_u32 v194, v36, 5, v40
v_xor_b32 v36, 1, v41
s_add_u32 m0, s37, 4096
s_nop 0
buffer_load_dwordx4 v202, s[4:7], s41 offen offset:0 lds
v_lshl_add_u32 v195, v36, 5, v40
v_xor_b32 v36, 2, v41
v_lshl_add_u32 v196, v36, 5, v40
v_xor_b32 v36, 3, v41
v_lshl_add_u32 v197, v36, 5, v40
v_xor_b32 v36, 4, v41
v_lshl_add_u32 v198, v36, 5, v40
v_xor_b32 v36, 5, v41
v_lshl_add_u32 v199, v36, 5, v40
v_xor_b32 v36, 6, v41
v_lshl_add_u32 v200, v36, 5, v40
buffer_load_dwordx4 v203, s[4:7], s41 offen offset:1024 lds
v_xor_b32 v36, 7, v41
v_lshl_add_u32 v201, v36, 5, v40
s_and_b32 s95, s27, 3
s_lshl_b32 s33, s95, 6
v_xor_b32 v194, s33, v194
v_xor_b32 v195, s33, v195
v_xor_b32 v196, s33, v196
v_xor_b32 v197, s33, v197
v_xor_b32 v198, s33, v198
v_xor_b32 v199, s33, v199
v_xor_b32 v200, s33, v200
buffer_load_dwordx4 v204, s[4:7], s41 offen offset:2048 lds
v_xor_b32 v201, s33, v201
s_lshl_b32 s95, s95, 10
s_add_u32 s33, s92, 32768
v_and_b32 v36, 15, v1
v_lshrrev_b32 v37, 4, v1
v_and_b32 v38, 7, v36
v_xor_b32 v38, v38, v37
v_lshlrev_b32 v38, 4, v38
v_lshl_add_u32 v24, v36, 7, v38
v_add_u32 v24, s33, v24
v_xor_b32 v25, 64, v24
buffer_load_dwordx4 v205, s[4:7], s41 offen offset:3072 lds
s_mov_b64 exec, 0
s_cmp_eq_u32 s3, 0
s_cbranch_scc1 .Lp2_pad
s_cmp_eq_u32 s3, 1
s_cbranch_scc1 .Lpad4
s_cmp_eq_u32 s3, 2
s_cbranch_scc1 .Lpad5
s_branch .Lpad6
.Ltch1:
s_mov_b64 exec, -1
v_and_b32 v36, 15, v1
v_lshrrev_b32 v37, 4, v1
v_lshrrev_b32 v38, 2, v36
v_and_b32 v39, 3, v36
v_lshl_add_u32 v40, v37, 3, v38
v_and_b32 v41, 1, v37
v_lshl_or_b32 v41, v41, 2, v38
v_lshlrev_b32 v40, 8, v40
v_lshl_add_u32 v40, v39, 3, v40
v_add_u32 v40, 65536, v40
s_add_u32 s37, s92, 65536
s_mov_b32 s42, 524288
s_mov_b32 s43, 655360
s_mov_b32 m0, s37
s_nop 0
buffer_load_dwordx4 v16, s[8:11], s42 offen offset:0 nt lds
v_xor_b32 v36, 0, v41
v_lshl_add_u32 v26, v36, 5, v40
v_xor_b32 v36, 1, v41
v_lshl_add_u32 v27, v36, 5, v40
v_xor_b32 v36, 2, v41
v_lshl_add_u32 v28, v36, 5, v40
v_xor_b32 v36, 3, v41
v_lshl_add_u32 v29, v36, 5, v40
v_xor_b32 v36, 4, v41
v_lshl_add_u32 v30, v36, 5, v40
v_xor_b32 v36, 5, v41
buffer_load_dwordx4 v17, s[8:11], s42 offen offset:1024 nt lds
v_lshl_add_u32 v31, v36, 5, v40
v_xor_b32 v36, 6, v41
v_lshl_add_u32 v32, v36, 5, v40
v_xor_b32 v36, 7, v41
v_lshl_add_u32 v33, v36, 5, v40
s_add_u32 s12, s70, 0
s_addc_u32 s13, s71, 0
s_and_b32 s13, s13, 0xffff
s_mov_b32 s14, 4194304
s_mov_b32 s15, 0x20000
s_lshl_b32 s33, s88, 8
buffer_load_dwordx4 v18, s[8:11], s42 offen offset:2048 nt lds
s_lshl_b32 s34, s3, 17
s_add_u32 s33, s33, s34
s_sub_u32 s34, 0x800000, s33
s_add_u32 s16, s74, s33
s_addc_u32 s17, s75, 0
s_and_b32 s17, s17, 0xffff
s_mov_b32 s18, s34
s_mov_b32 s19, 0x20000
s_sub_u32 s24, 15, s89
s_lshl_b32 s24, s24, 18
s_lshl_b32 s25, s89, 18
buffer_load_dwordx4 v19, s[8:11], s42 offen offset:3072 nt lds
s_mov_b64 exec, 0
s_cmp_eq_u32 s3, 0
s_cbranch_scc1 .Lpad7
s_cmp_eq_u32 s3, 1
s_cbranch_scc1 .Lpad8
s_cmp_eq_u32 s3, 2
s_cbranch_scc1 .Lpad9
s_branch .Lpad10
.Ltch2:
s_mov_b64 exec, -1
s_lshl_b32 s33, s3, 16
s_add_u32 s24, s24, s33
s_add_u32 s25, s25, s33
s_mov_b32 s26, 0
s_lshl_b32 s33, s28, 1
s_add_u32 s33, s33, s31
s_lshl_b32 s33, s33, 19
s_lshl_b32 s34, s27, 1
s_add_u32 s34, s34, s30
s_lshl_b32 s34, s34, 12
s_add_u32 s33, s33, s34
s_add_u32 m0, s37, 4096
s_nop 0
buffer_load_dwordx4 v16, s[8:11], s43 offen offset:0 nt lds
s_xor_b32 s34, s46, 1
s_lshl_b32 s34, s34, 24
s_add_u32 s33, s33, s34
s_add_u32 s20, s84, s33
s_addc_u32 s21, s85, 0
s_and_b32 s21, s21, 0xffff
s_mov_b32 s22, 524288
s_mov_b32 s23, 0x20000
s_cmp_eq_u32 s46, 1
s_cselect_b32 s47, 1040356334, 1.0
s_lshl_b32 s33, s2, 2
buffer_load_dwordx4 v17, s[8:11], s43 offen offset:1024 nt lds
s_add_u32 s33, s33, s3
v_and_b32 v36, 7, v1
s_lshl_b32 s34, s33, 10
v_lshlrev_b32 v36, 7, v36
v_add_u32 v36, s34, v36
v_add_u32 v36, 0x100000, v36
v_bfe_u32 v37, v1, 1, 1
v_and_b32 v38, 1, v1
v_lshlrev_b32 v37, 11, v37
v_lshl_add_u32 v37, v38, 7, v37
s_lshl_b32 s34, s33, 12
buffer_load_dwordx4 v18, s[8:11], s43 offen offset:2048 nt lds
v_add_u32 v37, s34, v37
v_add_u32 v37, 256, v37
v_and_b32 v38, 0x3c, v1
v_cmp_eq_u32 vcc, 8, v38
v_mov_b32 v39, s74
v_mov_b32 v40, s70
v_cndmask_b32 v36, v36, v37, vcc
v_cndmask_b32 v39, v39, v40, vcc
v_mov_b32 v207, s75
v_mov_b32 v40, s71
v_cndmask_b32 v207, v207, v40, vcc
buffer_load_dwordx4 v19, s[8:11], s43 offen offset:3072 nt lds
v_mov_b32 v208, 0x100000
v_mov_b32 v40, 256
v_cndmask_b32 v208, v208, v40, vcc
v_add_co_u32 v206, vcc, v39, v36
s_nop 1
v_addc_co_u32 v207, vcc, 0, v207, vcc
s_mov_b32 s35, s92
s_add_u32 s36, s92, 32768
s_add_u32 s39, s92, 163840
s_mov_b32 s54, 65536
s_mov_b32 s55, -98304
s_mov_b32 s38, 1
s_waitcnt vmcnt(8)
s_barrier
ds_read_b64_tr_b16 v[96:97], v194 offset:0
ds_read_b64_tr_b16 v[98:99], v194 offset:1024
ds_read_b128 v[64:67], v192 offset:0
ds_read_b64_tr_b16 v[100:101], v195 offset:0
ds_read_b64_tr_b16 v[102:103], v195 offset:1024
ds_read_b128 v[68:71], v192 offset:2048
ds_read_b64_tr_b16 v[104:105], v196 offset:0
ds_read_b64_tr_b16 v[106:107], v196 offset:1024
ds_read_b128 v[72:75], v192 offset:4096
ds_read_b64_tr_b16 v[108:109], v197 offset:0
ds_read_b64_tr_b16 v[110:111], v197 offset:1024
ds_read_b128 v[76:79], v192 offset:6144
s_waitcnt lgkmcnt(6)
ds_read_b64_tr_b16 v[112:113], v198 offset:0
ds_read_b64_tr_b16 v[114:115], v198 offset:1024
ds_read_b128 v[80:83], v192 offset:8192
ds_read_b64_tr_b16 v[116:117], v199 offset:0
ds_read_b64_tr_b16 v[118:119], v199 offset:1024
ds_read_b128 v[84:87], v192 offset:10240
ds_read_b64_tr_b16 v[120:121], v200 offset:0
ds_read_b64_tr_b16 v[122:123], v200 offset:1024
ds_read_b128 v[88:91], v192 offset:12288
ds_read_b64_tr_b16 v[124:125], v201 offset:0
ds_read_b64_tr_b16 v[126:127], v201 offset:1024
ds_read_b128 v[92:95], v192 offset:14336
s_cmp_eq_u32 s46, 1
s_waitcnt lgkmcnt(0)
s_cbranch_scc0 .Lp1_v
v_mfma_f32_16x16x32_bf16 a[0:3], v[96:99], v[64:67], 0
ds_read_b64_tr_b16 v[160:161], v194 offset:8192
v_mfma_f32_16x16x32_bf16 a[4:7], v[100:103], v[64:67], 0
s_add_u32 s37, s92, 98304
s_mov_b32 s40, 128
s_mov_b32 s41, 524416
v_mfma_f32_16x16x32_bf16 a[8:11], v[104:107], v[64:67], 0
s_mov_b32 m0, s37
ds_read_b64_tr_b16 v[162:163], v194 offset:9216
v_mfma_f32_16x16x32_bf16 a[12:15], v[108:111], v[64:67], 0
buffer_load_dwordx4 v202, s[4:7], s40 offen offset:0 lds
v_mfma_f32_16x16x32_bf16 a[16:19], v[112:115], v[64:67], 0
ds_read_b128 v[128:131], v193 offset:0
v_mfma_f32_16x16x32_bf16 a[20:23], v[116:119], v[64:67], 0
buffer_load_dwordx4 v203, s[4:7], s40 offen offset:1024 lds
v_mfma_f32_16x16x32_bf16 a[24:27], v[120:123], v[64:67], 0
ds_read_b64_tr_b16 v[164:165], v195 offset:8192
v_mfma_f32_16x16x32_bf16 a[28:31], v[124:127], v[64:67], 0
buffer_load_dwordx4 v204, s[4:7], s40 offen offset:2048 lds
v_mfma_f32_16x16x32_bf16 a[32:35], v[96:99], v[68:71], 0
ds_read_b64_tr_b16 v[166:167], v195 offset:9216
v_mfma_f32_16x16x32_bf16 a[36:39], v[100:103], v[68:71], 0
buffer_load_dwordx4 v205, s[4:7], s40 offen offset:3072 lds
v_mfma_f32_16x16x32_bf16 a[40:43], v[104:107], v[68:71], 0
s_add_u32 m0, s37, 4096
ds_read_b128 v[132:135], v193 offset:2048
v_mfma_f32_16x16x32_bf16 a[44:47], v[108:111], v[68:71], 0
buffer_load_dwordx4 v202, s[4:7], s41 offen offset:0 lds
v_mfma_f32_16x16x32_bf16 a[48:51], v[112:115], v[68:71], 0
ds_read_b64_tr_b16 v[168:169], v196 offset:8192
v_mfma_f32_16x16x32_bf16 a[52:55], v[116:119], v[68:71], 0
buffer_load_dwordx4 v203, s[4:7], s41 offen offset:1024 lds
v_mfma_f32_16x16x32_bf16 a[56:59], v[120:123], v[68:71], 0
ds_read_b64_tr_b16 v[170:171], v196 offset:9216
v_mfma_f32_16x16x32_bf16 a[60:63], v[124:127], v[68:71], 0
buffer_load_dwordx4 v204, s[4:7], s41 offen offset:2048 lds
v_mfma_f32_16x16x32_bf16 a[64:67], v[96:99], v[72:75], 0
ds_read_b128 v[136:139], v193 offset:4096
v_mfma_f32_16x16x32_bf16 a[68:71], v[100:103], v[72:75], 0
buffer_load_dwordx4 v205, s[4:7], s41 offen offset:3072 lds
v_mfma_f32_16x16x32_bf16 a[72:75], v[104:107], v[72:75], 0
ds_read_b64_tr_b16 v[172:173], v197 offset:8192
v_mfma_f32_16x16x32_bf16 a[76:79], v[108:111], v[72:75], 0
s_add_u32 s37, s92, 131072
s_mov_b32 s42, 1048576
s_mov_b32 s43, 1179648
v_mfma_f32_16x16x32_bf16 a[80:83], v[112:115], v[72:75], 0
s_mov_b32 m0, s37
ds_read_b64_tr_b16 v[174:175], v197 offset:9216
v_mfma_f32_16x16x32_bf16 a[84:87], v[116:119], v[72:75], 0
buffer_load_dwordx4 v16, s[8:11], s42 offen offset:0 nt lds
v_mfma_f32_16x16x32_bf16 a[88:91], v[120:123], v[72:75], 0
ds_read_b128 v[140:143], v193 offset:6144
v_mfma_f32_16x16x32_bf16 a[92:95], v[124:127], v[72:75], 0
buffer_load_dwordx4 v17, s[8:11], s42 offen offset:1024 nt lds
v_mfma_f32_16x16x32_bf16 a[96:99], v[96:99], v[76:79], 0
ds_read_b64_tr_b16 v[176:177], v198 offset:8192
v_mfma_f32_16x16x32_bf16 a[100:103], v[100:103], v[76:79], 0
buffer_load_dwordx4 v18, s[8:11], s42 offen offset:2048 nt lds
v_mfma_f32_16x16x32_bf16 a[104:107], v[104:107], v[76:79], 0
ds_read_b64_tr_b16 v[178:179], v198 offset:9216
v_mfma_f32_16x16x32_bf16 a[108:111], v[108:111], v[76:79], 0
buffer_load_dwordx4 v19, s[8:11], s42 offen offset:3072 nt lds
v_mfma_f32_16x16x32_bf16 a[112:115], v[112:115], v[76:79], 0
s_add_u32 m0, s37, 4096
ds_read_b128 v[144:147], v193 offset:8192
v_mfma_f32_16x16x32_bf16 a[116:119], v[116:119], v[76:79], 0
buffer_load_dwordx4 v16, s[8:11], s43 offen offset:0 nt lds
v_mfma_f32_16x16x32_bf16 a[120:123], v[120:123], v[76:79], 0
ds_read_b64_tr_b16 v[180:181], v199 offset:8192
v_mfma_f32_16x16x32_bf16 a[124:127], v[124:127], v[76:79], 0
buffer_load_dwordx4 v17, s[8:11], s43 offen offset:1024 nt lds
v_mfma_f32_16x16x32_bf16 a[128:131], v[96:99], v[80:83], 0
ds_read_b64_tr_b16 v[182:183], v199 offset:9216
v_mfma_f32_16x16x32_bf16 a[132:135], v[100:103], v[80:83], 0
buffer_load_dwordx4 v18, s[8:11], s43 offen offset:2048 nt lds
v_mfma_f32_16x16x32_bf16 a[136:139], v[104:107], v[80:83], 0
ds_read_b128 v[148:151], v193 offset:10240
v_mfma_f32_16x16x32_bf16 a[140:143], v[108:111], v[80:83], 0
buffer_load_dwordx4 v19, s[8:11], s43 offen offset:3072 nt lds
v_mfma_f32_16x16x32_bf16 a[144:147], v[112:115], v[80:83], 0
ds_read_b64_tr_b16 v[184:185], v200 offset:8192
v_mfma_f32_16x16x32_bf16 a[148:151], v[116:119], v[80:83], 0
v_mfma_f32_16x16x32_bf16 a[152:155], v[120:123], v[80:83], 0
ds_read_b64_tr_b16 v[186:187], v200 offset:9216
v_mfma_f32_16x16x32_bf16 a[156:159], v[124:127], v[80:83], 0
v_mfma_f32_16x16x32_bf16 a[160:163], v[96:99], v[84:87], 0
ds_read_b128 v[152:155], v193 offset:12288
v_mfma_f32_16x16x32_bf16 a[164:167], v[100:103], v[84:87], 0
v_mfma_f32_16x16x32_bf16 a[168:171], v[104:107], v[84:87], 0
ds_read_b64_tr_b16 v[188:189], v201 offset:8192
v_mfma_f32_16x16x32_bf16 a[172:175], v[108:111], v[84:87], 0
v_mfma_f32_16x16x32_bf16 a[176:179], v[112:115], v[84:87], 0
s_add_u32 s44, s35, 65536
ds_read_b64_tr_b16 v[190:191], v201 offset:9216
v_mfma_f32_16x16x32_bf16 a[180:183], v[116:119], v[84:87], 0
s_cmp_ge_u32 s44, s39
s_cselect_b32 s45, s55, s54
v_mfma_f32_16x16x32_bf16 a[184:187], v[120:123], v[84:87], 0
ds_read_b128 v[156:159], v193 offset:14336
v_mfma_f32_16x16x32_bf16 a[188:191], v[124:127], v[84:87], 0
s_add_u32 s44, s36, 65536
v_mfma_f32_16x16x32_bf16 a[192:195], v[96:99], v[88:91], 0
v_mfma_f32_16x16x32_bf16 a[196:199], v[100:103], v[88:91], 0
s_cmp_ge_u32 s44, s39
s_cselect_b32 s34, s55, s54
v_mfma_f32_16x16x32_bf16 a[200:203], v[104:107], v[88:91], 0
v_add_u32 v194, s45, v194
v_mfma_f32_16x16x32_bf16 a[204:207], v[108:111], v[88:91], 0
v_add_u32 v195, s45, v195
v_mfma_f32_16x16x32_bf16 a[208:211], v[112:115], v[88:91], 0
v_add_u32 v196, s45, v196
v_mfma_f32_16x16x32_bf16 a[212:215], v[116:119], v[88:91], 0
v_add_u32 v197, s45, v197
v_mfma_f32_16x16x32_bf16 a[216:219], v[120:123], v[88:91], 0
v_add_u32 v198, s45, v198
v_mfma_f32_16x16x32_bf16 a[220:223], v[124:127], v[88:91], 0
v_add_u32 v199, s45, v199
v_mfma_f32_16x16x32_bf16 a[224:227], v[96:99], v[92:95], 0
v_add_u32 v200, s45, v200
v_mfma_f32_16x16x32_bf16 a[228:231], v[100:103], v[92:95], 0
v_add_u32 v201, s45, v201
v_mfma_f32_16x16x32_bf16 a[232:235], v[104:107], v[92:95], 0
v_add_u32 v192, s34, v192
v_mfma_f32_16x16x32_bf16 a[236:239], v[108:111], v[92:95], 0
v_add_u32 v193, s34, v193
v_mfma_f32_16x16x32_bf16 a[240:243], v[112:115], v[92:95], 0
v_mfma_f32_16x16x32_bf16 a[244:247], v[116:119], v[92:95], 0
s_mov_b32 s40, 256
v_mfma_f32_16x16x32_bf16 a[248:251], v[120:123], v[92:95], 0
s_mov_b32 s41, 524544
v_mfma_f32_16x16x32_bf16 a[252:255], v[124:127], v[92:95], 0
s_waitcnt vmcnt(8) lgkmcnt(0)
s_barrier
v_mfma_f32_16x16x32_bf16 a[0:3], v[160:163], v[128:131], a[0:3]
s_mov_b32 m0, s35
ds_read_b64_tr_b16 v[96:97], v194 offset:0
v_mfma_f32_16x16x32_bf16 a[4:7], v[164:167], v[128:131], a[4:7]
buffer_load_dwordx4 v202, s[4:7], s40 offen offset:0 lds
v_mfma_f32_16x16x32_bf16 a[8:11], v[168:171], v[128:131], a[8:11]
ds_read_b64_tr_b16 v[98:99], v194 offset:1024
v_mfma_f32_16x16x32_bf16 a[12:15], v[172:175], v[128:131], a[12:15]
v_mfma_f32_16x16x32_bf16 a[16:19], v[176:179], v[128:131], a[16:19]
ds_read_b128 v[64:67], v192 offset:0
v_mfma_f32_16x16x32_bf16 a[20:23], v[180:183], v[128:131], a[20:23]
buffer_load_dwordx4 v203, s[4:7], s40 offen offset:1024 lds
v_mfma_f32_16x16x32_bf16 a[24:27], v[184:187], v[128:131], a[24:27]
ds_read_b64_tr_b16 v[100:101], v195 offset:0
v_mfma_f32_16x16x32_bf16 a[28:31], v[188:191], v[128:131], a[28:31]
v_mfma_f32_16x16x32_bf16 a[32:35], v[160:163], v[132:135], a[32:35]
ds_read_b64_tr_b16 v[102:103], v195 offset:1024
v_mfma_f32_16x16x32_bf16 a[36:39], v[164:167], v[132:135], a[36:39]
buffer_load_dwordx4 v204, s[4:7], s40 offen offset:2048 lds
v_mfma_f32_16x16x32_bf16 a[40:43], v[168:171], v[132:135], a[40:43]
ds_read_b128 v[68:71], v192 offset:2048
v_mfma_f32_16x16x32_bf16 a[44:47], v[172:175], v[132:135], a[44:47]
v_mfma_f32_16x16x32_bf16 a[48:51], v[176:179], v[132:135], a[48:51]
ds_read_b64_tr_b16 v[104:105], v196 offset:0
v_mfma_f32_16x16x32_bf16 a[52:55], v[180:183], v[132:135], a[52:55]
buffer_load_dwordx4 v205, s[4:7], s40 offen offset:3072 lds
v_mfma_f32_16x16x32_bf16 a[56:59], v[184:187], v[132:135], a[56:59]
ds_read_b64_tr_b16 v[106:107], v196 offset:1024
v_mfma_f32_16x16x32_bf16 a[60:63], v[188:191], v[132:135], a[60:63]
v_mfma_f32_16x16x32_bf16 a[64:67], v[160:163], v[136:139], a[64:67]
s_add_u32 m0, s35, 4096
ds_read_b128 v[72:75], v192 offset:4096
v_mfma_f32_16x16x32_bf16 a[68:71], v[164:167], v[136:139], a[68:71]
buffer_load_dwordx4 v202, s[4:7], s41 offen offset:0 lds
v_mfma_f32_16x16x32_bf16 a[72:75], v[168:171], v[136:139], a[72:75]
ds_read_b64_tr_b16 v[108:109], v197 offset:0
v_mfma_f32_16x16x32_bf16 a[76:79], v[172:175], v[136:139], a[76:79]
v_mfma_f32_16x16x32_bf16 a[80:83], v[176:179], v[136:139], a[80:83]
ds_read_b64_tr_b16 v[110:111], v197 offset:1024
v_mfma_f32_16x16x32_bf16 a[84:87], v[180:183], v[136:139], a[84:87]
buffer_load_dwordx4 v203, s[4:7], s41 offen offset:1024 lds
v_mfma_f32_16x16x32_bf16 a[88:91], v[184:187], v[136:139], a[88:91]
ds_read_b128 v[76:79], v192 offset:6144
v_mfma_f32_16x16x32_bf16 a[92:95], v[188:191], v[136:139], a[92:95]
v_mfma_f32_16x16x32_bf16 a[96:99], v[160:163], v[140:143], a[96:99]
ds_read_b64_tr_b16 v[112:113], v198 offset:0
v_mfma_f32_16x16x32_bf16 a[100:103], v[164:167], v[140:143], a[100:103]
buffer_load_dwordx4 v204, s[4:7], s41 offen offset:2048 lds
v_mfma_f32_16x16x32_bf16 a[104:107], v[168:171], v[140:143], a[104:107]
ds_read_b64_tr_b16 v[114:115], v198 offset:1024
v_mfma_f32_16x16x32_bf16 a[108:111], v[172:175], v[140:143], a[108:111]
v_mfma_f32_16x16x32_bf16 a[112:115], v[176:179], v[140:143], a[112:115]
ds_read_b128 v[80:83], v192 offset:8192
v_mfma_f32_16x16x32_bf16 a[116:119], v[180:183], v[140:143], a[116:119]
buffer_load_dwordx4 v205, s[4:7], s41 offen offset:3072 lds
v_mfma_f32_16x16x32_bf16 a[120:123], v[184:187], v[140:143], a[120:123]
ds_read_b64_tr_b16 v[116:117], v199 offset:0
v_mfma_f32_16x16x32_bf16 a[124:127], v[188:191], v[140:143], a[124:127]
v_mfma_f32_16x16x32_bf16 a[128:131], v[160:163], v[144:147], a[128:131]
ds_read_b64_tr_b16 v[118:119], v199 offset:1024
v_mfma_f32_16x16x32_bf16 a[132:135], v[164:167], v[144:147], a[132:135]
global_load_dword v35, v[206:207], off
v_mfma_f32_16x16x32_bf16 a[136:139], v[168:171], v[144:147], a[136:139]
ds_read_b128 v[84:87], v192 offset:10240
v_mfma_f32_16x16x32_bf16 a[140:143], v[172:175], v[144:147], a[140:143]
buffer_load_dword v2, v3, s[60:63], s90 offen sc1
v_mfma_f32_16x16x32_bf16 a[144:147], v[176:179], v[144:147], a[144:147]
s_mov_b32 s42, 1572864
ds_read_b64_tr_b16 v[120:121], v200 offset:0
v_mfma_f32_16x16x32_bf16 a[148:151], v[180:183], v[144:147], a[148:151]
s_mov_b32 s43, 1703936
v_mfma_f32_16x16x32_bf16 a[152:155], v[184:187], v[144:147], a[152:155]
ds_read_b64_tr_b16 v[122:123], v200 offset:1024
v_mfma_f32_16x16x32_bf16 a[156:159], v[188:191], v[144:147], a[156:159]
buffer_load_dword v11, v6, s[60:63], 0 offen sc1
v_mfma_f32_16x16x32_bf16 a[160:163], v[160:163], v[148:151], a[160:163]
s_add_u32 s40, s40, 128
ds_read_b128 v[88:91], v192 offset:12288
v_mfma_f32_16x16x32_bf16 a[164:167], v[164:167], v[148:151], a[164:167]
s_add_u32 s41, s41, 128
v_mfma_f32_16x16x32_bf16 a[168:171], v[168:171], v[148:151], a[168:171]
ds_read_b64_tr_b16 v[124:125], v201 offset:0
v_mfma_f32_16x16x32_bf16 a[172:175], v[172:175], v[148:151], a[172:175]
v_add_co_u32 v206, vcc, v208, v206
v_mfma_f32_16x16x32_bf16 a[176:179], v[176:179], v[148:151], a[176:179]
ds_read_b64_tr_b16 v[126:127], v201 offset:1024
v_mfma_f32_16x16x32_bf16 a[180:183], v[180:183], v[148:151], a[180:183]
v_addc_co_u32 v207, vcc, 0, v207, vcc
v_mfma_f32_16x16x32_bf16 a[184:187], v[184:187], v[148:151], a[184:187]
ds_read_b128 v[92:95], v192 offset:14336
v_mfma_f32_16x16x32_bf16 a[188:191], v[188:191], v[148:151], a[188:191]
buffer_store_dword v45, v44, s[60:63], 0 offen sc1
v_mfma_f32_16x16x32_bf16 a[192:195], v[160:163], v[152:155], a[192:195]
v_mfma_f32_16x16x32_bf16 a[196:199], v[164:167], v[152:155], a[196:199]
v_mfma_f32_16x16x32_bf16 a[200:203], v[168:171], v[152:155], a[200:203]
v_mfma_f32_16x16x32_bf16 a[204:207], v[172:175], v[152:155], a[204:207]
v_mfma_f32_16x16x32_bf16 a[208:211], v[176:179], v[152:155], a[208:211]
v_mfma_f32_16x16x32_bf16 a[212:215], v[180:183], v[152:155], a[212:215]
v_mfma_f32_16x16x32_bf16 a[216:219], v[184:187], v[152:155], a[216:219]
v_mfma_f32_16x16x32_bf16 a[220:223], v[188:191], v[152:155], a[220:223]
v_mfma_f32_16x16x32_bf16 a[224:227], v[160:163], v[156:159], a[224:227]
v_mfma_f32_16x16x32_bf16 a[228:231], v[164:167], v[156:159], a[228:231]
s_mov_b32 s37, s36
v_mfma_f32_16x16x32_bf16 a[232:235], v[168:171], v[156:159], a[232:235]
s_add_u32 s35, s35, s45
v_mfma_f32_16x16x32_bf16 a[236:239], v[172:175], v[156:159], a[236:239]
s_add_u32 s36, s36, s34
v_mfma_f32_16x16x32_bf16 a[240:243], v[176:179], v[156:159], a[240:243]
v_mfma_f32_16x16x32_bf16 a[244:247], v[180:183], v[156:159], a[244:247]
v_mfma_f32_16x16x32_bf16 a[248:251], v[184:187], v[156:159], a[248:251]
v_mfma_f32_16x16x32_bf16 a[252:255], v[188:191], v[156:159], a[252:255]
s_waitcnt lgkmcnt(0)
.Lp1_kloop:
v_mfma_f32_16x16x32_bf16 a[0:3], v[96:99], v[64:67], a[0:3]
s_mov_b32 m0, s37
ds_read_b64_tr_b16 v[160:161], v194 offset:8192
v_mfma_f32_16x16x32_bf16 a[4:7], v[100:103], v[64:67], a[4:7]
buffer_load_dwordx4 v16, s[8:11], s42 offen offset:0 nt lds
v_mfma_f32_16x16x32_bf16 a[8:11], v[104:107], v[64:67], a[8:11]
ds_read_b64_tr_b16 v[162:163], v194 offset:9216
v_mfma_f32_16x16x32_bf16 a[12:15], v[108:111], v[64:67], a[12:15]
v_mfma_f32_16x16x32_bf16 a[16:19], v[112:115], v[64:67], a[16:19]
ds_read_b128 v[128:131], v193 offset:0
v_mfma_f32_16x16x32_bf16 a[20:23], v[116:119], v[64:67], a[20:23]
buffer_load_dwordx4 v17, s[8:11], s42 offen offset:1024 nt lds
v_mfma_f32_16x16x32_bf16 a[24:27], v[120:123], v[64:67], a[24:27]
ds_read_b64_tr_b16 v[164:165], v195 offset:8192
v_mfma_f32_16x16x32_bf16 a[28:31], v[124:127], v[64:67], a[28:31]
v_mfma_f32_16x16x32_bf16 a[32:35], v[96:99], v[68:71], a[32:35]
ds_read_b64_tr_b16 v[166:167], v195 offset:9216
v_mfma_f32_16x16x32_bf16 a[36:39], v[100:103], v[68:71], a[36:39]
buffer_load_dwordx4 v18, s[8:11], s42 offen offset:2048 nt lds
v_mfma_f32_16x16x32_bf16 a[40:43], v[104:107], v[68:71], a[40:43]
ds_read_b128 v[132:135], v193 offset:2048
v_mfma_f32_16x16x32_bf16 a[44:47], v[108:111], v[68:71], a[44:47]
v_mfma_f32_16x16x32_bf16 a[48:51], v[112:115], v[68:71], a[48:51]
ds_read_b64_tr_b16 v[168:169], v196 offset:8192
.Lpad1:
s_cbranch_execz .Ltch0
v_mfma_f32_16x16x32_bf16 a[52:55], v[116:119], v[68:71], a[52:55]
buffer_load_dwordx4 v19, s[8:11], s42 offen offset:3072 nt lds
v_mfma_f32_16x16x32_bf16 a[56:59], v[120:123], v[68:71], a[56:59]
ds_read_b64_tr_b16 v[170:171], v196 offset:9216
v_mfma_f32_16x16x32_bf16 a[60:63], v[124:127], v[68:71], a[60:63]
v_mfma_f32_16x16x32_bf16 a[64:67], v[96:99], v[72:75], a[64:67]
s_add_u32 m0, s37, 4096
ds_read_b128 v[136:139], v193 offset:4096
v_mfma_f32_16x16x32_bf16 a[68:71], v[100:103], v[72:75], a[68:71]
buffer_load_dwordx4 v16, s[8:11], s43 offen offset:0 nt lds
v_mfma_f32_16x16x32_bf16 a[72:75], v[104:107], v[72:75], a[72:75]
ds_read_b64_tr_b16 v[172:173], v197 offset:8192
v_mfma_f32_16x16x32_bf16 a[76:79], v[108:111], v[72:75], a[76:79]
v_mfma_f32_16x16x32_bf16 a[80:83], v[112:115], v[72:75], a[80:83]
ds_read_b64_tr_b16 v[174:175], v197 offset:9216
v_mfma_f32_16x16x32_bf16 a[84:87], v[116:119], v[72:75], a[84:87]
buffer_load_dwordx4 v17, s[8:11], s43 offen offset:1024 nt lds
v_mfma_f32_16x16x32_bf16 a[88:91], v[120:123], v[72:75], a[88:91]
ds_read_b128 v[140:143], v193 offset:6144
v_mfma_f32_16x16x32_bf16 a[92:95], v[124:127], v[72:75], a[92:95]
v_mfma_f32_16x16x32_bf16 a[96:99], v[96:99], v[76:79], a[96:99]
ds_read_b64_tr_b16 v[176:177], v198 offset:8192
v_mfma_f32_16x16x32_bf16 a[100:103], v[100:103], v[76:79], a[100:103]
buffer_load_dwordx4 v18, s[8:11], s43 offen offset:2048 nt lds
v_mfma_f32_16x16x32_bf16 a[104:107], v[104:107], v[76:79], a[104:107]
ds_read_b64_tr_b16 v[178:179], v198 offset:9216
v_mfma_f32_16x16x32_bf16 a[108:111], v[108:111], v[76:79], a[108:111]
v_mfma_f32_16x16x32_bf16 a[112:115], v[112:115], v[76:79], a[112:115]
ds_read_b128 v[144:147], v193 offset:8192
v_mfma_f32_16x16x32_bf16 a[116:119], v[116:119], v[76:79], a[116:119]
buffer_load_dwordx4 v19, s[8:11], s43 offen offset:3072 nt lds
v_mfma_f32_16x16x32_bf16 a[120:123], v[120:123], v[76:79], a[120:123]
ds_read_b64_tr_b16 v[180:181], v199 offset:8192
v_mfma_f32_16x16x32_bf16 a[124:127], v[124:127], v[76:79], a[124:127]
v_mfma_f32_16x16x32_bf16 a[128:131], v[96:99], v[80:83], a[128:131]
ds_read_b64_tr_b16 v[182:183], v199 offset:9216
v_mfma_f32_16x16x32_bf16 a[132:135], v[100:103], v[80:83], a[132:135]
v_mfma_f32_16x16x32_bf16 a[136:139], v[104:107], v[80:83], a[136:139]
ds_read_b128 v[148:151], v193 offset:10240
v_mfma_f32_16x16x32_bf16 a[140:143], v[108:111], v[80:83], a[140:143]
v_mfma_f32_16x16x32_bf16 a[144:147], v[112:115], v[80:83], a[144:147]
ds_read_b64_tr_b16 v[184:185], v200 offset:8192
v_mfma_f32_16x16x32_bf16 a[148:151], v[116:119], v[80:83], a[148:151]
v_mfma_f32_16x16x32_bf16 a[152:155], v[120:123], v[80:83], a[152:155]
ds_read_b64_tr_b16 v[186:187], v200 offset:9216
v_mfma_f32_16x16x32_bf16 a[156:159], v[124:127], v[80:83], a[156:159]
v_mfma_f32_16x16x32_bf16 a[160:163], v[96:99], v[84:87], a[160:163]
s_add_u32 s42, s42, 524288
ds_read_b128 v[152:155], v193 offset:12288
v_mfma_f32_16x16x32_bf16 a[164:167], v[100:103], v[84:87], a[164:167]
s_add_u32 s43, s43, 524288
v_mfma_f32_16x16x32_bf16 a[168:171], v[104:107], v[84:87], a[168:171]
ds_read_b64_tr_b16 v[188:189], v201 offset:8192
v_mfma_f32_16x16x32_bf16 a[172:175], v[108:111], v[84:87], a[172:175]
v_mfma_f32_16x16x32_bf16 a[176:179], v[112:115], v[84:87], a[176:179]
s_add_u32 s44, s35, 65536
ds_read_b64_tr_b16 v[190:191], v201 offset:9216
v_mfma_f32_16x16x32_bf16 a[180:183], v[116:119], v[84:87], a[180:183]
s_cmp_ge_u32 s44, s39
s_cselect_b32 s45, s55, s54
v_mfma_f32_16x16x32_bf16 a[184:187], v[120:123], v[84:87], a[184:187]
ds_read_b128 v[156:159], v193 offset:14336
v_mfma_f32_16x16x32_bf16 a[188:191], v[124:127], v[84:87], a[188:191]
s_add_u32 s44, s36, 65536
v_mfma_f32_16x16x32_bf16 a[192:195], v[96:99], v[88:91], a[192:195]
v_mfma_f32_16x16x32_bf16 a[196:199], v[100:103], v[88:91], a[196:199]
s_cmp_ge_u32 s44, s39
s_cselect_b32 s34, s55, s54
v_mfma_f32_16x16x32_bf16 a[200:203], v[104:107], v[88:91], a[200:203]
v_add_u32 v194, s45, v194
v_mfma_f32_16x16x32_bf16 a[204:207], v[108:111], v[88:91], a[204:207]
v_add_u32 v195, s45, v195
v_mfma_f32_16x16x32_bf16 a[208:211], v[112:115], v[88:91], a[208:211]
v_add_u32 v196, s45, v196
v_mfma_f32_16x16x32_bf16 a[212:215], v[116:119], v[88:91], a[212:215]
v_add_u32 v197, s45, v197
v_mfma_f32_16x16x32_bf16 a[216:219], v[120:123], v[88:91], a[216:219]
v_add_u32 v198, s45, v198
v_mfma_f32_16x16x32_bf16 a[220:223], v[124:127], v[88:91], a[220:223]
v_add_u32 v199, s45, v199
v_mfma_f32_16x16x32_bf16 a[224:227], v[96:99], v[92:95], a[224:227]
v_add_u32 v200, s45, v200
v_mfma_f32_16x16x32_bf16 a[228:231], v[100:103], v[92:95], a[228:231]
v_add_u32 v201, s45, v201
v_mfma_f32_16x16x32_bf16 a[232:235], v[104:107], v[92:95], a[232:235]
v_add_u32 v192, s34, v192
v_mfma_f32_16x16x32_bf16 a[236:239], v[108:111], v[92:95], a[236:239]
v_add_u32 v193, s34, v193
v_mfma_f32_16x16x32_bf16 a[240:243], v[112:115], v[92:95], a[240:243]
v_mfma_f32_16x16x32_bf16 a[244:247], v[116:119], v[92:95], a[244:247]
v_mfma_f32_16x16x32_bf16 a[248:251], v[120:123], v[92:95], a[248:251]
v_mfma_f32_16x16x32_bf16 a[252:255], v[124:127], v[92:95], a[252:255]
s_waitcnt vmcnt(9) lgkmcnt(0)
s_barrier
v_mfma_f32_16x16x32_bf16 a[0:3], v[160:163], v[128:131], a[0:3]
s_mov_b32 m0, s35
ds_read_b64_tr_b16 v[96:97], v194 offset:0
v_mfma_f32_16x16x32_bf16 a[4:7], v[164:167], v[128:131], a[4:7]
buffer_load_dwordx4 v202, s[4:7], s40 offen offset:0 lds
v_mfma_f32_16x16x32_bf16 a[8:11], v[168:171], v[128:131], a[8:11]
ds_read_b64_tr_b16 v[98:99], v194 offset:1024
v_mfma_f32_16x16x32_bf16 a[12:15], v[172:175], v[128:131], a[12:15]
v_mfma_f32_16x16x32_bf16 a[16:19], v[176:179], v[128:131], a[16:19]
ds_read_b128 v[64:67], v192 offset:0
v_mfma_f32_16x16x32_bf16 a[20:23], v[180:183], v[128:131], a[20:23]
buffer_load_dwordx4 v203, s[4:7], s40 offen offset:1024 lds
v_mfma_f32_16x16x32_bf16 a[24:27], v[184:187], v[128:131], a[24:27]
ds_read_b64_tr_b16 v[100:101], v195 offset:0
v_mfma_f32_16x16x32_bf16 a[28:31], v[188:191], v[128:131], a[28:31]
v_mfma_f32_16x16x32_bf16 a[32:35], v[160:163], v[132:135], a[32:35]
ds_read_b64_tr_b16 v[102:103], v195 offset:1024
v_mfma_f32_16x16x32_bf16 a[36:39], v[164:167], v[132:135], a[36:39]
buffer_load_dwordx4 v204, s[4:7], s40 offen offset:2048 lds
v_mfma_f32_16x16x32_bf16 a[40:43], v[168:171], v[132:135], a[40:43]
ds_read_b128 v[68:71], v192 offset:2048
v_mfma_f32_16x16x32_bf16 a[44:47], v[172:175], v[132:135], a[44:47]
v_mfma_f32_16x16x32_bf16 a[48:51], v[176:179], v[132:135], a[48:51]
ds_read_b64_tr_b16 v[104:105], v196 offset:0
v_mfma_f32_16x16x32_bf16 a[52:55], v[180:183], v[132:135], a[52:55]
buffer_load_dwordx4 v205, s[4:7], s40 offen offset:3072 lds
v_mfma_f32_16x16x32_bf16 a[56:59], v[184:187], v[132:135], a[56:59]
ds_read_b64_tr_b16 v[106:107], v196 offset:1024
v_mfma_f32_16x16x32_bf16 a[60:63], v[188:191], v[132:135], a[60:63]
v_mfma_f32_16x16x32_bf16 a[64:67], v[160:163], v[136:139], a[64:67]
s_add_u32 m0, s35, 4096
ds_read_b128 v[72:75], v192 offset:4096
v_mfma_f32_16x16x32_bf16 a[68:71], v[164:167], v[136:139], a[68:71]
buffer_load_dwordx4 v202, s[4:7], s41 offen offset:0 lds
v_mfma_f32_16x16x32_bf16 a[72:75], v[168:171], v[136:139], a[72:75]
ds_read_b64_tr_b16 v[108:109], v197 offset:0
v_mfma_f32_16x16x32_bf16 a[76:79], v[172:175], v[136:139], a[76:79]
v_mfma_f32_16x16x32_bf16 a[80:83], v[176:179], v[136:139], a[80:83]
ds_read_b64_tr_b16 v[110:111], v197 offset:1024
v_mfma_f32_16x16x32_bf16 a[84:87], v[180:183], v[136:139], a[84:87]
buffer_load_dwordx4 v203, s[4:7], s41 offen offset:1024 lds
v_mfma_f32_16x16x32_bf16 a[88:91], v[184:187], v[136:139], a[88:91]
ds_read_b128 v[76:79], v192 offset:6144
v_mfma_f32_16x16x32_bf16 a[92:95], v[188:191], v[136:139], a[92:95]
v_mfma_f32_16x16x32_bf16 a[96:99], v[160:163], v[140:143], a[96:99]
ds_read_b64_tr_b16 v[112:113], v198 offset:0
v_mfma_f32_16x16x32_bf16 a[100:103], v[164:167], v[140:143], a[100:103]
buffer_load_dwordx4 v204, s[4:7], s41 offen offset:2048 lds
v_mfma_f32_16x16x32_bf16 a[104:107], v[168:171], v[140:143], a[104:107]
ds_read_b64_tr_b16 v[114:115], v198 offset:1024
v_mfma_f32_16x16x32_bf16 a[108:111], v[172:175], v[140:143], a[108:111]
v_mfma_f32_16x16x32_bf16 a[112:115], v[176:179], v[140:143], a[112:115]
ds_read_b128 v[80:83], v192 offset:8192
v_mfma_f32_16x16x32_bf16 a[116:119], v[180:183], v[140:143], a[116:119]
buffer_load_dwordx4 v205, s[4:7], s41 offen offset:3072 lds
v_mfma_f32_16x16x32_bf16 a[120:123], v[184:187], v[140:143], a[120:123]
ds_read_b64_tr_b16 v[116:117], v199 offset:0
v_mfma_f32_16x16x32_bf16 a[124:127], v[188:191], v[140:143], a[124:127]
v_mfma_f32_16x16x32_bf16 a[128:131], v[160:163], v[144:147], a[128:131]
ds_read_b64_tr_b16 v[118:119], v199 offset:1024
v_mfma_f32_16x16x32_bf16 a[132:135], v[164:167], v[144:147], a[132:135]
global_load_dword v35, v[206:207], off
v_mfma_f32_16x16x32_bf16 a[136:139], v[168:171], v[144:147], a[136:139]
ds_read_b128 v[84:87], v192 offset:10240
v_mfma_f32_16x16x32_bf16 a[140:143], v[172:175], v[144:147], a[140:143]
v_mfma_f32_16x16x32_bf16 a[144:147], v[176:179], v[144:147], a[144:147]
ds_read_b64_tr_b16 v[120:121], v200 offset:0
v_mfma_f32_16x16x32_bf16 a[148:151], v[180:183], v[144:147], a[148:151]
v_mfma_f32_16x16x32_bf16 a[152:155], v[184:187], v[144:147], a[152:155]
ds_read_b64_tr_b16 v[122:123], v200 offset:1024
v_mfma_f32_16x16x32_bf16 a[156:159], v[188:191], v[144:147], a[156:159]
v_mfma_f32_16x16x32_bf16 a[160:163], v[160:163], v[148:151], a[160:163]
s_add_u32 s40, s40, 128
ds_read_b128 v[88:91], v192 offset:12288
v_mfma_f32_16x16x32_bf16 a[164:167], v[164:167], v[148:151], a[164:167]
s_add_u32 s41, s41, 128
v_mfma_f32_16x16x32_bf16 a[168:171], v[168:171], v[148:151], a[168:171]
ds_read_b64_tr_b16 v[124:125], v201 offset:0
v_mfma_f32_16x16x32_bf16 a[172:175], v[172:175], v[148:151], a[172:175]
v_add_co_u32 v206, vcc, v208, v206
v_mfma_f32_16x16x32_bf16 a[176:179], v[176:179], v[148:151], a[176:179]
ds_read_b64_tr_b16 v[126:127], v201 offset:1024
v_mfma_f32_16x16x32_bf16 a[180:183], v[180:183], v[148:151], a[180:183]
v_addc_co_u32 v207, vcc, 0, v207, vcc
v_mfma_f32_16x16x32_bf16 a[184:187], v[184:187], v[148:151], a[184:187]
ds_read_b128 v[92:95], v192 offset:14336
v_mfma_f32_16x16x32_bf16 a[188:191], v[188:191], v[148:151], a[188:191]
v_mfma_f32_16x16x32_bf16 a[192:195], v[160:163], v[152:155], a[192:195]
v_mfma_f32_16x16x32_bf16 a[196:199], v[164:167], v[152:155], a[196:199]
v_mfma_f32_16x16x32_bf16 a[200:203], v[168:171], v[152:155], a[200:203]
v_mfma_f32_16x16x32_bf16 a[204:207], v[172:175], v[152:155], a[204:207]
v_mfma_f32_16x16x32_bf16 a[208:211], v[176:179], v[152:155], a[208:211]
v_mfma_f32_16x16x32_bf16 a[212:215], v[180:183], v[152:155], a[212:215]
v_mfma_f32_16x16x32_bf16 a[216:219], v[184:187], v[152:155], a[216:219]
v_mfma_f32_16x16x32_bf16 a[220:223], v[188:191], v[152:155], a[220:223]
v_mfma_f32_16x16x32_bf16 a[224:227], v[160:163], v[156:159], a[224:227]
v_mfma_f32_16x16x32_bf16 a[228:231], v[164:167], v[156:159], a[228:231]
s_mov_b32 s37, s36
v_mfma_f32_16x16x32_bf16 a[232:235], v[168:171], v[156:159], a[232:235]
s_add_u32 s35, s35, s45
v_mfma_f32_16x16x32_bf16 a[236:239], v[172:175], v[156:159], a[236:239]
s_add_u32 s36, s36, s34
v_mfma_f32_16x16x32_bf16 a[240:243], v[176:179], v[156:159], a[240:243]
s_add_u32 s38, s38, 1
v_mfma_f32_16x16x32_bf16 a[244:247], v[180:183], v[156:159], a[244:247]
s_cmp_lt_u32 s38, 6
v_mfma_f32_16x16x32_bf16 a[248:251], v[184:187], v[156:159], a[248:251]
v_mfma_f32_16x16x32_bf16 a[252:255], v[188:191], v[156:159], a[252:255]
s_waitcnt lgkmcnt(0)
s_cbranch_scc1 .Lp1_kloop
v_mfma_f32_16x16x32_bf16 a[0:3], v[96:99], v[64:67], a[0:3]
s_add_u32 m0, s92, 32768
ds_read_b64_tr_b16 v[160:161], v194 offset:8192
v_mfma_f32_16x16x32_bf16 a[4:7], v[100:103], v[64:67], a[4:7]
buffer_load_dwordx4 v20, s[12:15], s24 offen offset:0 lds
v_mfma_f32_16x16x32_bf16 a[8:11], v[104:107], v[64:67], a[8:11]
ds_read_b64_tr_b16 v[162:163], v194 offset:9216
v_mfma_f32_16x16x32_bf16 a[12:15], v[108:111], v[64:67], a[12:15]
v_mfma_f32_16x16x32_bf16 a[16:19], v[112:115], v[64:67], a[16:19]
ds_read_b128 v[128:131], v193 offset:0
v_mfma_f32_16x16x32_bf16 a[20:23], v[116:119], v[64:67], a[20:23]
buffer_load_dwordx4 v21, s[12:15], s24 offen offset:1024 lds
v_mfma_f32_16x16x32_bf16 a[24:27], v[120:123], v[64:67], a[24:27]
ds_read_b64_tr_b16 v[164:165], v195 offset:8192
v_mfma_f32_16x16x32_bf16 a[28:31], v[124:127], v[64:67], a[28:31]
v_mfma_f32_16x16x32_bf16 a[32:35], v[96:99], v[68:71], a[32:35]
ds_read_b64_tr_b16 v[166:167], v195 offset:9216
v_mfma_f32_16x16x32_bf16 a[36:39], v[100:103], v[68:71], a[36:39]
buffer_load_dwordx4 v22, s[12:15], s24 offen offset:2048 lds
v_mfma_f32_16x16x32_bf16 a[40:43], v[104:107], v[68:71], a[40:43]
ds_read_b128 v[132:135], v193 offset:2048
v_mfma_f32_16x16x32_bf16 a[44:47], v[108:111], v[68:71], a[44:47]
v_mfma_f32_16x16x32_bf16 a[48:51], v[112:115], v[68:71], a[48:51]
ds_read_b64_tr_b16 v[168:169], v196 offset:8192
v_mfma_f32_16x16x32_bf16 a[52:55], v[116:119], v[68:71], a[52:55]
buffer_load_dwordx4 v23, s[12:15], s24 offen offset:3072 lds
v_mfma_f32_16x16x32_bf16 a[56:59], v[120:123], v[68:71], a[56:59]
ds_read_b64_tr_b16 v[170:171], v196 offset:9216
v_mfma_f32_16x16x32_bf16 a[60:63], v[124:127], v[68:71], a[60:63]
v_mfma_f32_16x16x32_bf16 a[64:67], v[96:99], v[72:75], a[64:67]
s_add_u32 m0, s92, 36864
ds_read_b128 v[136:139], v193 offset:4096
v_mfma_f32_16x16x32_bf16 a[68:71], v[100:103], v[72:75], a[68:71]
buffer_load_dwordx4 v20, s[12:15], s25 offen offset:0 lds
v_mfma_f32_16x16x32_bf16 a[72:75], v[104:107], v[72:75], a[72:75]
ds_read_b64_tr_b16 v[172:173], v197 offset:8192
v_mfma_f32_16x16x32_bf16 a[76:79], v[108:111], v[72:75], a[76:79]
v_mfma_f32_16x16x32_bf16 a[80:83], v[112:115], v[72:75], a[80:83]
ds_read_b64_tr_b16 v[174:175], v197 offset:9216
v_mfma_f32_16x16x32_bf16 a[84:87], v[116:119], v[72:75], a[84:87]
buffer_load_dwordx4 v21, s[12:15], s25 offen offset:1024 lds
v_mfma_f32_16x16x32_bf16 a[88:91], v[120:123], v[72:75], a[88:91]
ds_read_b128 v[140:143], v193 offset:6144
v_mfma_f32_16x16x32_bf16 a[92:95], v[124:127], v[72:75], a[92:95]
v_mfma_f32_16x16x32_bf16 a[96:99], v[96:99], v[76:79], a[96:99]
ds_read_b64_tr_b16 v[176:177], v198 offset:8192
v_mfma_f32_16x16x32_bf16 a[100:103], v[100:103], v[76:79], a[100:103]
buffer_load_dwordx4 v22, s[12:15], s25 offen offset:2048 lds
v_mfma_f32_16x16x32_bf16 a[104:107], v[104:107], v[76:79], a[104:107]
ds_read_b64_tr_b16 v[178:179], v198 offset:9216
v_mfma_f32_16x16x32_bf16 a[108:111], v[108:111], v[76:79], a[108:111]
v_mfma_f32_16x16x32_bf16 a[112:115], v[112:115], v[76:79], a[112:115]
ds_read_b128 v[144:147], v193 offset:8192
v_mfma_f32_16x16x32_bf16 a[116:119], v[116:119], v[76:79], a[116:119]
buffer_load_dwordx4 v23, s[12:15], s25 offen offset:3072 lds
v_mfma_f32_16x16x32_bf16 a[120:123], v[120:123], v[76:79], a[120:123]
ds_read_b64_tr_b16 v[180:181], v199 offset:8192
v_mfma_f32_16x16x32_bf16 a[124:127], v[124:127], v[76:79], a[124:127]
v_mfma_f32_16x16x32_bf16 a[128:131], v[96:99], v[80:83], a[128:131]
ds_read_b64_tr_b16 v[182:183], v199 offset:9216
v_mfma_f32_16x16x32_bf16 a[132:135], v[100:103], v[80:83], a[132:135]
v_mfma_f32_16x16x32_bf16 a[136:139], v[104:107], v[80:83], a[136:139]
ds_read_b128 v[148:151], v193 offset:10240
v_mfma_f32_16x16x32_bf16 a[140:143], v[108:111], v[80:83], a[140:143]
v_mfma_f32_16x16x32_bf16 a[144:147], v[112:115], v[80:83], a[144:147]
ds_read_b64_tr_b16 v[184:185], v200 offset:8192
v_mfma_f32_16x16x32_bf16 a[148:151], v[116:119], v[80:83], a[148:151]
v_mfma_f32_16x16x32_bf16 a[152:155], v[120:123], v[80:83], a[152:155]
ds_read_b64_tr_b16 v[186:187], v200 offset:9216
v_mfma_f32_16x16x32_bf16 a[156:159], v[124:127], v[80:83], a[156:159]
v_mfma_f32_16x16x32_bf16 a[160:163], v[96:99], v[84:87], a[160:163]
s_add_u32 s24, s24, 128
s_add_u32 s25, s25, 128
ds_read_b128 v[152:155], v193 offset:12288
v_mfma_f32_16x16x32_bf16 a[164:167], v[100:103], v[84:87], a[164:167]
v_mfma_f32_16x16x32_bf16 a[168:171], v[104:107], v[84:87], a[168:171]
ds_read_b64_tr_b16 v[188:189], v201 offset:8192
v_mfma_f32_16x16x32_bf16 a[172:175], v[108:111], v[84:87], a[172:175]
v_mfma_f32_16x16x32_bf16 a[176:179], v[112:115], v[84:87], a[176:179]
s_add_u32 s44, s35, 65536
ds_read_b64_tr_b16 v[190:191], v201 offset:9216
v_mfma_f32_16x16x32_bf16 a[180:183], v[116:119], v[84:87], a[180:183]
s_cmp_ge_u32 s44, s39
s_cselect_b32 s45, s55, s54
v_mfma_f32_16x16x32_bf16 a[184:187], v[120:123], v[84:87], a[184:187]
ds_read_b128 v[156:159], v193 offset:14336
v_mfma_f32_16x16x32_bf16 a[188:191], v[124:127], v[84:87], a[188:191]
s_add_u32 s44, s36, 65536
v_mfma_f32_16x16x32_bf16 a[192:195], v[96:99], v[88:91], a[192:195]
v_mfma_f32_16x16x32_bf16 a[196:199], v[100:103], v[88:91], a[196:199]
s_cmp_ge_u32 s44, s39
s_cselect_b32 s34, s55, s54
v_mfma_f32_16x16x32_bf16 a[200:203], v[104:107], v[88:91], a[200:203]
v_add_u32 v194, s45, v194
v_mfma_f32_16x16x32_bf16 a[204:207], v[108:111], v[88:91], a[204:207]
v_add_u32 v195, s45, v195
v_mfma_f32_16x16x32_bf16 a[208:211], v[112:115], v[88:91], a[208:211]
v_add_u32 v196, s45, v196
v_mfma_f32_16x16x32_bf16 a[212:215], v[116:119], v[88:91], a[212:215]
v_add_u32 v197, s45, v197
v_mfma_f32_16x16x32_bf16 a[216:219], v[120:123], v[88:91], a[216:219]
v_add_u32 v198, s45, v198
v_mfma_f32_16x16x32_bf16 a[220:223], v[124:127], v[88:91], a[220:223]
v_add_u32 v199, s45, v199
v_mfma_f32_16x16x32_bf16 a[224:227], v[96:99], v[92:95], a[224:227]
v_add_u32 v200, s45, v200
v_mfma_f32_16x16x32_bf16 a[228:231], v[100:103], v[92:95], a[228:231]
v_add_u32 v201, s45, v201
v_mfma_f32_16x16x32_bf16 a[232:235], v[104:107], v[92:95], a[232:235]
v_add_u32 v192, s34, v192
v_mfma_f32_16x16x32_bf16 a[236:239], v[108:111], v[92:95], a[236:239]
v_add_u32 v193, s34, v193
v_mfma_f32_16x16x32_bf16 a[240:243], v[112:115], v[92:95], a[240:243]
v_mfma_f32_16x16x32_bf16 a[244:247], v[116:119], v[92:95], a[244:247]
v_mfma_f32_16x16x32_bf16 a[248:251], v[120:123], v[92:95], a[248:251]
v_mfma_f32_16x16x32_bf16 a[252:255], v[124:127], v[92:95], a[252:255]
s_waitcnt vmcnt(9) lgkmcnt(0)
s_barrier
v_mfma_f32_16x16x32_bf16 a[0:3], v[160:163], v[128:131], a[0:3]
s_add_u32 m0, s91, 65536
ds_read_b64_tr_b16 v[96:97], v194 offset:0
v_mfma_f32_16x16x32_bf16 a[4:7], v[164:167], v[128:131], a[4:7]
buffer_load_dwordx4 v16, s[16:19], s26 offen offset:0 lds
v_mfma_f32_16x16x32_bf16 a[8:11], v[168:171], v[128:131], a[8:11]
ds_read_b64_tr_b16 v[98:99], v194 offset:1024
v_mfma_f32_16x16x32_bf16 a[12:15], v[172:175], v[128:131], a[12:15]
v_mfma_f32_16x16x32_bf16 a[16:19], v[176:179], v[128:131], a[16:19]
ds_read_b128 v[64:67], v192 offset:0
v_mfma_f32_16x16x32_bf16 a[20:23], v[180:183], v[128:131], a[20:23]
buffer_load_dwordx4 v17, s[16:19], s26 offen offset:1024 lds
v_mfma_f32_16x16x32_bf16 a[24:27], v[184:187], v[128:131], a[24:27]
ds_read_b64_tr_b16 v[100:101], v195 offset:0
v_mfma_f32_16x16x32_bf16 a[28:31], v[188:191], v[128:131], a[28:31]
v_mfma_f32_16x16x32_bf16 a[32:35], v[160:163], v[132:135], a[32:35]
ds_read_b64_tr_b16 v[102:103], v195 offset:1024
v_mfma_f32_16x16x32_bf16 a[36:39], v[164:167], v[132:135], a[36:39]
buffer_load_dwordx4 v18, s[16:19], s26 offen offset:2048 lds
v_mfma_f32_16x16x32_bf16 a[40:43], v[168:171], v[132:135], a[40:43]
ds_read_b128 v[68:71], v192 offset:2048
v_mfma_f32_16x16x32_bf16 a[44:47], v[172:175], v[132:135], a[44:47]
v_mfma_f32_16x16x32_bf16 a[48:51], v[176:179], v[132:135], a[48:51]
ds_read_b64_tr_b16 v[104:105], v196 offset:0
v_mfma_f32_16x16x32_bf16 a[52:55], v[180:183], v[132:135], a[52:55]
buffer_load_dwordx4 v19, s[16:19], s26 offen offset:3072 lds
v_mfma_f32_16x16x32_bf16 a[56:59], v[184:187], v[132:135], a[56:59]
ds_read_b64_tr_b16 v[106:107], v196 offset:1024
v_mfma_f32_16x16x32_bf16 a[60:63], v[188:191], v[132:135], a[60:63]
s_add_u32 s26, s26, 524288
v_mfma_f32_16x16x32_bf16 a[64:67], v[160:163], v[136:139], a[64:67]
s_add_u32 m0, s91, 81920
ds_read_b128 v[72:75], v192 offset:4096
v_mfma_f32_16x16x32_bf16 a[68:71], v[164:167], v[136:139], a[68:71]
buffer_load_dwordx4 v16, s[16:19], s26 offen offset:0 lds
v_mfma_f32_16x16x32_bf16 a[72:75], v[168:171], v[136:139], a[72:75]
ds_read_b64_tr_b16 v[108:109], v197 offset:0
v_mfma_f32_16x16x32_bf16 a[76:79], v[172:175], v[136:139], a[76:79]
v_mfma_f32_16x16x32_bf16 a[80:83], v[176:179], v[136:139], a[80:83]
ds_read_b64_tr_b16 v[110:111], v197 offset:1024
v_mfma_f32_16x16x32_bf16 a[84:87], v[180:183], v[136:139], a[84:87]
buffer_load_dwordx4 v17, s[16:19], s26 offen offset:1024 lds
v_mfma_f32_16x16x32_bf16 a[88:91], v[184:187], v[136:139], a[88:91]
ds_read_b128 v[76:79], v192 offset:6144
v_mfma_f32_16x16x32_bf16 a[92:95], v[188:191], v[136:139], a[92:95]
v_mfma_f32_16x16x32_bf16 a[96:99], v[160:163], v[140:143], a[96:99]
ds_read_b64_tr_b16 v[112:113], v198 offset:0
v_mfma_f32_16x16x32_bf16 a[100:103], v[164:167], v[140:143], a[100:103]
buffer_load_dwordx4 v18, s[16:19], s26 offen offset:2048 lds
v_mfma_f32_16x16x32_bf16 a[104:107], v[168:171], v[140:143], a[104:107]
ds_read_b64_tr_b16 v[114:115], v198 offset:1024
v_mfma_f32_16x16x32_bf16 a[108:111], v[172:175], v[140:143], a[108:111]
v_mfma_f32_16x16x32_bf16 a[112:115], v[176:179], v[140:143], a[112:115]
ds_read_b128 v[80:83], v192 offset:8192
v_mfma_f32_16x16x32_bf16 a[116:119], v[180:183], v[140:143], a[116:119]
buffer_load_dwordx4 v19, s[16:19], s26 offen offset:3072 lds
v_mfma_f32_16x16x32_bf16 a[120:123], v[184:187], v[140:143], a[120:123]
ds_read_b64_tr_b16 v[116:117], v199 offset:0
v_mfma_f32_16x16x32_bf16 a[124:127], v[188:191], v[140:143], a[124:127]
s_add_u32 s26, s26, 524288
v_mfma_f32_16x16x32_bf16 a[128:131], v[160:163], v[144:147], a[128:131]
ds_read_b64_tr_b16 v[118:119], v199 offset:1024
v_mfma_f32_16x16x32_bf16 a[132:135], v[164:167], v[144:147], a[132:135]
global_load_dword v35, v[206:207], off
v_mfma_f32_16x16x32_bf16 a[136:139], v[168:171], v[144:147], a[136:139]
ds_read_b128 v[84:87], v192 offset:10240
v_mfma_f32_16x16x32_bf16 a[140:143], v[172:175], v[144:147], a[140:143]
v_mfma_f32_16x16x32_bf16 a[144:147], v[176:179], v[144:147], a[144:147]
ds_read_b64_tr_b16 v[120:121], v200 offset:0
v_mfma_f32_16x16x32_bf16 a[148:151], v[180:183], v[144:147], a[148:151]
v_add_u32 v11, 8, v11
v_and_b32 v11, -8, v11
v_mfma_f32_16x16x32_bf16 a[152:155], v[184:187], v[144:147], a[152:155]
ds_read_b64_tr_b16 v[122:123], v200 offset:1024
v_mfma_f32_16x16x32_bf16 a[156:159], v[188:191], v[144:147], a[156:159]
v_mfma_f32_16x16x32_bf16 a[160:163], v[160:163], v[148:151], a[160:163]
ds_read_b128 v[88:91], v192 offset:12288
v_mfma_f32_16x16x32_bf16 a[164:167], v[164:167], v[148:151], a[164:167]
s_nop 0
v_or_b32 v0, 0, v11
v_mfma_f32_16x16x32_bf16 a[168:171], v[168:171], v[148:151], a[168:171]
ds_read_b64_tr_b16 v[124:125], v201 offset:0
v_mfma_f32_16x16x32_bf16 a[172:175], v[172:175], v[148:151], a[172:175]
v_add_co_u32 v206, vcc, v208, v206
v_mfma_f32_16x16x32_bf16 a[176:179], v[176:179], v[148:151], a[176:179]
ds_read_b64_tr_b16 v[126:127], v201 offset:1024
v_mfma_f32_16x16x32_bf16 a[180:183], v[180:183], v[148:151], a[180:183]
v_addc_co_u32 v207, vcc, 0, v207, vcc
v_mfma_f32_16x16x32_bf16 a[184:187], v[184:187], v[148:151], a[184:187]
ds_read_b128 v[92:95], v192 offset:14336
v_mfma_f32_16x16x32_bf16 a[188:191], v[188:191], v[148:151], a[188:191]
buffer_store_dword v0, v6, s[60:63], 0 offen sc1
v_mfma_f32_16x16x32_bf16 a[192:195], v[160:163], v[152:155], a[192:195]
v_mfma_f32_16x16x32_bf16 a[196:199], v[164:167], v[152:155], a[196:199]
v_mfma_f32_16x16x32_bf16 a[200:203], v[168:171], v[152:155], a[200:203]
v_mfma_f32_16x16x32_bf16 a[204:207], v[172:175], v[152:155], a[204:207]
buffer_load_dword v35, v4, s[96:99], 0 offen
v_mfma_f32_16x16x32_bf16 a[208:211], v[176:179], v[152:155], a[208:211]
v_mfma_f32_16x16x32_bf16 a[212:215], v[180:183], v[152:155], a[212:215]
v_mfma_f32_16x16x32_bf16 a[216:219], v[184:187], v[152:155], a[216:219]
v_mfma_f32_16x16x32_bf16 a[220:223], v[188:191], v[152:155], a[220:223]
v_mfma_f32_16x16x32_bf16 a[224:227], v[160:163], v[156:159], a[224:227]
v_mfma_f32_16x16x32_bf16 a[228:231], v[164:167], v[156:159], a[228:231]
s_mov_b32 s37, s36
v_mfma_f32_16x16x32_bf16 a[232:235], v[168:171], v[156:159], a[232:235]
s_add_u32 s35, s35, s45
v_mfma_f32_16x16x32_bf16 a[236:239], v[172:175], v[156:159], a[236:239]
s_add_u32 s36, s36, s34
v_mfma_f32_16x16x32_bf16 a[240:243], v[176:179], v[156:159], a[240:243]
v_mfma_f32_16x16x32_bf16 a[244:247], v[180:183], v[156:159], a[244:247]
v_mfma_f32_16x16x32_bf16 a[248:251], v[184:187], v[156:159], a[248:251]
v_mfma_f32_16x16x32_bf16 a[252:255], v[188:191], v[156:159], a[252:255]
s_waitcnt lgkmcnt(0)
v_mfma_f32_16x16x32_bf16 a[0:3], v[96:99], v[64:67], a[0:3]
s_add_u32 m0, s92, 98304
ds_read_b64_tr_b16 v[160:161], v194 offset:8192
v_mfma_f32_16x16x32_bf16 a[4:7], v[100:103], v[64:67], a[4:7]
buffer_load_dwordx4 v20, s[12:15], s24 offen offset:0 lds
v_mfma_f32_16x16x32_bf16 a[8:11], v[104:107], v[64:67], a[8:11]
ds_read_b64_tr_b16 v[162:163], v194 offset:9216
v_mfma_f32_16x16x32_bf16 a[12:15], v[108:111], v[64:67], a[12:15]
v_mfma_f32_16x16x32_bf16 a[16:19], v[112:115], v[64:67], a[16:19]
ds_read_b128 v[128:131], v193 offset:0
v_mfma_f32_16x16x32_bf16 a[20:23], v[116:119], v[64:67], a[20:23]
buffer_load_dwordx4 v21, s[12:15], s24 offen offset:1024 lds
v_mfma_f32_16x16x32_bf16 a[24:27], v[120:123], v[64:67], a[24:27]
ds_read_b64_tr_b16 v[164:165], v195 offset:8192
v_mfma_f32_16x16x32_bf16 a[28:31], v[124:127], v[64:67], a[28:31]
v_mfma_f32_16x16x32_bf16 a[32:35], v[96:99], v[68:71], a[32:35]
ds_read_b64_tr_b16 v[166:167], v195 offset:9216
v_mfma_f32_16x16x32_bf16 a[36:39], v[100:103], v[68:71], a[36:39]
buffer_load_dwordx4 v22, s[12:15], s24 offen offset:2048 lds
v_mfma_f32_16x16x32_bf16 a[40:43], v[104:107], v[68:71], a[40:43]
ds_read_b128 v[132:135], v193 offset:2048
v_mfma_f32_16x16x32_bf16 a[44:47], v[108:111], v[68:71], a[44:47]
v_mfma_f32_16x16x32_bf16 a[48:51], v[112:115], v[68:71], a[48:51]
ds_read_b64_tr_b16 v[168:169], v196 offset:8192
v_mfma_f32_16x16x32_bf16 a[52:55], v[116:119], v[68:71], a[52:55]
buffer_load_dwordx4 v23, s[12:15], s24 offen offset:3072 lds
v_mfma_f32_16x16x32_bf16 a[56:59], v[120:123], v[68:71], a[56:59]
ds_read_b64_tr_b16 v[170:171], v196 offset:9216
v_mfma_f32_16x16x32_bf16 a[60:63], v[124:127], v[68:71], a[60:63]
v_mfma_f32_16x16x32_bf16 a[64:67], v[96:99], v[72:75], a[64:67]
s_add_u32 m0, s92, 102400
ds_read_b128 v[136:139], v193 offset:4096
v_mfma_f32_16x16x32_bf16 a[68:71], v[100:103], v[72:75], a[68:71]
buffer_load_dwordx4 v20, s[12:15], s25 offen offset:0 lds
v_mfma_f32_16x16x32_bf16 a[72:75], v[104:107], v[72:75], a[72:75]
ds_read_b64_tr_b16 v[172:173], v197 offset:8192
v_mfma_f32_16x16x32_bf16 a[76:79], v[108:111], v[72:75], a[76:79]
v_mfma_f32_16x16x32_bf16 a[80:83], v[112:115], v[72:75], a[80:83]
ds_read_b64_tr_b16 v[174:175], v197 offset:9216
v_mfma_f32_16x16x32_bf16 a[84:87], v[116:119], v[72:75], a[84:87]
buffer_load_dwordx4 v21, s[12:15], s25 offen offset:1024 lds
v_mfma_f32_16x16x32_bf16 a[88:91], v[120:123], v[72:75], a[88:91]
ds_read_b128 v[140:143], v193 offset:6144
v_mfma_f32_16x16x32_bf16 a[92:95], v[124:127], v[72:75], a[92:95]
v_mfma_f32_16x16x32_bf16 a[96:99], v[96:99], v[76:79], a[96:99]
ds_read_b64_tr_b16 v[176:177], v198 offset:8192
v_mfma_f32_16x16x32_bf16 a[100:103], v[100:103], v[76:79], a[100:103]
buffer_load_dwordx4 v22, s[12:15], s25 offen offset:2048 lds
v_mfma_f32_16x16x32_bf16 a[104:107], v[104:107], v[76:79], a[104:107]
ds_read_b64_tr_b16 v[178:179], v198 offset:9216
v_mfma_f32_16x16x32_bf16 a[108:111], v[108:111], v[76:79], a[108:111]
v_mfma_f32_16x16x32_bf16 a[112:115], v[112:115], v[76:79], a[112:115]
ds_read_b128 v[144:147], v193 offset:8192
v_mfma_f32_16x16x32_bf16 a[116:119], v[116:119], v[76:79], a[116:119]
buffer_load_dwordx4 v23, s[12:15], s25 offen offset:3072 lds
v_mfma_f32_16x16x32_bf16 a[120:123], v[120:123], v[76:79], a[120:123]
ds_read_b64_tr_b16 v[180:181], v199 offset:8192
v_mfma_f32_16x16x32_bf16 a[124:127], v[124:127], v[76:79], a[124:127]
v_mfma_f32_16x16x32_bf16 a[128:131], v[96:99], v[80:83], a[128:131]
ds_read_b64_tr_b16 v[182:183], v199 offset:9216
v_mfma_f32_16x16x32_bf16 a[132:135], v[100:103], v[80:83], a[132:135]
v_mfma_f32_16x16x32_bf16 a[136:139], v[104:107], v[80:83], a[136:139]
ds_read_b128 v[148:151], v193 offset:10240
v_mfma_f32_16x16x32_bf16 a[140:143], v[108:111], v[80:83], a[140:143]
v_mfma_f32_16x16x32_bf16 a[144:147], v[112:115], v[80:83], a[144:147]
ds_read_b64_tr_b16 v[184:185], v200 offset:8192
v_mfma_f32_16x16x32_bf16 a[148:151], v[116:119], v[80:83], a[148:151]
v_mfma_f32_16x16x32_bf16 a[152:155], v[120:123], v[80:83], a[152:155]
ds_read_b64_tr_b16 v[186:187], v200 offset:9216
v_mfma_f32_16x16x32_bf16 a[156:159], v[124:127], v[80:83], a[156:159]
v_mfma_f32_16x16x32_bf16 a[160:163], v[96:99], v[84:87], a[160:163]
s_add_u32 s24, s24, 128
s_add_u32 s25, s25, 128
ds_read_b128 v[152:155], v193 offset:12288
v_mfma_f32_16x16x32_bf16 a[164:167], v[100:103], v[84:87], a[164:167]
v_mfma_f32_16x16x32_bf16 a[168:171], v[104:107], v[84:87], a[168:171]
ds_read_b64_tr_b16 v[188:189], v201 offset:8192
v_mfma_f32_16x16x32_bf16 a[172:175], v[108:111], v[84:87], a[172:175]
v_mfma_f32_16x16x32_bf16 a[176:179], v[112:115], v[84:87], a[176:179]
ds_read_b64_tr_b16 v[190:191], v201 offset:9216
v_mfma_f32_16x16x32_bf16 a[180:183], v[116:119], v[84:87], a[180:183]
v_mfma_f32_16x16x32_bf16 a[184:187], v[120:123], v[84:87], a[184:187]
ds_read_b128 v[156:159], v193 offset:14336
v_mfma_f32_16x16x32_bf16 a[188:191], v[124:127], v[84:87], a[188:191]
v_mfma_f32_16x16x32_bf16 a[192:195], v[96:99], v[88:91], a[192:195]
v_mfma_f32_16x16x32_bf16 a[196:199], v[100:103], v[88:91], a[196:199]
v_mfma_f32_16x16x32_bf16 a[200:203], v[104:107], v[88:91], a[200:203]
v_mfma_f32_16x16x32_bf16 a[204:207], v[108:111], v[88:91], a[204:207]
v_mfma_f32_16x16x32_bf16 a[208:211], v[112:115], v[88:91], a[208:211]
v_mfma_f32_16x16x32_bf16 a[212:215], v[116:119], v[88:91], a[212:215]
v_mfma_f32_16x16x32_bf16 a[216:219], v[120:123], v[88:91], a[216:219]
v_mfma_f32_16x16x32_bf16 a[220:223], v[124:127], v[88:91], a[220:223]
v_mfma_f32_16x16x32_bf16 a[224:227], v[96:99], v[92:95], a[224:227]
v_mfma_f32_16x16x32_bf16 a[228:231], v[100:103], v[92:95], a[228:231]
v_mfma_f32_16x16x32_bf16 a[232:235], v[104:107], v[92:95], a[232:235]
v_mfma_f32_16x16x32_bf16 a[236:239], v[108:111], v[92:95], a[236:239]
v_mfma_f32_16x16x32_bf16 a[240:243], v[112:115], v[92:95], a[240:243]
v_mfma_f32_16x16x32_bf16 a[244:247], v[116:119], v[92:95], a[244:247]
v_mfma_f32_16x16x32_bf16 a[248:251], v[120:123], v[92:95], a[248:251]
v_mfma_f32_16x16x32_bf16 a[252:255], v[124:127], v[92:95], a[252:255]
s_waitcnt vmcnt(15) lgkmcnt(0)
s_barrier
v_mfma_f32_16x16x32_bf16 a[0:3], v[160:163], v[128:131], a[0:3]
s_add_u32 m0, s91, 0
ds_read_b64_tr_b16 v[64:65], v26 offset:0
.Lpad2:
s_cbranch_execz .Ltch0
v_mfma_f32_16x16x32_bf16 a[4:7], v[164:167], v[128:131], a[4:7]
buffer_load_dwordx4 v16, s[16:19], s26 offen offset:0 lds
v_mfma_f32_16x16x32_bf16 a[8:11], v[168:171], v[128:131], a[8:11]
ds_read_b64_tr_b16 v[66:67], v26 offset:1024
v_mfma_f32_16x16x32_bf16 a[12:15], v[172:175], v[128:131], a[12:15]
v_mfma_f32_16x16x32_bf16 a[16:19], v[176:179], v[128:131], a[16:19]
ds_read_b64_tr_b16 v[68:69], v27 offset:0
v_mfma_f32_16x16x32_bf16 a[20:23], v[180:183], v[128:131], a[20:23]
buffer_load_dwordx4 v17, s[16:19], s26 offen offset:1024 lds
v_mfma_f32_16x16x32_bf16 a[24:27], v[184:187], v[128:131], a[24:27]
ds_read_b64_tr_b16 v[70:71], v27 offset:1024
v_mfma_f32_16x16x32_bf16 a[28:31], v[188:191], v[128:131], a[28:31]
v_mfma_f32_16x16x32_bf16 a[32:35], v[160:163], v[132:135], a[32:35]
ds_read_b64_tr_b16 v[72:73], v28 offset:0
v_mfma_f32_16x16x32_bf16 a[36:39], v[164:167], v[132:135], a[36:39]
buffer_load_dwordx4 v18, s[16:19], s26 offen offset:2048 lds
v_mfma_f32_16x16x32_bf16 a[40:43], v[168:171], v[132:135], a[40:43]
ds_read_b64_tr_b16 v[74:75], v28 offset:1024
v_mfma_f32_16x16x32_bf16 a[44:47], v[172:175], v[132:135], a[44:47]
v_mfma_f32_16x16x32_bf16 a[48:51], v[176:179], v[132:135], a[48:51]
ds_read_b64_tr_b16 v[76:77], v29 offset:0
v_mfma_f32_16x16x32_bf16 a[52:55], v[180:183], v[132:135], a[52:55]
buffer_load_dwordx4 v19, s[16:19], s26 offen offset:3072 lds
v_mfma_f32_16x16x32_bf16 a[56:59], v[184:187], v[132:135], a[56:59]
ds_read_b64_tr_b16 v[78:79], v29 offset:1024
v_mfma_f32_16x16x32_bf16 a[60:63], v[188:191], v[132:135], a[60:63]
s_add_u32 s26, s26, 524288
v_mfma_f32_16x16x32_bf16 a[64:67], v[160:163], v[136:139], a[64:67]
s_add_u32 m0, s92, 131072
ds_read_b64_tr_b16 v[80:81], v30 offset:0
v_mfma_f32_16x16x32_bf16 a[68:71], v[164:167], v[136:139], a[68:71]
buffer_load_dwordx4 v20, s[12:15], s24 offen offset:0 lds
v_mfma_f32_16x16x32_bf16 a[72:75], v[168:171], v[136:139], a[72:75]
ds_read_b64_tr_b16 v[82:83], v30 offset:1024
v_mfma_f32_16x16x32_bf16 a[76:79], v[172:175], v[136:139], a[76:79]
v_mfma_f32_16x16x32_bf16 a[80:83], v[176:179], v[136:139], a[80:83]
ds_read_b64_tr_b16 v[84:85], v31 offset:0
v_mfma_f32_16x16x32_bf16 a[84:87], v[180:183], v[136:139], a[84:87]
buffer_load_dwordx4 v21, s[12:15], s24 offen offset:1024 lds
v_mfma_f32_16x16x32_bf16 a[88:91], v[184:187], v[136:139], a[88:91]
ds_read_b64_tr_b16 v[86:87], v31 offset:1024
v_mfma_f32_16x16x32_bf16 a[92:95], v[188:191], v[136:139], a[92:95]
v_mfma_f32_16x16x32_bf16 a[96:99], v[160:163], v[140:143], a[96:99]
ds_read_b64_tr_b16 v[88:89], v32 offset:0
v_mfma_f32_16x16x32_bf16 a[100:103], v[164:167], v[140:143], a[100:103]
buffer_load_dwordx4 v22, s[12:15], s24 offen offset:2048 lds
v_mfma_f32_16x16x32_bf16 a[104:107], v[168:171], v[140:143], a[104:107]
ds_read_b64_tr_b16 v[90:91], v32 offset:1024
v_mfma_f32_16x16x32_bf16 a[108:111], v[172:175], v[140:143], a[108:111]
v_mfma_f32_16x16x32_bf16 a[112:115], v[176:179], v[140:143], a[112:115]
ds_read_b64_tr_b16 v[92:93], v33 offset:0
v_mfma_f32_16x16x32_bf16 a[116:119], v[180:183], v[140:143], a[116:119]
buffer_load_dwordx4 v23, s[12:15], s24 offen offset:3072 lds
v_mfma_f32_16x16x32_bf16 a[120:123], v[184:187], v[140:143], a[120:123]
ds_read_b64_tr_b16 v[94:95], v33 offset:1024
v_mfma_f32_16x16x32_bf16 a[124:127], v[188:191], v[140:143], a[124:127]
v_mfma_f32_16x16x32_bf16 a[128:131], v[160:163], v[144:147], a[128:131]
s_add_u32 m0, s92, 135168
v_mfma_f32_16x16x32_bf16 a[132:135], v[164:167], v[144:147], a[132:135]
buffer_load_dwordx4 v20, s[12:15], s25 offen offset:0 lds
v_mfma_f32_16x16x32_bf16 a[136:139], v[168:171], v[144:147], a[136:139]
ds_read_b128 v[48:51], v24 offset:0
v_mfma_f32_16x16x32_bf16 a[140:143], v[172:175], v[144:147], a[140:143]
v_mfma_f32_16x16x32_bf16 a[144:147], v[176:179], v[144:147], a[144:147]
ds_read_b128 v[52:55], v24 offset:2048
v_mfma_f32_16x16x32_bf16 a[148:151], v[180:183], v[144:147], a[148:151]
buffer_load_dwordx4 v21, s[12:15], s25 offen offset:1024 lds
v_mfma_f32_16x16x32_bf16 a[152:155], v[184:187], v[144:147], a[152:155]
ds_read_b128 v[56:59], v24 offset:4096
v_mfma_f32_16x16x32_bf16 a[156:159], v[188:191], v[144:147], a[156:159]
v_mfma_f32_16x16x32_bf16 a[160:163], v[160:163], v[148:151], a[160:163]
v_mfma_f32_16x16x32_bf16 a[164:167], v[164:167], v[148:151], a[164:167]
buffer_load_dwordx4 v22, s[12:15], s25 offen offset:2048 lds
v_mfma_f32_16x16x32_bf16 a[168:171], v[168:171], v[148:151], a[168:171]
v_mfma_f32_16x16x32_bf16 a[172:175], v[172:175], v[148:151], a[172:175]
v_mfma_f32_16x16x32_bf16 a[176:179], v[176:179], v[148:151], a[176:179]
v_mfma_f32_16x16x32_bf16 a[180:183], v[180:183], v[148:151], a[180:183]
buffer_load_dwordx4 v23, s[12:15], s25 offen offset:3072 lds
v_mfma_f32_16x16x32_bf16 a[184:187], v[184:187], v[148:151], a[184:187]
v_mfma_f32_16x16x32_bf16 a[188:191], v[188:191], v[148:151], a[188:191]
v_mfma_f32_16x16x32_bf16 a[192:195], v[160:163], v[152:155], a[192:195]
v_mfma_f32_16x16x32_bf16 a[196:199], v[164:167], v[152:155], a[196:199]
s_add_u32 s24, s24, 128
s_add_u32 s25, s25, 128
v_mfma_f32_16x16x32_bf16 a[200:203], v[168:171], v[152:155], a[200:203]
v_mfma_f32_16x16x32_bf16 a[204:207], v[172:175], v[152:155], a[204:207]
v_mfma_f32_16x16x32_bf16 a[208:211], v[176:179], v[152:155], a[208:211]
v_mfma_f32_16x16x32_bf16 a[212:215], v[180:183], v[152:155], a[212:215]
v_mfma_f32_16x16x32_bf16 a[216:219], v[184:187], v[152:155], a[216:219]
v_mfma_f32_16x16x32_bf16 a[220:223], v[188:191], v[152:155], a[220:223]
v_mfma_f32_16x16x32_bf16 a[224:227], v[160:163], v[156:159], a[224:227]
v_mfma_f32_16x16x32_bf16 a[228:231], v[164:167], v[156:159], a[228:231]
v_mfma_f32_16x16x32_bf16 a[232:235], v[168:171], v[156:159], a[232:235]
v_mfma_f32_16x16x32_bf16 a[236:239], v[172:175], v[156:159], a[236:239]
v_mfma_f32_16x16x32_bf16 a[240:243], v[176:179], v[156:159], a[240:243]
v_mfma_f32_16x16x32_bf16 a[244:247], v[180:183], v[156:159], a[244:247]
v_mfma_f32_16x16x32_bf16 a[248:251], v[184:187], v[156:159], a[248:251]
v_mfma_f32_16x16x32_bf16 a[252:255], v[188:191], v[156:159], a[252:255]
s_waitcnt lgkmcnt(0)
s_branch .Lp2
.Lp1_v_pad:
s_cbranch_execz .Ltch0
.Lp1_v:
v_mfma_f32_16x16x32_bf16 a[0:3], v[64:67], v[96:99], 0
ds_read_b64_tr_b16 v[160:161], v194 offset:8192
v_mfma_f32_16x16x32_bf16 a[4:7], v[64:67], v[100:103], 0
s_add_u32 s37, s92, 98304
s_mov_b32 s40, 128
s_mov_b32 s41, 524416
v_mfma_f32_16x16x32_bf16 a[8:11], v[64:67], v[104:107], 0
s_mov_b32 m0, s37
ds_read_b64_tr_b16 v[162:163], v194 offset:9216
v_mfma_f32_16x16x32_bf16 a[12:15], v[64:67], v[108:111], 0
buffer_load_dwordx4 v202, s[4:7], s40 offen offset:0 lds
v_mfma_f32_16x16x32_bf16 a[16:19], v[64:67], v[112:115], 0
ds_read_b128 v[128:131], v193 offset:0
v_mfma_f32_16x16x32_bf16 a[20:23], v[64:67], v[116:119], 0
buffer_load_dwordx4 v203, s[4:7], s40 offen offset:1024 lds
v_mfma_f32_16x16x32_bf16 a[24:27], v[64:67], v[120:123], 0
ds_read_b64_tr_b16 v[164:165], v195 offset:8192
v_mfma_f32_16x16x32_bf16 a[28:31], v[64:67], v[124:127], 0
buffer_load_dwordx4 v204, s[4:7], s40 offen offset:2048 lds
v_mfma_f32_16x16x32_bf16 a[32:35], v[68:71], v[96:99], 0
ds_read_b64_tr_b16 v[166:167], v195 offset:9216
v_mfma_f32_16x16x32_bf16 a[36:39], v[68:71], v[100:103], 0
buffer_load_dwordx4 v205, s[4:7], s40 offen offset:3072 lds
v_mfma_f32_16x16x32_bf16 a[40:43], v[68:71], v[104:107], 0
s_add_u32 m0, s37, 4096
ds_read_b128 v[132:135], v193 offset:2048
v_mfma_f32_16x16x32_bf16 a[44:47], v[68:71], v[108:111], 0
buffer_load_dwordx4 v202, s[4:7], s41 offen offset:0 lds
v_mfma_f32_16x16x32_bf16 a[48:51], v[68:71], v[112:115], 0
ds_read_b64_tr_b16 v[168:169], v196 offset:8192
v_mfma_f32_16x16x32_bf16 a[52:55], v[68:71], v[116:119], 0
buffer_load_dwordx4 v203, s[4:7], s41 offen offset:1024 lds
v_mfma_f32_16x16x32_bf16 a[56:59], v[68:71], v[120:123], 0
ds_read_b64_tr_b16 v[170:171], v196 offset:9216
v_mfma_f32_16x16x32_bf16 a[60:63], v[68:71], v[124:127], 0
buffer_load_dwordx4 v204, s[4:7], s41 offen offset:2048 lds
v_mfma_f32_16x16x32_bf16 a[64:67], v[72:75], v[96:99], 0
ds_read_b128 v[136:139], v193 offset:4096
v_mfma_f32_16x16x32_bf16 a[68:71], v[72:75], v[100:103], 0
buffer_load_dwordx4 v205, s[4:7], s41 offen offset:3072 lds
v_mfma_f32_16x16x32_bf16 a[72:75], v[72:75], v[104:107], 0
ds_read_b64_tr_b16 v[172:173], v197 offset:8192
v_mfma_f32_16x16x32_bf16 a[76:79], v[72:75], v[108:111], 0
s_add_u32 s37, s92, 131072
s_mov_b32 s42, 1048576
s_mov_b32 s43, 1179648
v_mfma_f32_16x16x32_bf16 a[80:83], v[72:75], v[112:115], 0
s_mov_b32 m0, s37
ds_read_b64_tr_b16 v[174:175], v197 offset:9216
v_mfma_f32_16x16x32_bf16 a[84:87], v[72:75], v[116:119], 0
buffer_load_dwordx4 v16, s[8:11], s42 offen offset:0 nt lds
v_mfma_f32_16x16x32_bf16 a[88:91], v[72:75], v[120:123], 0
ds_read_b128 v[140:143], v193 offset:6144
v_mfma_f32_16x16x32_bf16 a[92:95], v[72:75], v[124:127], 0
buffer_load_dwordx4 v17, s[8:11], s42 offen offset:1024 nt lds
v_mfma_f32_16x16x32_bf16 a[96:99], v[76:79], v[96:99], 0
ds_read_b64_tr_b16 v[176:177], v198 offset:8192
v_mfma_f32_16x16x32_bf16 a[100:103], v[76:79], v[100:103], 0
buffer_load_dwordx4 v18, s[8:11], s42 offen offset:2048 nt lds
v_mfma_f32_16x16x32_bf16 a[104:107], v[76:79], v[104:107], 0
ds_read_b64_tr_b16 v[178:179], v198 offset:9216
v_mfma_f32_16x16x32_bf16 a[108:111], v[76:79], v[108:111], 0
buffer_load_dwordx4 v19, s[8:11], s42 offen offset:3072 nt lds
v_mfma_f32_16x16x32_bf16 a[112:115], v[76:79], v[112:115], 0
s_add_u32 m0, s37, 4096
ds_read_b128 v[144:147], v193 offset:8192
v_mfma_f32_16x16x32_bf16 a[116:119], v[76:79], v[116:119], 0
buffer_load_dwordx4 v16, s[8:11], s43 offen offset:0 nt lds
v_mfma_f32_16x16x32_bf16 a[120:123], v[76:79], v[120:123], 0
ds_read_b64_tr_b16 v[180:181], v199 offset:8192
v_mfma_f32_16x16x32_bf16 a[124:127], v[76:79], v[124:127], 0
buffer_load_dwordx4 v17, s[8:11], s43 offen offset:1024 nt lds
v_mfma_f32_16x16x32_bf16 a[128:131], v[80:83], v[96:99], 0
ds_read_b64_tr_b16 v[182:183], v199 offset:9216
v_mfma_f32_16x16x32_bf16 a[132:135], v[80:83], v[100:103], 0
buffer_load_dwordx4 v18, s[8:11], s43 offen offset:2048 nt lds
v_mfma_f32_16x16x32_bf16 a[136:139], v[80:83], v[104:107], 0
ds_read_b128 v[148:151], v193 offset:10240
v_mfma_f32_16x16x32_bf16 a[140:143], v[80:83], v[108:111], 0
buffer_load_dwordx4 v19, s[8:11], s43 offen offset:3072 nt lds
v_mfma_f32_16x16x32_bf16 a[144:147], v[80:83], v[112:115], 0
ds_read_b64_tr_b16 v[184:185], v200 offset:8192
v_mfma_f32_16x16x32_bf16 a[148:151], v[80:83], v[116:119], 0
v_mfma_f32_16x16x32_bf16 a[152:155], v[80:83], v[120:123], 0
ds_read_b64_tr_b16 v[186:187], v200 offset:9216
v_mfma_f32_16x16x32_bf16 a[156:159], v[80:83], v[124:127], 0
v_mfma_f32_16x16x32_bf16 a[160:163], v[84:87], v[96:99], 0
ds_read_b128 v[152:155], v193 offset:12288
v_mfma_f32_16x16x32_bf16 a[164:167], v[84:87], v[100:103], 0
v_mfma_f32_16x16x32_bf16 a[168:171], v[84:87], v[104:107], 0
ds_read_b64_tr_b16 v[188:189], v201 offset:8192
v_mfma_f32_16x16x32_bf16 a[172:175], v[84:87], v[108:111], 0
v_mfma_f32_16x16x32_bf16 a[176:179], v[84:87], v[112:115], 0
s_add_u32 s44, s35, 65536
ds_read_b64_tr_b16 v[190:191], v201 offset:9216
v_mfma_f32_16x16x32_bf16 a[180:183], v[84:87], v[116:119], 0
s_cmp_ge_u32 s44, s39
s_cselect_b32 s45, s55, s54
v_mfma_f32_16x16x32_bf16 a[184:187], v[84:87], v[120:123], 0
ds_read_b128 v[156:159], v193 offset:14336
v_mfma_f32_16x16x32_bf16 a[188:191], v[84:87], v[124:127], 0
s_add_u32 s44, s36, 65536
v_mfma_f32_16x16x32_bf16 a[192:195], v[88:91], v[96:99], 0
v_mfma_f32_16x16x32_bf16 a[196:199], v[88:91], v[100:103], 0
s_cmp_ge_u32 s44, s39
s_cselect_b32 s34, s55, s54
v_mfma_f32_16x16x32_bf16 a[200:203], v[88:91], v[104:107], 0
v_add_u32 v194, s45, v194
v_mfma_f32_16x16x32_bf16 a[204:207], v[88:91], v[108:111], 0
v_add_u32 v195, s45, v195
v_mfma_f32_16x16x32_bf16 a[208:211], v[88:91], v[112:115], 0
v_add_u32 v196, s45, v196
v_mfma_f32_16x16x32_bf16 a[212:215], v[88:91], v[116:119], 0
v_add_u32 v197, s45, v197
v_mfma_f32_16x16x32_bf16 a[216:219], v[88:91], v[120:123], 0
v_add_u32 v198, s45, v198
v_mfma_f32_16x16x32_bf16 a[220:223], v[88:91], v[124:127], 0
v_add_u32 v199, s45, v199
v_mfma_f32_16x16x32_bf16 a[224:227], v[92:95], v[96:99], 0
v_add_u32 v200, s45, v200
v_mfma_f32_16x16x32_bf16 a[228:231], v[92:95], v[100:103], 0
v_add_u32 v201, s45, v201
v_mfma_f32_16x16x32_bf16 a[232:235], v[92:95], v[104:107], 0
v_add_u32 v192, s34, v192
v_mfma_f32_16x16x32_bf16 a[236:239], v[92:95], v[108:111], 0
v_add_u32 v193, s34, v193
v_mfma_f32_16x16x32_bf16 a[240:243], v[92:95], v[112:115], 0
v_mfma_f32_16x16x32_bf16 a[244:247], v[92:95], v[116:119], 0
s_mov_b32 s40, 256
v_mfma_f32_16x16x32_bf16 a[248:251], v[92:95], v[120:123], 0
s_mov_b32 s41, 524544
v_mfma_f32_16x16x32_bf16 a[252:255], v[92:95], v[124:127], 0
s_waitcnt vmcnt(8) lgkmcnt(0)
s_barrier
v_mfma_f32_16x16x32_bf16 a[0:3], v[128:131], v[160:163], a[0:3]
s_mov_b32 m0, s35
ds_read_b64_tr_b16 v[96:97], v194 offset:0
v_mfma_f32_16x16x32_bf16 a[4:7], v[128:131], v[164:167], a[4:7]
buffer_load_dwordx4 v202, s[4:7], s40 offen offset:0 lds
v_mfma_f32_16x16x32_bf16 a[8:11], v[128:131], v[168:171], a[8:11]
ds_read_b64_tr_b16 v[98:99], v194 offset:1024
v_mfma_f32_16x16x32_bf16 a[12:15], v[128:131], v[172:175], a[12:15]
v_mfma_f32_16x16x32_bf16 a[16:19], v[128:131], v[176:179], a[16:19]
ds_read_b128 v[64:67], v192 offset:0
v_mfma_f32_16x16x32_bf16 a[20:23], v[128:131], v[180:183], a[20:23]
buffer_load_dwordx4 v203, s[4:7], s40 offen offset:1024 lds
v_mfma_f32_16x16x32_bf16 a[24:27], v[128:131], v[184:187], a[24:27]
ds_read_b64_tr_b16 v[100:101], v195 offset:0
v_mfma_f32_16x16x32_bf16 a[28:31], v[128:131], v[188:191], a[28:31]
v_mfma_f32_16x16x32_bf16 a[32:35], v[132:135], v[160:163], a[32:35]
ds_read_b64_tr_b16 v[102:103], v195 offset:1024
v_mfma_f32_16x16x32_bf16 a[36:39], v[132:135], v[164:167], a[36:39]
buffer_load_dwordx4 v204, s[4:7], s40 offen offset:2048 lds
v_mfma_f32_16x16x32_bf16 a[40:43], v[132:135], v[168:171], a[40:43]
ds_read_b128 v[68:71], v192 offset:2048
v_mfma_f32_16x16x32_bf16 a[44:47], v[132:135], v[172:175], a[44:47]
v_mfma_f32_16x16x32_bf16 a[48:51], v[132:135], v[176:179], a[48:51]
ds_read_b64_tr_b16 v[104:105], v196 offset:0
v_mfma_f32_16x16x32_bf16 a[52:55], v[132:135], v[180:183], a[52:55]
buffer_load_dwordx4 v205, s[4:7], s40 offen offset:3072 lds
v_mfma_f32_16x16x32_bf16 a[56:59], v[132:135], v[184:187], a[56:59]
ds_read_b64_tr_b16 v[106:107], v196 offset:1024
v_mfma_f32_16x16x32_bf16 a[60:63], v[132:135], v[188:191], a[60:63]
v_mfma_f32_16x16x32_bf16 a[64:67], v[136:139], v[160:163], a[64:67]
s_add_u32 m0, s35, 4096
ds_read_b128 v[72:75], v192 offset:4096
v_mfma_f32_16x16x32_bf16 a[68:71], v[136:139], v[164:167], a[68:71]
buffer_load_dwordx4 v202, s[4:7], s41 offen offset:0 lds
v_mfma_f32_16x16x32_bf16 a[72:75], v[136:139], v[168:171], a[72:75]
ds_read_b64_tr_b16 v[108:109], v197 offset:0
v_mfma_f32_16x16x32_bf16 a[76:79], v[136:139], v[172:175], a[76:79]
v_mfma_f32_16x16x32_bf16 a[80:83], v[136:139], v[176:179], a[80:83]
ds_read_b64_tr_b16 v[110:111], v197 offset:1024
v_mfma_f32_16x16x32_bf16 a[84:87], v[136:139], v[180:183], a[84:87]
buffer_load_dwordx4 v203, s[4:7], s41 offen offset:1024 lds
v_mfma_f32_16x16x32_bf16 a[88:91], v[136:139], v[184:187], a[88:91]
ds_read_b128 v[76:79], v192 offset:6144
v_mfma_f32_16x16x32_bf16 a[92:95], v[136:139], v[188:191], a[92:95]
v_mfma_f32_16x16x32_bf16 a[96:99], v[140:143], v[160:163], a[96:99]
ds_read_b64_tr_b16 v[112:113], v198 offset:0
v_mfma_f32_16x16x32_bf16 a[100:103], v[140:143], v[164:167], a[100:103]
buffer_load_dwordx4 v204, s[4:7], s41 offen offset:2048 lds
v_mfma_f32_16x16x32_bf16 a[104:107], v[140:143], v[168:171], a[104:107]
ds_read_b64_tr_b16 v[114:115], v198 offset:1024
v_mfma_f32_16x16x32_bf16 a[108:111], v[140:143], v[172:175], a[108:111]
v_mfma_f32_16x16x32_bf16 a[112:115], v[140:143], v[176:179], a[112:115]
ds_read_b128 v[80:83], v192 offset:8192
v_mfma_f32_16x16x32_bf16 a[116:119], v[140:143], v[180:183], a[116:119]
buffer_load_dwordx4 v205, s[4:7], s41 offen offset:3072 lds
v_mfma_f32_16x16x32_bf16 a[120:123], v[140:143], v[184:187], a[120:123]
ds_read_b64_tr_b16 v[116:117], v199 offset:0
v_mfma_f32_16x16x32_bf16 a[124:127], v[140:143], v[188:191], a[124:127]
v_mfma_f32_16x16x32_bf16 a[128:131], v[144:147], v[160:163], a[128:131]
ds_read_b64_tr_b16 v[118:119], v199 offset:1024
v_mfma_f32_16x16x32_bf16 a[132:135], v[144:147], v[164:167], a[132:135]
global_load_dword v35, v[206:207], off
v_mfma_f32_16x16x32_bf16 a[136:139], v[144:147], v[168:171], a[136:139]
ds_read_b128 v[84:87], v192 offset:10240
v_mfma_f32_16x16x32_bf16 a[140:143], v[144:147], v[172:175], a[140:143]
buffer_load_dword v2, v3, s[60:63], s90 offen sc1
v_mfma_f32_16x16x32_bf16 a[144:147], v[144:147], v[176:179], a[144:147]
s_mov_b32 s42, 1572864
ds_read_b64_tr_b16 v[120:121], v200 offset:0
v_mfma_f32_16x16x32_bf16 a[148:151], v[144:147], v[180:183], a[148:151]
s_mov_b32 s43, 1703936
v_mfma_f32_16x16x32_bf16 a[152:155], v[144:147], v[184:187], a[152:155]
ds_read_b64_tr_b16 v[122:123], v200 offset:1024
v_mfma_f32_16x16x32_bf16 a[156:159], v[144:147], v[188:191], a[156:159]
buffer_load_dword v11, v6, s[60:63], 0 offen sc1
v_mfma_f32_16x16x32_bf16 a[160:163], v[148:151], v[160:163], a[160:163]
s_add_u32 s40, s40, 128
ds_read_b128 v[88:91], v192 offset:12288
v_mfma_f32_16x16x32_bf16 a[164:167], v[148:151], v[164:167], a[164:167]
s_add_u32 s41, s41, 128
v_mfma_f32_16x16x32_bf16 a[168:171], v[148:151], v[168:171], a[168:171]
ds_read_b64_tr_b16 v[124:125], v201 offset:0
v_mfma_f32_16x16x32_bf16 a[172:175], v[148:151], v[172:175], a[172:175]
v_add_co_u32 v206, vcc, v208, v206
v_mfma_f32_16x16x32_bf16 a[176:179], v[148:151], v[176:179], a[176:179]
ds_read_b64_tr_b16 v[126:127], v201 offset:1024
v_mfma_f32_16x16x32_bf16 a[180:183], v[148:151], v[180:183], a[180:183]
v_addc_co_u32 v207, vcc, 0, v207, vcc
v_mfma_f32_16x16x32_bf16 a[184:187], v[148:151], v[184:187], a[184:187]
ds_read_b128 v[92:95], v192 offset:14336
v_mfma_f32_16x16x32_bf16 a[188:191], v[148:151], v[188:191], a[188:191]
buffer_store_dword v45, v44, s[60:63], 0 offen sc1
v_mfma_f32_16x16x32_bf16 a[192:195], v[152:155], v[160:163], a[192:195]
v_mfma_f32_16x16x32_bf16 a[196:199], v[152:155], v[164:167], a[196:199]
v_mfma_f32_16x16x32_bf16 a[200:203], v[152:155], v[168:171], a[200:203]
v_mfma_f32_16x16x32_bf16 a[204:207], v[152:155], v[172:175], a[204:207]
v_mfma_f32_16x16x32_bf16 a[208:211], v[152:155], v[176:179], a[208:211]
v_mfma_f32_16x16x32_bf16 a[212:215], v[152:155], v[180:183], a[212:215]
v_mfma_f32_16x16x32_bf16 a[216:219], v[152:155], v[184:187], a[216:219]
v_mfma_f32_16x16x32_bf16 a[220:223], v[152:155], v[188:191], a[220:223]
v_mfma_f32_16x16x32_bf16 a[224:227], v[156:159], v[160:163], a[224:227]
v_mfma_f32_16x16x32_bf16 a[228:231], v[156:159], v[164:167], a[228:231]
s_mov_b32 s37, s36
v_mfma_f32_16x16x32_bf16 a[232:235], v[156:159], v[168:171], a[232:235]
s_add_u32 s35, s35, s45
v_mfma_f32_16x16x32_bf16 a[236:239], v[156:159], v[172:175], a[236:239]
s_add_u32 s36, s36, s34
v_mfma_f32_16x16x32_bf16 a[240:243], v[156:159], v[176:179], a[240:243]
v_mfma_f32_16x16x32_bf16 a[244:247], v[156:159], v[180:183], a[244:247]
v_mfma_f32_16x16x32_bf16 a[248:251], v[156:159], v[184:187], a[248:251]
v_mfma_f32_16x16x32_bf16 a[252:255], v[156:159], v[188:191], a[252:255]
s_waitcnt lgkmcnt(0)
.Lp1_vloop:
v_mfma_f32_16x16x32_bf16 a[0:3], v[64:67], v[96:99], a[0:3]
s_mov_b32 m0, s37
ds_read_b64_tr_b16 v[160:161], v194 offset:8192
v_mfma_f32_16x16x32_bf16 a[4:7], v[64:67], v[100:103], a[4:7]
buffer_load_dwordx4 v16, s[8:11], s42 offen offset:0 nt lds
v_mfma_f32_16x16x32_bf16 a[8:11], v[64:67], v[104:107], a[8:11]
ds_read_b64_tr_b16 v[162:163], v194 offset:9216
v_mfma_f32_16x16x32_bf16 a[12:15], v[64:67], v[108:111], a[12:15]
v_mfma_f32_16x16x32_bf16 a[16:19], v[64:67], v[112:115], a[16:19]
ds_read_b128 v[128:131], v193 offset:0
v_mfma_f32_16x16x32_bf16 a[20:23], v[64:67], v[116:119], a[20:23]
buffer_load_dwordx4 v17, s[8:11], s42 offen offset:1024 nt lds
v_mfma_f32_16x16x32_bf16 a[24:27], v[64:67], v[120:123], a[24:27]
ds_read_b64_tr_b16 v[164:165], v195 offset:8192
v_mfma_f32_16x16x32_bf16 a[28:31], v[64:67], v[124:127], a[28:31]
v_mfma_f32_16x16x32_bf16 a[32:35], v[68:71], v[96:99], a[32:35]
ds_read_b64_tr_b16 v[166:167], v195 offset:9216
v_mfma_f32_16x16x32_bf16 a[36:39], v[68:71], v[100:103], a[36:39]
buffer_load_dwordx4 v18, s[8:11], s42 offen offset:2048 nt lds
v_mfma_f32_16x16x32_bf16 a[40:43], v[68:71], v[104:107], a[40:43]
ds_read_b128 v[132:135], v193 offset:2048
v_mfma_f32_16x16x32_bf16 a[44:47], v[68:71], v[108:111], a[44:47]
v_mfma_f32_16x16x32_bf16 a[48:51], v[68:71], v[112:115], a[48:51]
ds_read_b64_tr_b16 v[168:169], v196 offset:8192
v_mfma_f32_16x16x32_bf16 a[52:55], v[68:71], v[116:119], a[52:55]
buffer_load_dwordx4 v19, s[8:11], s42 offen offset:3072 nt lds
v_mfma_f32_16x16x32_bf16 a[56:59], v[68:71], v[120:123], a[56:59]
ds_read_b64_tr_b16 v[170:171], v196 offset:9216
v_mfma_f32_16x16x32_bf16 a[60:63], v[68:71], v[124:127], a[60:63]
v_mfma_f32_16x16x32_bf16 a[64:67], v[72:75], v[96:99], a[64:67]
s_add_u32 m0, s37, 4096
ds_read_b128 v[136:139], v193 offset:4096
v_mfma_f32_16x16x32_bf16 a[68:71], v[72:75], v[100:103], a[68:71]
buffer_load_dwordx4 v16, s[8:11], s43 offen offset:0 nt lds
v_mfma_f32_16x16x32_bf16 a[72:75], v[72:75], v[104:107], a[72:75]
ds_read_b64_tr_b16 v[172:173], v197 offset:8192
v_mfma_f32_16x16x32_bf16 a[76:79], v[72:75], v[108:111], a[76:79]
v_mfma_f32_16x16x32_bf16 a[80:83], v[72:75], v[112:115], a[80:83]
ds_read_b64_tr_b16 v[174:175], v197 offset:9216
v_mfma_f32_16x16x32_bf16 a[84:87], v[72:75], v[116:119], a[84:87]
buffer_load_dwordx4 v17, s[8:11], s43 offen offset:1024 nt lds
v_mfma_f32_16x16x32_bf16 a[88:91], v[72:75], v[120:123], a[88:91]
ds_read_b128 v[140:143], v193 offset:6144
v_mfma_f32_16x16x32_bf16 a[92:95], v[72:75], v[124:127], a[92:95]
v_mfma_f32_16x16x32_bf16 a[96:99], v[76:79], v[96:99], a[96:99]
ds_read_b64_tr_b16 v[176:177], v198 offset:8192
v_mfma_f32_16x16x32_bf16 a[100:103], v[76:79], v[100:103], a[100:103]
buffer_load_dwordx4 v18, s[8:11], s43 offen offset:2048 nt lds
v_mfma_f32_16x16x32_bf16 a[104:107], v[76:79], v[104:107], a[104:107]
ds_read_b64_tr_b16 v[178:179], v198 offset:9216
v_mfma_f32_16x16x32_bf16 a[108:111], v[76:79], v[108:111], a[108:111]
v_mfma_f32_16x16x32_bf16 a[112:115], v[76:79], v[112:115], a[112:115]
ds_read_b128 v[144:147], v193 offset:8192
v_mfma_f32_16x16x32_bf16 a[116:119], v[76:79], v[116:119], a[116:119]
buffer_load_dwordx4 v19, s[8:11], s43 offen offset:3072 nt lds
v_mfma_f32_16x16x32_bf16 a[120:123], v[76:79], v[120:123], a[120:123]
ds_read_b64_tr_b16 v[180:181], v199 offset:8192
v_mfma_f32_16x16x32_bf16 a[124:127], v[76:79], v[124:127], a[124:127]
v_mfma_f32_16x16x32_bf16 a[128:131], v[80:83], v[96:99], a[128:131]
ds_read_b64_tr_b16 v[182:183], v199 offset:9216
v_mfma_f32_16x16x32_bf16 a[132:135], v[80:83], v[100:103], a[132:135]
v_mfma_f32_16x16x32_bf16 a[136:139], v[80:83], v[104:107], a[136:139]
ds_read_b128 v[148:151], v193 offset:10240
v_mfma_f32_16x16x32_bf16 a[140:143], v[80:83], v[108:111], a[140:143]
v_mfma_f32_16x16x32_bf16 a[144:147], v[80:83], v[112:115], a[144:147]
ds_read_b64_tr_b16 v[184:185], v200 offset:8192
v_mfma_f32_16x16x32_bf16 a[148:151], v[80:83], v[116:119], a[148:151]
v_mfma_f32_16x16x32_bf16 a[152:155], v[80:83], v[120:123], a[152:155]
ds_read_b64_tr_b16 v[186:187], v200 offset:9216
v_mfma_f32_16x16x32_bf16 a[156:159], v[80:83], v[124:127], a[156:159]
v_mfma_f32_16x16x32_bf16 a[160:163], v[84:87], v[96:99], a[160:163]
s_add_u32 s42, s42, 524288
ds_read_b128 v[152:155], v193 offset:12288
v_mfma_f32_16x16x32_bf16 a[164:167], v[84:87], v[100:103], a[164:167]
s_add_u32 s43, s43, 524288
v_mfma_f32_16x16x32_bf16 a[168:171], v[84:87], v[104:107], a[168:171]
ds_read_b64_tr_b16 v[188:189], v201 offset:8192
v_mfma_f32_16x16x32_bf16 a[172:175], v[84:87], v[108:111], a[172:175]
v_mfma_f32_16x16x32_bf16 a[176:179], v[84:87], v[112:115], a[176:179]
s_add_u32 s44, s35, 65536
ds_read_b64_tr_b16 v[190:191], v201 offset:9216
v_mfma_f32_16x16x32_bf16 a[180:183], v[84:87], v[116:119], a[180:183]
s_cmp_ge_u32 s44, s39
s_cselect_b32 s45, s55, s54
v_mfma_f32_16x16x32_bf16 a[184:187], v[84:87], v[120:123], a[184:187]
ds_read_b128 v[156:159], v193 offset:14336
v_mfma_f32_16x16x32_bf16 a[188:191], v[84:87], v[124:127], a[188:191]
s_add_u32 s44, s36, 65536
v_mfma_f32_16x16x32_bf16 a[192:195], v[88:91], v[96:99], a[192:195]
v_mfma_f32_16x16x32_bf16 a[196:199], v[88:91], v[100:103], a[196:199]
s_cmp_ge_u32 s44, s39
s_cselect_b32 s34, s55, s54
v_mfma_f32_16x16x32_bf16 a[200:203], v[88:91], v[104:107], a[200:203]
v_add_u32 v194, s45, v194
v_mfma_f32_16x16x32_bf16 a[204:207], v[88:91], v[108:111], a[204:207]
v_add_u32 v195, s45, v195
v_mfma_f32_16x16x32_bf16 a[208:211], v[88:91], v[112:115], a[208:211]
v_add_u32 v196, s45, v196
v_mfma_f32_16x16x32_bf16 a[212:215], v[88:91], v[116:119], a[212:215]
v_add_u32 v197, s45, v197
v_mfma_f32_16x16x32_bf16 a[216:219], v[88:91], v[120:123], a[216:219]
v_add_u32 v198, s45, v198
v_mfma_f32_16x16x32_bf16 a[220:223], v[88:91], v[124:127], a[220:223]
v_add_u32 v199, s45, v199
v_mfma_f32_16x16x32_bf16 a[224:227], v[92:95], v[96:99], a[224:227]
v_add_u32 v200, s45, v200
v_mfma_f32_16x16x32_bf16 a[228:231], v[92:95], v[100:103], a[228:231]
v_add_u32 v201, s45, v201
v_mfma_f32_16x16x32_bf16 a[232:235], v[92:95], v[104:107], a[232:235]
v_add_u32 v192, s34, v192
v_mfma_f32_16x16x32_bf16 a[236:239], v[92:95], v[108:111], a[236:239]
v_add_u32 v193, s34, v193
v_mfma_f32_16x16x32_bf16 a[240:243], v[92:95], v[112:115], a[240:243]
v_mfma_f32_16x16x32_bf16 a[244:247], v[92:95], v[116:119], a[244:247]
v_mfma_f32_16x16x32_bf16 a[248:251], v[92:95], v[120:123], a[248:251]
v_mfma_f32_16x16x32_bf16 a[252:255], v[92:95], v[124:127], a[252:255]
s_waitcnt vmcnt(9) lgkmcnt(0)
s_barrier
v_mfma_f32_16x16x32_bf16 a[0:3], v[128:131], v[160:163], a[0:3]
s_mov_b32 m0, s35
ds_read_b64_tr_b16 v[96:97], v194 offset:0
v_mfma_f32_16x16x32_bf16 a[4:7], v[128:131], v[164:167], a[4:7]
buffer_load_dwordx4 v202, s[4:7], s40 offen offset:0 lds
v_mfma_f32_16x16x32_bf16 a[8:11], v[128:131], v[168:171], a[8:11]
ds_read_b64_tr_b16 v[98:99], v194 offset:1024
v_mfma_f32_16x16x32_bf16 a[12:15], v[128:131], v[172:175], a[12:15]
v_mfma_f32_16x16x32_bf16 a[16:19], v[128:131], v[176:179], a[16:19]
ds_read_b128 v[64:67], v192 offset:0
v_mfma_f32_16x16x32_bf16 a[20:23], v[128:131], v[180:183], a[20:23]
buffer_load_dwordx4 v203, s[4:7], s40 offen offset:1024 lds
v_mfma_f32_16x16x32_bf16 a[24:27], v[128:131], v[184:187], a[24:27]
ds_read_b64_tr_b16 v[100:101], v195 offset:0
v_mfma_f32_16x16x32_bf16 a[28:31], v[128:131], v[188:191], a[28:31]
v_mfma_f32_16x16x32_bf16 a[32:35], v[132:135], v[160:163], a[32:35]
ds_read_b64_tr_b16 v[102:103], v195 offset:1024
v_mfma_f32_16x16x32_bf16 a[36:39], v[132:135], v[164:167], a[36:39]
buffer_load_dwordx4 v204, s[4:7], s40 offen offset:2048 lds
v_mfma_f32_16x16x32_bf16 a[40:43], v[132:135], v[168:171], a[40:43]
ds_read_b128 v[68:71], v192 offset:2048
v_mfma_f32_16x16x32_bf16 a[44:47], v[132:135], v[172:175], a[44:47]
v_mfma_f32_16x16x32_bf16 a[48:51], v[132:135], v[176:179], a[48:51]
ds_read_b64_tr_b16 v[104:105], v196 offset:0
v_mfma_f32_16x16x32_bf16 a[52:55], v[132:135], v[180:183], a[52:55]
buffer_load_dwordx4 v205, s[4:7], s40 offen offset:3072 lds
v_mfma_f32_16x16x32_bf16 a[56:59], v[132:135], v[184:187], a[56:59]
ds_read_b64_tr_b16 v[106:107], v196 offset:1024
v_mfma_f32_16x16x32_bf16 a[60:63], v[132:135], v[188:191], a[60:63]
v_mfma_f32_16x16x32_bf16 a[64:67], v[136:139], v[160:163], a[64:67]
s_add_u32 m0, s35, 4096
ds_read_b128 v[72:75], v192 offset:4096
v_mfma_f32_16x16x32_bf16 a[68:71], v[136:139], v[164:167], a[68:71]
buffer_load_dwordx4 v202, s[4:7], s41 offen offset:0 lds
v_mfma_f32_16x16x32_bf16 a[72:75], v[136:139], v[168:171], a[72:75]
ds_read_b64_tr_b16 v[108:109], v197 offset:0
v_mfma_f32_16x16x32_bf16 a[76:79], v[136:139], v[172:175], a[76:79]
v_mfma_f32_16x16x32_bf16 a[80:83], v[136:139], v[176:179], a[80:83]
ds_read_b64_tr_b16 v[110:111], v197 offset:1024
v_mfma_f32_16x16x32_bf16 a[84:87], v[136:139], v[180:183], a[84:87]
buffer_load_dwordx4 v203, s[4:7], s41 offen offset:1024 lds
v_mfma_f32_16x16x32_bf16 a[88:91], v[136:139], v[184:187], a[88:91]
ds_read_b128 v[76:79], v192 offset:6144
v_mfma_f32_16x16x32_bf16 a[92:95], v[136:139], v[188:191], a[92:95]
v_mfma_f32_16x16x32_bf16 a[96:99], v[140:143], v[160:163], a[96:99]
ds_read_b64_tr_b16 v[112:113], v198 offset:0
v_mfma_f32_16x16x32_bf16 a[100:103], v[140:143], v[164:167], a[100:103]
buffer_load_dwordx4 v204, s[4:7], s41 offen offset:2048 lds
v_mfma_f32_16x16x32_bf16 a[104:107], v[140:143], v[168:171], a[104:107]
ds_read_b64_tr_b16 v[114:115], v198 offset:1024
v_mfma_f32_16x16x32_bf16 a[108:111], v[140:143], v[172:175], a[108:111]
v_mfma_f32_16x16x32_bf16 a[112:115], v[140:143], v[176:179], a[112:115]
ds_read_b128 v[80:83], v192 offset:8192
v_mfma_f32_16x16x32_bf16 a[116:119], v[140:143], v[180:183], a[116:119]
buffer_load_dwordx4 v205, s[4:7], s41 offen offset:3072 lds
v_mfma_f32_16x16x32_bf16 a[120:123], v[140:143], v[184:187], a[120:123]
ds_read_b64_tr_b16 v[116:117], v199 offset:0
v_mfma_f32_16x16x32_bf16 a[124:127], v[140:143], v[188:191], a[124:127]
v_mfma_f32_16x16x32_bf16 a[128:131], v[144:147], v[160:163], a[128:131]
ds_read_b64_tr_b16 v[118:119], v199 offset:1024
v_mfma_f32_16x16x32_bf16 a[132:135], v[144:147], v[164:167], a[132:135]
global_load_dword v35, v[206:207], off
v_mfma_f32_16x16x32_bf16 a[136:139], v[144:147], v[168:171], a[136:139]
ds_read_b128 v[84:87], v192 offset:10240
v_mfma_f32_16x16x32_bf16 a[140:143], v[144:147], v[172:175], a[140:143]
v_mfma_f32_16x16x32_bf16 a[144:147], v[144:147], v[176:179], a[144:147]
ds_read_b64_tr_b16 v[120:121], v200 offset:0
v_mfma_f32_16x16x32_bf16 a[148:151], v[144:147], v[180:183], a[148:151]
v_mfma_f32_16x16x32_bf16 a[152:155], v[144:147], v[184:187], a[152:155]
ds_read_b64_tr_b16 v[122:123], v200 offset:1024
.Lpad3:
s_cbranch_execz .Ltch0
v_mfma_f32_16x16x32_bf16 a[156:159], v[144:147], v[188:191], a[156:159]
v_mfma_f32_16x16x32_bf16 a[160:163], v[148:151], v[160:163], a[160:163]
s_add_u32 s40, s40, 128
ds_read_b128 v[88:91], v192 offset:12288
v_mfma_f32_16x16x32_bf16 a[164:167], v[148:151], v[164:167], a[164:167]
s_add_u32 s41, s41, 128
v_mfma_f32_16x16x32_bf16 a[168:171], v[148:151], v[168:171], a[168:171]
ds_read_b64_tr_b16 v[124:125], v201 offset:0
v_mfma_f32_16x16x32_bf16 a[172:175], v[148:151], v[172:175], a[172:175]
v_add_co_u32 v206, vcc, v208, v206
v_mfma_f32_16x16x32_bf16 a[176:179], v[148:151], v[176:179], a[176:179]
ds_read_b64_tr_b16 v[126:127], v201 offset:1024
v_mfma_f32_16x16x32_bf16 a[180:183], v[148:151], v[180:183], a[180:183]
v_addc_co_u32 v207, vcc, 0, v207, vcc
v_mfma_f32_16x16x32_bf16 a[184:187], v[148:151], v[184:187], a[184:187]
ds_read_b128 v[92:95], v192 offset:14336
v_mfma_f32_16x16x32_bf16 a[188:191], v[148:151], v[188:191], a[188:191]
v_mfma_f32_16x16x32_bf16 a[192:195], v[152:155], v[160:163], a[192:195]
v_mfma_f32_16x16x32_bf16 a[196:199], v[152:155], v[164:167], a[196:199]
v_mfma_f32_16x16x32_bf16 a[200:203], v[152:155], v[168:171], a[200:203]
v_mfma_f32_16x16x32_bf16 a[204:207], v[152:155], v[172:175], a[204:207]
v_mfma_f32_16x16x32_bf16 a[208:211], v[152:155], v[176:179], a[208:211]
v_mfma_f32_16x16x32_bf16 a[212:215], v[152:155], v[180:183], a[212:215]
v_mfma_f32_16x16x32_bf16 a[216:219], v[152:155], v[184:187], a[216:219]
v_mfma_f32_16x16x32_bf16 a[220:223], v[152:155], v[188:191], a[220:223]
v_mfma_f32_16x16x32_bf16 a[224:227], v[156:159], v[160:163], a[224:227]
v_mfma_f32_16x16x32_bf16 a[228:231], v[156:159], v[164:167], a[228:231]
s_mov_b32 s37, s36
v_mfma_f32_16x16x32_bf16 a[232:235], v[156:159], v[168:171], a[232:235]
s_add_u32 s35, s35, s45
v_mfma_f32_16x16x32_bf16 a[236:239], v[156:159], v[172:175], a[236:239]
s_add_u32 s36, s36, s34
v_mfma_f32_16x16x32_bf16 a[240:243], v[156:159], v[176:179], a[240:243]
s_add_u32 s38, s38, 1
v_mfma_f32_16x16x32_bf16 a[244:247], v[156:159], v[180:183], a[244:247]
s_cmp_lt_u32 s38, 6
v_mfma_f32_16x16x32_bf16 a[248:251], v[156:159], v[184:187], a[248:251]
v_mfma_f32_16x16x32_bf16 a[252:255], v[156:159], v[188:191], a[252:255]
s_waitcnt lgkmcnt(0)
s_cbranch_scc1 .Lp1_vloop
v_mfma_f32_16x16x32_bf16 a[0:3], v[64:67], v[96:99], a[0:3]
s_add_u32 m0, s92, 32768
ds_read_b64_tr_b16 v[160:161], v194 offset:8192
v_mfma_f32_16x16x32_bf16 a[4:7], v[64:67], v[100:103], a[4:7]
buffer_load_dwordx4 v20, s[12:15], s24 offen offset:0 lds
v_mfma_f32_16x16x32_bf16 a[8:11], v[64:67], v[104:107], a[8:11]
ds_read_b64_tr_b16 v[162:163], v194 offset:9216
v_mfma_f32_16x16x32_bf16 a[12:15], v[64:67], v[108:111], a[12:15]
v_mfma_f32_16x16x32_bf16 a[16:19], v[64:67], v[112:115], a[16:19]
ds_read_b128 v[128:131], v193 offset:0
v_mfma_f32_16x16x32_bf16 a[20:23], v[64:67], v[116:119], a[20:23]
buffer_load_dwordx4 v21, s[12:15], s24 offen offset:1024 lds
v_mfma_f32_16x16x32_bf16 a[24:27], v[64:67], v[120:123], a[24:27]
ds_read_b64_tr_b16 v[164:165], v195 offset:8192
v_mfma_f32_16x16x32_bf16 a[28:31], v[64:67], v[124:127], a[28:31]
v_mfma_f32_16x16x32_bf16 a[32:35], v[68:71], v[96:99], a[32:35]
ds_read_b64_tr_b16 v[166:167], v195 offset:9216
v_mfma_f32_16x16x32_bf16 a[36:39], v[68:71], v[100:103], a[36:39]
buffer_load_dwordx4 v22, s[12:15], s24 offen offset:2048 lds
v_mfma_f32_16x16x32_bf16 a[40:43], v[68:71], v[104:107], a[40:43]
ds_read_b128 v[132:135], v193 offset:2048
v_mfma_f32_16x16x32_bf16 a[44:47], v[68:71], v[108:111], a[44:47]
v_mfma_f32_16x16x32_bf16 a[48:51], v[68:71], v[112:115], a[48:51]
ds_read_b64_tr_b16 v[168:169], v196 offset:8192
v_mfma_f32_16x16x32_bf16 a[52:55], v[68:71], v[116:119], a[52:55]
buffer_load_dwordx4 v23, s[12:15], s24 offen offset:3072 lds
v_mfma_f32_16x16x32_bf16 a[56:59], v[68:71], v[120:123], a[56:59]
ds_read_b64_tr_b16 v[170:171], v196 offset:9216
v_mfma_f32_16x16x32_bf16 a[60:63], v[68:71], v[124:127], a[60:63]
v_mfma_f32_16x16x32_bf16 a[64:67], v[72:75], v[96:99], a[64:67]
s_add_u32 m0, s92, 36864
ds_read_b128 v[136:139], v193 offset:4096
v_mfma_f32_16x16x32_bf16 a[68:71], v[72:75], v[100:103], a[68:71]
buffer_load_dwordx4 v20, s[12:15], s25 offen offset:0 lds
v_mfma_f32_16x16x32_bf16 a[72:75], v[72:75], v[104:107], a[72:75]
ds_read_b64_tr_b16 v[172:173], v197 offset:8192
v_mfma_f32_16x16x32_bf16 a[76:79], v[72:75], v[108:111], a[76:79]
v_mfma_f32_16x16x32_bf16 a[80:83], v[72:75], v[112:115], a[80:83]
ds_read_b64_tr_b16 v[174:175], v197 offset:9216
v_mfma_f32_16x16x32_bf16 a[84:87], v[72:75], v[116:119], a[84:87]
buffer_load_dwordx4 v21, s[12:15], s25 offen offset:1024 lds
v_mfma_f32_16x16x32_bf16 a[88:91], v[72:75], v[120:123], a[88:91]
ds_read_b128 v[140:143], v193 offset:6144
v_mfma_f32_16x16x32_bf16 a[92:95], v[72:75], v[124:127], a[92:95]
v_mfma_f32_16x16x32_bf16 a[96:99], v[76:79], v[96:99], a[96:99]
ds_read_b64_tr_b16 v[176:177], v198 offset:8192
v_mfma_f32_16x16x32_bf16 a[100:103], v[76:79], v[100:103], a[100:103]
buffer_load_dwordx4 v22, s[12:15], s25 offen offset:2048 lds
v_mfma_f32_16x16x32_bf16 a[104:107], v[76:79], v[104:107], a[104:107]
ds_read_b64_tr_b16 v[178:179], v198 offset:9216
v_mfma_f32_16x16x32_bf16 a[108:111], v[76:79], v[108:111], a[108:111]
v_mfma_f32_16x16x32_bf16 a[112:115], v[76:79], v[112:115], a[112:115]
ds_read_b128 v[144:147], v193 offset:8192
v_mfma_f32_16x16x32_bf16 a[116:119], v[76:79], v[116:119], a[116:119]
buffer_load_dwordx4 v23, s[12:15], s25 offen offset:3072 lds
v_mfma_f32_16x16x32_bf16 a[120:123], v[76:79], v[120:123], a[120:123]
ds_read_b64_tr_b16 v[180:181], v199 offset:8192
v_mfma_f32_16x16x32_bf16 a[124:127], v[76:79], v[124:127], a[124:127]
v_mfma_f32_16x16x32_bf16 a[128:131], v[80:83], v[96:99], a[128:131]
ds_read_b64_tr_b16 v[182:183], v199 offset:9216
v_mfma_f32_16x16x32_bf16 a[132:135], v[80:83], v[100:103], a[132:135]
v_mfma_f32_16x16x32_bf16 a[136:139], v[80:83], v[104:107], a[136:139]
ds_read_b128 v[148:151], v193 offset:10240
v_mfma_f32_16x16x32_bf16 a[140:143], v[80:83], v[108:111], a[140:143]
v_mfma_f32_16x16x32_bf16 a[144:147], v[80:83], v[112:115], a[144:147]
ds_read_b64_tr_b16 v[184:185], v200 offset:8192
v_mfma_f32_16x16x32_bf16 a[148:151], v[80:83], v[116:119], a[148:151]
v_mfma_f32_16x16x32_bf16 a[152:155], v[80:83], v[120:123], a[152:155]
ds_read_b64_tr_b16 v[186:187], v200 offset:9216
v_mfma_f32_16x16x32_bf16 a[156:159], v[80:83], v[124:127], a[156:159]
v_mfma_f32_16x16x32_bf16 a[160:163], v[84:87], v[96:99], a[160:163]
s_add_u32 s24, s24, 128
s_add_u32 s25, s25, 128
ds_read_b128 v[152:155], v193 offset:12288
v_mfma_f32_16x16x32_bf16 a[164:167], v[84:87], v[100:103], a[164:167]
v_mfma_f32_16x16x32_bf16 a[168:171], v[84:87], v[104:107], a[168:171]
ds_read_b64_tr_b16 v[188:189], v201 offset:8192
v_mfma_f32_16x16x32_bf16 a[172:175], v[84:87], v[108:111], a[172:175]
v_mfma_f32_16x16x32_bf16 a[176:179], v[84:87], v[112:115], a[176:179]
s_add_u32 s44, s35, 65536
ds_read_b64_tr_b16 v[190:191], v201 offset:9216
v_mfma_f32_16x16x32_bf16 a[180:183], v[84:87], v[116:119], a[180:183]
s_cmp_ge_u32 s44, s39
s_cselect_b32 s45, s55, s54
v_mfma_f32_16x16x32_bf16 a[184:187], v[84:87], v[120:123], a[184:187]
ds_read_b128 v[156:159], v193 offset:14336
v_mfma_f32_16x16x32_bf16 a[188:191], v[84:87], v[124:127], a[188:191]
s_add_u32 s44, s36, 65536
v_mfma_f32_16x16x32_bf16 a[192:195], v[88:91], v[96:99], a[192:195]
v_mfma_f32_16x16x32_bf16 a[196:199], v[88:91], v[100:103], a[196:199]
s_cmp_ge_u32 s44, s39
s_cselect_b32 s34, s55, s54
v_mfma_f32_16x16x32_bf16 a[200:203], v[88:91], v[104:107], a[200:203]
v_add_u32 v194, s45, v194
v_mfma_f32_16x16x32_bf16 a[204:207], v[88:91], v[108:111], a[204:207]
v_add_u32 v195, s45, v195
v_mfma_f32_16x16x32_bf16 a[208:211], v[88:91], v[112:115], a[208:211]
v_add_u32 v196, s45, v196
v_mfma_f32_16x16x32_bf16 a[212:215], v[88:91], v[116:119], a[212:215]
v_add_u32 v197, s45, v197
v_mfma_f32_16x16x32_bf16 a[216:219], v[88:91], v[120:123], a[216:219]
v_add_u32 v198, s45, v198
v_mfma_f32_16x16x32_bf16 a[220:223], v[88:91], v[124:127], a[220:223]
v_add_u32 v199, s45, v199
v_mfma_f32_16x16x32_bf16 a[224:227], v[92:95], v[96:99], a[224:227]
v_add_u32 v200, s45, v200
v_mfma_f32_16x16x32_bf16 a[228:231], v[92:95], v[100:103], a[228:231]
v_add_u32 v201, s45, v201
v_mfma_f32_16x16x32_bf16 a[232:235], v[92:95], v[104:107], a[232:235]
v_add_u32 v192, s34, v192
v_mfma_f32_16x16x32_bf16 a[236:239], v[92:95], v[108:111], a[236:239]
v_add_u32 v193, s34, v193
v_mfma_f32_16x16x32_bf16 a[240:243], v[92:95], v[112:115], a[240:243]
v_mfma_f32_16x16x32_bf16 a[244:247], v[92:95], v[116:119], a[244:247]
v_mfma_f32_16x16x32_bf16 a[248:251], v[92:95], v[120:123], a[248:251]
v_mfma_f32_16x16x32_bf16 a[252:255], v[92:95], v[124:127], a[252:255]
s_waitcnt vmcnt(9) lgkmcnt(0)
s_barrier
v_mfma_f32_16x16x32_bf16 a[0:3], v[128:131], v[160:163], a[0:3]
s_add_u32 m0, s91, 65536
ds_read_b64_tr_b16 v[96:97], v194 offset:0
v_mfma_f32_16x16x32_bf16 a[4:7], v[128:131], v[164:167], a[4:7]
buffer_load_dwordx4 v16, s[16:19], s26 offen offset:0 lds
v_mfma_f32_16x16x32_bf16 a[8:11], v[128:131], v[168:171], a[8:11]
ds_read_b64_tr_b16 v[98:99], v194 offset:1024
v_mfma_f32_16x16x32_bf16 a[12:15], v[128:131], v[172:175], a[12:15]
v_mfma_f32_16x16x32_bf16 a[16:19], v[128:131], v[176:179], a[16:19]
ds_read_b128 v[64:67], v192 offset:0
v_mfma_f32_16x16x32_bf16 a[20:23], v[128:131], v[180:183], a[20:23]
buffer_load_dwordx4 v17, s[16:19], s26 offen offset:1024 lds
v_mfma_f32_16x16x32_bf16 a[24:27], v[128:131], v[184:187], a[24:27]
ds_read_b64_tr_b16 v[100:101], v195 offset:0
v_mfma_f32_16x16x32_bf16 a[28:31], v[128:131], v[188:191], a[28:31]
v_mfma_f32_16x16x32_bf16 a[32:35], v[132:135], v[160:163], a[32:35]
ds_read_b64_tr_b16 v[102:103], v195 offset:1024
v_mfma_f32_16x16x32_bf16 a[36:39], v[132:135], v[164:167], a[36:39]
buffer_load_dwordx4 v18, s[16:19], s26 offen offset:2048 lds
v_mfma_f32_16x16x32_bf16 a[40:43], v[132:135], v[168:171], a[40:43]
ds_read_b128 v[68:71], v192 offset:2048
v_mfma_f32_16x16x32_bf16 a[44:47], v[132:135], v[172:175], a[44:47]
v_mfma_f32_16x16x32_bf16 a[48:51], v[132:135], v[176:179], a[48:51]
ds_read_b64_tr_b16 v[104:105], v196 offset:0
v_mfma_f32_16x16x32_bf16 a[52:55], v[132:135], v[180:183], a[52:55]
buffer_load_dwordx4 v19, s[16:19], s26 offen offset:3072 lds
v_mfma_f32_16x16x32_bf16 a[56:59], v[132:135], v[184:187], a[56:59]
ds_read_b64_tr_b16 v[106:107], v196 offset:1024
v_mfma_f32_16x16x32_bf16 a[60:63], v[132:135], v[188:191], a[60:63]
s_add_u32 s26, s26, 524288
v_mfma_f32_16x16x32_bf16 a[64:67], v[136:139], v[160:163], a[64:67]
s_add_u32 m0, s91, 81920
ds_read_b128 v[72:75], v192 offset:4096
v_mfma_f32_16x16x32_bf16 a[68:71], v[136:139], v[164:167], a[68:71]
buffer_load_dwordx4 v16, s[16:19], s26 offen offset:0 lds
v_mfma_f32_16x16x32_bf16 a[72:75], v[136:139], v[168:171], a[72:75]
ds_read_b64_tr_b16 v[108:109], v197 offset:0
v_mfma_f32_16x16x32_bf16 a[76:79], v[136:139], v[172:175], a[76:79]
v_mfma_f32_16x16x32_bf16 a[80:83], v[136:139], v[176:179], a[80:83]
ds_read_b64_tr_b16 v[110:111], v197 offset:1024
v_mfma_f32_16x16x32_bf16 a[84:87], v[136:139], v[180:183], a[84:87]
buffer_load_dwordx4 v17, s[16:19], s26 offen offset:1024 lds
v_mfma_f32_16x16x32_bf16 a[88:91], v[136:139], v[184:187], a[88:91]
ds_read_b128 v[76:79], v192 offset:6144
v_mfma_f32_16x16x32_bf16 a[92:95], v[136:139], v[188:191], a[92:95]
v_mfma_f32_16x16x32_bf16 a[96:99], v[140:143], v[160:163], a[96:99]
ds_read_b64_tr_b16 v[112:113], v198 offset:0
v_mfma_f32_16x16x32_bf16 a[100:103], v[140:143], v[164:167], a[100:103]
buffer_load_dwordx4 v18, s[16:19], s26 offen offset:2048 lds
v_mfma_f32_16x16x32_bf16 a[104:107], v[140:143], v[168:171], a[104:107]
ds_read_b64_tr_b16 v[114:115], v198 offset:1024
v_mfma_f32_16x16x32_bf16 a[108:111], v[140:143], v[172:175], a[108:111]
v_mfma_f32_16x16x32_bf16 a[112:115], v[140:143], v[176:179], a[112:115]
ds_read_b128 v[80:83], v192 offset:8192
v_mfma_f32_16x16x32_bf16 a[116:119], v[140:143], v[180:183], a[116:119]
buffer_load_dwordx4 v19, s[16:19], s26 offen offset:3072 lds
v_mfma_f32_16x16x32_bf16 a[120:123], v[140:143], v[184:187], a[120:123]
ds_read_b64_tr_b16 v[116:117], v199 offset:0
v_mfma_f32_16x16x32_bf16 a[124:127], v[140:143], v[188:191], a[124:127]
s_add_u32 s26, s26, 524288
v_mfma_f32_16x16x32_bf16 a[128:131], v[144:147], v[160:163], a[128:131]
ds_read_b64_tr_b16 v[118:119], v199 offset:1024
v_mfma_f32_16x16x32_bf16 a[132:135], v[144:147], v[164:167], a[132:135]
global_load_dword v35, v[206:207], off
v_mfma_f32_16x16x32_bf16 a[136:139], v[144:147], v[168:171], a[136:139]
ds_read_b128 v[84:87], v192 offset:10240
v_mfma_f32_16x16x32_bf16 a[140:143], v[144:147], v[172:175], a[140:143]
v_mfma_f32_16x16x32_bf16 a[144:147], v[144:147], v[176:179], a[144:147]
ds_read_b64_tr_b16 v[120:121], v200 offset:0
v_mfma_f32_16x16x32_bf16 a[148:151], v[144:147], v[180:183], a[148:151]
v_add_u32 v11, 8, v11
v_and_b32 v11, -8, v11
v_mfma_f32_16x16x32_bf16 a[152:155], v[144:147], v[184:187], a[152:155]
ds_read_b64_tr_b16 v[122:123], v200 offset:1024
v_mfma_f32_16x16x32_bf16 a[156:159], v[144:147], v[188:191], a[156:159]
v_mfma_f32_16x16x32_bf16 a[160:163], v[148:151], v[160:163], a[160:163]
ds_read_b128 v[88:91], v192 offset:12288
v_mfma_f32_16x16x32_bf16 a[164:167], v[148:151], v[164:167], a[164:167]
s_nop 0
v_or_b32 v0, 0, v11
v_mfma_f32_16x16x32_bf16 a[168:171], v[148:151], v[168:171], a[168:171]
ds_read_b64_tr_b16 v[124:125], v201 offset:0
v_mfma_f32_16x16x32_bf16 a[172:175], v[148:151], v[172:175], a[172:175]
v_add_co_u32 v206, vcc, v208, v206
v_mfma_f32_16x16x32_bf16 a[176:179], v[148:151], v[176:179], a[176:179]
ds_read_b64_tr_b16 v[126:127], v201 offset:1024
v_mfma_f32_16x16x32_bf16 a[180:183], v[148:151], v[180:183], a[180:183]
v_addc_co_u32 v207, vcc, 0, v207, vcc
v_mfma_f32_16x16x32_bf16 a[184:187], v[148:151], v[184:187], a[184:187]
ds_read_b128 v[92:95], v192 offset:14336
v_mfma_f32_16x16x32_bf16 a[188:191], v[148:151], v[188:191], a[188:191]
buffer_store_dword v0, v6, s[60:63], 0 offen sc1
v_mfma_f32_16x16x32_bf16 a[192:195], v[152:155], v[160:163], a[192:195]
v_mfma_f32_16x16x32_bf16 a[196:199], v[152:155], v[164:167], a[196:199]
v_mfma_f32_16x16x32_bf16 a[200:203], v[152:155], v[168:171], a[200:203]
v_mfma_f32_16x16x32_bf16 a[204:207], v[152:155], v[172:175], a[204:207]
buffer_load_dword v35, v4, s[96:99], 0 offen
v_mfma_f32_16x16x32_bf16 a[208:211], v[152:155], v[176:179], a[208:211]
v_mfma_f32_16x16x32_bf16 a[212:215], v[152:155], v[180:183], a[212:215]
v_mfma_f32_16x16x32_bf16 a[216:219], v[152:155], v[184:187], a[216:219]
v_mfma_f32_16x16x32_bf16 a[220:223], v[152:155], v[188:191], a[220:223]
v_mfma_f32_16x16x32_bf16 a[224:227], v[156:159], v[160:163], a[224:227]
v_mfma_f32_16x16x32_bf16 a[228:231], v[156:159], v[164:167], a[228:231]
s_mov_b32 s37, s36
v_mfma_f32_16x16x32_bf16 a[232:235], v[156:159], v[168:171], a[232:235]
s_add_u32 s35, s35, s45
v_mfma_f32_16x16x32_bf16 a[236:239], v[156:159], v[172:175], a[236:239]
s_add_u32 s36, s36, s34
v_mfma_f32_16x16x32_bf16 a[240:243], v[156:159], v[176:179], a[240:243]
v_mfma_f32_16x16x32_bf16 a[244:247], v[156:159], v[180:183], a[244:247]
v_mfma_f32_16x16x32_bf16 a[248:251], v[156:159], v[184:187], a[248:251]
v_mfma_f32_16x16x32_bf16 a[252:255], v[156:159], v[188:191], a[252:255]
s_waitcnt lgkmcnt(0)
v_mfma_f32_16x16x32_bf16 a[0:3], v[64:67], v[96:99], a[0:3]
s_add_u32 m0, s92, 98304
ds_read_b64_tr_b16 v[160:161], v194 offset:8192
v_mfma_f32_16x16x32_bf16 a[4:7], v[64:67], v[100:103], a[4:7]
buffer_load_dwordx4 v20, s[12:15], s24 offen offset:0 lds
v_mfma_f32_16x16x32_bf16 a[8:11], v[64:67], v[104:107], a[8:11]
ds_read_b64_tr_b16 v[162:163], v194 offset:9216
v_mfma_f32_16x16x32_bf16 a[12:15], v[64:67], v[108:111], a[12:15]
v_mfma_f32_16x16x32_bf16 a[16:19], v[64:67], v[112:115], a[16:19]
ds_read_b128 v[128:131], v193 offset:0
v_mfma_f32_16x16x32_bf16 a[20:23], v[64:67], v[116:119], a[20:23]
buffer_load_dwordx4 v21, s[12:15], s24 offen offset:1024 lds
v_mfma_f32_16x16x32_bf16 a[24:27], v[64:67], v[120:123], a[24:27]
ds_read_b64_tr_b16 v[164:165], v195 offset:8192
v_mfma_f32_16x16x32_bf16 a[28:31], v[64:67], v[124:127], a[28:31]
v_mfma_f32_16x16x32_bf16 a[32:35], v[68:71], v[96:99], a[32:35]
ds_read_b64_tr_b16 v[166:167], v195 offset:9216
v_mfma_f32_16x16x32_bf16 a[36:39], v[68:71], v[100:103], a[36:39]
buffer_load_dwordx4 v22, s[12:15], s24 offen offset:2048 lds
v_mfma_f32_16x16x32_bf16 a[40:43], v[68:71], v[104:107], a[40:43]
ds_read_b128 v[132:135], v193 offset:2048
v_mfma_f32_16x16x32_bf16 a[44:47], v[68:71], v[108:111], a[44:47]
v_mfma_f32_16x16x32_bf16 a[48:51], v[68:71], v[112:115], a[48:51]
ds_read_b64_tr_b16 v[168:169], v196 offset:8192
v_mfma_f32_16x16x32_bf16 a[52:55], v[68:71], v[116:119], a[52:55]
buffer_load_dwordx4 v23, s[12:15], s24 offen offset:3072 lds
v_mfma_f32_16x16x32_bf16 a[56:59], v[68:71], v[120:123], a[56:59]
ds_read_b64_tr_b16 v[170:171], v196 offset:9216
v_mfma_f32_16x16x32_bf16 a[60:63], v[68:71], v[124:127], a[60:63]
v_mfma_f32_16x16x32_bf16 a[64:67], v[72:75], v[96:99], a[64:67]
s_add_u32 m0, s92, 102400
ds_read_b128 v[136:139], v193 offset:4096
v_mfma_f32_16x16x32_bf16 a[68:71], v[72:75], v[100:103], a[68:71]
buffer_load_dwordx4 v20, s[12:15], s25 offen offset:0 lds
v_mfma_f32_16x16x32_bf16 a[72:75], v[72:75], v[104:107], a[72:75]
ds_read_b64_tr_b16 v[172:173], v197 offset:8192
v_mfma_f32_16x16x32_bf16 a[76:79], v[72:75], v[108:111], a[76:79]
v_mfma_f32_16x16x32_bf16 a[80:83], v[72:75], v[112:115], a[80:83]
ds_read_b64_tr_b16 v[174:175], v197 offset:9216
v_mfma_f32_16x16x32_bf16 a[84:87], v[72:75], v[116:119], a[84:87]
buffer_load_dwordx4 v21, s[12:15], s25 offen offset:1024 lds
v_mfma_f32_16x16x32_bf16 a[88:91], v[72:75], v[120:123], a[88:91]
ds_read_b128 v[140:143], v193 offset:6144
v_mfma_f32_16x16x32_bf16 a[92:95], v[72:75], v[124:127], a[92:95]
v_mfma_f32_16x16x32_bf16 a[96:99], v[76:79], v[96:99], a[96:99]
ds_read_b64_tr_b16 v[176:177], v198 offset:8192
v_mfma_f32_16x16x32_bf16 a[100:103], v[76:79], v[100:103], a[100:103]
buffer_load_dwordx4 v22, s[12:15], s25 offen offset:2048 lds
v_mfma_f32_16x16x32_bf16 a[104:107], v[76:79], v[104:107], a[104:107]
ds_read_b64_tr_b16 v[178:179], v198 offset:9216
v_mfma_f32_16x16x32_bf16 a[108:111], v[76:79], v[108:111], a[108:111]
v_mfma_f32_16x16x32_bf16 a[112:115], v[76:79], v[112:115], a[112:115]
ds_read_b128 v[144:147], v193 offset:8192
v_mfma_f32_16x16x32_bf16 a[116:119], v[76:79], v[116:119], a[116:119]
buffer_load_dwordx4 v23, s[12:15], s25 offen offset:3072 lds
v_mfma_f32_16x16x32_bf16 a[120:123], v[76:79], v[120:123], a[120:123]
ds_read_b64_tr_b16 v[180:181], v199 offset:8192
v_mfma_f32_16x16x32_bf16 a[124:127], v[76:79], v[124:127], a[124:127]
v_mfma_f32_16x16x32_bf16 a[128:131], v[80:83], v[96:99], a[128:131]
ds_read_b64_tr_b16 v[182:183], v199 offset:9216
v_mfma_f32_16x16x32_bf16 a[132:135], v[80:83], v[100:103], a[132:135]
v_mfma_f32_16x16x32_bf16 a[136:139], v[80:83], v[104:107], a[136:139]
ds_read_b128 v[148:151], v193 offset:10240
v_mfma_f32_16x16x32_bf16 a[140:143], v[80:83], v[108:111], a[140:143]
v_mfma_f32_16x16x32_bf16 a[144:147], v[80:83], v[112:115], a[144:147]
ds_read_b64_tr_b16 v[184:185], v200 offset:8192
v_mfma_f32_16x16x32_bf16 a[148:151], v[80:83], v[116:119], a[148:151]
v_mfma_f32_16x16x32_bf16 a[152:155], v[80:83], v[120:123], a[152:155]
ds_read_b64_tr_b16 v[186:187], v200 offset:9216
v_mfma_f32_16x16x32_bf16 a[156:159], v[80:83], v[124:127], a[156:159]
v_mfma_f32_16x16x32_bf16 a[160:163], v[84:87], v[96:99], a[160:163]
s_add_u32 s24, s24, 128
s_add_u32 s25, s25, 128
ds_read_b128 v[152:155], v193 offset:12288
v_mfma_f32_16x16x32_bf16 a[164:167], v[84:87], v[100:103], a[164:167]
v_mfma_f32_16x16x32_bf16 a[168:171], v[84:87], v[104:107], a[168:171]
ds_read_b64_tr_b16 v[188:189], v201 offset:8192
v_mfma_f32_16x16x32_bf16 a[172:175], v[84:87], v[108:111], a[172:175]
v_mfma_f32_16x16x32_bf16 a[176:179], v[84:87], v[112:115], a[176:179]
ds_read_b64_tr_b16 v[190:191], v201 offset:9216
v_mfma_f32_16x16x32_bf16 a[180:183], v[84:87], v[116:119], a[180:183]
v_mfma_f32_16x16x32_bf16 a[184:187], v[84:87], v[120:123], a[184:187]
ds_read_b128 v[156:159], v193 offset:14336
v_mfma_f32_16x16x32_bf16 a[188:191], v[84:87], v[124:127], a[188:191]
v_mfma_f32_16x16x32_bf16 a[192:195], v[88:91], v[96:99], a[192:195]
v_mfma_f32_16x16x32_bf16 a[196:199], v[88:91], v[100:103], a[196:199]
v_mfma_f32_16x16x32_bf16 a[200:203], v[88:91], v[104:107], a[200:203]
v_mfma_f32_16x16x32_bf16 a[204:207], v[88:91], v[108:111], a[204:207]
v_mfma_f32_16x16x32_bf16 a[208:211], v[88:91], v[112:115], a[208:211]
v_mfma_f32_16x16x32_bf16 a[212:215], v[88:91], v[116:119], a[212:215]
v_mfma_f32_16x16x32_bf16 a[216:219], v[88:91], v[120:123], a[216:219]
v_mfma_f32_16x16x32_bf16 a[220:223], v[88:91], v[124:127], a[220:223]
v_mfma_f32_16x16x32_bf16 a[224:227], v[92:95], v[96:99], a[224:227]
v_mfma_f32_16x16x32_bf16 a[228:231], v[92:95], v[100:103], a[228:231]
v_mfma_f32_16x16x32_bf16 a[232:235], v[92:95], v[104:107], a[232:235]
v_mfma_f32_16x16x32_bf16 a[236:239], v[92:95], v[108:111], a[236:239]
v_mfma_f32_16x16x32_bf16 a[240:243], v[92:95], v[112:115], a[240:243]
v_mfma_f32_16x16x32_bf16 a[244:247], v[92:95], v[116:119], a[244:247]
v_mfma_f32_16x16x32_bf16 a[248:251], v[92:95], v[120:123], a[248:251]
v_mfma_f32_16x16x32_bf16 a[252:255], v[92:95], v[124:127], a[252:255]
s_waitcnt vmcnt(15) lgkmcnt(0)
s_barrier
v_mfma_f32_16x16x32_bf16 a[0:3], v[128:131], v[160:163], a[0:3]
s_add_u32 m0, s91, 0
ds_read_b64_tr_b16 v[64:65], v26 offset:0
v_mfma_f32_16x16x32_bf16 a[4:7], v[128:131], v[164:167], a[4:7]
buffer_load_dwordx4 v16, s[16:19], s26 offen offset:0 lds
v_mfma_f32_16x16x32_bf16 a[8:11], v[128:131], v[168:171], a[8:11]
ds_read_b64_tr_b16 v[66:67], v26 offset:1024
v_mfma_f32_16x16x32_bf16 a[12:15], v[128:131], v[172:175], a[12:15]
v_mfma_f32_16x16x32_bf16 a[16:19], v[128:131], v[176:179], a[16:19]
ds_read_b64_tr_b16 v[68:69], v27 offset:0
v_mfma_f32_16x16x32_bf16 a[20:23], v[128:131], v[180:183], a[20:23]
buffer_load_dwordx4 v17, s[16:19], s26 offen offset:1024 lds
v_mfma_f32_16x16x32_bf16 a[24:27], v[128:131], v[184:187], a[24:27]
ds_read_b64_tr_b16 v[70:71], v27 offset:1024
v_mfma_f32_16x16x32_bf16 a[28:31], v[128:131], v[188:191], a[28:31]
v_mfma_f32_16x16x32_bf16 a[32:35], v[132:135], v[160:163], a[32:35]
ds_read_b64_tr_b16 v[72:73], v28 offset:0
v_mfma_f32_16x16x32_bf16 a[36:39], v[132:135], v[164:167], a[36:39]
buffer_load_dwordx4 v18, s[16:19], s26 offen offset:2048 lds
v_mfma_f32_16x16x32_bf16 a[40:43], v[132:135], v[168:171], a[40:43]
ds_read_b64_tr_b16 v[74:75], v28 offset:1024
v_mfma_f32_16x16x32_bf16 a[44:47], v[132:135], v[172:175], a[44:47]
v_mfma_f32_16x16x32_bf16 a[48:51], v[132:135], v[176:179], a[48:51]
ds_read_b64_tr_b16 v[76:77], v29 offset:0
v_mfma_f32_16x16x32_bf16 a[52:55], v[132:135], v[180:183], a[52:55]
buffer_load_dwordx4 v19, s[16:19], s26 offen offset:3072 lds
v_mfma_f32_16x16x32_bf16 a[56:59], v[132:135], v[184:187], a[56:59]
ds_read_b64_tr_b16 v[78:79], v29 offset:1024
v_mfma_f32_16x16x32_bf16 a[60:63], v[132:135], v[188:191], a[60:63]
s_add_u32 s26, s26, 524288
v_mfma_f32_16x16x32_bf16 a[64:67], v[136:139], v[160:163], a[64:67]
s_add_u32 m0, s92, 131072
ds_read_b64_tr_b16 v[80:81], v30 offset:0
v_mfma_f32_16x16x32_bf16 a[68:71], v[136:139], v[164:167], a[68:71]
buffer_load_dwordx4 v20, s[12:15], s24 offen offset:0 lds
v_mfma_f32_16x16x32_bf16 a[72:75], v[136:139], v[168:171], a[72:75]
ds_read_b64_tr_b16 v[82:83], v30 offset:1024
v_mfma_f32_16x16x32_bf16 a[76:79], v[136:139], v[172:175], a[76:79]
v_mfma_f32_16x16x32_bf16 a[80:83], v[136:139], v[176:179], a[80:83]
ds_read_b64_tr_b16 v[84:85], v31 offset:0
v_mfma_f32_16x16x32_bf16 a[84:87], v[136:139], v[180:183], a[84:87]
buffer_load_dwordx4 v21, s[12:15], s24 offen offset:1024 lds
v_mfma_f32_16x16x32_bf16 a[88:91], v[136:139], v[184:187], a[88:91]
ds_read_b64_tr_b16 v[86:87], v31 offset:1024
v_mfma_f32_16x16x32_bf16 a[92:95], v[136:139], v[188:191], a[92:95]
v_mfma_f32_16x16x32_bf16 a[96:99], v[140:143], v[160:163], a[96:99]
ds_read_b64_tr_b16 v[88:89], v32 offset:0
v_mfma_f32_16x16x32_bf16 a[100:103], v[140:143], v[164:167], a[100:103]
buffer_load_dwordx4 v22, s[12:15], s24 offen offset:2048 lds
v_mfma_f32_16x16x32_bf16 a[104:107], v[140:143], v[168:171], a[104:107]
ds_read_b64_tr_b16 v[90:91], v32 offset:1024
v_mfma_f32_16x16x32_bf16 a[108:111], v[140:143], v[172:175], a[108:111]
v_mfma_f32_16x16x32_bf16 a[112:115], v[140:143], v[176:179], a[112:115]
ds_read_b64_tr_b16 v[92:93], v33 offset:0
v_mfma_f32_16x16x32_bf16 a[116:119], v[140:143], v[180:183], a[116:119]
buffer_load_dwordx4 v23, s[12:15], s24 offen offset:3072 lds
v_mfma_f32_16x16x32_bf16 a[120:123], v[140:143], v[184:187], a[120:123]
ds_read_b64_tr_b16 v[94:95], v33 offset:1024
v_mfma_f32_16x16x32_bf16 a[124:127], v[140:143], v[188:191], a[124:127]
v_mfma_f32_16x16x32_bf16 a[128:131], v[144:147], v[160:163], a[128:131]
s_add_u32 m0, s92, 135168
v_mfma_f32_16x16x32_bf16 a[132:135], v[144:147], v[164:167], a[132:135]
buffer_load_dwordx4 v20, s[12:15], s25 offen offset:0 lds
v_mfma_f32_16x16x32_bf16 a[136:139], v[144:147], v[168:171], a[136:139]
ds_read_b128 v[48:51], v24 offset:0
v_mfma_f32_16x16x32_bf16 a[140:143], v[144:147], v[172:175], a[140:143]
v_mfma_f32_16x16x32_bf16 a[144:147], v[144:147], v[176:179], a[144:147]
ds_read_b128 v[52:55], v24 offset:2048
v_mfma_f32_16x16x32_bf16 a[148:151], v[144:147], v[180:183], a[148:151]
buffer_load_dwordx4 v21, s[12:15], s25 offen offset:1024 lds
v_mfma_f32_16x16x32_bf16 a[152:155], v[144:147], v[184:187], a[152:155]
ds_read_b128 v[56:59], v24 offset:4096
v_mfma_f32_16x16x32_bf16 a[156:159], v[144:147], v[188:191], a[156:159]
v_mfma_f32_16x16x32_bf16 a[160:163], v[148:151], v[160:163], a[160:163]
v_mfma_f32_16x16x32_bf16 a[164:167], v[148:151], v[164:167], a[164:167]
buffer_load_dwordx4 v22, s[12:15], s25 offen offset:2048 lds
v_mfma_f32_16x16x32_bf16 a[168:171], v[148:151], v[168:171], a[168:171]
v_mfma_f32_16x16x32_bf16 a[172:175], v[148:151], v[172:175], a[172:175]
v_mfma_f32_16x16x32_bf16 a[176:179], v[148:151], v[176:179], a[176:179]
v_mfma_f32_16x16x32_bf16 a[180:183], v[148:151], v[180:183], a[180:183]
buffer_load_dwordx4 v23, s[12:15], s25 offen offset:3072 lds
v_mfma_f32_16x16x32_bf16 a[184:187], v[148:151], v[184:187], a[184:187]
v_mfma_f32_16x16x32_bf16 a[188:191], v[148:151], v[188:191], a[188:191]
v_mfma_f32_16x16x32_bf16 a[192:195], v[152:155], v[160:163], a[192:195]
v_mfma_f32_16x16x32_bf16 a[196:199], v[152:155], v[164:167], a[196:199]
s_add_u32 s24, s24, 128
s_add_u32 s25, s25, 128
v_mfma_f32_16x16x32_bf16 a[200:203], v[152:155], v[168:171], a[200:203]
v_mfma_f32_16x16x32_bf16 a[204:207], v[152:155], v[172:175], a[204:207]
v_mfma_f32_16x16x32_bf16 a[208:211], v[152:155], v[176:179], a[208:211]
v_mfma_f32_16x16x32_bf16 a[212:215], v[152:155], v[180:183], a[212:215]
v_mfma_f32_16x16x32_bf16 a[216:219], v[152:155], v[184:187], a[216:219]
v_mfma_f32_16x16x32_bf16 a[220:223], v[152:155], v[188:191], a[220:223]
v_mfma_f32_16x16x32_bf16 a[224:227], v[156:159], v[160:163], a[224:227]
v_mfma_f32_16x16x32_bf16 a[228:231], v[156:159], v[164:167], a[228:231]
v_mfma_f32_16x16x32_bf16 a[232:235], v[156:159], v[168:171], a[232:235]
v_mfma_f32_16x16x32_bf16 a[236:239], v[156:159], v[172:175], a[236:239]
v_mfma_f32_16x16x32_bf16 a[240:243], v[156:159], v[176:179], a[240:243]
v_mfma_f32_16x16x32_bf16 a[244:247], v[156:159], v[180:183], a[244:247]
v_mfma_f32_16x16x32_bf16 a[248:251], v[156:159], v[184:187], a[248:251]
v_mfma_f32_16x16x32_bf16 a[252:255], v[156:159], v[188:191], a[252:255]
s_waitcnt lgkmcnt(0)
.Lp2_pad:
s_cbranch_execz .Ltch1
.Lp2:
v_mfma_f32_16x16x32_bf16 v[128:131], v[64:67], v[48:51], 0
ds_read_b128 v[60:63], v24 offset:6144
v_mfma_f32_16x16x32_bf16 v[132:135], v[68:71], v[48:51], 0
v_add_u32 v24, 65536, v24
v_accvgpr_read_b32 v36, a0
v_mfma_f32_16x16x32_bf16 v[136:139], v[72:75], v[48:51], 0
ds_read_b64_tr_b16 v[96:97], v26 offset:8192
ds_read_b64_tr_b16 v[98:99], v26 offset:9216
v_mfma_f32_16x16x32_bf16 v[140:143], v[76:79], v[48:51], 0
v_accvgpr_read_b32 v37, a1
v_mul_f32 v36, s47, v36
v_mfma_f32_16x16x32_bf16 v[144:147], v[80:83], v[48:51], 0
ds_read_b64_tr_b16 v[100:101], v27 offset:8192
ds_read_b64_tr_b16 v[102:103], v27 offset:9216
v_mfma_f32_16x16x32_bf16 v[148:151], v[84:87], v[48:51], 0
v_mul_f32 v37, s47, v37
v_cvt_pk_bf16_f32 v40, v36, v37
v_mfma_f32_16x16x32_bf16 v[152:155], v[88:91], v[48:51], 0
ds_read_b64_tr_b16 v[104:105], v28 offset:8192
ds_read_b64_tr_b16 v[106:107], v28 offset:9216
v_mfma_f32_16x16x32_bf16 v[156:159], v[92:95], v[48:51], 0
v_accvgpr_read_b32 v38, a2
v_accvgpr_read_b32 v39, a3
v_mfma_f32_16x16x32_bf16 v[160:163], v[64:67], v[52:55], 0
ds_read_b128 v[48:51], v25 offset:0
v_mul_f32 v38, s47, v38
v_mfma_f32_16x16x32_bf16 v[164:167], v[68:71], v[52:55], 0
v_mul_f32 v39, s47, v39
v_cvt_pk_bf16_f32 v41, v38, v39
v_mfma_f32_16x16x32_bf16 v[168:171], v[72:75], v[52:55], 0
s_add_u32 m0, s91, 16384
ds_read_b64_tr_b16 v[108:109], v29 offset:8192
ds_read_b64_tr_b16 v[110:111], v29 offset:9216
v_mfma_f32_16x16x32_bf16 v[172:175], v[76:79], v[52:55], 0
buffer_load_dwordx4 v16, s[16:19], s26 offen offset:0 lds
v_accvgpr_read_b32 v36, a4
v_mfma_f32_16x16x32_bf16 v[176:179], v[80:83], v[52:55], 0
ds_read_b64_tr_b16 v[112:113], v30 offset:8192
ds_read_b64_tr_b16 v[114:115], v30 offset:9216
v_mfma_f32_16x16x32_bf16 v[180:183], v[84:87], v[52:55], 0
v_accvgpr_read_b32 v37, a5
v_mul_f32 v36, s47, v36
v_mfma_f32_16x16x32_bf16 v[184:187], v[88:91], v[52:55], 0
ds_read_b64_tr_b16 v[116:117], v31 offset:8192
ds_read_b64_tr_b16 v[118:119], v31 offset:9216
v_mfma_f32_16x16x32_bf16 v[188:191], v[92:95], v[52:55], 0
buffer_load_dwordx4 v17, s[16:19], s26 offen offset:1024 lds
v_mul_f32 v37, s47, v37
v_mfma_f32_16x16x32_bf16 v[192:195], v[64:67], v[56:59], 0
ds_read_b128 v[52:55], v25 offset:2048
v_cvt_pk_bf16_f32 v42, v36, v37
v_mfma_f32_16x16x32_bf16 v[196:199], v[68:71], v[56:59], 0
v_accvgpr_read_b32 v38, a6
v_accvgpr_read_b32 v39, a7
v_mfma_f32_16x16x32_bf16 v[200:203], v[72:75], v[56:59], 0
ds_read_b64_tr_b16 v[120:121], v32 offset:8192
ds_read_b64_tr_b16 v[122:123], v32 offset:9216
.Lpad4:
s_cbranch_execz .Ltch1
v_mfma_f32_16x16x32_bf16 v[204:207], v[76:79], v[56:59], 0
buffer_load_dwordx4 v18, s[16:19], s26 offen offset:2048 lds
v_mul_f32 v38, s47, v38
v_mfma_f32_16x16x32_bf16 v[208:211], v[80:83], v[56:59], 0
ds_read_b64_tr_b16 v[124:125], v33 offset:8192
ds_read_b64_tr_b16 v[126:127], v33 offset:9216
v_mfma_f32_16x16x32_bf16 v[212:215], v[84:87], v[56:59], 0
v_mul_f32 v39, s47, v39
v_cvt_pk_bf16_f32 v43, v38, v39
v_mfma_f32_16x16x32_bf16 v[216:219], v[88:91], v[56:59], 0
s_waitcnt lgkmcnt(15)
v_permlane32_swap_b32 v40, v42
v_mfma_f32_16x16x32_bf16 v[220:223], v[92:95], v[56:59], 0
v_permlane32_swap_b32 v41, v43
v_mfma_f32_16x16x32_bf16 v[224:227], v[64:67], v[60:63], 0
s_xor_b32 s48, s95, 0
ds_read_b128 v[56:59], v25 offset:4096
v_mfma_f32_16x16x32_bf16 v[228:231], v[68:71], v[60:63], 0
buffer_load_dwordx4 v19, s[16:19], s26 offen offset:3072 lds
v_accvgpr_read_b32 v36, a128
v_mfma_f32_16x16x32_bf16 v[232:235], v[72:75], v[60:63], 0
buffer_store_dwordx4 v[40:43], v7, s[20:23], s48 offen sc1
v_accvgpr_read_b32 v37, a129
v_mfma_f32_16x16x32_bf16 v[236:239], v[76:79], v[60:63], 0
s_add_u32 s26, s26, 524288
v_mul_f32 v36, s47, v36
v_mul_f32 v37, s47, v37
v_mfma_f32_16x16x32_bf16 v[240:243], v[80:83], v[60:63], 0
v_cvt_pk_bf16_f32 v44, v36, v37
v_accvgpr_read_b32 v38, a130
v_mfma_f32_16x16x32_bf16 v[244:247], v[84:87], v[60:63], 0
v_accvgpr_read_b32 v39, a131
v_mul_f32 v38, s47, v38
v_mfma_f32_16x16x32_bf16 v[248:251], v[88:91], v[60:63], 0
v_mul_f32 v39, s47, v39
v_cvt_pk_bf16_f32 v45, v38, v39
v_mfma_f32_16x16x32_bf16 v[252:255], v[92:95], v[60:63], 0
s_waitcnt vmcnt(17) lgkmcnt(0)
s_barrier
v_accvgpr_read_b32 v36, a132
v_mfma_f32_16x16x32_bf16 v[128:131], v[96:99], v[48:51], v[128:131]
ds_read_b128 v[60:63], v25 offset:6144
v_accvgpr_read_b32 v37, a133
v_mfma_f32_16x16x32_bf16 v[132:135], v[100:103], v[48:51], v[132:135]
v_add_u32 v25, 65536, v25
v_mul_f32 v36, s47, v36
v_mfma_f32_16x16x32_bf16 v[136:139], v[104:107], v[48:51], v[136:139]
s_add_u32 m0, s92, 32768
ds_read_b64_tr_b16 v[64:65], v26 offset:16384
ds_read_b64_tr_b16 v[66:67], v26 offset:17408
v_mfma_f32_16x16x32_bf16 v[140:143], v[108:111], v[48:51], v[140:143]
buffer_load_dwordx4 v20, s[12:15], s24 offen offset:0 lds
v_mul_f32 v37, s47, v37
v_mfma_f32_16x16x32_bf16 v[144:147], v[112:115], v[48:51], v[144:147]
ds_read_b64_tr_b16 v[68:69], v27 offset:16384
ds_read_b64_tr_b16 v[70:71], v27 offset:17408
v_mfma_f32_16x16x32_bf16 v[148:151], v[116:119], v[48:51], v[148:151]
v_cvt_pk_bf16_f32 v46, v36, v37
v_accvgpr_read_b32 v38, a134
v_mfma_f32_16x16x32_bf16 v[152:155], v[120:123], v[48:51], v[152:155]
ds_read_b64_tr_b16 v[72:73], v28 offset:16384
ds_read_b64_tr_b16 v[74:75], v28 offset:17408
v_mfma_f32_16x16x32_bf16 v[156:159], v[124:127], v[48:51], v[156:159]
buffer_load_dwordx4 v21, s[12:15], s24 offen offset:1024 lds
v_accvgpr_read_b32 v39, a135
v_mfma_f32_16x16x32_bf16 v[160:163], v[96:99], v[52:55], v[160:163]
ds_read_b128 v[48:51], v24 offset:0
v_mul_f32 v38, s47, v38
v_mfma_f32_16x16x32_bf16 v[164:167], v[100:103], v[52:55], v[164:167]
v_mul_f32 v39, s47, v39
v_cvt_pk_bf16_f32 v47, v38, v39
v_mfma_f32_16x16x32_bf16 v[168:171], v[104:107], v[52:55], v[168:171]
ds_read_b64_tr_b16 v[76:77], v29 offset:16384
ds_read_b64_tr_b16 v[78:79], v29 offset:17408
v_mfma_f32_16x16x32_bf16 v[172:175], v[108:111], v[52:55], v[172:175]
buffer_load_dwordx4 v22, s[12:15], s24 offen offset:2048 lds
v_mfma_f32_16x16x32_bf16 v[176:179], v[112:115], v[52:55], v[176:179]
ds_read_b64_tr_b16 v[80:81], v30 offset:16384
ds_read_b64_tr_b16 v[82:83], v30 offset:17408
v_mfma_f32_16x16x32_bf16 v[180:183], v[116:119], v[52:55], v[180:183]
v_permlane32_swap_b32 v44, v46
v_mfma_f32_16x16x32_bf16 v[184:187], v[120:123], v[52:55], v[184:187]
ds_read_b64_tr_b16 v[84:85], v31 offset:16384
ds_read_b64_tr_b16 v[86:87], v31 offset:17408
v_mfma_f32_16x16x32_bf16 v[188:191], v[124:127], v[52:55], v[188:191]
buffer_load_dwordx4 v23, s[12:15], s24 offen offset:3072 lds
v_mfma_f32_16x16x32_bf16 v[192:195], v[96:99], v[56:59], v[192:195]
ds_read_b128 v[52:55], v24 offset:2048
v_mfma_f32_16x16x32_bf16 v[196:199], v[100:103], v[56:59], v[196:199]
v_permlane32_swap_b32 v45, v47
v_mfma_f32_16x16x32_bf16 v[200:203], v[104:107], v[56:59], v[200:203]
s_add_u32 m0, s92, 36864
ds_read_b64_tr_b16 v[88:89], v32 offset:16384
ds_read_b64_tr_b16 v[90:91], v32 offset:17408
v_mfma_f32_16x16x32_bf16 v[204:207], v[108:111], v[56:59], v[204:207]
buffer_load_dwordx4 v20, s[12:15], s25 offen offset:0 lds
v_accvgpr_write_b32 a128, v44
v_mfma_f32_16x16x32_bf16 v[208:211], v[112:115], v[56:59], v[208:211]
ds_read_b64_tr_b16 v[92:93], v33 offset:16384
ds_read_b64_tr_b16 v[94:95], v33 offset:17408
v_mfma_f32_16x16x32_bf16 v[212:215], v[116:119], v[56:59], v[212:215]
v_accvgpr_write_b32 a129, v45
v_accvgpr_write_b32 a130, v46
v_mfma_f32_16x16x32_bf16 v[216:219], v[120:123], v[56:59], v[216:219]
s_waitcnt lgkmcnt(15)
v_accvgpr_write_b32 a131, v47
v_accvgpr_read_b32 v36, a8
v_mfma_f32_16x16x32_bf16 v[220:223], v[124:127], v[56:59], v[220:223]
v_accvgpr_read_b32 v37, a9
v_mul_f32 v36, s47, v36
v_mfma_f32_16x16x32_bf16 v[224:227], v[96:99], v[60:63], v[224:227]
ds_read_b128 v[56:59], v24 offset:4096
v_mul_f32 v37, s47, v37
v_mfma_f32_16x16x32_bf16 v[228:231], v[100:103], v[60:63], v[228:231]
buffer_load_dwordx4 v21, s[12:15], s25 offen offset:1024 lds
v_cvt_pk_bf16_f32 v40, v36, v37
v_mfma_f32_16x16x32_bf16 v[232:235], v[104:107], v[60:63], v[232:235]
v_accvgpr_read_b32 v38, a10
v_accvgpr_read_b32 v39, a11
v_mfma_f32_16x16x32_bf16 v[236:239], v[108:111], v[60:63], v[236:239]
v_mul_f32 v38, s47, v38
v_mul_f32 v39, s47, v39
v_mfma_f32_16x16x32_bf16 v[240:243], v[112:115], v[60:63], v[240:243]
v_cvt_pk_bf16_f32 v41, v38, v39
v_accvgpr_read_b32 v36, a12
v_mfma_f32_16x16x32_bf16 v[244:247], v[116:119], v[60:63], v[244:247]
v_accvgpr_read_b32 v37, a13
v_mul_f32 v36, s47, v36
v_mfma_f32_16x16x32_bf16 v[248:251], v[120:123], v[60:63], v[248:251]
v_mul_f32 v37, s47, v37
v_cvt_pk_bf16_f32 v42, v36, v37
v_mfma_f32_16x16x32_bf16 v[252:255], v[124:127], v[60:63], v[252:255]
s_waitcnt lgkmcnt(0)
v_accvgpr_read_b32 v38, a14
v_accvgpr_read_b32 v39, a15
v_mfma_f32_16x16x32_bf16 v[128:131], v[64:67], v[48:51], v[128:131]
ds_read_b128 v[60:63], v24 offset:6144
v_mul_f32 v38, s47, v38
v_mfma_f32_16x16x32_bf16 v[132:135], v[68:71], v[48:51], v[132:135]
v_add_u32 v24, 32768, v24
v_mul_f32 v39, s47, v39
v_mfma_f32_16x16x32_bf16 v[136:139], v[72:75], v[48:51], v[136:139]
ds_read_b64_tr_b16 v[96:97], v26 offset:24576
ds_read_b64_tr_b16 v[98:99], v26 offset:25600
v_mfma_f32_16x16x32_bf16 v[140:143], v[76:79], v[48:51], v[140:143]
buffer_load_dwordx4 v22, s[12:15], s25 offen offset:2048 lds
v_cvt_pk_bf16_f32 v43, v38, v39
v_mfma_f32_16x16x32_bf16 v[144:147], v[80:83], v[48:51], v[144:147]
ds_read_b64_tr_b16 v[100:101], v27 offset:24576
ds_read_b64_tr_b16 v[102:103], v27 offset:25600
v_mfma_f32_16x16x32_bf16 v[148:151], v[84:87], v[48:51], v[148:151]
v_permlane32_swap_b32 v40, v42
v_mfma_f32_16x16x32_bf16 v[152:155], v[88:91], v[48:51], v[152:155]
ds_read_b64_tr_b16 v[104:105], v28 offset:24576
ds_read_b64_tr_b16 v[106:107], v28 offset:25600
v_mfma_f32_16x16x32_bf16 v[156:159], v[92:95], v[48:51], v[156:159]
buffer_load_dwordx4 v23, s[12:15], s25 offen offset:3072 lds
v_accvgpr_read_b32 v36, a136
v_mfma_f32_16x16x32_bf16 v[160:163], v[64:67], v[52:55], v[160:163]
ds_read_b128 v[48:51], v25 offset:0
v_accvgpr_read_b32 v37, a137
v_mfma_f32_16x16x32_bf16 v[164:167], v[68:71], v[52:55], v[164:167]
s_add_u32 s24, s24, 128
s_add_u32 s25, s25, 128
v_mul_f32 v36, s47, v36
v_mfma_f32_16x16x32_bf16 v[168:171], v[72:75], v[52:55], v[168:171]
s_add_u32 m0, s91, 65536
ds_read_b64_tr_b16 v[108:109], v29 offset:24576
ds_read_b64_tr_b16 v[110:111], v29 offset:25600
v_mfma_f32_16x16x32_bf16 v[172:175], v[76:79], v[52:55], v[172:175]
buffer_load_dwordx4 v16, s[16:19], s26 offen offset:0 lds
v_mul_f32 v37, s47, v37
v_mfma_f32_16x16x32_bf16 v[176:179], v[80:83], v[52:55], v[176:179]
ds_read_b64_tr_b16 v[112:113], v30 offset:24576
ds_read_b64_tr_b16 v[114:115], v30 offset:25600
v_mfma_f32_16x16x32_bf16 v[180:183], v[84:87], v[52:55], v[180:183]
v_permlane32_swap_b32 v41, v43
v_mfma_f32_16x16x32_bf16 v[184:187], v[88:91], v[52:55], v[184:187]
ds_read_b64_tr_b16 v[116:117], v31 offset:24576
ds_read_b64_tr_b16 v[118:119], v31 offset:25600
v_mfma_f32_16x16x32_bf16 v[188:191], v[92:95], v[52:55], v[188:191]
s_xor_b32 s48, s95, 1024
buffer_load_dwordx4 v17, s[16:19], s26 offen offset:1024 lds
v_mfma_f32_16x16x32_bf16 v[192:195], v[64:67], v[56:59], v[192:195]
ds_read_b128 v[52:55], v25 offset:2048
v_cvt_pk_bf16_f32 v44, v36, v37
v_mfma_f32_16x16x32_bf16 v[196:199], v[68:71], v[56:59], v[196:199]
buffer_store_dwordx4 v[40:43], v7, s[20:23], s48 offen sc1
v_accvgpr_read_b32 v38, a138
v_mfma_f32_16x16x32_bf16 v[200:203], v[72:75], v[56:59], v[200:203]
ds_read_b64_tr_b16 v[120:121], v32 offset:24576
ds_read_b64_tr_b16 v[122:123], v32 offset:25600
v_mfma_f32_16x16x32_bf16 v[204:207], v[76:79], v[56:59], v[204:207]
buffer_load_dwordx4 v18, s[16:19], s26 offen offset:2048 lds
v_accvgpr_read_b32 v39, a139
v_mfma_f32_16x16x32_bf16 v[208:211], v[80:83], v[56:59], v[208:211]
ds_read_b64_tr_b16 v[124:125], v33 offset:24576
ds_read_b64_tr_b16 v[126:127], v33 offset:25600
v_mfma_f32_16x16x32_bf16 v[212:215], v[84:87], v[56:59], v[212:215]
v_mul_f32 v38, s47, v38
v_mul_f32 v39, s47, v39
v_mfma_f32_16x16x32_bf16 v[216:219], v[88:91], v[56:59], v[216:219]
s_waitcnt lgkmcnt(15)
v_cvt_pk_bf16_f32 v45, v38, v39
v_accvgpr_read_b32 v36, a140
v_mfma_f32_16x16x32_bf16 v[220:223], v[92:95], v[56:59], v[220:223]
v_xor_b32 v26, 0x10000, v26
v_accvgpr_read_b32 v37, a141
v_mfma_f32_16x16x32_bf16 v[224:227], v[64:67], v[60:63], v[224:227]
ds_read_b128 v[56:59], v25 offset:4096
v_xor_b32 v27, 0x10000, v27
v_mfma_f32_16x16x32_bf16 v[228:231], v[68:71], v[60:63], v[228:231]
buffer_load_dwordx4 v19, s[16:19], s26 offen offset:3072 lds
v_xor_b32 v28, 0x10000, v28
v_mfma_f32_16x16x32_bf16 v[232:235], v[72:75], v[60:63], v[232:235]
v_xor_b32 v29, 0x10000, v29
v_mul_f32 v36, s47, v36
v_mfma_f32_16x16x32_bf16 v[236:239], v[76:79], v[60:63], v[236:239]
s_add_u32 s26, s26, 524288
v_xor_b32 v30, 0x10000, v30
v_mul_f32 v37, s47, v37
v_mfma_f32_16x16x32_bf16 v[240:243], v[80:83], v[60:63], v[240:243]
v_xor_b32 v31, 0x10000, v31
v_cvt_pk_bf16_f32 v46, v36, v37
v_mfma_f32_16x16x32_bf16 v[244:247], v[84:87], v[60:63], v[244:247]
v_xor_b32 v32, 0x10000, v32
v_accvgpr_read_b32 v38, a142
v_mfma_f32_16x16x32_bf16 v[248:251], v[88:91], v[60:63], v[248:251]
v_xor_b32 v33, 0x10000, v33
v_accvgpr_read_b32 v39, a143
v_mfma_f32_16x16x32_bf16 v[252:255], v[92:95], v[60:63], v[252:255]
s_waitcnt vmcnt(18) lgkmcnt(0)
s_barrier
v_mul_f32 v38, s47, v38
v_mfma_f32_16x16x32_bf16 v[128:131], v[96:99], v[48:51], v[128:131]
ds_read_b128 v[60:63], v25 offset:6144
v_mul_f32 v39, s47, v39
v_mfma_f32_16x16x32_bf16 v[132:135], v[100:103], v[48:51], v[132:135]
v_add_u32 v25, 32768, v25
v_cvt_pk_bf16_f32 v47, v38, v39
v_mfma_f32_16x16x32_bf16 v[136:139], v[104:107], v[48:51], v[136:139]
s_add_u32 m0, s92, 98304
ds_read_b64_tr_b16 v[64:65], v26 offset:0
ds_read_b64_tr_b16 v[66:67], v26 offset:1024
v_mfma_f32_16x16x32_bf16 v[140:143], v[108:111], v[48:51], v[140:143]
buffer_load_dwordx4 v20, s[12:15], s24 offen offset:0 lds
v_mfma_f32_16x16x32_bf16 v[144:147], v[112:115], v[48:51], v[144:147]
ds_read_b64_tr_b16 v[68:69], v27 offset:0
ds_read_b64_tr_b16 v[70:71], v27 offset:1024
v_mfma_f32_16x16x32_bf16 v[148:151], v[116:119], v[48:51], v[148:151]
v_permlane32_swap_b32 v44, v46
v_mfma_f32_16x16x32_bf16 v[152:155], v[120:123], v[48:51], v[152:155]
ds_read_b64_tr_b16 v[72:73], v28 offset:0
ds_read_b64_tr_b16 v[74:75], v28 offset:1024
v_mfma_f32_16x16x32_bf16 v[156:159], v[124:127], v[48:51], v[156:159]
buffer_load_dwordx4 v21, s[12:15], s24 offen offset:1024 lds
v_mfma_f32_16x16x32_bf16 v[160:163], v[96:99], v[52:55], v[160:163]
ds_read_b128 v[48:51], v24 offset:0
v_mfma_f32_16x16x32_bf16 v[164:167], v[100:103], v[52:55], v[164:167]
v_permlane32_swap_b32 v45, v47
v_mfma_f32_16x16x32_bf16 v[168:171], v[104:107], v[52:55], v[168:171]
ds_read_b64_tr_b16 v[76:77], v29 offset:0
ds_read_b64_tr_b16 v[78:79], v29 offset:1024
v_mfma_f32_16x16x32_bf16 v[172:175], v[108:111], v[52:55], v[172:175]
buffer_load_dwordx4 v22, s[12:15], s24 offen offset:2048 lds
v_accvgpr_write_b32 a132, v44
v_mfma_f32_16x16x32_bf16 v[176:179], v[112:115], v[52:55], v[176:179]
ds_read_b64_tr_b16 v[80:81], v30 offset:0
ds_read_b64_tr_b16 v[82:83], v30 offset:1024
v_mfma_f32_16x16x32_bf16 v[180:183], v[116:119], v[52:55], v[180:183]
v_accvgpr_write_b32 a133, v45
v_accvgpr_write_b32 a134, v46
v_mfma_f32_16x16x32_bf16 v[184:187], v[120:123], v[52:55], v[184:187]
ds_read_b64_tr_b16 v[84:85], v31 offset:0
ds_read_b64_tr_b16 v[86:87], v31 offset:1024
v_mfma_f32_16x16x32_bf16 v[188:191], v[124:127], v[52:55], v[188:191]
buffer_load_dwordx4 v23, s[12:15], s24 offen offset:3072 lds
v_accvgpr_write_b32 a135, v47
v_mfma_f32_16x16x32_bf16 v[192:195], v[96:99], v[56:59], v[192:195]
ds_read_b128 v[52:55], v24 offset:2048
v_accvgpr_read_b32 v36, a16
v_mfma_f32_16x16x32_bf16 v[196:199], v[100:103], v[56:59], v[196:199]
v_accvgpr_read_b32 v37, a17
v_mul_f32 v36, s47, v36
v_mfma_f32_16x16x32_bf16 v[200:203], v[104:107], v[56:59], v[200:203]
s_add_u32 m0, s92, 102400
ds_read_b64_tr_b16 v[88:89], v32 offset:0
ds_read_b64_tr_b16 v[90:91], v32 offset:1024
v_mfma_f32_16x16x32_bf16 v[204:207], v[108:111], v[56:59], v[204:207]
buffer_load_dwordx4 v20, s[12:15], s25 offen offset:0 lds
v_mul_f32 v37, s47, v37
v_mfma_f32_16x16x32_bf16 v[208:211], v[112:115], v[56:59], v[208:211]
ds_read_b64_tr_b16 v[92:93], v33 offset:0
ds_read_b64_tr_b16 v[94:95], v33 offset:1024
v_mfma_f32_16x16x32_bf16 v[212:215], v[116:119], v[56:59], v[212:215]
v_cvt_pk_bf16_f32 v40, v36, v37
v_accvgpr_read_b32 v38, a18
v_mfma_f32_16x16x32_bf16 v[216:219], v[120:123], v[56:59], v[216:219]
s_waitcnt lgkmcnt(15)
v_accvgpr_read_b32 v39, a19
v_mul_f32 v38, s47, v38
v_mfma_f32_16x16x32_bf16 v[220:223], v[124:127], v[56:59], v[220:223]
v_mul_f32 v39, s47, v39
v_cvt_pk_bf16_f32 v41, v38, v39
v_mfma_f32_16x16x32_bf16 v[224:227], v[96:99], v[60:63], v[224:227]
ds_read_b128 v[56:59], v24 offset:4096
v_accvgpr_read_b32 v36, a20
v_mfma_f32_16x16x32_bf16 v[228:231], v[100:103], v[60:63], v[228:231]
buffer_load_dwordx4 v21, s[12:15], s25 offen offset:1024 lds
v_accvgpr_read_b32 v37, a21
v_mfma_f32_16x16x32_bf16 v[232:235], v[104:107], v[60:63], v[232:235]
v_mul_f32 v36, s47, v36
v_mul_f32 v37, s47, v37
v_mfma_f32_16x16x32_bf16 v[236:239], v[108:111], v[60:63], v[236:239]
v_cvt_pk_bf16_f32 v42, v36, v37
v_accvgpr_read_b32 v38, a22
v_mfma_f32_16x16x32_bf16 v[240:243], v[112:115], v[60:63], v[240:243]
v_accvgpr_read_b32 v39, a23
v_mul_f32 v38, s47, v38
v_mfma_f32_16x16x32_bf16 v[244:247], v[116:119], v[60:63], v[244:247]
v_mul_f32 v39, s47, v39
v_cvt_pk_bf16_f32 v43, v38, v39
v_mfma_f32_16x16x32_bf16 v[248:251], v[120:123], v[60:63], v[248:251]
v_permlane32_swap_b32 v40, v42
v_mfma_f32_16x16x32_bf16 v[252:255], v[124:127], v[60:63], v[252:255]
s_waitcnt lgkmcnt(0)
v_permlane32_swap_b32 v41, v43
v_mfma_f32_16x16x32_bf16 v[128:131], v[64:67], v[48:51], v[128:131]
s_xor_b32 s48, s95, 2048
ds_read_b128 v[60:63], v24 offset:6144
v_mfma_f32_16x16x32_bf16 v[132:135], v[68:71], v[48:51], v[132:135]
buffer_store_dwordx4 v[40:43], v7, s[20:23], s48 offen sc1
v_add_u32 v24, 4294868992, v24
v_mfma_f32_16x16x32_bf16 v[136:139], v[72:75], v[48:51], v[136:139]
ds_read_b64_tr_b16 v[96:97], v26 offset:8192
ds_read_b64_tr_b16 v[98:99], v26 offset:9216
v_mfma_f32_16x16x32_bf16 v[140:143], v[76:79], v[48:51], v[140:143]
buffer_load_dwordx4 v22, s[12:15], s25 offen offset:2048 lds
v_accvgpr_read_b32 v36, a144
v_mfma_f32_16x16x32_bf16 v[144:147], v[80:83], v[48:51], v[144:147]
ds_read_b64_tr_b16 v[100:101], v27 offset:8192
ds_read_b64_tr_b16 v[102:103], v27 offset:9216
v_mfma_f32_16x16x32_bf16 v[148:151], v[84:87], v[48:51], v[148:151]
v_accvgpr_read_b32 v37, a145
v_mul_f32 v36, s47, v36
v_mfma_f32_16x16x32_bf16 v[152:155], v[88:91], v[48:51], v[152:155]
ds_read_b64_tr_b16 v[104:105], v28 offset:8192
ds_read_b64_tr_b16 v[106:107], v28 offset:9216
v_mfma_f32_16x16x32_bf16 v[156:159], v[92:95], v[48:51], v[156:159]
buffer_load_dwordx4 v23, s[12:15], s25 offen offset:3072 lds
v_mul_f32 v37, s47, v37
v_mfma_f32_16x16x32_bf16 v[160:163], v[64:67], v[52:55], v[160:163]
ds_read_b128 v[48:51], v25 offset:0
v_cvt_pk_bf16_f32 v44, v36, v37
v_mfma_f32_16x16x32_bf16 v[164:167], v[68:71], v[52:55], v[164:167]
s_add_u32 s24, s24, 128
s_add_u32 s25, s25, 128
v_accvgpr_read_b32 v38, a146
v_mfma_f32_16x16x32_bf16 v[168:171], v[72:75], v[52:55], v[168:171]
s_add_u32 m0, s91, 81920
ds_read_b64_tr_b16 v[108:109], v29 offset:8192
ds_read_b64_tr_b16 v[110:111], v29 offset:9216
v_mfma_f32_16x16x32_bf16 v[172:175], v[76:79], v[52:55], v[172:175]
buffer_load_dwordx4 v16, s[16:19], s26 offen offset:0 lds
v_accvgpr_read_b32 v39, a147
v_mfma_f32_16x16x32_bf16 v[176:179], v[80:83], v[52:55], v[176:179]
ds_read_b64_tr_b16 v[112:113], v30 offset:8192
ds_read_b64_tr_b16 v[114:115], v30 offset:9216
v_mfma_f32_16x16x32_bf16 v[180:183], v[84:87], v[52:55], v[180:183]
v_mul_f32 v38, s47, v38
v_mul_f32 v39, s47, v39
v_mfma_f32_16x16x32_bf16 v[184:187], v[88:91], v[52:55], v[184:187]
ds_read_b64_tr_b16 v[116:117], v31 offset:8192
ds_read_b64_tr_b16 v[118:119], v31 offset:9216
v_mfma_f32_16x16x32_bf16 v[188:191], v[92:95], v[52:55], v[188:191]
buffer_load_dwordx4 v17, s[16:19], s26 offen offset:1024 lds
v_cvt_pk_bf16_f32 v45, v38, v39
v_mfma_f32_16x16x32_bf16 v[192:195], v[64:67], v[56:59], v[192:195]
ds_read_b128 v[52:55], v25 offset:2048
v_accvgpr_read_b32 v36, a148
v_mfma_f32_16x16x32_bf16 v[196:199], v[68:71], v[56:59], v[196:199]
v_accvgpr_read_b32 v37, a149
v_mul_f32 v36, s47, v36
v_mfma_f32_16x16x32_bf16 v[200:203], v[72:75], v[56:59], v[200:203]
ds_read_b64_tr_b16 v[120:121], v32 offset:8192
ds_read_b64_tr_b16 v[122:123], v32 offset:9216
v_mfma_f32_16x16x32_bf16 v[204:207], v[76:79], v[56:59], v[204:207]
buffer_load_dwordx4 v18, s[16:19], s26 offen offset:2048 lds
v_mul_f32 v37, s47, v37
v_mfma_f32_16x16x32_bf16 v[208:211], v[80:83], v[56:59], v[208:211]
ds_read_b64_tr_b16 v[124:125], v33 offset:8192
ds_read_b64_tr_b16 v[126:127], v33 offset:9216
v_mfma_f32_16x16x32_bf16 v[212:215], v[84:87], v[56:59], v[212:215]
v_cvt_pk_bf16_f32 v46, v36, v37
v_accvgpr_read_b32 v38, a150
v_mfma_f32_16x16x32_bf16 v[216:219], v[88:91], v[56:59], v[216:219]
s_waitcnt lgkmcnt(15)
v_accvgpr_read_b32 v39, a151
v_mul_f32 v38, s47, v38
v_mfma_f32_16x16x32_bf16 v[220:223], v[92:95], v[56:59], v[220:223]
v_mul_f32 v39, s47, v39
v_cvt_pk_bf16_f32 v47, v38, v39
v_mfma_f32_16x16x32_bf16 v[224:227], v[64:67], v[60:63], v[224:227]
ds_read_b128 v[56:59], v25 offset:4096
v_mfma_f32_16x16x32_bf16 v[228:231], v[68:71], v[60:63], v[228:231]
buffer_load_dwordx4 v19, s[16:19], s26 offen offset:3072 lds
v_mfma_f32_16x16x32_bf16 v[232:235], v[72:75], v[60:63], v[232:235]
v_permlane32_swap_b32 v44, v46
v_mfma_f32_16x16x32_bf16 v[236:239], v[76:79], v[60:63], v[236:239]
s_add_u32 s26, s26, 524288
v_permlane32_swap_b32 v45, v47
v_mfma_f32_16x16x32_bf16 v[240:243], v[80:83], v[60:63], v[240:243]
v_accvgpr_write_b32 a136, v44
v_accvgpr_write_b32 a137, v45
v_mfma_f32_16x16x32_bf16 v[244:247], v[84:87], v[60:63], v[244:247]
v_accvgpr_write_b32 a138, v46
v_accvgpr_write_b32 a139, v47
v_mfma_f32_16x16x32_bf16 v[248:251], v[88:91], v[60:63], v[248:251]
v_accvgpr_read_b32 v36, a24
v_accvgpr_read_b32 v37, a25
v_mfma_f32_16x16x32_bf16 v[252:255], v[92:95], v[60:63], v[252:255]
s_waitcnt vmcnt(18) lgkmcnt(0)
s_barrier
v_mul_f32 v36, s47, v36
v_mfma_f32_16x16x32_bf16 v[128:131], v[96:99], v[48:51], v[128:131]
ds_read_b128 v[60:63], v25 offset:6144
v_mul_f32 v37, s47, v37
v_mfma_f32_16x16x32_bf16 v[132:135], v[100:103], v[48:51], v[132:135]
v_add_u32 v25, 4294868992, v25
v_cvt_pk_bf16_f32 v40, v36, v37
v_mfma_f32_16x16x32_bf16 v[136:139], v[104:107], v[48:51], v[136:139]
s_add_u32 m0, s92, 131072
ds_read_b64_tr_b16 v[64:65], v26 offset:16384
ds_read_b64_tr_b16 v[66:67], v26 offset:17408
v_mfma_f32_16x16x32_bf16 v[140:143], v[108:111], v[48:51], v[140:143]
buffer_load_dwordx4 v20, s[12:15], s24 offen offset:0 lds
v_accvgpr_read_b32 v38, a26
v_mfma_f32_16x16x32_bf16 v[144:147], v[112:115], v[48:51], v[144:147]
ds_read_b64_tr_b16 v[68:69], v27 offset:16384
ds_read_b64_tr_b16 v[70:71], v27 offset:17408
v_mfma_f32_16x16x32_bf16 v[148:151], v[116:119], v[48:51], v[148:151]
v_accvgpr_read_b32 v39, a27
v_mul_f32 v38, s47, v38
v_mfma_f32_16x16x32_bf16 v[152:155], v[120:123], v[48:51], v[152:155]
ds_read_b64_tr_b16 v[72:73], v28 offset:16384
ds_read_b64_tr_b16 v[74:75], v28 offset:17408
v_mfma_f32_16x16x32_bf16 v[156:159], v[124:127], v[48:51], v[156:159]
buffer_load_dwordx4 v21, s[12:15], s24 offen offset:1024 lds
v_mul_f32 v39, s47, v39
v_mfma_f32_16x16x32_bf16 v[160:163], v[96:99], v[52:55], v[160:163]
ds_read_b128 v[48:51], v24 offset:0
v_cvt_pk_bf16_f32 v41, v38, v39
v_mfma_f32_16x16x32_bf16 v[164:167], v[100:103], v[52:55], v[164:167]
v_accvgpr_read_b32 v36, a28
v_accvgpr_read_b32 v37, a29
v_mfma_f32_16x16x32_bf16 v[168:171], v[104:107], v[52:55], v[168:171]
ds_read_b64_tr_b16 v[76:77], v29 offset:16384
ds_read_b64_tr_b16 v[78:79], v29 offset:17408
v_mfma_f32_16x16x32_bf16 v[172:175], v[108:111], v[52:55], v[172:175]
buffer_load_dwordx4 v22, s[12:15], s24 offen offset:2048 lds
v_mul_f32 v36, s47, v36
v_mfma_f32_16x16x32_bf16 v[176:179], v[112:115], v[52:55], v[176:179]
ds_read_b64_tr_b16 v[80:81], v30 offset:16384
ds_read_b64_tr_b16 v[82:83], v30 offset:17408
v_mfma_f32_16x16x32_bf16 v[180:183], v[116:119], v[52:55], v[180:183]
v_mul_f32 v37, s47, v37
v_cvt_pk_bf16_f32 v42, v36, v37
v_mfma_f32_16x16x32_bf16 v[184:187], v[120:123], v[52:55], v[184:187]
ds_read_b64_tr_b16 v[84:85], v31 offset:16384
ds_read_b64_tr_b16 v[86:87], v31 offset:17408
v_mfma_f32_16x16x32_bf16 v[188:191], v[124:127], v[52:55], v[188:191]
buffer_load_dwordx4 v23, s[12:15], s24 offen offset:3072 lds
v_accvgpr_read_b32 v38, a30
v_mfma_f32_16x16x32_bf16 v[192:195], v[96:99], v[56:59], v[192:195]
ds_read_b128 v[52:55], v24 offset:2048
v_accvgpr_read_b32 v39, a31
v_mfma_f32_16x16x32_bf16 v[196:199], v[100:103], v[56:59], v[196:199]
v_mul_f32 v38, s47, v38
v_mul_f32 v39, s47, v39
v_mfma_f32_16x16x32_bf16 v[200:203], v[104:107], v[56:59], v[200:203]
s_add_u32 m0, s92, 135168
ds_read_b64_tr_b16 v[88:89], v32 offset:16384
ds_read_b64_tr_b16 v[90:91], v32 offset:17408
v_mfma_f32_16x16x32_bf16 v[204:207], v[108:111], v[56:59], v[204:207]
buffer_load_dwordx4 v20, s[12:15], s25 offen offset:0 lds
v_cvt_pk_bf16_f32 v43, v38, v39
v_mfma_f32_16x16x32_bf16 v[208:211], v[112:115], v[56:59], v[208:211]
ds_read_b64_tr_b16 v[92:93], v33 offset:16384
ds_read_b64_tr_b16 v[94:95], v33 offset:17408
v_mfma_f32_16x16x32_bf16 v[212:215], v[116:119], v[56:59], v[212:215]
v_permlane32_swap_b32 v40, v42
v_mfma_f32_16x16x32_bf16 v[216:219], v[120:123], v[56:59], v[216:219]
s_waitcnt lgkmcnt(15)
v_permlane32_swap_b32 v41, v43
v_mfma_f32_16x16x32_bf16 v[220:223], v[124:127], v[56:59], v[220:223]
s_xor_b32 s48, s95, 3072
buffer_store_dwordx4 v[40:43], v7, s[20:23], s48 offen sc1
v_mfma_f32_16x16x32_bf16 v[224:227], v[96:99], v[60:63], v[224:227]
ds_read_b128 v[56:59], v24 offset:4096
v_accvgpr_read_b32 v36, a152
v_mfma_f32_16x16x32_bf16 v[228:231], v[100:103], v[60:63], v[228:231]
buffer_load_dwordx4 v21, s[12:15], s25 offen offset:1024 lds
v_accvgpr_read_b32 v37, a153
v_mfma_f32_16x16x32_bf16 v[232:235], v[104:107], v[60:63], v[232:235]
v_mul_f32 v36, s47, v36
v_mul_f32 v37, s47, v37
v_mfma_f32_16x16x32_bf16 v[236:239], v[108:111], v[60:63], v[236:239]
v_cvt_pk_bf16_f32 v44, v36, v37
v_accvgpr_read_b32 v38, a154
v_mfma_f32_16x16x32_bf16 v[240:243], v[112:115], v[60:63], v[240:243]
v_accvgpr_read_b32 v39, a155
v_mul_f32 v38, s47, v38
v_mfma_f32_16x16x32_bf16 v[244:247], v[116:119], v[60:63], v[244:247]
v_mul_f32 v39, s47, v39
v_cvt_pk_bf16_f32 v45, v38, v39
v_mfma_f32_16x16x32_bf16 v[248:251], v[120:123], v[60:63], v[248:251]
v_accvgpr_read_b32 v36, a156
v_accvgpr_read_b32 v37, a157
v_mfma_f32_16x16x32_bf16 v[252:255], v[124:127], v[60:63], v[252:255]
s_waitcnt lgkmcnt(0)
v_mul_f32 v36, s47, v36
v_mul_f32 v37, s47, v37
v_mfma_f32_16x16x32_bf16 v[128:131], v[64:67], v[48:51], v[128:131]
ds_read_b128 v[60:63], v24 offset:6144
v_cvt_pk_bf16_f32 v46, v36, v37
v_mfma_f32_16x16x32_bf16 v[132:135], v[68:71], v[48:51], v[132:135]
v_add_u32 v24, 65536, v24
v_accvgpr_read_b32 v38, a158
v_mfma_f32_16x16x32_bf16 v[136:139], v[72:75], v[48:51], v[136:139]
ds_read_b64_tr_b16 v[96:97], v26 offset:24576
ds_read_b64_tr_b16 v[98:99], v26 offset:25600
v_mfma_f32_16x16x32_bf16 v[140:143], v[76:79], v[48:51], v[140:143]
buffer_load_dwordx4 v22, s[12:15], s25 offen offset:2048 lds
v_accvgpr_read_b32 v39, a159
v_mfma_f32_16x16x32_bf16 v[144:147], v[80:83], v[48:51], v[144:147]
ds_read_b64_tr_b16 v[100:101], v27 offset:24576
ds_read_b64_tr_b16 v[102:103], v27 offset:25600
v_mfma_f32_16x16x32_bf16 v[148:151], v[84:87], v[48:51], v[148:151]
v_mul_f32 v38, s47, v38
v_mul_f32 v39, s47, v39
v_mfma_f32_16x16x32_bf16 v[152:155], v[88:91], v[48:51], v[152:155]
ds_read_b64_tr_b16 v[104:105], v28 offset:24576
ds_read_b64_tr_b16 v[106:107], v28 offset:25600
v_mfma_f32_16x16x32_bf16 v[156:159], v[92:95], v[48:51], v[156:159]
buffer_load_dwordx4 v23, s[12:15], s25 offen offset:3072 lds
v_cvt_pk_bf16_f32 v47, v38, v39
v_mfma_f32_16x16x32_bf16 v[160:163], v[64:67], v[52:55], v[160:163]
ds_read_b128 v[48:51], v25 offset:0
v_mfma_f32_16x16x32_bf16 v[164:167], v[68:71], v[52:55], v[164:167]
s_add_u32 s24, s24, 128
s_add_u32 s25, s25, 128
v_mfma_f32_16x16x32_bf16 v[168:171], v[72:75], v[52:55], v[168:171]
s_add_u32 m0, s91, 0
ds_read_b64_tr_b16 v[108:109], v29 offset:24576
ds_read_b64_tr_b16 v[110:111], v29 offset:25600
v_mfma_f32_16x16x32_bf16 v[172:175], v[76:79], v[52:55], v[172:175]
buffer_load_dwordx4 v16, s[16:19], s26 offen offset:0 lds
v_mfma_f32_16x16x32_bf16 v[176:179], v[80:83], v[52:55], v[176:179]
ds_read_b64_tr_b16 v[112:113], v30 offset:24576
ds_read_b64_tr_b16 v[114:115], v30 offset:25600
.Lpad5:
s_cbranch_execz .Ltch1
v_mfma_f32_16x16x32_bf16 v[180:183], v[84:87], v[52:55], v[180:183]
v_permlane32_swap_b32 v44, v46
v_mfma_f32_16x16x32_bf16 v[184:187], v[88:91], v[52:55], v[184:187]
ds_read_b64_tr_b16 v[116:117], v31 offset:24576
ds_read_b64_tr_b16 v[118:119], v31 offset:25600
v_mfma_f32_16x16x32_bf16 v[188:191], v[92:95], v[52:55], v[188:191]
buffer_load_dwordx4 v17, s[16:19], s26 offen offset:1024 lds
v_mfma_f32_16x16x32_bf16 v[192:195], v[64:67], v[56:59], v[192:195]
ds_read_b128 v[52:55], v25 offset:2048
v_mfma_f32_16x16x32_bf16 v[196:199], v[68:71], v[56:59], v[196:199]
v_permlane32_swap_b32 v45, v47
v_mfma_f32_16x16x32_bf16 v[200:203], v[72:75], v[56:59], v[200:203]
ds_read_b64_tr_b16 v[120:121], v32 offset:24576
ds_read_b64_tr_b16 v[122:123], v32 offset:25600
v_mfma_f32_16x16x32_bf16 v[204:207], v[76:79], v[56:59], v[204:207]
buffer_load_dwordx4 v18, s[16:19], s26 offen offset:2048 lds
v_accvgpr_write_b32 a140, v44
v_mfma_f32_16x16x32_bf16 v[208:211], v[80:83], v[56:59], v[208:211]
ds_read_b64_tr_b16 v[124:125], v33 offset:24576
ds_read_b64_tr_b16 v[126:127], v33 offset:25600
v_mfma_f32_16x16x32_bf16 v[212:215], v[84:87], v[56:59], v[212:215]
v_accvgpr_write_b32 a141, v45
v_accvgpr_write_b32 a142, v46
v_mfma_f32_16x16x32_bf16 v[216:219], v[88:91], v[56:59], v[216:219]
s_waitcnt lgkmcnt(15)
v_accvgpr_write_b32 a143, v47
v_accvgpr_read_b32 v36, a32
v_mfma_f32_16x16x32_bf16 v[220:223], v[92:95], v[56:59], v[220:223]
v_xor_b32 v26, 0x10000, v26
v_accvgpr_read_b32 v37, a33
v_mfma_f32_16x16x32_bf16 v[224:227], v[64:67], v[60:63], v[224:227]
ds_read_b128 v[56:59], v25 offset:4096
v_xor_b32 v27, 0x10000, v27
v_mfma_f32_16x16x32_bf16 v[228:231], v[68:71], v[60:63], v[228:231]
buffer_load_dwordx4 v19, s[16:19], s26 offen offset:3072 lds
v_xor_b32 v28, 0x10000, v28
v_mfma_f32_16x16x32_bf16 v[232:235], v[72:75], v[60:63], v[232:235]
v_xor_b32 v29, 0x10000, v29
v_mul_f32 v36, s47, v36
v_mfma_f32_16x16x32_bf16 v[236:239], v[76:79], v[60:63], v[236:239]
s_add_u32 s26, s26, 524288
v_xor_b32 v30, 0x10000, v30
v_mul_f32 v37, s47, v37
v_mfma_f32_16x16x32_bf16 v[240:243], v[80:83], v[60:63], v[240:243]
v_xor_b32 v31, 0x10000, v31
v_cvt_pk_bf16_f32 v40, v36, v37
v_mfma_f32_16x16x32_bf16 v[244:247], v[84:87], v[60:63], v[244:247]
v_xor_b32 v32, 0x10000, v32
v_accvgpr_read_b32 v38, a34
v_mfma_f32_16x16x32_bf16 v[248:251], v[88:91], v[60:63], v[248:251]
v_xor_b32 v33, 0x10000, v33
v_accvgpr_read_b32 v39, a35
v_mfma_f32_16x16x32_bf16 v[252:255], v[92:95], v[60:63], v[252:255]
s_waitcnt vmcnt(17) lgkmcnt(0)
s_barrier
v_mul_f32 v38, s47, v38
v_mfma_f32_16x16x32_bf16 v[128:131], v[96:99], v[48:51], v[128:131]
ds_read_b128 v[60:63], v25 offset:6144
v_mul_f32 v39, s47, v39
v_mfma_f32_16x16x32_bf16 v[132:135], v[100:103], v[48:51], v[132:135]
v_add_u32 v25, 65536, v25
v_cvt_pk_bf16_f32 v41, v38, v39
v_mfma_f32_16x16x32_bf16 v[136:139], v[104:107], v[48:51], v[136:139]
s_add_u32 m0, s92, 32768
ds_read_b64_tr_b16 v[64:65], v26 offset:0
ds_read_b64_tr_b16 v[66:67], v26 offset:1024
v_mfma_f32_16x16x32_bf16 v[140:143], v[108:111], v[48:51], v[140:143]
buffer_load_dwordx4 v20, s[12:15], s24 offen offset:0 lds
v_accvgpr_read_b32 v36, a36
v_mfma_f32_16x16x32_bf16 v[144:147], v[112:115], v[48:51], v[144:147]
ds_read_b64_tr_b16 v[68:69], v27 offset:0
ds_read_b64_tr_b16 v[70:71], v27 offset:1024
v_mfma_f32_16x16x32_bf16 v[148:151], v[116:119], v[48:51], v[148:151]
v_accvgpr_read_b32 v37, a37
v_mul_f32 v36, s47, v36
v_mfma_f32_16x16x32_bf16 v[152:155], v[120:123], v[48:51], v[152:155]
ds_read_b64_tr_b16 v[72:73], v28 offset:0
ds_read_b64_tr_b16 v[74:75], v28 offset:1024
v_mfma_f32_16x16x32_bf16 v[156:159], v[124:127], v[48:51], v[156:159]
buffer_load_dwordx4 v21, s[12:15], s24 offen offset:1024 lds
v_mul_f32 v37, s47, v37
v_mfma_f32_16x16x32_bf16 v[160:163], v[96:99], v[52:55], v[160:163]
ds_read_b128 v[48:51], v24 offset:0
v_cvt_pk_bf16_f32 v42, v36, v37
v_mfma_f32_16x16x32_bf16 v[164:167], v[100:103], v[52:55], v[164:167]
v_accvgpr_read_b32 v38, a38
v_accvgpr_read_b32 v39, a39
v_mfma_f32_16x16x32_bf16 v[168:171], v[104:107], v[52:55], v[168:171]
ds_read_b64_tr_b16 v[76:77], v29 offset:0
ds_read_b64_tr_b16 v[78:79], v29 offset:1024
v_mfma_f32_16x16x32_bf16 v[172:175], v[108:111], v[52:55], v[172:175]
buffer_load_dwordx4 v22, s[12:15], s24 offen offset:2048 lds
v_mul_f32 v38, s47, v38
v_mfma_f32_16x16x32_bf16 v[176:179], v[112:115], v[52:55], v[176:179]
ds_read_b64_tr_b16 v[80:81], v30 offset:0
ds_read_b64_tr_b16 v[82:83], v30 offset:1024
v_mfma_f32_16x16x32_bf16 v[180:183], v[116:119], v[52:55], v[180:183]
v_mul_f32 v39, s47, v39
v_cvt_pk_bf16_f32 v43, v38, v39
v_mfma_f32_16x16x32_bf16 v[184:187], v[120:123], v[52:55], v[184:187]
ds_read_b64_tr_b16 v[84:85], v31 offset:0
ds_read_b64_tr_b16 v[86:87], v31 offset:1024
v_mfma_f32_16x16x32_bf16 v[188:191], v[124:127], v[52:55], v[188:191]
buffer_load_dwordx4 v23, s[12:15], s24 offen offset:3072 lds
v_accvgpr_read_b32 v36, a160
v_mfma_f32_16x16x32_bf16 v[192:195], v[96:99], v[56:59], v[192:195]
ds_read_b128 v[52:55], v24 offset:2048
v_accvgpr_read_b32 v37, a161
v_mfma_f32_16x16x32_bf16 v[196:199], v[100:103], v[56:59], v[196:199]
v_permlane32_swap_b32 v40, v42
v_mfma_f32_16x16x32_bf16 v[200:203], v[104:107], v[56:59], v[200:203]
s_add_u32 m0, s92, 36864
ds_read_b64_tr_b16 v[88:89], v32 offset:0
ds_read_b64_tr_b16 v[90:91], v32 offset:1024
v_mfma_f32_16x16x32_bf16 v[204:207], v[108:111], v[56:59], v[204:207]
buffer_load_dwordx4 v20, s[12:15], s25 offen offset:0 lds
v_mul_f32 v36, s47, v36
v_mfma_f32_16x16x32_bf16 v[208:211], v[112:115], v[56:59], v[208:211]
ds_read_b64_tr_b16 v[92:93], v33 offset:0
ds_read_b64_tr_b16 v[94:95], v33 offset:1024
v_mfma_f32_16x16x32_bf16 v[212:215], v[116:119], v[56:59], v[212:215]
v_permlane32_swap_b32 v41, v43
v_mfma_f32_16x16x32_bf16 v[216:219], v[120:123], v[56:59], v[216:219]
s_waitcnt lgkmcnt(15)
s_xor_b32 s48, s95, 65536
v_mul_f32 v37, s47, v37
v_mfma_f32_16x16x32_bf16 v[220:223], v[124:127], v[56:59], v[220:223]
buffer_store_dwordx4 v[40:43], v7, s[20:23], s48 offen sc1
v_cvt_pk_bf16_f32 v44, v36, v37
v_mfma_f32_16x16x32_bf16 v[224:227], v[96:99], v[60:63], v[224:227]
ds_read_b128 v[56:59], v24 offset:4096
v_accvgpr_read_b32 v38, a162
v_mfma_f32_16x16x32_bf16 v[228:231], v[100:103], v[60:63], v[228:231]
buffer_load_dwordx4 v21, s[12:15], s25 offen offset:1024 lds
v_accvgpr_read_b32 v39, a163
v_mfma_f32_16x16x32_bf16 v[232:235], v[104:107], v[60:63], v[232:235]
v_mul_f32 v38, s47, v38
v_mul_f32 v39, s47, v39
v_mfma_f32_16x16x32_bf16 v[236:239], v[108:111], v[60:63], v[236:239]
v_cvt_pk_bf16_f32 v45, v38, v39
v_accvgpr_read_b32 v36, a164
v_mfma_f32_16x16x32_bf16 v[240:243], v[112:115], v[60:63], v[240:243]
v_accvgpr_read_b32 v37, a165
v_mul_f32 v36, s47, v36
v_mfma_f32_16x16x32_bf16 v[244:247], v[116:119], v[60:63], v[244:247]
v_mul_f32 v37, s47, v37
v_cvt_pk_bf16_f32 v46, v36, v37
v_mfma_f32_16x16x32_bf16 v[248:251], v[120:123], v[60:63], v[248:251]
v_accvgpr_read_b32 v38, a166
v_accvgpr_read_b32 v39, a167
v_mfma_f32_16x16x32_bf16 v[252:255], v[124:127], v[60:63], v[252:255]
s_waitcnt lgkmcnt(0)
v_mul_f32 v38, s47, v38
v_mul_f32 v39, s47, v39
v_mfma_f32_16x16x32_bf16 v[128:131], v[64:67], v[48:51], v[128:131]
ds_read_b128 v[60:63], v24 offset:6144
v_cvt_pk_bf16_f32 v47, v38, v39
v_mfma_f32_16x16x32_bf16 v[132:135], v[68:71], v[48:51], v[132:135]
v_add_u32 v24, 32768, v24
v_mfma_f32_16x16x32_bf16 v[136:139], v[72:75], v[48:51], v[136:139]
ds_read_b64_tr_b16 v[96:97], v26 offset:8192
ds_read_b64_tr_b16 v[98:99], v26 offset:9216
v_mfma_f32_16x16x32_bf16 v[140:143], v[76:79], v[48:51], v[140:143]
buffer_load_dwordx4 v22, s[12:15], s25 offen offset:2048 lds
v_mfma_f32_16x16x32_bf16 v[144:147], v[80:83], v[48:51], v[144:147]
ds_read_b64_tr_b16 v[100:101], v27 offset:8192
ds_read_b64_tr_b16 v[102:103], v27 offset:9216
v_mfma_f32_16x16x32_bf16 v[148:151], v[84:87], v[48:51], v[148:151]
v_permlane32_swap_b32 v44, v46
v_mfma_f32_16x16x32_bf16 v[152:155], v[88:91], v[48:51], v[152:155]
ds_read_b64_tr_b16 v[104:105], v28 offset:8192
ds_read_b64_tr_b16 v[106:107], v28 offset:9216
v_mfma_f32_16x16x32_bf16 v[156:159], v[92:95], v[48:51], v[156:159]
buffer_load_dwordx4 v23, s[12:15], s25 offen offset:3072 lds
v_mfma_f32_16x16x32_bf16 v[160:163], v[64:67], v[52:55], v[160:163]
ds_read_b128 v[48:51], v25 offset:0
v_mfma_f32_16x16x32_bf16 v[164:167], v[68:71], v[52:55], v[164:167]
s_add_u32 s24, s24, 128
s_add_u32 s25, s25, 128
buffer_load_dword v35, v4, s[96:99], 0 offen
v_mfma_f32_16x16x32_bf16 v[168:171], v[72:75], v[52:55], v[168:171]
s_add_u32 m0, s91, 16384
ds_read_b64_tr_b16 v[108:109], v29 offset:8192
ds_read_b64_tr_b16 v[110:111], v29 offset:9216
v_mfma_f32_16x16x32_bf16 v[172:175], v[76:79], v[52:55], v[172:175]
buffer_load_dwordx4 v16, s[16:19], s26 offen offset:0 lds
v_mfma_f32_16x16x32_bf16 v[176:179], v[80:83], v[52:55], v[176:179]
ds_read_b64_tr_b16 v[112:113], v30 offset:8192
ds_read_b64_tr_b16 v[114:115], v30 offset:9216
v_mfma_f32_16x16x32_bf16 v[180:183], v[84:87], v[52:55], v[180:183]
v_permlane32_swap_b32 v45, v47
v_mfma_f32_16x16x32_bf16 v[184:187], v[88:91], v[52:55], v[184:187]
ds_read_b64_tr_b16 v[116:117], v31 offset:8192
ds_read_b64_tr_b16 v[118:119], v31 offset:9216
v_mfma_f32_16x16x32_bf16 v[188:191], v[92:95], v[52:55], v[188:191]
buffer_load_dwordx4 v17, s[16:19], s26 offen offset:1024 lds
v_accvgpr_write_b32 a160, v44
v_mfma_f32_16x16x32_bf16 v[192:195], v[64:67], v[56:59], v[192:195]
ds_read_b128 v[52:55], v25 offset:2048
v_accvgpr_write_b32 a161, v45
v_mfma_f32_16x16x32_bf16 v[196:199], v[68:71], v[56:59], v[196:199]
v_accvgpr_write_b32 a162, v46
v_accvgpr_write_b32 a163, v47
v_mfma_f32_16x16x32_bf16 v[200:203], v[72:75], v[56:59], v[200:203]
ds_read_b64_tr_b16 v[120:121], v32 offset:8192
ds_read_b64_tr_b16 v[122:123], v32 offset:9216
v_mfma_f32_16x16x32_bf16 v[204:207], v[76:79], v[56:59], v[204:207]
buffer_load_dwordx4 v18, s[16:19], s26 offen offset:2048 lds
v_accvgpr_read_b32 v36, a40
v_mfma_f32_16x16x32_bf16 v[208:211], v[80:83], v[56:59], v[208:211]
ds_read_b64_tr_b16 v[124:125], v33 offset:8192
ds_read_b64_tr_b16 v[126:127], v33 offset:9216
v_mfma_f32_16x16x32_bf16 v[212:215], v[84:87], v[56:59], v[212:215]
v_accvgpr_read_b32 v37, a41
v_mul_f32 v36, s47, v36
v_mfma_f32_16x16x32_bf16 v[216:219], v[88:91], v[56:59], v[216:219]
s_waitcnt lgkmcnt(15)
v_mul_f32 v37, s47, v37
v_cvt_pk_bf16_f32 v40, v36, v37
v_mfma_f32_16x16x32_bf16 v[220:223], v[92:95], v[56:59], v[220:223]
v_accvgpr_read_b32 v38, a42
v_accvgpr_read_b32 v39, a43
v_mfma_f32_16x16x32_bf16 v[224:227], v[64:67], v[60:63], v[224:227]
ds_read_b128 v[56:59], v25 offset:4096
v_mul_f32 v38, s47, v38
v_mfma_f32_16x16x32_bf16 v[228:231], v[68:71], v[60:63], v[228:231]
buffer_load_dwordx4 v19, s[16:19], s26 offen offset:3072 lds
v_mul_f32 v39, s47, v39
v_mfma_f32_16x16x32_bf16 v[232:235], v[72:75], v[60:63], v[232:235]
v_cvt_pk_bf16_f32 v41, v38, v39
v_accvgpr_read_b32 v36, a44
v_mfma_f32_16x16x32_bf16 v[236:239], v[76:79], v[60:63], v[236:239]
s_add_u32 s26, s26, 524288
v_accvgpr_read_b32 v37, a45
v_mul_f32 v36, s47, v36
v_mfma_f32_16x16x32_bf16 v[240:243], v[80:83], v[60:63], v[240:243]
v_mul_f32 v37, s47, v37
v_cvt_pk_bf16_f32 v42, v36, v37
v_mfma_f32_16x16x32_bf16 v[244:247], v[84:87], v[60:63], v[244:247]
v_accvgpr_read_b32 v38, a46
v_accvgpr_read_b32 v39, a47
v_mfma_f32_16x16x32_bf16 v[248:251], v[88:91], v[60:63], v[248:251]
v_mul_f32 v38, s47, v38
v_mul_f32 v39, s47, v39
v_mfma_f32_16x16x32_bf16 v[252:255], v[92:95], v[60:63], v[252:255]
s_waitcnt vmcnt(18) lgkmcnt(0)
s_barrier
v_cvt_pk_bf16_f32 v43, v38, v39
v_mfma_f32_16x16x32_bf16 v[128:131], v[96:99], v[48:51], v[128:131]
ds_read_b128 v[60:63], v25 offset:6144
v_accvgpr_read_b32 v36, a168
v_mfma_f32_16x16x32_bf16 v[132:135], v[100:103], v[48:51], v[132:135]
v_add_u32 v25, 32768, v25
v_accvgpr_read_b32 v37, a169
v_mfma_f32_16x16x32_bf16 v[136:139], v[104:107], v[48:51], v[136:139]
s_add_u32 m0, s92, 98304
ds_read_b64_tr_b16 v[64:65], v26 offset:16384
ds_read_b64_tr_b16 v[66:67], v26 offset:17408
v_mfma_f32_16x16x32_bf16 v[140:143], v[108:111], v[48:51], v[140:143]
buffer_load_dwordx4 v20, s[12:15], s24 offen offset:0 lds
v_mul_f32 v36, s47, v36
v_mfma_f32_16x16x32_bf16 v[144:147], v[112:115], v[48:51], v[144:147]
ds_read_b64_tr_b16 v[68:69], v27 offset:16384
ds_read_b64_tr_b16 v[70:71], v27 offset:17408
v_mfma_f32_16x16x32_bf16 v[148:151], v[116:119], v[48:51], v[148:151]
v_permlane32_swap_b32 v40, v42
v_mfma_f32_16x16x32_bf16 v[152:155], v[120:123], v[48:51], v[152:155]
ds_read_b64_tr_b16 v[72:73], v28 offset:16384
ds_read_b64_tr_b16 v[74:75], v28 offset:17408
v_mfma_f32_16x16x32_bf16 v[156:159], v[124:127], v[48:51], v[156:159]
buffer_load_dwordx4 v21, s[12:15], s24 offen offset:1024 lds
v_mul_f32 v37, s47, v37
v_mfma_f32_16x16x32_bf16 v[160:163], v[96:99], v[52:55], v[160:163]
ds_read_b128 v[48:51], v24 offset:0
v_cvt_pk_bf16_f32 v44, v36, v37
v_mfma_f32_16x16x32_bf16 v[164:167], v[100:103], v[52:55], v[164:167]
v_permlane32_swap_b32 v41, v43
v_mfma_f32_16x16x32_bf16 v[168:171], v[104:107], v[52:55], v[168:171]
ds_read_b64_tr_b16 v[76:77], v29 offset:16384
ds_read_b64_tr_b16 v[78:79], v29 offset:17408
v_mfma_f32_16x16x32_bf16 v[172:175], v[108:111], v[52:55], v[172:175]
s_xor_b32 s48, s95, 66560
buffer_load_dwordx4 v22, s[12:15], s24 offen offset:2048 lds
v_mfma_f32_16x16x32_bf16 v[176:179], v[112:115], v[52:55], v[176:179]
ds_read_b64_tr_b16 v[80:81], v30 offset:16384
ds_read_b64_tr_b16 v[82:83], v30 offset:17408
v_mfma_f32_16x16x32_bf16 v[180:183], v[116:119], v[52:55], v[180:183]
buffer_store_dwordx4 v[40:43], v7, s[20:23], s48 offen sc1
v_accvgpr_read_b32 v38, a170
v_mfma_f32_16x16x32_bf16 v[184:187], v[120:123], v[52:55], v[184:187]
ds_read_b64_tr_b16 v[84:85], v31 offset:16384
ds_read_b64_tr_b16 v[86:87], v31 offset:17408
v_mfma_f32_16x16x32_bf16 v[188:191], v[124:127], v[52:55], v[188:191]
buffer_load_dwordx4 v23, s[12:15], s24 offen offset:3072 lds
v_accvgpr_read_b32 v39, a171
v_mfma_f32_16x16x32_bf16 v[192:195], v[96:99], v[56:59], v[192:195]
ds_read_b128 v[52:55], v24 offset:2048
v_mul_f32 v38, s47, v38
v_mfma_f32_16x16x32_bf16 v[196:199], v[100:103], v[56:59], v[196:199]
v_mul_f32 v39, s47, v39
v_cvt_pk_bf16_f32 v45, v38, v39
v_mfma_f32_16x16x32_bf16 v[200:203], v[104:107], v[56:59], v[200:203]
s_add_u32 m0, s92, 102400
ds_read_b64_tr_b16 v[88:89], v32 offset:16384
ds_read_b64_tr_b16 v[90:91], v32 offset:17408
v_mfma_f32_16x16x32_bf16 v[204:207], v[108:111], v[56:59], v[204:207]
buffer_load_dwordx4 v20, s[12:15], s25 offen offset:0 lds
v_accvgpr_read_b32 v36, a172
v_mfma_f32_16x16x32_bf16 v[208:211], v[112:115], v[56:59], v[208:211]
ds_read_b64_tr_b16 v[92:93], v33 offset:16384
ds_read_b64_tr_b16 v[94:95], v33 offset:17408
v_mfma_f32_16x16x32_bf16 v[212:215], v[116:119], v[56:59], v[212:215]
v_accvgpr_read_b32 v37, a173
v_mul_f32 v36, s47, v36
v_mfma_f32_16x16x32_bf16 v[216:219], v[120:123], v[56:59], v[216:219]
s_waitcnt lgkmcnt(15)
v_mul_f32 v37, s47, v37
v_cvt_pk_bf16_f32 v46, v36, v37
v_mfma_f32_16x16x32_bf16 v[220:223], v[124:127], v[56:59], v[220:223]
v_accvgpr_read_b32 v38, a174
v_accvgpr_read_b32 v39, a175
v_mfma_f32_16x16x32_bf16 v[224:227], v[96:99], v[60:63], v[224:227]
ds_read_b128 v[56:59], v24 offset:4096
v_mul_f32 v38, s47, v38
v_mfma_f32_16x16x32_bf16 v[228:231], v[100:103], v[60:63], v[228:231]
buffer_load_dwordx4 v21, s[12:15], s25 offen offset:1024 lds
v_mul_f32 v39, s47, v39
v_mfma_f32_16x16x32_bf16 v[232:235], v[104:107], v[60:63], v[232:235]
v_cvt_pk_bf16_f32 v47, v38, v39
v_mfma_f32_16x16x32_bf16 v[236:239], v[108:111], v[60:63], v[236:239]
v_permlane32_swap_b32 v44, v46
v_mfma_f32_16x16x32_bf16 v[240:243], v[112:115], v[60:63], v[240:243]
v_permlane32_swap_b32 v45, v47
v_mfma_f32_16x16x32_bf16 v[244:247], v[116:119], v[60:63], v[244:247]
v_accvgpr_write_b32 a164, v44
v_accvgpr_write_b32 a165, v45
v_mfma_f32_16x16x32_bf16 v[248:251], v[120:123], v[60:63], v[248:251]
v_accvgpr_write_b32 a166, v46
v_accvgpr_write_b32 a167, v47
v_mfma_f32_16x16x32_bf16 v[252:255], v[124:127], v[60:63], v[252:255]
s_waitcnt lgkmcnt(0)
v_accvgpr_read_b32 v36, a48
v_accvgpr_read_b32 v37, a49
v_mfma_f32_16x16x32_bf16 v[128:131], v[64:67], v[48:51], v[128:131]
ds_read_b128 v[60:63], v24 offset:6144
v_mul_f32 v36, s47, v36
v_mfma_f32_16x16x32_bf16 v[132:135], v[68:71], v[48:51], v[132:135]
v_add_u32 v24, 4294868992, v24
v_mul_f32 v37, s47, v37
v_mfma_f32_16x16x32_bf16 v[136:139], v[72:75], v[48:51], v[136:139]
ds_read_b64_tr_b16 v[96:97], v26 offset:24576
ds_read_b64_tr_b16 v[98:99], v26 offset:25600
v_mfma_f32_16x16x32_bf16 v[140:143], v[76:79], v[48:51], v[140:143]
buffer_load_dwordx4 v22, s[12:15], s25 offen offset:2048 lds
v_cvt_pk_bf16_f32 v40, v36, v37
v_mfma_f32_16x16x32_bf16 v[144:147], v[80:83], v[48:51], v[144:147]
ds_read_b64_tr_b16 v[100:101], v27 offset:24576
ds_read_b64_tr_b16 v[102:103], v27 offset:25600
v_mfma_f32_16x16x32_bf16 v[148:151], v[84:87], v[48:51], v[148:151]
v_accvgpr_read_b32 v38, a50
v_accvgpr_read_b32 v39, a51
v_mfma_f32_16x16x32_bf16 v[152:155], v[88:91], v[48:51], v[152:155]
ds_read_b64_tr_b16 v[104:105], v28 offset:24576
ds_read_b64_tr_b16 v[106:107], v28 offset:25600
v_mfma_f32_16x16x32_bf16 v[156:159], v[92:95], v[48:51], v[156:159]
buffer_load_dwordx4 v23, s[12:15], s25 offen offset:3072 lds
v_mul_f32 v38, s47, v38
v_mfma_f32_16x16x32_bf16 v[160:163], v[64:67], v[52:55], v[160:163]
ds_read_b128 v[48:51], v25 offset:0
v_mul_f32 v39, s47, v39
v_mfma_f32_16x16x32_bf16 v[164:167], v[68:71], v[52:55], v[164:167]
s_add_u32 s24, s24, 128
s_add_u32 s25, s25, 128
v_cvt_pk_bf16_f32 v41, v38, v39
v_mfma_f32_16x16x32_bf16 v[168:171], v[72:75], v[52:55], v[168:171]
s_add_u32 m0, s91, 65536
ds_read_b64_tr_b16 v[108:109], v29 offset:24576
ds_read_b64_tr_b16 v[110:111], v29 offset:25600
v_mfma_f32_16x16x32_bf16 v[172:175], v[76:79], v[52:55], v[172:175]
buffer_load_dwordx4 v16, s[16:19], s26 offen offset:0 lds
v_accvgpr_read_b32 v36, a52
v_mfma_f32_16x16x32_bf16 v[176:179], v[80:83], v[52:55], v[176:179]
ds_read_b64_tr_b16 v[112:113], v30 offset:24576
ds_read_b64_tr_b16 v[114:115], v30 offset:25600
v_mfma_f32_16x16x32_bf16 v[180:183], v[84:87], v[52:55], v[180:183]
v_accvgpr_read_b32 v37, a53
v_mul_f32 v36, s47, v36
v_mfma_f32_16x16x32_bf16 v[184:187], v[88:91], v[52:55], v[184:187]
ds_read_b64_tr_b16 v[116:117], v31 offset:24576
ds_read_b64_tr_b16 v[118:119], v31 offset:25600
v_mfma_f32_16x16x32_bf16 v[188:191], v[92:95], v[52:55], v[188:191]
buffer_load_dwordx4 v17, s[16:19], s26 offen offset:1024 lds
v_mul_f32 v37, s47, v37
v_mfma_f32_16x16x32_bf16 v[192:195], v[64:67], v[56:59], v[192:195]
ds_read_b128 v[52:55], v25 offset:2048
v_cvt_pk_bf16_f32 v42, v36, v37
v_mfma_f32_16x16x32_bf16 v[196:199], v[68:71], v[56:59], v[196:199]
v_accvgpr_read_b32 v38, a54
v_accvgpr_read_b32 v39, a55
v_mfma_f32_16x16x32_bf16 v[200:203], v[72:75], v[56:59], v[200:203]
ds_read_b64_tr_b16 v[120:121], v32 offset:24576
ds_read_b64_tr_b16 v[122:123], v32 offset:25600
v_mfma_f32_16x16x32_bf16 v[204:207], v[76:79], v[56:59], v[204:207]
buffer_load_dwordx4 v18, s[16:19], s26 offen offset:2048 lds
v_mul_f32 v38, s47, v38
v_mfma_f32_16x16x32_bf16 v[208:211], v[80:83], v[56:59], v[208:211]
ds_read_b64_tr_b16 v[124:125], v33 offset:24576
ds_read_b64_tr_b16 v[126:127], v33 offset:25600
v_mfma_f32_16x16x32_bf16 v[212:215], v[84:87], v[56:59], v[212:215]
v_mul_f32 v39, s47, v39
v_cvt_pk_bf16_f32 v43, v38, v39
v_mfma_f32_16x16x32_bf16 v[216:219], v[88:91], v[56:59], v[216:219]
s_waitcnt lgkmcnt(15)
v_permlane32_swap_b32 v40, v42
v_mfma_f32_16x16x32_bf16 v[220:223], v[92:95], v[56:59], v[220:223]
v_xor_b32 v26, 0x10000, v26
v_mfma_f32_16x16x32_bf16 v[224:227], v[64:67], v[60:63], v[224:227]
ds_read_b128 v[56:59], v25 offset:4096
v_xor_b32 v27, 0x10000, v27
v_mfma_f32_16x16x32_bf16 v[228:231], v[68:71], v[60:63], v[228:231]
buffer_load_dwordx4 v19, s[16:19], s26 offen offset:3072 lds
v_xor_b32 v28, 0x10000, v28
v_mfma_f32_16x16x32_bf16 v[232:235], v[72:75], v[60:63], v[232:235]
v_xor_b32 v29, 0x10000, v29
v_mfma_f32_16x16x32_bf16 v[236:239], v[76:79], v[60:63], v[236:239]
s_add_u32 s26, s26, 524288
v_xor_b32 v30, 0x10000, v30
v_mfma_f32_16x16x32_bf16 v[240:243], v[80:83], v[60:63], v[240:243]
v_xor_b32 v31, 0x10000, v31
v_mfma_f32_16x16x32_bf16 v[244:247], v[84:87], v[60:63], v[244:247]
v_xor_b32 v32, 0x10000, v32
v_mfma_f32_16x16x32_bf16 v[248:251], v[88:91], v[60:63], v[248:251]
v_xor_b32 v33, 0x10000, v33
v_mfma_f32_16x16x32_bf16 v[252:255], v[92:95], v[60:63], v[252:255]
s_waitcnt vmcnt(18) lgkmcnt(0)
s_barrier
v_accvgpr_read_b32 v36, a56
v_mfma_f32_16x16x32_bf16 v[128:131], v[96:99], v[48:51], v[128:131]
ds_read_b128 v[60:63], v25 offset:6144
v_accvgpr_read_b32 v37, a57
v_mfma_f32_16x16x32_bf16 v[132:135], v[100:103], v[48:51], v[132:135]
v_add_u32 v25, 4294868992, v25
v_mul_f32 v36, s47, v36
v_mfma_f32_16x16x32_bf16 v[136:139], v[104:107], v[48:51], v[136:139]
s_add_u32 m0, s92, 131072
ds_read_b64_tr_b16 v[64:65], v26 offset:0
ds_read_b64_tr_b16 v[66:67], v26 offset:1024
v_mfma_f32_16x16x32_bf16 v[140:143], v[108:111], v[48:51], v[140:143]
buffer_load_dwordx4 v20, s[12:15], s24 offen offset:0 lds
v_mul_f32 v37, s47, v37
v_mfma_f32_16x16x32_bf16 v[144:147], v[112:115], v[48:51], v[144:147]
ds_read_b64_tr_b16 v[68:69], v27 offset:0
ds_read_b64_tr_b16 v[70:71], v27 offset:1024
v_mfma_f32_16x16x32_bf16 v[148:151], v[116:119], v[48:51], v[148:151]
v_permlane32_swap_b32 v41, v43
v_mfma_f32_16x16x32_bf16 v[152:155], v[120:123], v[48:51], v[152:155]
ds_read_b64_tr_b16 v[72:73], v28 offset:0
ds_read_b64_tr_b16 v[74:75], v28 offset:1024
v_mfma_f32_16x16x32_bf16 v[156:159], v[124:127], v[48:51], v[156:159]
s_xor_b32 s48, s95, 67584
buffer_load_dwordx4 v21, s[12:15], s24 offen offset:1024 lds
v_mfma_f32_16x16x32_bf16 v[160:163], v[96:99], v[52:55], v[160:163]
ds_read_b128 v[48:51], v24 offset:0
v_cvt_pk_bf16_f32 v44, v36, v37
v_mfma_f32_16x16x32_bf16 v[164:167], v[100:103], v[52:55], v[164:167]
buffer_store_dwordx4 v[40:43], v7, s[20:23], s48 offen sc1
v_accvgpr_read_b32 v38, a58
v_mfma_f32_16x16x32_bf16 v[168:171], v[104:107], v[52:55], v[168:171]
ds_read_b64_tr_b16 v[76:77], v29 offset:0
ds_read_b64_tr_b16 v[78:79], v29 offset:1024
v_mfma_f32_16x16x32_bf16 v[172:175], v[108:111], v[52:55], v[172:175]
buffer_load_dwordx4 v22, s[12:15], s24 offen offset:2048 lds
v_accvgpr_read_b32 v39, a59
v_mfma_f32_16x16x32_bf16 v[176:179], v[112:115], v[52:55], v[176:179]
ds_read_b64_tr_b16 v[80:81], v30 offset:0
ds_read_b64_tr_b16 v[82:83], v30 offset:1024
v_mfma_f32_16x16x32_bf16 v[180:183], v[116:119], v[52:55], v[180:183]
v_mul_f32 v38, s47, v38
v_mul_f32 v39, s47, v39
v_mfma_f32_16x16x32_bf16 v[184:187], v[120:123], v[52:55], v[184:187]
ds_read_b64_tr_b16 v[84:85], v31 offset:0
ds_read_b64_tr_b16 v[86:87], v31 offset:1024
v_mfma_f32_16x16x32_bf16 v[188:191], v[124:127], v[52:55], v[188:191]
buffer_load_dwordx4 v23, s[12:15], s24 offen offset:3072 lds
v_cvt_pk_bf16_f32 v45, v38, v39
v_mfma_f32_16x16x32_bf16 v[192:195], v[96:99], v[56:59], v[192:195]
ds_read_b128 v[52:55], v24 offset:2048
v_accvgpr_read_b32 v36, a60
v_mfma_f32_16x16x32_bf16 v[196:199], v[100:103], v[56:59], v[196:199]
v_accvgpr_read_b32 v37, a61
v_mul_f32 v36, s47, v36
v_mfma_f32_16x16x32_bf16 v[200:203], v[104:107], v[56:59], v[200:203]
s_add_u32 m0, s92, 135168
ds_read_b64_tr_b16 v[88:89], v32 offset:0
ds_read_b64_tr_b16 v[90:91], v32 offset:1024
v_mfma_f32_16x16x32_bf16 v[204:207], v[108:111], v[56:59], v[204:207]
buffer_load_dwordx4 v20, s[12:15], s25 offen offset:0 lds
v_mul_f32 v37, s47, v37
v_mfma_f32_16x16x32_bf16 v[208:211], v[112:115], v[56:59], v[208:211]
ds_read_b64_tr_b16 v[92:93], v33 offset:0
ds_read_b64_tr_b16 v[94:95], v33 offset:1024
v_mfma_f32_16x16x32_bf16 v[212:215], v[116:119], v[56:59], v[212:215]
v_cvt_pk_bf16_f32 v46, v36, v37
v_accvgpr_read_b32 v38, a62
v_mfma_f32_16x16x32_bf16 v[216:219], v[120:123], v[56:59], v[216:219]
s_waitcnt lgkmcnt(15)
v_accvgpr_read_b32 v39, a63
v_mul_f32 v38, s47, v38
v_mfma_f32_16x16x32_bf16 v[220:223], v[124:127], v[56:59], v[220:223]
v_mul_f32 v39, s47, v39
v_cvt_pk_bf16_f32 v47, v38, v39
v_mfma_f32_16x16x32_bf16 v[224:227], v[96:99], v[60:63], v[224:227]
ds_read_b128 v[56:59], v24 offset:4096
v_accvgpr_read_b32 v36, a176
v_mfma_f32_16x16x32_bf16 v[228:231], v[100:103], v[60:63], v[228:231]
buffer_load_dwordx4 v21, s[12:15], s25 offen offset:1024 lds
v_accvgpr_read_b32 v37, a177
v_mfma_f32_16x16x32_bf16 v[232:235], v[104:107], v[60:63], v[232:235]
v_permlane32_swap_b32 v44, v46
v_mfma_f32_16x16x32_bf16 v[236:239], v[108:111], v[60:63], v[236:239]
v_permlane32_swap_b32 v45, v47
v_mfma_f32_16x16x32_bf16 v[240:243], v[112:115], v[60:63], v[240:243]
s_xor_b32 s48, s95, 68608
buffer_store_dwordx4 v[44:47], v7, s[20:23], s48 offen sc1
v_mfma_f32_16x16x32_bf16 v[244:247], v[116:119], v[60:63], v[244:247]
v_mul_f32 v36, s47, v36
v_mul_f32 v37, s47, v37
v_mfma_f32_16x16x32_bf16 v[248:251], v[120:123], v[60:63], v[248:251]
v_cvt_pk_bf16_f32 v40, v36, v37
v_accvgpr_read_b32 v38, a178
v_mfma_f32_16x16x32_bf16 v[252:255], v[124:127], v[60:63], v[252:255]
s_waitcnt lgkmcnt(0)
v_accvgpr_read_b32 v39, a179
v_mul_f32 v38, s47, v38
v_mfma_f32_16x16x32_bf16 v[128:131], v[64:67], v[48:51], v[128:131]
ds_read_b128 v[60:63], v24 offset:6144
v_mul_f32 v39, s47, v39
v_mfma_f32_16x16x32_bf16 v[132:135], v[68:71], v[48:51], v[132:135]
v_add_u32 v24, 65536, v24
v_cvt_pk_bf16_f32 v41, v38, v39
v_mfma_f32_16x16x32_bf16 v[136:139], v[72:75], v[48:51], v[136:139]
ds_read_b64_tr_b16 v[96:97], v26 offset:8192
ds_read_b64_tr_b16 v[98:99], v26 offset:9216
v_mfma_f32_16x16x32_bf16 v[140:143], v[76:79], v[48:51], v[140:143]
buffer_load_dwordx4 v22, s[12:15], s25 offen offset:2048 lds
v_accvgpr_read_b32 v36, a180
v_mfma_f32_16x16x32_bf16 v[144:147], v[80:83], v[48:51], v[144:147]
ds_read_b64_tr_b16 v[100:101], v27 offset:8192
ds_read_b64_tr_b16 v[102:103], v27 offset:9216
.Lpad6:
s_cbranch_execz .Ltch1
v_mfma_f32_16x16x32_bf16 v[148:151], v[84:87], v[48:51], v[148:151]
v_accvgpr_read_b32 v37, a181
v_mul_f32 v36, s47, v36
v_mfma_f32_16x16x32_bf16 v[152:155], v[88:91], v[48:51], v[152:155]
ds_read_b64_tr_b16 v[104:105], v28 offset:8192
ds_read_b64_tr_b16 v[106:107], v28 offset:9216
v_mfma_f32_16x16x32_bf16 v[156:159], v[92:95], v[48:51], v[156:159]
buffer_load_dwordx4 v23, s[12:15], s25 offen offset:3072 lds
v_mul_f32 v37, s47, v37
v_mfma_f32_16x16x32_bf16 v[160:163], v[64:67], v[52:55], v[160:163]
ds_read_b128 v[48:51], v25 offset:0
v_cvt_pk_bf16_f32 v42, v36, v37
v_mfma_f32_16x16x32_bf16 v[164:167], v[68:71], v[52:55], v[164:167]
s_add_u32 s24, s24, 128
s_add_u32 s25, s25, 128
v_accvgpr_read_b32 v38, a182
v_mfma_f32_16x16x32_bf16 v[168:171], v[72:75], v[52:55], v[168:171]
s_add_u32 m0, s91, 81920
ds_read_b64_tr_b16 v[108:109], v29 offset:8192
ds_read_b64_tr_b16 v[110:111], v29 offset:9216
v_mfma_f32_16x16x32_bf16 v[172:175], v[76:79], v[52:55], v[172:175]
buffer_load_dwordx4 v16, s[16:19], s26 offen offset:0 lds
v_accvgpr_read_b32 v39, a183
v_mfma_f32_16x16x32_bf16 v[176:179], v[80:83], v[52:55], v[176:179]
ds_read_b64_tr_b16 v[112:113], v30 offset:8192
ds_read_b64_tr_b16 v[114:115], v30 offset:9216
v_mfma_f32_16x16x32_bf16 v[180:183], v[84:87], v[52:55], v[180:183]
v_mul_f32 v38, s47, v38
v_mul_f32 v39, s47, v39
v_mfma_f32_16x16x32_bf16 v[184:187], v[88:91], v[52:55], v[184:187]
ds_read_b64_tr_b16 v[116:117], v31 offset:8192
ds_read_b64_tr_b16 v[118:119], v31 offset:9216
v_mfma_f32_16x16x32_bf16 v[188:191], v[92:95], v[52:55], v[188:191]
buffer_load_dwordx4 v17, s[16:19], s26 offen offset:1024 lds
v_cvt_pk_bf16_f32 v43, v38, v39
v_mfma_f32_16x16x32_bf16 v[192:195], v[64:67], v[56:59], v[192:195]
ds_read_b128 v[52:55], v25 offset:2048
v_mfma_f32_16x16x32_bf16 v[196:199], v[68:71], v[56:59], v[196:199]
v_permlane32_swap_b32 v40, v42
v_mfma_f32_16x16x32_bf16 v[200:203], v[72:75], v[56:59], v[200:203]
ds_read_b64_tr_b16 v[120:121], v32 offset:8192
ds_read_b64_tr_b16 v[122:123], v32 offset:9216
v_mfma_f32_16x16x32_bf16 v[204:207], v[76:79], v[56:59], v[204:207]
buffer_load_dwordx4 v18, s[16:19], s26 offen offset:2048 lds
v_mfma_f32_16x16x32_bf16 v[208:211], v[80:83], v[56:59], v[208:211]
ds_read_b64_tr_b16 v[124:125], v33 offset:8192
ds_read_b64_tr_b16 v[126:127], v33 offset:9216
v_mfma_f32_16x16x32_bf16 v[212:215], v[84:87], v[56:59], v[212:215]
v_permlane32_swap_b32 v41, v43
v_mfma_f32_16x16x32_bf16 v[216:219], v[88:91], v[56:59], v[216:219]
s_waitcnt lgkmcnt(15)
v_accvgpr_write_b32 a168, v40
v_accvgpr_write_b32 a169, v41
v_mfma_f32_16x16x32_bf16 v[220:223], v[92:95], v[56:59], v[220:223]
v_accvgpr_write_b32 a170, v42
v_accvgpr_write_b32 a171, v43
v_mfma_f32_16x16x32_bf16 v[224:227], v[64:67], v[60:63], v[224:227]
ds_read_b128 v[56:59], v25 offset:4096
v_accvgpr_read_b32 v36, a64
v_mfma_f32_16x16x32_bf16 v[228:231], v[68:71], v[60:63], v[228:231]
buffer_load_dwordx4 v19, s[16:19], s26 offen offset:3072 lds
v_accvgpr_read_b32 v37, a65
v_mfma_f32_16x16x32_bf16 v[232:235], v[72:75], v[60:63], v[232:235]
v_mul_f32 v36, s47, v36
v_mul_f32 v37, s47, v37
v_mfma_f32_16x16x32_bf16 v[236:239], v[76:79], v[60:63], v[236:239]
s_add_u32 s26, s26, 524288
v_cvt_pk_bf16_f32 v44, v36, v37
v_accvgpr_read_b32 v38, a66
v_mfma_f32_16x16x32_bf16 v[240:243], v[80:83], v[60:63], v[240:243]
v_accvgpr_read_b32 v39, a67
v_mul_f32 v38, s47, v38
v_mfma_f32_16x16x32_bf16 v[244:247], v[84:87], v[60:63], v[244:247]
v_mul_f32 v39, s47, v39
v_cvt_pk_bf16_f32 v45, v38, v39
v_mfma_f32_16x16x32_bf16 v[248:251], v[88:91], v[60:63], v[248:251]
v_accvgpr_read_b32 v36, a68
v_accvgpr_read_b32 v37, a69
v_mfma_f32_16x16x32_bf16 v[252:255], v[92:95], v[60:63], v[252:255]
s_waitcnt vmcnt(18) lgkmcnt(0)
s_barrier
v_mul_f32 v36, s47, v36
v_mfma_f32_16x16x32_bf16 v[128:131], v[96:99], v[48:51], v[128:131]
ds_read_b128 v[60:63], v25 offset:6144
v_mul_f32 v37, s47, v37
v_mfma_f32_16x16x32_bf16 v[132:135], v[100:103], v[48:51], v[132:135]
v_add_u32 v25, 65536, v25
v_cvt_pk_bf16_f32 v46, v36, v37
v_mfma_f32_16x16x32_bf16 v[136:139], v[104:107], v[48:51], v[136:139]
s_add_u32 m0, s92, 32768
ds_read_b64_tr_b16 v[64:65], v26 offset:16384
ds_read_b64_tr_b16 v[66:67], v26 offset:17408
v_mfma_f32_16x16x32_bf16 v[140:143], v[108:111], v[48:51], v[140:143]
buffer_load_dwordx4 v20, s[12:15], s24 offen offset:0 lds
v_accvgpr_read_b32 v38, a70
v_mfma_f32_16x16x32_bf16 v[144:147], v[112:115], v[48:51], v[144:147]
ds_read_b64_tr_b16 v[68:69], v27 offset:16384
ds_read_b64_tr_b16 v[70:71], v27 offset:17408
v_mfma_f32_16x16x32_bf16 v[148:151], v[116:119], v[48:51], v[148:151]
v_accvgpr_read_b32 v39, a71
v_mul_f32 v38, s47, v38
v_mfma_f32_16x16x32_bf16 v[152:155], v[120:123], v[48:51], v[152:155]
ds_read_b64_tr_b16 v[72:73], v28 offset:16384
ds_read_b64_tr_b16 v[74:75], v28 offset:17408
v_mfma_f32_16x16x32_bf16 v[156:159], v[124:127], v[48:51], v[156:159]
buffer_load_dwordx4 v21, s[12:15], s24 offen offset:1024 lds
v_mul_f32 v39, s47, v39
v_mfma_f32_16x16x32_bf16 v[160:163], v[96:99], v[52:55], v[160:163]
ds_read_b128 v[48:51], v24 offset:0
v_cvt_pk_bf16_f32 v47, v38, v39
v_mfma_f32_16x16x32_bf16 v[164:167], v[100:103], v[52:55], v[164:167]
v_permlane32_swap_b32 v44, v46
v_mfma_f32_16x16x32_bf16 v[168:171], v[104:107], v[52:55], v[168:171]
ds_read_b64_tr_b16 v[76:77], v29 offset:16384
ds_read_b64_tr_b16 v[78:79], v29 offset:17408
v_mfma_f32_16x16x32_bf16 v[172:175], v[108:111], v[52:55], v[172:175]
buffer_load_dwordx4 v22, s[12:15], s24 offen offset:2048 lds
v_accvgpr_read_b32 v36, a184
v_mfma_f32_16x16x32_bf16 v[176:179], v[112:115], v[52:55], v[176:179]
ds_read_b64_tr_b16 v[80:81], v30 offset:16384
ds_read_b64_tr_b16 v[82:83], v30 offset:17408
v_mfma_f32_16x16x32_bf16 v[180:183], v[116:119], v[52:55], v[180:183]
v_permlane32_swap_b32 v45, v47
v_mfma_f32_16x16x32_bf16 v[184:187], v[120:123], v[52:55], v[184:187]
ds_read_b64_tr_b16 v[84:85], v31 offset:16384
ds_read_b64_tr_b16 v[86:87], v31 offset:17408
v_mfma_f32_16x16x32_bf16 v[188:191], v[124:127], v[52:55], v[188:191]
s_xor_b32 s48, s95, 131072
buffer_load_dwordx4 v23, s[12:15], s24 offen offset:3072 lds
v_mfma_f32_16x16x32_bf16 v[192:195], v[96:99], v[56:59], v[192:195]
ds_read_b128 v[52:55], v24 offset:2048
v_accvgpr_read_b32 v37, a185
v_mfma_f32_16x16x32_bf16 v[196:199], v[100:103], v[56:59], v[196:199]
buffer_store_dwordx4 v[44:47], v7, s[20:23], s48 offen sc1
v_mul_f32 v36, s47, v36
v_mfma_f32_16x16x32_bf16 v[200:203], v[104:107], v[56:59], v[200:203]
s_add_u32 m0, s92, 36864
ds_read_b64_tr_b16 v[88:89], v32 offset:16384
ds_read_b64_tr_b16 v[90:91], v32 offset:17408
v_mfma_f32_16x16x32_bf16 v[204:207], v[108:111], v[56:59], v[204:207]
buffer_load_dwordx4 v20, s[12:15], s25 offen offset:0 lds
v_mul_f32 v37, s47, v37
v_mfma_f32_16x16x32_bf16 v[208:211], v[112:115], v[56:59], v[208:211]
ds_read_b64_tr_b16 v[92:93], v33 offset:16384
ds_read_b64_tr_b16 v[94:95], v33 offset:17408
v_mfma_f32_16x16x32_bf16 v[212:215], v[116:119], v[56:59], v[212:215]
v_cvt_pk_bf16_f32 v40, v36, v37
v_accvgpr_read_b32 v38, a186
v_mfma_f32_16x16x32_bf16 v[216:219], v[120:123], v[56:59], v[216:219]
s_waitcnt lgkmcnt(15)
v_accvgpr_read_b32 v39, a187
v_mul_f32 v38, s47, v38
v_mfma_f32_16x16x32_bf16 v[220:223], v[124:127], v[56:59], v[220:223]
v_mul_f32 v39, s47, v39
v_cvt_pk_bf16_f32 v41, v38, v39
v_mfma_f32_16x16x32_bf16 v[224:227], v[96:99], v[60:63], v[224:227]
ds_read_b128 v[56:59], v24 offset:4096
v_accvgpr_read_b32 v36, a188
v_mfma_f32_16x16x32_bf16 v[228:231], v[100:103], v[60:63], v[228:231]
buffer_load_dwordx4 v21, s[12:15], s25 offen offset:1024 lds
v_accvgpr_read_b32 v37, a189
v_mfma_f32_16x16x32_bf16 v[232:235], v[104:107], v[60:63], v[232:235]
v_mul_f32 v36, s47, v36
v_mul_f32 v37, s47, v37
v_mfma_f32_16x16x32_bf16 v[236:239], v[108:111], v[60:63], v[236:239]
v_cvt_pk_bf16_f32 v42, v36, v37
v_accvgpr_read_b32 v38, a190
v_mfma_f32_16x16x32_bf16 v[240:243], v[112:115], v[60:63], v[240:243]
v_accvgpr_read_b32 v39, a191
v_mul_f32 v38, s47, v38
v_mfma_f32_16x16x32_bf16 v[244:247], v[116:119], v[60:63], v[244:247]
v_mul_f32 v39, s47, v39
v_cvt_pk_bf16_f32 v43, v38, v39
v_mfma_f32_16x16x32_bf16 v[248:251], v[120:123], v[60:63], v[248:251]
v_permlane32_swap_b32 v40, v42
v_mfma_f32_16x16x32_bf16 v[252:255], v[124:127], v[60:63], v[252:255]
s_waitcnt lgkmcnt(0)
v_permlane32_swap_b32 v41, v43
v_mfma_f32_16x16x32_bf16 v[128:131], v[64:67], v[48:51], v[128:131]
ds_read_b128 v[60:63], v24 offset:6144
v_accvgpr_write_b32 a172, v40
v_mfma_f32_16x16x32_bf16 v[132:135], v[68:71], v[48:51], v[132:135]
v_add_u32 v24, 32768, v24
v_accvgpr_write_b32 a173, v41
v_mfma_f32_16x16x32_bf16 v[136:139], v[72:75], v[48:51], v[136:139]
ds_read_b64_tr_b16 v[96:97], v26 offset:24576
ds_read_b64_tr_b16 v[98:99], v26 offset:25600
v_mfma_f32_16x16x32_bf16 v[140:143], v[76:79], v[48:51], v[140:143]
buffer_load_dwordx4 v22, s[12:15], s25 offen offset:2048 lds
v_accvgpr_write_b32 a174, v42
v_mfma_f32_16x16x32_bf16 v[144:147], v[80:83], v[48:51], v[144:147]
ds_read_b64_tr_b16 v[100:101], v27 offset:24576
ds_read_b64_tr_b16 v[102:103], v27 offset:25600
v_mfma_f32_16x16x32_bf16 v[148:151], v[84:87], v[48:51], v[148:151]
v_accvgpr_write_b32 a175, v43
v_accvgpr_read_b32 v36, a72
v_mfma_f32_16x16x32_bf16 v[152:155], v[88:91], v[48:51], v[152:155]
ds_read_b64_tr_b16 v[104:105], v28 offset:24576
ds_read_b64_tr_b16 v[106:107], v28 offset:25600
v_mfma_f32_16x16x32_bf16 v[156:159], v[92:95], v[48:51], v[156:159]
buffer_load_dwordx4 v23, s[12:15], s25 offen offset:3072 lds
v_accvgpr_read_b32 v37, a73
v_mfma_f32_16x16x32_bf16 v[160:163], v[64:67], v[52:55], v[160:163]
ds_read_b128 v[48:51], v25 offset:0
v_mul_f32 v36, s47, v36
v_mfma_f32_16x16x32_bf16 v[164:167], v[68:71], v[52:55], v[164:167]
s_add_u32 s24, s24, 128
s_add_u32 s25, s25, 128
v_mul_f32 v37, s47, v37
v_mfma_f32_16x16x32_bf16 v[168:171], v[72:75], v[52:55], v[168:171]
s_add_u32 m0, s91, 0
ds_read_b64_tr_b16 v[108:109], v29 offset:24576
ds_read_b64_tr_b16 v[110:111], v29 offset:25600
v_mfma_f32_16x16x32_bf16 v[172:175], v[76:79], v[52:55], v[172:175]
buffer_load_dwordx4 v16, s[16:19], s26 offen offset:0 lds
v_cvt_pk_bf16_f32 v44, v36, v37
v_mfma_f32_16x16x32_bf16 v[176:179], v[80:83], v[52:55], v[176:179]
ds_read_b64_tr_b16 v[112:113], v30 offset:24576
ds_read_b64_tr_b16 v[114:115], v30 offset:25600
v_mfma_f32_16x16x32_bf16 v[180:183], v[84:87], v[52:55], v[180:183]
v_accvgpr_read_b32 v38, a74
v_accvgpr_read_b32 v39, a75
v_mfma_f32_16x16x32_bf16 v[184:187], v[88:91], v[52:55], v[184:187]
ds_read_b64_tr_b16 v[116:117], v31 offset:24576
ds_read_b64_tr_b16 v[118:119], v31 offset:25600
v_mfma_f32_16x16x32_bf16 v[188:191], v[92:95], v[52:55], v[188:191]
buffer_load_dwordx4 v17, s[16:19], s26 offen offset:1024 lds
v_mul_f32 v38, s47, v38
v_mfma_f32_16x16x32_bf16 v[192:195], v[64:67], v[56:59], v[192:195]
ds_read_b128 v[52:55], v25 offset:2048
v_mul_f32 v39, s47, v39
v_mfma_f32_16x16x32_bf16 v[196:199], v[68:71], v[56:59], v[196:199]
v_cvt_pk_bf16_f32 v45, v38, v39
v_accvgpr_read_b32 v36, a76
v_mfma_f32_16x16x32_bf16 v[200:203], v[72:75], v[56:59], v[200:203]
ds_read_b64_tr_b16 v[120:121], v32 offset:24576
ds_read_b64_tr_b16 v[122:123], v32 offset:25600
v_mfma_f32_16x16x32_bf16 v[204:207], v[76:79], v[56:59], v[204:207]
buffer_load_dwordx4 v18, s[16:19], s26 offen offset:2048 lds
v_accvgpr_read_b32 v37, a77
v_mfma_f32_16x16x32_bf16 v[208:211], v[80:83], v[56:59], v[208:211]
ds_read_b64_tr_b16 v[124:125], v33 offset:24576
ds_read_b64_tr_b16 v[126:127], v33 offset:25600
v_mfma_f32_16x16x32_bf16 v[212:215], v[84:87], v[56:59], v[212:215]
v_mul_f32 v36, s47, v36
v_mul_f32 v37, s47, v37
v_mfma_f32_16x16x32_bf16 v[216:219], v[88:91], v[56:59], v[216:219]
s_waitcnt lgkmcnt(15)
v_cvt_pk_bf16_f32 v46, v36, v37
v_accvgpr_read_b32 v38, a78
v_mfma_f32_16x16x32_bf16 v[220:223], v[92:95], v[56:59], v[220:223]
v_xor_b32 v26, 0x10000, v26
v_accvgpr_read_b32 v39, a79
v_mfma_f32_16x16x32_bf16 v[224:227], v[64:67], v[60:63], v[224:227]
ds_read_b128 v[56:59], v25 offset:4096
v_xor_b32 v27, 0x10000, v27
v_mfma_f32_16x16x32_bf16 v[228:231], v[68:71], v[60:63], v[228:231]
buffer_load_dwordx4 v19, s[16:19], s26 offen offset:3072 lds
v_xor_b32 v28, 0x10000, v28
v_mfma_f32_16x16x32_bf16 v[232:235], v[72:75], v[60:63], v[232:235]
v_xor_b32 v29, 0x10000, v29
v_mul_f32 v38, s47, v38
v_mfma_f32_16x16x32_bf16 v[236:239], v[76:79], v[60:63], v[236:239]
s_add_u32 s26, s26, 524288
v_xor_b32 v30, 0x10000, v30
v_mul_f32 v39, s47, v39
v_mfma_f32_16x16x32_bf16 v[240:243], v[80:83], v[60:63], v[240:243]
v_xor_b32 v31, 0x10000, v31
v_cvt_pk_bf16_f32 v47, v38, v39
v_mfma_f32_16x16x32_bf16 v[244:247], v[84:87], v[60:63], v[244:247]
v_xor_b32 v32, 0x10000, v32
v_accvgpr_read_b32 v36, a192
v_mfma_f32_16x16x32_bf16 v[248:251], v[88:91], v[60:63], v[248:251]
v_xor_b32 v33, 0x10000, v33
v_accvgpr_read_b32 v37, a193
v_mfma_f32_16x16x32_bf16 v[252:255], v[92:95], v[60:63], v[252:255]
s_waitcnt vmcnt(17) lgkmcnt(0)
s_barrier
v_mul_f32 v36, s47, v36
v_mfma_f32_16x16x32_bf16 v[128:131], v[96:99], v[48:51], v[128:131]
ds_read_b128 v[60:63], v25 offset:6144
v_mul_f32 v37, s47, v37
v_mfma_f32_16x16x32_bf16 v[132:135], v[100:103], v[48:51], v[132:135]
v_add_u32 v25, 32768, v25
v_cvt_pk_bf16_f32 v40, v36, v37
v_mfma_f32_16x16x32_bf16 v[136:139], v[104:107], v[48:51], v[136:139]
s_add_u32 m0, s92, 98304
ds_read_b64_tr_b16 v[64:65], v26 offset:0
ds_read_b64_tr_b16 v[66:67], v26 offset:1024
v_mfma_f32_16x16x32_bf16 v[140:143], v[108:111], v[48:51], v[140:143]
buffer_load_dwordx4 v20, s[12:15], s24 offen offset:0 lds
v_accvgpr_read_b32 v38, a194
v_mfma_f32_16x16x32_bf16 v[144:147], v[112:115], v[48:51], v[144:147]
ds_read_b64_tr_b16 v[68:69], v27 offset:0
ds_read_b64_tr_b16 v[70:71], v27 offset:1024
v_mfma_f32_16x16x32_bf16 v[148:151], v[116:119], v[48:51], v[148:151]
v_permlane32_swap_b32 v44, v46
v_mfma_f32_16x16x32_bf16 v[152:155], v[120:123], v[48:51], v[152:155]
ds_read_b64_tr_b16 v[72:73], v28 offset:0
ds_read_b64_tr_b16 v[74:75], v28 offset:1024
v_mfma_f32_16x16x32_bf16 v[156:159], v[124:127], v[48:51], v[156:159]
buffer_load_dwordx4 v21, s[12:15], s24 offen offset:1024 lds
v_accvgpr_read_b32 v39, a195
v_mfma_f32_16x16x32_bf16 v[160:163], v[96:99], v[52:55], v[160:163]
ds_read_b128 v[48:51], v24 offset:0
v_mul_f32 v38, s47, v38
v_mfma_f32_16x16x32_bf16 v[164:167], v[100:103], v[52:55], v[164:167]
v_permlane32_swap_b32 v45, v47
v_mfma_f32_16x16x32_bf16 v[168:171], v[104:107], v[52:55], v[168:171]
ds_read_b64_tr_b16 v[76:77], v29 offset:0
ds_read_b64_tr_b16 v[78:79], v29 offset:1024
v_mfma_f32_16x16x32_bf16 v[172:175], v[108:111], v[52:55], v[172:175]
s_xor_b32 s48, s95, 132096
buffer_load_dwordx4 v22, s[12:15], s24 offen offset:2048 lds
v_mfma_f32_16x16x32_bf16 v[176:179], v[112:115], v[52:55], v[176:179]
ds_read_b64_tr_b16 v[80:81], v30 offset:0
ds_read_b64_tr_b16 v[82:83], v30 offset:1024
v_mfma_f32_16x16x32_bf16 v[180:183], v[116:119], v[52:55], v[180:183]
buffer_store_dwordx4 v[44:47], v7, s[20:23], s48 offen sc1
v_mul_f32 v39, s47, v39
v_mfma_f32_16x16x32_bf16 v[184:187], v[120:123], v[52:55], v[184:187]
ds_read_b64_tr_b16 v[84:85], v31 offset:0
ds_read_b64_tr_b16 v[86:87], v31 offset:1024
v_mfma_f32_16x16x32_bf16 v[188:191], v[124:127], v[52:55], v[188:191]
buffer_load_dwordx4 v23, s[12:15], s24 offen offset:3072 lds
v_cvt_pk_bf16_f32 v41, v38, v39
v_mfma_f32_16x16x32_bf16 v[192:195], v[96:99], v[56:59], v[192:195]
ds_read_b128 v[52:55], v24 offset:2048
v_accvgpr_read_b32 v36, a196
v_mfma_f32_16x16x32_bf16 v[196:199], v[100:103], v[56:59], v[196:199]
v_accvgpr_read_b32 v37, a197
v_mul_f32 v36, s47, v36
v_mfma_f32_16x16x32_bf16 v[200:203], v[104:107], v[56:59], v[200:203]
s_add_u32 m0, s92, 102400
ds_read_b64_tr_b16 v[88:89], v32 offset:0
ds_read_b64_tr_b16 v[90:91], v32 offset:1024
v_mfma_f32_16x16x32_bf16 v[204:207], v[108:111], v[56:59], v[204:207]
buffer_load_dwordx4 v20, s[12:15], s25 offen offset:0 lds
v_mul_f32 v37, s47, v37
v_mfma_f32_16x16x32_bf16 v[208:211], v[112:115], v[56:59], v[208:211]
ds_read_b64_tr_b16 v[92:93], v33 offset:0
ds_read_b64_tr_b16 v[94:95], v33 offset:1024
v_mfma_f32_16x16x32_bf16 v[212:215], v[116:119], v[56:59], v[212:215]
v_cvt_pk_bf16_f32 v42, v36, v37
v_accvgpr_read_b32 v38, a198
v_mfma_f32_16x16x32_bf16 v[216:219], v[120:123], v[56:59], v[216:219]
s_waitcnt lgkmcnt(15)
v_accvgpr_read_b32 v39, a199
v_mul_f32 v38, s47, v38
v_mfma_f32_16x16x32_bf16 v[220:223], v[124:127], v[56:59], v[220:223]
v_mul_f32 v39, s47, v39
v_cvt_pk_bf16_f32 v43, v38, v39
v_mfma_f32_16x16x32_bf16 v[224:227], v[96:99], v[60:63], v[224:227]
ds_read_b128 v[56:59], v24 offset:4096
v_mfma_f32_16x16x32_bf16 v[228:231], v[100:103], v[60:63], v[228:231]
buffer_load_dwordx4 v21, s[12:15], s25 offen offset:1024 lds
v_mfma_f32_16x16x32_bf16 v[232:235], v[104:107], v[60:63], v[232:235]
v_permlane32_swap_b32 v40, v42
v_mfma_f32_16x16x32_bf16 v[236:239], v[108:111], v[60:63], v[236:239]
v_permlane32_swap_b32 v41, v43
v_mfma_f32_16x16x32_bf16 v[240:243], v[112:115], v[60:63], v[240:243]
v_accvgpr_write_b32 a192, v40
v_accvgpr_write_b32 a193, v41
v_mfma_f32_16x16x32_bf16 v[244:247], v[116:119], v[60:63], v[244:247]
v_accvgpr_write_b32 a194, v42
v_accvgpr_write_b32 a195, v43
v_mfma_f32_16x16x32_bf16 v[248:251], v[120:123], v[60:63], v[248:251]
v_accvgpr_read_b32 v36, a80
v_accvgpr_read_b32 v37, a81
v_mfma_f32_16x16x32_bf16 v[252:255], v[124:127], v[60:63], v[252:255]
s_waitcnt lgkmcnt(0)
v_mul_f32 v36, s47, v36
v_mul_f32 v37, s47, v37
v_mfma_f32_16x16x32_bf16 v[128:131], v[64:67], v[48:51], v[128:131]
ds_read_b128 v[60:63], v24 offset:6144
v_cvt_pk_bf16_f32 v44, v36, v37
v_mfma_f32_16x16x32_bf16 v[132:135], v[68:71], v[48:51], v[132:135]
v_add_u32 v24, 4294868992, v24
v_accvgpr_read_b32 v38, a82
v_mfma_f32_16x16x32_bf16 v[136:139], v[72:75], v[48:51], v[136:139]
ds_read_b64_tr_b16 v[96:97], v26 offset:8192
ds_read_b64_tr_b16 v[98:99], v26 offset:9216
v_mfma_f32_16x16x32_bf16 v[140:143], v[76:79], v[48:51], v[140:143]
s_lshl_b32 s28, s88, 19
buffer_load_dwordx4 v22, s[12:15], s25 offen offset:2048 lds
v_mfma_f32_16x16x32_bf16 v[144:147], v[80:83], v[48:51], v[144:147]
ds_read_b64_tr_b16 v[100:101], v27 offset:8192
ds_read_b64_tr_b16 v[102:103], v27 offset:9216
v_mfma_f32_16x16x32_bf16 v[148:151], v[84:87], v[48:51], v[148:151]
s_add_u32 s28, s28, s91
v_accvgpr_read_b32 v39, a83
v_mul_f32 v38, s47, v38
v_mfma_f32_16x16x32_bf16 v[152:155], v[88:91], v[48:51], v[152:155]
ds_read_b64_tr_b16 v[104:105], v28 offset:8192
ds_read_b64_tr_b16 v[106:107], v28 offset:9216
v_mfma_f32_16x16x32_bf16 v[156:159], v[92:95], v[48:51], v[156:159]
s_lshl_b32 s29, s89, 15
buffer_load_dwordx4 v23, s[12:15], s25 offen offset:3072 lds
v_mfma_f32_16x16x32_bf16 v[160:163], v[64:67], v[52:55], v[160:163]
s_sub_u32 s54, 0x80000, s29
ds_read_b128 v[48:51], v25 offset:0
v_mfma_f32_16x16x32_bf16 v[164:167], v[68:71], v[52:55], v[164:167]
s_add_u32 s24, s24, 128
s_add_u32 s25, s25, 128
v_mul_f32 v39, s47, v39
v_mfma_f32_16x16x32_bf16 v[168:171], v[72:75], v[52:55], v[168:171]
s_add_u32 m0, s91, 16384
ds_read_b64_tr_b16 v[108:109], v29 offset:8192
ds_read_b64_tr_b16 v[110:111], v29 offset:9216
v_mfma_f32_16x16x32_bf16 v[172:175], v[76:79], v[52:55], v[172:175]
s_sub_u32 s29, s54, s91
buffer_load_dwordx4 v16, s[16:19], s26 offen offset:0 lds
v_mfma_f32_16x16x32_bf16 v[176:179], v[80:83], v[52:55], v[176:179]
ds_read_b64_tr_b16 v[112:113], v30 offset:8192
ds_read_b64_tr_b16 v[114:115], v30 offset:9216
v_mfma_f32_16x16x32_bf16 v[180:183], v[84:87], v[52:55], v[180:183]
s_add_u32 s4, s84, s28
s_addc_u32 s5, s85, 0
v_cvt_pk_bf16_f32 v45, v38, v39
v_mfma_f32_16x16x32_bf16 v[184:187], v[88:91], v[52:55], v[184:187]
ds_read_b64_tr_b16 v[116:117], v31 offset:8192
ds_read_b64_tr_b16 v[118:119], v31 offset:9216
v_mfma_f32_16x16x32_bf16 v[188:191], v[92:95], v[52:55], v[188:191]
s_and_b32 s5, s5, 0xffff
buffer_load_dwordx4 v17, s[16:19], s26 offen offset:1024 lds
v_mfma_f32_16x16x32_bf16 v[192:195], v[64:67], v[56:59], v[192:195]
s_mov_b32 s6, s29
ds_read_b128 v[52:55], v25 offset:2048
v_mfma_f32_16x16x32_bf16 v[196:199], v[68:71], v[56:59], v[196:199]
s_mov_b32 s7, 0x20000
v_accvgpr_read_b32 v36, a84
v_accvgpr_read_b32 v37, a85
v_mfma_f32_16x16x32_bf16 v[200:203], v[72:75], v[56:59], v[200:203]
ds_read_b64_tr_b16 v[120:121], v32 offset:8192
ds_read_b64_tr_b16 v[122:123], v32 offset:9216
v_mfma_f32_16x16x32_bf16 v[204:207], v[76:79], v[56:59], v[204:207]
s_add_u32 s28, s28, 16760832
buffer_load_dwordx4 v18, s[16:19], s26 offen offset:2048 lds
v_mfma_f32_16x16x32_bf16 v[208:211], v[80:83], v[56:59], v[208:211]
ds_read_b64_tr_b16 v[124:125], v33 offset:8192
ds_read_b64_tr_b16 v[126:127], v33 offset:9216
v_mfma_f32_16x16x32_bf16 v[212:215], v[84:87], v[56:59], v[212:215]
s_add_u32 s29, s29, 16384
v_mul_f32 v36, s47, v36
v_mul_f32 v37, s47, v37
v_mfma_f32_16x16x32_bf16 v[216:219], v[88:91], v[56:59], v[216:219]
s_waitcnt lgkmcnt(15)
s_add_u32 s45, s54, 16384
v_cvt_pk_bf16_f32 v46, v36, v37
v_mfma_f32_16x16x32_bf16 v[220:223], v[92:95], v[56:59], v[220:223]
s_add_u32 s8, s84, s28
s_addc_u32 s9, s85, 0
v_accvgpr_read_b32 v38, a86
v_mfma_f32_16x16x32_bf16 v[224:227], v[64:67], v[60:63], v[224:227]
s_and_b32 s9, s9, 0xffff
ds_read_b128 v[56:59], v25 offset:4096
v_mfma_f32_16x16x32_bf16 v[228:231], v[68:71], v[60:63], v[228:231]
s_mov_b32 s10, s29
buffer_load_dwordx4 v19, s[16:19], s26 offen offset:3072 lds
v_mfma_f32_16x16x32_bf16 v[232:235], v[72:75], v[60:63], v[232:235]
s_mov_b32 s11, 0x20000
v_accvgpr_read_b32 v39, a87
v_mul_f32 v38, s47, v38
v_mfma_f32_16x16x32_bf16 v[236:239], v[76:79], v[60:63], v[236:239]
s_add_u32 s26, s26, 524288
v_mul_f32 v39, s47, v39
v_cvt_pk_bf16_f32 v47, v38, v39
v_mfma_f32_16x16x32_bf16 v[240:243], v[80:83], v[60:63], v[240:243]
s_and_b32 s46, s89, 1
v_permlane32_swap_b32 v44, v46
v_mfma_f32_16x16x32_bf16 v[244:247], v[84:87], v[60:63], v[244:247]
s_lshl_b32 s46, s46, 15
v_permlane32_swap_b32 v45, v47
v_mfma_f32_16x16x32_bf16 v[248:251], v[88:91], v[60:63], v[248:251]
s_add_u32 s46, s46, s91
s_xor_b32 s48, s95, 133120
v_accvgpr_read_b32 v36, a200
v_mfma_f32_16x16x32_bf16 v[252:255], v[92:95], v[60:63], v[252:255]
s_waitcnt vmcnt(17) lgkmcnt(0)
s_barrier
s_mov_b32 s39, s46
v_mfma_f32_16x16x32_bf16 v[128:131], v[96:99], v[48:51], v[128:131]
s_mov_b32 s27, 0x30000
ds_read_b128 v[60:63], v25 offset:6144
v_mfma_f32_16x16x32_bf16 v[132:135], v[100:103], v[48:51], v[132:135]
buffer_store_dwordx4 v[44:47], v7, s[20:23], s48 offen sc1
v_add_u32 v25, 4294868992, v25
v_mfma_f32_16x16x32_bf16 v[136:139], v[104:107], v[48:51], v[136:139]
s_add_u32 m0, s92, 131072
ds_read_b64_tr_b16 v[64:65], v26 offset:16384
ds_read_b64_tr_b16 v[66:67], v26 offset:17408
v_mfma_f32_16x16x32_bf16 v[140:143], v[108:111], v[48:51], v[140:143]
buffer_load_dwordx4 v20, s[12:15], s24 offen offset:0 lds
v_accvgpr_read_b32 v37, a201
v_mfma_f32_16x16x32_bf16 v[144:147], v[112:115], v[48:51], v[144:147]
ds_read_b64_tr_b16 v[68:69], v27 offset:16384
ds_read_b64_tr_b16 v[70:71], v27 offset:17408
v_mfma_f32_16x16x32_bf16 v[148:151], v[116:119], v[48:51], v[148:151]
v_mul_f32 v36, s47, v36
v_mul_f32 v37, s47, v37
v_mfma_f32_16x16x32_bf16 v[152:155], v[120:123], v[48:51], v[152:155]
ds_read_b64_tr_b16 v[72:73], v28 offset:16384
ds_read_b64_tr_b16 v[74:75], v28 offset:17408
v_mfma_f32_16x16x32_bf16 v[156:159], v[124:127], v[48:51], v[156:159]
buffer_load_dwordx4 v21, s[12:15], s24 offen offset:1024 lds
v_cvt_pk_bf16_f32 v40, v36, v37
v_mfma_f32_16x16x32_bf16 v[160:163], v[96:99], v[52:55], v[160:163]
ds_read_b128 v[48:51], v24 offset:0
v_accvgpr_read_b32 v38, a202
v_mfma_f32_16x16x32_bf16 v[164:167], v[100:103], v[52:55], v[164:167]
v_accvgpr_read_b32 v39, a203
v_mul_f32 v38, s47, v38
v_mfma_f32_16x16x32_bf16 v[168:171], v[104:107], v[52:55], v[168:171]
ds_read_b64_tr_b16 v[76:77], v29 offset:16384
ds_read_b64_tr_b16 v[78:79], v29 offset:17408
v_mfma_f32_16x16x32_bf16 v[172:175], v[108:111], v[52:55], v[172:175]
buffer_load_dwordx4 v22, s[12:15], s24 offen offset:2048 lds
v_mul_f32 v39, s47, v39
v_mfma_f32_16x16x32_bf16 v[176:179], v[112:115], v[52:55], v[176:179]
ds_read_b64_tr_b16 v[80:81], v30 offset:16384
ds_read_b64_tr_b16 v[82:83], v30 offset:17408
v_mfma_f32_16x16x32_bf16 v[180:183], v[116:119], v[52:55], v[180:183]
v_cvt_pk_bf16_f32 v41, v38, v39
v_accvgpr_read_b32 v36, a204
v_mfma_f32_16x16x32_bf16 v[184:187], v[120:123], v[52:55], v[184:187]
ds_read_b64_tr_b16 v[84:85], v31 offset:16384
ds_read_b64_tr_b16 v[86:87], v31 offset:17408
v_mfma_f32_16x16x32_bf16 v[188:191], v[124:127], v[52:55], v[188:191]
buffer_load_dwordx4 v23, s[12:15], s24 offen offset:3072 lds
v_accvgpr_read_b32 v37, a205
v_mfma_f32_16x16x32_bf16 v[192:195], v[96:99], v[56:59], v[192:195]
ds_read_b128 v[52:55], v24 offset:2048
v_mul_f32 v36, s47, v36
v_mfma_f32_16x16x32_bf16 v[196:199], v[100:103], v[56:59], v[196:199]
v_mul_f32 v37, s47, v37
v_cvt_pk_bf16_f32 v42, v36, v37
v_mfma_f32_16x16x32_bf16 v[200:203], v[104:107], v[56:59], v[200:203]
s_add_u32 m0, s92, 135168
ds_read_b64_tr_b16 v[88:89], v32 offset:16384
ds_read_b64_tr_b16 v[90:91], v32 offset:17408
v_mfma_f32_16x16x32_bf16 v[204:207], v[108:111], v[56:59], v[204:207]
buffer_load_dwordx4 v20, s[12:15], s25 offen offset:0 lds
v_accvgpr_read_b32 v38, a206
v_mfma_f32_16x16x32_bf16 v[208:211], v[112:115], v[56:59], v[208:211]
ds_read_b64_tr_b16 v[92:93], v33 offset:16384
ds_read_b64_tr_b16 v[94:95], v33 offset:17408
v_mfma_f32_16x16x32_bf16 v[212:215], v[116:119], v[56:59], v[212:215]
v_accvgpr_read_b32 v39, a207
v_mul_f32 v38, s47, v38
v_mfma_f32_16x16x32_bf16 v[216:219], v[120:123], v[56:59], v[216:219]
s_waitcnt lgkmcnt(15)
v_mul_f32 v39, s47, v39
v_cvt_pk_bf16_f32 v43, v38, v39
v_mfma_f32_16x16x32_bf16 v[220:223], v[124:127], v[56:59], v[220:223]
v_permlane32_swap_b32 v40, v42
v_mfma_f32_16x16x32_bf16 v[224:227], v[96:99], v[60:63], v[224:227]
ds_read_b128 v[56:59], v24 offset:4096
v_mfma_f32_16x16x32_bf16 v[228:231], v[100:103], v[60:63], v[228:231]
buffer_load_dwordx4 v21, s[12:15], s25 offen offset:1024 lds
v_mfma_f32_16x16x32_bf16 v[232:235], v[104:107], v[60:63], v[232:235]
v_permlane32_swap_b32 v41, v43
v_mfma_f32_16x16x32_bf16 v[236:239], v[108:111], v[60:63], v[236:239]
v_accvgpr_write_b32 a196, v40
.Lpad7:
s_cbranch_execz .Ltch2
v_accvgpr_write_b32 a197, v41
v_mfma_f32_16x16x32_bf16 v[240:243], v[112:115], v[60:63], v[240:243]
v_accvgpr_write_b32 a198, v42
v_accvgpr_write_b32 a199, v43
v_mfma_f32_16x16x32_bf16 v[244:247], v[116:119], v[60:63], v[244:247]
v_accvgpr_read_b32 v36, a88
v_accvgpr_read_b32 v37, a89
v_mfma_f32_16x16x32_bf16 v[248:251], v[120:123], v[60:63], v[248:251]
v_mul_f32 v36, s47, v36
v_mul_f32 v37, s47, v37
v_mfma_f32_16x16x32_bf16 v[252:255], v[124:127], v[60:63], v[252:255]
s_waitcnt lgkmcnt(0)
v_cvt_pk_bf16_f32 v44, v36, v37
v_accvgpr_read_b32 v38, a90
v_mfma_f32_16x16x32_bf16 v[128:131], v[64:67], v[48:51], v[128:131]
ds_read_b128 v[60:63], v24 offset:6144
v_accvgpr_read_b32 v39, a91
v_mfma_f32_16x16x32_bf16 v[132:135], v[68:71], v[48:51], v[132:135]
v_add_u32 v24, 65536, v24
v_mul_f32 v38, s47, v38
v_mfma_f32_16x16x32_bf16 v[136:139], v[72:75], v[48:51], v[136:139]
ds_read_b64_tr_b16 v[96:97], v26 offset:24576
ds_read_b64_tr_b16 v[98:99], v26 offset:25600
v_mfma_f32_16x16x32_bf16 v[140:143], v[76:79], v[48:51], v[140:143]
buffer_load_dwordx4 v22, s[12:15], s25 offen offset:2048 lds
v_mul_f32 v39, s47, v39
v_mfma_f32_16x16x32_bf16 v[144:147], v[80:83], v[48:51], v[144:147]
ds_read_b64_tr_b16 v[100:101], v27 offset:24576
ds_read_b64_tr_b16 v[102:103], v27 offset:25600
v_mfma_f32_16x16x32_bf16 v[148:151], v[84:87], v[48:51], v[148:151]
v_cvt_pk_bf16_f32 v45, v38, v39
v_accvgpr_read_b32 v36, a92
v_mfma_f32_16x16x32_bf16 v[152:155], v[88:91], v[48:51], v[152:155]
ds_read_b64_tr_b16 v[104:105], v28 offset:24576
ds_read_b64_tr_b16 v[106:107], v28 offset:25600
v_mfma_f32_16x16x32_bf16 v[156:159], v[92:95], v[48:51], v[156:159]
buffer_load_dwordx4 v23, s[12:15], s25 offen offset:3072 lds
v_accvgpr_read_b32 v37, a93
v_mfma_f32_16x16x32_bf16 v[160:163], v[64:67], v[52:55], v[160:163]
ds_read_b128 v[48:51], v25 offset:0
v_mul_f32 v36, s47, v36
v_mfma_f32_16x16x32_bf16 v[164:167], v[68:71], v[52:55], v[164:167]
s_add_u32 s24, s24, 128
s_add_u32 s25, s25, 128
buffer_load_dword v35, v4, s[96:99], 0 offen
v_mfma_f32_16x16x32_bf16 v[168:171], v[72:75], v[52:55], v[168:171]
s_add_u32 m0, s91, 65536
ds_read_b64_tr_b16 v[108:109], v29 offset:24576
ds_read_b64_tr_b16 v[110:111], v29 offset:25600
v_mfma_f32_16x16x32_bf16 v[172:175], v[76:79], v[52:55], v[172:175]
buffer_load_dwordx4 v16, s[16:19], s26 offen offset:0 lds
v_mul_f32 v37, s47, v37
v_mfma_f32_16x16x32_bf16 v[176:179], v[80:83], v[52:55], v[176:179]
ds_read_b64_tr_b16 v[112:113], v30 offset:24576
ds_read_b64_tr_b16 v[114:115], v30 offset:25600
v_mfma_f32_16x16x32_bf16 v[180:183], v[84:87], v[52:55], v[180:183]
v_cvt_pk_bf16_f32 v46, v36, v37
v_accvgpr_read_b32 v38, a94
v_mfma_f32_16x16x32_bf16 v[184:187], v[88:91], v[52:55], v[184:187]
ds_read_b64_tr_b16 v[116:117], v31 offset:24576
ds_read_b64_tr_b16 v[118:119], v31 offset:25600
v_mfma_f32_16x16x32_bf16 v[188:191], v[92:95], v[52:55], v[188:191]
buffer_load_dwordx4 v17, s[16:19], s26 offen offset:1024 lds
v_accvgpr_read_b32 v39, a95
v_mfma_f32_16x16x32_bf16 v[192:195], v[64:67], v[56:59], v[192:195]
ds_read_b128 v[52:55], v25 offset:2048
v_mul_f32 v38, s47, v38
v_mfma_f32_16x16x32_bf16 v[196:199], v[68:71], v[56:59], v[196:199]
v_mul_f32 v39, s47, v39
v_cvt_pk_bf16_f32 v47, v38, v39
v_mfma_f32_16x16x32_bf16 v[200:203], v[72:75], v[56:59], v[200:203]
ds_read_b64_tr_b16 v[120:121], v32 offset:24576
ds_read_b64_tr_b16 v[122:123], v32 offset:25600
v_mfma_f32_16x16x32_bf16 v[204:207], v[76:79], v[56:59], v[204:207]
buffer_load_dwordx4 v18, s[16:19], s26 offen offset:2048 lds
v_mfma_f32_16x16x32_bf16 v[208:211], v[80:83], v[56:59], v[208:211]
ds_read_b64_tr_b16 v[124:125], v33 offset:24576
ds_read_b64_tr_b16 v[126:127], v33 offset:25600
v_mfma_f32_16x16x32_bf16 v[212:215], v[84:87], v[56:59], v[212:215]
v_permlane32_swap_b32 v44, v46
v_mfma_f32_16x16x32_bf16 v[216:219], v[88:91], v[56:59], v[216:219]
s_waitcnt lgkmcnt(15)
v_permlane32_swap_b32 v45, v47
v_mfma_f32_16x16x32_bf16 v[220:223], v[92:95], v[56:59], v[220:223]
s_xor_b32 s48, s95, 134144
v_xor_b32 v26, 0x10000, v26
v_mfma_f32_16x16x32_bf16 v[224:227], v[64:67], v[60:63], v[224:227]
ds_read_b128 v[56:59], v25 offset:4096
v_xor_b32 v27, 0x10000, v27
v_mfma_f32_16x16x32_bf16 v[228:231], v[68:71], v[60:63], v[228:231]
buffer_load_dwordx4 v19, s[16:19], s26 offen offset:3072 lds
v_xor_b32 v28, 0x10000, v28
v_mfma_f32_16x16x32_bf16 v[232:235], v[72:75], v[60:63], v[232:235]
buffer_store_dwordx4 v[44:47], v7, s[20:23], s48 offen sc1
v_xor_b32 v29, 0x10000, v29
v_mfma_f32_16x16x32_bf16 v[236:239], v[76:79], v[60:63], v[236:239]
s_add_u32 s26, s26, 524288
v_xor_b32 v30, 0x10000, v30
v_accvgpr_read_b32 v36, a96
v_mfma_f32_16x16x32_bf16 v[240:243], v[80:83], v[60:63], v[240:243]
v_xor_b32 v31, 0x10000, v31
v_accvgpr_read_b32 v37, a97
v_mfma_f32_16x16x32_bf16 v[244:247], v[84:87], v[60:63], v[244:247]
v_xor_b32 v32, 0x10000, v32
v_mul_f32 v36, s47, v36
v_mfma_f32_16x16x32_bf16 v[248:251], v[88:91], v[60:63], v[248:251]
v_xor_b32 v33, 0x10000, v33
v_mul_f32 v37, s47, v37
v_mfma_f32_16x16x32_bf16 v[252:255], v[92:95], v[60:63], v[252:255]
s_waitcnt vmcnt(19) lgkmcnt(0)
s_barrier
v_cvt_pk_bf16_f32 v40, v36, v37
v_mfma_f32_16x16x32_bf16 v[128:131], v[96:99], v[48:51], v[128:131]
ds_read_b128 v[60:63], v25 offset:6144
v_accvgpr_read_b32 v38, a98
v_mfma_f32_16x16x32_bf16 v[132:135], v[100:103], v[48:51], v[132:135]
v_add_u32 v25, 65536, v25
v_accvgpr_read_b32 v39, a99
v_mfma_f32_16x16x32_bf16 v[136:139], v[104:107], v[48:51], v[136:139]
s_add_u32 m0, s92, 32768
ds_read_b64_tr_b16 v[64:65], v26 offset:0
ds_read_b64_tr_b16 v[66:67], v26 offset:1024
v_mfma_f32_16x16x32_bf16 v[140:143], v[108:111], v[48:51], v[140:143]
buffer_load_dwordx4 v20, s[12:15], s24 offen offset:0 lds
v_mul_f32 v38, s47, v38
v_mfma_f32_16x16x32_bf16 v[144:147], v[112:115], v[48:51], v[144:147]
ds_read_b64_tr_b16 v[68:69], v27 offset:0
ds_read_b64_tr_b16 v[70:71], v27 offset:1024
v_mfma_f32_16x16x32_bf16 v[148:151], v[116:119], v[48:51], v[148:151]
v_mul_f32 v39, s47, v39
v_cvt_pk_bf16_f32 v41, v38, v39
v_mfma_f32_16x16x32_bf16 v[152:155], v[120:123], v[48:51], v[152:155]
ds_read_b64_tr_b16 v[72:73], v28 offset:0
ds_read_b64_tr_b16 v[74:75], v28 offset:1024
v_mfma_f32_16x16x32_bf16 v[156:159], v[124:127], v[48:51], v[156:159]
buffer_load_dwordx4 v21, s[12:15], s24 offen offset:1024 lds
v_accvgpr_read_b32 v36, a100
v_mfma_f32_16x16x32_bf16 v[160:163], v[96:99], v[52:55], v[160:163]
ds_read_b128 v[48:51], v24 offset:0
v_accvgpr_read_b32 v37, a101
v_mfma_f32_16x16x32_bf16 v[164:167], v[100:103], v[52:55], v[164:167]
v_mul_f32 v36, s47, v36
v_mul_f32 v37, s47, v37
v_mfma_f32_16x16x32_bf16 v[168:171], v[104:107], v[52:55], v[168:171]
ds_read_b64_tr_b16 v[76:77], v29 offset:0
ds_read_b64_tr_b16 v[78:79], v29 offset:1024
v_mfma_f32_16x16x32_bf16 v[172:175], v[108:111], v[52:55], v[172:175]
buffer_load_dwordx4 v22, s[12:15], s24 offen offset:2048 lds
v_cvt_pk_bf16_f32 v42, v36, v37
v_mfma_f32_16x16x32_bf16 v[176:179], v[112:115], v[52:55], v[176:179]
ds_read_b64_tr_b16 v[80:81], v30 offset:0
ds_read_b64_tr_b16 v[82:83], v30 offset:1024
v_mfma_f32_16x16x32_bf16 v[180:183], v[116:119], v[52:55], v[180:183]
v_accvgpr_read_b32 v38, a102
v_accvgpr_read_b32 v39, a103
v_mfma_f32_16x16x32_bf16 v[184:187], v[120:123], v[52:55], v[184:187]
ds_read_b64_tr_b16 v[84:85], v31 offset:0
ds_read_b64_tr_b16 v[86:87], v31 offset:1024
v_mfma_f32_16x16x32_bf16 v[188:191], v[124:127], v[52:55], v[188:191]
buffer_load_dwordx4 v23, s[12:15], s24 offen offset:3072 lds
v_mul_f32 v38, s47, v38
v_mfma_f32_16x16x32_bf16 v[192:195], v[96:99], v[56:59], v[192:195]
ds_read_b128 v[52:55], v24 offset:2048
v_mul_f32 v39, s47, v39
v_mfma_f32_16x16x32_bf16 v[196:199], v[100:103], v[56:59], v[196:199]
v_cvt_pk_bf16_f32 v43, v38, v39
v_mfma_f32_16x16x32_bf16 v[200:203], v[104:107], v[56:59], v[200:203]
s_add_u32 m0, s92, 36864
ds_read_b64_tr_b16 v[88:89], v32 offset:0
ds_read_b64_tr_b16 v[90:91], v32 offset:1024
v_mfma_f32_16x16x32_bf16 v[204:207], v[108:111], v[56:59], v[204:207]
buffer_load_dwordx4 v20, s[12:15], s25 offen offset:0 lds
v_accvgpr_read_b32 v36, a208
v_mfma_f32_16x16x32_bf16 v[208:211], v[112:115], v[56:59], v[208:211]
ds_read_b64_tr_b16 v[92:93], v33 offset:0
ds_read_b64_tr_b16 v[94:95], v33 offset:1024
v_mfma_f32_16x16x32_bf16 v[212:215], v[116:119], v[56:59], v[212:215]
v_permlane32_swap_b32 v40, v42
v_mfma_f32_16x16x32_bf16 v[216:219], v[120:123], v[56:59], v[216:219]
s_waitcnt lgkmcnt(15)
v_permlane32_swap_b32 v41, v43
v_mfma_f32_16x16x32_bf16 v[220:223], v[124:127], v[56:59], v[220:223]
s_xor_b32 s48, s95, 196608
buffer_store_dwordx4 v[40:43], v7, s[20:23], s48 offen sc1
v_mfma_f32_16x16x32_bf16 v[224:227], v[96:99], v[60:63], v[224:227]
ds_read_b128 v[56:59], v24 offset:4096
v_accvgpr_read_b32 v37, a209
v_mfma_f32_16x16x32_bf16 v[228:231], v[100:103], v[60:63], v[228:231]
buffer_load_dwordx4 v21, s[12:15], s25 offen offset:1024 lds
v_mul_f32 v36, s47, v36
v_mfma_f32_16x16x32_bf16 v[232:235], v[104:107], v[60:63], v[232:235]
v_mul_f32 v37, s47, v37
v_cvt_pk_bf16_f32 v44, v36, v37
v_mfma_f32_16x16x32_bf16 v[236:239], v[108:111], v[60:63], v[236:239]
v_accvgpr_read_b32 v38, a210
v_accvgpr_read_b32 v39, a211
v_mfma_f32_16x16x32_bf16 v[240:243], v[112:115], v[60:63], v[240:243]
v_mul_f32 v38, s47, v38
v_mul_f32 v39, s47, v39
v_mfma_f32_16x16x32_bf16 v[244:247], v[116:119], v[60:63], v[244:247]
v_cvt_pk_bf16_f32 v45, v38, v39
v_accvgpr_read_b32 v36, a212
v_mfma_f32_16x16x32_bf16 v[248:251], v[120:123], v[60:63], v[248:251]
v_accvgpr_read_b32 v37, a213
v_mul_f32 v36, s47, v36
v_mfma_f32_16x16x32_bf16 v[252:255], v[124:127], v[60:63], v[252:255]
s_waitcnt lgkmcnt(0)
v_mul_f32 v37, s47, v37
v_cvt_pk_bf16_f32 v46, v36, v37
v_mfma_f32_16x16x32_bf16 v[128:131], v[64:67], v[48:51], v[128:131]
ds_read_b128 v[60:63], v24 offset:6144
v_accvgpr_read_b32 v38, a214
v_mfma_f32_16x16x32_bf16 v[132:135], v[68:71], v[48:51], v[132:135]
v_add_u32 v24, 32768, v24
v_accvgpr_read_b32 v39, a215
v_mfma_f32_16x16x32_bf16 v[136:139], v[72:75], v[48:51], v[136:139]
ds_read_b64_tr_b16 v[96:97], v26 offset:8192
ds_read_b64_tr_b16 v[98:99], v26 offset:9216
v_mfma_f32_16x16x32_bf16 v[140:143], v[76:79], v[48:51], v[140:143]
buffer_load_dwordx4 v22, s[12:15], s25 offen offset:2048 lds
v_mul_f32 v38, s47, v38
v_mfma_f32_16x16x32_bf16 v[144:147], v[80:83], v[48:51], v[144:147]
ds_read_b64_tr_b16 v[100:101], v27 offset:8192
ds_read_b64_tr_b16 v[102:103], v27 offset:9216
v_mfma_f32_16x16x32_bf16 v[148:151], v[84:87], v[48:51], v[148:151]
v_mul_f32 v39, s47, v39
v_cvt_pk_bf16_f32 v47, v38, v39
v_mfma_f32_16x16x32_bf16 v[152:155], v[88:91], v[48:51], v[152:155]
ds_read_b64_tr_b16 v[104:105], v28 offset:8192
ds_read_b64_tr_b16 v[106:107], v28 offset:9216
v_mfma_f32_16x16x32_bf16 v[156:159], v[92:95], v[48:51], v[156:159]
buffer_load_dwordx4 v23, s[12:15], s25 offen offset:3072 lds
v_mfma_f32_16x16x32_bf16 v[160:163], v[64:67], v[52:55], v[160:163]
ds_read_b128 v[48:51], v25 offset:0
v_mfma_f32_16x16x32_bf16 v[164:167], v[68:71], v[52:55], v[164:167]
s_add_u32 s24, s24, 128
s_add_u32 s25, s25, 128
v_mfma_f32_16x16x32_bf16 v[168:171], v[72:75], v[52:55], v[168:171]
s_add_u32 m0, s91, 81920
ds_read_b64_tr_b16 v[108:109], v29 offset:8192
ds_read_b64_tr_b16 v[110:111], v29 offset:9216
v_mfma_f32_16x16x32_bf16 v[172:175], v[76:79], v[52:55], v[172:175]
buffer_load_dwordx4 v16, s[16:19], s26 offen offset:0 lds
v_mfma_f32_16x16x32_bf16 v[176:179], v[80:83], v[52:55], v[176:179]
ds_read_b64_tr_b16 v[112:113], v30 offset:8192
ds_read_b64_tr_b16 v[114:115], v30 offset:9216
v_mfma_f32_16x16x32_bf16 v[180:183], v[84:87], v[52:55], v[180:183]
v_permlane32_swap_b32 v44, v46
v_mfma_f32_16x16x32_bf16 v[184:187], v[88:91], v[52:55], v[184:187]
ds_read_b64_tr_b16 v[116:117], v31 offset:8192
ds_read_b64_tr_b16 v[118:119], v31 offset:9216
v_mfma_f32_16x16x32_bf16 v[188:191], v[92:95], v[52:55], v[188:191]
buffer_load_dwordx4 v17, s[16:19], s26 offen offset:1024 lds
v_mfma_f32_16x16x32_bf16 v[192:195], v[64:67], v[56:59], v[192:195]
ds_read_b128 v[52:55], v25 offset:2048
v_mfma_f32_16x16x32_bf16 v[196:199], v[68:71], v[56:59], v[196:199]
v_permlane32_swap_b32 v45, v47
v_mfma_f32_16x16x32_bf16 v[200:203], v[72:75], v[56:59], v[200:203]
ds_read_b64_tr_b16 v[120:121], v32 offset:8192
ds_read_b64_tr_b16 v[122:123], v32 offset:9216
v_mfma_f32_16x16x32_bf16 v[204:207], v[76:79], v[56:59], v[204:207]
buffer_load_dwordx4 v18, s[16:19], s26 offen offset:2048 lds
v_accvgpr_write_b32 a200, v44
v_mfma_f32_16x16x32_bf16 v[208:211], v[80:83], v[56:59], v[208:211]
ds_read_b64_tr_b16 v[124:125], v33 offset:8192
ds_read_b64_tr_b16 v[126:127], v33 offset:9216
v_mfma_f32_16x16x32_bf16 v[212:215], v[84:87], v[56:59], v[212:215]
v_accvgpr_write_b32 a201, v45
v_accvgpr_write_b32 a202, v46
v_mfma_f32_16x16x32_bf16 v[216:219], v[88:91], v[56:59], v[216:219]
s_waitcnt lgkmcnt(15)
v_accvgpr_write_b32 a203, v47
v_accvgpr_read_b32 v36, a104
v_mfma_f32_16x16x32_bf16 v[220:223], v[92:95], v[56:59], v[220:223]
v_accvgpr_read_b32 v37, a105
v_mul_f32 v36, s47, v36
v_mfma_f32_16x16x32_bf16 v[224:227], v[64:67], v[60:63], v[224:227]
ds_read_b128 v[56:59], v25 offset:4096
v_mul_f32 v37, s47, v37
v_mfma_f32_16x16x32_bf16 v[228:231], v[68:71], v[60:63], v[228:231]
buffer_load_dwordx4 v19, s[16:19], s26 offen offset:3072 lds
v_cvt_pk_bf16_f32 v40, v36, v37
v_mfma_f32_16x16x32_bf16 v[232:235], v[72:75], v[60:63], v[232:235]
v_accvgpr_read_b32 v38, a106
v_accvgpr_read_b32 v39, a107
v_mfma_f32_16x16x32_bf16 v[236:239], v[76:79], v[60:63], v[236:239]
s_add_u32 s26, s26, 524288
v_mul_f32 v38, s47, v38
v_mul_f32 v39, s47, v39
v_mfma_f32_16x16x32_bf16 v[240:243], v[80:83], v[60:63], v[240:243]
v_cvt_pk_bf16_f32 v41, v38, v39
v_accvgpr_read_b32 v36, a108
v_mfma_f32_16x16x32_bf16 v[244:247], v[84:87], v[60:63], v[244:247]
v_accvgpr_read_b32 v37, a109
v_mul_f32 v36, s47, v36
v_mfma_f32_16x16x32_bf16 v[248:251], v[88:91], v[60:63], v[248:251]
v_mul_f32 v37, s47, v37
v_cvt_pk_bf16_f32 v42, v36, v37
v_mfma_f32_16x16x32_bf16 v[252:255], v[92:95], v[60:63], v[252:255]
s_waitcnt vmcnt(19) lgkmcnt(0)
s_barrier
v_accvgpr_read_b32 v38, a110
v_mfma_f32_16x16x32_bf16 v[128:131], v[96:99], v[48:51], v[128:131]
ds_read_b128 v[60:63], v25 offset:6144
v_accvgpr_read_b32 v39, a111
v_mfma_f32_16x16x32_bf16 v[132:135], v[100:103], v[48:51], v[132:135]
v_add_u32 v25, 32768, v25
v_mul_f32 v38, s47, v38
v_mfma_f32_16x16x32_bf16 v[136:139], v[104:107], v[48:51], v[136:139]
s_add_u32 m0, s92, 98304
ds_read_b64_tr_b16 v[64:65], v26 offset:16384
ds_read_b64_tr_b16 v[66:67], v26 offset:17408
v_mfma_f32_16x16x32_bf16 v[140:143], v[108:111], v[48:51], v[140:143]
buffer_load_dwordx4 v20, s[12:15], s24 offen offset:0 lds
v_mul_f32 v39, s47, v39
v_mfma_f32_16x16x32_bf16 v[144:147], v[112:115], v[48:51], v[144:147]
ds_read_b64_tr_b16 v[68:69], v27 offset:16384
ds_read_b64_tr_b16 v[70:71], v27 offset:17408
v_mfma_f32_16x16x32_bf16 v[148:151], v[116:119], v[48:51], v[148:151]
v_cvt_pk_bf16_f32 v43, v38, v39
v_mfma_f32_16x16x32_bf16 v[152:155], v[120:123], v[48:51], v[152:155]
ds_read_b64_tr_b16 v[72:73], v28 offset:16384
ds_read_b64_tr_b16 v[74:75], v28 offset:17408
v_mfma_f32_16x16x32_bf16 v[156:159], v[124:127], v[48:51], v[156:159]
buffer_load_dwordx4 v21, s[12:15], s24 offen offset:1024 lds
v_accvgpr_read_b32 v36, a216
v_mfma_f32_16x16x32_bf16 v[160:163], v[96:99], v[52:55], v[160:163]
ds_read_b128 v[48:51], v24 offset:0
v_accvgpr_read_b32 v37, a217
v_mfma_f32_16x16x32_bf16 v[164:167], v[100:103], v[52:55], v[164:167]
v_permlane32_swap_b32 v40, v42
v_mfma_f32_16x16x32_bf16 v[168:171], v[104:107], v[52:55], v[168:171]
ds_read_b64_tr_b16 v[76:77], v29 offset:16384
ds_read_b64_tr_b16 v[78:79], v29 offset:17408
v_mfma_f32_16x16x32_bf16 v[172:175], v[108:111], v[52:55], v[172:175]
buffer_load_dwordx4 v22, s[12:15], s24 offen offset:2048 lds
v_mul_f32 v36, s47, v36
v_mfma_f32_16x16x32_bf16 v[176:179], v[112:115], v[52:55], v[176:179]
ds_read_b64_tr_b16 v[80:81], v30 offset:16384
ds_read_b64_tr_b16 v[82:83], v30 offset:17408
v_mfma_f32_16x16x32_bf16 v[180:183], v[116:119], v[52:55], v[180:183]
v_permlane32_swap_b32 v41, v43
v_mfma_f32_16x16x32_bf16 v[184:187], v[120:123], v[52:55], v[184:187]
ds_read_b64_tr_b16 v[84:85], v31 offset:16384
ds_read_b64_tr_b16 v[86:87], v31 offset:17408
v_mfma_f32_16x16x32_bf16 v[188:191], v[124:127], v[52:55], v[188:191]
s_xor_b32 s48, s95, 197632
buffer_load_dwordx4 v23, s[12:15], s24 offen offset:3072 lds
v_mfma_f32_16x16x32_bf16 v[192:195], v[96:99], v[56:59], v[192:195]
ds_read_b128 v[52:55], v24 offset:2048
v_mul_f32 v37, s47, v37
v_mfma_f32_16x16x32_bf16 v[196:199], v[100:103], v[56:59], v[196:199]
buffer_store_dwordx4 v[40:43], v7, s[20:23], s48 offen sc1
v_cvt_pk_bf16_f32 v44, v36, v37
v_mfma_f32_16x16x32_bf16 v[200:203], v[104:107], v[56:59], v[200:203]
s_add_u32 m0, s92, 102400
ds_read_b64_tr_b16 v[88:89], v32 offset:16384
ds_read_b64_tr_b16 v[90:91], v32 offset:17408
v_mfma_f32_16x16x32_bf16 v[204:207], v[108:111], v[56:59], v[204:207]
buffer_load_dwordx4 v20, s[12:15], s25 offen offset:0 lds
v_accvgpr_read_b32 v38, a218
v_mfma_f32_16x16x32_bf16 v[208:211], v[112:115], v[56:59], v[208:211]
ds_read_b64_tr_b16 v[92:93], v33 offset:16384
ds_read_b64_tr_b16 v[94:95], v33 offset:17408
v_mfma_f32_16x16x32_bf16 v[212:215], v[116:119], v[56:59], v[212:215]
v_accvgpr_read_b32 v39, a219
v_mul_f32 v38, s47, v38
v_mfma_f32_16x16x32_bf16 v[216:219], v[120:123], v[56:59], v[216:219]
s_waitcnt lgkmcnt(15)
v_mul_f32 v39, s47, v39
v_cvt_pk_bf16_f32 v45, v38, v39
v_mfma_f32_16x16x32_bf16 v[220:223], v[124:127], v[56:59], v[220:223]
v_accvgpr_read_b32 v36, a220
v_accvgpr_read_b32 v37, a221
v_mfma_f32_16x16x32_bf16 v[224:227], v[96:99], v[60:63], v[224:227]
ds_read_b128 v[56:59], v24 offset:4096
v_mul_f32 v36, s47, v36
v_mfma_f32_16x16x32_bf16 v[228:231], v[100:103], v[60:63], v[228:231]
buffer_load_dwordx4 v21, s[12:15], s25 offen offset:1024 lds
v_mul_f32 v37, s47, v37
v_mfma_f32_16x16x32_bf16 v[232:235], v[104:107], v[60:63], v[232:235]
v_cvt_pk_bf16_f32 v46, v36, v37
v_accvgpr_read_b32 v38, a222
v_mfma_f32_16x16x32_bf16 v[236:239], v[108:111], v[60:63], v[236:239]
v_accvgpr_read_b32 v39, a223
v_mul_f32 v38, s47, v38
v_mfma_f32_16x16x32_bf16 v[240:243], v[112:115], v[60:63], v[240:243]
v_mul_f32 v39, s47, v39
v_cvt_pk_bf16_f32 v47, v38, v39
v_mfma_f32_16x16x32_bf16 v[244:247], v[116:119], v[60:63], v[244:247]
v_permlane32_swap_b32 v44, v46
v_mfma_f32_16x16x32_bf16 v[248:251], v[120:123], v[60:63], v[248:251]
v_permlane32_swap_b32 v45, v47
v_mfma_f32_16x16x32_bf16 v[252:255], v[124:127], v[60:63], v[252:255]
s_waitcnt lgkmcnt(0)
v_accvgpr_write_b32 a204, v44
v_accvgpr_write_b32 a205, v45
v_mfma_f32_16x16x32_bf16 v[128:131], v[64:67], v[48:51], v[128:131]
ds_read_b128 v[60:63], v24 offset:6144
v_accvgpr_write_b32 a206, v46
v_mfma_f32_16x16x32_bf16 v[132:135], v[68:71], v[48:51], v[132:135]
v_add_u32 v24, 4294868992, v24
v_accvgpr_write_b32 a207, v47
v_mfma_f32_16x16x32_bf16 v[136:139], v[72:75], v[48:51], v[136:139]
ds_read_b64_tr_b16 v[96:97], v26 offset:24576
ds_read_b64_tr_b16 v[98:99], v26 offset:25600
v_mfma_f32_16x16x32_bf16 v[140:143], v[76:79], v[48:51], v[140:143]
buffer_load_dwordx4 v22, s[12:15], s25 offen offset:2048 lds
v_accvgpr_read_b32 v36, a112
v_mfma_f32_16x16x32_bf16 v[144:147], v[80:83], v[48:51], v[144:147]
ds_read_b64_tr_b16 v[100:101], v27 offset:24576
ds_read_b64_tr_b16 v[102:103], v27 offset:25600
v_mfma_f32_16x16x32_bf16 v[148:151], v[84:87], v[48:51], v[148:151]
v_accvgpr_read_b32 v37, a113
v_mul_f32 v36, s47, v36
v_mfma_f32_16x16x32_bf16 v[152:155], v[88:91], v[48:51], v[152:155]
ds_read_b64_tr_b16 v[104:105], v28 offset:24576
ds_read_b64_tr_b16 v[106:107], v28 offset:25600
v_mfma_f32_16x16x32_bf16 v[156:159], v[92:95], v[48:51], v[156:159]
buffer_load_dwordx4 v23, s[12:15], s25 offen offset:3072 lds
v_mul_f32 v37, s47, v37
v_mfma_f32_16x16x32_bf16 v[160:163], v[64:67], v[52:55], v[160:163]
ds_read_b128 v[48:51], v25 offset:0
v_cvt_pk_bf16_f32 v40, v36, v37
v_mfma_f32_16x16x32_bf16 v[164:167], v[68:71], v[52:55], v[164:167]
s_add_u32 s24, s24, 128
s_add_u32 s25, s25, 128
v_accvgpr_read_b32 v38, a114
v_mfma_f32_16x16x32_bf16 v[168:171], v[72:75], v[52:55], v[168:171]
s_add_u32 m0, s91, 0
ds_read_b64_tr_b16 v[108:109], v29 offset:24576
ds_read_b64_tr_b16 v[110:111], v29 offset:25600
v_mfma_f32_16x16x32_bf16 v[172:175], v[76:79], v[52:55], v[172:175]
buffer_load_dwordx4 v16, s[16:19], s26 offen offset:0 lds
v_accvgpr_read_b32 v39, a115
v_mfma_f32_16x16x32_bf16 v[176:179], v[80:83], v[52:55], v[176:179]
ds_read_b64_tr_b16 v[112:113], v30 offset:24576
ds_read_b64_tr_b16 v[114:115], v30 offset:25600
v_mfma_f32_16x16x32_bf16 v[180:183], v[84:87], v[52:55], v[180:183]
v_mul_f32 v38, s47, v38
v_mul_f32 v39, s47, v39
v_mfma_f32_16x16x32_bf16 v[184:187], v[88:91], v[52:55], v[184:187]
ds_read_b64_tr_b16 v[116:117], v31 offset:24576
ds_read_b64_tr_b16 v[118:119], v31 offset:25600
v_mfma_f32_16x16x32_bf16 v[188:191], v[92:95], v[52:55], v[188:191]
buffer_load_dwordx4 v17, s[16:19], s26 offen offset:1024 lds
v_cvt_pk_bf16_f32 v41, v38, v39
v_mfma_f32_16x16x32_bf16 v[192:195], v[64:67], v[56:59], v[192:195]
ds_read_b128 v[52:55], v25 offset:2048
v_accvgpr_read_b32 v36, a116
v_mfma_f32_16x16x32_bf16 v[196:199], v[68:71], v[56:59], v[196:199]
v_accvgpr_read_b32 v37, a117
v_mul_f32 v36, s47, v36
v_mfma_f32_16x16x32_bf16 v[200:203], v[72:75], v[56:59], v[200:203]
ds_read_b64_tr_b16 v[120:121], v32 offset:24576
ds_read_b64_tr_b16 v[122:123], v32 offset:25600
v_mfma_f32_16x16x32_bf16 v[204:207], v[76:79], v[56:59], v[204:207]
buffer_load_dwordx4 v18, s[16:19], s26 offen offset:2048 lds
v_mul_f32 v37, s47, v37
v_mfma_f32_16x16x32_bf16 v[208:211], v[80:83], v[56:59], v[208:211]
ds_read_b64_tr_b16 v[124:125], v33 offset:24576
ds_read_b64_tr_b16 v[126:127], v33 offset:25600
v_mfma_f32_16x16x32_bf16 v[212:215], v[84:87], v[56:59], v[212:215]
v_cvt_pk_bf16_f32 v42, v36, v37
v_accvgpr_read_b32 v38, a118
v_mfma_f32_16x16x32_bf16 v[216:219], v[88:91], v[56:59], v[216:219]
s_waitcnt lgkmcnt(15)
v_accvgpr_read_b32 v39, a119
v_mul_f32 v38, s47, v38
v_mfma_f32_16x16x32_bf16 v[220:223], v[92:95], v[56:59], v[220:223]
v_xor_b32 v26, 0x10000, v26
v_mul_f32 v39, s47, v39
v_mfma_f32_16x16x32_bf16 v[224:227], v[64:67], v[60:63], v[224:227]
ds_read_b128 v[56:59], v25 offset:4096
v_xor_b32 v27, 0x10000, v27
v_mfma_f32_16x16x32_bf16 v[228:231], v[68:71], v[60:63], v[228:231]
buffer_load_dwordx4 v19, s[16:19], s26 offen offset:3072 lds
v_xor_b32 v28, 0x10000, v28
v_mfma_f32_16x16x32_bf16 v[232:235], v[72:75], v[60:63], v[232:235]
v_xor_b32 v29, 0x10000, v29
v_cvt_pk_bf16_f32 v43, v38, v39
v_mfma_f32_16x16x32_bf16 v[236:239], v[76:79], v[60:63], v[236:239]
s_add_u32 s26, s26, 524288
v_xor_b32 v30, 0x10000, v30
v_accvgpr_read_b32 v36, a224
v_mfma_f32_16x16x32_bf16 v[240:243], v[80:83], v[60:63], v[240:243]
v_xor_b32 v31, 0x10000, v31
v_accvgpr_read_b32 v37, a225
v_mfma_f32_16x16x32_bf16 v[244:247], v[84:87], v[60:63], v[244:247]
v_xor_b32 v32, 0x10000, v32
v_mul_f32 v36, s47, v36
v_mfma_f32_16x16x32_bf16 v[248:251], v[88:91], v[60:63], v[248:251]
v_xor_b32 v33, 0x10000, v33
v_mul_f32 v37, s47, v37
v_mfma_f32_16x16x32_bf16 v[252:255], v[92:95], v[60:63], v[252:255]
s_waitcnt vmcnt(26)
s_waitcnt vmcnt(17) lgkmcnt(0)
s_barrier
v_cvt_pk_bf16_f32 v44, v36, v37
v_mfma_f32_16x16x32_bf16 v[128:131], v[96:99], v[48:51], v[128:131]
ds_read_b128 v[60:63], v25 offset:6144
v_accvgpr_read_b32 v38, a226
v_mfma_f32_16x16x32_bf16 v[132:135], v[100:103], v[48:51], v[132:135]
s_nop 0
v_add_u32 v25, 4294868992, v25
v_accvgpr_read_b32 v39, a227
v_or_b32 v0, 1, v11
v_mfma_f32_16x16x32_bf16 v[136:139], v[104:107], v[48:51], v[136:139]
s_add_u32 m0, s92, 131072
ds_read_b64_tr_b16 v[64:65], v26 offset:0
ds_read_b64_tr_b16 v[66:67], v26 offset:1024
v_mfma_f32_16x16x32_bf16 v[140:143], v[108:111], v[48:51], v[140:143]
buffer_load_dwordx4 v20, s[12:15], s24 offen offset:0 lds
v_mul_f32 v38, s47, v38
v_mfma_f32_16x16x32_bf16 v[144:147], v[112:115], v[48:51], v[144:147]
ds_read_b64_tr_b16 v[68:69], v27 offset:0
ds_read_b64_tr_b16 v[70:71], v27 offset:1024
v_mfma_f32_16x16x32_bf16 v[148:151], v[116:119], v[48:51], v[148:151]
buffer_store_dword v0, v6, s[60:63], 0 offen sc1
v_permlane32_swap_b32 v40, v42
v_mfma_f32_16x16x32_bf16 v[152:155], v[120:123], v[48:51], v[152:155]
ds_read_b64_tr_b16 v[72:73], v28 offset:0
ds_read_b64_tr_b16 v[74:75], v28 offset:1024
v_mfma_f32_16x16x32_bf16 v[156:159], v[124:127], v[48:51], v[156:159]
buffer_load_dwordx4 v21, s[12:15], s24 offen offset:1024 lds
v_mul_f32 v39, s47, v39
v_mfma_f32_16x16x32_bf16 v[160:163], v[96:99], v[52:55], v[160:163]
ds_read_b128 v[48:51], v24 offset:0
v_cvt_pk_bf16_f32 v45, v38, v39
v_mfma_f32_16x16x32_bf16 v[164:167], v[100:103], v[52:55], v[164:167]
v_permlane32_swap_b32 v41, v43
v_mfma_f32_16x16x32_bf16 v[168:171], v[104:107], v[52:55], v[168:171]
ds_read_b64_tr_b16 v[76:77], v29 offset:0
ds_read_b64_tr_b16 v[78:79], v29 offset:1024
v_mfma_f32_16x16x32_bf16 v[172:175], v[108:111], v[52:55], v[172:175]
s_xor_b32 s48, s95, 198656
buffer_load_dwordx4 v22, s[12:15], s24 offen offset:2048 lds
v_mfma_f32_16x16x32_bf16 v[176:179], v[112:115], v[52:55], v[176:179]
ds_read_b64_tr_b16 v[80:81], v30 offset:0
ds_read_b64_tr_b16 v[82:83], v30 offset:1024
v_mfma_f32_16x16x32_bf16 v[180:183], v[116:119], v[52:55], v[180:183]
buffer_store_dwordx4 v[40:43], v7, s[20:23], s48 offen sc1
v_accvgpr_read_b32 v36, a228
v_mfma_f32_16x16x32_bf16 v[184:187], v[120:123], v[52:55], v[184:187]
ds_read_b64_tr_b16 v[84:85], v31 offset:0
ds_read_b64_tr_b16 v[86:87], v31 offset:1024
v_mfma_f32_16x16x32_bf16 v[188:191], v[124:127], v[52:55], v[188:191]
buffer_load_dwordx4 v23, s[12:15], s24 offen offset:3072 lds
v_accvgpr_read_b32 v37, a229
v_mfma_f32_16x16x32_bf16 v[192:195], v[96:99], v[56:59], v[192:195]
ds_read_b128 v[52:55], v24 offset:2048
.Lpad8:
s_cbranch_execz .Ltch2
v_mul_f32 v36, s47, v36
v_mfma_f32_16x16x32_bf16 v[196:199], v[100:103], v[56:59], v[196:199]
v_mul_f32 v37, s47, v37
v_cvt_pk_bf16_f32 v46, v36, v37
v_mfma_f32_16x16x32_bf16 v[200:203], v[104:107], v[56:59], v[200:203]
s_add_u32 m0, s92, 135168
ds_read_b64_tr_b16 v[88:89], v32 offset:0
ds_read_b64_tr_b16 v[90:91], v32 offset:1024
v_mfma_f32_16x16x32_bf16 v[204:207], v[108:111], v[56:59], v[204:207]
buffer_load_dwordx4 v20, s[12:15], s25 offen offset:0 lds
v_accvgpr_read_b32 v38, a230
v_mfma_f32_16x16x32_bf16 v[208:211], v[112:115], v[56:59], v[208:211]
ds_read_b64_tr_b16 v[92:93], v33 offset:0
ds_read_b64_tr_b16 v[94:95], v33 offset:1024
v_mfma_f32_16x16x32_bf16 v[212:215], v[116:119], v[56:59], v[212:215]
v_accvgpr_read_b32 v39, a231
v_mul_f32 v38, s47, v38
v_mfma_f32_16x16x32_bf16 v[216:219], v[120:123], v[56:59], v[216:219]
s_waitcnt lgkmcnt(15)
v_mul_f32 v39, s47, v39
v_cvt_pk_bf16_f32 v47, v38, v39
v_mfma_f32_16x16x32_bf16 v[220:223], v[124:127], v[56:59], v[220:223]
v_permlane32_swap_b32 v44, v46
v_mfma_f32_16x16x32_bf16 v[224:227], v[96:99], v[60:63], v[224:227]
ds_read_b128 v[56:59], v24 offset:4096
v_mfma_f32_16x16x32_bf16 v[228:231], v[100:103], v[60:63], v[228:231]
buffer_load_dwordx4 v21, s[12:15], s25 offen offset:1024 lds
v_mfma_f32_16x16x32_bf16 v[232:235], v[104:107], v[60:63], v[232:235]
v_permlane32_swap_b32 v45, v47
v_mfma_f32_16x16x32_bf16 v[236:239], v[108:111], v[60:63], v[236:239]
v_accvgpr_write_b32 a224, v44
v_accvgpr_write_b32 a225, v45
v_mfma_f32_16x16x32_bf16 v[240:243], v[112:115], v[60:63], v[240:243]
v_accvgpr_write_b32 a226, v46
v_accvgpr_write_b32 a227, v47
v_mfma_f32_16x16x32_bf16 v[244:247], v[116:119], v[60:63], v[244:247]
v_accvgpr_read_b32 v36, a120
v_accvgpr_read_b32 v37, a121
v_mfma_f32_16x16x32_bf16 v[248:251], v[120:123], v[60:63], v[248:251]
v_mul_f32 v36, s47, v36
v_mul_f32 v37, s47, v37
v_mfma_f32_16x16x32_bf16 v[252:255], v[124:127], v[60:63], v[252:255]
s_waitcnt lgkmcnt(0)
v_cvt_pk_bf16_f32 v40, v36, v37
v_accvgpr_read_b32 v38, a122
v_mfma_f32_16x16x32_bf16 v[128:131], v[64:67], v[48:51], v[128:131]
ds_read_b128 v[60:63], v24 offset:6144
v_accvgpr_read_b32 v39, a123
v_mfma_f32_16x16x32_bf16 v[132:135], v[68:71], v[48:51], v[132:135]
v_add_u32 v24, 65536, v24
v_mul_f32 v38, s47, v38
v_mfma_f32_16x16x32_bf16 v[136:139], v[72:75], v[48:51], v[136:139]
ds_read_b64_tr_b16 v[96:97], v26 offset:8192
ds_read_b64_tr_b16 v[98:99], v26 offset:9216
v_mfma_f32_16x16x32_bf16 v[140:143], v[76:79], v[48:51], v[140:143]
buffer_load_dwordx4 v22, s[12:15], s25 offen offset:2048 lds
v_mul_f32 v39, s47, v39
v_mfma_f32_16x16x32_bf16 v[144:147], v[80:83], v[48:51], v[144:147]
ds_read_b64_tr_b16 v[100:101], v27 offset:8192
ds_read_b64_tr_b16 v[102:103], v27 offset:9216
v_mfma_f32_16x16x32_bf16 v[148:151], v[84:87], v[48:51], v[148:151]
v_cvt_pk_bf16_f32 v41, v38, v39
v_accvgpr_read_b32 v36, a124
v_mfma_f32_16x16x32_bf16 v[152:155], v[88:91], v[48:51], v[152:155]
ds_read_b64_tr_b16 v[104:105], v28 offset:8192
ds_read_b64_tr_b16 v[106:107], v28 offset:9216
v_mfma_f32_16x16x32_bf16 v[156:159], v[92:95], v[48:51], v[156:159]
buffer_load_dwordx4 v23, s[12:15], s25 offen offset:3072 lds
v_accvgpr_read_b32 v37, a125
v_mfma_f32_16x16x32_bf16 v[160:163], v[64:67], v[52:55], v[160:163]
ds_read_b128 v[48:51], v25 offset:0
v_mul_f32 v36, s47, v36
v_mfma_f32_16x16x32_bf16 v[164:167], v[68:71], v[52:55], v[164:167]
s_add_u32 s24, s24, 128
s_add_u32 s25, s25, 128
v_mul_f32 v37, s47, v37
v_mfma_f32_16x16x32_bf16 v[168:171], v[72:75], v[52:55], v[168:171]
s_add_u32 m0, s91, 16384
ds_read_b64_tr_b16 v[108:109], v29 offset:8192
ds_read_b64_tr_b16 v[110:111], v29 offset:9216
v_mfma_f32_16x16x32_bf16 v[172:175], v[76:79], v[52:55], v[172:175]
buffer_load_dwordx4 v16, s[16:19], s26 offen offset:0 lds
v_cvt_pk_bf16_f32 v42, v36, v37
v_mfma_f32_16x16x32_bf16 v[176:179], v[80:83], v[52:55], v[176:179]
ds_read_b64_tr_b16 v[112:113], v30 offset:8192
ds_read_b64_tr_b16 v[114:115], v30 offset:9216
v_mfma_f32_16x16x32_bf16 v[180:183], v[84:87], v[52:55], v[180:183]
v_accvgpr_read_b32 v38, a126
v_accvgpr_read_b32 v39, a127
v_mfma_f32_16x16x32_bf16 v[184:187], v[88:91], v[52:55], v[184:187]
ds_read_b64_tr_b16 v[116:117], v31 offset:8192
ds_read_b64_tr_b16 v[118:119], v31 offset:9216
v_mfma_f32_16x16x32_bf16 v[188:191], v[92:95], v[52:55], v[188:191]
buffer_load_dwordx4 v17, s[16:19], s26 offen offset:1024 lds
v_mul_f32 v38, s47, v38
v_mfma_f32_16x16x32_bf16 v[192:195], v[64:67], v[56:59], v[192:195]
ds_read_b128 v[52:55], v25 offset:2048
v_mul_f32 v39, s47, v39
v_mfma_f32_16x16x32_bf16 v[196:199], v[68:71], v[56:59], v[196:199]
v_cvt_pk_bf16_f32 v43, v38, v39
v_mfma_f32_16x16x32_bf16 v[200:203], v[72:75], v[56:59], v[200:203]
ds_read_b64_tr_b16 v[120:121], v32 offset:8192
ds_read_b64_tr_b16 v[122:123], v32 offset:9216
v_mfma_f32_16x16x32_bf16 v[204:207], v[76:79], v[56:59], v[204:207]
buffer_load_dwordx4 v18, s[16:19], s26 offen offset:2048 lds
v_accvgpr_read_b32 v36, a232
v_mfma_f32_16x16x32_bf16 v[208:211], v[80:83], v[56:59], v[208:211]
ds_read_b64_tr_b16 v[124:125], v33 offset:8192
ds_read_b64_tr_b16 v[126:127], v33 offset:9216
v_mfma_f32_16x16x32_bf16 v[212:215], v[84:87], v[56:59], v[212:215]
v_permlane32_swap_b32 v40, v42
v_mfma_f32_16x16x32_bf16 v[216:219], v[88:91], v[56:59], v[216:219]
s_waitcnt lgkmcnt(15)
v_permlane32_swap_b32 v41, v43
v_mfma_f32_16x16x32_bf16 v[220:223], v[92:95], v[56:59], v[220:223]
s_xor_b32 s48, s95, 199680
buffer_store_dwordx4 v[40:43], v7, s[20:23], s48 offen sc1
v_mfma_f32_16x16x32_bf16 v[224:227], v[64:67], v[60:63], v[224:227]
ds_read_b128 v[56:59], v25 offset:4096
v_accvgpr_read_b32 v37, a233
v_mfma_f32_16x16x32_bf16 v[228:231], v[68:71], v[60:63], v[228:231]
buffer_load_dwordx4 v19, s[16:19], s26 offen offset:3072 lds
v_mul_f32 v36, s47, v36
v_mfma_f32_16x16x32_bf16 v[232:235], v[72:75], v[60:63], v[232:235]
v_mul_f32 v37, s47, v37
v_cvt_pk_bf16_f32 v44, v36, v37
v_mfma_f32_16x16x32_bf16 v[236:239], v[76:79], v[60:63], v[236:239]
s_add_u32 s26, s26, 524288
v_accvgpr_read_b32 v38, a234
v_accvgpr_read_b32 v39, a235
v_mfma_f32_16x16x32_bf16 v[240:243], v[80:83], v[60:63], v[240:243]
v_mul_f32 v38, s47, v38
v_mul_f32 v39, s47, v39
v_mfma_f32_16x16x32_bf16 v[244:247], v[84:87], v[60:63], v[244:247]
buffer_load_dword v8, v3, s[60:63], s90 offen sc1
v_cvt_pk_bf16_f32 v45, v38, v39
v_mfma_f32_16x16x32_bf16 v[248:251], v[88:91], v[60:63], v[248:251]
v_accvgpr_read_b32 v36, a236
v_accvgpr_read_b32 v37, a237
v_mfma_f32_16x16x32_bf16 v[252:255], v[92:95], v[60:63], v[252:255]
s_waitcnt vmcnt(20) lgkmcnt(0)
s_barrier
v_mul_f32 v36, s47, v36
v_mfma_f32_16x16x32_bf16 v[128:131], v[96:99], v[48:51], v[128:131]
ds_read_b128 v[60:63], v25 offset:6144
v_mul_f32 v37, s47, v37
v_mfma_f32_16x16x32_bf16 v[132:135], v[100:103], v[48:51], v[132:135]
v_add_u32 v25, 65536, v25
v_cvt_pk_bf16_f32 v46, v36, v37
v_mfma_f32_16x16x32_bf16 v[136:139], v[104:107], v[48:51], v[136:139]
s_add_u32 m0, s92, 32768
ds_read_b64_tr_b16 v[64:65], v26 offset:16384
ds_read_b64_tr_b16 v[66:67], v26 offset:17408
v_mfma_f32_16x16x32_bf16 v[140:143], v[108:111], v[48:51], v[140:143]
buffer_load_dwordx4 v20, s[12:15], s24 offen offset:0 lds
v_accvgpr_read_b32 v38, a238
v_mfma_f32_16x16x32_bf16 v[144:147], v[112:115], v[48:51], v[144:147]
ds_read_b64_tr_b16 v[68:69], v27 offset:16384
ds_read_b64_tr_b16 v[70:71], v27 offset:17408
v_mfma_f32_16x16x32_bf16 v[148:151], v[116:119], v[48:51], v[148:151]
v_accvgpr_read_b32 v39, a239
v_mul_f32 v38, s47, v38
v_mfma_f32_16x16x32_bf16 v[152:155], v[120:123], v[48:51], v[152:155]
ds_read_b64_tr_b16 v[72:73], v28 offset:16384
ds_read_b64_tr_b16 v[74:75], v28 offset:17408
v_mfma_f32_16x16x32_bf16 v[156:159], v[124:127], v[48:51], v[156:159]
buffer_load_dwordx4 v21, s[12:15], s24 offen offset:1024 lds
v_mul_f32 v39, s47, v39
v_mfma_f32_16x16x32_bf16 v[160:163], v[96:99], v[52:55], v[160:163]
ds_read_b128 v[48:51], v24 offset:0
v_cvt_pk_bf16_f32 v47, v38, v39
v_mfma_f32_16x16x32_bf16 v[164:167], v[100:103], v[52:55], v[164:167]
v_permlane32_swap_b32 v44, v46
v_mfma_f32_16x16x32_bf16 v[168:171], v[104:107], v[52:55], v[168:171]
ds_read_b64_tr_b16 v[76:77], v29 offset:16384
ds_read_b64_tr_b16 v[78:79], v29 offset:17408
v_mfma_f32_16x16x32_bf16 v[172:175], v[108:111], v[52:55], v[172:175]
buffer_load_dwordx4 v22, s[12:15], s24 offen offset:2048 lds
v_mfma_f32_16x16x32_bf16 v[176:179], v[112:115], v[52:55], v[176:179]
ds_read_b64_tr_b16 v[80:81], v30 offset:16384
ds_read_b64_tr_b16 v[82:83], v30 offset:17408
v_mfma_f32_16x16x32_bf16 v[180:183], v[116:119], v[52:55], v[180:183]
v_permlane32_swap_b32 v45, v47
v_mfma_f32_16x16x32_bf16 v[184:187], v[120:123], v[52:55], v[184:187]
ds_read_b64_tr_b16 v[84:85], v31 offset:16384
ds_read_b64_tr_b16 v[86:87], v31 offset:17408
v_mfma_f32_16x16x32_bf16 v[188:191], v[124:127], v[52:55], v[188:191]
buffer_load_dwordx4 v23, s[12:15], s24 offen offset:3072 lds
v_accvgpr_write_b32 a228, v44
v_mfma_f32_16x16x32_bf16 v[192:195], v[96:99], v[56:59], v[192:195]
ds_read_b128 v[52:55], v24 offset:2048
v_accvgpr_write_b32 a229, v45
v_mfma_f32_16x16x32_bf16 v[196:199], v[100:103], v[56:59], v[196:199]
v_accvgpr_write_b32 a230, v46
v_accvgpr_write_b32 a231, v47
v_mfma_f32_16x16x32_bf16 v[200:203], v[104:107], v[56:59], v[200:203]
s_add_u32 m0, s92, 36864
ds_read_b64_tr_b16 v[88:89], v32 offset:16384
ds_read_b64_tr_b16 v[90:91], v32 offset:17408
v_mfma_f32_16x16x32_bf16 v[204:207], v[108:111], v[56:59], v[204:207]
buffer_load_dwordx4 v20, s[12:15], s25 offen offset:0 lds
v_accvgpr_read_b32 v36, a240
v_mfma_f32_16x16x32_bf16 v[208:211], v[112:115], v[56:59], v[208:211]
ds_read_b64_tr_b16 v[92:93], v33 offset:16384
ds_read_b64_tr_b16 v[94:95], v33 offset:17408
v_mfma_f32_16x16x32_bf16 v[212:215], v[116:119], v[56:59], v[212:215]
v_accvgpr_read_b32 v37, a241
v_mul_f32 v36, s47, v36
v_mfma_f32_16x16x32_bf16 v[216:219], v[120:123], v[56:59], v[216:219]
s_waitcnt lgkmcnt(15)
v_mul_f32 v37, s47, v37
v_cvt_pk_bf16_f32 v40, v36, v37
v_mfma_f32_16x16x32_bf16 v[220:223], v[124:127], v[56:59], v[220:223]
v_accvgpr_read_b32 v38, a242
v_accvgpr_read_b32 v39, a243
v_mfma_f32_16x16x32_bf16 v[224:227], v[96:99], v[60:63], v[224:227]
ds_read_b128 v[56:59], v24 offset:4096
v_mul_f32 v38, s47, v38
v_mfma_f32_16x16x32_bf16 v[228:231], v[100:103], v[60:63], v[228:231]
buffer_load_dwordx4 v21, s[12:15], s25 offen offset:1024 lds
v_mul_f32 v39, s47, v39
v_mfma_f32_16x16x32_bf16 v[232:235], v[104:107], v[60:63], v[232:235]
v_cvt_pk_bf16_f32 v41, v38, v39
v_accvgpr_read_b32 v36, a244
v_mfma_f32_16x16x32_bf16 v[236:239], v[108:111], v[60:63], v[236:239]
v_accvgpr_read_b32 v37, a245
v_mul_f32 v36, s47, v36
v_mfma_f32_16x16x32_bf16 v[240:243], v[112:115], v[60:63], v[240:243]
v_mul_f32 v37, s47, v37
v_cvt_pk_bf16_f32 v42, v36, v37
v_mfma_f32_16x16x32_bf16 v[244:247], v[116:119], v[60:63], v[244:247]
v_accvgpr_read_b32 v38, a246
v_accvgpr_read_b32 v39, a247
v_mfma_f32_16x16x32_bf16 v[248:251], v[120:123], v[60:63], v[248:251]
v_mul_f32 v38, s47, v38
v_mul_f32 v39, s47, v39
v_mfma_f32_16x16x32_bf16 v[252:255], v[124:127], v[60:63], v[252:255]
s_waitcnt lgkmcnt(0)
v_cvt_pk_bf16_f32 v43, v38, v39
v_mfma_f32_16x16x32_bf16 v[128:131], v[64:67], v[48:51], v[128:131]
ds_read_b128 v[60:63], v24 offset:6144
v_mfma_f32_16x16x32_bf16 v[132:135], v[68:71], v[48:51], v[132:135]
v_add_u32 v24, 32768, v24
v_mfma_f32_16x16x32_bf16 v[136:139], v[72:75], v[48:51], v[136:139]
ds_read_b64_tr_b16 v[96:97], v26 offset:24576
ds_read_b64_tr_b16 v[98:99], v26 offset:25600
v_mfma_f32_16x16x32_bf16 v[140:143], v[76:79], v[48:51], v[140:143]
buffer_load_dwordx4 v22, s[12:15], s25 offen offset:2048 lds
v_mfma_f32_16x16x32_bf16 v[144:147], v[80:83], v[48:51], v[144:147]
ds_read_b64_tr_b16 v[100:101], v27 offset:24576
ds_read_b64_tr_b16 v[102:103], v27 offset:25600
v_mfma_f32_16x16x32_bf16 v[148:151], v[84:87], v[48:51], v[148:151]
v_permlane32_swap_b32 v40, v42
v_mfma_f32_16x16x32_bf16 v[152:155], v[88:91], v[48:51], v[152:155]
ds_read_b64_tr_b16 v[104:105], v28 offset:24576
ds_read_b64_tr_b16 v[106:107], v28 offset:25600
v_mfma_f32_16x16x32_bf16 v[156:159], v[92:95], v[48:51], v[156:159]
buffer_load_dwordx4 v23, s[12:15], s25 offen offset:3072 lds
v_mfma_f32_16x16x32_bf16 v[160:163], v[64:67], v[52:55], v[160:163]
ds_read_b128 v[48:51], v25 offset:0
v_mfma_f32_16x16x32_bf16 v[164:167], v[68:71], v[52:55], v[164:167]
s_add_u32 s24, s24, 128
s_add_u32 s25, s25, 128
v_mfma_f32_16x16x32_bf16 v[168:171], v[72:75], v[52:55], v[168:171]
ds_read_b64_tr_b16 v[108:109], v29 offset:24576
ds_read_b64_tr_b16 v[110:111], v29 offset:25600
v_mfma_f32_16x16x32_bf16 v[172:175], v[76:79], v[52:55], v[172:175]
v_permlane32_swap_b32 v41, v43
v_mfma_f32_16x16x32_bf16 v[176:179], v[80:83], v[52:55], v[176:179]
ds_read_b64_tr_b16 v[112:113], v30 offset:24576
ds_read_b64_tr_b16 v[114:115], v30 offset:25600
v_mfma_f32_16x16x32_bf16 v[180:183], v[84:87], v[52:55], v[180:183]
v_accvgpr_write_b32 a232, v40
v_accvgpr_write_b32 a233, v41
v_mfma_f32_16x16x32_bf16 v[184:187], v[88:91], v[52:55], v[184:187]
ds_read_b64_tr_b16 v[116:117], v31 offset:24576
ds_read_b64_tr_b16 v[118:119], v31 offset:25600
v_mfma_f32_16x16x32_bf16 v[188:191], v[92:95], v[52:55], v[188:191]
v_accvgpr_write_b32 a234, v42
v_accvgpr_write_b32 a235, v43
v_mfma_f32_16x16x32_bf16 v[192:195], v[64:67], v[56:59], v[192:195]
ds_read_b128 v[52:55], v25 offset:2048
v_accvgpr_read_b32 v36, a248
v_mfma_f32_16x16x32_bf16 v[196:199], v[68:71], v[56:59], v[196:199]
v_accvgpr_read_b32 v37, a249
v_mul_f32 v36, s47, v36
v_mfma_f32_16x16x32_bf16 v[200:203], v[72:75], v[56:59], v[200:203]
ds_read_b64_tr_b16 v[120:121], v32 offset:24576
ds_read_b64_tr_b16 v[122:123], v32 offset:25600
v_mfma_f32_16x16x32_bf16 v[204:207], v[76:79], v[56:59], v[204:207]
v_mul_f32 v37, s47, v37
v_cvt_pk_bf16_f32 v44, v36, v37
v_mfma_f32_16x16x32_bf16 v[208:211], v[80:83], v[56:59], v[208:211]
ds_read_b64_tr_b16 v[124:125], v33 offset:24576
ds_read_b64_tr_b16 v[126:127], v33 offset:25600
v_mfma_f32_16x16x32_bf16 v[212:215], v[84:87], v[56:59], v[212:215]
s_waitcnt vmcnt(8)
v_and_b32 v0, 7, v8
v_cmp_lt_u32 s[28:29], v0, v14
v_mfma_f32_16x16x32_bf16 v[216:219], v[88:91], v[56:59], v[216:219]
s_waitcnt lgkmcnt(15)
v_cmp_eq_u32 s[30:31], v8, v2
v_accvgpr_read_b32 v38, a250
v_mfma_f32_16x16x32_bf16 v[220:223], v[92:95], v[56:59], v[220:223]
v_xor_b32 v26, 0x10000, v26
v_accvgpr_read_b32 v39, a251
v_mfma_f32_16x16x32_bf16 v[224:227], v[64:67], v[60:63], v[224:227]
ds_read_b128 v[56:59], v25 offset:4096
v_xor_b32 v27, 0x10000, v27
v_mfma_f32_16x16x32_bf16 v[228:231], v[68:71], v[60:63], v[228:231]
v_xor_b32 v28, 0x10000, v28
v_mul_f32 v38, s47, v38
v_mfma_f32_16x16x32_bf16 v[232:235], v[72:75], v[60:63], v[232:235]
ds_read_b128 v[20:23], v25 offset:6144
v_xor_b32 v29, 0x10000, v29
v_mfma_f32_16x16x32_bf16 v[236:239], v[76:79], v[60:63], v[236:239]
v_xor_b32 v30, 0x10000, v30
v_mul_f32 v39, s47, v39
v_mfma_f32_16x16x32_bf16 v[240:243], v[80:83], v[60:63], v[240:243]
v_xor_b32 v31, 0x10000, v31
v_cvt_pk_bf16_f32 v45, v38, v39
v_mfma_f32_16x16x32_bf16 v[244:247], v[84:87], v[60:63], v[244:247]
v_xor_b32 v32, 0x10000, v32
v_accvgpr_read_b32 v36, a252
v_mfma_f32_16x16x32_bf16 v[248:251], v[88:91], v[60:63], v[248:251]
s_or_b64 s[28:29], s[28:29], s[30:31]
s_cbranch_scc1 .Lspin1
.Lspin1_ret:
s_add_u32 s28, s39, 0
s_and_b32 s28, s28, 0xffff
v_xor_b32 v33, 0x10000, v33
v_mfma_f32_16x16x32_bf16 v[252:255], v[92:95], v[60:63], v[252:255]
s_waitcnt vmcnt(14) lgkmcnt(0)
s_barrier
s_mov_b32 s40, 0
v_mfma_f32_16x16x32_bf16 v[128:131], v[96:99], v[48:51], v[128:131]
s_add_u32 m0, s28, 65536
v_accvgpr_read_b32 v37, a253
v_mul_f32 v36, s47, v36
v_mfma_f32_16x16x32_bf16 v[132:135], v[100:103], v[48:51], v[132:135]
buffer_load_dwordx4 v7, s[4:7], s40 offen offset:0 sc1 lds
v_add_u32 v25, 32768, v25
v_mfma_f32_16x16x32_bf16 v[136:139], v[104:107], v[48:51], v[136:139]
ds_read_b64_tr_b16 v[64:65], v26 offset:0
ds_read_b64_tr_b16 v[66:67], v26 offset:1024
v_mfma_f32_16x16x32_bf16 v[140:143], v[108:111], v[48:51], v[140:143]
v_mul_f32 v37, s47, v37
v_cvt_pk_bf16_f32 v46, v36, v37
v_mfma_f32_16x16x32_bf16 v[144:147], v[112:115], v[48:51], v[144:147]
ds_read_b64_tr_b16 v[68:69], v27 offset:0
ds_read_b64_tr_b16 v[70:71], v27 offset:1024
v_mfma_f32_16x16x32_bf16 v[148:151], v[116:119], v[48:51], v[148:151]
buffer_load_dwordx4 v7, s[4:7], s40 offen offset:1024 sc1 lds
v_accvgpr_read_b32 v38, a254
v_mfma_f32_16x16x32_bf16 v[152:155], v[120:123], v[48:51], v[152:155]
ds_read_b64_tr_b16 v[72:73], v28 offset:0
ds_read_b64_tr_b16 v[74:75], v28 offset:1024
v_mfma_f32_16x16x32_bf16 v[156:159], v[124:127], v[48:51], v[156:159]
v_accvgpr_read_b32 v39, a255
v_mul_f32 v38, s47, v38
v_mfma_f32_16x16x32_bf16 v[160:163], v[96:99], v[52:55], v[160:163]
ds_read_b128 v[48:51], v24 offset:0
v_mul_f32 v39, s47, v39
v_mfma_f32_16x16x32_bf16 v[164:167], v[100:103], v[52:55], v[164:167]
buffer_load_dwordx4 v7, s[4:7], s40 offen offset:2048 sc1 lds
v_cvt_pk_bf16_f32 v47, v38, v39
v_mfma_f32_16x16x32_bf16 v[168:171], v[104:107], v[52:55], v[168:171]
ds_read_b64_tr_b16 v[76:77], v29 offset:0
ds_read_b64_tr_b16 v[78:79], v29 offset:1024
v_mfma_f32_16x16x32_bf16 v[172:175], v[108:111], v[52:55], v[172:175]
v_permlane32_swap_b32 v44, v46
v_mfma_f32_16x16x32_bf16 v[176:179], v[112:115], v[52:55], v[176:179]
ds_read_b64_tr_b16 v[80:81], v30 offset:0
ds_read_b64_tr_b16 v[82:83], v30 offset:1024
v_mfma_f32_16x16x32_bf16 v[180:183], v[116:119], v[52:55], v[180:183]
buffer_load_dwordx4 v7, s[4:7], s40 offen offset:3072 sc1 lds
v_mfma_f32_16x16x32_bf16 v[184:187], v[120:123], v[52:55], v[184:187]
ds_read_b64_tr_b16 v[84:85], v31 offset:0
ds_read_b64_tr_b16 v[86:87], v31 offset:1024
v_mfma_f32_16x16x32_bf16 v[188:191], v[124:127], v[52:55], v[188:191]
s_add_u32 s28, s39, 16384
s_and_b32 s28, s28, 0xffff
v_mfma_f32_16x16x32_bf16 v[192:195], v[96:99], v[56:59], v[192:195]
s_mov_b32 s41, 16384
ds_read_b128 v[52:55], v24 offset:2048
v_mfma_f32_16x16x32_bf16 v[196:199], v[100:103], v[56:59], v[196:199]
s_add_u32 m0, s28, 65536
v_permlane32_swap_b32 v45, v47
v_mfma_f32_16x16x32_bf16 v[200:203], v[104:107], v[56:59], v[200:203]
buffer_load_dwordx4 v7, s[4:7], s41 offen offset:0 sc1 lds
ds_read_b64_tr_b16 v[88:89], v32 offset:0
ds_read_b64_tr_b16 v[90:91], v32 offset:1024
v_mfma_f32_16x16x32_bf16 v[204:207], v[108:111], v[56:59], v[204:207]
v_accvgpr_write_b32 a236, v44
v_accvgpr_write_b32 a237, v45
v_mfma_f32_16x16x32_bf16 v[208:211], v[112:115], v[56:59], v[208:211]
ds_read_b64_tr_b16 v[92:93], v33 offset:0
ds_read_b64_tr_b16 v[94:95], v33 offset:1024
v_mfma_f32_16x16x32_bf16 v[212:215], v[116:119], v[56:59], v[212:215]
v_accvgpr_write_b32 a238, v46
v_accvgpr_write_b32 a239, v47
v_mfma_f32_16x16x32_bf16 v[216:219], v[120:123], v[56:59], v[216:219]
buffer_load_dwordx4 v7, s[4:7], s41 offen offset:1024 sc1 lds
v_mfma_f32_16x16x32_bf16 v[220:223], v[124:127], v[56:59], v[220:223]
v_mfma_f32_16x16x32_bf16 v[224:227], v[96:99], v[20:23], v[224:227]
ds_read_b128 v[56:59], v24 offset:4096
v_mfma_f32_16x16x32_bf16 v[228:231], v[100:103], v[20:23], v[228:231]
v_mfma_f32_16x16x32_bf16 v[232:235], v[104:107], v[20:23], v[232:235]
buffer_load_dwordx4 v7, s[4:7], s41 offen offset:2048 sc1 lds
v_mfma_f32_16x16x32_bf16 v[236:239], v[108:111], v[20:23], v[236:239]
v_mfma_f32_16x16x32_bf16 v[240:243], v[112:115], v[20:23], v[240:243]
v_mfma_f32_16x16x32_bf16 v[244:247], v[116:119], v[20:23], v[244:247]
v_mfma_f32_16x16x32_bf16 v[248:251], v[120:123], v[20:23], v[248:251]
buffer_load_dwordx4 v7, s[4:7], s41 offen offset:3072 sc1 lds
v_mfma_f32_16x16x32_bf16 v[252:255], v[124:127], v[20:23], v[252:255]
s_waitcnt lgkmcnt(0)
v_mfma_f32_16x16x32_bf16 v[128:131], v[64:67], v[48:51], v[128:131]
s_add_u32 s28, s39, 32768
s_and_b32 s28, s28, 0xffff
ds_read_b128 v[60:63], v24 offset:6144
v_mfma_f32_16x16x32_bf16 v[132:135], v[68:71], v[48:51], v[132:135]
s_mov_b32 s40, 32768
v_add_u32 v24, 4294868992, v24
v_mfma_f32_16x16x32_bf16 v[136:139], v[72:75], v[48:51], v[136:139]
s_add_u32 m0, s28, 65536
ds_read_b64_tr_b16 v[96:97], v26 offset:8192
ds_read_b64_tr_b16 v[98:99], v26 offset:9216
v_mfma_f32_16x16x32_bf16 v[140:143], v[76:79], v[48:51], v[140:143]
buffer_load_dwordx4 v7, s[4:7], s40 offen offset:0 sc1 lds
v_mfma_f32_16x16x32_bf16 v[144:147], v[80:83], v[48:51], v[144:147]
ds_read_b64_tr_b16 v[100:101], v27 offset:8192
ds_read_b64_tr_b16 v[102:103], v27 offset:9216
v_mfma_f32_16x16x32_bf16 v[148:151], v[84:87], v[48:51], v[148:151]
s_sub_u32 s32, s66, 4194304
s_subb_u32 s33, s67, 0
v_mfma_f32_16x16x32_bf16 v[152:155], v[88:91], v[48:51], v[152:155]
ds_read_b64_tr_b16 v[104:105], v28 offset:8192
ds_read_b64_tr_b16 v[106:107], v28 offset:9216
v_mfma_f32_16x16x32_bf16 v[156:159], v[92:95], v[48:51], v[156:159]
buffer_load_dwordx4 v7, s[4:7], s40 offen offset:1024 sc1 lds
v_mfma_f32_16x16x32_bf16 v[160:163], v[64:67], v[52:55], v[160:163]
ds_read_b128 v[48:51], v25 offset:0
v_mfma_f32_16x16x32_bf16 v[164:167], v[68:71], v[52:55], v[164:167]
s_sub_u32 s34, s80, 10485760
s_subb_u32 s35, s81, 0
v_mfma_f32_16x16x32_bf16 v[168:171], v[72:75], v[52:55], v[168:171]
ds_read_b64_tr_b16 v[108:109], v29 offset:8192
ds_read_b64_tr_b16 v[110:111], v29 offset:9216
v_mfma_f32_16x16x32_bf16 v[172:175], v[76:79], v[52:55], v[172:175]
buffer_load_dwordx4 v7, s[4:7], s40 offen offset:2048 sc1 lds
v_mfma_f32_16x16x32_bf16 v[176:179], v[80:83], v[52:55], v[176:179]
ds_read_b64_tr_b16 v[112:113], v30 offset:8192
ds_read_b64_tr_b16 v[114:115], v30 offset:9216
v_mfma_f32_16x16x32_bf16 v[180:183], v[84:87], v[52:55], v[180:183]
s_sub_u32 s36, s68, 18874368
s_subb_u32 s37, s69, 0
s_lshl_b32 s38, s2, 2
v_mfma_f32_16x16x32_bf16 v[184:187], v[88:91], v[52:55], v[184:187]
ds_read_b64_tr_b16 v[116:117], v31 offset:8192
ds_read_b64_tr_b16 v[118:119], v31 offset:9216
v_mfma_f32_16x16x32_bf16 v[188:191], v[92:95], v[52:55], v[188:191]
s_add_u32 s38, s38, s3
buffer_load_dwordx4 v7, s[4:7], s40 offen offset:3072 sc1 lds
v_mfma_f32_16x16x32_bf16 v[192:195], v[64:67], v[56:59], v[192:195]
s_lshl_b32 s38, s38, 7
ds_read_b128 v[52:55], v25 offset:2048
v_mfma_f32_16x16x32_bf16 v[196:199], v[68:71], v[56:59], v[196:199]
s_add_u32 s28, s39, 49152
s_and_b32 s28, s28, 0xffff
v_min_u32 v9, 19, v1
v_mfma_f32_16x16x32_bf16 v[200:203], v[72:75], v[56:59], v[200:203]
s_mov_b32 s41, 49152
ds_read_b64_tr_b16 v[120:121], v32 offset:8192
ds_read_b64_tr_b16 v[122:123], v32 offset:9216
v_mfma_f32_16x16x32_bf16 v[204:207], v[76:79], v[56:59], v[204:207]
s_add_u32 m0, s28, 65536
v_mov_b32 v2, s64
v_mov_b32 v3, s65
v_mfma_f32_16x16x32_bf16 v[208:211], v[80:83], v[56:59], v[208:211]
buffer_load_dwordx4 v7, s[4:7], s41 offen offset:0 sc1 lds
ds_read_b64_tr_b16 v[124:125], v33 offset:8192
ds_read_b64_tr_b16 v[126:127], v33 offset:9216
v_mfma_f32_16x16x32_bf16 v[212:215], v[84:87], v[56:59], v[212:215]
v_cmp_le_u32 vcc, 4, v9
v_mov_b32 v5, s32
v_mfma_f32_16x16x32_bf16 v[216:219], v[88:91], v[56:59], v[216:219]
s_waitcnt lgkmcnt(15)
v_mov_b32 v10, s33
v_cndmask_b32 v2, v2, v5, vcc
v_mfma_f32_16x16x32_bf16 v[220:223], v[92:95], v[56:59], v[220:223]
v_cndmask_b32 v3, v3, v10, vcc
v_cmp_le_u32 vcc, 10, v9
v_mfma_f32_16x16x32_bf16 v[224:227], v[64:67], v[60:63], v[224:227]
buffer_load_dwordx4 v7, s[4:7], s41 offen offset:1024 sc1 lds
ds_read_b128 v[56:59], v25 offset:4096
v_mfma_f32_16x16x32_bf16 v[228:231], v[68:71], v[60:63], v[228:231]
v_mov_b32 v5, s34
v_mov_b32 v10, s35
v_mfma_f32_16x16x32_bf16 v[232:235], v[72:75], v[60:63], v[232:235]
v_cndmask_b32 v2, v2, v5, vcc
v_cndmask_b32 v3, v3, v10, vcc
v_mfma_f32_16x16x32_bf16 v[236:239], v[76:79], v[60:63], v[236:239]
v_cmp_le_u32 vcc, 18, v9
v_mov_b32 v5, s36
v_mfma_f32_16x16x32_bf16 v[240:243], v[80:83], v[60:63], v[240:243]
buffer_load_dwordx4 v7, s[4:7], s41 offen offset:2048 sc1 lds
v_mov_b32 v10, s37
v_mfma_f32_16x16x32_bf16 v[244:247], v[84:87], v[60:63], v[244:247]
v_cndmask_b32 v2, v2, v5, vcc
v_cndmask_b32 v3, v3, v10, vcc
v_mfma_f32_16x16x32_bf16 v[248:251], v[88:91], v[60:63], v[248:251]
v_lshlrev_b32 v9, 20, v9
v_add_u32 v9, s38, v9
v_mfma_f32_16x16x32_bf16 v[252:255], v[92:95], v[60:63], v[252:255]
s_waitcnt vmcnt(15) lgkmcnt(0)
s_barrier
v_add_co_u32 v2, vcc, v2, v9
v_mfma_f32_16x16x32_bf16 v[128:131], v[96:99], v[48:51], v[128:131]
buffer_load_dwordx4 v7, s[4:7], s41 offen offset:3072 sc1 lds
ds_read_b128 v[60:63], v25 offset:6144
v_mfma_f32_16x16x32_bf16 v[132:135], v[100:103], v[48:51], v[132:135]
v_add_u32 v25, 4294868992, v25
v_nop
v_mfma_f32_16x16x32_bf16 v[136:139], v[104:107], v[48:51], v[136:139]
ds_read_b64_tr_b16 v[64:65], v26 offset:16384
ds_read_b64_tr_b16 v[66:67], v26 offset:17408
v_mfma_f32_16x16x32_bf16 v[140:143], v[108:111], v[48:51], v[140:143]
v_nop
v_addc_co_u32 v3, vcc, 0, v3, vcc
v_mfma_f32_16x16x32_bf16 v[144:147], v[112:115], v[48:51], v[144:147]
ds_read_b64_tr_b16 v[68:69], v27 offset:16384
ds_read_b64_tr_b16 v[70:71], v27 offset:17408
v_mfma_f32_16x16x32_bf16 v[148:151], v[116:119], v[48:51], v[148:151]
s_lshl_b32 s93, s2, 4
s_add_u32 s93, s93, s3
s_lshl_b32 s93, s93, 2
v_mfma_f32_16x16x32_bf16 v[152:155], v[120:123], v[48:51], v[152:155]
ds_read_b64_tr_b16 v[72:73], v28 offset:16384
ds_read_b64_tr_b16 v[74:75], v28 offset:17408
v_mfma_f32_16x16x32_bf16 v[156:159], v[124:127], v[48:51], v[156:159]
s_add_u32 s93, s93, 0x10000
v_mfma_f32_16x16x32_bf16 v[160:163], v[96:99], v[52:55], v[160:163]
s_xor_b32 s48, s95, 262144
ds_read_b128 v[48:51], v24 offset:0
v_mfma_f32_16x16x32_bf16 v[164:167], v[100:103], v[52:55], v[164:167]
buffer_store_dwordx4 a[128:131], v7, s[20:23], s48 offen sc1
v_lshlrev_b32 v9, 20, v1
v_mfma_f32_16x16x32_bf16 v[168:171], v[104:107], v[52:55], v[168:171]
ds_read_b64_tr_b16 v[76:77], v29 offset:16384
ds_read_b64_tr_b16 v[78:79], v29 offset:17408
v_mfma_f32_16x16x32_bf16 v[172:175], v[108:111], v[52:55], v[172:175]
v_add_u32 v5, s93, v9
v_mfma_f32_16x16x32_bf16 v[176:179], v[112:115], v[52:55], v[176:179]
ds_read_b64_tr_b16 v[80:81], v30 offset:16384
ds_read_b64_tr_b16 v[82:83], v30 offset:17408
v_mfma_f32_16x16x32_bf16 v[180:183], v[116:119], v[52:55], v[180:183]
v_mfma_f32_16x16x32_bf16 v[184:187], v[120:123], v[52:55], v[184:187]
ds_read_b64_tr_b16 v[84:85], v31 offset:16384
ds_read_b64_tr_b16 v[86:87], v31 offset:17408
v_mfma_f32_16x16x32_bf16 v[188:191], v[124:127], v[52:55], v[188:191]
.Lpad9:
s_cbranch_execz .Ltch2
v_mfma_f32_16x16x32_bf16 v[192:195], v[96:99], v[56:59], v[192:195]
ds_read_b128 v[52:55], v24 offset:2048
v_mfma_f32_16x16x32_bf16 v[196:199], v[100:103], v[56:59], v[196:199]
v_mfma_f32_16x16x32_bf16 v[200:203], v[104:107], v[56:59], v[200:203]
ds_read_b64_tr_b16 v[88:89], v32 offset:16384
ds_read_b64_tr_b16 v[90:91], v32 offset:17408
v_mfma_f32_16x16x32_bf16 v[204:207], v[108:111], v[56:59], v[204:207]
v_mfma_f32_16x16x32_bf16 v[208:211], v[112:115], v[56:59], v[208:211]
ds_read_b64_tr_b16 v[92:93], v33 offset:16384
ds_read_b64_tr_b16 v[94:95], v33 offset:17408
v_mfma_f32_16x16x32_bf16 v[212:215], v[116:119], v[56:59], v[212:215]
v_mfma_f32_16x16x32_bf16 v[216:219], v[120:123], v[56:59], v[216:219]
s_waitcnt lgkmcnt(15)
v_mfma_f32_16x16x32_bf16 v[220:223], v[124:127], v[56:59], v[220:223]
v_mfma_f32_16x16x32_bf16 v[224:227], v[96:99], v[60:63], v[224:227]
ds_read_b128 v[56:59], v24 offset:4096
v_mfma_f32_16x16x32_bf16 v[228:231], v[100:103], v[60:63], v[228:231]
s_xor_b32 s48, s95, 263168
v_mfma_f32_16x16x32_bf16 v[232:235], v[104:107], v[60:63], v[232:235]
buffer_store_dwordx4 a[132:135], v7, s[20:23], s48 offen sc1
v_mfma_f32_16x16x32_bf16 v[236:239], v[108:111], v[60:63], v[236:239]
v_mfma_f32_16x16x32_bf16 v[240:243], v[112:115], v[60:63], v[240:243]
v_mfma_f32_16x16x32_bf16 v[244:247], v[116:119], v[60:63], v[244:247]
v_mfma_f32_16x16x32_bf16 v[248:251], v[120:123], v[60:63], v[248:251]
v_mfma_f32_16x16x32_bf16 v[252:255], v[124:127], v[60:63], v[252:255]
s_waitcnt lgkmcnt(0)
v_mfma_f32_16x16x32_bf16 v[128:131], v[64:67], v[48:51], v[128:131]
ds_read_b128 v[60:63], v24 offset:6144
v_mfma_f32_16x16x32_bf16 v[132:135], v[68:71], v[48:51], v[132:135]
v_mfma_f32_16x16x32_bf16 v[136:139], v[72:75], v[48:51], v[136:139]
ds_read_b64_tr_b16 v[96:97], v26 offset:24576
ds_read_b64_tr_b16 v[98:99], v26 offset:25600
v_mfma_f32_16x16x32_bf16 v[140:143], v[76:79], v[48:51], v[140:143]
v_mfma_f32_16x16x32_bf16 v[144:147], v[80:83], v[48:51], v[144:147]
ds_read_b64_tr_b16 v[100:101], v27 offset:24576
ds_read_b64_tr_b16 v[102:103], v27 offset:25600
v_mfma_f32_16x16x32_bf16 v[148:151], v[84:87], v[48:51], v[148:151]
v_mfma_f32_16x16x32_bf16 v[152:155], v[88:91], v[48:51], v[152:155]
ds_read_b64_tr_b16 v[104:105], v28 offset:24576
ds_read_b64_tr_b16 v[106:107], v28 offset:25600
v_mfma_f32_16x16x32_bf16 v[156:159], v[92:95], v[48:51], v[156:159]
v_mfma_f32_16x16x32_bf16 v[160:163], v[64:67], v[52:55], v[160:163]
ds_read_b128 v[48:51], v25 offset:0
v_mfma_f32_16x16x32_bf16 v[164:167], v[68:71], v[52:55], v[164:167]
v_mfma_f32_16x16x32_bf16 v[168:171], v[72:75], v[52:55], v[168:171]
s_xor_b32 s48, s95, 264192
ds_read_b64_tr_b16 v[108:109], v29 offset:24576
ds_read_b64_tr_b16 v[110:111], v29 offset:25600
v_mfma_f32_16x16x32_bf16 v[172:175], v[76:79], v[52:55], v[172:175]
buffer_store_dwordx4 a[136:139], v7, s[20:23], s48 offen sc1
v_mfma_f32_16x16x32_bf16 v[176:179], v[80:83], v[52:55], v[176:179]
ds_read_b64_tr_b16 v[112:113], v30 offset:24576
ds_read_b64_tr_b16 v[114:115], v30 offset:25600
v_mfma_f32_16x16x32_bf16 v[180:183], v[84:87], v[52:55], v[180:183]
v_mfma_f32_16x16x32_bf16 v[184:187], v[88:91], v[52:55], v[184:187]
ds_read_b64_tr_b16 v[116:117], v31 offset:24576
ds_read_b64_tr_b16 v[118:119], v31 offset:25600
v_mfma_f32_16x16x32_bf16 v[188:191], v[92:95], v[52:55], v[188:191]
v_mfma_f32_16x16x32_bf16 v[192:195], v[64:67], v[56:59], v[192:195]
ds_read_b128 v[52:55], v25 offset:2048
v_mfma_f32_16x16x32_bf16 v[196:199], v[68:71], v[56:59], v[196:199]
v_mfma_f32_16x16x32_bf16 v[200:203], v[72:75], v[56:59], v[200:203]
ds_read_b64_tr_b16 v[120:121], v32 offset:24576
ds_read_b64_tr_b16 v[122:123], v32 offset:25600
v_mfma_f32_16x16x32_bf16 v[204:207], v[76:79], v[56:59], v[204:207]
v_mfma_f32_16x16x32_bf16 v[208:211], v[80:83], v[56:59], v[208:211]
ds_read_b64_tr_b16 v[124:125], v33 offset:24576
ds_read_b64_tr_b16 v[126:127], v33 offset:25600
v_mfma_f32_16x16x32_bf16 v[212:215], v[84:87], v[56:59], v[212:215]
v_mfma_f32_16x16x32_bf16 v[216:219], v[88:91], v[56:59], v[216:219]
s_waitcnt lgkmcnt(15)
v_and_b32 v8, 31, v1
v_lshrrev_b32 v9, 5, v1
v_mfma_f32_16x16x32_bf16 v[220:223], v[92:95], v[56:59], v[220:223]
v_lshrrev_b32 v10, 4, v8
v_and_b32 v13, 15, v1
v_mfma_f32_16x16x32_bf16 v[224:227], v[64:67], v[60:63], v[224:227]
ds_read_b128 v[56:59], v25 offset:4096
v_lshlrev_b32 v13, 4, v13
v_mfma_f32_16x16x32_bf16 v[228:231], v[68:71], v[60:63], v[228:231]
v_lshl_add_u32 v13, v9, 8, v13
v_subrev_u32 v13, s91, v13
v_mfma_f32_16x16x32_bf16 v[232:235], v[72:75], v[60:63], v[232:235]
ds_read_b128 v[20:23], v25 offset:6144
v_lshl_add_u32 v32, v10, 12, v13
v_mfma_f32_16x16x32_bf16 v[236:239], v[76:79], v[60:63], v[236:239]
v_add_u32 v32, 0x10000, v32
v_lshl_add_u32 v33, v10, 9, v13
v_mfma_f32_16x16x32_bf16 v[240:243], v[80:83], v[60:63], v[240:243]
v_lshlrev_b32 v9, 2, v9
v_sub_u32 v8, v8, v9
v_mfma_f32_16x16x32_bf16 v[244:247], v[84:87], v[60:63], v[244:247]
v_mov_b32 v34, 0xff800000
v_add_u32 v12, s46, v32
v_mfma_f32_16x16x32_bf16 v[248:251], v[88:91], v[60:63], v[248:251]
s_add_u32 s28, s39, 0
s_and_b32 s28, s28, 0xffff
v_mfma_f32_16x16x32_bf16 v[252:255], v[92:95], v[60:63], v[252:255]
s_waitcnt  lgkmcnt(0)
s_waitcnt vmcnt(3)
s_barrier
s_mov_b32 s40, 16384
v_mfma_f32_16x16x32_bf16 v[128:131], v[96:99], v[48:51], v[128:131]
s_add_u32 m0, s28, 0
v_mfma_f32_16x16x32_bf16 v[132:135], v[100:103], v[48:51], v[132:135]
buffer_load_dwordx4 v7, s[8:11], s40 offen offset:0 sc1 lds
v_mfma_f32_16x16x32_bf16 v[136:139], v[104:107], v[48:51], v[136:139]
v_mfma_f32_16x16x32_bf16 v[140:143], v[108:111], v[48:51], v[140:143]
v_mfma_f32_16x16x32_bf16 v[144:147], v[112:115], v[48:51], v[144:147]
v_mfma_f32_16x16x32_bf16 v[148:151], v[116:119], v[48:51], v[148:151]
buffer_load_dwordx4 v7, s[8:11], s40 offen offset:1024 sc1 lds
v_mfma_f32_16x16x32_bf16 v[152:155], v[120:123], v[48:51], v[152:155]
v_mfma_f32_16x16x32_bf16 v[156:159], v[124:127], v[48:51], v[156:159]
v_mfma_f32_16x16x32_bf16 v[160:163], v[96:99], v[52:55], v[160:163]
v_mfma_f32_16x16x32_bf16 v[164:167], v[100:103], v[52:55], v[164:167]
buffer_load_dwordx4 v7, s[8:11], s40 offen offset:2048 sc1 lds
v_mfma_f32_16x16x32_bf16 v[168:171], v[104:107], v[52:55], v[168:171]
ds_read_b128 a[0:3], v12 offset:0
v_mfma_f32_16x16x32_bf16 v[172:175], v[108:111], v[52:55], v[172:175]
v_mfma_f32_16x16x32_bf16 v[176:179], v[112:115], v[52:55], v[176:179]
ds_read_b128 a[4:7], v12 offset:512
v_mfma_f32_16x16x32_bf16 v[180:183], v[116:119], v[52:55], v[180:183]
buffer_load_dwordx4 v7, s[8:11], s40 offen offset:3072 sc1 lds
v_mfma_f32_16x16x32_bf16 v[184:187], v[120:123], v[52:55], v[184:187]
ds_read_b128 a[8:11], v12 offset:1024
v_mfma_f32_16x16x32_bf16 v[188:191], v[124:127], v[52:55], v[188:191]
v_mfma_f32_16x16x32_bf16 v[192:195], v[96:99], v[56:59], v[192:195]
ds_read_b128 a[12:15], v12 offset:1536
v_mfma_f32_16x16x32_bf16 v[196:199], v[100:103], v[56:59], v[196:199]
v_mfma_f32_16x16x32_bf16 v[200:203], v[104:107], v[56:59], v[200:203]
ds_read_b128 a[16:19], v12 offset:2048
v_cvt_pk_bf16_f32 v64, v128, v129
v_mfma_f32_16x16x32_bf16 v[204:207], v[108:111], v[56:59], v[204:207]
v_cvt_pk_bf16_f32 v66, v160, v161
v_cvt_pk_bf16_f32 v65, v130, v131
v_mfma_f32_16x16x32_bf16 v[208:211], v[112:115], v[56:59], v[208:211]
ds_read_b128 a[20:23], v12 offset:2560
v_cvt_pk_bf16_f32 v67, v162, v163
v_mfma_f32_16x16x32_bf16 v[212:215], v[116:119], v[56:59], v[212:215]
v_cvt_pk_bf16_f32 v68, v132, v133
v_cvt_pk_bf16_f32 v70, v164, v165
v_mfma_f32_16x16x32_bf16 v[216:219], v[120:123], v[56:59], v[216:219]
ds_read_b128 a[24:27], v12 offset:3072
v_cvt_pk_bf16_f32 v69, v134, v135
v_mfma_f32_16x16x32_bf16 v[220:223], v[124:127], v[56:59], v[220:223]
v_cvt_pk_bf16_f32 v71, v166, v167
v_mfma_f32_16x16x32_bf16 v[224:227], v[96:99], v[20:23], v[224:227]
ds_read_b128 a[28:31], v12 offset:3584
v_mfma_f32_16x16x32_bf16 v[228:231], v[100:103], v[20:23], v[228:231]
v_permlane16_swap_b32 v64, v66
v_mfma_f32_16x16x32_bf16 v[232:235], v[104:107], v[20:23], v[232:235]
v_permlane16_swap_b32 v65, v67
v_mfma_f32_16x16x32_bf16 v[236:239], v[108:111], v[20:23], v[236:239]
v_cvt_pk_bf16_f32 v72, v136, v137
v_cvt_pk_bf16_f32 v74, v168, v169
v_mfma_f32_16x16x32_bf16 v[240:243], v[112:115], v[20:23], v[240:243]
v_cvt_pk_bf16_f32 v73, v138, v139
v_cvt_pk_bf16_f32 v75, v170, v171
v_mfma_f32_16x16x32_bf16 v[244:247], v[116:119], v[20:23], v[244:247]
v_permlane16_swap_b32 v68, v70
v_mfma_f32_16x16x32_bf16 v[248:251], v[120:123], v[20:23], v[248:251]
v_permlane16_swap_b32 v69, v71
v_mfma_f32_16x16x32_bf16 v[252:255], v[124:127], v[20:23], v[252:255]
v_cvt_pk_bf16_f32 v76, v140, v141
v_cvt_pk_bf16_f32 v78, v172, v173
v_cvt_pk_bf16_f32 v77, v142, v143
v_cvt_pk_bf16_f32 v79, v174, v175
v_permlane16_swap_b32 v72, v74
v_permlane16_swap_b32 v73, v75
v_cvt_pk_bf16_f32 v80, v144, v145
v_cvt_pk_bf16_f32 v82, v176, v177
v_cvt_pk_bf16_f32 v81, v146, v147
v_cvt_pk_bf16_f32 v83, v178, v179
v_permlane16_swap_b32 v76, v78
v_permlane16_swap_b32 v77, v79
v_cvt_pk_bf16_f32 v84, v148, v149
v_cvt_pk_bf16_f32 v86, v180, v181
v_cvt_pk_bf16_f32 v85, v150, v151
v_cvt_pk_bf16_f32 v87, v182, v183
v_permlane16_swap_b32 v80, v82
v_permlane16_swap_b32 v81, v83
v_cvt_pk_bf16_f32 v88, v152, v153
v_cvt_pk_bf16_f32 v90, v184, v185
v_cvt_pk_bf16_f32 v89, v154, v155
v_cvt_pk_bf16_f32 v91, v186, v187
v_permlane16_swap_b32 v84, v86
v_permlane16_swap_b32 v85, v87
v_cvt_pk_bf16_f32 v92, v156, v157
v_cvt_pk_bf16_f32 v94, v188, v189
v_cvt_pk_bf16_f32 v93, v158, v159
v_cvt_pk_bf16_f32 v95, v190, v191
v_permlane16_swap_b32 v88, v90
v_permlane16_swap_b32 v89, v91
v_permlane16_swap_b32 v92, v94
v_permlane16_swap_b32 v93, v95
v_mov_b32 v62, v12
v_add_u32 v63, s46, v33
s_waitcnt lgkmcnt(0)
v_mfma_f32_32x32x16_bf16 v[128:143], a[0:3], v[64:67], 0
s_add_u32 s28, s39, 16384
s_and_b32 s28, s28, 0xffff
s_mov_b32 s41, 32768
s_add_u32 m0, s28, 0
v_cvt_pk_bf16_f32 v96, v192, v193
v_cvt_pk_bf16_f32 v98, v224, v225
v_cvt_pk_bf16_f32 v97, v194, v195
v_mfma_f32_32x32x16_bf16 v[128:143], a[4:7], v[68:71], v[128:143]
buffer_load_dwordx4 v7, s[8:11], s41 offen offset:0 sc1 lds
v_cvt_pk_bf16_f32 v99, v226, v227
v_cvt_pk_bf16_f32 v100, v196, v197
v_cvt_pk_bf16_f32 v102, v228, v229
v_cvt_pk_bf16_f32 v101, v198, v199
v_cvt_pk_bf16_f32 v103, v230, v231
v_mfma_f32_32x32x16_bf16 v[128:143], a[8:11], v[72:75], v[128:143]
buffer_load_dwordx4 v7, s[8:11], s41 offen offset:1024 sc1 lds
v_permlane16_swap_b32 v96, v98
v_permlane16_swap_b32 v97, v99
v_cvt_pk_bf16_f32 v104, v200, v201
v_mfma_f32_32x32x16_bf16 v[128:143], a[12:15], v[76:79], v[128:143]
buffer_load_dwordx4 v7, s[8:11], s41 offen offset:2048 sc1 lds
v_cvt_pk_bf16_f32 v106, v232, v233
v_cvt_pk_bf16_f32 v105, v202, v203
v_cvt_pk_bf16_f32 v107, v234, v235
v_permlane16_swap_b32 v100, v102
v_mfma_f32_32x32x16_bf16 v[128:143], a[16:19], v[80:83], v[128:143]
buffer_load_dwordx4 v7, s[8:11], s41 offen offset:3072 sc1 lds
v_permlane16_swap_b32 v101, v103
v_cvt_pk_bf16_f32 v108, v204, v205
v_cvt_pk_bf16_f32 v110, v236, v237
v_cvt_pk_bf16_f32 v109, v206, v207
v_mfma_f32_32x32x16_bf16 v[128:143], a[20:23], v[84:87], v[128:143]
s_add_u32 s28, s39, 32768
s_and_b32 s28, s28, 0xffff
v_cvt_pk_bf16_f32 v111, v238, v239
v_permlane16_swap_b32 v104, v106
v_permlane16_swap_b32 v105, v107
v_mfma_f32_32x32x16_bf16 v[128:143], a[24:27], v[88:91], v[128:143]
s_mov_b32 s40, 49152
v_cvt_pk_bf16_f32 v112, v208, v209
v_cvt_pk_bf16_f32 v114, v240, v241
v_cvt_pk_bf16_f32 v113, v210, v211
v_cvt_pk_bf16_f32 v115, v242, v243
v_permlane16_swap_b32 v108, v110
v_mfma_f32_32x32x16_bf16 v[128:143], a[28:31], v[92:95], v[128:143]
s_add_u32 m0, s28, 0
v_permlane16_swap_b32 v109, v111
v_cvt_pk_bf16_f32 v116, v212, v213
v_cvt_pk_bf16_f32 v118, v244, v245
v_cvt_pk_bf16_f32 v117, v214, v215
v_cvt_pk_bf16_f32 v119, v246, v247
v_permlane16_swap_b32 v112, v114
v_permlane16_swap_b32 v113, v115
v_cvt_pk_bf16_f32 v120, v216, v217
v_cvt_pk_bf16_f32 v122, v248, v249
v_cvt_pk_bf16_f32 v121, v218, v219
v_cvt_pk_bf16_f32 v123, v250, v251
v_permlane16_swap_b32 v116, v118
v_permlane16_swap_b32 v117, v119
v_cvt_pk_bf16_f32 v124, v220, v221
v_cvt_pk_bf16_f32 v126, v252, v253
v_cvt_pk_bf16_f32 v125, v222, v223
v_cvt_pk_bf16_f32 v127, v254, v255
v_permlane16_swap_b32 v120, v122
v_permlane16_swap_b32 v121, v123
v_permlane16_swap_b32 v124, v126
v_permlane16_swap_b32 v125, v127
v_mfma_f32_32x32x16_bf16 v[160:175], a[0:3], v[96:99], 0
buffer_load_dwordx4 v7, s[8:11], s40 offen offset:0 sc1 lds
ds_read_b128 a[0:3], v62 offset:8192
v_mfma_f32_32x32x16_bf16 v[160:175], a[4:7], v[100:103], v[160:175]
ds_read_b128 a[4:7], v62 offset:8704
v_mfma_f32_32x32x16_bf16 v[160:175], a[8:11], v[104:107], v[160:175]
buffer_load_dwordx4 v7, s[8:11], s40 offen offset:1024 sc1 lds
ds_read_b128 a[8:11], v62 offset:9216
v_max3_f32 v52, v128, v129, v130
v_max3_f32 v53, v131, v132, v133
v_max3_f32 v52, v52, v134, v135
v_mfma_f32_32x32x16_bf16 v[160:175], a[12:15], v[108:111], v[160:175]
ds_read_b128 a[12:15], v62 offset:9728
v_max3_f32 v53, v53, v136, v137
v_max3_f32 v52, v52, v138, v139
v_max3_f32 v53, v53, v140, v141
v_max3_f32 v52, v52, v142, v143
v_max_f32 v52, v52, v53
v_mfma_f32_32x32x16_bf16 v[160:175], a[16:19], v[112:115], v[160:175]
buffer_load_dwordx4 v7, s[8:11], s40 offen offset:2048 sc1 lds
ds_read_b128 a[16:19], v62 offset:10240
v_mov_b32 v53, v52
v_nop
v_mfma_f32_32x32x16_bf16 v[160:175], a[20:23], v[116:119], v[160:175]
s_add_u32 s46, s46, 16384
s_mov_b32 s49, s46
ds_read_b128 a[20:23], v62 offset:10752
v_permlane32_swap_b32 v52, v53
v_max_f32 v52, v52, v53
v_sub_f32 v192, 0, v52
v_sub_f32 v193, 0, v52
v_mfma_f32_32x32x16_bf16 v[160:175], a[24:27], v[120:123], v[160:175]
buffer_load_dwordx4 v7, s[8:11], s40 offen offset:3072 sc1 lds
ds_read_b128 a[24:27], v62 offset:11264
v_sub_f32 v194, 0, v52
v_sub_f32 v195, 0, v52
v_sub_f32 v196, 0, v52
v_mfma_f32_32x32x16_bf16 v[160:175], a[28:31], v[124:127], v[160:175]
s_xor_b32 s48, s95, 265216
ds_read_b128 a[28:31], v62 offset:11776
v_sub_f32 v197, 0, v52
v_sub_f32 v198, 0, v52
v_sub_f32 v199, 0, v52
v_sub_f32 v200, 0, v52
v_sub_f32 v201, 0, v52
v_sub_f32 v202, 0, v52
v_sub_f32 v203, 0, v52
v_sub_f32 v204, 0, v52
v_sub_f32 v205, 0, v52
v_sub_f32 v206, 0, v52
v_sub_f32 v207, 0, v52
s_waitcnt lgkmcnt(0)
s_cmp_eq_u32 s89, 0
v_mfma_f32_32x32x16_bf16 v[144:159], a[0:3], v[64:67], v[192:207]
buffer_store_dwordx4 a[140:143], v7, s[20:23], s48 offen sc1
v_add_u32 v62, s46, v32
v_add_f32 v128, v128, v192
v_add_f32 v129, v129, v192
v_add_f32 v130, v130, v192
v_add_f32 v131, v131, v192
v_mfma_f32_32x32x16_bf16 v[144:159], a[4:7], v[68:71], v[144:159]
s_xor_b32 s48, s95, 327680
v_add_f32 v132, v132, v192
v_add_f32 v133, v133, v192
v_add_f32 v134, v134, v192
v_add_f32 v135, v135, v192
v_mfma_f32_32x32x16_bf16 v[144:159], a[8:11], v[72:75], v[144:159]
s_cmp_eq_u32 s89, 0
s_cbranch_scc1 .Lj0a
.Lj0a_ret:
buffer_store_dwordx4 a[160:163], v7, s[20:23], s48 offen sc1
v_max3_f32 v56, v160, v161, v162
v_max3_f32 v57, v163, v164, v165
v_mfma_f32_32x32x16_bf16 v[144:159], a[12:15], v[76:79], v[144:159]
v_max3_f32 v56, v56, v166, v167
v_max3_f32 v57, v57, v168, v169
v_max3_f32 v56, v56, v170, v171
v_max3_f32 v57, v57, v172, v173
v_max3_f32 v56, v56, v174, v175
v_max_f32 v56, v56, v57
v_mfma_f32_32x32x16_bf16 v[144:159], a[16:19], v[80:83], v[144:159]
s_xor_b32 s48, s95, 328704
v_mov_b32 v57, v56
v_nop
s_nop 0
v_permlane32_swap_b32 v56, v57
v_max_f32 v56, v56, v57
v_sub_f32 v208, 0, v56
v_mfma_f32_32x32x16_bf16 v[144:159], a[20:23], v[84:87], v[144:159]
buffer_store_dwordx4 a[164:167], v7, s[20:23], s48 offen sc1
v_sub_f32 v209, 0, v56
v_sub_f32 v210, 0, v56
v_sub_f32 v211, 0, v56
v_sub_f32 v212, 0, v56
v_sub_f32 v213, 0, v56
v_mfma_f32_32x32x16_bf16 v[144:159], a[24:27], v[88:91], v[144:159]
v_sub_f32 v214, 0, v56
v_sub_f32 v215, 0, v56
v_sub_f32 v216, 0, v56
v_sub_f32 v217, 0, v56
v_sub_f32 v218, 0, v56
v_sub_f32 v219, 0, v56
v_sub_f32 v220, 0, v56
v_sub_f32 v221, 0, v56
v_sub_f32 v222, 0, v56
v_sub_f32 v223, 0, v56
v_mfma_f32_32x32x16_bf16 v[144:159], a[28:31], v[92:95], v[144:159]
s_xor_b32 s48, s95, 329728
s_cmp_eq_u32 s89, 0
s_cbranch_scc1 .Lj0b
.Lj0b_ret:
v_add_f32 v136, v136, v192
v_add_f32 v137, v137, v192
v_add_f32 v138, v138, v192
v_mfma_f32_32x32x16_bf16 v[176:191], a[0:3], v[96:99], v[208:223]
buffer_store_dwordx4 a[168:171], v7, s[20:23], s48 offen sc1
ds_read_b128 a[0:3], v62 offset:0
v_add_f32 v139, v139, v192
v_add_f32 v140, v140, v192
v_add_f32 v141, v141, v192
v_mfma_f32_32x32x16_bf16 v[176:191], a[4:7], v[100:103], v[176:191]
s_xor_b32 s48, s95, 330752
ds_read_b128 a[4:7], v62 offset:512
v_add_f32 v142, v142, v192
v_add_f32 v143, v143, v192
v_exp_f32 v52, v128
v_mfma_f32_32x32x16_bf16 v[176:191], a[8:11], v[104:107], v[176:191]
buffer_store_dwordx4 a[172:175], v7, s[20:23], s48 offen sc1
ds_read_b128 a[8:11], v62 offset:1024
v_exp_f32 v53, v129
v_mov_b32 v60, v52
v_mfma_f32_32x32x16_bf16 v[176:191], a[12:15], v[108:111], v[176:191]
s_cmp_eq_u32 s89, 0
s_cbranch_scc1 .Lj0c
.Lj0c_ret:
ds_read_b128 a[12:15], v62 offset:1536
v_exp_f32 v54, v130
v_mfma_f32_32x32x16_bf16 v[176:191], a[16:19], v[112:115], v[176:191]
s_xor_b32 s48, s95, 393216
ds_read_b128 a[16:19], v62 offset:2048
v_add_f32 v60, v60, v53
v_cvt_pk_bf16_f32 v224, v52, v53
v_exp_f32 v55, v131
v_mfma_f32_32x32x16_bf16 v[176:191], a[20:23], v[116:119], v[176:191]
buffer_store_dwordx4 a[192:195], v7, s[20:23], s48 offen sc1
ds_read_b128 a[20:23], v62 offset:2560
v_add_f32 v60, v60, v54
v_exp_f32 v52, v132
v_mfma_f32_32x32x16_bf16 v[176:191], a[24:27], v[120:123], v[176:191]
ds_read_b128 a[24:27], v62 offset:3072
v_add_f32 v60, v60, v55
v_cvt_pk_bf16_f32 v225, v54, v55
v_exp_f32 v53, v133
v_add_f32 v60, v60, v52
v_mfma_f32_32x32x16_bf16 v[176:191], a[28:31], v[124:127], v[176:191]
ds_read_b128 a[28:31], v62 offset:3584
v_exp_f32 v54, v134
v_add_f32 v60, v60, v53
v_cvt_pk_bf16_f32 v226, v52, v53
v_exp_f32 v55, v135
v_add_f32 v60, v60, v54
v_exp_f32 v52, v136
v_add_f32 v60, v60, v55
v_cvt_pk_bf16_f32 v227, v54, v55
v_exp_f32 v53, v137
v_add_f32 v60, v60, v52
v_exp_f32 v54, v138
v_add_f32 v60, v60, v53
v_cvt_pk_bf16_f32 v228, v52, v53
v_exp_f32 v55, v139
v_add_f32 v60, v60, v54
v_exp_f32 v52, v140
v_add_f32 v60, v60, v55
v_cvt_pk_bf16_f32 v229, v54, v55
v_exp_f32 v53, v141
v_add_f32 v60, v60, v52
v_exp_f32 v54, v142
v_add_f32 v60, v60, v53
v_cvt_pk_bf16_f32 v230, v52, v53
v_exp_f32 v55, v143
v_add_f32 v60, v60, v54
v_add_f32 v60, v60, v55
v_cvt_pk_bf16_f32 v231, v54, v55
s_cmp_eq_u32 s89, 0
s_cselect_b64 vcc, -1, 0
v_cndmask_b32 v14, v208, v14, vcc
v_cndmask_b32 v13, v8, v13, vcc
v_add_f32 v160, v160, v14
v_add_f32 v161, v161, v14
v_add_f32 v162, v162, v14
v_add_f32 v163, v163, v14
v_add_f32 v164, v164, v14
v_add_f32 v165, v165, v14
v_add_f32 v166, v166, v14
v_add_f32 v167, v167, v14
v_add_f32 v168, v168, v14
v_add_f32 v169, v169, v14
v_add_f32 v170, v170, v14
v_add_f32 v171, v171, v14
v_add_f32 v172, v172, v14
v_add_f32 v173, v173, v14
v_add_f32 v174, v174, v14
v_add_f32 v175, v175, v14
v_exp_f32 v56, v160
v_exp_f32 v57, v161
v_mov_b32 v61, v56
v_exp_f32 v58, v162
v_add_f32 v61, v61, v57
v_cvt_pk_bf16_f32 v240, v56, v57
v_exp_f32 v59, v163
v_add_f32 v61, v61, v58
v_exp_f32 v56, v164
v_add_f32 v61, v61, v59
v_cvt_pk_bf16_f32 v241, v58, v59
v_exp_f32 v57, v165
v_add_f32 v61, v61, v56
v_exp_f32 v58, v166
v_add_f32 v61, v61, v57
v_cvt_pk_bf16_f32 v242, v56, v57
v_exp_f32 v59, v167
v_add_f32 v61, v61, v58
v_exp_f32 v56, v168
v_add_f32 v61, v61, v59
v_cvt_pk_bf16_f32 v243, v58, v59
v_exp_f32 v57, v169
v_add_f32 v61, v61, v56
v_exp_f32 v58, v170
v_add_f32 v61, v61, v57
v_cvt_pk_bf16_f32 v244, v56, v57
v_exp_f32 v59, v171
v_add_f32 v61, v61, v58
v_exp_f32 v56, v172
v_add_f32 v61, v61, v59
v_cvt_pk_bf16_f32 v245, v58, v59
v_exp_f32 v57, v173
v_add_f32 v61, v61, v56
v_exp_f32 v58, v174
v_add_f32 v61, v61, v57
v_cvt_pk_bf16_f32 v246, v56, v57
v_exp_f32 v59, v175
v_add_f32 v61, v61, v58
v_add_f32 v61, v61, v59
v_cvt_pk_bf16_f32 v247, v58, v59
s_add_u32 s46, s46, 16384
s_and_b32 s46, s46, 0xffff
s_mov_b32 s44, 65536
s_xor_b32 s50, s46, 0x18000
s_lshl_b32 s28, s89, 2
s_add_u32 s29, s28, s3
s_max_u32 s29, s29, 3
s_sub_u32 s42, s29, 3
s_sub_u32 s56, 57, s28
s_add_u32 s56, s56, s3
s_mov_b32 s57, 2
s_min_u32 s43, s42, s57
s_sub_u32 s55, s43, 1
s_getpc_b64 s[58:59]
.Lpc0:
s_add_u32 s58, s58, .Ljob0-.Lpc0
s_addc_u32 s59, s59, 0
s_waitcnt vmcnt(14) lgkmcnt(0)
s_barrier
v_mfma_f32_32x32x16_bf16 v[128:143], a[0:3], v[64:67], v[192:207]
ds_read_b128 a[32:35], v63 offset:0
v_exp_f32 v52, v144
v_exp_f32 v56, v176
v_add_f32 v60, v60, v52
v_mfma_f32_32x32x16_bf16 v[160:175], a[0:3], v[96:99], v[208:223]
s_xor_b32 s48, s95, 394240
ds_read_b128 a[0:3], v62 offset:8192
v_exp_f32 v53, v145
v_add_f32 v61, v61, v56
v_cmp_gt_i32 s[32:33], 0, v13
v_mfma_f32_32x32x16_bf16 v[128:143], a[4:7], v[68:71], v[128:143]
buffer_store_dwordx4 a[196:199], v7, s[20:23], s48 offen sc1
ds_read_b128 a[36:39], v63 offset:1024
v_exp_f32 v57, v177
v_add_f32 v60, v60, v53
v_cvt_pk_bf16_f32 v232, v52, v53
v_mfma_f32_32x32x16_bf16 v[160:175], a[4:7], v[100:103], v[160:175]
ds_read_b128 a[4:7], v62 offset:8704
v_exp_f32 v54, v146
v_add_f32 v61, v61, v57
v_cvt_pk_bf16_f32 v248, v56, v57
v_nop
v_mfma_f32_32x32x16_bf16 v[128:143], a[8:11], v[72:75], v[128:143]
ds_read_b128 a[40:43], v63 offset:2048
v_exp_f32 v58, v178
v_add_f32 v60, v60, v54
v_cmp_gt_i32 s[34:35], 1, v13
v_cndmask_b32 v36, v14, v34, s[32:33]
v_mfma_f32_32x32x16_bf16 v[160:175], a[8:11], v[104:107], v[160:175]
s_xor_b32 s48, s95, 395264
ds_read_b128 a[8:11], v62 offset:9216
v_exp_f32 v55, v147
v_add_f32 v61, v61, v58
v_cmp_gt_i32 s[32:33], 2, v13
v_mfma_f32_32x32x16_bf16 v[128:143], a[12:15], v[76:79], v[128:143]
buffer_store_dwordx4 a[200:203], v7, s[20:23], s48 offen sc1
ds_read_b128 a[44:47], v63 offset:3072
v_exp_f32 v59, v179
v_add_f32 v60, v60, v55
v_cvt_pk_bf16_f32 v233, v54, v55
v_mfma_f32_32x32x16_bf16 v[160:175], a[12:15], v[108:111], v[160:175]
ds_read_b128 a[12:15], v62 offset:9728
v_exp_f32 v52, v148
v_add_f32 v61, v61, v59
v_cvt_pk_bf16_f32 v249, v58, v59
v_cndmask_b32 v37, v14, v34, s[34:35]
v_mfma_f32_32x32x16_bf16 v[128:143], a[16:19], v[80:83], v[128:143]
ds_read_b128 a[48:51], v63 offset:4096
v_exp_f32 v56, v180
v_add_f32 v60, v60, v52
v_cmp_gt_i32 s[34:35], 3, v13
v_cndmask_b32 v38, v14, v34, s[32:33]
v_mfma_f32_32x32x16_bf16 v[160:175], a[16:19], v[112:115], v[160:175]
s_xor_b32 s48, s95, 396288
ds_read_b128 a[16:19], v62 offset:10240
ds_read_b128 a[56:59], v63 offset:6144
v_exp_f32 v53, v149
v_add_f32 v61, v61, v56
v_mfma_f32_32x32x16_bf16 v[128:143], a[20:23], v[84:87], v[128:143]
buffer_store_dwordx4 a[204:207], v7, s[20:23], s48 offen sc1
ds_read_b128 a[52:55], v63 offset:5120
v_exp_f32 v57, v181
v_add_f32 v60, v60, v53
v_cvt_pk_bf16_f32 v234, v52, v53
v_mfma_f32_32x32x16_bf16 v[160:175], a[20:23], v[116:119], v[160:175]
ds_read_b128 a[20:23], v62 offset:10752
ds_read_b128 a[60:63], v63 offset:7168
v_exp_f32 v54, v150
v_add_f32 v61, v61, v57
v_cvt_pk_bf16_f32 v250, v56, v57
v_mfma_f32_32x32x16_bf16 v[128:143], a[24:27], v[88:91], v[128:143]
v_exp_f32 v58, v182
v_add_f32 v60, v60, v54
v_cmp_gt_i32 s[32:33], 8, v13
v_cndmask_b32 v39, v14, v34, s[34:35]
v_cmp_gt_i32 s[34:35], 9, v13
v_mfma_f32_32x32x16_bf16 v[160:175], a[24:27], v[120:123], v[160:175]
s_xor_b32 s48, s95, 458752
ds_read_b128 a[24:27], v62 offset:11264
v_exp_f32 v55, v151
v_add_f32 v61, v61, v58
v_cndmask_b32 v40, v14, v34, s[32:33]
v_mfma_f32_32x32x16_bf16 v[128:143], a[28:31], v[92:95], v[128:143]
buffer_store_dwordx4 a[224:227], v7, s[20:23], s48 offen sc1
v_exp_f32 v59, v183
v_add_f32 v60, v60, v55
v_cvt_pk_bf16_f32 v235, v54, v55
v_cmp_gt_i32 s[32:33], 10, v13
v_mfma_f32_32x32x16_bf16 v[160:175], a[28:31], v[124:127], v[160:175]
s_waitcnt lgkmcnt(1)
ds_read_b128 a[28:31], v62 offset:11776
v_exp_f32 v52, v152
v_add_f32 v61, v61, v59
v_cvt_pk_bf16_f32 v251, v58, v59
v_mfma_f32_32x32x16_bf16 a[64:79], a[32:35], v[224:227], 0
v_exp_f32 v56, v184
v_add_f32 v60, v60, v52
v_exp_f32 v53, v153
v_add_f32 v61, v61, v56
v_mfma_f32_32x32x16_bf16 a[176:191], a[32:35], v[240:243], 0
s_xor_b32 s48, s95, 459776
ds_read_b128 a[32:35], v63 offset:8192
v_add_u32 v62, s46, v32
v_exp_f32 v57, v185
v_add_f32 v60, v60, v53
v_cvt_pk_bf16_f32 v236, v52, v53
v_mfma_f32_32x32x16_bf16 a[80:95], a[36:39], v[224:227], 0
buffer_store_dwordx4 a[228:231], v7, s[20:23], s48 offen sc1
v_exp_f32 v54, v154
v_add_f32 v61, v61, v57
v_cvt_pk_bf16_f32 v252, v56, v57
v_cndmask_b32 v41, v14, v34, s[34:35]
v_mfma_f32_32x32x16_bf16 a[208:223], a[36:39], v[240:243], 0
ds_read_b128 a[36:39], v63 offset:9216
v_exp_f32 v58, v186
v_add_f32 v60, v60, v54
v_cmp_gt_i32 s[34:35], 11, v13
v_cndmask_b32 v42, v14, v34, s[32:33]
v_mfma_f32_32x32x16_bf16 a[112:127], a[40:43], v[224:227], 0
v_exp_f32 v55, v155
v_add_f32 v61, v61, v58
v_cmp_gt_i32 s[32:33], 16, v13
v_cndmask_b32 v43, v14, v34, s[34:35]
v_cmp_gt_i32 s[34:35], 17, v13
v_mfma_f32_32x32x16_bf16 a[240:255], a[40:43], v[240:243], 0
.Lpad10:
s_cbranch_execz .Ltch2
s_xor_b32 s48, s95, 460800
ds_read_b128 a[40:43], v63 offset:10240
v_exp_f32 v59, v187
v_add_f32 v60, v60, v55
v_cvt_pk_bf16_f32 v237, v54, v55
v_mfma_f32_32x32x16_bf16 a[144:159], a[44:47], v[224:227], 0
buffer_store_dwordx4 a[232:235], v7, s[20:23], s48 offen sc1
v_exp_f32 v52, v156
v_add_f32 v61, v61, v59
v_cvt_pk_bf16_f32 v253, v58, v59
v_cndmask_b32 v44, v14, v34, s[32:33]
v_mfma_f32_32x32x16_bf16 a[96:111], a[44:47], v[240:243], 0
ds_read_b128 a[44:47], v63 offset:11264
v_exp_f32 v56, v188
v_add_f32 v60, v60, v52
v_cmp_gt_i32 s[32:33], 18, v13
v_cndmask_b32 v45, v14, v34, s[34:35]
v_mfma_f32_32x32x16_bf16 a[64:79], a[48:51], v[228:231], a[64:79]
v_exp_f32 v53, v157
v_add_f32 v61, v61, v56
v_cmp_gt_i32 s[34:35], 19, v13
v_cndmask_b32 v46, v14, v34, s[32:33]
v_cmp_gt_i32 s[32:33], 24, v13
v_mfma_f32_32x32x16_bf16 a[176:191], a[48:51], v[244:247], a[176:191]
s_xor_b32 s48, s95, 461824
ds_read_b128 a[48:51], v63 offset:12288
v_exp_f32 v57, v189
v_add_f32 v60, v60, v53
v_cvt_pk_bf16_f32 v238, v52, v53
v_mfma_f32_32x32x16_bf16 a[80:95], a[52:55], v[228:231], a[80:95]
buffer_store_dwordx4 a[236:239], v7, s[20:23], s48 offen sc1
v_exp_f32 v54, v158
v_add_f32 v61, v61, v57
v_cvt_pk_bf16_f32 v254, v56, v57
v_cndmask_b32 v47, v14, v34, s[34:35]
v_mfma_f32_32x32x16_bf16 a[208:223], a[52:55], v[244:247], a[208:223]
ds_read_b128 a[52:55], v63 offset:13312
v_exp_f32 v58, v190
v_add_f32 v60, v60, v54
v_cmp_gt_i32 s[34:35], 25, v13
v_cndmask_b32 v48, v14, v34, s[32:33]
v_mfma_f32_32x32x16_bf16 a[112:127], a[56:59], v[228:231], a[112:127]
v_exp_f32 v55, v159
v_add_f32 v61, v61, v58
v_cmp_gt_i32 s[32:33], 26, v13
v_cndmask_b32 v49, v14, v34, s[34:35]
v_cmp_gt_i32 s[34:35], 27, v13
v_mfma_f32_32x32x16_bf16 a[240:255], a[56:59], v[244:247], a[240:255]
ds_read_b128 a[56:59], v63 offset:14336
v_exp_f32 v59, v191
v_add_f32 v60, v60, v55
v_cvt_pk_bf16_f32 v239, v54, v55
v_cndmask_b32 v50, v14, v34, s[32:33]
v_mfma_f32_32x32x16_bf16 a[144:159], a[60:63], v[228:231], a[144:159]
v_add_f32 v61, v61, v59
v_cvt_pk_bf16_f32 v255, v58, v59
v_nop
v_cndmask_b32 v51, v14, v34, s[34:35]
v_cmp_gt_i32 s[32:33], 0, v8
v_nop
v_mfma_f32_32x32x16_bf16 a[96:111], a[60:63], v[244:247], a[96:111]
s_waitcnt vmcnt(17) lgkmcnt(7)
s_barrier
ds_read_b128 a[60:63], v63 offset:15360
v_cmp_gt_i32 s[34:35], 1, v8
v_cndmask_b32 v16, v192, v34, s[32:33]
v_cmp_gt_i32 s[32:33], 2, v8
v_cndmask_b32 v17, v193, v34, s[34:35]
v_cmp_gt_i32 s[34:35], 3, v8
v_cndmask_b32 v18, v194, v34, s[32:33]
v_cmp_gt_i32 s[32:33], 8, v8
v_cndmask_b32 v19, v195, v34, s[34:35]
v_cmp_gt_i32 s[34:35], 9, v8
v_cndmask_b32 v20, v196, v34, s[32:33]
v_cmp_gt_i32 s[32:33], 10, v8
v_cndmask_b32 v21, v197, v34, s[34:35]
v_cmp_gt_i32 s[34:35], 11, v8
v_cndmask_b32 v22, v198, v34, s[32:33]
v_cmp_gt_i32 s[32:33], 16, v8
v_cndmask_b32 v23, v199, v34, s[34:35]
v_cmp_gt_i32 s[34:35], 17, v8
v_cndmask_b32 v24, v200, v34, s[32:33]
v_cmp_gt_i32 s[32:33], 18, v8
v_cndmask_b32 v25, v201, v34, s[34:35]
v_cmp_gt_i32 s[34:35], 19, v8
v_cndmask_b32 v26, v202, v34, s[32:33]
v_cmp_gt_i32 s[32:33], 24, v8
v_cndmask_b32 v27, v203, v34, s[34:35]
v_cmp_gt_i32 s[34:35], 25, v8
v_cndmask_b32 v28, v204, v34, s[32:33]
v_cmp_gt_i32 s[32:33], 26, v8
v_cndmask_b32 v29, v205, v34, s[34:35]
v_cmp_gt_i32 s[34:35], 27, v8
v_cndmask_b32 v30, v206, v34, s[32:33]
v_nop
v_cndmask_b32 v31, v207, v34, s[34:35]
s_cmp_eq_u32 s29, 3
s_cbranch_scc1 .LT1
.LD1:
v_mfma_f32_32x32x16_bf16 v[144:159], a[0:3], v[64:67], v[192:207]
s_cmp_lt_u32 s44, s54
s_cselect_b32 m0, s50, s27
v_add_u32 v63, s49, v33
v_exp_f32 v52, v128
v_exp_f32 v56, v160
v_mfma_f32_32x32x16_bf16 v[176:191], a[0:3], v[96:99], v[208:223]
ds_read_b128 a[0:3], v62 offset:0
v_add_f32 v60, v60, v52
v_exp_f32 v53, v129
v_add_f32 v61, v61, v56
v_mfma_f32_32x32x16_bf16 v[144:159], a[4:7], v[68:71], v[144:159]
v_exp_f32 v57, v161
v_add_f32 v60, v60, v53
v_cvt_pk_bf16_f32 v224, v52, v53
v_mfma_f32_32x32x16_bf16 v[176:191], a[4:7], v[100:103], v[176:191]
ds_read_b128 a[4:7], v62 offset:512
v_exp_f32 v54, v130
v_add_f32 v61, v61, v57
v_cvt_pk_bf16_f32 v240, v56, v57
v_mfma_f32_32x32x16_bf16 v[144:159], a[8:11], v[72:75], v[144:159]
s_mov_b32 s49, s46
buffer_load_dwordx4 v7, s[4:7], s44 offen offset:0 sc1 lds
v_exp_f32 v58, v162
v_add_f32 v60, v60, v54
v_mfma_f32_32x32x16_bf16 v[176:191], a[8:11], v[104:107], v[176:191]
ds_read_b128 a[8:11], v62 offset:1024
v_exp_f32 v55, v131
v_add_f32 v61, v61, v58
v_mfma_f32_32x32x16_bf16 v[144:159], a[12:15], v[76:79], v[144:159]
v_exp_f32 v59, v163
v_add_f32 v60, v60, v55
v_cvt_pk_bf16_f32 v225, v54, v55
v_mfma_f32_32x32x16_bf16 v[176:191], a[12:15], v[108:111], v[176:191]
ds_read_b128 a[12:15], v62 offset:1536
v_exp_f32 v52, v132
v_add_f32 v61, v61, v59
v_cvt_pk_bf16_f32 v241, v58, v59
v_mfma_f32_32x32x16_bf16 v[144:159], a[16:19], v[80:83], v[144:159]
s_add_u32 s46, s46, 16384
v_exp_f32 v56, v164
v_add_f32 v60, v60, v52
v_mfma_f32_32x32x16_bf16 v[176:191], a[16:19], v[112:115], v[176:191]
ds_read_b128 a[16:19], v62 offset:2048
v_exp_f32 v53, v133
v_add_f32 v61, v61, v56
v_mfma_f32_32x32x16_bf16 v[144:159], a[20:23], v[84:87], v[144:159]
buffer_load_dwordx4 v7, s[4:7], s44 offen offset:1024 sc1 lds
v_exp_f32 v57, v165
v_add_f32 v60, v60, v53
v_cvt_pk_bf16_f32 v226, v52, v53
v_mfma_f32_32x32x16_bf16 v[176:191], a[20:23], v[116:119], v[176:191]
ds_read_b128 a[20:23], v62 offset:2560
v_exp_f32 v54, v134
v_add_f32 v61, v61, v57
v_cvt_pk_bf16_f32 v242, v56, v57
v_mfma_f32_32x32x16_bf16 v[144:159], a[24:27], v[88:91], v[144:159]
v_exp_f32 v58, v166
v_add_f32 v60, v60, v54
v_mfma_f32_32x32x16_bf16 v[176:191], a[24:27], v[120:123], v[176:191]
ds_read_b128 a[24:27], v62 offset:3072
v_exp_f32 v55, v135
v_add_f32 v61, v61, v58
v_mfma_f32_32x32x16_bf16 v[144:159], a[28:31], v[92:95], v[144:159]
v_exp_f32 v59, v167
v_add_f32 v60, v60, v55
v_cvt_pk_bf16_f32 v227, v54, v55
v_mfma_f32_32x32x16_bf16 v[176:191], a[28:31], v[124:127], v[176:191]
s_waitcnt lgkmcnt(7)
ds_read_b128 a[28:31], v62 offset:3584
v_exp_f32 v52, v136
v_add_f32 v61, v61, v59
v_cvt_pk_bf16_f32 v243, v58, v59
v_mfma_f32_32x32x16_bf16 a[64:79], a[32:35], v[232:235], a[64:79]
buffer_load_dwordx4 v7, s[4:7], s44 offen offset:2048 sc1 lds
v_exp_f32 v56, v168
v_add_f32 v60, v60, v52
v_exp_f32 v53, v137
v_mfma_f32_32x32x16_bf16 a[176:191], a[32:35], v[248:251], a[176:191]
ds_read_b128 a[32:35], v63 offset:0
v_add_f32 v61, v61, v56
v_exp_f32 v57, v169
v_add_f32 v60, v60, v53
v_cvt_pk_bf16_f32 v228, v52, v53
v_mfma_f32_32x32x16_bf16 a[80:95], a[36:39], v[232:235], a[80:95]
s_and_b32 s46, s46, 0xffff
v_exp_f32 v54, v138
v_add_f32 v61, v61, v57
v_cvt_pk_bf16_f32 v244, v56, v57
v_mfma_f32_32x32x16_bf16 a[208:223], a[36:39], v[248:251], a[208:223]
ds_read_b128 a[36:39], v63 offset:1024
v_exp_f32 v58, v170
v_add_f32 v60, v60, v54
v_mfma_f32_32x32x16_bf16 a[112:127], a[40:43], v[232:235], a[112:127]
v_exp_f32 v55, v139
v_add_f32 v61, v61, v58
v_mfma_f32_32x32x16_bf16 a[240:255], a[40:43], v[248:251], a[240:255]
ds_read_b128 a[40:43], v63 offset:2048
v_exp_f32 v59, v171
v_add_f32 v60, v60, v55
v_cvt_pk_bf16_f32 v229, v54, v55
v_mfma_f32_32x32x16_bf16 a[144:159], a[44:47], v[232:235], a[144:159]
buffer_load_dwordx4 v7, s[4:7], s44 offen offset:3072 sc1 lds
v_exp_f32 v52, v140
v_add_f32 v61, v61, v59
v_cvt_pk_bf16_f32 v245, v58, v59
v_mfma_f32_32x32x16_bf16 a[96:111], a[44:47], v[248:251], a[96:111]
ds_read_b128 a[44:47], v63 offset:3072
v_exp_f32 v56, v172
v_add_f32 v60, v60, v52
v_mfma_f32_32x32x16_bf16 a[64:79], a[48:51], v[236:239], a[64:79]
v_exp_f32 v53, v141
v_add_f32 v61, v61, v56
v_mfma_f32_32x32x16_bf16 a[176:191], a[48:51], v[252:255], a[176:191]
ds_read_b128 a[48:51], v63 offset:4096
v_exp_f32 v57, v173
v_add_f32 v60, v60, v53
v_cvt_pk_bf16_f32 v230, v52, v53
v_mfma_f32_32x32x16_bf16 a[80:95], a[52:55], v[236:239], a[80:95]
s_sub_u32 s55, s55, 1
v_exp_f32 v54, v142
v_add_f32 v61, v61, v57
v_cvt_pk_bf16_f32 v246, v56, v57
v_mfma_f32_32x32x16_bf16 a[208:223], a[52:55], v[252:255], a[208:223]
ds_read_b128 a[52:55], v63 offset:5120
v_exp_f32 v58, v174
v_add_f32 v60, v60, v54
v_mfma_f32_32x32x16_bf16 a[112:127], a[56:59], v[236:239], a[112:127]
v_exp_f32 v55, v143
v_add_f32 v61, v61, v58
v_mfma_f32_32x32x16_bf16 a[240:255], a[56:59], v[252:255], a[240:255]
ds_read_b128 a[56:59], v63 offset:6144
v_exp_f32 v59, v175
v_add_f32 v60, v60, v55
v_cvt_pk_bf16_f32 v231, v54, v55
v_mfma_f32_32x32x16_bf16 a[144:159], a[60:63], v[236:239], a[144:159]
v_add_f32 v61, v61, v59
v_cvt_pk_bf16_f32 v247, v58, v59
v_mfma_f32_32x32x16_bf16 a[96:111], a[60:63], v[252:255], a[96:111]
s_waitcnt lgkmcnt(7)
ds_read_b128 a[60:63], v63 offset:7168
s_cbranch_scc1 .Lev_d1
.LD0:
v_mfma_f32_32x32x16_bf16 v[128:143], a[0:3], v[64:67], v[192:207]
s_cmp_lt_u32 s44, s45
s_cselect_b32 m0, s46, s27
v_exp_f32 v52, v144
v_exp_f32 v56, v176
v_add_f32 v60, v60, v52
v_mfma_f32_32x32x16_bf16 v[160:175], a[0:3], v[96:99], v[208:223]
ds_read_b128 a[0:3], v62 offset:8192
v_exp_f32 v53, v145
v_add_f32 v61, v61, v56
v_mfma_f32_32x32x16_bf16 v[128:143], a[4:7], v[68:71], v[128:143]
v_exp_f32 v57, v177
v_add_f32 v60, v60, v53
v_cvt_pk_bf16_f32 v232, v52, v53
v_mfma_f32_32x32x16_bf16 v[160:175], a[4:7], v[100:103], v[160:175]
ds_read_b128 a[4:7], v62 offset:8704
v_exp_f32 v54, v146
v_add_f32 v61, v61, v57
v_cvt_pk_bf16_f32 v248, v56, v57
v_mfma_f32_32x32x16_bf16 v[128:143], a[8:11], v[72:75], v[128:143]
buffer_load_dwordx4 v7, s[8:11], s44 offen offset:0 sc1 lds
v_exp_f32 v58, v178
v_add_f32 v60, v60, v54
v_mfma_f32_32x32x16_bf16 v[160:175], a[8:11], v[104:107], v[160:175]
ds_read_b128 a[8:11], v62 offset:9216
v_exp_f32 v55, v147
v_add_f32 v61, v61, v58
v_mfma_f32_32x32x16_bf16 v[128:143], a[12:15], v[76:79], v[128:143]
v_exp_f32 v59, v179
v_add_f32 v60, v60, v55
v_cvt_pk_bf16_f32 v233, v54, v55
v_mfma_f32_32x32x16_bf16 v[160:175], a[12:15], v[108:111], v[160:175]
ds_read_b128 a[12:15], v62 offset:9728
v_exp_f32 v52, v148
v_add_f32 v61, v61, v59
v_cvt_pk_bf16_f32 v249, v58, v59
v_mfma_f32_32x32x16_bf16 v[128:143], a[16:19], v[80:83], v[128:143]
s_xor_b32 s50, s46, 0x18000
v_exp_f32 v56, v180
v_add_f32 v60, v60, v52
v_mfma_f32_32x32x16_bf16 v[160:175], a[16:19], v[112:115], v[160:175]
ds_read_b128 a[16:19], v62 offset:10240
v_exp_f32 v53, v149
v_add_f32 v61, v61, v56
v_mfma_f32_32x32x16_bf16 v[128:143], a[20:23], v[84:87], v[128:143]
buffer_load_dwordx4 v7, s[8:11], s44 offen offset:1024 sc1 lds
v_exp_f32 v57, v181
v_add_f32 v60, v60, v53
v_cvt_pk_bf16_f32 v234, v52, v53
v_mfma_f32_32x32x16_bf16 v[160:175], a[20:23], v[116:119], v[160:175]
ds_read_b128 a[20:23], v62 offset:10752
v_exp_f32 v54, v150
v_add_f32 v61, v61, v57
v_cvt_pk_bf16_f32 v250, v56, v57
v_mfma_f32_32x32x16_bf16 v[128:143], a[24:27], v[88:91], v[128:143]
v_exp_f32 v58, v182
v_add_f32 v60, v60, v54
v_mfma_f32_32x32x16_bf16 v[160:175], a[24:27], v[120:123], v[160:175]
ds_read_b128 a[24:27], v62 offset:11264
v_exp_f32 v55, v151
v_add_f32 v61, v61, v58
v_mfma_f32_32x32x16_bf16 v[128:143], a[28:31], v[92:95], v[128:143]
v_exp_f32 v59, v183
v_add_f32 v60, v60, v55
v_cvt_pk_bf16_f32 v235, v54, v55
v_mfma_f32_32x32x16_bf16 v[160:175], a[28:31], v[124:127], v[160:175]
s_waitcnt lgkmcnt(7)
ds_read_b128 a[28:31], v62 offset:11776
v_exp_f32 v52, v152
v_add_f32 v61, v61, v59
v_cvt_pk_bf16_f32 v251, v58, v59
v_mfma_f32_32x32x16_bf16 a[64:79], a[32:35], v[224:227], a[64:79]
buffer_load_dwordx4 v7, s[8:11], s44 offen offset:2048 sc1 lds
v_exp_f32 v56, v184
v_add_f32 v60, v60, v52
v_exp_f32 v53, v153
v_mfma_f32_32x32x16_bf16 a[176:191], a[32:35], v[240:243], a[176:191]
ds_read_b128 a[32:35], v63 offset:8192
v_add_u32 v62, s46, v32
v_add_f32 v61, v61, v56
v_exp_f32 v57, v185
v_add_f32 v60, v60, v53
v_mfma_f32_32x32x16_bf16 a[80:95], a[36:39], v[224:227], a[80:95]
v_cvt_pk_bf16_f32 v236, v52, v53
v_exp_f32 v54, v154
v_add_f32 v61, v61, v57
v_cvt_pk_bf16_f32 v252, v56, v57
v_mfma_f32_32x32x16_bf16 a[208:223], a[36:39], v[240:243], a[208:223]
ds_read_b128 a[36:39], v63 offset:9216
v_exp_f32 v58, v186
v_add_f32 v60, v60, v54
v_mfma_f32_32x32x16_bf16 a[112:127], a[40:43], v[224:227], a[112:127]
v_exp_f32 v55, v155
v_add_f32 v61, v61, v58
v_mfma_f32_32x32x16_bf16 a[240:255], a[40:43], v[240:243], a[240:255]
ds_read_b128 a[40:43], v63 offset:10240
v_exp_f32 v59, v187
v_add_f32 v60, v60, v55
v_cvt_pk_bf16_f32 v237, v54, v55
v_mfma_f32_32x32x16_bf16 a[144:159], a[44:47], v[224:227], a[144:159]
buffer_load_dwordx4 v7, s[8:11], s44 offen offset:3072 sc1 lds
v_exp_f32 v52, v156
v_add_f32 v61, v61, v59
v_cvt_pk_bf16_f32 v253, v58, v59
v_mfma_f32_32x32x16_bf16 a[96:111], a[44:47], v[240:243], a[96:111]
ds_read_b128 a[44:47], v63 offset:11264
v_exp_f32 v56, v188
v_add_f32 v60, v60, v52
v_mfma_f32_32x32x16_bf16 a[64:79], a[48:51], v[228:231], a[64:79]
s_add_u32 s44, s44, 16384
v_exp_f32 v53, v157
v_add_f32 v61, v61, v56
v_mfma_f32_32x32x16_bf16 a[176:191], a[48:51], v[244:247], a[176:191]
ds_read_b128 a[48:51], v63 offset:12288
v_exp_f32 v57, v189
v_add_f32 v60, v60, v53
v_cvt_pk_bf16_f32 v238, v52, v53
v_mfma_f32_32x32x16_bf16 a[80:95], a[52:55], v[228:231], a[80:95]
s_sub_u32 s55, s55, 1
v_exp_f32 v54, v158
v_add_f32 v61, v61, v57
v_cvt_pk_bf16_f32 v254, v56, v57
v_mfma_f32_32x32x16_bf16 a[208:223], a[52:55], v[244:247], a[208:223]
ds_read_b128 a[52:55], v63 offset:13312
v_exp_f32 v58, v190
v_add_f32 v60, v60, v54
v_mfma_f32_32x32x16_bf16 a[112:127], a[56:59], v[228:231], a[112:127]
v_exp_f32 v55, v159
v_add_f32 v61, v61, v58
v_mfma_f32_32x32x16_bf16 a[240:255], a[56:59], v[244:247], a[240:255]
ds_read_b128 a[56:59], v63 offset:14336
v_exp_f32 v59, v191
v_add_f32 v60, v60, v55
v_cvt_pk_bf16_f32 v239, v54, v55
v_mfma_f32_32x32x16_bf16 a[144:159], a[60:63], v[228:231], a[144:159]
v_add_f32 v61, v61, v59
v_cvt_pk_bf16_f32 v255, v58, v59
v_mfma_f32_32x32x16_bf16 a[96:111], a[60:63], v[244:247], a[96:111]
s_waitcnt vmcnt(8) lgkmcnt(7)
s_barrier
ds_read_b128 a[60:63], v63 offset:15360
s_cbranch_scc0 .LD1
s_cmp_eq_u32 s43, s42
s_cbranch_scc1 .LT1
s_swappc_b64 s[30:31], s[58:59]
.Lev_d0_r:
s_branch .LD1
.LT0:
v_mfma_f32_32x32x16_bf16 v[128:143], a[0:3], v[64:67], v[192:207]
s_cmp_lt_u32 s44, s45
s_cselect_b32 m0, s46, s27
v_exp_f32 v52, v144
v_exp_f32 v56, v176
v_add_f32 v60, v60, v52
v_mfma_f32_32x32x16_bf16 v[160:175], a[0:3], v[96:99], v[36:51]
s_add_u32 s43, s42, 4
s_mov_b32 s42, s56
ds_read_b128 a[0:3], v62 offset:8192
v_exp_f32 v53, v145
v_add_f32 v61, v61, v56
v_mfma_f32_32x32x16_bf16 v[128:143], a[4:7], v[68:71], v[128:143]
s_max_u32 s55, s57, s43
s_min_u32 s55, s55, s42
s_sub_u32 s93, s55, s43
v_exp_f32 v57, v177
v_add_f32 v60, v60, v53
v_cvt_pk_bf16_f32 v232, v52, v53
v_mfma_f32_32x32x16_bf16 v[160:175], a[4:7], v[100:103], v[160:175]
s_mov_b32 s43, s55
ds_read_b128 a[4:7], v62 offset:8704
v_exp_f32 v54, v146
v_add_f32 v61, v61, v57
v_cvt_pk_bf16_f32 v248, v56, v57
v_mfma_f32_32x32x16_bf16 v[128:143], a[8:11], v[72:75], v[128:143]
s_mov_b32 s55, s93
buffer_load_dwordx4 v7, s[8:11], s44 offen offset:0 sc1 lds
v_exp_f32 v58, v178
v_add_f32 v60, v60, v54
v_mfma_f32_32x32x16_bf16 v[160:175], a[8:11], v[104:107], v[160:175]
ds_read_b128 a[8:11], v62 offset:9216
v_exp_f32 v55, v147
v_add_f32 v61, v61, v58
v_mfma_f32_32x32x16_bf16 v[128:143], a[12:15], v[76:79], v[128:143]
v_exp_f32 v59, v179
v_add_f32 v60, v60, v55
v_cvt_pk_bf16_f32 v233, v54, v55
v_mfma_f32_32x32x16_bf16 v[160:175], a[12:15], v[108:111], v[160:175]
ds_read_b128 a[12:15], v62 offset:9728
v_exp_f32 v52, v148
v_add_f32 v61, v61, v59
v_cvt_pk_bf16_f32 v249, v58, v59
v_mfma_f32_32x32x16_bf16 v[128:143], a[16:19], v[80:83], v[128:143]
s_xor_b32 s50, s46, 0x18000
v_exp_f32 v56, v180
v_add_f32 v60, v60, v52
v_mfma_f32_32x32x16_bf16 v[160:175], a[16:19], v[112:115], v[160:175]
ds_read_b128 a[16:19], v62 offset:10240
v_exp_f32 v53, v149
v_add_f32 v61, v61, v56
v_mfma_f32_32x32x16_bf16 v[128:143], a[20:23], v[84:87], v[128:143]
buffer_load_dwordx4 v7, s[8:11], s44 offen offset:1024 sc1 lds
v_exp_f32 v57, v181
v_add_f32 v60, v60, v53
v_cvt_pk_bf16_f32 v234, v52, v53
v_mfma_f32_32x32x16_bf16 v[160:175], a[20:23], v[116:119], v[160:175]
ds_read_b128 a[20:23], v62 offset:10752
v_exp_f32 v54, v150
v_add_f32 v61, v61, v57
v_cvt_pk_bf16_f32 v250, v56, v57
v_mfma_f32_32x32x16_bf16 v[128:143], a[24:27], v[88:91], v[128:143]
v_exp_f32 v58, v182
v_add_f32 v60, v60, v54
v_mfma_f32_32x32x16_bf16 v[160:175], a[24:27], v[120:123], v[160:175]
ds_read_b128 a[24:27], v62 offset:11264
v_exp_f32 v55, v151
v_add_f32 v61, v61, v58
v_mfma_f32_32x32x16_bf16 v[128:143], a[28:31], v[92:95], v[128:143]
v_exp_f32 v59, v183
v_add_f32 v60, v60, v55
v_cvt_pk_bf16_f32 v235, v54, v55
v_mfma_f32_32x32x16_bf16 v[160:175], a[28:31], v[124:127], v[160:175]
s_waitcnt lgkmcnt(7)
ds_read_b128 a[28:31], v62 offset:11776
v_exp_f32 v52, v152
v_add_f32 v61, v61, v59
v_cvt_pk_bf16_f32 v251, v58, v59
v_mfma_f32_32x32x16_bf16 a[64:79], a[32:35], v[224:227], a[64:79]
buffer_load_dwordx4 v7, s[8:11], s44 offen offset:2048 sc1 lds
v_exp_f32 v56, v184
v_add_f32 v60, v60, v52
v_exp_f32 v53, v153
v_mfma_f32_32x32x16_bf16 a[176:191], a[32:35], v[240:243], a[176:191]
ds_read_b128 a[32:35], v63 offset:8192
v_add_u32 v62, s46, v32
v_add_f32 v61, v61, v56
v_exp_f32 v57, v185
v_add_f32 v60, v60, v53
v_mfma_f32_32x32x16_bf16 a[80:95], a[36:39], v[224:227], a[80:95]
v_cvt_pk_bf16_f32 v236, v52, v53
v_exp_f32 v54, v154
v_add_f32 v61, v61, v57
v_cvt_pk_bf16_f32 v252, v56, v57
v_mfma_f32_32x32x16_bf16 a[208:223], a[36:39], v[240:243], a[208:223]
ds_read_b128 a[36:39], v63 offset:9216
v_exp_f32 v58, v186
v_add_f32 v60, v60, v54
v_mfma_f32_32x32x16_bf16 a[112:127], a[40:43], v[224:227], a[112:127]
v_exp_f32 v55, v155
v_add_f32 v61, v61, v58
v_mfma_f32_32x32x16_bf16 a[240:255], a[40:43], v[240:243], a[240:255]
ds_read_b128 a[40:43], v63 offset:10240
v_exp_f32 v59, v187
v_add_f32 v60, v60, v55
v_cvt_pk_bf16_f32 v237, v54, v55
v_mfma_f32_32x32x16_bf16 a[144:159], a[44:47], v[224:227], a[144:159]
buffer_load_dwordx4 v7, s[8:11], s44 offen offset:3072 sc1 lds
v_exp_f32 v52, v156
v_add_f32 v61, v61, v59
v_cvt_pk_bf16_f32 v253, v58, v59
v_mfma_f32_32x32x16_bf16 a[96:111], a[44:47], v[240:243], a[96:111]
ds_read_b128 a[44:47], v63 offset:11264
v_exp_f32 v56, v188
v_add_f32 v60, v60, v52
v_mfma_f32_32x32x16_bf16 a[64:79], a[48:51], v[228:231], a[64:79]
s_add_u32 s44, s44, 16384
v_exp_f32 v53, v157
v_add_f32 v61, v61, v56
v_mfma_f32_32x32x16_bf16 a[176:191], a[48:51], v[244:247], a[176:191]
ds_read_b128 a[48:51], v63 offset:12288
v_exp_f32 v57, v189
v_add_f32 v60, v60, v53
v_cvt_pk_bf16_f32 v238, v52, v53
v_mfma_f32_32x32x16_bf16 a[80:95], a[52:55], v[228:231], a[80:95]
v_exp_f32 v54, v158
v_add_f32 v61, v61, v57
v_cvt_pk_bf16_f32 v254, v56, v57
v_mfma_f32_32x32x16_bf16 a[208:223], a[52:55], v[244:247], a[208:223]
ds_read_b128 a[52:55], v63 offset:13312
v_exp_f32 v58, v190
v_add_f32 v60, v60, v54
v_mfma_f32_32x32x16_bf16 a[112:127], a[56:59], v[228:231], a[112:127]
v_exp_f32 v55, v159
v_add_f32 v61, v61, v58
v_mfma_f32_32x32x16_bf16 a[240:255], a[56:59], v[244:247], a[240:255]
ds_read_b128 a[56:59], v63 offset:14336
v_exp_f32 v59, v191
v_add_f32 v60, v60, v55
v_cvt_pk_bf16_f32 v239, v54, v55
v_mfma_f32_32x32x16_bf16 a[144:159], a[60:63], v[228:231], a[144:159]
v_add_f32 v61, v61, v59
v_cvt_pk_bf16_f32 v255, v58, v59
v_mfma_f32_32x32x16_bf16 a[96:111], a[60:63], v[244:247], a[96:111]
s_waitcnt vmcnt(8) lgkmcnt(7)
s_barrier
ds_read_b128 a[60:63], v63 offset:15360
v_mfma_f32_32x32x16_bf16 v[144:159], a[0:3], v[64:67], v[192:207]
s_cmp_lt_u32 s44, s54
s_cselect_b32 m0, s50, s27
ds_read_b128 a[0:3], v62 offset:0
v_add_u32 v63, s49, v33
v_exp_f32 v52, v128
v_mfma_f32_32x32x16_bf16 v[144:159], a[4:7], v[68:71], v[144:159]
ds_read_b128 a[4:7], v62 offset:512
v_exp_f32 v56, v160
v_add_f32 v60, v60, v52
v_exp_f32 v53, v129
v_mfma_f32_32x32x16_bf16 v[144:159], a[8:11], v[72:75], v[144:159]
ds_read_b128 a[8:11], v62 offset:1024
v_add_f32 v61, v61, v56
v_exp_f32 v57, v161
v_add_f32 v60, v60, v53
v_cvt_pk_bf16_f32 v224, v52, v53
v_mfma_f32_32x32x16_bf16 v[144:159], a[12:15], v[76:79], v[144:159]
s_mov_b32 s49, s46
buffer_load_dwordx4 v7, s[4:7], s44 offen offset:0 sc1 lds
ds_read_b128 a[12:15], v62 offset:1536
v_exp_f32 v54, v130
v_mfma_f32_32x32x16_bf16 v[144:159], a[16:19], v[80:83], v[144:159]
ds_read_b128 a[16:19], v62 offset:2048
v_add_f32 v61, v61, v57
v_cvt_pk_bf16_f32 v240, v56, v57
v_exp_f32 v58, v162
v_add_f32 v60, v60, v54
v_mfma_f32_32x32x16_bf16 v[144:159], a[20:23], v[84:87], v[144:159]
ds_read_b128 a[20:23], v62 offset:2560
v_exp_f32 v55, v131
v_add_f32 v61, v61, v58
v_exp_f32 v59, v163
v_mfma_f32_32x32x16_bf16 v[144:159], a[24:27], v[88:91], v[144:159]
s_add_u32 s46, s46, 16384
ds_read_b128 a[24:27], v62 offset:3072
v_add_f32 v60, v60, v55
v_cvt_pk_bf16_f32 v225, v54, v55
v_exp_f32 v52, v132
v_mfma_f32_32x32x16_bf16 v[144:159], a[28:31], v[92:95], v[144:159]
s_waitcnt lgkmcnt(7)
buffer_load_dwordx4 v7, s[4:7], s44 offen offset:1024 sc1 lds
ds_read_b128 a[28:31], v62 offset:3584
v_add_f32 v61, v61, v59
v_cvt_pk_bf16_f32 v241, v58, v59
v_mfma_f32_32x32x16_bf16 a[64:79], a[32:35], v[232:235], a[64:79]
v_exp_f32 v56, v164
v_add_f32 v60, v60, v52
v_exp_f32 v53, v133
v_add_f32 v61, v61, v56
v_mfma_f32_32x32x16_bf16 a[176:191], a[32:35], v[248:251], a[176:191]
s_and_b32 s36, s86, 1
ds_read_b128 a[32:35], v63 offset:0
v_exp_f32 v57, v165
v_add_f32 v60, v60, v53
v_cvt_pk_bf16_f32 v226, v52, v53
v_mfma_f32_32x32x16_bf16 a[80:95], a[36:39], v[232:235], a[80:95]
v_exp_f32 v54, v134
v_add_f32 v61, v61, v57
v_cvt_pk_bf16_f32 v242, v56, v57
v_exp_f32 v58, v166
v_mfma_f32_32x32x16_bf16 a[208:223], a[36:39], v[248:251], a[208:223]
s_lshl_b32 s36, s36, 3
ds_read_b128 a[36:39], v63 offset:1024
v_add_f32 v60, v60, v54
v_exp_f32 v55, v135
v_add_f32 v61, v61, v58
v_mfma_f32_32x32x16_bf16 a[112:127], a[40:43], v[232:235], a[112:127]
s_and_b32 s28, s87, 7
buffer_load_dwordx4 v7, s[4:7], s44 offen offset:2048 sc1 lds
v_exp_f32 v59, v167
v_add_f32 v60, v60, v55
v_cvt_pk_bf16_f32 v227, v54, v55
v_mfma_f32_32x32x16_bf16 a[240:255], a[40:43], v[248:251], a[240:255]
s_and_b32 s46, s46, 0xffff
ds_read_b128 a[40:43], v63 offset:2048
v_exp_f32 v52, v136
v_add_f32 v61, v61, v59
v_cvt_pk_bf16_f32 v243, v58, v59
v_mfma_f32_32x32x16_bf16 a[144:159], a[44:47], v[232:235], a[144:159]
v_exp_f32 v56, v168
v_add_f32 v60, v60, v52
v_exp_f32 v53, v137
v_add_f32 v61, v61, v56
v_mfma_f32_32x32x16_bf16 a[96:111], a[44:47], v[248:251], a[96:111]
s_or_b32 s36, s36, s28
ds_read_b128 a[44:47], v63 offset:3072
v_exp_f32 v57, v169
v_add_f32 v60, v60, v53
v_cvt_pk_bf16_f32 v228, v52, v53
v_mfma_f32_32x32x16_bf16 a[64:79], a[48:51], v[236:239], a[64:79]
s_lshr_b32 s37, s86, 1
buffer_load_dwordx4 v7, s[4:7], s44 offen offset:3072 sc1 lds
v_exp_f32 v54, v138
v_add_f32 v61, v61, v57
v_cvt_pk_bf16_f32 v244, v56, v57
v_mfma_f32_32x32x16_bf16 a[176:191], a[48:51], v[252:255], a[176:191]
ds_read_b128 a[48:51], v63 offset:4096
v_exp_f32 v58, v170
v_add_f32 v60, v60, v54
v_exp_f32 v55, v139
v_mfma_f32_32x32x16_bf16 a[80:95], a[52:55], v[236:239], a[80:95]
s_lshl_b32 s37, s37, 2
s_lshr_b32 s28, s87, 3
v_add_f32 v61, v61, v58
v_exp_f32 v59, v171
v_add_f32 v60, v60, v55
v_cvt_pk_bf16_f32 v229, v54, v55
v_mfma_f32_32x32x16_bf16 a[208:223], a[52:55], v[252:255], a[208:223]
s_or_b32 s37, s37, s28
ds_read_b128 a[52:55], v63 offset:5120
v_exp_f32 v52, v140
v_add_f32 v61, v61, v59
v_cvt_pk_bf16_f32 v245, v58, v59
v_mfma_f32_32x32x16_bf16 a[112:127], a[56:59], v[236:239], a[112:127]
v_exp_f32 v56, v172
v_add_f32 v60, v60, v52
v_exp_f32 v53, v141
v_add_f32 v61, v61, v56
v_mfma_f32_32x32x16_bf16 a[240:255], a[56:59], v[252:255], a[240:255]
s_lshl_b32 s24, s36, 18
ds_read_b128 a[56:59], v63 offset:6144
v_exp_f32 v57, v173
v_add_f32 v60, v60, v53
v_cvt_pk_bf16_f32 v230, v52, v53
v_mfma_f32_32x32x16_bf16 a[144:159], a[60:63], v[236:239], a[144:159]
s_lshl_b32 s28, s3, 16
s_add_u32 s24, s24, s28
s_mov_b32 s25, 0
s_mul_i32 s26, s3, 98304
s_lshl_b32 s28, s37, 7
s_add_u32 s26, s26, s28
s_add_u32 s47, s26, 4096
s_mul_i32 s48, s3, 7168
s_mov_b32 s12, s64
s_and_b32 s13, s65, 0xffff
s_mov_b32 s14, 0x400000
s_mov_b32 s16, s66
s_and_b32 s17, s67, 0xffff
s_mov_b32 s18, 0x600000
s_lshl_b32 s29, s36, 18
s_lshl_b32 s28, s3, 13
s_add_u32 s29, s29, s28
s_sub_u32 s29, s29, 34816
.Lpad11:
s_nop 0
s_cmp_eq_u32 s36, 0
s_cselect_b32 s29, 0x7f000000, s29
s_lshl_b32 s32, s3, 2
s_and_b32 s33, s3, 1
s_lshl_b32 s33, s33, 1
s_lshr_b32 s34, s3, 1
s_lshl_b32 s35, s34, 13
s_add_u32 s35, s35, 65536
s_mov_b32 s38, 131072
v_exp_f32 v54, v142
v_add_f32 v61, v61, v57
v_cvt_pk_bf16_f32 v246, v56, v57
v_exp_f32 v58, v174
v_add_f32 v60, v60, v54
v_exp_f32 v55, v143
v_add_f32 v61, v61, v58
v_exp_f32 v59, v175
v_add_f32 v60, v60, v55
v_cvt_pk_bf16_f32 v231, v54, v55
v_add_f32 v61, v61, v59
v_cvt_pk_bf16_f32 v247, v58, v59
v_mfma_f32_32x32x16_bf16 a[96:111], a[60:63], v[252:255], a[96:111]
s_waitcnt lgkmcnt(7)
ds_read_b128 a[60:63], v63 offset:7168
v_mfma_f32_32x32x16_bf16 v[128:143], a[0:3], v[64:67], v[192:207]
s_cmp_lt_u32 s44, s45
s_cselect_b32 m0, s46, s27
ds_read_b128 a[0:3], v62 offset:8192
v_exp_f32 v52, v144
v_lshrrev_b32 v56, 3, v1
v_mfma_f32_32x32x16_bf16 v[128:143], a[4:7], v[68:71], v[128:143]
ds_read_b128 a[4:7], v62 offset:8704
v_exp_f32 v53, v145
v_add_f32 v60, v60, v52
v_and_b32 v57, 7, v1
v_xor_b32 v58, v57, v56
v_mfma_f32_32x32x16_bf16 v[128:143], a[8:11], v[72:75], v[128:143]
ds_read_b128 a[8:11], v62 offset:9216
v_exp_f32 v54, v146
v_add_f32 v60, v60, v53
v_cvt_pk_bf16_f32 v232, v52, v53
v_lshlrev_b32 v58, 4, v58
v_mfma_f32_32x32x16_bf16 v[128:143], a[12:15], v[76:79], v[128:143]
buffer_load_dwordx4 v7, s[8:11], s44 offen offset:0 sc1 lds
ds_read_b128 a[12:15], v62 offset:9728
v_lshl_add_u32 v36, v56, 11, v58
v_add_u32 v37, 15360, v36
v_add_u32 v38, 30720, v36
v_mfma_f32_32x32x16_bf16 v[128:143], a[16:19], v[80:83], v[128:143]
ds_read_b128 a[16:19], v62 offset:10240
v_exp_f32 v55, v147
v_add_f32 v60, v60, v54
v_add_u32 v39, 46080, v36
v_add_u32 v58, s32, v56
v_mfma_f32_32x32x16_bf16 v[128:143], a[20:23], v[84:87], v[128:143]
ds_read_b128 a[20:23], v62 offset:10752
v_exp_f32 v52, v148
v_add_f32 v60, v60, v55
v_cvt_pk_bf16_f32 v233, v54, v55
v_xor_b32 v58, v58, v57
v_mfma_f32_32x32x16_bf16 v[128:143], a[24:27], v[88:91], v[128:143]
s_xor_b32 s50, s46, 0x18000
ds_read_b128 a[24:27], v62 offset:11264
v_exp_f32 v53, v149
v_add_f32 v60, v60, v52
v_and_b32 v58, 7, v58
v_mfma_f32_32x32x16_bf16 v[128:143], a[28:31], v[92:95], v[128:143]
s_waitcnt lgkmcnt(7)
buffer_load_dwordx4 v7, s[8:11], s44 offen offset:1024 sc1 lds
ds_read_b128 a[28:31], v62 offset:11776
v_lshlrev_b32 v58, 4, v58
v_lshl_add_u32 v58, v56, 11, v58
v_mfma_f32_32x32x16_bf16 a[64:79], a[32:35], v[224:227], a[64:79]
v_exp_f32 v54, v150
v_add_f32 v60, v60, v53
v_cvt_pk_bf16_f32 v234, v52, v53
v_add_u32 v58, s29, v58
v_and_b32 v59, 32, v1
v_mfma_f32_32x32x16_bf16 a[176:191], a[32:35], v[240:243], a[176:191]
ds_read_b128 a[32:35], v63 offset:8192
v_add_u32 v62, s46, v32
v_exp_f32 v55, v151
v_add_f32 v60, v60, v54
v_lshlrev_b32 v59, 25, v59
v_mfma_f32_32x32x16_bf16 a[80:95], a[36:39], v[224:227], a[80:95]
v_exp_f32 v52, v152
v_add_f32 v60, v60, v55
v_cvt_pk_bf16_f32 v235, v54, v55
v_or_b32 v42, v59, v58
v_bfe_u32 v58, v1, 4, 1
v_mfma_f32_32x32x16_bf16 a[208:223], a[36:39], v[240:243], a[208:223]
ds_read_b128 a[36:39], v63 offset:9216
v_lshlrev_b32 v58, 1, v58
v_xor_b32 v58, v58, v57
v_lshlrev_b32 v58, 4, v58
v_mul_u32_u24 v59, 6144, v56
v_add_u32 v40, v59, v58
v_mfma_f32_32x32x16_bf16 a[112:127], a[40:43], v[224:227], a[112:127]
buffer_load_dwordx4 v7, s[8:11], s44 offen offset:2048 sc1 lds
v_exp_f32 v53, v153
v_add_f32 v60, v60, v52
v_xor_b32 v41, 64, v40
v_add_u32 v41, 48128, v41
v_mfma_f32_32x32x16_bf16 a[240:255], a[40:43], v[240:243], a[240:255]
ds_read_b128 a[40:43], v63 offset:10240
v_exp_f32 v54, v154
v_add_f32 v60, v60, v53
v_cvt_pk_bf16_f32 v236, v52, v53
v_and_b32 v56, 15, v1
v_mfma_f32_32x32x16_bf16 a[144:159], a[44:47], v[224:227], a[144:159]
v_exp_f32 v55, v155
v_add_f32 v60, v60, v54
v_lshrrev_b32 v57, 4, v1
v_and_b32 v58, 7, v56
v_xor_b32 v58, v58, v57
v_mfma_f32_32x32x16_bf16 a[96:111], a[44:47], v[240:243], a[96:111]
ds_read_b128 a[44:47], v63 offset:11264
v_lshlrev_b32 v58, 4, v58
v_lshl_add_u32 v43, v56, 7, v58
v_add_u32 v43, s35, v43
v_xor_b32 v44, 64, v43
v_and_b32 v56, 15, v1
v_mfma_f32_32x32x16_bf16 a[64:79], a[48:51], v[228:231], a[64:79]
buffer_load_dwordx4 v7, s[8:11], s44 offen offset:3072 sc1 lds
v_exp_f32 v52, v156
v_add_f32 v60, v60, v55
v_cvt_pk_bf16_f32 v237, v54, v55
v_lshrrev_b32 v57, 2, v56
v_mfma_f32_32x32x16_bf16 a[176:191], a[48:51], v[244:247], a[176:191]
ds_read_b128 a[48:51], v63 offset:12288
v_exp_f32 v53, v157
v_add_f32 v60, v60, v52
v_mul_u32_u24 v57, 6656, v57
v_add_u32 v57, 71680, v57
v_mfma_f32_32x32x16_bf16 a[80:95], a[52:55], v[228:231], a[80:95]
s_add_u32 s44, s44, 16384
v_exp_f32 v54, v158
v_add_f32 v60, v60, v53
v_cvt_pk_bf16_f32 v238, v52, v53
v_mov_b32 v58, -2048
v_mov_b32 v59, s34
v_mfma_f32_32x32x16_bf16 a[208:223], a[52:55], v[244:247], a[208:223]
ds_read_b128 a[52:55], v63 offset:13312
v_cmp_eq_u32 vcc, 0, v59
v_nop
v_nop
v_cndmask_b32 v57, v58, v57, vcc
v_add_u32 v45, v57, v43
v_mfma_f32_32x32x16_bf16 a[112:127], a[56:59], v[228:231], a[112:127]
v_exp_f32 v55, v159
v_add_f32 v60, v60, v54
v_add_u32 v46, v57, v44
v_bfe_u32 v56, v1, 2, 2
v_bfe_u32 v57, v1, 4, 1
v_mfma_f32_32x32x16_bf16 a[240:255], a[56:59], v[244:247], a[240:255]
ds_read_b128 a[56:59], v63 offset:14336
v_add_f32 v60, v60, v55
v_cvt_pk_bf16_f32 v239, v54, v55
v_lshl_add_u32 v58, v57, 3, v56
v_lshlrev_b32 v58, 7, v58
v_lshrrev_b32 v59, 5, v1
v_mfma_f32_32x32x16_bf16 a[144:159], a[60:63], v[228:231], a[144:159]
v_mul_u32_u24 v59, 7168, v59
v_add3_u32 v58, v58, v59, s38
v_lshrrev_b32 v56, 1, v56
v_lshl_or_b32 v56, v57, 1, v56
v_xor_b32 v56, s33, v56
v_lshl_add_u32 v58, v56, 5, v58
v_and_b32 v56, 3, v1
v_lshl_add_u32 v47, v56, 3, v58
v_xor_b32 v48, 32, v47
v_lshrrev_b32 v57, 1, v56
v_and_b32 v56, 1, v56
v_lshl_or_b32 v56, v56, 1, v57
v_lshl_add_u32 v49, v56, 3, v58
v_xor_b32 v50, 32, v49
v_mfma_f32_32x32x16_bf16 a[96:111], a[60:63], v[244:247], a[96:111]
s_waitcnt vmcnt(8) lgkmcnt(7)
s_barrier
ds_read_b128 a[60:63], v63 offset:15360
s_branch .LS1
.LT1:
v_mfma_f32_32x32x16_bf16 v[144:159], a[0:3], v[64:67], v[192:207]
s_cmp_lt_u32 s44, s54
s_cselect_b32 m0, s50, s27
v_add_u32 v63, s49, v33
v_exp_f32 v52, v128
v_exp_f32 v56, v160
v_mfma_f32_32x32x16_bf16 v[176:191], a[0:3], v[96:99], v[36:51]
s_add_u32 s43, s42, 4
ds_read_b128 a[0:3], v62 offset:0
v_add_f32 v60, v60, v52
v_exp_f32 v53, v129
v_add_f32 v61, v61, v56
v_mfma_f32_32x32x16_bf16 v[144:159], a[4:7], v[68:71], v[144:159]
s_mov_b32 s42, s56
s_max_u32 s55, s57, s43
s_min_u32 s55, s55, s42
v_exp_f32 v57, v161
v_add_f32 v60, v60, v53
v_cvt_pk_bf16_f32 v224, v52, v53
v_mfma_f32_32x32x16_bf16 v[176:191], a[4:7], v[100:103], v[176:191]
s_sub_u32 s93, s55, s43
ds_read_b128 a[4:7], v62 offset:512
v_exp_f32 v54, v130
v_add_f32 v61, v61, v57
v_cvt_pk_bf16_f32 v240, v56, v57
v_mfma_f32_32x32x16_bf16 v[144:159], a[8:11], v[72:75], v[144:159]
s_mov_b32 s49, s46
s_mov_b32 s43, s55
buffer_load_dwordx4 v7, s[4:7], s44 offen offset:0 sc1 lds
v_exp_f32 v58, v162
v_add_f32 v60, v60, v54
v_mfma_f32_32x32x16_bf16 v[176:191], a[8:11], v[104:107], v[176:191]
s_mov_b32 s55, s93
ds_read_b128 a[8:11], v62 offset:1024
v_exp_f32 v55, v131
v_add_f32 v61, v61, v58
v_mfma_f32_32x32x16_bf16 v[144:159], a[12:15], v[76:79], v[144:159]
v_exp_f32 v59, v163
v_add_f32 v60, v60, v55
v_cvt_pk_bf16_f32 v225, v54, v55
v_mfma_f32_32x32x16_bf16 v[176:191], a[12:15], v[108:111], v[176:191]
ds_read_b128 a[12:15], v62 offset:1536
v_exp_f32 v52, v132
v_add_f32 v61, v61, v59
v_cvt_pk_bf16_f32 v241, v58, v59
v_mfma_f32_32x32x16_bf16 v[144:159], a[16:19], v[80:83], v[144:159]
s_add_u32 s46, s46, 16384
v_exp_f32 v56, v164
v_add_f32 v60, v60, v52
v_mfma_f32_32x32x16_bf16 v[176:191], a[16:19], v[112:115], v[176:191]
ds_read_b128 a[16:19], v62 offset:2048
v_exp_f32 v53, v133
v_add_f32 v61, v61, v56
v_mfma_f32_32x32x16_bf16 v[144:159], a[20:23], v[84:87], v[144:159]
buffer_load_dwordx4 v7, s[4:7], s44 offen offset:1024 sc1 lds
v_exp_f32 v57, v165
v_add_f32 v60, v60, v53
v_cvt_pk_bf16_f32 v226, v52, v53
v_mfma_f32_32x32x16_bf16 v[176:191], a[20:23], v[116:119], v[176:191]
ds_read_b128 a[20:23], v62 offset:2560
v_exp_f32 v54, v134
v_add_f32 v61, v61, v57
v_cvt_pk_bf16_f32 v242, v56, v57
v_mfma_f32_32x32x16_bf16 v[144:159], a[24:27], v[88:91], v[144:159]
v_exp_f32 v58, v166
v_add_f32 v60, v60, v54
v_mfma_f32_32x32x16_bf16 v[176:191], a[24:27], v[120:123], v[176:191]
ds_read_b128 a[24:27], v62 offset:3072
v_exp_f32 v55, v135
v_add_f32 v61, v61, v58
v_mfma_f32_32x32x16_bf16 v[144:159], a[28:31], v[92:95], v[144:159]
v_exp_f32 v59, v167
v_add_f32 v60, v60, v55
v_cvt_pk_bf16_f32 v227, v54, v55
v_mfma_f32_32x32x16_bf16 v[176:191], a[28:31], v[124:127], v[176:191]
s_waitcnt lgkmcnt(7)
ds_read_b128 a[28:31], v62 offset:3584
v_exp_f32 v52, v136
v_add_f32 v61, v61, v59
v_cvt_pk_bf16_f32 v243, v58, v59
v_mfma_f32_32x32x16_bf16 a[64:79], a[32:35], v[232:235], a[64:79]
buffer_load_dwordx4 v7, s[4:7], s44 offen offset:2048 sc1 lds
v_exp_f32 v56, v168
v_add_f32 v60, v60, v52
v_exp_f32 v53, v137
v_mfma_f32_32x32x16_bf16 a[176:191], a[32:35], v[248:251], a[176:191]
ds_read_b128 a[32:35], v63 offset:0
v_add_f32 v61, v61, v56
v_exp_f32 v57, v169
v_add_f32 v60, v60, v53
v_cvt_pk_bf16_f32 v228, v52, v53
v_mfma_f32_32x32x16_bf16 a[80:95], a[36:39], v[232:235], a[80:95]
s_and_b32 s46, s46, 0xffff
v_exp_f32 v54, v138
v_add_f32 v61, v61, v57
v_cvt_pk_bf16_f32 v244, v56, v57
v_mfma_f32_32x32x16_bf16 a[208:223], a[36:39], v[248:251], a[208:223]
ds_read_b128 a[36:39], v63 offset:1024
v_exp_f32 v58, v170
v_add_f32 v60, v60, v54
v_mfma_f32_32x32x16_bf16 a[112:127], a[40:43], v[232:235], a[112:127]
v_exp_f32 v55, v139
v_add_f32 v61, v61, v58
v_mfma_f32_32x32x16_bf16 a[240:255], a[40:43], v[248:251], a[240:255]
ds_read_b128 a[40:43], v63 offset:2048
v_exp_f32 v59, v171
v_add_f32 v60, v60, v55
v_cvt_pk_bf16_f32 v229, v54, v55
v_mfma_f32_32x32x16_bf16 a[144:159], a[44:47], v[232:235], a[144:159]
buffer_load_dwordx4 v7, s[4:7], s44 offen offset:3072 sc1 lds
v_exp_f32 v52, v140
v_add_f32 v61, v61, v59
v_cvt_pk_bf16_f32 v245, v58, v59
v_mfma_f32_32x32x16_bf16 a[96:111], a[44:47], v[248:251], a[96:111]
ds_read_b128 a[44:47], v63 offset:3072
v_exp_f32 v56, v172
v_add_f32 v60, v60, v52
v_mfma_f32_32x32x16_bf16 a[64:79], a[48:51], v[236:239], a[64:79]
v_exp_f32 v53, v141
v_add_f32 v61, v61, v56
v_mfma_f32_32x32x16_bf16 a[176:191], a[48:51], v[252:255], a[176:191]
ds_read_b128 a[48:51], v63 offset:4096
v_exp_f32 v57, v173
v_add_f32 v60, v60, v53
v_cvt_pk_bf16_f32 v230, v52, v53
v_mfma_f32_32x32x16_bf16 a[80:95], a[52:55], v[236:239], a[80:95]
v_exp_f32 v54, v142
v_add_f32 v61, v61, v57
v_cvt_pk_bf16_f32 v246, v56, v57
v_mfma_f32_32x32x16_bf16 a[208:223], a[52:55], v[252:255], a[208:223]
ds_read_b128 a[52:55], v63 offset:5120
v_exp_f32 v58, v174
v_add_f32 v60, v60, v54
v_mfma_f32_32x32x16_bf16 a[112:127], a[56:59], v[236:239], a[112:127]
v_exp_f32 v55, v143
v_add_f32 v61, v61, v58
v_mfma_f32_32x32x16_bf16 a[240:255], a[56:59], v[252:255], a[240:255]
ds_read_b128 a[56:59], v63 offset:6144
v_exp_f32 v59, v175
v_add_f32 v60, v60, v55
v_cvt_pk_bf16_f32 v231, v54, v55
v_mfma_f32_32x32x16_bf16 a[144:159], a[60:63], v[236:239], a[144:159]
v_add_f32 v61, v61, v59
v_cvt_pk_bf16_f32 v247, v58, v59
v_mfma_f32_32x32x16_bf16 a[96:111], a[60:63], v[252:255], a[96:111]
s_waitcnt lgkmcnt(7)
ds_read_b128 a[60:63], v63 offset:7168
v_mfma_f32_32x32x16_bf16 v[128:143], a[0:3], v[64:67], v[192:207]
s_cmp_lt_u32 s44, s45
s_cselect_b32 m0, s46, s27
ds_read_b128 a[0:3], v62 offset:8192
v_exp_f32 v52, v144
v_mfma_f32_32x32x16_bf16 v[128:143], a[4:7], v[68:71], v[128:143]
ds_read_b128 a[4:7], v62 offset:8704
v_exp_f32 v56, v176
v_add_f32 v60, v60, v52
v_exp_f32 v53, v145
v_mfma_f32_32x32x16_bf16 v[128:143], a[8:11], v[72:75], v[128:143]
ds_read_b128 a[8:11], v62 offset:9216
v_add_f32 v61, v61, v56
v_exp_f32 v57, v177
v_add_f32 v60, v60, v53
v_cvt_pk_bf16_f32 v232, v52, v53
v_mfma_f32_32x32x16_bf16 v[128:143], a[12:15], v[76:79], v[128:143]
buffer_load_dwordx4 v7, s[8:11], s44 offen offset:0 sc1 lds
ds_read_b128 a[12:15], v62 offset:9728
v_exp_f32 v54, v146
v_add_f32 v61, v61, v57
v_mfma_f32_32x32x16_bf16 v[128:143], a[16:19], v[80:83], v[128:143]
s_and_b32 s36, s86, 1
ds_read_b128 a[16:19], v62 offset:10240
v_cvt_pk_bf16_f32 v248, v56, v57
v_exp_f32 v58, v178
v_add_f32 v60, v60, v54
v_mfma_f32_32x32x16_bf16 v[128:143], a[20:23], v[84:87], v[128:143]
ds_read_b128 a[20:23], v62 offset:10752
v_exp_f32 v55, v147
v_add_f32 v61, v61, v58
v_exp_f32 v59, v179
v_mfma_f32_32x32x16_bf16 v[128:143], a[24:27], v[88:91], v[128:143]
s_xor_b32 s50, s46, 0x18000
ds_read_b128 a[24:27], v62 offset:11264
v_add_f32 v60, v60, v55
v_cvt_pk_bf16_f32 v233, v54, v55
v_exp_f32 v52, v148
v_mfma_f32_32x32x16_bf16 v[128:143], a[28:31], v[92:95], v[128:143]
s_waitcnt lgkmcnt(7)
buffer_load_dwordx4 v7, s[8:11], s44 offen offset:1024 sc1 lds
ds_read_b128 a[28:31], v62 offset:11776
v_add_f32 v61, v61, v59
v_cvt_pk_bf16_f32 v249, v58, v59
v_mfma_f32_32x32x16_bf16 a[64:79], a[32:35], v[224:227], a[64:79]
v_exp_f32 v56, v180
v_add_f32 v60, v60, v52
v_exp_f32 v53, v149
v_add_f32 v61, v61, v56
v_mfma_f32_32x32x16_bf16 a[176:191], a[32:35], v[240:243], a[176:191]
ds_read_b128 a[32:35], v63 offset:8192
v_add_u32 v62, s46, v32
v_exp_f32 v57, v181
v_add_f32 v60, v60, v53
v_cvt_pk_bf16_f32 v234, v52, v53
v_mfma_f32_32x32x16_bf16 a[80:95], a[36:39], v[224:227], a[80:95]
v_exp_f32 v54, v150
v_add_f32 v61, v61, v57
v_cvt_pk_bf16_f32 v250, v56, v57
v_exp_f32 v58, v182
v_mfma_f32_32x32x16_bf16 a[208:223], a[36:39], v[240:243], a[208:223]
s_lshl_b32 s36, s36, 3
ds_read_b128 a[36:39], v63 offset:9216
v_add_f32 v60, v60, v54
v_exp_f32 v55, v151
v_add_f32 v61, v61, v58
v_mfma_f32_32x32x16_bf16 a[112:127], a[40:43], v[224:227], a[112:127]
s_and_b32 s28, s87, 7
buffer_load_dwordx4 v7, s[8:11], s44 offen offset:2048 sc1 lds
v_exp_f32 v59, v183
v_add_f32 v60, v60, v55
v_cvt_pk_bf16_f32 v235, v54, v55
v_mfma_f32_32x32x16_bf16 a[240:255], a[40:43], v[240:243], a[240:255]
s_or_b32 s36, s36, s28
ds_read_b128 a[40:43], v63 offset:10240
v_exp_f32 v52, v152
v_add_f32 v61, v61, v59
v_cvt_pk_bf16_f32 v251, v58, v59
v_mfma_f32_32x32x16_bf16 a[144:159], a[44:47], v[224:227], a[144:159]
v_exp_f32 v56, v184
v_add_f32 v60, v60, v52
v_exp_f32 v53, v153
v_add_f32 v61, v61, v56
v_mfma_f32_32x32x16_bf16 a[96:111], a[44:47], v[240:243], a[96:111]
s_lshr_b32 s37, s86, 1
ds_read_b128 a[44:47], v63 offset:11264
v_exp_f32 v57, v185
v_add_f32 v60, v60, v53
v_cvt_pk_bf16_f32 v236, v52, v53
v_mfma_f32_32x32x16_bf16 a[64:79], a[48:51], v[228:231], a[64:79]
s_lshl_b32 s37, s37, 2
buffer_load_dwordx4 v7, s[8:11], s44 offen offset:3072 sc1 lds
v_exp_f32 v54, v154
v_add_f32 v61, v61, v57
v_cvt_pk_bf16_f32 v252, v56, v57
v_mfma_f32_32x32x16_bf16 a[176:191], a[48:51], v[244:247], a[176:191]
ds_read_b128 a[48:51], v63 offset:12288
v_exp_f32 v58, v186
v_add_f32 v60, v60, v54
v_exp_f32 v55, v155
v_mfma_f32_32x32x16_bf16 a[80:95], a[52:55], v[228:231], a[80:95]
s_add_u32 s44, s44, 16384
s_lshr_b32 s28, s87, 3
v_add_f32 v61, v61, v58
v_exp_f32 v59, v187
v_add_f32 v60, v60, v55
v_cvt_pk_bf16_f32 v237, v54, v55
v_mfma_f32_32x32x16_bf16 a[208:223], a[52:55], v[244:247], a[208:223]
s_or_b32 s37, s37, s28
ds_read_b128 a[52:55], v63 offset:13312
v_exp_f32 v52, v156
v_add_f32 v61, v61, v59
v_cvt_pk_bf16_f32 v253, v58, v59
v_mfma_f32_32x32x16_bf16 a[112:127], a[56:59], v[228:231], a[112:127]
v_exp_f32 v56, v188
v_add_f32 v60, v60, v52
v_exp_f32 v53, v157
v_add_f32 v61, v61, v56
v_mfma_f32_32x32x16_bf16 a[240:255], a[56:59], v[244:247], a[240:255]
s_lshl_b32 s24, s36, 18
ds_read_b128 a[56:59], v63 offset:14336
v_exp_f32 v57, v189
v_add_f32 v60, v60, v53
v_cvt_pk_bf16_f32 v238, v52, v53
v_mfma_f32_32x32x16_bf16 a[144:159], a[60:63], v[228:231], a[144:159]
s_lshl_b32 s28, s3, 16
s_add_u32 s24, s24, s28
s_mov_b32 s25, 0
s_mul_i32 s26, s3, 98304
s_lshl_b32 s28, s37, 7
s_add_u32 s26, s26, s28
s_add_u32 s47, s26, 4096
s_mul_i32 s48, s3, 7168
s_mov_b32 s12, s64
s_and_b32 s13, s65, 0xffff
s_mov_b32 s14, 0x400000
s_mov_b32 s16, s66
s_and_b32 s17, s67, 0xffff
s_mov_b32 s18, 0x600000
s_lshl_b32 s29, s36, 18
s_lshl_b32 s28, s3, 13
s_add_u32 s29, s29, s28
s_sub_u32 s29, s29, 34816
s_cmp_eq_u32 s36, 0
s_cselect_b32 s29, 0x7f000000, s29
s_lshl_b32 s32, s3, 2
s_and_b32 s33, s3, 1
s_lshl_b32 s33, s33, 1
s_lshr_b32 s34, s3, 1
s_lshl_b32 s35, s34, 13
s_add_u32 s35, s35, 65536
s_mov_b32 s38, 131072
v_exp_f32 v54, v158
v_add_f32 v61, v61, v57
v_cvt_pk_bf16_f32 v254, v56, v57
v_exp_f32 v58, v190
v_add_f32 v60, v60, v54
v_exp_f32 v55, v159
v_add_f32 v61, v61, v58
v_exp_f32 v59, v191
v_add_f32 v60, v60, v55
v_cvt_pk_bf16_f32 v239, v54, v55
v_add_f32 v61, v61, v59
v_cvt_pk_bf16_f32 v255, v58, v59
v_mfma_f32_32x32x16_bf16 a[96:111], a[60:63], v[244:247], a[96:111]
s_waitcnt vmcnt(8) lgkmcnt(7)
s_barrier
ds_read_b128 a[60:63], v63 offset:15360
v_mfma_f32_32x32x16_bf16 v[144:159], a[0:3], v[64:67], v[192:207]
s_cmp_lt_u32 s44, s54
s_cselect_b32 m0, s50, s27
ds_read_b128 a[0:3], v62 offset:0
v_add_u32 v63, s49, v33
v_exp_f32 v52, v128
v_mfma_f32_32x32x16_bf16 v[144:159], a[4:7], v[68:71], v[144:159]
ds_read_b128 a[4:7], v62 offset:512
v_exp_f32 v53, v129
v_add_f32 v60, v60, v52
v_lshrrev_b32 v56, 3, v1
v_and_b32 v57, 7, v1
v_mfma_f32_32x32x16_bf16 v[144:159], a[8:11], v[72:75], v[144:159]
ds_read_b128 a[8:11], v62 offset:1024
v_exp_f32 v54, v130
v_add_f32 v60, v60, v53
v_cvt_pk_bf16_f32 v224, v52, v53
v_xor_b32 v58, v57, v56
v_mfma_f32_32x32x16_bf16 v[144:159], a[12:15], v[76:79], v[144:159]
s_mov_b32 s49, s46
buffer_load_dwordx4 v7, s[4:7], s44 offen offset:0 sc1 lds
ds_read_b128 a[12:15], v62 offset:1536
v_lshlrev_b32 v58, 4, v58
v_lshl_add_u32 v36, v56, 11, v58
v_mfma_f32_32x32x16_bf16 v[144:159], a[16:19], v[80:83], v[144:159]
ds_read_b128 a[16:19], v62 offset:2048
v_exp_f32 v55, v131
v_add_f32 v60, v60, v54
v_add_u32 v37, 15360, v36
v_add_u32 v38, 30720, v36
v_mfma_f32_32x32x16_bf16 v[144:159], a[20:23], v[84:87], v[144:159]
ds_read_b128 a[20:23], v62 offset:2560
v_exp_f32 v52, v132
v_add_f32 v60, v60, v55
v_cvt_pk_bf16_f32 v225, v54, v55
v_add_u32 v39, 46080, v36
v_mfma_f32_32x32x16_bf16 v[144:159], a[24:27], v[88:91], v[144:159]
s_add_u32 s46, s46, 16384
ds_read_b128 a[24:27], v62 offset:3072
v_exp_f32 v53, v133
v_add_f32 v60, v60, v52
v_add_u32 v58, s32, v56
v_mfma_f32_32x32x16_bf16 v[144:159], a[28:31], v[92:95], v[144:159]
s_waitcnt lgkmcnt(7)
buffer_load_dwordx4 v7, s[4:7], s44 offen offset:1024 sc1 lds
ds_read_b128 a[28:31], v62 offset:3584
v_xor_b32 v58, v58, v57
v_and_b32 v58, 7, v58
v_mfma_f32_32x32x16_bf16 a[64:79], a[32:35], v[232:235], a[64:79]
v_exp_f32 v54, v134
v_add_f32 v60, v60, v53
v_cvt_pk_bf16_f32 v226, v52, v53
v_lshlrev_b32 v58, 4, v58
v_lshl_add_u32 v58, v56, 11, v58
v_mfma_f32_32x32x16_bf16 a[176:191], a[32:35], v[248:251], a[176:191]
ds_read_b128 a[32:35], v63 offset:0
v_exp_f32 v55, v135
v_add_f32 v60, v60, v54
v_add_u32 v58, s29, v58
v_and_b32 v59, 32, v1
v_mfma_f32_32x32x16_bf16 a[80:95], a[36:39], v[232:235], a[80:95]
v_exp_f32 v52, v136
v_add_f32 v60, v60, v55
v_cvt_pk_bf16_f32 v227, v54, v55
v_lshlrev_b32 v59, 25, v59
v_or_b32 v42, v59, v58
v_mfma_f32_32x32x16_bf16 a[208:223], a[36:39], v[248:251], a[208:223]
ds_read_b128 a[36:39], v63 offset:1024
v_bfe_u32 v58, v1, 4, 1
v_lshlrev_b32 v58, 1, v58
v_xor_b32 v58, v58, v57
v_lshlrev_b32 v58, 4, v58
v_mul_u32_u24 v59, 6144, v56
v_mfma_f32_32x32x16_bf16 a[112:127], a[40:43], v[232:235], a[112:127]
buffer_load_dwordx4 v7, s[4:7], s44 offen offset:2048 sc1 lds
v_exp_f32 v53, v137
v_add_f32 v60, v60, v52
v_add_u32 v40, v59, v58
v_xor_b32 v41, 64, v40
v_mfma_f32_32x32x16_bf16 a[240:255], a[40:43], v[248:251], a[240:255]
s_and_b32 s46, s46, 0xffff
ds_read_b128 a[40:43], v63 offset:2048
v_exp_f32 v54, v138
v_add_f32 v60, v60, v53
v_cvt_pk_bf16_f32 v228, v52, v53
v_mfma_f32_32x32x16_bf16 a[144:159], a[44:47], v[232:235], a[144:159]
v_exp_f32 v55, v139
v_add_f32 v60, v60, v54
v_add_u32 v41, 48128, v41
v_and_b32 v56, 15, v1
v_lshrrev_b32 v57, 4, v1
v_mfma_f32_32x32x16_bf16 a[96:111], a[44:47], v[248:251], a[96:111]
ds_read_b128 a[44:47], v63 offset:3072
v_and_b32 v58, 7, v56
v_xor_b32 v58, v58, v57
v_lshlrev_b32 v58, 4, v58
v_lshl_add_u32 v43, v56, 7, v58
v_add_u32 v43, s35, v43
v_mfma_f32_32x32x16_bf16 a[64:79], a[48:51], v[236:239], a[64:79]
buffer_load_dwordx4 v7, s[4:7], s44 offen offset:3072 sc1 lds
v_exp_f32 v52, v140
v_add_f32 v60, v60, v55
v_cvt_pk_bf16_f32 v229, v54, v55
v_xor_b32 v44, 64, v43
v_mfma_f32_32x32x16_bf16 a[176:191], a[48:51], v[252:255], a[176:191]
ds_read_b128 a[48:51], v63 offset:4096
v_exp_f32 v53, v141
v_add_f32 v60, v60, v52
v_and_b32 v56, 15, v1
v_lshrrev_b32 v57, 2, v56
v_mfma_f32_32x32x16_bf16 a[80:95], a[52:55], v[236:239], a[80:95]
v_exp_f32 v54, v142
v_add_f32 v60, v60, v53
v_cvt_pk_bf16_f32 v230, v52, v53
v_mul_u32_u24 v57, 6656, v57
v_add_u32 v57, 71680, v57
v_mfma_f32_32x32x16_bf16 a[208:223], a[52:55], v[252:255], a[208:223]
ds_read_b128 a[52:55], v63 offset:5120
v_mov_b32 v58, -2048
v_mov_b32 v59, s34
v_cmp_eq_u32 vcc, 0, v59
v_nop
v_nop
v_mfma_f32_32x32x16_bf16 a[112:127], a[56:59], v[236:239], a[112:127]
v_exp_f32 v55, v143
v_add_f32 v60, v60, v54
v_cndmask_b32 v57, v58, v57, vcc
v_add_u32 v45, v57, v43
v_add_u32 v46, v57, v44
v_mfma_f32_32x32x16_bf16 a[240:255], a[56:59], v[252:255], a[240:255]
ds_read_b128 a[56:59], v63 offset:6144
v_add_f32 v60, v60, v55
v_cvt_pk_bf16_f32 v231, v54, v55
v_bfe_u32 v56, v1, 2, 2
v_bfe_u32 v57, v1, 4, 1
v_lshl_add_u32 v58, v57, 3, v56
v_mfma_f32_32x32x16_bf16 a[144:159], a[60:63], v[236:239], a[144:159]
v_lshlrev_b32 v58, 7, v58
v_lshrrev_b32 v59, 5, v1
v_mul_u32_u24 v59, 7168, v59
v_add3_u32 v58, v58, v59, s38
v_lshrrev_b32 v56, 1, v56
v_lshl_or_b32 v56, v57, 1, v56
v_xor_b32 v56, s33, v56
v_lshl_add_u32 v58, v56, 5, v58
v_and_b32 v56, 3, v1
v_lshl_add_u32 v47, v56, 3, v58
v_xor_b32 v48, 32, v47
v_lshrrev_b32 v57, 1, v56
v_and_b32 v56, 1, v56
v_lshl_or_b32 v56, v56, 1, v57
v_lshl_add_u32 v49, v56, 3, v58
v_xor_b32 v50, 32, v49
v_mfma_f32_32x32x16_bf16 a[96:111], a[60:63], v[252:255], a[96:111]
s_waitcnt lgkmcnt(7)
ds_read_b128 a[60:63], v63 offset:7168
s_branch .LS0
.LS1:
v_mfma_f32_32x32x16_bf16 v[144:159], a[0:3], v[64:67], v[192:207]
s_cmp_lt_u32 s44, s54
s_cselect_b32 m0, s50, s27
ds_read_b128 a[0:3], v62 offset:0
v_add_u32 v63, s49, v33
v_exp_f32 v52, v128
v_mfma_f32_32x32x16_bf16 v[144:159], a[4:7], v[68:71], v[144:159]
ds_read_b128 a[4:7], v62 offset:512
v_exp_f32 v53, v129
v_add_f32 v60, v60, v52
v_exp_f32 v54, v130
v_mfma_f32_32x32x16_bf16 v[144:159], a[8:11], v[72:75], v[144:159]
s_mov_b32 s49, s46
buffer_load_dwordx4 v7, s[4:7], s44 offen offset:0 sc1 lds
ds_read_b128 a[8:11], v62 offset:1024
v_add_f32 v60, v60, v53
v_cvt_pk_bf16_f32 v224, v52, v53
v_mfma_f32_32x32x16_bf16 v[144:159], a[12:15], v[76:79], v[144:159]
ds_read_b128 a[12:15], v62 offset:1536
v_exp_f32 v55, v131
v_add_f32 v60, v60, v54
v_exp_f32 v52, v132
v_mfma_f32_32x32x16_bf16 v[144:159], a[16:19], v[80:83], v[144:159]
s_add_u32 s46, s46, 16384
ds_read_b128 a[16:19], v62 offset:2048
v_add_f32 v60, v60, v55
v_cvt_pk_bf16_f32 v225, v54, v55
v_exp_f32 v53, v133
v_mfma_f32_32x32x16_bf16 v[144:159], a[20:23], v[84:87], v[144:159]
buffer_load_dwordx4 v7, s[4:7], s44 offen offset:1024 sc1 lds
ds_read_b128 a[20:23], v62 offset:2560
v_add_f32 v60, v60, v52
v_exp_f32 v54, v134
v_mfma_f32_32x32x16_bf16 v[144:159], a[24:27], v[88:91], v[144:159]
ds_read_b128 a[24:27], v62 offset:3072
v_add_f32 v60, v60, v53
v_cvt_pk_bf16_f32 v226, v52, v53
v_exp_f32 v55, v135
v_add_f32 v60, v60, v54
v_mfma_f32_32x32x16_bf16 v[144:159], a[28:31], v[92:95], v[144:159]
s_waitcnt lgkmcnt(7)
ds_read_b128 a[28:31], v62 offset:3584
.Lpad12:
s_nop 0
v_exp_f32 v52, v136
v_add_f32 v60, v60, v55
v_cvt_pk_bf16_f32 v227, v54, v55
v_mfma_f32_32x32x16_bf16 a[64:79], a[32:35], v[232:235], a[64:79]
buffer_load_dwordx4 v7, s[4:7], s44 offen offset:2048 sc1 lds
ds_read_b128 a[32:35], v63 offset:0
v_exp_f32 v53, v137
v_add_f32 v60, v60, v52
v_mfma_f32_32x32x16_bf16 a[80:95], a[36:39], v[232:235], a[80:95]
s_and_b32 s46, s46, 0xffff
ds_read_b128 a[36:39], v63 offset:1024
v_exp_f32 v54, v138
v_add_f32 v60, v60, v53
v_cvt_pk_bf16_f32 v228, v52, v53
v_mfma_f32_32x32x16_bf16 a[112:127], a[40:43], v[232:235], a[112:127]
ds_read_b128 a[40:43], v63 offset:2048
v_exp_f32 v55, v139
v_add_f32 v60, v60, v54
v_exp_f32 v52, v140
v_mfma_f32_32x32x16_bf16 a[144:159], a[44:47], v[232:235], a[144:159]
buffer_load_dwordx4 v7, s[4:7], s44 offen offset:3072 sc1 lds
ds_read_b128 a[44:47], v63 offset:3072
v_add_f32 v60, v60, v55
v_cvt_pk_bf16_f32 v229, v54, v55
v_mfma_f32_32x32x16_bf16 a[64:79], a[48:51], v[236:239], a[64:79]
ds_read_b128 a[48:51], v63 offset:4096
v_exp_f32 v53, v141
v_add_f32 v60, v60, v52
v_exp_f32 v54, v142
v_mfma_f32_32x32x16_bf16 a[80:95], a[52:55], v[236:239], a[80:95]
s_sub_u32 s55, s55, 1
ds_read_b128 a[52:55], v63 offset:5120
v_add_f32 v60, v60, v53
v_cvt_pk_bf16_f32 v230, v52, v53
v_exp_f32 v55, v143
v_mfma_f32_32x32x16_bf16 a[112:127], a[56:59], v[236:239], a[112:127]
ds_read_b128 a[56:59], v63 offset:6144
v_add_f32 v60, v60, v54
v_add_f32 v60, v60, v55
v_cvt_pk_bf16_f32 v231, v54, v55
v_mfma_f32_32x32x16_bf16 a[144:159], a[60:63], v[236:239], a[144:159]
s_waitcnt lgkmcnt(7)
ds_read_b128 a[60:63], v63 offset:7168
s_cbranch_scc1 .Lev_s1
.LS0:
v_mfma_f32_32x32x16_bf16 v[128:143], a[0:3], v[64:67], v[192:207]
s_cmp_lt_u32 s44, s45
s_cselect_b32 m0, s46, s27
ds_read_b128 a[0:3], v62 offset:8192
v_exp_f32 v52, v144
v_mfma_f32_32x32x16_bf16 v[128:143], a[4:7], v[68:71], v[128:143]
ds_read_b128 a[4:7], v62 offset:8704
v_exp_f32 v53, v145
v_add_f32 v60, v60, v52
v_exp_f32 v54, v146
v_mfma_f32_32x32x16_bf16 v[128:143], a[8:11], v[72:75], v[128:143]
buffer_load_dwordx4 v7, s[8:11], s44 offen offset:0 sc1 lds
ds_read_b128 a[8:11], v62 offset:9216
v_add_f32 v60, v60, v53
v_cvt_pk_bf16_f32 v232, v52, v53
v_mfma_f32_32x32x16_bf16 v[128:143], a[12:15], v[76:79], v[128:143]
ds_read_b128 a[12:15], v62 offset:9728
v_exp_f32 v55, v147
v_add_f32 v60, v60, v54
v_exp_f32 v52, v148
v_mfma_f32_32x32x16_bf16 v[128:143], a[16:19], v[80:83], v[128:143]
s_xor_b32 s50, s46, 0x18000
ds_read_b128 a[16:19], v62 offset:10240
v_add_f32 v60, v60, v55
v_cvt_pk_bf16_f32 v233, v54, v55
v_exp_f32 v53, v149
v_mfma_f32_32x32x16_bf16 v[128:143], a[20:23], v[84:87], v[128:143]
buffer_load_dwordx4 v7, s[8:11], s44 offen offset:1024 sc1 lds
ds_read_b128 a[20:23], v62 offset:10752
v_add_f32 v60, v60, v52
v_exp_f32 v54, v150
v_mfma_f32_32x32x16_bf16 v[128:143], a[24:27], v[88:91], v[128:143]
ds_read_b128 a[24:27], v62 offset:11264
v_add_f32 v60, v60, v53
v_cvt_pk_bf16_f32 v234, v52, v53
v_exp_f32 v55, v151
v_add_f32 v60, v60, v54
v_mfma_f32_32x32x16_bf16 v[128:143], a[28:31], v[92:95], v[128:143]
s_waitcnt lgkmcnt(7)
ds_read_b128 a[28:31], v62 offset:11776
v_exp_f32 v52, v152
v_add_f32 v60, v60, v55
v_cvt_pk_bf16_f32 v235, v54, v55
v_mfma_f32_32x32x16_bf16 a[64:79], a[32:35], v[224:227], a[64:79]
buffer_load_dwordx4 v7, s[8:11], s44 offen offset:2048 sc1 lds
ds_read_b128 a[32:35], v63 offset:8192
v_exp_f32 v53, v153
v_add_f32 v60, v60, v52
v_mfma_f32_32x32x16_bf16 a[80:95], a[36:39], v[224:227], a[80:95]
ds_read_b128 a[36:39], v63 offset:9216
v_add_u32 v62, s46, v32
v_exp_f32 v54, v154
v_add_f32 v60, v60, v53
v_cvt_pk_bf16_f32 v236, v52, v53
v_mfma_f32_32x32x16_bf16 a[112:127], a[40:43], v[224:227], a[112:127]
ds_read_b128 a[40:43], v63 offset:10240
v_exp_f32 v55, v155
v_add_f32 v60, v60, v54
v_exp_f32 v52, v156
v_mfma_f32_32x32x16_bf16 a[144:159], a[44:47], v[224:227], a[144:159]
buffer_load_dwordx4 v7, s[8:11], s44 offen offset:3072 sc1 lds
ds_read_b128 a[44:47], v63 offset:11264
v_add_f32 v60, v60, v55
v_cvt_pk_bf16_f32 v237, v54, v55
v_mfma_f32_32x32x16_bf16 a[64:79], a[48:51], v[228:231], a[64:79]
s_add_u32 s44, s44, 16384
ds_read_b128 a[48:51], v63 offset:12288
v_exp_f32 v53, v157
v_add_f32 v60, v60, v52
v_mfma_f32_32x32x16_bf16 a[80:95], a[52:55], v[228:231], a[80:95]
s_sub_u32 s55, s55, 1
ds_read_b128 a[52:55], v63 offset:13312
v_exp_f32 v54, v158
v_add_f32 v60, v60, v53
v_cvt_pk_bf16_f32 v238, v52, v53
v_mfma_f32_32x32x16_bf16 a[112:127], a[56:59], v[228:231], a[112:127]
ds_read_b128 a[56:59], v63 offset:14336
v_exp_f32 v55, v159
v_add_f32 v60, v60, v54
v_add_f32 v60, v60, v55
v_cvt_pk_bf16_f32 v239, v54, v55
v_mfma_f32_32x32x16_bf16 a[144:159], a[60:63], v[228:231], a[144:159]
s_waitcnt vmcnt(8) lgkmcnt(7)
s_barrier
ds_read_b128 a[60:63], v63 offset:15360
s_cbranch_scc0 .LS1
s_cmp_eq_u32 s43, s42
s_cbranch_scc1 .LE1
s_swappc_b64 s[30:31], s[58:59]
.Lev_s0_r:
s_branch .LS1
.LE0:
v_mfma_f32_32x32x16_bf16 v[128:143], a[0:3], v[64:67], v[16:31]
s_cmp_lt_u32 s44, s45
s_cselect_b32 m0, s46, s27
v_exp_f32 v52, v144
v_exp_f32 v53, v145
v_add_f32 v60, v60, v52
v_mfma_f32_32x32x16_bf16 v[128:143], a[4:7], v[68:71], v[128:143]
buffer_load_dwordx4 v7, s[8:11], s44 offen offset:0 sc1 lds
buffer_load_dwordx4 v7, s[8:11], s44 offen offset:1024 sc1 lds
v_exp_f32 v54, v146
v_add_f32 v60, v60, v53
v_cvt_pk_bf16_f32 v232, v52, v53
v_mfma_f32_32x32x16_bf16 v[128:143], a[8:11], v[72:75], v[128:143]
buffer_load_dwordx4 v7, s[8:11], s44 offen offset:2048 sc1 lds
buffer_load_dwordx4 v7, s[8:11], s44 offen offset:3072 sc1 lds
v_exp_f32 v55, v147
v_add_f32 v60, v60, v54
v_mfma_f32_32x32x16_bf16 v[128:143], a[12:15], v[76:79], v[128:143]
s_add_u32 m0, s48, 131072
buffer_load_dword v35, v4, s[96:99], 0 offen
v_exp_f32 v52, v148
v_add_f32 v60, v60, v55
v_cvt_pk_bf16_f32 v233, v54, v55
v_mfma_f32_32x32x16_bf16 v[128:143], a[16:19], v[80:83], v[128:143]
s_xor_b32 s50, s46, 0x18000
buffer_load_dwordx4 v40, s[16:19], s26 offen offset:0 lds
v_exp_f32 v53, v149
v_add_f32 v60, v60, v52
v_mfma_f32_32x32x16_bf16 v[128:143], a[20:23], v[84:87], v[128:143]
buffer_load_dwordx4 v41, s[16:19], s26 offen offset:1024 lds
v_exp_f32 v54, v150
v_add_f32 v60, v60, v53
v_cvt_pk_bf16_f32 v234, v52, v53
v_mfma_f32_32x32x16_bf16 v[128:143], a[24:27], v[88:91], v[128:143]
buffer_load_dwordx4 v40, s[16:19], s26 offen offset:2048 lds
v_exp_f32 v55, v151
v_add_f32 v60, v60, v54
v_mfma_f32_32x32x16_bf16 v[128:143], a[28:31], v[92:95], v[128:143]
s_waitcnt lgkmcnt(0)
buffer_load_dwordx4 v41, s[16:19], s26 offen offset:3072 lds
v_exp_f32 v52, v152
v_add_f32 v60, v60, v55
v_cvt_pk_bf16_f32 v235, v54, v55
v_exp_f32 v53, v153
v_mfma_f32_32x32x16_bf16 a[64:79], a[32:35], v[224:227], a[64:79]
s_add_u32 m0, s48, 135168
ds_read_b128 a[32:35], v63 offset:8192
v_add_f32 v60, v60, v52
v_exp_f32 v54, v154
v_mfma_f32_32x32x16_bf16 a[80:95], a[36:39], v[224:227], a[80:95]
buffer_load_dwordx4 v40, s[16:19], s47 offen offset:0 lds
ds_read_b128 a[36:39], v63 offset:9216
v_add_f32 v60, v60, v53
v_cvt_pk_bf16_f32 v236, v52, v53
v_exp_f32 v55, v155
v_add_f32 v60, v60, v54
v_mfma_f32_32x32x16_bf16 a[112:127], a[40:43], v[224:227], a[112:127]
buffer_load_dwordx4 v41, s[16:19], s47 offen offset:1024 lds
ds_read_b128 a[40:43], v63 offset:10240
v_exp_f32 v52, v156
v_add_f32 v60, v60, v55
v_cvt_pk_bf16_f32 v237, v54, v55
v_mfma_f32_32x32x16_bf16 a[144:159], a[44:47], v[224:227], a[144:159]
buffer_load_dwordx4 v42, s[12:15], s25 offen offset:2048 lds
ds_read_b128 a[44:47], v63 offset:11264
v_exp_f32 v53, v157
v_add_f32 v60, v60, v52
v_mfma_f32_32x32x16_bf16 a[64:79], a[48:51], v[228:231], a[64:79]
s_add_u32 s44, s44, 16384
s_add_u32 m0, s91, 65536
ds_read_b128 a[48:51], v63 offset:12288
v_exp_f32 v54, v158
v_add_f32 v60, v60, v53
v_cvt_pk_bf16_f32 v238, v52, v53
v_mfma_f32_32x32x16_bf16 a[80:95], a[52:55], v[228:231], a[80:95]
buffer_load_dwordx4 v36, s[12:15], s24 offen offset:0 lds
buffer_load_dwordx4 v37, s[12:15], s24 offen offset:1024 lds
ds_read_b128 a[52:55], v63 offset:13312
v_exp_f32 v55, v159
v_add_f32 v60, v60, v54
v_mfma_f32_32x32x16_bf16 a[112:127], a[56:59], v[228:231], a[112:127]
buffer_load_dwordx4 v38, s[12:15], s24 offen offset:2048 lds
buffer_load_dwordx4 v39, s[12:15], s24 offen offset:3072 lds
ds_read_b128 a[56:59], v63 offset:14336
v_add_f32 v60, v60, v55
v_cvt_pk_bf16_f32 v239, v54, v55
v_mfma_f32_32x32x16_bf16 a[144:159], a[60:63], v[228:231], a[144:159]
s_waitcnt vmcnt(20)
s_barrier
s_add_u32 s26, s26, 393216
s_add_u32 s47, s47, 393216
s_add_u32 s25, s25, 128
s_add_u32 s24, s24, 128
ds_read_b128 a[60:63], v63 offset:15360
v_add_u32 v63, s49, v33
s_waitcnt lgkmcnt(4)
v_mfma_f32_32x32x16_bf16 a[64:79], a[32:35], v[232:235], a[64:79]
s_add_u32 m0, s91, 81920
ds_read_b128 a[32:35], v63 offset:0
v_exp_f32 v52, v128
v_exp_f32 v53, v129
v_add_f32 v60, v60, v52
v_mfma_f32_32x32x16_bf16 a[80:95], a[36:39], v[232:235], a[80:95]
buffer_load_dwordx4 v36, s[12:15], s24 offen offset:0 lds
ds_read_b128 a[36:39], v63 offset:1024
v_exp_f32 v54, v130
v_add_f32 v60, v60, v53
v_cvt_pk_bf16_f32 v224, v52, v53
v_mfma_f32_32x32x16_bf16 a[112:127], a[40:43], v[232:235], a[112:127]
buffer_load_dwordx4 v37, s[12:15], s24 offen offset:1024 lds
ds_read_b128 a[40:43], v63 offset:2048
v_exp_f32 v55, v131
v_add_f32 v60, v60, v54
v_exp_f32 v52, v132
v_mfma_f32_32x32x16_bf16 a[144:159], a[44:47], v[232:235], a[144:159]
s_waitcnt lgkmcnt(3)
ds_read_b128 a[44:47], v63 offset:3072
v_add_f32 v60, v60, v55
v_cvt_pk_bf16_f32 v225, v54, v55
v_exp_f32 v53, v133
v_add_f32 v60, v60, v52
v_mfma_f32_32x32x16_bf16 a[64:79], a[48:51], v[236:239], a[64:79]
buffer_load_dwordx4 v38, s[12:15], s24 offen offset:2048 lds
ds_read_b128 a[48:51], v63 offset:4096
v_exp_f32 v54, v134
v_add_f32 v60, v60, v53
v_cvt_pk_bf16_f32 v226, v52, v53
v_mfma_f32_32x32x16_bf16 a[80:95], a[52:55], v[236:239], a[80:95]
buffer_load_dwordx4 v39, s[12:15], s24 offen offset:3072 lds
ds_read_b128 a[52:55], v63 offset:5120
v_exp_f32 v55, v135
v_add_f32 v60, v60, v54
v_exp_f32 v52, v136
v_mfma_f32_32x32x16_bf16 a[112:127], a[56:59], v[236:239], a[112:127]
s_add_u32 s24, s24, 128
ds_read_b128 a[56:59], v63 offset:6144
v_add_f32 v60, v60, v55
v_cvt_pk_bf16_f32 v227, v54, v55
v_exp_f32 v53, v137
v_add_f32 v60, v60, v52
v_mfma_f32_32x32x16_bf16 a[144:159], a[60:63], v[236:239], a[144:159]
ds_read_b128 a[60:63], v63 offset:7168
v_exp_f32 v54, v138
v_add_f32 v60, v60, v53
v_cvt_pk_bf16_f32 v228, v52, v53
v_exp_f32 v55, v139
v_add_f32 v60, v60, v54
v_exp_f32 v52, v140
v_add_f32 v60, v60, v55
v_cvt_pk_bf16_f32 v229, v54, v55
v_exp_f32 v53, v141
v_add_f32 v60, v60, v52
v_exp_f32 v54, v142
v_add_f32 v60, v60, v53
v_cvt_pk_bf16_f32 v230, v52, v53
v_exp_f32 v55, v143
v_add_f32 v60, v60, v54
v_add_f32 v60, v60, v55
v_cvt_pk_bf16_f32 v231, v54, v55
s_waitcnt lgkmcnt(4)
v_mfma_f32_32x32x16_bf16 a[64:79], a[32:35], v[224:227], a[64:79]
v_mfma_f32_32x32x16_bf16 a[80:95], a[36:39], v[224:227], a[80:95]
v_mfma_f32_32x32x16_bf16 a[112:127], a[40:43], v[224:227], a[112:127]
v_mfma_f32_32x32x16_bf16 a[144:159], a[44:47], v[224:227], a[144:159]
s_waitcnt lgkmcnt(0)
v_mfma_f32_32x32x16_bf16 a[64:79], a[48:51], v[228:231], a[64:79]
v_mfma_f32_32x32x16_bf16 a[80:95], a[52:55], v[228:231], a[80:95]
v_mfma_f32_32x32x16_bf16 a[112:127], a[56:59], v[228:231], a[112:127]
v_mfma_f32_32x32x16_bf16 a[144:159], a[60:63], v[228:231], a[144:159]
s_waitcnt vmcnt(16)
s_barrier
s_branch .LEend
.LE1:
v_mfma_f32_32x32x16_bf16 v[144:159], a[0:3], v[64:67], v[16:31]
s_add_u32 m0, s48, 131072
buffer_load_dword v35, v4, s[96:99], 0 offen
v_add_u32 v63, s49, v33
v_exp_f32 v52, v128
v_exp_f32 v53, v129
v_add_f32 v60, v60, v52
v_mfma_f32_32x32x16_bf16 v[144:159], a[4:7], v[68:71], v[144:159]
buffer_load_dwordx4 v40, s[16:19], s26 offen offset:0 lds
v_exp_f32 v54, v130
v_add_f32 v60, v60, v53
v_cvt_pk_bf16_f32 v224, v52, v53
v_mfma_f32_32x32x16_bf16 v[144:159], a[8:11], v[72:75], v[144:159]
buffer_load_dwordx4 v41, s[16:19], s26 offen offset:1024 lds
v_exp_f32 v55, v131
v_add_f32 v60, v60, v54
v_mfma_f32_32x32x16_bf16 v[144:159], a[12:15], v[76:79], v[144:159]
buffer_load_dwordx4 v40, s[16:19], s26 offen offset:2048 lds
v_exp_f32 v52, v132
v_add_f32 v60, v60, v55
v_cvt_pk_bf16_f32 v225, v54, v55
v_mfma_f32_32x32x16_bf16 v[144:159], a[16:19], v[80:83], v[144:159]
buffer_load_dwordx4 v41, s[16:19], s26 offen offset:3072 lds
v_exp_f32 v53, v133
v_add_f32 v60, v60, v52
v_mfma_f32_32x32x16_bf16 v[144:159], a[20:23], v[84:87], v[144:159]
s_add_u32 m0, s48, 135168
v_exp_f32 v54, v134
v_add_f32 v60, v60, v53
v_cvt_pk_bf16_f32 v226, v52, v53
v_mfma_f32_32x32x16_bf16 v[144:159], a[24:27], v[88:91], v[144:159]
buffer_load_dwordx4 v40, s[16:19], s47 offen offset:0 lds
v_exp_f32 v55, v135
v_add_f32 v60, v60, v54
v_mfma_f32_32x32x16_bf16 v[144:159], a[28:31], v[92:95], v[144:159]
s_waitcnt lgkmcnt(0)
buffer_load_dwordx4 v41, s[16:19], s47 offen offset:1024 lds
v_exp_f32 v52, v136
v_add_f32 v60, v60, v55
v_cvt_pk_bf16_f32 v227, v54, v55
v_exp_f32 v53, v137
v_mfma_f32_32x32x16_bf16 a[64:79], a[32:35], v[232:235], a[64:79]
buffer_load_dwordx4 v42, s[12:15], s25 offen offset:2048 lds
ds_read_b128 a[32:35], v63 offset:0
v_add_f32 v60, v60, v52
v_exp_f32 v54, v138
v_add_f32 v60, v60, v53
v_cvt_pk_bf16_f32 v228, v52, v53
v_mfma_f32_32x32x16_bf16 a[80:95], a[36:39], v[232:235], a[80:95]
s_add_u32 m0, s91, 65536
ds_read_b128 a[36:39], v63 offset:1024
v_exp_f32 v55, v139
v_add_f32 v60, v60, v54
v_mfma_f32_32x32x16_bf16 a[112:127], a[40:43], v[232:235], a[112:127]
buffer_load_dwordx4 v36, s[12:15], s24 offen offset:0 lds
ds_read_b128 a[40:43], v63 offset:2048
v_exp_f32 v52, v140
v_add_f32 v60, v60, v55
v_cvt_pk_bf16_f32 v229, v54, v55
v_mfma_f32_32x32x16_bf16 a[144:159], a[44:47], v[232:235], a[144:159]
buffer_load_dwordx4 v37, s[12:15], s24 offen offset:1024 lds
ds_read_b128 a[44:47], v63 offset:3072
v_exp_f32 v53, v141
v_add_f32 v60, v60, v52
v_mfma_f32_32x32x16_bf16 a[64:79], a[48:51], v[236:239], a[64:79]
buffer_load_dwordx4 v38, s[12:15], s24 offen offset:2048 lds
ds_read_b128 a[48:51], v63 offset:4096
v_exp_f32 v54, v142
v_add_f32 v60, v60, v53
v_cvt_pk_bf16_f32 v230, v52, v53
v_mfma_f32_32x32x16_bf16 a[80:95], a[52:55], v[236:239], a[80:95]
buffer_load_dwordx4 v39, s[12:15], s24 offen offset:3072 lds
ds_read_b128 a[52:55], v63 offset:5120
v_exp_f32 v55, v143
v_add_f32 v60, v60, v54
v_mfma_f32_32x32x16_bf16 a[112:127], a[56:59], v[236:239], a[112:127]
ds_read_b128 a[56:59], v63 offset:6144
v_add_f32 v60, v60, v55
v_cvt_pk_bf16_f32 v231, v54, v55
v_mfma_f32_32x32x16_bf16 a[144:159], a[60:63], v[236:239], a[144:159]
s_add_u32 s26, s26, 393216
s_add_u32 s47, s47, 393216
s_add_u32 s25, s25, 128
s_add_u32 s24, s24, 128
ds_read_b128 a[60:63], v63 offset:7168
s_waitcnt lgkmcnt(4)
v_mfma_f32_32x32x16_bf16 a[64:79], a[32:35], v[224:227], a[64:79]
s_add_u32 m0, s91, 81920
ds_read_b128 a[32:35], v63 offset:8192
v_exp_f32 v52, v144
v_exp_f32 v53, v145
v_add_f32 v60, v60, v52
v_mfma_f32_32x32x16_bf16 a[80:95], a[36:39], v[224:227], a[80:95]
buffer_load_dwordx4 v36, s[12:15], s24 offen offset:0 lds
ds_read_b128 a[36:39], v63 offset:9216
v_exp_f32 v54, v146
v_add_f32 v60, v60, v53
v_cvt_pk_bf16_f32 v232, v52, v53
v_mfma_f32_32x32x16_bf16 a[112:127], a[40:43], v[224:227], a[112:127]
buffer_load_dwordx4 v37, s[12:15], s24 offen offset:1024 lds
ds_read_b128 a[40:43], v63 offset:10240
v_exp_f32 v55, v147
v_add_f32 v60, v60, v54
v_exp_f32 v52, v148
v_mfma_f32_32x32x16_bf16 a[144:159], a[44:47], v[224:227], a[144:159]
s_waitcnt lgkmcnt(3)
ds_read_b128 a[44:47], v63 offset:11264
v_add_f32 v60, v60, v55
v_cvt_pk_bf16_f32 v233, v54, v55
v_exp_f32 v53, v149
v_add_f32 v60, v60, v52
v_mfma_f32_32x32x16_bf16 a[64:79], a[48:51], v[228:231], a[64:79]
buffer_load_dwordx4 v38, s[12:15], s24 offen offset:2048 lds
ds_read_b128 a[48:51], v63 offset:12288
v_exp_f32 v54, v150
v_add_f32 v60, v60, v53
v_cvt_pk_bf16_f32 v234, v52, v53
v_mfma_f32_32x32x16_bf16 a[80:95], a[52:55], v[228:231], a[80:95]
buffer_load_dwordx4 v39, s[12:15], s24 offen offset:3072 lds
ds_read_b128 a[52:55], v63 offset:13312
v_exp_f32 v55, v151
v_add_f32 v60, v60, v54
v_exp_f32 v52, v152
v_mfma_f32_32x32x16_bf16 a[112:127], a[56:59], v[228:231], a[112:127]
s_add_u32 s24, s24, 128
ds_read_b128 a[56:59], v63 offset:14336
v_add_f32 v60, v60, v55
v_cvt_pk_bf16_f32 v235, v54, v55
v_exp_f32 v53, v153
v_add_f32 v60, v60, v52
v_mfma_f32_32x32x16_bf16 a[144:159], a[60:63], v[228:231], a[144:159]
s_waitcnt vmcnt(16)
s_barrier
ds_read_b128 a[60:63], v63 offset:15360
v_add_u32 v63, s49, v33
v_exp_f32 v54, v154
v_add_f32 v60, v60, v53
v_cvt_pk_bf16_f32 v236, v52, v53
v_exp_f32 v55, v155
v_add_f32 v60, v60, v54
v_exp_f32 v52, v156
v_add_f32 v60, v60, v55
v_cvt_pk_bf16_f32 v237, v54, v55
v_exp_f32 v53, v157
v_add_f32 v60, v60, v52
v_exp_f32 v54, v158
v_add_f32 v60, v60, v53
v_cvt_pk_bf16_f32 v238, v52, v53
v_exp_f32 v55, v159
v_add_f32 v60, v60, v54
v_add_f32 v60, v60, v55
v_cvt_pk_bf16_f32 v239, v54, v55
s_waitcnt lgkmcnt(4)
v_mfma_f32_32x32x16_bf16 a[64:79], a[32:35], v[232:235], a[64:79]
v_mfma_f32_32x32x16_bf16 a[80:95], a[36:39], v[232:235], a[80:95]
v_mfma_f32_32x32x16_bf16 a[112:127], a[40:43], v[232:235], a[112:127]
v_mfma_f32_32x32x16_bf16 a[144:159], a[44:47], v[232:235], a[144:159]
s_waitcnt lgkmcnt(0)
v_mfma_f32_32x32x16_bf16 a[64:79], a[48:51], v[236:239], a[64:79]
v_mfma_f32_32x32x16_bf16 a[80:95], a[52:55], v[236:239], a[80:95]
v_mfma_f32_32x32x16_bf16 a[112:127], a[56:59], v[236:239], a[112:127]
v_mfma_f32_32x32x16_bf16 a[144:159], a[60:63], v[236:239], a[144:159]
.LEend:
s_cmp_lt_u32 s3, 2
s_cbranch_scc0 .LEend2
s_cmp_eq_u32 s3, 0
s_cbranch_scc0 .LEend1
s_mov_b64 exec, 0
s_branch .Lpad13
.Ltch3:
s_branch .Lpad14
.Ltch4:
s_branch .Lpad16
.Ltch5:
s_branch .Lpad18
.Ltch6:
s_mov_b64 exec, -1
s_branch .LEend3
.LEend1:
s_mov_b64 exec, 0
s_branch .Lpad15
.Ltch7:
s_branch .Lpad17
.Ltch8:
s_branch .Lpad19
.Ltch9:
s_mov_b64 exec, -1
.LEend3:
s_barrier
.LEend2:
v_mov_b32 v8, v60
v_mov_b32 v9, v61
s_add_u32 m0, s48, 98304
s_nop 0
buffer_load_dwordx4 v40, s[16:19], s26 offen offset:0 lds
buffer_load_dwordx4 v41, s[16:19], s26 offen offset:1024 lds
buffer_load_dwordx4 v40, s[16:19], s26 offen offset:2048 lds
buffer_load_dwordx4 v41, s[16:19], s26 offen offset:3072 lds
v_permlane32_swap_b32 v60, v8
v_permlane32_swap_b32 v61, v9
v_add_f32 v60, v60, v8
v_add_f32 v61, v61, v9
s_add_u32 m0, s48, 102400
s_nop 0
buffer_load_dwordx4 v40, s[16:19], s47 offen offset:0 lds
buffer_load_dwordx4 v41, s[16:19], s47 offen offset:1024 lds
buffer_load_dwordx4 v42, s[12:15], s25 offen offset:2048 lds
s_add_u32 m0, s91, 0
s_nop 0
buffer_load_dwordx4 v36, s[12:15], s24 offen offset:0 lds
buffer_load_dwordx4 v37, s[12:15], s24 offen offset:1024 lds
buffer_load_dwordx4 v38, s[12:15], s24 offen offset:2048 lds
buffer_load_dwordx4 v39, s[12:15], s24 offen offset:3072 lds
s_add_u32 s26, s26, 393216
s_add_u32 s47, s47, 393216
s_add_u32 s25, s25, 128
s_add_u32 s24, s24, 128
s_mov_b32 s28, 1602224128
v_cmp_nge_f32 s[30:31], s28, v60
v_cmp_nge_f32 s[32:33], s28, v61
s_or_b64 s[34:35], s[30:31], s[32:33]
s_cbranch_scc1 .Lx
.Lx_ret:
v_rcp_f32 v52, v60
v_rcp_f32 v53, v61
s_lshl_b32 s28, s88, 13
s_lshl_b32 s29, s3, 18
s_add_u32 s28, s28, s29
s_add_u32 s28, s28, 33554432
s_add_u32 s20, s84, s28
s_addc_u32 s21, s85, 0
s_and_b32 s21, s21, 0xffff
s_mov_b32 s22, 16777216
s_mov_b32 s23, 0x20000
s_sub_u32 s40, 15, s89
s_lshl_b32 s40, s40, 20
s_add_u32 s41, s40, 4096
s_lshl_b32 s42, s89, 20
s_add_u32 s43, s42, 4096
s_cmp_lt_u32 s3, 2
s_waitcnt vmcnt(15)
s_barrier
ds_read_b128 a[128:131], v45
ds_read_b128 a[132:135], v43 offset:0
ds_read_b64_tr_b16 a[164:165], v47 offset:0
ds_read_b64_tr_b16 a[166:167], v47 offset:512
ds_read_b64_tr_b16 a[168:169], v48 offset:0
ds_read_b64_tr_b16 a[170:171], v48 offset:512
ds_read_b128 a[136:139], v43 offset:2048
ds_read_b128 a[140:143], v43 offset:4096
ds_read_b128 a[160:163], v43 offset:6144
ds_read_b64_tr_b16 a[172:173], v47 offset:2048
ds_read_b64_tr_b16 a[174:175], v47 offset:2560
ds_read_b64_tr_b16 a[192:193], v48 offset:2048
ds_read_b64_tr_b16 a[194:195], v48 offset:2560
s_cselect_b32 s39, 1, 0
s_waitcnt lgkmcnt(8)
ds_read_b64_tr_b16 a[196:197], v49 offset:4096
ds_read_b64_tr_b16 a[198:199], v49 offset:4608
ds_read_b64_tr_b16 a[200:201], v50 offset:4096
ds_read_b64_tr_b16 a[202:203], v50 offset:4608
s_waitcnt lgkmcnt(0)
v_mfma_f32_16x16x32_bf16 v[160:163], a[172:175], a[128:131], 0
v_mfma_f32_16x16x32_bf16 v[164:167], a[192:195], a[128:131], 0
s_add_u32 m0, s48, 32768
ds_read_b64_tr_b16 a[20:21], v47 offset:14336
ds_read_b64_tr_b16 a[22:23], v47 offset:14848
v_mfma_f32_16x16x32_bf16 v[200:203], a[128:131], a[196:199], 0
buffer_load_dwordx4 v40, s[16:19], s26 offen offset:0 lds
v_accvgpr_read_b32 v72, a64
v_mfma_f32_16x16x32_bf16 v[204:207], a[128:131], a[200:203], 0
ds_read_b128 a[0:3], v46
v_accvgpr_read_b32 v73, a65
v_mfma_f32_16x16x32_bf16 v[128:131], a[164:167], a[132:135], 0
ds_read_b64_tr_b16 a[24:25], v48 offset:14336
ds_read_b64_tr_b16 a[26:27], v48 offset:14848
v_mfma_f32_16x16x32_bf16 v[132:135], a[168:171], a[132:135], 0
buffer_load_dwordx4 v41, s[16:19], s26 offen offset:1024 lds
v_mul_f32 v72, v52, v72
v_mfma_f32_16x16x32_bf16 v[168:171], a[172:175], a[132:135], 0
ds_read_b128 a[4:7], v44 offset:0
v_mul_f32 v73, v52, v73
v_mfma_f32_16x16x32_bf16 v[172:175], a[192:195], a[132:135], 0
ds_read_b64_tr_b16 a[28:29], v47 offset:16384
ds_read_b64_tr_b16 a[30:31], v47 offset:16896
v_mfma_f32_16x16x32_bf16 v[208:211], a[132:135], a[196:199], 0
buffer_load_dwordx4 v40, s[16:19], s26 offen offset:2048 lds
v_cvt_pk_bf16_f32 v64, v72, v73
v_mfma_f32_16x16x32_bf16 v[212:215], a[132:135], a[200:203], 0
ds_read_b128 a[8:11], v44 offset:2048
v_accvgpr_read_b32 v74, a66
v_mfma_f32_16x16x32_bf16 v[136:139], a[164:167], a[136:139], 0
ds_read_b64_tr_b16 a[32:33], v48 offset:16384
ds_read_b64_tr_b16 a[34:35], v48 offset:16896
v_mfma_f32_16x16x32_bf16 v[140:143], a[168:171], a[136:139], 0
buffer_load_dwordx4 v41, s[16:19], s26 offen offset:3072 lds
v_accvgpr_read_b32 v75, a67
v_mfma_f32_16x16x32_bf16 v[176:179], a[172:175], a[136:139], 0
ds_read_b128 a[12:15], v44 offset:4096
v_mul_f32 v74, v52, v74
v_mfma_f32_16x16x32_bf16 v[180:183], a[192:195], a[136:139], 0
s_add_u32 m0, s48, 36864
ds_read_b64_tr_b16 a[36:37], v49 offset:18432
ds_read_b64_tr_b16 a[38:39], v49 offset:18944
v_mfma_f32_16x16x32_bf16 v[216:219], a[136:139], a[196:199], 0
buffer_load_dwordx4 v40, s[16:19], s47 offen offset:0 lds
v_mul_f32 v75, v52, v75
v_mfma_f32_16x16x32_bf16 v[220:223], a[136:139], a[200:203], 0
ds_read_b128 a[16:19], v44 offset:6144
v_cvt_pk_bf16_f32 v65, v74, v75
v_mfma_f32_16x16x32_bf16 v[144:147], a[164:167], a[140:143], 0
ds_read_b64_tr_b16 a[40:41], v50 offset:18432
ds_read_b64_tr_b16 a[42:43], v50 offset:18944
v_mfma_f32_16x16x32_bf16 v[148:151], a[168:171], a[140:143], 0
s_cmp_eq_u32 s39, 1
s_mov_b32 s28, 16384
s_cselect_b32 s28, 4294934528, s28
buffer_load_dwordx4 v41, s[16:19], s47 offen offset:1024 lds
v_mfma_f32_16x16x32_bf16 v[184:187], a[172:175], a[140:143], 0
v_add_u32 v43, 16384, v43
v_add_u32 v44, 16384, v44
v_mfma_f32_16x16x32_bf16 v[188:191], a[192:195], a[140:143], 0
v_add_u32 v47, 4294934528, v47
v_add_u32 v48, 4294934528, v48
v_mfma_f32_16x16x32_bf16 v[224:227], a[140:143], a[196:199], 0
buffer_load_dwordx4 v42, s[12:15], s25 offen offset:2048 lds
v_add_u32 v49, 4294934528, v49
v_add_u32 v50, 4294934528, v50
v_mfma_f32_16x16x32_bf16 v[228:231], a[140:143], a[200:203], 0
s_add_u32 s26, s26, 393216
s_add_u32 s47, s47, 393216
s_add_u32 s25, s25, 128
v_add_u32 v45, s28, v45
v_add_u32 v46, s28, v46
v_mfma_f32_16x16x32_bf16 v[152:155], a[164:167], a[160:163], 0
s_add_u32 m0, s91, 16384
v_accvgpr_read_b32 v72, a68
v_accvgpr_read_b32 v73, a69
v_mfma_f32_16x16x32_bf16 v[156:159], a[168:171], a[160:163], 0
buffer_load_dwordx4 v36, s[12:15], s24 offen offset:0 lds
v_mul_f32 v72, v52, v72
v_mfma_f32_16x16x32_bf16 v[192:195], a[172:175], a[160:163], 0
buffer_load_dwordx4 v37, s[12:15], s24 offen offset:1024 lds
v_mul_f32 v73, v52, v73
v_mfma_f32_16x16x32_bf16 v[196:199], a[192:195], a[160:163], 0
buffer_load_dwordx4 v38, s[12:15], s24 offen offset:2048 lds
v_cvt_pk_bf16_f32 v66, v72, v73
v_mfma_f32_16x16x32_bf16 v[232:235], a[160:163], a[196:199], 0
buffer_load_dwordx4 v39, s[12:15], s24 offen offset:3072 lds
v_accvgpr_read_b32 v74, a70
v_mfma_f32_16x16x32_bf16 v[236:239], a[160:163], a[200:203], 0
s_waitcnt vmcnt(15) lgkmcnt(0)
s_barrier
v_accvgpr_read_b32 v75, a71
.Lpad13:
s_cbranch_execz .Ltch3
v_mfma_f32_16x16x32_bf16 v[160:163], a[28:31], a[0:3], v[160:163]
s_add_u32 s24, s24, 128
v_mul_f32 v74, v52, v74
v_mul_f32 v75, v52, v75
v_mfma_f32_16x16x32_bf16 v[164:167], a[32:35], a[0:3], v[164:167]
s_add_u32 m0, s48, 131072
ds_read_b64_tr_b16 a[164:165], v47 offset:0
ds_read_b64_tr_b16 a[166:167], v47 offset:512
v_mfma_f32_16x16x32_bf16 v[200:203], a[0:3], a[36:39], v[200:203]
buffer_load_dwordx4 v40, s[16:19], s26 offen offset:0 lds
v_cvt_pk_bf16_f32 v67, v74, v75
v_mfma_f32_16x16x32_bf16 v[204:207], a[0:3], a[40:43], v[204:207]
ds_read_b128 a[128:131], v45
v_accvgpr_read_b32 v72, a72
v_mfma_f32_16x16x32_bf16 v[128:131], a[20:23], a[4:7], v[128:131]
ds_read_b64_tr_b16 a[168:169], v48 offset:0
ds_read_b64_tr_b16 a[170:171], v48 offset:512
v_mfma_f32_16x16x32_bf16 v[132:135], a[24:27], a[4:7], v[132:135]
buffer_load_dwordx4 v41, s[16:19], s26 offen offset:1024 lds
v_accvgpr_read_b32 v73, a73
v_mfma_f32_16x16x32_bf16 v[168:171], a[28:31], a[4:7], v[168:171]
ds_read_b128 a[132:135], v43 offset:0
v_mul_f32 v72, v52, v72
v_mfma_f32_16x16x32_bf16 v[172:175], a[32:35], a[4:7], v[172:175]
ds_read_b64_tr_b16 a[172:173], v47 offset:2048
ds_read_b64_tr_b16 a[174:175], v47 offset:2560
v_mfma_f32_16x16x32_bf16 v[208:211], a[4:7], a[36:39], v[208:211]
buffer_load_dwordx4 v40, s[16:19], s26 offen offset:2048 lds
v_mul_f32 v73, v52, v73
v_mfma_f32_16x16x32_bf16 v[212:215], a[4:7], a[40:43], v[212:215]
ds_read_b128 a[136:139], v43 offset:2048
v_cvt_pk_bf16_f32 v68, v72, v73
v_mfma_f32_16x16x32_bf16 v[136:139], a[20:23], a[8:11], v[136:139]
ds_read_b64_tr_b16 a[192:193], v48 offset:2048
ds_read_b64_tr_b16 a[194:195], v48 offset:2560
v_mfma_f32_16x16x32_bf16 v[140:143], a[24:27], a[8:11], v[140:143]
buffer_load_dwordx4 v41, s[16:19], s26 offen offset:3072 lds
v_accvgpr_read_b32 v74, a74
v_mfma_f32_16x16x32_bf16 v[176:179], a[28:31], a[8:11], v[176:179]
ds_read_b128 a[140:143], v43 offset:4096
v_accvgpr_read_b32 v75, a75
v_mfma_f32_16x16x32_bf16 v[180:183], a[32:35], a[8:11], v[180:183]
s_add_u32 m0, s48, 135168
ds_read_b64_tr_b16 a[196:197], v49 offset:4096
ds_read_b64_tr_b16 a[198:199], v49 offset:4608
v_mfma_f32_16x16x32_bf16 v[216:219], a[8:11], a[36:39], v[216:219]
buffer_load_dwordx4 v40, s[16:19], s47 offen offset:0 lds
v_mul_f32 v74, v52, v74
v_mfma_f32_16x16x32_bf16 v[220:223], a[8:11], a[40:43], v[220:223]
ds_read_b128 a[160:163], v43 offset:6144
v_mul_f32 v75, v52, v75
v_mfma_f32_16x16x32_bf16 v[144:147], a[20:23], a[12:15], v[144:147]
ds_read_b64_tr_b16 a[200:201], v50 offset:4096
ds_read_b64_tr_b16 a[202:203], v50 offset:4608
v_mfma_f32_16x16x32_bf16 v[148:151], a[24:27], a[12:15], v[148:151]
buffer_load_dwordx4 v41, s[16:19], s47 offen offset:1024 lds
v_cvt_pk_bf16_f32 v69, v74, v75
v_mfma_f32_16x16x32_bf16 v[184:187], a[28:31], a[12:15], v[184:187]
buffer_store_dwordx4 v[64:67], v7, s[20:23], s40 offen offset:0 sc1
v_accvgpr_read_b32 v72, a76
v_mfma_f32_16x16x32_bf16 v[188:191], a[32:35], a[12:15], v[188:191]
v_accvgpr_read_b32 v73, a77
v_mul_f32 v72, v52, v72
v_mfma_f32_16x16x32_bf16 v[224:227], a[12:15], a[36:39], v[224:227]
buffer_load_dwordx4 v42, s[12:15], s25 offen offset:2048 lds
v_mul_f32 v73, v52, v73
v_mfma_f32_16x16x32_bf16 v[228:231], a[12:15], a[40:43], v[228:231]
v_cvt_pk_bf16_f32 v70, v72, v73
v_accvgpr_read_b32 v74, a78
v_mfma_f32_16x16x32_bf16 v[152:155], a[20:23], a[16:19], v[152:155]
s_add_u32 s26, s26, 393216
s_add_u32 s47, s47, 393216
s_add_u32 s25, s25, 128
v_mfma_f32_16x16x32_bf16 v[156:159], a[24:27], a[16:19], v[156:159]
v_accvgpr_read_b32 v75, a79
v_mul_f32 v74, v52, v74
v_mfma_f32_16x16x32_bf16 v[192:195], a[28:31], a[16:19], v[192:195]
v_mul_f32 v75, v52, v75
v_cvt_pk_bf16_f32 v71, v74, v75
v_mfma_f32_16x16x32_bf16 v[196:199], a[32:35], a[16:19], v[196:199]
buffer_store_dwordx4 v[68:71], v7, s[20:23], s40 offen offset:1024 sc1
v_mfma_f32_16x16x32_bf16 v[232:235], a[16:19], a[36:39], v[232:235]
v_mfma_f32_16x16x32_bf16 v[236:239], a[16:19], a[40:43], v[236:239]
s_waitcnt lgkmcnt(0)
v_mfma_f32_16x16x32_bf16 v[160:163], a[172:175], a[128:131], v[160:163]
v_mfma_f32_16x16x32_bf16 v[164:167], a[192:195], a[128:131], v[164:167]
s_add_u32 m0, s91, 65536
ds_read_b64_tr_b16 a[20:21], v47 offset:14336
ds_read_b64_tr_b16 a[22:23], v47 offset:14848
v_mfma_f32_16x16x32_bf16 v[200:203], a[128:131], a[196:199], v[200:203]
buffer_load_dwordx4 v36, s[12:15], s24 offen offset:0 lds
v_accvgpr_read_b32 v72, a80
v_mfma_f32_16x16x32_bf16 v[204:207], a[128:131], a[200:203], v[204:207]
ds_read_b128 a[0:3], v46
v_accvgpr_read_b32 v73, a81
v_mfma_f32_16x16x32_bf16 v[128:131], a[164:167], a[132:135], v[128:131]
ds_read_b64_tr_b16 a[24:25], v48 offset:14336
ds_read_b64_tr_b16 a[26:27], v48 offset:14848
v_mfma_f32_16x16x32_bf16 v[132:135], a[168:171], a[132:135], v[132:135]
v_mul_f32 v72, v52, v72
v_mul_f32 v73, v52, v73
v_mfma_f32_16x16x32_bf16 v[168:171], a[172:175], a[132:135], v[168:171]
ds_read_b128 a[4:7], v44 offset:0
v_cvt_pk_bf16_f32 v64, v72, v73
v_mfma_f32_16x16x32_bf16 v[172:175], a[192:195], a[132:135], v[172:175]
ds_read_b64_tr_b16 a[28:29], v47 offset:16384
ds_read_b64_tr_b16 a[30:31], v47 offset:16896
v_mfma_f32_16x16x32_bf16 v[208:211], a[132:135], a[196:199], v[208:211]
buffer_load_dwordx4 v37, s[12:15], s24 offen offset:1024 lds
v_accvgpr_read_b32 v74, a82
v_mfma_f32_16x16x32_bf16 v[212:215], a[132:135], a[200:203], v[212:215]
ds_read_b128 a[8:11], v44 offset:2048
v_accvgpr_read_b32 v75, a83
v_mfma_f32_16x16x32_bf16 v[136:139], a[164:167], a[136:139], v[136:139]
ds_read_b64_tr_b16 a[32:33], v48 offset:16384
ds_read_b64_tr_b16 a[34:35], v48 offset:16896
v_mfma_f32_16x16x32_bf16 v[140:143], a[168:171], a[136:139], v[140:143]
v_mul_f32 v74, v52, v74
v_mul_f32 v75, v52, v75
v_mfma_f32_16x16x32_bf16 v[176:179], a[172:175], a[136:139], v[176:179]
ds_read_b128 a[12:15], v44 offset:4096
v_cvt_pk_bf16_f32 v65, v74, v75
v_mfma_f32_16x16x32_bf16 v[180:183], a[192:195], a[136:139], v[180:183]
ds_read_b64_tr_b16 a[36:37], v49 offset:18432
ds_read_b64_tr_b16 a[38:39], v49 offset:18944
v_mfma_f32_16x16x32_bf16 v[216:219], a[136:139], a[196:199], v[216:219]
buffer_load_dwordx4 v38, s[12:15], s24 offen offset:2048 lds
v_accvgpr_read_b32 v72, a84
v_mfma_f32_16x16x32_bf16 v[220:223], a[136:139], a[200:203], v[220:223]
ds_read_b128 a[16:19], v44 offset:6144
v_accvgpr_read_b32 v73, a85
v_mfma_f32_16x16x32_bf16 v[144:147], a[164:167], a[140:143], v[144:147]
ds_read_b64_tr_b16 a[40:41], v50 offset:18432
ds_read_b64_tr_b16 a[42:43], v50 offset:18944
v_mfma_f32_16x16x32_bf16 v[148:151], a[168:171], a[140:143], v[148:151]
s_cmp_eq_u32 s39, 1
s_mov_b32 s28, 4294885376
s_cselect_b32 s28, 4294901760, s28
v_mfma_f32_16x16x32_bf16 v[184:187], a[172:175], a[140:143], v[184:187]
v_add_u32 v43, 4294885376, v43
v_add_u32 v44, 4294885376, v44
v_mfma_f32_16x16x32_bf16 v[188:191], a[192:195], a[140:143], v[188:191]
v_add_u32 v47, 4294901760, v47
v_add_u32 v48, 4294901760, v48
v_mfma_f32_16x16x32_bf16 v[224:227], a[140:143], a[196:199], v[224:227]
buffer_load_dwordx4 v39, s[12:15], s24 offen offset:3072 lds
v_add_u32 v49, 4294901760, v49
v_add_u32 v50, 4294901760, v50
v_mfma_f32_16x16x32_bf16 v[228:231], a[140:143], a[200:203], v[228:231]
v_add_u32 v45, s28, v45
v_add_u32 v46, s28, v46
v_mfma_f32_16x16x32_bf16 v[152:155], a[164:167], a[160:163], v[152:155]
v_mul_f32 v72, v52, v72
v_mul_f32 v73, v52, v73
v_mfma_f32_16x16x32_bf16 v[156:159], a[168:171], a[160:163], v[156:159]
s_add_u32 s24, s24, 128
v_cvt_pk_bf16_f32 v66, v72, v73
v_accvgpr_read_b32 v74, a86
v_mfma_f32_16x16x32_bf16 v[192:195], a[172:175], a[160:163], v[192:195]
v_accvgpr_read_b32 v75, a87
v_mul_f32 v74, v52, v74
v_mfma_f32_16x16x32_bf16 v[196:199], a[192:195], a[160:163], v[196:199]
v_mul_f32 v75, v52, v75
v_cvt_pk_bf16_f32 v67, v74, v75
v_mfma_f32_16x16x32_bf16 v[232:235], a[160:163], a[196:199], v[232:235]
buffer_store_dwordx4 v[64:67], v7, s[20:23], s40 offen offset:2048 sc1
v_mfma_f32_16x16x32_bf16 v[236:239], a[160:163], a[200:203], v[236:239]
s_waitcnt vmcnt(18) lgkmcnt(0)
s_barrier
v_mfma_f32_16x16x32_bf16 v[160:163], a[28:31], a[0:3], v[160:163]
v_mfma_f32_16x16x32_bf16 v[164:167], a[32:35], a[0:3], v[164:167]
s_add_u32 m0, s48, 98304
ds_read_b64_tr_b16 a[164:165], v47 offset:0
ds_read_b64_tr_b16 a[166:167], v47 offset:512
v_mfma_f32_16x16x32_bf16 v[200:203], a[0:3], a[36:39], v[200:203]
buffer_load_dwordx4 v40, s[16:19], s26 offen offset:0 lds
v_accvgpr_read_b32 v72, a88
v_mfma_f32_16x16x32_bf16 v[204:207], a[0:3], a[40:43], v[204:207]
ds_read_b128 a[128:131], v45
v_accvgpr_read_b32 v73, a89
v_mfma_f32_16x16x32_bf16 v[128:131], a[20:23], a[4:7], v[128:131]
ds_read_b64_tr_b16 a[168:169], v48 offset:0
ds_read_b64_tr_b16 a[170:171], v48 offset:512
v_mfma_f32_16x16x32_bf16 v[132:135], a[24:27], a[4:7], v[132:135]
buffer_load_dwordx4 v41, s[16:19], s26 offen offset:1024 lds
v_mul_f32 v72, v52, v72
v_mfma_f32_16x16x32_bf16 v[168:171], a[28:31], a[4:7], v[168:171]
ds_read_b128 a[132:135], v43 offset:0
v_mul_f32 v73, v52, v73
v_mfma_f32_16x16x32_bf16 v[172:175], a[32:35], a[4:7], v[172:175]
ds_read_b64_tr_b16 a[172:173], v47 offset:2048
ds_read_b64_tr_b16 a[174:175], v47 offset:2560
v_mfma_f32_16x16x32_bf16 v[208:211], a[4:7], a[36:39], v[208:211]
buffer_load_dwordx4 v40, s[16:19], s26 offen offset:2048 lds
v_cvt_pk_bf16_f32 v68, v72, v73
v_mfma_f32_16x16x32_bf16 v[212:215], a[4:7], a[40:43], v[212:215]
ds_read_b128 a[136:139], v43 offset:2048
v_accvgpr_read_b32 v74, a90
v_mfma_f32_16x16x32_bf16 v[136:139], a[20:23], a[8:11], v[136:139]
ds_read_b64_tr_b16 a[192:193], v48 offset:2048
ds_read_b64_tr_b16 a[194:195], v48 offset:2560
v_mfma_f32_16x16x32_bf16 v[140:143], a[24:27], a[8:11], v[140:143]
buffer_load_dwordx4 v41, s[16:19], s26 offen offset:3072 lds
v_accvgpr_read_b32 v75, a91
v_mfma_f32_16x16x32_bf16 v[176:179], a[28:31], a[8:11], v[176:179]
ds_read_b128 a[140:143], v43 offset:4096
v_mul_f32 v74, v52, v74
v_mfma_f32_16x16x32_bf16 v[180:183], a[32:35], a[8:11], v[180:183]
s_add_u32 m0, s48, 102400
ds_read_b64_tr_b16 a[196:197], v49 offset:4096
ds_read_b64_tr_b16 a[198:199], v49 offset:4608
v_mfma_f32_16x16x32_bf16 v[216:219], a[8:11], a[36:39], v[216:219]
buffer_load_dwordx4 v40, s[16:19], s47 offen offset:0 lds
v_mul_f32 v75, v52, v75
v_mfma_f32_16x16x32_bf16 v[220:223], a[8:11], a[40:43], v[220:223]
ds_read_b128 a[160:163], v43 offset:6144
v_cvt_pk_bf16_f32 v69, v74, v75
v_mfma_f32_16x16x32_bf16 v[144:147], a[20:23], a[12:15], v[144:147]
ds_read_b64_tr_b16 a[200:201], v50 offset:4096
ds_read_b64_tr_b16 a[202:203], v50 offset:4608
v_mfma_f32_16x16x32_bf16 v[148:151], a[24:27], a[12:15], v[148:151]
buffer_load_dwordx4 v41, s[16:19], s47 offen offset:1024 lds
v_accvgpr_read_b32 v72, a92
v_mfma_f32_16x16x32_bf16 v[184:187], a[28:31], a[12:15], v[184:187]
v_accvgpr_read_b32 v73, a93
v_mul_f32 v72, v52, v72
v_mfma_f32_16x16x32_bf16 v[188:191], a[32:35], a[12:15], v[188:191]
v_mul_f32 v73, v52, v73
v_cvt_pk_bf16_f32 v70, v72, v73
v_mfma_f32_16x16x32_bf16 v[224:227], a[12:15], a[36:39], v[224:227]
buffer_load_dwordx4 v42, s[12:15], s25 offen offset:2048 lds
v_accvgpr_read_b32 v74, a94
v_mfma_f32_16x16x32_bf16 v[228:231], a[12:15], a[40:43], v[228:231]
v_accvgpr_read_b32 v75, a95
v_mul_f32 v74, v52, v74
v_mfma_f32_16x16x32_bf16 v[152:155], a[20:23], a[16:19], v[152:155]
s_add_u32 s26, s26, 393216
s_add_u32 s47, s47, 393216
s_add_u32 s25, s25, 128
v_mfma_f32_16x16x32_bf16 v[156:159], a[24:27], a[16:19], v[156:159]
v_mul_f32 v75, v52, v75
v_cvt_pk_bf16_f32 v71, v74, v75
v_mfma_f32_16x16x32_bf16 v[192:195], a[28:31], a[16:19], v[192:195]
buffer_store_dwordx4 v[68:71], v7, s[20:23], s40 offen offset:3072 sc1
v_mfma_f32_16x16x32_bf16 v[196:199], a[32:35], a[16:19], v[196:199]
v_mfma_f32_16x16x32_bf16 v[232:235], a[16:19], a[36:39], v[232:235]
v_mfma_f32_16x16x32_bf16 v[236:239], a[16:19], a[40:43], v[236:239]
s_waitcnt lgkmcnt(0)
v_mfma_f32_16x16x32_bf16 v[160:163], a[172:175], a[128:131], v[160:163]
v_mfma_f32_16x16x32_bf16 v[164:167], a[192:195], a[128:131], v[164:167]
s_add_u32 m0, s91, 81920
ds_read_b64_tr_b16 a[20:21], v47 offset:14336
ds_read_b64_tr_b16 a[22:23], v47 offset:14848
v_mfma_f32_16x16x32_bf16 v[200:203], a[128:131], a[196:199], v[200:203]
buffer_load_dwordx4 v36, s[12:15], s24 offen offset:0 lds
v_accvgpr_read_b32 v72, a112
v_mfma_f32_16x16x32_bf16 v[204:207], a[128:131], a[200:203], v[204:207]
ds_read_b128 a[0:3], v46
v_accvgpr_read_b32 v73, a113
v_mfma_f32_16x16x32_bf16 v[128:131], a[164:167], a[132:135], v[128:131]
ds_read_b64_tr_b16 a[24:25], v48 offset:14336
ds_read_b64_tr_b16 a[26:27], v48 offset:14848
v_mfma_f32_16x16x32_bf16 v[132:135], a[168:171], a[132:135], v[132:135]
v_mul_f32 v72, v52, v72
v_mul_f32 v73, v52, v73
v_mfma_f32_16x16x32_bf16 v[168:171], a[172:175], a[132:135], v[168:171]
ds_read_b128 a[4:7], v44 offset:0
v_cvt_pk_bf16_f32 v64, v72, v73
v_mfma_f32_16x16x32_bf16 v[172:175], a[192:195], a[132:135], v[172:175]
ds_read_b64_tr_b16 a[28:29], v47 offset:16384
ds_read_b64_tr_b16 a[30:31], v47 offset:16896
v_mfma_f32_16x16x32_bf16 v[208:211], a[132:135], a[196:199], v[208:211]
buffer_load_dwordx4 v37, s[12:15], s24 offen offset:1024 lds
v_accvgpr_read_b32 v74, a114
v_mfma_f32_16x16x32_bf16 v[212:215], a[132:135], a[200:203], v[212:215]
ds_read_b128 a[8:11], v44 offset:2048
v_accvgpr_read_b32 v75, a115
v_mfma_f32_16x16x32_bf16 v[136:139], a[164:167], a[136:139], v[136:139]
ds_read_b64_tr_b16 a[32:33], v48 offset:16384
ds_read_b64_tr_b16 a[34:35], v48 offset:16896
v_mfma_f32_16x16x32_bf16 v[140:143], a[168:171], a[136:139], v[140:143]
v_mul_f32 v74, v52, v74
v_mul_f32 v75, v52, v75
v_mfma_f32_16x16x32_bf16 v[176:179], a[172:175], a[136:139], v[176:179]
ds_read_b128 a[12:15], v44 offset:4096
v_cvt_pk_bf16_f32 v65, v74, v75
v_mfma_f32_16x16x32_bf16 v[180:183], a[192:195], a[136:139], v[180:183]
ds_read_b64_tr_b16 a[36:37], v49 offset:18432
ds_read_b64_tr_b16 a[38:39], v49 offset:18944
v_mfma_f32_16x16x32_bf16 v[216:219], a[136:139], a[196:199], v[216:219]
buffer_load_dwordx4 v38, s[12:15], s24 offen offset:2048 lds
v_accvgpr_read_b32 v72, a116
v_mfma_f32_16x16x32_bf16 v[220:223], a[136:139], a[200:203], v[220:223]
ds_read_b128 a[16:19], v44 offset:6144
v_accvgpr_read_b32 v73, a117
v_mfma_f32_16x16x32_bf16 v[144:147], a[164:167], a[140:143], v[144:147]
ds_read_b64_tr_b16 a[40:41], v50 offset:18432
ds_read_b64_tr_b16 a[42:43], v50 offset:18944
v_mfma_f32_16x16x32_bf16 v[148:151], a[168:171], a[140:143], v[148:151]
s_cmp_eq_u32 s39, 1
s_mov_b32 s28, 16384
s_cselect_b32 s28, 98304, s28
v_mfma_f32_16x16x32_bf16 v[184:187], a[172:175], a[140:143], v[184:187]
v_add_u32 v43, 16384, v43
v_add_u32 v44, 16384, v44
v_mfma_f32_16x16x32_bf16 v[188:191], a[192:195], a[140:143], v[188:191]
v_add_u32 v47, 98304, v47
v_add_u32 v48, 98304, v48
v_mfma_f32_16x16x32_bf16 v[224:227], a[140:143], a[196:199], v[224:227]
buffer_load_dwordx4 v39, s[12:15], s24 offen offset:3072 lds
v_add_u32 v49, 98304, v49
v_add_u32 v50, 98304, v50
v_mfma_f32_16x16x32_bf16 v[228:231], a[140:143], a[200:203], v[228:231]
v_add_u32 v45, s28, v45
v_add_u32 v46, s28, v46
v_mfma_f32_16x16x32_bf16 v[152:155], a[164:167], a[160:163], v[152:155]
v_mul_f32 v72, v52, v72
v_mul_f32 v73, v52, v73
v_mfma_f32_16x16x32_bf16 v[156:159], a[168:171], a[160:163], v[156:159]
s_add_u32 s24, s24, 128
v_cvt_pk_bf16_f32 v66, v72, v73
v_accvgpr_read_b32 v74, a118
v_mfma_f32_16x16x32_bf16 v[192:195], a[172:175], a[160:163], v[192:195]
v_accvgpr_read_b32 v75, a119
v_mul_f32 v74, v52, v74
v_mfma_f32_16x16x32_bf16 v[196:199], a[192:195], a[160:163], v[196:199]
v_mul_f32 v75, v52, v75
v_cvt_pk_bf16_f32 v67, v74, v75
v_mfma_f32_16x16x32_bf16 v[232:235], a[160:163], a[196:199], v[232:235]
buffer_store_dwordx4 v[64:67], v7, s[20:23], s41 offen offset:0 sc1
v_mfma_f32_16x16x32_bf16 v[236:239], a[160:163], a[200:203], v[236:239]
s_waitcnt vmcnt(19) lgkmcnt(0)
s_barrier
v_mfma_f32_16x16x32_bf16 v[160:163], a[28:31], a[0:3], v[160:163]
v_mfma_f32_16x16x32_bf16 v[164:167], a[32:35], a[0:3], v[164:167]
s_add_u32 m0, s48, 32768
ds_read_b64_tr_b16 a[164:165], v47 offset:0
ds_read_b64_tr_b16 a[166:167], v47 offset:512
v_mfma_f32_16x16x32_bf16 v[200:203], a[0:3], a[36:39], v[200:203]
buffer_load_dwordx4 v40, s[16:19], s26 offen offset:0 lds
v_accvgpr_read_b32 v72, a120
v_mfma_f32_16x16x32_bf16 v[204:207], a[0:3], a[40:43], v[204:207]
ds_read_b128 a[128:131], v45
v_accvgpr_read_b32 v73, a121
v_mfma_f32_16x16x32_bf16 v[128:131], a[20:23], a[4:7], v[128:131]
ds_read_b64_tr_b16 a[168:169], v48 offset:0
ds_read_b64_tr_b16 a[170:171], v48 offset:512
v_mfma_f32_16x16x32_bf16 v[132:135], a[24:27], a[4:7], v[132:135]
buffer_load_dwordx4 v41, s[16:19], s26 offen offset:1024 lds
v_mul_f32 v72, v52, v72
v_mfma_f32_16x16x32_bf16 v[168:171], a[28:31], a[4:7], v[168:171]
ds_read_b128 a[132:135], v43 offset:0
v_mul_f32 v73, v52, v73
v_mfma_f32_16x16x32_bf16 v[172:175], a[32:35], a[4:7], v[172:175]
ds_read_b64_tr_b16 a[172:173], v47 offset:2048
ds_read_b64_tr_b16 a[174:175], v47 offset:2560
v_mfma_f32_16x16x32_bf16 v[208:211], a[4:7], a[36:39], v[208:211]
buffer_load_dwordx4 v40, s[16:19], s26 offen offset:2048 lds
v_cvt_pk_bf16_f32 v68, v72, v73
v_mfma_f32_16x16x32_bf16 v[212:215], a[4:7], a[40:43], v[212:215]
ds_read_b128 a[136:139], v43 offset:2048
v_accvgpr_read_b32 v74, a122
v_mfma_f32_16x16x32_bf16 v[136:139], a[20:23], a[8:11], v[136:139]
ds_read_b64_tr_b16 a[192:193], v48 offset:2048
ds_read_b64_tr_b16 a[194:195], v48 offset:2560
v_mfma_f32_16x16x32_bf16 v[140:143], a[24:27], a[8:11], v[140:143]
buffer_load_dwordx4 v41, s[16:19], s26 offen offset:3072 lds
v_accvgpr_read_b32 v75, a123
v_mfma_f32_16x16x32_bf16 v[176:179], a[28:31], a[8:11], v[176:179]
ds_read_b128 a[140:143], v43 offset:4096
v_mul_f32 v74, v52, v74
v_mfma_f32_16x16x32_bf16 v[180:183], a[32:35], a[8:11], v[180:183]
s_add_u32 m0, s48, 36864
ds_read_b64_tr_b16 a[196:197], v49 offset:4096
ds_read_b64_tr_b16 a[198:199], v49 offset:4608
v_mfma_f32_16x16x32_bf16 v[216:219], a[8:11], a[36:39], v[216:219]
buffer_load_dwordx4 v40, s[16:19], s47 offen offset:0 lds
v_mul_f32 v75, v52, v75
v_mfma_f32_16x16x32_bf16 v[220:223], a[8:11], a[40:43], v[220:223]
ds_read_b128 a[160:163], v43 offset:6144
v_cvt_pk_bf16_f32 v69, v74, v75
v_mfma_f32_16x16x32_bf16 v[144:147], a[20:23], a[12:15], v[144:147]
ds_read_b64_tr_b16 a[200:201], v50 offset:4096
ds_read_b64_tr_b16 a[202:203], v50 offset:4608
v_mfma_f32_16x16x32_bf16 v[148:151], a[24:27], a[12:15], v[148:151]
buffer_load_dwordx4 v41, s[16:19], s47 offen offset:1024 lds
v_accvgpr_read_b32 v72, a124
v_mfma_f32_16x16x32_bf16 v[184:187], a[28:31], a[12:15], v[184:187]
v_accvgpr_read_b32 v73, a125
v_mul_f32 v72, v52, v72
v_mfma_f32_16x16x32_bf16 v[188:191], a[32:35], a[12:15], v[188:191]
v_mul_f32 v73, v52, v73
v_cvt_pk_bf16_f32 v70, v72, v73
v_mfma_f32_16x16x32_bf16 v[224:227], a[12:15], a[36:39], v[224:227]
buffer_load_dwordx4 v42, s[12:15], s25 offen offset:2048 lds
v_accvgpr_read_b32 v74, a126
v_mfma_f32_16x16x32_bf16 v[228:231], a[12:15], a[40:43], v[228:231]
v_accvgpr_read_b32 v75, a127
v_mul_f32 v74, v52, v74
v_mfma_f32_16x16x32_bf16 v[152:155], a[20:23], a[16:19], v[152:155]
s_add_u32 s26, s26, 393216
s_add_u32 s47, s47, 393216
s_add_u32 s25, s25, 128
v_mfma_f32_16x16x32_bf16 v[156:159], a[24:27], a[16:19], v[156:159]
v_mul_f32 v75, v52, v75
v_cvt_pk_bf16_f32 v71, v74, v75
v_mfma_f32_16x16x32_bf16 v[192:195], a[28:31], a[16:19], v[192:195]
buffer_store_dwordx4 v[68:71], v7, s[20:23], s41 offen offset:1024 sc1
v_mfma_f32_16x16x32_bf16 v[196:199], a[32:35], a[16:19], v[196:199]
v_mfma_f32_16x16x32_bf16 v[232:235], a[16:19], a[36:39], v[232:235]
v_mfma_f32_16x16x32_bf16 v[236:239], a[16:19], a[40:43], v[236:239]
s_waitcnt lgkmcnt(0)
v_mfma_f32_16x16x32_bf16 v[160:163], a[172:175], a[128:131], v[160:163]
v_mfma_f32_16x16x32_bf16 v[164:167], a[192:195], a[128:131], v[164:167]
s_add_u32 m0, s91, 0
ds_read_b64_tr_b16 a[20:21], v47 offset:14336
ds_read_b64_tr_b16 a[22:23], v47 offset:14848
v_mfma_f32_16x16x32_bf16 v[200:203], a[128:131], a[196:199], v[200:203]
buffer_load_dwordx4 v36, s[12:15], s24 offen offset:0 lds
v_accvgpr_read_b32 v72, a144
v_mfma_f32_16x16x32_bf16 v[204:207], a[128:131], a[200:203], v[204:207]
ds_read_b128 a[0:3], v46
v_accvgpr_read_b32 v73, a145
v_mfma_f32_16x16x32_bf16 v[128:131], a[164:167], a[132:135], v[128:131]
ds_read_b64_tr_b16 a[24:25], v48 offset:14336
ds_read_b64_tr_b16 a[26:27], v48 offset:14848
v_mfma_f32_16x16x32_bf16 v[132:135], a[168:171], a[132:135], v[132:135]
v_mul_f32 v72, v52, v72
v_mul_f32 v73, v52, v73
v_mfma_f32_16x16x32_bf16 v[168:171], a[172:175], a[132:135], v[168:171]
ds_read_b128 a[4:7], v44 offset:0
v_cvt_pk_bf16_f32 v64, v72, v73
v_mfma_f32_16x16x32_bf16 v[172:175], a[192:195], a[132:135], v[172:175]
ds_read_b64_tr_b16 a[28:29], v47 offset:16384
ds_read_b64_tr_b16 a[30:31], v47 offset:16896
v_mfma_f32_16x16x32_bf16 v[208:211], a[132:135], a[196:199], v[208:211]
buffer_load_dwordx4 v37, s[12:15], s24 offen offset:1024 lds
v_accvgpr_read_b32 v74, a146
v_mfma_f32_16x16x32_bf16 v[212:215], a[132:135], a[200:203], v[212:215]
ds_read_b128 a[8:11], v44 offset:2048
v_accvgpr_read_b32 v75, a147
v_mfma_f32_16x16x32_bf16 v[136:139], a[164:167], a[136:139], v[136:139]
ds_read_b64_tr_b16 a[32:33], v48 offset:16384
ds_read_b64_tr_b16 a[34:35], v48 offset:16896
v_mfma_f32_16x16x32_bf16 v[140:143], a[168:171], a[136:139], v[140:143]
v_mul_f32 v74, v52, v74
v_mul_f32 v75, v52, v75
v_mfma_f32_16x16x32_bf16 v[176:179], a[172:175], a[136:139], v[176:179]
ds_read_b128 a[12:15], v44 offset:4096
v_cvt_pk_bf16_f32 v65, v74, v75
v_mfma_f32_16x16x32_bf16 v[180:183], a[192:195], a[136:139], v[180:183]
ds_read_b64_tr_b16 a[36:37], v49 offset:18432
ds_read_b64_tr_b16 a[38:39], v49 offset:18944
v_mfma_f32_16x16x32_bf16 v[216:219], a[136:139], a[196:199], v[216:219]
buffer_load_dwordx4 v38, s[12:15], s24 offen offset:2048 lds
v_accvgpr_read_b32 v72, a148
v_mfma_f32_16x16x32_bf16 v[220:223], a[136:139], a[200:203], v[220:223]
ds_read_b128 a[16:19], v44 offset:6144
v_accvgpr_read_b32 v73, a149
v_mfma_f32_16x16x32_bf16 v[144:147], a[164:167], a[140:143], v[144:147]
ds_read_b64_tr_b16 a[40:41], v50 offset:18432
ds_read_b64_tr_b16 a[42:43], v50 offset:18944
v_mfma_f32_16x16x32_bf16 v[148:151], a[168:171], a[140:143], v[148:151]
s_cmp_eq_u32 s39, 1
s_mov_b32 s28, 49152
s_cselect_b32 s28, 4294934528, s28
v_mfma_f32_16x16x32_bf16 v[184:187], a[172:175], a[140:143], v[184:187]
v_add_u32 v43, 49152, v43
v_add_u32 v44, 49152, v44
v_mfma_f32_16x16x32_bf16 v[188:191], a[192:195], a[140:143], v[188:191]
v_add_u32 v47, 4294934528, v47
v_add_u32 v48, 4294934528, v48
v_mfma_f32_16x16x32_bf16 v[224:227], a[140:143], a[196:199], v[224:227]
buffer_load_dwordx4 v39, s[12:15], s24 offen offset:3072 lds
v_add_u32 v49, 4294934528, v49
v_add_u32 v50, 4294934528, v50
v_mfma_f32_16x16x32_bf16 v[228:231], a[140:143], a[200:203], v[228:231]
v_add_u32 v45, s28, v45
v_add_u32 v46, s28, v46
v_mfma_f32_16x16x32_bf16 v[152:155], a[164:167], a[160:163], v[152:155]
v_mul_f32 v72, v52, v72
v_mul_f32 v73, v52, v73
v_mfma_f32_16x16x32_bf16 v[156:159], a[168:171], a[160:163], v[156:159]
s_add_u32 s24, s24, 128
v_cvt_pk_bf16_f32 v66, v72, v73
v_accvgpr_read_b32 v74, a150
v_mfma_f32_16x16x32_bf16 v[192:195], a[172:175], a[160:163], v[192:195]
v_accvgpr_read_b32 v75, a151
v_mul_f32 v74, v52, v74
v_mfma_f32_16x16x32_bf16 v[196:199], a[192:195], a[160:163], v[196:199]
v_mul_f32 v75, v52, v75
v_cvt_pk_bf16_f32 v67, v74, v75
v_mfma_f32_16x16x32_bf16 v[232:235], a[160:163], a[196:199], v[232:235]
buffer_store_dwordx4 v[64:67], v7, s[20:23], s41 offen offset:2048 sc1
v_mfma_f32_16x16x32_bf16 v[236:239], a[160:163], a[200:203], v[236:239]
s_waitcnt vmcnt(19) lgkmcnt(0)
s_barrier
v_mfma_f32_16x16x32_bf16 v[160:163], a[28:31], a[0:3], v[160:163]
v_mfma_f32_16x16x32_bf16 v[164:167], a[32:35], a[0:3], v[164:167]
s_add_u32 m0, s48, 131072
ds_read_b64_tr_b16 a[164:165], v47 offset:0
ds_read_b64_tr_b16 a[166:167], v47 offset:512
v_mfma_f32_16x16x32_bf16 v[200:203], a[0:3], a[36:39], v[200:203]
buffer_load_dwordx4 v40, s[16:19], s26 offen offset:0 lds
v_accvgpr_read_b32 v72, a152
v_mfma_f32_16x16x32_bf16 v[204:207], a[0:3], a[40:43], v[204:207]
ds_read_b128 a[128:131], v45
v_accvgpr_read_b32 v73, a153
v_mfma_f32_16x16x32_bf16 v[128:131], a[20:23], a[4:7], v[128:131]
ds_read_b64_tr_b16 a[168:169], v48 offset:0
ds_read_b64_tr_b16 a[170:171], v48 offset:512
v_mfma_f32_16x16x32_bf16 v[132:135], a[24:27], a[4:7], v[132:135]
buffer_load_dwordx4 v41, s[16:19], s26 offen offset:1024 lds
v_mul_f32 v72, v52, v72
v_mfma_f32_16x16x32_bf16 v[168:171], a[28:31], a[4:7], v[168:171]
ds_read_b128 a[132:135], v43 offset:0
v_mul_f32 v73, v52, v73
v_mfma_f32_16x16x32_bf16 v[172:175], a[32:35], a[4:7], v[172:175]
ds_read_b64_tr_b16 a[172:173], v47 offset:2048
ds_read_b64_tr_b16 a[174:175], v47 offset:2560
v_mfma_f32_16x16x32_bf16 v[208:211], a[4:7], a[36:39], v[208:211]
buffer_load_dwordx4 v40, s[16:19], s26 offen offset:2048 lds
v_cvt_pk_bf16_f32 v68, v72, v73
v_mfma_f32_16x16x32_bf16 v[212:215], a[4:7], a[40:43], v[212:215]
ds_read_b128 a[136:139], v43 offset:2048
v_accvgpr_read_b32 v74, a154
v_mfma_f32_16x16x32_bf16 v[136:139], a[20:23], a[8:11], v[136:139]
ds_read_b64_tr_b16 a[192:193], v48 offset:2048
ds_read_b64_tr_b16 a[194:195], v48 offset:2560
v_mfma_f32_16x16x32_bf16 v[140:143], a[24:27], a[8:11], v[140:143]
buffer_load_dwordx4 v41, s[16:19], s26 offen offset:3072 lds
v_accvgpr_read_b32 v75, a155
v_mfma_f32_16x16x32_bf16 v[176:179], a[28:31], a[8:11], v[176:179]
ds_read_b128 a[140:143], v43 offset:4096
v_mul_f32 v74, v52, v74
v_mfma_f32_16x16x32_bf16 v[180:183], a[32:35], a[8:11], v[180:183]
s_add_u32 m0, s48, 135168
ds_read_b64_tr_b16 a[196:197], v49 offset:4096
ds_read_b64_tr_b16 a[198:199], v49 offset:4608
v_mfma_f32_16x16x32_bf16 v[216:219], a[8:11], a[36:39], v[216:219]
buffer_load_dwordx4 v40, s[16:19], s47 offen offset:0 lds
v_mul_f32 v75, v52, v75
v_mfma_f32_16x16x32_bf16 v[220:223], a[8:11], a[40:43], v[220:223]
ds_read_b128 a[160:163], v43 offset:6144
.Lpad14:
s_cbranch_execz .Ltch4
v_cvt_pk_bf16_f32 v69, v74, v75
v_mfma_f32_16x16x32_bf16 v[144:147], a[20:23], a[12:15], v[144:147]
ds_read_b64_tr_b16 a[200:201], v50 offset:4096
ds_read_b64_tr_b16 a[202:203], v50 offset:4608
v_mfma_f32_16x16x32_bf16 v[148:151], a[24:27], a[12:15], v[148:151]
buffer_load_dwordx4 v41, s[16:19], s47 offen offset:1024 lds
v_accvgpr_read_b32 v72, a156
v_mfma_f32_16x16x32_bf16 v[184:187], a[28:31], a[12:15], v[184:187]
v_accvgpr_read_b32 v73, a157
v_mul_f32 v72, v52, v72
v_mfma_f32_16x16x32_bf16 v[188:191], a[32:35], a[12:15], v[188:191]
v_mul_f32 v73, v52, v73
v_cvt_pk_bf16_f32 v70, v72, v73
v_mfma_f32_16x16x32_bf16 v[224:227], a[12:15], a[36:39], v[224:227]
buffer_load_dwordx4 v42, s[12:15], s25 offen offset:2048 lds
v_accvgpr_read_b32 v74, a158
v_mfma_f32_16x16x32_bf16 v[228:231], a[12:15], a[40:43], v[228:231]
v_accvgpr_read_b32 v75, a159
v_mul_f32 v74, v52, v74
v_mfma_f32_16x16x32_bf16 v[152:155], a[20:23], a[16:19], v[152:155]
s_add_u32 s26, s26, 393216
s_add_u32 s47, s47, 393216
s_add_u32 s25, s25, 128
v_mfma_f32_16x16x32_bf16 v[156:159], a[24:27], a[16:19], v[156:159]
v_mul_f32 v75, v52, v75
v_cvt_pk_bf16_f32 v71, v74, v75
v_mfma_f32_16x16x32_bf16 v[192:195], a[28:31], a[16:19], v[192:195]
buffer_store_dwordx4 v[68:71], v7, s[20:23], s41 offen offset:3072 sc1
v_mfma_f32_16x16x32_bf16 v[196:199], a[32:35], a[16:19], v[196:199]
v_mfma_f32_16x16x32_bf16 v[232:235], a[16:19], a[36:39], v[232:235]
v_mfma_f32_16x16x32_bf16 v[236:239], a[16:19], a[40:43], v[236:239]
s_waitcnt lgkmcnt(0)
v_mfma_f32_16x16x32_bf16 v[160:163], a[172:175], a[128:131], v[160:163]
v_mfma_f32_16x16x32_bf16 v[164:167], a[192:195], a[128:131], v[164:167]
s_add_u32 m0, s91, 16384
ds_read_b64_tr_b16 a[20:21], v47 offset:14336
ds_read_b64_tr_b16 a[22:23], v47 offset:14848
v_mfma_f32_16x16x32_bf16 v[200:203], a[128:131], a[196:199], v[200:203]
buffer_load_dwordx4 v36, s[12:15], s24 offen offset:0 lds
v_accvgpr_read_b32 v72, a176
v_mfma_f32_16x16x32_bf16 v[204:207], a[128:131], a[200:203], v[204:207]
ds_read_b128 a[0:3], v46
v_accvgpr_read_b32 v73, a177
v_mfma_f32_16x16x32_bf16 v[128:131], a[164:167], a[132:135], v[128:131]
ds_read_b64_tr_b16 a[24:25], v48 offset:14336
ds_read_b64_tr_b16 a[26:27], v48 offset:14848
v_mfma_f32_16x16x32_bf16 v[132:135], a[168:171], a[132:135], v[132:135]
v_mul_f32 v72, v53, v72
v_mul_f32 v73, v53, v73
v_mfma_f32_16x16x32_bf16 v[168:171], a[172:175], a[132:135], v[168:171]
ds_read_b128 a[4:7], v44 offset:0
v_cvt_pk_bf16_f32 v64, v72, v73
v_mfma_f32_16x16x32_bf16 v[172:175], a[192:195], a[132:135], v[172:175]
ds_read_b64_tr_b16 a[28:29], v47 offset:16384
ds_read_b64_tr_b16 a[30:31], v47 offset:16896
v_mfma_f32_16x16x32_bf16 v[208:211], a[132:135], a[196:199], v[208:211]
buffer_load_dwordx4 v37, s[12:15], s24 offen offset:1024 lds
v_accvgpr_read_b32 v74, a178
v_mfma_f32_16x16x32_bf16 v[212:215], a[132:135], a[200:203], v[212:215]
ds_read_b128 a[8:11], v44 offset:2048
v_accvgpr_read_b32 v75, a179
v_mfma_f32_16x16x32_bf16 v[136:139], a[164:167], a[136:139], v[136:139]
ds_read_b64_tr_b16 a[32:33], v48 offset:16384
ds_read_b64_tr_b16 a[34:35], v48 offset:16896
v_mfma_f32_16x16x32_bf16 v[140:143], a[168:171], a[136:139], v[140:143]
v_mul_f32 v74, v53, v74
v_mul_f32 v75, v53, v75
v_mfma_f32_16x16x32_bf16 v[176:179], a[172:175], a[136:139], v[176:179]
ds_read_b128 a[12:15], v44 offset:4096
v_cvt_pk_bf16_f32 v65, v74, v75
v_mfma_f32_16x16x32_bf16 v[180:183], a[192:195], a[136:139], v[180:183]
ds_read_b64_tr_b16 a[36:37], v49 offset:18432
ds_read_b64_tr_b16 a[38:39], v49 offset:18944
v_mfma_f32_16x16x32_bf16 v[216:219], a[136:139], a[196:199], v[216:219]
buffer_load_dwordx4 v38, s[12:15], s24 offen offset:2048 lds
v_accvgpr_read_b32 v72, a180
v_mfma_f32_16x16x32_bf16 v[220:223], a[136:139], a[200:203], v[220:223]
ds_read_b128 a[16:19], v44 offset:6144
v_accvgpr_read_b32 v73, a181
v_mfma_f32_16x16x32_bf16 v[144:147], a[164:167], a[140:143], v[144:147]
ds_read_b64_tr_b16 a[40:41], v50 offset:18432
ds_read_b64_tr_b16 a[42:43], v50 offset:18944
v_mfma_f32_16x16x32_bf16 v[148:151], a[168:171], a[140:143], v[148:151]
s_cmp_eq_u32 s39, 1
s_mov_b32 s28, 16384
s_cselect_b32 s28, 4294901760, s28
v_mfma_f32_16x16x32_bf16 v[184:187], a[172:175], a[140:143], v[184:187]
v_add_u32 v43, 16384, v43
v_add_u32 v44, 16384, v44
v_mfma_f32_16x16x32_bf16 v[188:191], a[192:195], a[140:143], v[188:191]
v_add_u32 v47, 4294901760, v47
v_add_u32 v48, 4294901760, v48
v_mfma_f32_16x16x32_bf16 v[224:227], a[140:143], a[196:199], v[224:227]
buffer_load_dwordx4 v39, s[12:15], s24 offen offset:3072 lds
v_add_u32 v49, 4294901760, v49
v_add_u32 v50, 4294901760, v50
v_mfma_f32_16x16x32_bf16 v[228:231], a[140:143], a[200:203], v[228:231]
v_add_u32 v45, s28, v45
v_add_u32 v46, s28, v46
v_mfma_f32_16x16x32_bf16 v[152:155], a[164:167], a[160:163], v[152:155]
v_mul_f32 v72, v53, v72
v_mul_f32 v73, v53, v73
v_mfma_f32_16x16x32_bf16 v[156:159], a[168:171], a[160:163], v[156:159]
s_add_u32 s24, s24, 128
v_cvt_pk_bf16_f32 v66, v72, v73
v_accvgpr_read_b32 v74, a182
v_mfma_f32_16x16x32_bf16 v[192:195], a[172:175], a[160:163], v[192:195]
v_accvgpr_read_b32 v75, a183
v_mul_f32 v74, v53, v74
v_mfma_f32_16x16x32_bf16 v[196:199], a[192:195], a[160:163], v[196:199]
v_mul_f32 v75, v53, v75
v_cvt_pk_bf16_f32 v67, v74, v75
v_mfma_f32_16x16x32_bf16 v[232:235], a[160:163], a[196:199], v[232:235]
buffer_store_dwordx4 v[64:67], v7, s[20:23], s42 offen offset:0 sc1
v_mfma_f32_16x16x32_bf16 v[236:239], a[160:163], a[200:203], v[236:239]
s_waitcnt vmcnt(19) lgkmcnt(0)
s_barrier
v_mfma_f32_16x16x32_bf16 v[160:163], a[28:31], a[0:3], v[160:163]
v_mfma_f32_16x16x32_bf16 v[164:167], a[32:35], a[0:3], v[164:167]
s_add_u32 m0, s48, 98304
ds_read_b64_tr_b16 a[164:165], v47 offset:0
ds_read_b64_tr_b16 a[166:167], v47 offset:512
v_mfma_f32_16x16x32_bf16 v[200:203], a[0:3], a[36:39], v[200:203]
buffer_load_dwordx4 v40, s[16:19], s26 offen offset:0 lds
v_accvgpr_read_b32 v72, a184
v_mfma_f32_16x16x32_bf16 v[204:207], a[0:3], a[40:43], v[204:207]
ds_read_b128 a[128:131], v45
v_accvgpr_read_b32 v73, a185
v_mfma_f32_16x16x32_bf16 v[128:131], a[20:23], a[4:7], v[128:131]
ds_read_b64_tr_b16 a[168:169], v48 offset:0
ds_read_b64_tr_b16 a[170:171], v48 offset:512
v_mfma_f32_16x16x32_bf16 v[132:135], a[24:27], a[4:7], v[132:135]
buffer_load_dwordx4 v41, s[16:19], s26 offen offset:1024 lds
v_mul_f32 v72, v53, v72
v_mfma_f32_16x16x32_bf16 v[168:171], a[28:31], a[4:7], v[168:171]
ds_read_b128 a[132:135], v43 offset:0
v_mul_f32 v73, v53, v73
v_mfma_f32_16x16x32_bf16 v[172:175], a[32:35], a[4:7], v[172:175]
ds_read_b64_tr_b16 a[172:173], v47 offset:2048
ds_read_b64_tr_b16 a[174:175], v47 offset:2560
v_mfma_f32_16x16x32_bf16 v[208:211], a[4:7], a[36:39], v[208:211]
buffer_load_dwordx4 v40, s[16:19], s26 offen offset:2048 lds
v_cvt_pk_bf16_f32 v68, v72, v73
v_mfma_f32_16x16x32_bf16 v[212:215], a[4:7], a[40:43], v[212:215]
ds_read_b128 a[136:139], v43 offset:2048
v_accvgpr_read_b32 v74, a186
v_mfma_f32_16x16x32_bf16 v[136:139], a[20:23], a[8:11], v[136:139]
ds_read_b64_tr_b16 a[192:193], v48 offset:2048
ds_read_b64_tr_b16 a[194:195], v48 offset:2560
v_mfma_f32_16x16x32_bf16 v[140:143], a[24:27], a[8:11], v[140:143]
buffer_load_dwordx4 v41, s[16:19], s26 offen offset:3072 lds
v_accvgpr_read_b32 v75, a187
v_mfma_f32_16x16x32_bf16 v[176:179], a[28:31], a[8:11], v[176:179]
ds_read_b128 a[140:143], v43 offset:4096
v_mul_f32 v74, v53, v74
v_mfma_f32_16x16x32_bf16 v[180:183], a[32:35], a[8:11], v[180:183]
s_add_u32 m0, s48, 102400
ds_read_b64_tr_b16 a[196:197], v49 offset:4096
ds_read_b64_tr_b16 a[198:199], v49 offset:4608
v_mfma_f32_16x16x32_bf16 v[216:219], a[8:11], a[36:39], v[216:219]
buffer_load_dwordx4 v40, s[16:19], s47 offen offset:0 lds
v_mul_f32 v75, v53, v75
v_mfma_f32_16x16x32_bf16 v[220:223], a[8:11], a[40:43], v[220:223]
ds_read_b128 a[160:163], v43 offset:6144
v_cvt_pk_bf16_f32 v69, v74, v75
v_mfma_f32_16x16x32_bf16 v[144:147], a[20:23], a[12:15], v[144:147]
ds_read_b64_tr_b16 a[200:201], v50 offset:4096
ds_read_b64_tr_b16 a[202:203], v50 offset:4608
v_mfma_f32_16x16x32_bf16 v[148:151], a[24:27], a[12:15], v[148:151]
buffer_load_dwordx4 v41, s[16:19], s47 offen offset:1024 lds
v_accvgpr_read_b32 v72, a188
v_mfma_f32_16x16x32_bf16 v[184:187], a[28:31], a[12:15], v[184:187]
v_accvgpr_read_b32 v73, a189
v_mul_f32 v72, v53, v72
v_mfma_f32_16x16x32_bf16 v[188:191], a[32:35], a[12:15], v[188:191]
v_mul_f32 v73, v53, v73
v_cvt_pk_bf16_f32 v70, v72, v73
v_mfma_f32_16x16x32_bf16 v[224:227], a[12:15], a[36:39], v[224:227]
buffer_load_dwordx4 v42, s[12:15], s25 offen offset:2048 lds
v_accvgpr_read_b32 v74, a190
v_mfma_f32_16x16x32_bf16 v[228:231], a[12:15], a[40:43], v[228:231]
v_accvgpr_read_b32 v75, a191
v_mul_f32 v74, v53, v74
v_mfma_f32_16x16x32_bf16 v[152:155], a[20:23], a[16:19], v[152:155]
s_add_u32 s26, s26, 393216
s_add_u32 s47, s47, 393216
s_add_u32 s25, s25, 128
v_mfma_f32_16x16x32_bf16 v[156:159], a[24:27], a[16:19], v[156:159]
v_mul_f32 v75, v53, v75
v_cvt_pk_bf16_f32 v71, v74, v75
v_mfma_f32_16x16x32_bf16 v[192:195], a[28:31], a[16:19], v[192:195]
buffer_load_dword v35, v4, s[96:99], 0 offen
v_mfma_f32_16x16x32_bf16 v[196:199], a[32:35], a[16:19], v[196:199]
buffer_store_dwordx4 v[68:71], v7, s[20:23], s42 offen offset:1024 sc1
v_mfma_f32_16x16x32_bf16 v[232:235], a[16:19], a[36:39], v[232:235]
v_mfma_f32_16x16x32_bf16 v[236:239], a[16:19], a[40:43], v[236:239]
s_waitcnt lgkmcnt(0)
v_mfma_f32_16x16x32_bf16 v[160:163], a[172:175], a[128:131], v[160:163]
v_mfma_f32_16x16x32_bf16 v[164:167], a[192:195], a[128:131], v[164:167]
s_add_u32 m0, s91, 65536
ds_read_b64_tr_b16 a[20:21], v47 offset:14336
ds_read_b64_tr_b16 a[22:23], v47 offset:14848
v_mfma_f32_16x16x32_bf16 v[200:203], a[128:131], a[196:199], v[200:203]
buffer_load_dwordx4 v36, s[12:15], s24 offen offset:0 lds
v_accvgpr_read_b32 v72, a208
v_mfma_f32_16x16x32_bf16 v[204:207], a[128:131], a[200:203], v[204:207]
ds_read_b128 a[0:3], v46
v_accvgpr_read_b32 v73, a209
v_mfma_f32_16x16x32_bf16 v[128:131], a[164:167], a[132:135], v[128:131]
ds_read_b64_tr_b16 a[24:25], v48 offset:14336
ds_read_b64_tr_b16 a[26:27], v48 offset:14848
v_mfma_f32_16x16x32_bf16 v[132:135], a[168:171], a[132:135], v[132:135]
v_mul_f32 v72, v53, v72
v_mul_f32 v73, v53, v73
v_mfma_f32_16x16x32_bf16 v[168:171], a[172:175], a[132:135], v[168:171]
ds_read_b128 a[4:7], v44 offset:0
v_cvt_pk_bf16_f32 v64, v72, v73
v_mfma_f32_16x16x32_bf16 v[172:175], a[192:195], a[132:135], v[172:175]
ds_read_b64_tr_b16 a[28:29], v47 offset:16384
ds_read_b64_tr_b16 a[30:31], v47 offset:16896
v_mfma_f32_16x16x32_bf16 v[208:211], a[132:135], a[196:199], v[208:211]
buffer_load_dwordx4 v37, s[12:15], s24 offen offset:1024 lds
v_accvgpr_read_b32 v74, a210
v_mfma_f32_16x16x32_bf16 v[212:215], a[132:135], a[200:203], v[212:215]
ds_read_b128 a[8:11], v44 offset:2048
v_accvgpr_read_b32 v75, a211
v_mfma_f32_16x16x32_bf16 v[136:139], a[164:167], a[136:139], v[136:139]
ds_read_b64_tr_b16 a[32:33], v48 offset:16384
ds_read_b64_tr_b16 a[34:35], v48 offset:16896
v_mfma_f32_16x16x32_bf16 v[140:143], a[168:171], a[136:139], v[140:143]
v_mul_f32 v74, v53, v74
v_mul_f32 v75, v53, v75
v_mfma_f32_16x16x32_bf16 v[176:179], a[172:175], a[136:139], v[176:179]
ds_read_b128 a[12:15], v44 offset:4096
v_cvt_pk_bf16_f32 v65, v74, v75
v_mfma_f32_16x16x32_bf16 v[180:183], a[192:195], a[136:139], v[180:183]
ds_read_b64_tr_b16 a[36:37], v49 offset:18432
ds_read_b64_tr_b16 a[38:39], v49 offset:18944
v_mfma_f32_16x16x32_bf16 v[216:219], a[136:139], a[196:199], v[216:219]
buffer_load_dwordx4 v38, s[12:15], s24 offen offset:2048 lds
v_accvgpr_read_b32 v72, a212
v_mfma_f32_16x16x32_bf16 v[220:223], a[136:139], a[200:203], v[220:223]
ds_read_b128 a[16:19], v44 offset:6144
v_accvgpr_read_b32 v73, a213
v_mfma_f32_16x16x32_bf16 v[144:147], a[164:167], a[140:143], v[144:147]
ds_read_b64_tr_b16 a[40:41], v50 offset:18432
ds_read_b64_tr_b16 a[42:43], v50 offset:18944
v_mfma_f32_16x16x32_bf16 v[148:151], a[168:171], a[140:143], v[148:151]
s_cmp_eq_u32 s39, 1
s_mov_b32 s28, 4294885376
s_cselect_b32 s28, 98304, s28
v_mfma_f32_16x16x32_bf16 v[184:187], a[172:175], a[140:143], v[184:187]
v_add_u32 v43, 4294885376, v43
v_add_u32 v44, 4294885376, v44
v_mfma_f32_16x16x32_bf16 v[188:191], a[192:195], a[140:143], v[188:191]
v_add_u32 v47, 98304, v47
v_add_u32 v48, 98304, v48
v_mfma_f32_16x16x32_bf16 v[224:227], a[140:143], a[196:199], v[224:227]
buffer_load_dwordx4 v39, s[12:15], s24 offen offset:3072 lds
v_add_u32 v49, 98304, v49
v_add_u32 v50, 98304, v50
v_mfma_f32_16x16x32_bf16 v[228:231], a[140:143], a[200:203], v[228:231]
v_add_u32 v45, s28, v45
v_add_u32 v46, s28, v46
v_mfma_f32_16x16x32_bf16 v[152:155], a[164:167], a[160:163], v[152:155]
v_mul_f32 v72, v53, v72
v_mul_f32 v73, v53, v73
v_mfma_f32_16x16x32_bf16 v[156:159], a[168:171], a[160:163], v[156:159]
s_add_u32 s24, s24, 128
v_cvt_pk_bf16_f32 v66, v72, v73
v_accvgpr_read_b32 v74, a214
v_mfma_f32_16x16x32_bf16 v[192:195], a[172:175], a[160:163], v[192:195]
v_accvgpr_read_b32 v75, a215
v_mul_f32 v74, v53, v74
v_mfma_f32_16x16x32_bf16 v[196:199], a[192:195], a[160:163], v[196:199]
v_mul_f32 v75, v53, v75
v_cvt_pk_bf16_f32 v67, v74, v75
v_mfma_f32_16x16x32_bf16 v[232:235], a[160:163], a[196:199], v[232:235]
buffer_store_dwordx4 v[64:67], v7, s[20:23], s42 offen offset:2048 sc1
v_mfma_f32_16x16x32_bf16 v[236:239], a[160:163], a[200:203], v[236:239]
s_waitcnt vmcnt(20) lgkmcnt(0)
s_barrier
v_mfma_f32_16x16x32_bf16 v[160:163], a[28:31], a[0:3], v[160:163]
v_mfma_f32_16x16x32_bf16 v[164:167], a[32:35], a[0:3], v[164:167]
s_add_u32 m0, s48, 32768
ds_read_b64_tr_b16 a[164:165], v47 offset:0
ds_read_b64_tr_b16 a[166:167], v47 offset:512
v_mfma_f32_16x16x32_bf16 v[200:203], a[0:3], a[36:39], v[200:203]
buffer_load_dwordx4 v40, s[16:19], s26 offen offset:0 lds
v_accvgpr_read_b32 v72, a216
v_mfma_f32_16x16x32_bf16 v[204:207], a[0:3], a[40:43], v[204:207]
ds_read_b128 a[128:131], v45
v_accvgpr_read_b32 v73, a217
v_mfma_f32_16x16x32_bf16 v[128:131], a[20:23], a[4:7], v[128:131]
ds_read_b64_tr_b16 a[168:169], v48 offset:0
ds_read_b64_tr_b16 a[170:171], v48 offset:512
v_mfma_f32_16x16x32_bf16 v[132:135], a[24:27], a[4:7], v[132:135]
buffer_load_dwordx4 v41, s[16:19], s26 offen offset:1024 lds
v_mul_f32 v72, v53, v72
v_mfma_f32_16x16x32_bf16 v[168:171], a[28:31], a[4:7], v[168:171]
ds_read_b128 a[132:135], v43 offset:0
v_mul_f32 v73, v53, v73
v_mfma_f32_16x16x32_bf16 v[172:175], a[32:35], a[4:7], v[172:175]
ds_read_b64_tr_b16 a[172:173], v47 offset:2048
ds_read_b64_tr_b16 a[174:175], v47 offset:2560
v_mfma_f32_16x16x32_bf16 v[208:211], a[4:7], a[36:39], v[208:211]
buffer_load_dwordx4 v40, s[16:19], s26 offen offset:2048 lds
v_cvt_pk_bf16_f32 v68, v72, v73
v_mfma_f32_16x16x32_bf16 v[212:215], a[4:7], a[40:43], v[212:215]
ds_read_b128 a[136:139], v43 offset:2048
v_accvgpr_read_b32 v74, a218
v_mfma_f32_16x16x32_bf16 v[136:139], a[20:23], a[8:11], v[136:139]
ds_read_b64_tr_b16 a[192:193], v48 offset:2048
ds_read_b64_tr_b16 a[194:195], v48 offset:2560
v_mfma_f32_16x16x32_bf16 v[140:143], a[24:27], a[8:11], v[140:143]
buffer_load_dwordx4 v41, s[16:19], s26 offen offset:3072 lds
v_accvgpr_read_b32 v75, a219
v_mfma_f32_16x16x32_bf16 v[176:179], a[28:31], a[8:11], v[176:179]
ds_read_b128 a[140:143], v43 offset:4096
v_mul_f32 v74, v53, v74
v_mfma_f32_16x16x32_bf16 v[180:183], a[32:35], a[8:11], v[180:183]
s_add_u32 m0, s48, 36864
ds_read_b64_tr_b16 a[196:197], v49 offset:4096
ds_read_b64_tr_b16 a[198:199], v49 offset:4608
v_mfma_f32_16x16x32_bf16 v[216:219], a[8:11], a[36:39], v[216:219]
buffer_load_dwordx4 v40, s[16:19], s47 offen offset:0 lds
v_mul_f32 v75, v53, v75
v_mfma_f32_16x16x32_bf16 v[220:223], a[8:11], a[40:43], v[220:223]
ds_read_b128 a[160:163], v43 offset:6144
v_cvt_pk_bf16_f32 v69, v74, v75
v_mfma_f32_16x16x32_bf16 v[144:147], a[20:23], a[12:15], v[144:147]
ds_read_b64_tr_b16 a[200:201], v50 offset:4096
ds_read_b64_tr_b16 a[202:203], v50 offset:4608
v_mfma_f32_16x16x32_bf16 v[148:151], a[24:27], a[12:15], v[148:151]
buffer_load_dwordx4 v41, s[16:19], s47 offen offset:1024 lds
v_accvgpr_read_b32 v72, a220
v_mfma_f32_16x16x32_bf16 v[184:187], a[28:31], a[12:15], v[184:187]
v_accvgpr_read_b32 v73, a221
v_mul_f32 v72, v53, v72
v_mfma_f32_16x16x32_bf16 v[188:191], a[32:35], a[12:15], v[188:191]
v_mul_f32 v73, v53, v73
v_cvt_pk_bf16_f32 v70, v72, v73
v_mfma_f32_16x16x32_bf16 v[224:227], a[12:15], a[36:39], v[224:227]
buffer_load_dwordx4 v42, s[12:15], s25 offen offset:2048 lds
v_accvgpr_read_b32 v74, a222
v_mfma_f32_16x16x32_bf16 v[228:231], a[12:15], a[40:43], v[228:231]
v_accvgpr_read_b32 v75, a223
v_mul_f32 v74, v53, v74
v_mfma_f32_16x16x32_bf16 v[152:155], a[20:23], a[16:19], v[152:155]
s_add_u32 s26, s26, 393216
s_add_u32 s47, s47, 393216
s_add_u32 s25, s25, 128
v_mfma_f32_16x16x32_bf16 v[156:159], a[24:27], a[16:19], v[156:159]
v_mul_f32 v75, v53, v75
v_cvt_pk_bf16_f32 v71, v74, v75
v_mfma_f32_16x16x32_bf16 v[192:195], a[28:31], a[16:19], v[192:195]
buffer_store_dwordx4 v[68:71], v7, s[20:23], s42 offen offset:3072 sc1
v_mfma_f32_16x16x32_bf16 v[196:199], a[32:35], a[16:19], v[196:199]
v_mfma_f32_16x16x32_bf16 v[232:235], a[16:19], a[36:39], v[232:235]
v_mfma_f32_16x16x32_bf16 v[236:239], a[16:19], a[40:43], v[236:239]
s_waitcnt lgkmcnt(0)
v_mfma_f32_16x16x32_bf16 v[160:163], a[172:175], a[128:131], v[160:163]
v_mfma_f32_16x16x32_bf16 v[164:167], a[192:195], a[128:131], v[164:167]
s_add_u32 m0, s91, 81920
ds_read_b64_tr_b16 a[20:21], v47 offset:14336
ds_read_b64_tr_b16 a[22:23], v47 offset:14848
v_mfma_f32_16x16x32_bf16 v[200:203], a[128:131], a[196:199], v[200:203]
buffer_load_dwordx4 v36, s[12:15], s24 offen offset:0 lds
v_accvgpr_read_b32 v72, a240
v_mfma_f32_16x16x32_bf16 v[204:207], a[128:131], a[200:203], v[204:207]
ds_read_b128 a[0:3], v46
v_accvgpr_read_b32 v73, a241
v_mfma_f32_16x16x32_bf16 v[128:131], a[164:167], a[132:135], v[128:131]
ds_read_b64_tr_b16 a[24:25], v48 offset:14336
ds_read_b64_tr_b16 a[26:27], v48 offset:14848
v_mfma_f32_16x16x32_bf16 v[132:135], a[168:171], a[132:135], v[132:135]
v_mul_f32 v72, v53, v72
v_mul_f32 v73, v53, v73
v_mfma_f32_16x16x32_bf16 v[168:171], a[172:175], a[132:135], v[168:171]
ds_read_b128 a[4:7], v44 offset:0
v_cvt_pk_bf16_f32 v64, v72, v73
v_mfma_f32_16x16x32_bf16 v[172:175], a[192:195], a[132:135], v[172:175]
ds_read_b64_tr_b16 a[28:29], v47 offset:16384
ds_read_b64_tr_b16 a[30:31], v47 offset:16896
v_mfma_f32_16x16x32_bf16 v[208:211], a[132:135], a[196:199], v[208:211]
buffer_load_dwordx4 v37, s[12:15], s24 offen offset:1024 lds
v_accvgpr_read_b32 v74, a242
v_mfma_f32_16x16x32_bf16 v[212:215], a[132:135], a[200:203], v[212:215]
ds_read_b128 a[8:11], v44 offset:2048
v_accvgpr_read_b32 v75, a243
v_mfma_f32_16x16x32_bf16 v[136:139], a[164:167], a[136:139], v[136:139]
ds_read_b64_tr_b16 a[32:33], v48 offset:16384
ds_read_b64_tr_b16 a[34:35], v48 offset:16896
v_mfma_f32_16x16x32_bf16 v[140:143], a[168:171], a[136:139], v[140:143]
v_mul_f32 v74, v53, v74
v_mul_f32 v75, v53, v75
v_mfma_f32_16x16x32_bf16 v[176:179], a[172:175], a[136:139], v[176:179]
ds_read_b128 a[12:15], v44 offset:4096
v_cvt_pk_bf16_f32 v65, v74, v75
v_mfma_f32_16x16x32_bf16 v[180:183], a[192:195], a[136:139], v[180:183]
ds_read_b64_tr_b16 a[36:37], v49 offset:18432
ds_read_b64_tr_b16 a[38:39], v49 offset:18944
v_mfma_f32_16x16x32_bf16 v[216:219], a[136:139], a[196:199], v[216:219]
buffer_load_dwordx4 v38, s[12:15], s24 offen offset:2048 lds
v_accvgpr_read_b32 v72, a244
v_mfma_f32_16x16x32_bf16 v[220:223], a[136:139], a[200:203], v[220:223]
ds_read_b128 a[16:19], v44 offset:6144
v_accvgpr_read_b32 v73, a245
v_mfma_f32_16x16x32_bf16 v[144:147], a[164:167], a[140:143], v[144:147]
ds_read_b64_tr_b16 a[40:41], v50 offset:18432
ds_read_b64_tr_b16 a[42:43], v50 offset:18944
v_mfma_f32_16x16x32_bf16 v[148:151], a[168:171], a[140:143], v[148:151]
s_cmp_eq_u32 s39, 1
s_mov_b32 s28, 16384
s_cselect_b32 s28, 4294934528, s28
v_mfma_f32_16x16x32_bf16 v[184:187], a[172:175], a[140:143], v[184:187]
v_add_u32 v43, 16384, v43
v_add_u32 v44, 16384, v44
v_mfma_f32_16x16x32_bf16 v[188:191], a[192:195], a[140:143], v[188:191]
v_add_u32 v47, 4294934528, v47
v_add_u32 v48, 4294934528, v48
v_mfma_f32_16x16x32_bf16 v[224:227], a[140:143], a[196:199], v[224:227]
buffer_load_dwordx4 v39, s[12:15], s24 offen offset:3072 lds
v_add_u32 v49, 4294934528, v49
v_add_u32 v50, 4294934528, v50
v_mfma_f32_16x16x32_bf16 v[228:231], a[140:143], a[200:203], v[228:231]
v_add_u32 v45, s28, v45
v_add_u32 v46, s28, v46
v_mfma_f32_16x16x32_bf16 v[152:155], a[164:167], a[160:163], v[152:155]
v_mul_f32 v72, v53, v72
v_mul_f32 v73, v53, v73
v_mfma_f32_16x16x32_bf16 v[156:159], a[168:171], a[160:163], v[156:159]
s_add_u32 s24, s24, 128
v_cvt_pk_bf16_f32 v66, v72, v73
v_accvgpr_read_b32 v74, a246
v_mfma_f32_16x16x32_bf16 v[192:195], a[172:175], a[160:163], v[192:195]
v_accvgpr_read_b32 v75, a247
v_mul_f32 v74, v53, v74
v_mfma_f32_16x16x32_bf16 v[196:199], a[192:195], a[160:163], v[196:199]
v_mul_f32 v75, v53, v75
v_cvt_pk_bf16_f32 v67, v74, v75
v_mfma_f32_16x16x32_bf16 v[232:235], a[160:163], a[196:199], v[232:235]
buffer_store_dwordx4 v[64:67], v7, s[20:23], s43 offen offset:0 sc1
v_mfma_f32_16x16x32_bf16 v[236:239], a[160:163], a[200:203], v[236:239]
s_waitcnt vmcnt(20) lgkmcnt(0)
s_barrier
v_mfma_f32_16x16x32_bf16 v[160:163], a[28:31], a[0:3], v[160:163]
v_mfma_f32_16x16x32_bf16 v[164:167], a[32:35], a[0:3], v[164:167]
s_add_u32 m0, s48, 131072
ds_read_b64_tr_b16 a[164:165], v47 offset:0
ds_read_b64_tr_b16 a[166:167], v47 offset:512
v_mfma_f32_16x16x32_bf16 v[200:203], a[0:3], a[36:39], v[200:203]
buffer_load_dwordx4 v40, s[16:19], s26 offen offset:0 lds
v_accvgpr_read_b32 v72, a248
v_mfma_f32_16x16x32_bf16 v[204:207], a[0:3], a[40:43], v[204:207]
ds_read_b128 a[128:131], v45
v_accvgpr_read_b32 v73, a249
v_mfma_f32_16x16x32_bf16 v[128:131], a[20:23], a[4:7], v[128:131]
ds_read_b64_tr_b16 a[168:169], v48 offset:0
ds_read_b64_tr_b16 a[170:171], v48 offset:512
v_mfma_f32_16x16x32_bf16 v[132:135], a[24:27], a[4:7], v[132:135]
buffer_load_dwordx4 v41, s[16:19], s26 offen offset:1024 lds
v_mul_f32 v72, v53, v72
v_mfma_f32_16x16x32_bf16 v[168:171], a[28:31], a[4:7], v[168:171]
ds_read_b128 a[132:135], v43 offset:0
v_mul_f32 v73, v53, v73
v_mfma_f32_16x16x32_bf16 v[172:175], a[32:35], a[4:7], v[172:175]
ds_read_b64_tr_b16 a[172:173], v47 offset:2048
ds_read_b64_tr_b16 a[174:175], v47 offset:2560
v_mfma_f32_16x16x32_bf16 v[208:211], a[4:7], a[36:39], v[208:211]
buffer_load_dwordx4 v40, s[16:19], s26 offen offset:2048 lds
v_cvt_pk_bf16_f32 v68, v72, v73
v_mfma_f32_16x16x32_bf16 v[212:215], a[4:7], a[40:43], v[212:215]
ds_read_b128 a[136:139], v43 offset:2048
v_accvgpr_read_b32 v74, a250
v_mfma_f32_16x16x32_bf16 v[136:139], a[20:23], a[8:11], v[136:139]
ds_read_b64_tr_b16 a[192:193], v48 offset:2048
ds_read_b64_tr_b16 a[194:195], v48 offset:2560
v_mfma_f32_16x16x32_bf16 v[140:143], a[24:27], a[8:11], v[140:143]
buffer_load_dwordx4 v41, s[16:19], s26 offen offset:3072 lds
v_accvgpr_read_b32 v75, a251
v_mfma_f32_16x16x32_bf16 v[176:179], a[28:31], a[8:11], v[176:179]
ds_read_b128 a[140:143], v43 offset:4096
v_mul_f32 v74, v53, v74
v_mfma_f32_16x16x32_bf16 v[180:183], a[32:35], a[8:11], v[180:183]
s_add_u32 m0, s48, 135168
ds_read_b64_tr_b16 a[196:197], v49 offset:4096
ds_read_b64_tr_b16 a[198:199], v49 offset:4608
v_mfma_f32_16x16x32_bf16 v[216:219], a[8:11], a[36:39], v[216:219]
buffer_load_dwordx4 v40, s[16:19], s47 offen offset:0 lds
v_mul_f32 v75, v53, v75
v_mfma_f32_16x16x32_bf16 v[220:223], a[8:11], a[40:43], v[220:223]
ds_read_b128 a[160:163], v43 offset:6144
v_cvt_pk_bf16_f32 v69, v74, v75
v_mfma_f32_16x16x32_bf16 v[144:147], a[20:23], a[12:15], v[144:147]
ds_read_b64_tr_b16 a[200:201], v50 offset:4096
ds_read_b64_tr_b16 a[202:203], v50 offset:4608
v_mfma_f32_16x16x32_bf16 v[148:151], a[24:27], a[12:15], v[148:151]
buffer_load_dwordx4 v41, s[16:19], s47 offen offset:1024 lds
v_accvgpr_read_b32 v72, a252
v_mfma_f32_16x16x32_bf16 v[184:187], a[28:31], a[12:15], v[184:187]
v_accvgpr_read_b32 v73, a253
v_mul_f32 v72, v53, v72
v_mfma_f32_16x16x32_bf16 v[188:191], a[32:35], a[12:15], v[188:191]
v_mul_f32 v73, v53, v73
v_cvt_pk_bf16_f32 v70, v72, v73
v_mfma_f32_16x16x32_bf16 v[224:227], a[12:15], a[36:39], v[224:227]
buffer_load_dwordx4 v42, s[12:15], s25 offen offset:2048 lds
v_accvgpr_read_b32 v74, a254
v_mfma_f32_16x16x32_bf16 v[228:231], a[12:15], a[40:43], v[228:231]
v_accvgpr_read_b32 v75, a255
v_mul_f32 v74, v53, v74
v_mfma_f32_16x16x32_bf16 v[152:155], a[20:23], a[16:19], v[152:155]
s_add_u32 s26, s26, 393216
s_add_u32 s47, s47, 393216
s_add_u32 s25, s25, 128
v_mfma_f32_16x16x32_bf16 v[156:159], a[24:27], a[16:19], v[156:159]
v_mul_f32 v75, v53, v75
v_cvt_pk_bf16_f32 v71, v74, v75
v_mfma_f32_16x16x32_bf16 v[192:195], a[28:31], a[16:19], v[192:195]
buffer_store_dwordx4 v[68:71], v7, s[20:23], s43 offen offset:1024 sc1
v_mfma_f32_16x16x32_bf16 v[196:199], a[32:35], a[16:19], v[196:199]
v_mfma_f32_16x16x32_bf16 v[232:235], a[16:19], a[36:39], v[232:235]
v_mfma_f32_16x16x32_bf16 v[236:239], a[16:19], a[40:43], v[236:239]
s_waitcnt lgkmcnt(0)
v_mfma_f32_16x16x32_bf16 v[160:163], a[172:175], a[128:131], v[160:163]
v_mfma_f32_16x16x32_bf16 v[164:167], a[192:195], a[128:131], v[164:167]
s_add_u32 m0, s91, 0
ds_read_b64_tr_b16 a[20:21], v47 offset:14336
ds_read_b64_tr_b16 a[22:23], v47 offset:14848
v_mfma_f32_16x16x32_bf16 v[200:203], a[128:131], a[196:199], v[200:203]
buffer_load_dwordx4 v36, s[12:15], s24 offen offset:0 lds
v_accvgpr_read_b32 v72, a96
v_mfma_f32_16x16x32_bf16 v[204:207], a[128:131], a[200:203], v[204:207]
ds_read_b128 a[0:3], v46
v_accvgpr_read_b32 v73, a97
v_mfma_f32_16x16x32_bf16 v[128:131], a[164:167], a[132:135], v[128:131]
ds_read_b64_tr_b16 a[24:25], v48 offset:14336
ds_read_b64_tr_b16 a[26:27], v48 offset:14848
v_mfma_f32_16x16x32_bf16 v[132:135], a[168:171], a[132:135], v[132:135]
v_mul_f32 v72, v53, v72
v_mul_f32 v73, v53, v73
v_mfma_f32_16x16x32_bf16 v[168:171], a[172:175], a[132:135], v[168:171]
ds_read_b128 a[4:7], v44 offset:0
v_cvt_pk_bf16_f32 v64, v72, v73
v_mfma_f32_16x16x32_bf16 v[172:175], a[192:195], a[132:135], v[172:175]
ds_read_b64_tr_b16 a[28:29], v47 offset:16384
ds_read_b64_tr_b16 a[30:31], v47 offset:16896
.Lpad15:
s_cbranch_execz .Ltch7
v_mfma_f32_16x16x32_bf16 v[208:211], a[132:135], a[196:199], v[208:211]
buffer_load_dwordx4 v37, s[12:15], s24 offen offset:1024 lds
v_accvgpr_read_b32 v74, a98
v_mfma_f32_16x16x32_bf16 v[212:215], a[132:135], a[200:203], v[212:215]
ds_read_b128 a[8:11], v44 offset:2048
v_accvgpr_read_b32 v75, a99
v_mfma_f32_16x16x32_bf16 v[136:139], a[164:167], a[136:139], v[136:139]
ds_read_b64_tr_b16 a[32:33], v48 offset:16384
ds_read_b64_tr_b16 a[34:35], v48 offset:16896
v_mfma_f32_16x16x32_bf16 v[140:143], a[168:171], a[136:139], v[140:143]
v_mul_f32 v74, v53, v74
v_mul_f32 v75, v53, v75
v_mfma_f32_16x16x32_bf16 v[176:179], a[172:175], a[136:139], v[176:179]
ds_read_b128 a[12:15], v44 offset:4096
v_cvt_pk_bf16_f32 v65, v74, v75
v_mfma_f32_16x16x32_bf16 v[180:183], a[192:195], a[136:139], v[180:183]
ds_read_b64_tr_b16 a[36:37], v49 offset:18432
ds_read_b64_tr_b16 a[38:39], v49 offset:18944
v_mfma_f32_16x16x32_bf16 v[216:219], a[136:139], a[196:199], v[216:219]
buffer_load_dwordx4 v38, s[12:15], s24 offen offset:2048 lds
v_accvgpr_read_b32 v72, a100
v_mfma_f32_16x16x32_bf16 v[220:223], a[136:139], a[200:203], v[220:223]
ds_read_b128 a[16:19], v44 offset:6144
v_accvgpr_read_b32 v73, a101
v_mfma_f32_16x16x32_bf16 v[144:147], a[164:167], a[140:143], v[144:147]
ds_read_b64_tr_b16 a[40:41], v50 offset:18432
ds_read_b64_tr_b16 a[42:43], v50 offset:18944
v_mfma_f32_16x16x32_bf16 v[148:151], a[168:171], a[140:143], v[148:151]
s_cmp_eq_u32 s39, 1
s_mov_b32 s28, 49152
s_cselect_b32 s28, 4294901760, s28
v_mfma_f32_16x16x32_bf16 v[184:187], a[172:175], a[140:143], v[184:187]
v_add_u32 v43, 49152, v43
v_add_u32 v44, 49152, v44
v_mfma_f32_16x16x32_bf16 v[188:191], a[192:195], a[140:143], v[188:191]
v_add_u32 v47, 4294901760, v47
v_add_u32 v48, 4294901760, v48
v_mfma_f32_16x16x32_bf16 v[224:227], a[140:143], a[196:199], v[224:227]
buffer_load_dwordx4 v39, s[12:15], s24 offen offset:3072 lds
v_add_u32 v49, 4294901760, v49
v_add_u32 v50, 4294901760, v50
v_mfma_f32_16x16x32_bf16 v[228:231], a[140:143], a[200:203], v[228:231]
v_add_u32 v45, s28, v45
v_add_u32 v46, s28, v46
v_mfma_f32_16x16x32_bf16 v[152:155], a[164:167], a[160:163], v[152:155]
v_mul_f32 v72, v53, v72
v_mul_f32 v73, v53, v73
v_mfma_f32_16x16x32_bf16 v[156:159], a[168:171], a[160:163], v[156:159]
s_add_u32 s24, s24, 128
v_cvt_pk_bf16_f32 v66, v72, v73
v_accvgpr_read_b32 v74, a102
v_mfma_f32_16x16x32_bf16 v[192:195], a[172:175], a[160:163], v[192:195]
v_accvgpr_read_b32 v75, a103
v_mul_f32 v74, v53, v74
v_mfma_f32_16x16x32_bf16 v[196:199], a[192:195], a[160:163], v[196:199]
v_mul_f32 v75, v53, v75
v_cvt_pk_bf16_f32 v67, v74, v75
v_mfma_f32_16x16x32_bf16 v[232:235], a[160:163], a[196:199], v[232:235]
buffer_store_dwordx4 v[64:67], v7, s[20:23], s43 offen offset:2048 sc1
v_mfma_f32_16x16x32_bf16 v[236:239], a[160:163], a[200:203], v[236:239]
s_waitcnt vmcnt(19) lgkmcnt(0)
s_barrier
v_mfma_f32_16x16x32_bf16 v[160:163], a[28:31], a[0:3], v[160:163]
v_mfma_f32_16x16x32_bf16 v[164:167], a[32:35], a[0:3], v[164:167]
s_add_u32 m0, s48, 98304
ds_read_b64_tr_b16 a[164:165], v47 offset:0
ds_read_b64_tr_b16 a[166:167], v47 offset:512
v_mfma_f32_16x16x32_bf16 v[200:203], a[0:3], a[36:39], v[200:203]
buffer_load_dwordx4 v40, s[16:19], s26 offen offset:0 lds
v_accvgpr_read_b32 v72, a104
v_mfma_f32_16x16x32_bf16 v[204:207], a[0:3], a[40:43], v[204:207]
ds_read_b128 a[128:131], v45
v_accvgpr_read_b32 v73, a105
v_mfma_f32_16x16x32_bf16 v[128:131], a[20:23], a[4:7], v[128:131]
ds_read_b64_tr_b16 a[168:169], v48 offset:0
ds_read_b64_tr_b16 a[170:171], v48 offset:512
v_mfma_f32_16x16x32_bf16 v[132:135], a[24:27], a[4:7], v[132:135]
buffer_load_dwordx4 v41, s[16:19], s26 offen offset:1024 lds
v_mul_f32 v72, v53, v72
v_mfma_f32_16x16x32_bf16 v[168:171], a[28:31], a[4:7], v[168:171]
ds_read_b128 a[132:135], v43 offset:0
v_mul_f32 v73, v53, v73
v_mfma_f32_16x16x32_bf16 v[172:175], a[32:35], a[4:7], v[172:175]
ds_read_b64_tr_b16 a[172:173], v47 offset:2048
ds_read_b64_tr_b16 a[174:175], v47 offset:2560
v_mfma_f32_16x16x32_bf16 v[208:211], a[4:7], a[36:39], v[208:211]
buffer_load_dwordx4 v40, s[16:19], s26 offen offset:2048 lds
v_cvt_pk_bf16_f32 v68, v72, v73
v_mfma_f32_16x16x32_bf16 v[212:215], a[4:7], a[40:43], v[212:215]
ds_read_b128 a[136:139], v43 offset:2048
v_accvgpr_read_b32 v74, a106
v_mfma_f32_16x16x32_bf16 v[136:139], a[20:23], a[8:11], v[136:139]
ds_read_b64_tr_b16 a[192:193], v48 offset:2048
ds_read_b64_tr_b16 a[194:195], v48 offset:2560
v_mfma_f32_16x16x32_bf16 v[140:143], a[24:27], a[8:11], v[140:143]
buffer_load_dwordx4 v41, s[16:19], s26 offen offset:3072 lds
v_accvgpr_read_b32 v75, a107
v_mfma_f32_16x16x32_bf16 v[176:179], a[28:31], a[8:11], v[176:179]
ds_read_b128 a[140:143], v43 offset:4096
v_mul_f32 v74, v53, v74
v_mfma_f32_16x16x32_bf16 v[180:183], a[32:35], a[8:11], v[180:183]
s_add_u32 m0, s48, 102400
ds_read_b64_tr_b16 a[196:197], v49 offset:4096
ds_read_b64_tr_b16 a[198:199], v49 offset:4608
v_mfma_f32_16x16x32_bf16 v[216:219], a[8:11], a[36:39], v[216:219]
buffer_load_dwordx4 v40, s[16:19], s47 offen offset:0 lds
v_mul_f32 v75, v53, v75
v_mfma_f32_16x16x32_bf16 v[220:223], a[8:11], a[40:43], v[220:223]
ds_read_b128 a[160:163], v43 offset:6144
v_cvt_pk_bf16_f32 v69, v74, v75
v_mfma_f32_16x16x32_bf16 v[144:147], a[20:23], a[12:15], v[144:147]
ds_read_b64_tr_b16 a[200:201], v50 offset:4096
ds_read_b64_tr_b16 a[202:203], v50 offset:4608
v_mfma_f32_16x16x32_bf16 v[148:151], a[24:27], a[12:15], v[148:151]
buffer_load_dwordx4 v41, s[16:19], s47 offen offset:1024 lds
v_accvgpr_read_b32 v72, a108
v_mfma_f32_16x16x32_bf16 v[184:187], a[28:31], a[12:15], v[184:187]
v_accvgpr_read_b32 v73, a109
v_mul_f32 v72, v53, v72
v_mfma_f32_16x16x32_bf16 v[188:191], a[32:35], a[12:15], v[188:191]
v_mul_f32 v73, v53, v73
v_cvt_pk_bf16_f32 v70, v72, v73
v_mfma_f32_16x16x32_bf16 v[224:227], a[12:15], a[36:39], v[224:227]
buffer_load_dwordx4 v42, s[12:15], s25 offen offset:2048 lds
v_accvgpr_read_b32 v74, a110
v_mfma_f32_16x16x32_bf16 v[228:231], a[12:15], a[40:43], v[228:231]
v_accvgpr_read_b32 v75, a111
v_mul_f32 v74, v53, v74
v_mfma_f32_16x16x32_bf16 v[152:155], a[20:23], a[16:19], v[152:155]
s_add_u32 s26, s26, 393216
s_add_u32 s47, s47, 393216
s_add_u32 s25, s25, 128
v_mfma_f32_16x16x32_bf16 v[156:159], a[24:27], a[16:19], v[156:159]
v_mul_f32 v75, v53, v75
v_cvt_pk_bf16_f32 v71, v74, v75
v_mfma_f32_16x16x32_bf16 v[192:195], a[28:31], a[16:19], v[192:195]
buffer_store_dwordx4 v[68:71], v7, s[20:23], s43 offen offset:3072 sc1
v_mfma_f32_16x16x32_bf16 v[196:199], a[32:35], a[16:19], v[196:199]
v_mfma_f32_16x16x32_bf16 v[232:235], a[16:19], a[36:39], v[232:235]
v_mfma_f32_16x16x32_bf16 v[236:239], a[16:19], a[40:43], v[236:239]
s_waitcnt lgkmcnt(0)
v_mfma_f32_16x16x32_bf16 v[160:163], a[172:175], a[128:131], v[160:163]
v_mfma_f32_16x16x32_bf16 v[164:167], a[192:195], a[128:131], v[164:167]
s_add_u32 m0, s91, 16384
ds_read_b64_tr_b16 a[20:21], v47 offset:14336
ds_read_b64_tr_b16 a[22:23], v47 offset:14848
v_mfma_f32_16x16x32_bf16 v[200:203], a[128:131], a[196:199], v[200:203]
buffer_load_dwordx4 v36, s[12:15], s24 offen offset:0 lds
v_mfma_f32_16x16x32_bf16 v[204:207], a[128:131], a[200:203], v[204:207]
ds_read_b128 a[0:3], v46
v_mfma_f32_16x16x32_bf16 v[128:131], a[164:167], a[132:135], v[128:131]
ds_read_b64_tr_b16 a[24:25], v48 offset:14336
ds_read_b64_tr_b16 a[26:27], v48 offset:14848
v_mfma_f32_16x16x32_bf16 v[132:135], a[168:171], a[132:135], v[132:135]
v_mfma_f32_16x16x32_bf16 v[168:171], a[172:175], a[132:135], v[168:171]
ds_read_b128 a[4:7], v44 offset:0
v_mfma_f32_16x16x32_bf16 v[172:175], a[192:195], a[132:135], v[172:175]
ds_read_b64_tr_b16 a[28:29], v47 offset:16384
ds_read_b64_tr_b16 a[30:31], v47 offset:16896
v_mfma_f32_16x16x32_bf16 v[208:211], a[132:135], a[196:199], v[208:211]
buffer_load_dwordx4 v37, s[12:15], s24 offen offset:1024 lds
v_mfma_f32_16x16x32_bf16 v[212:215], a[132:135], a[200:203], v[212:215]
ds_read_b128 a[8:11], v44 offset:2048
v_mfma_f32_16x16x32_bf16 v[136:139], a[164:167], a[136:139], v[136:139]
ds_read_b64_tr_b16 a[32:33], v48 offset:16384
ds_read_b64_tr_b16 a[34:35], v48 offset:16896
v_mfma_f32_16x16x32_bf16 v[140:143], a[168:171], a[136:139], v[140:143]
v_mfma_f32_16x16x32_bf16 v[176:179], a[172:175], a[136:139], v[176:179]
ds_read_b128 a[12:15], v44 offset:4096
v_mfma_f32_16x16x32_bf16 v[180:183], a[192:195], a[136:139], v[180:183]
ds_read_b64_tr_b16 a[36:37], v49 offset:18432
ds_read_b64_tr_b16 a[38:39], v49 offset:18944
v_mfma_f32_16x16x32_bf16 v[216:219], a[136:139], a[196:199], v[216:219]
buffer_load_dwordx4 v38, s[12:15], s24 offen offset:2048 lds
v_mfma_f32_16x16x32_bf16 v[220:223], a[136:139], a[200:203], v[220:223]
ds_read_b128 a[16:19], v44 offset:6144
v_mfma_f32_16x16x32_bf16 v[144:147], a[164:167], a[140:143], v[144:147]
ds_read_b64_tr_b16 a[40:41], v50 offset:18432
ds_read_b64_tr_b16 a[42:43], v50 offset:18944
v_mfma_f32_16x16x32_bf16 v[148:151], a[168:171], a[140:143], v[148:151]
s_cmp_eq_u32 s39, 1
s_mov_b32 s28, 16384
s_cselect_b32 s28, 98304, s28
v_mfma_f32_16x16x32_bf16 v[184:187], a[172:175], a[140:143], v[184:187]
v_add_u32 v43, 16384, v43
v_add_u32 v44, 16384, v44
v_mfma_f32_16x16x32_bf16 v[188:191], a[192:195], a[140:143], v[188:191]
v_add_u32 v47, 98304, v47
v_add_u32 v48, 98304, v48
v_mfma_f32_16x16x32_bf16 v[224:227], a[140:143], a[196:199], v[224:227]
buffer_load_dwordx4 v39, s[12:15], s24 offen offset:3072 lds
v_add_u32 v49, 98304, v49
v_add_u32 v50, 98304, v50
v_mfma_f32_16x16x32_bf16 v[228:231], a[140:143], a[200:203], v[228:231]
v_add_u32 v45, s28, v45
v_add_u32 v46, s28, v46
v_mfma_f32_16x16x32_bf16 v[152:155], a[164:167], a[160:163], v[152:155]
v_mfma_f32_16x16x32_bf16 v[156:159], a[168:171], a[160:163], v[156:159]
s_add_u32 s24, s24, 128
v_mfma_f32_16x16x32_bf16 v[192:195], a[172:175], a[160:163], v[192:195]
v_mfma_f32_16x16x32_bf16 v[196:199], a[192:195], a[160:163], v[196:199]
v_mfma_f32_16x16x32_bf16 v[232:235], a[160:163], a[196:199], v[232:235]
v_mfma_f32_16x16x32_bf16 v[236:239], a[160:163], a[200:203], v[236:239]
s_waitcnt vmcnt(18) lgkmcnt(0)
s_barrier
v_mfma_f32_16x16x32_bf16 v[160:163], a[28:31], a[0:3], v[160:163]
v_mfma_f32_16x16x32_bf16 v[164:167], a[32:35], a[0:3], v[164:167]
s_add_u32 m0, s48, 32768
ds_read_b64_tr_b16 a[164:165], v47 offset:0
ds_read_b64_tr_b16 a[166:167], v47 offset:512
v_mfma_f32_16x16x32_bf16 v[200:203], a[0:3], a[36:39], v[200:203]
buffer_load_dwordx4 v40, s[16:19], s26 offen offset:0 lds
v_mfma_f32_16x16x32_bf16 v[204:207], a[0:3], a[40:43], v[204:207]
ds_read_b128 a[128:131], v45
v_mfma_f32_16x16x32_bf16 v[128:131], a[20:23], a[4:7], v[128:131]
ds_read_b64_tr_b16 a[168:169], v48 offset:0
ds_read_b64_tr_b16 a[170:171], v48 offset:512
v_mfma_f32_16x16x32_bf16 v[132:135], a[24:27], a[4:7], v[132:135]
buffer_load_dwordx4 v41, s[16:19], s26 offen offset:1024 lds
v_mfma_f32_16x16x32_bf16 v[168:171], a[28:31], a[4:7], v[168:171]
ds_read_b128 a[132:135], v43 offset:0
v_mfma_f32_16x16x32_bf16 v[172:175], a[32:35], a[4:7], v[172:175]
ds_read_b64_tr_b16 a[172:173], v47 offset:2048
ds_read_b64_tr_b16 a[174:175], v47 offset:2560
v_mfma_f32_16x16x32_bf16 v[208:211], a[4:7], a[36:39], v[208:211]
buffer_load_dwordx4 v40, s[16:19], s26 offen offset:2048 lds
v_mfma_f32_16x16x32_bf16 v[212:215], a[4:7], a[40:43], v[212:215]
ds_read_b128 a[136:139], v43 offset:2048
v_mfma_f32_16x16x32_bf16 v[136:139], a[20:23], a[8:11], v[136:139]
ds_read_b64_tr_b16 a[192:193], v48 offset:2048
ds_read_b64_tr_b16 a[194:195], v48 offset:2560
v_mfma_f32_16x16x32_bf16 v[140:143], a[24:27], a[8:11], v[140:143]
buffer_load_dwordx4 v41, s[16:19], s26 offen offset:3072 lds
v_mfma_f32_16x16x32_bf16 v[176:179], a[28:31], a[8:11], v[176:179]
ds_read_b128 a[140:143], v43 offset:4096
v_mfma_f32_16x16x32_bf16 v[180:183], a[32:35], a[8:11], v[180:183]
s_add_u32 m0, s48, 36864
ds_read_b64_tr_b16 a[196:197], v49 offset:4096
ds_read_b64_tr_b16 a[198:199], v49 offset:4608
v_mfma_f32_16x16x32_bf16 v[216:219], a[8:11], a[36:39], v[216:219]
buffer_load_dwordx4 v40, s[16:19], s47 offen offset:0 lds
v_mfma_f32_16x16x32_bf16 v[220:223], a[8:11], a[40:43], v[220:223]
ds_read_b128 a[160:163], v43 offset:6144
v_mfma_f32_16x16x32_bf16 v[144:147], a[20:23], a[12:15], v[144:147]
ds_read_b64_tr_b16 a[200:201], v50 offset:4096
ds_read_b64_tr_b16 a[202:203], v50 offset:4608
v_mfma_f32_16x16x32_bf16 v[148:151], a[24:27], a[12:15], v[148:151]
buffer_load_dwordx4 v41, s[16:19], s47 offen offset:1024 lds
v_mfma_f32_16x16x32_bf16 v[184:187], a[28:31], a[12:15], v[184:187]
v_mfma_f32_16x16x32_bf16 v[188:191], a[32:35], a[12:15], v[188:191]
v_mfma_f32_16x16x32_bf16 v[224:227], a[12:15], a[36:39], v[224:227]
buffer_load_dwordx4 v42, s[12:15], s25 offen offset:2048 lds
v_mfma_f32_16x16x32_bf16 v[228:231], a[12:15], a[40:43], v[228:231]
v_mfma_f32_16x16x32_bf16 v[152:155], a[20:23], a[16:19], v[152:155]
s_add_u32 s26, s26, 393216
s_add_u32 s47, s47, 393216
s_add_u32 s25, s25, 128
v_mfma_f32_16x16x32_bf16 v[156:159], a[24:27], a[16:19], v[156:159]
v_mfma_f32_16x16x32_bf16 v[192:195], a[28:31], a[16:19], v[192:195]
v_mfma_f32_16x16x32_bf16 v[196:199], a[32:35], a[16:19], v[196:199]
v_mfma_f32_16x16x32_bf16 v[232:235], a[16:19], a[36:39], v[232:235]
v_mfma_f32_16x16x32_bf16 v[236:239], a[16:19], a[40:43], v[236:239]
s_waitcnt lgkmcnt(0)
v_mfma_f32_16x16x32_bf16 v[160:163], a[172:175], a[128:131], v[160:163]
s_bfe_u32 s51, s86, 0x10001
s_lshl_b32 s51, s51, 3
s_and_b32 s54, s87, 7
v_mfma_f32_16x16x32_bf16 v[164:167], a[192:195], a[128:131], v[164:167]
s_add_u32 m0, s91, 65536
ds_read_b64_tr_b16 a[20:21], v47 offset:14336
ds_read_b64_tr_b16 a[22:23], v47 offset:14848
v_mfma_f32_16x16x32_bf16 v[200:203], a[128:131], a[196:199], v[200:203]
s_or_b32 s51, s51, s54
buffer_load_dwordx4 v36, s[12:15], s24 offen offset:0 lds
v_mfma_f32_16x16x32_bf16 v[204:207], a[128:131], a[200:203], v[204:207]
s_lshr_b32 s52, s86, 2
ds_read_b128 a[0:3], v46
v_mfma_f32_16x16x32_bf16 v[128:131], a[164:167], a[132:135], v[128:131]
ds_read_b64_tr_b16 a[24:25], v48 offset:14336
ds_read_b64_tr_b16 a[26:27], v48 offset:14848
v_mfma_f32_16x16x32_bf16 v[132:135], a[168:171], a[132:135], v[132:135]
s_lshl_b32 s52, s52, 2
s_lshr_b32 s54, s87, 3
s_or_b32 s52, s52, s54
v_mfma_f32_16x16x32_bf16 v[168:171], a[172:175], a[132:135], v[168:171]
s_lshl_b32 s52, s52, 8
ds_read_b128 a[4:7], v44 offset:0
v_mfma_f32_16x16x32_bf16 v[172:175], a[192:195], a[132:135], v[172:175]
ds_read_b64_tr_b16 a[28:29], v47 offset:16384
ds_read_b64_tr_b16 a[30:31], v47 offset:16896
v_mfma_f32_16x16x32_bf16 v[208:211], a[132:135], a[196:199], v[208:211]
s_and_b32 s55, s86, 1
buffer_load_dwordx4 v37, s[12:15], s24 offen offset:1024 lds
v_mfma_f32_16x16x32_bf16 v[212:215], a[132:135], a[200:203], v[212:215]
s_lshl_b32 s54, s51, 2
ds_read_b128 a[8:11], v44 offset:2048
v_mfma_f32_16x16x32_bf16 v[136:139], a[164:167], a[136:139], v[136:139]
ds_read_b64_tr_b16 a[32:33], v48 offset:16384
ds_read_b64_tr_b16 a[34:35], v48 offset:16896
v_mfma_f32_16x16x32_bf16 v[140:143], a[168:171], a[136:139], v[140:143]
s_add_u32 s54, s54, s3
s_lshl_b32 s54, s54, 18
s_add_u32 s54, s54, 33554432
v_mfma_f32_16x16x32_bf16 v[176:179], a[172:175], a[136:139], v[176:179]
ds_read_b128 a[12:15], v44 offset:4096
v_mfma_f32_16x16x32_bf16 v[180:183], a[192:195], a[136:139], v[180:183]
ds_read_b64_tr_b16 a[36:37], v49 offset:18432
ds_read_b64_tr_b16 a[38:39], v49 offset:18944
v_mfma_f32_16x16x32_bf16 v[216:219], a[136:139], a[196:199], v[216:219]
buffer_load_dwordx4 v38, s[12:15], s24 offen offset:2048 lds
v_mfma_f32_16x16x32_bf16 v[220:223], a[136:139], a[200:203], v[220:223]
ds_read_b128 a[16:19], v44 offset:6144
v_mfma_f32_16x16x32_bf16 v[144:147], a[164:167], a[140:143], v[144:147]
ds_read_b64_tr_b16 a[40:41], v50 offset:18432
ds_read_b64_tr_b16 a[42:43], v50 offset:18944
v_mfma_f32_16x16x32_bf16 v[148:151], a[168:171], a[140:143], v[148:151]
s_cmp_eq_u32 s39, 1
s_mov_b32 s28, 4294885376
s_cselect_b32 s28, 4294934528, s28
v_mfma_f32_16x16x32_bf16 v[184:187], a[172:175], a[140:143], v[184:187]
v_add_u32 v43, 4294885376, v43
v_add_u32 v44, 4294885376, v44
v_mfma_f32_16x16x32_bf16 v[188:191], a[192:195], a[140:143], v[188:191]
v_add_u32 v47, 4294934528, v47
v_add_u32 v48, 4294934528, v48
v_mfma_f32_16x16x32_bf16 v[224:227], a[140:143], a[196:199], v[224:227]
buffer_load_dwordx4 v39, s[12:15], s24 offen offset:3072 lds
v_add_u32 v49, 4294934528, v49
v_add_u32 v50, 4294934528, v50
v_mfma_f32_16x16x32_bf16 v[228:231], a[140:143], a[200:203], v[228:231]
v_add_u32 v45, s28, v45
v_add_u32 v46, s28, v46
v_mfma_f32_16x16x32_bf16 v[152:155], a[164:167], a[160:163], v[152:155]
s_add_u32 s4, s84, s54
s_addc_u32 s5, s85, 0
s_and_b32 s5, s5, 0xffff
v_mfma_f32_16x16x32_bf16 v[156:159], a[168:171], a[160:163], v[156:159]
s_add_u32 s24, s24, 128
s_lshl_b32 s54, s3, 15
s_add_u32 s54, s54, s52
v_mfma_f32_16x16x32_bf16 v[192:195], a[172:175], a[160:163], v[192:195]
s_add_u32 s8, s80, s54
s_addc_u32 s9, s81, 0
s_and_b32 s9, s9, 0xffff
v_mfma_f32_16x16x32_bf16 v[196:199], a[192:195], a[160:163], v[196:199]
s_mul_i32 s44, s55, 172032
s_mul_i32 s45, s55, 5505024
s_mov_b32 s56, 172032
v_mfma_f32_16x16x32_bf16 v[232:235], a[160:163], a[196:199], v[232:235]
s_mov_b32 s57, 5505024
v_mfma_f32_16x16x32_bf16 v[236:239], a[160:163], a[200:203], v[236:239]
s_waitcnt vmcnt(16) lgkmcnt(0)
s_barrier
v_mfma_f32_16x16x32_bf16 v[160:163], a[28:31], a[0:3], v[160:163]
s_cmp_eq_u32 s55, 0
s_cselect_b32 s6, s56, 0x40000
s_cselect_b32 s57, s57, 0x800000
v_mfma_f32_16x16x32_bf16 v[164:167], a[32:35], a[0:3], v[164:167]
s_add_u32 m0, s48, 131072
ds_read_b64_tr_b16 a[164:165], v47 offset:0
ds_read_b64_tr_b16 a[166:167], v47 offset:512
v_mfma_f32_16x16x32_bf16 v[200:203], a[0:3], a[36:39], v[200:203]
s_sub_u32 s10, s57, s54
buffer_load_dwordx4 v40, s[16:19], s26 offen offset:0 lds
v_mfma_f32_16x16x32_bf16 v[204:207], a[0:3], a[40:43], v[204:207]
s_and_b32 s54, s3, 1
ds_read_b128 a[128:131], v45
v_mfma_f32_16x16x32_bf16 v[128:131], a[20:23], a[4:7], v[128:131]
ds_read_b64_tr_b16 a[168:169], v48 offset:0
ds_read_b64_tr_b16 a[170:171], v48 offset:512
v_mfma_f32_16x16x32_bf16 v[132:135], a[24:27], a[4:7], v[132:135]
s_lshl_b32 s54, s54, 13
buffer_load_dwordx4 v41, s[16:19], s26 offen offset:1024 lds
v_mfma_f32_16x16x32_bf16 v[168:171], a[28:31], a[4:7], v[168:171]
s_add_u32 s54, s54, 98304
ds_read_b128 a[132:135], v43 offset:0
v_mfma_f32_16x16x32_bf16 v[172:175], a[32:35], a[4:7], v[172:175]
ds_read_b64_tr_b16 a[172:173], v47 offset:2048
ds_read_b64_tr_b16 a[174:175], v47 offset:2560
v_mfma_f32_16x16x32_bf16 v[208:211], a[4:7], a[36:39], v[208:211]
s_lshr_b32 s56, s3, 1
buffer_load_dwordx4 v40, s[16:19], s26 offen offset:2048 lds
v_mfma_f32_16x16x32_bf16 v[212:215], a[4:7], a[40:43], v[212:215]
s_lshl_b32 s56, s56, 2
ds_read_b128 a[136:139], v43 offset:2048
v_mfma_f32_16x16x32_bf16 v[136:139], a[20:23], a[8:11], v[136:139]
ds_read_b64_tr_b16 a[192:193], v48 offset:2048
ds_read_b64_tr_b16 a[194:195], v48 offset:2560
v_mfma_f32_16x16x32_bf16 v[140:143], a[24:27], a[8:11], v[140:143]
s_sub_u32 s57, 15, s51
buffer_load_dwordx4 v41, s[16:19], s26 offen offset:3072 lds
v_mfma_f32_16x16x32_bf16 v[176:179], a[28:31], a[8:11], v[176:179]
s_min_u32 s57, s57, s51
ds_read_b128 a[140:143], v43 offset:4096
v_mfma_f32_16x16x32_bf16 v[180:183], a[32:35], a[8:11], v[180:183]
s_add_u32 m0, s48, 135168
ds_read_b64_tr_b16 a[196:197], v49 offset:4096
ds_read_b64_tr_b16 a[198:199], v49 offset:4608
v_mfma_f32_16x16x32_bf16 v[216:219], a[8:11], a[36:39], v[216:219]
s_lshl_b32 s57, s57, 10
buffer_load_dwordx4 v40, s[16:19], s47 offen offset:0 lds
v_mfma_f32_16x16x32_bf16 v[220:223], a[8:11], a[40:43], v[220:223]
ds_read_b128 a[160:163], v43 offset:6144
v_lshrrev_b32 v252, 4, v1
v_mfma_f32_16x16x32_bf16 v[144:147], a[20:23], a[12:15], v[144:147]
ds_read_b64_tr_b16 a[200:201], v50 offset:4096
ds_read_b64_tr_b16 a[202:203], v50 offset:4608
v_mfma_f32_16x16x32_bf16 v[148:151], a[24:27], a[12:15], v[148:151]
buffer_load_dwordx4 v41, s[16:19], s47 offen offset:1024 lds
v_and_b32 v253, 15, v1
v_mfma_f32_16x16x32_bf16 v[184:187], a[28:31], a[12:15], v[184:187]
v_lshlrev_b32 v254, 1, v252
v_xor_b32 v254, v254, v253
v_mfma_f32_16x16x32_bf16 v[188:191], a[32:35], a[12:15], v[188:191]
v_lshlrev_b32 v254, 4, v254
v_lshl_add_u32 v240, v252, 11, v254
v_mfma_f32_16x16x32_bf16 v[224:227], a[12:15], a[36:39], v[224:227]
buffer_load_dwordx4 v42, s[12:15], s25 offen offset:2048 lds
v_xor_b32 v241, 0x80, v240
v_mfma_f32_16x16x32_bf16 v[228:231], a[12:15], a[40:43], v[228:231]
v_add_u32 v241, 7168, v241
v_add_u32 v242, 14336, v240
v_mfma_f32_16x16x32_bf16 v[152:155], a[20:23], a[16:19], v[152:155]
s_add_u32 s26, s26, 393216
s_add_u32 s47, s47, 393216
s_add_u32 s25, s25, 128
v_mfma_f32_16x16x32_bf16 v[156:159], a[24:27], a[16:19], v[156:159]
v_add_u32 v243, 14336, v241
v_lshlrev_b32 v254, 9, v252
v_mfma_f32_16x16x32_bf16 v[192:195], a[28:31], a[16:19], v[192:195]
v_lshl_add_u32 v254, v253, 4, v254
v_add_u32 v244, s54, v254
v_mfma_f32_16x16x32_bf16 v[196:199], a[32:35], a[16:19], v[196:199]
v_bfe_u32 v254, v1, 2, 3
v_lshrrev_b32 v255, 5, v1
v_mfma_f32_16x16x32_bf16 v[232:235], a[16:19], a[36:39], v[232:235]
v_lshl_add_u32 v255, v255, 4, v254
v_lshlrev_b32 v255, 8, v255
v_mfma_f32_16x16x32_bf16 v[236:239], a[16:19], a[40:43], v[236:239]
s_waitcnt lgkmcnt(0)
v_and_b32 v253, 3, v1
v_lshl_add_u32 v255, v253, 3, v255
v_mfma_f32_16x16x32_bf16 v[160:163], a[172:175], a[128:131], v[160:163]
v_add_u32 v255, 114688, v255
v_xor_b32 v254, s56, v254
v_mfma_f32_16x16x32_bf16 v[164:167], a[192:195], a[128:131], v[164:167]
s_add_u32 m0, s91, 81920
ds_read_b64_tr_b16 a[20:21], v47 offset:14336
ds_read_b64_tr_b16 a[22:23], v47 offset:14848
v_mfma_f32_16x16x32_bf16 v[200:203], a[128:131], a[196:199], v[200:203]
buffer_load_dwordx4 v36, s[12:15], s24 offen offset:0 lds
v_xor_b32 v253, 0, v254
v_mfma_f32_16x16x32_bf16 v[204:207], a[128:131], a[200:203], v[204:207]
ds_read_b128 a[0:3], v46
v_lshl_add_u32 v245, v253, 5, v255
v_mfma_f32_16x16x32_bf16 v[128:131], a[164:167], a[132:135], v[128:131]
ds_read_b64_tr_b16 a[24:25], v48 offset:14336
ds_read_b64_tr_b16 a[26:27], v48 offset:14848
v_mfma_f32_16x16x32_bf16 v[132:135], a[168:171], a[132:135], v[132:135]
v_xor_b32 v253, 1, v254
v_lshl_add_u32 v246, v253, 5, v255
v_mfma_f32_16x16x32_bf16 v[168:171], a[172:175], a[132:135], v[168:171]
ds_read_b128 a[4:7], v44 offset:0
v_xor_b32 v253, 2, v254
v_mfma_f32_16x16x32_bf16 v[172:175], a[192:195], a[132:135], v[172:175]
ds_read_b64_tr_b16 a[28:29], v47 offset:16384
ds_read_b64_tr_b16 a[30:31], v47 offset:16896
v_mfma_f32_16x16x32_bf16 v[208:211], a[132:135], a[196:199], v[208:211]
buffer_load_dwordx4 v37, s[12:15], s24 offen offset:1024 lds
v_lshl_add_u32 v247, v253, 5, v255
v_mfma_f32_16x16x32_bf16 v[212:215], a[132:135], a[200:203], v[212:215]
ds_read_b128 a[8:11], v44 offset:2048
v_xor_b32 v253, 3, v254
v_mfma_f32_16x16x32_bf16 v[136:139], a[164:167], a[136:139], v[136:139]
ds_read_b64_tr_b16 a[32:33], v48 offset:16384
ds_read_b64_tr_b16 a[34:35], v48 offset:16896
v_mfma_f32_16x16x32_bf16 v[140:143], a[168:171], a[136:139], v[140:143]
v_lshl_add_u32 v248, v253, 5, v255
v_and_b32 v253, 31, v1
v_mfma_f32_16x16x32_bf16 v[176:179], a[172:175], a[136:139], v[176:179]
ds_read_b128 a[12:15], v44 offset:4096
v_lshl_add_u32 v249, v253, 5, s57
v_mfma_f32_16x16x32_bf16 v[180:183], a[192:195], a[136:139], v[180:183]
ds_read_b64_tr_b16 a[36:37], v49 offset:18432
ds_read_b64_tr_b16 a[38:39], v49 offset:18944
v_mfma_f32_16x16x32_bf16 v[216:219], a[136:139], a[196:199], v[216:219]
buffer_load_dwordx4 v38, s[12:15], s24 offen offset:2048 lds
v_mfma_f32_16x16x32_bf16 v[220:223], a[136:139], a[200:203], v[220:223]
ds_read_b128 a[16:19], v44 offset:6144
v_mfma_f32_16x16x32_bf16 v[144:147], a[164:167], a[140:143], v[144:147]
ds_read_b64_tr_b16 a[40:41], v50 offset:18432
ds_read_b64_tr_b16 a[42:43], v50 offset:18944
v_mfma_f32_16x16x32_bf16 v[148:151], a[168:171], a[140:143], v[148:151]
s_cmp_eq_u32 s39, 1
s_mov_b32 s28, 16384
s_cselect_b32 s28, 4294901760, s28
v_mfma_f32_16x16x32_bf16 v[184:187], a[172:175], a[140:143], v[184:187]
v_add_u32 v43, 16384, v43
v_add_u32 v44, 16384, v44
v_mfma_f32_16x16x32_bf16 v[188:191], a[192:195], a[140:143], v[188:191]
v_add_u32 v47, 4294901760, v47
v_add_u32 v48, 4294901760, v48
v_mfma_f32_16x16x32_bf16 v[224:227], a[140:143], a[196:199], v[224:227]
buffer_load_dwordx4 v39, s[12:15], s24 offen offset:3072 lds
v_add_u32 v49, 4294901760, v49
v_add_u32 v50, 4294901760, v50
v_mfma_f32_16x16x32_bf16 v[228:231], a[140:143], a[200:203], v[228:231]
v_add_u32 v45, s28, v45
v_add_u32 v46, s28, v46
v_mfma_f32_16x16x32_bf16 v[152:155], a[164:167], a[160:163], v[152:155]
v_mfma_f32_16x16x32_bf16 v[156:159], a[168:171], a[160:163], v[156:159]
s_add_u32 s24, s24, 128
v_mfma_f32_16x16x32_bf16 v[192:195], a[172:175], a[160:163], v[192:195]
v_mfma_f32_16x16x32_bf16 v[196:199], a[192:195], a[160:163], v[196:199]
v_mfma_f32_16x16x32_bf16 v[232:235], a[160:163], a[196:199], v[232:235]
v_mfma_f32_16x16x32_bf16 v[236:239], a[160:163], a[200:203], v[236:239]
s_waitcnt vmcnt(26)
s_waitcnt vmcnt(15) lgkmcnt(0)
s_barrier
v_mfma_f32_16x16x32_bf16 v[160:163], a[28:31], a[0:3], v[160:163]
s_nop 0
v_or_b32 v0, 3, v11
v_mfma_f32_16x16x32_bf16 v[164:167], a[32:35], a[0:3], v[164:167]
s_add_u32 m0, s48, 98304
ds_read_b64_tr_b16 a[164:165], v47 offset:0
ds_read_b64_tr_b16 a[166:167], v47 offset:512
v_mfma_f32_16x16x32_bf16 v[200:203], a[0:3], a[36:39], v[200:203]
buffer_load_dwordx4 v40, s[16:19], s26 offen offset:0 lds
v_mfma_f32_16x16x32_bf16 v[204:207], a[0:3], a[40:43], v[204:207]
ds_read_b128 a[128:131], v45
v_mfma_f32_16x16x32_bf16 v[128:131], a[20:23], a[4:7], v[128:131]
ds_read_b64_tr_b16 a[168:169], v48 offset:0
ds_read_b64_tr_b16 a[170:171], v48 offset:512
v_mfma_f32_16x16x32_bf16 v[132:135], a[24:27], a[4:7], v[132:135]
buffer_load_dwordx4 v41, s[16:19], s26 offen offset:1024 lds
v_mfma_f32_16x16x32_bf16 v[168:171], a[28:31], a[4:7], v[168:171]
ds_read_b128 a[132:135], v43 offset:0
v_mfma_f32_16x16x32_bf16 v[172:175], a[32:35], a[4:7], v[172:175]
ds_read_b64_tr_b16 a[172:173], v47 offset:2048
ds_read_b64_tr_b16 a[174:175], v47 offset:2560
v_mfma_f32_16x16x32_bf16 v[208:211], a[4:7], a[36:39], v[208:211]
buffer_load_dwordx4 v40, s[16:19], s26 offen offset:2048 lds
v_mfma_f32_16x16x32_bf16 v[212:215], a[4:7], a[40:43], v[212:215]
ds_read_b128 a[136:139], v43 offset:2048
v_mfma_f32_16x16x32_bf16 v[136:139], a[20:23], a[8:11], v[136:139]
ds_read_b64_tr_b16 a[192:193], v48 offset:2048
ds_read_b64_tr_b16 a[194:195], v48 offset:2560
v_mfma_f32_16x16x32_bf16 v[140:143], a[24:27], a[8:11], v[140:143]
buffer_load_dwordx4 v41, s[16:19], s26 offen offset:3072 lds
v_mfma_f32_16x16x32_bf16 v[176:179], a[28:31], a[8:11], v[176:179]
ds_read_b128 a[140:143], v43 offset:4096
v_mfma_f32_16x16x32_bf16 v[180:183], a[32:35], a[8:11], v[180:183]
s_add_u32 m0, s48, 102400
ds_read_b64_tr_b16 a[196:197], v49 offset:4096
ds_read_b64_tr_b16 a[198:199], v49 offset:4608
.Lpad16:
s_cbranch_execz .Ltch5
v_mfma_f32_16x16x32_bf16 v[216:219], a[8:11], a[36:39], v[216:219]
buffer_load_dwordx4 v40, s[16:19], s47 offen offset:0 lds
v_mfma_f32_16x16x32_bf16 v[220:223], a[8:11], a[40:43], v[220:223]
ds_read_b128 a[160:163], v43 offset:6144
v_mfma_f32_16x16x32_bf16 v[144:147], a[20:23], a[12:15], v[144:147]
ds_read_b64_tr_b16 a[200:201], v50 offset:4096
ds_read_b64_tr_b16 a[202:203], v50 offset:4608
v_mfma_f32_16x16x32_bf16 v[148:151], a[24:27], a[12:15], v[148:151]
buffer_load_dwordx4 v41, s[16:19], s47 offen offset:1024 lds
v_mfma_f32_16x16x32_bf16 v[184:187], a[28:31], a[12:15], v[184:187]
v_mfma_f32_16x16x32_bf16 v[188:191], a[32:35], a[12:15], v[188:191]
v_mfma_f32_16x16x32_bf16 v[224:227], a[12:15], a[36:39], v[224:227]
buffer_load_dwordx4 v42, s[12:15], s25 offen offset:2048 lds
v_mfma_f32_16x16x32_bf16 v[228:231], a[12:15], a[40:43], v[228:231]
v_mfma_f32_16x16x32_bf16 v[152:155], a[20:23], a[16:19], v[152:155]
s_add_u32 s26, s26, 393216
s_add_u32 s47, s47, 393216
s_add_u32 s25, s25, 128
v_mfma_f32_16x16x32_bf16 v[156:159], a[24:27], a[16:19], v[156:159]
v_mfma_f32_16x16x32_bf16 v[192:195], a[28:31], a[16:19], v[192:195]
buffer_load_dword v35, v4, s[96:99], 0 offen
buffer_store_dword v0, v6, s[60:63], 0 offen sc1
v_mfma_f32_16x16x32_bf16 v[196:199], a[32:35], a[16:19], v[196:199]
v_mfma_f32_16x16x32_bf16 v[232:235], a[16:19], a[36:39], v[232:235]
v_mfma_f32_16x16x32_bf16 v[236:239], a[16:19], a[40:43], v[236:239]
s_waitcnt lgkmcnt(0)
v_mfma_f32_16x16x32_bf16 v[160:163], a[172:175], a[128:131], v[160:163]
v_mfma_f32_16x16x32_bf16 v[164:167], a[192:195], a[128:131], v[164:167]
s_add_u32 m0, s91, 0
ds_read_b64_tr_b16 a[20:21], v47 offset:14336
ds_read_b64_tr_b16 a[22:23], v47 offset:14848
v_mfma_f32_16x16x32_bf16 v[200:203], a[128:131], a[196:199], v[200:203]
buffer_load_dwordx4 v36, s[12:15], s24 offen offset:0 lds
v_mfma_f32_16x16x32_bf16 v[204:207], a[128:131], a[200:203], v[204:207]
ds_read_b128 a[0:3], v46
v_mfma_f32_16x16x32_bf16 v[128:131], a[164:167], a[132:135], v[128:131]
ds_read_b64_tr_b16 a[24:25], v48 offset:14336
ds_read_b64_tr_b16 a[26:27], v48 offset:14848
v_mfma_f32_16x16x32_bf16 v[132:135], a[168:171], a[132:135], v[132:135]
v_mfma_f32_16x16x32_bf16 v[168:171], a[172:175], a[132:135], v[168:171]
ds_read_b128 a[4:7], v44 offset:0
v_mfma_f32_16x16x32_bf16 v[172:175], a[192:195], a[132:135], v[172:175]
ds_read_b64_tr_b16 a[28:29], v47 offset:16384
ds_read_b64_tr_b16 a[30:31], v47 offset:16896
v_mfma_f32_16x16x32_bf16 v[208:211], a[132:135], a[196:199], v[208:211]
buffer_load_dwordx4 v37, s[12:15], s24 offen offset:1024 lds
v_mfma_f32_16x16x32_bf16 v[212:215], a[132:135], a[200:203], v[212:215]
ds_read_b128 a[8:11], v44 offset:2048
v_mfma_f32_16x16x32_bf16 v[136:139], a[164:167], a[136:139], v[136:139]
ds_read_b64_tr_b16 a[32:33], v48 offset:16384
ds_read_b64_tr_b16 a[34:35], v48 offset:16896
v_mfma_f32_16x16x32_bf16 v[140:143], a[168:171], a[136:139], v[140:143]
v_mfma_f32_16x16x32_bf16 v[176:179], a[172:175], a[136:139], v[176:179]
ds_read_b128 a[12:15], v44 offset:4096
v_mfma_f32_16x16x32_bf16 v[180:183], a[192:195], a[136:139], v[180:183]
ds_read_b64_tr_b16 a[36:37], v49 offset:18432
ds_read_b64_tr_b16 a[38:39], v49 offset:18944
v_mfma_f32_16x16x32_bf16 v[216:219], a[136:139], a[196:199], v[216:219]
buffer_load_dwordx4 v38, s[12:15], s24 offen offset:2048 lds
v_mfma_f32_16x16x32_bf16 v[220:223], a[136:139], a[200:203], v[220:223]
ds_read_b128 a[16:19], v44 offset:6144
v_mfma_f32_16x16x32_bf16 v[144:147], a[164:167], a[140:143], v[144:147]
ds_read_b64_tr_b16 a[40:41], v50 offset:18432
ds_read_b64_tr_b16 a[42:43], v50 offset:18944
v_mfma_f32_16x16x32_bf16 v[148:151], a[168:171], a[140:143], v[148:151]
s_cmp_eq_u32 s39, 1
s_mov_b32 s28, 49152
s_cselect_b32 s28, 98304, s28
v_mfma_f32_16x16x32_bf16 v[184:187], a[172:175], a[140:143], v[184:187]
v_add_u32 v43, 49152, v43
v_add_u32 v44, 49152, v44
v_mfma_f32_16x16x32_bf16 v[188:191], a[192:195], a[140:143], v[188:191]
v_add_u32 v47, 98304, v47
v_add_u32 v48, 98304, v48
v_mfma_f32_16x16x32_bf16 v[224:227], a[140:143], a[196:199], v[224:227]
buffer_load_dwordx4 v39, s[12:15], s24 offen offset:3072 lds
v_add_u32 v49, 98304, v49
v_add_u32 v50, 98304, v50
v_mfma_f32_16x16x32_bf16 v[228:231], a[140:143], a[200:203], v[228:231]
v_add_u32 v45, s28, v45
v_add_u32 v46, s28, v46
v_mfma_f32_16x16x32_bf16 v[152:155], a[164:167], a[160:163], v[152:155]
v_mfma_f32_16x16x32_bf16 v[156:159], a[168:171], a[160:163], v[156:159]
s_add_u32 s24, s24, 128
v_mfma_f32_16x16x32_bf16 v[192:195], a[172:175], a[160:163], v[192:195]
v_mfma_f32_16x16x32_bf16 v[196:199], a[192:195], a[160:163], v[196:199]
v_mfma_f32_16x16x32_bf16 v[232:235], a[160:163], a[196:199], v[232:235]
v_mfma_f32_16x16x32_bf16 v[236:239], a[160:163], a[200:203], v[236:239]
s_waitcnt vmcnt(17) lgkmcnt(0)
s_barrier
v_mfma_f32_16x16x32_bf16 v[160:163], a[28:31], a[0:3], v[160:163]
v_mfma_f32_16x16x32_bf16 v[164:167], a[32:35], a[0:3], v[164:167]
s_add_u32 m0, s48, 32768
ds_read_b64_tr_b16 a[164:165], v47 offset:0
ds_read_b64_tr_b16 a[166:167], v47 offset:512
v_mfma_f32_16x16x32_bf16 v[200:203], a[0:3], a[36:39], v[200:203]
buffer_load_dwordx4 v40, s[16:19], s26 offen offset:0 lds
v_mfma_f32_16x16x32_bf16 v[204:207], a[0:3], a[40:43], v[204:207]
ds_read_b128 a[128:131], v45
v_mfma_f32_16x16x32_bf16 v[128:131], a[20:23], a[4:7], v[128:131]
ds_read_b64_tr_b16 a[168:169], v48 offset:0
ds_read_b64_tr_b16 a[170:171], v48 offset:512
v_mfma_f32_16x16x32_bf16 v[132:135], a[24:27], a[4:7], v[132:135]
buffer_load_dwordx4 v41, s[16:19], s26 offen offset:1024 lds
v_mfma_f32_16x16x32_bf16 v[168:171], a[28:31], a[4:7], v[168:171]
ds_read_b128 a[132:135], v43 offset:0
v_mfma_f32_16x16x32_bf16 v[172:175], a[32:35], a[4:7], v[172:175]
ds_read_b64_tr_b16 a[172:173], v47 offset:2048
ds_read_b64_tr_b16 a[174:175], v47 offset:2560
v_mfma_f32_16x16x32_bf16 v[208:211], a[4:7], a[36:39], v[208:211]
buffer_load_dwordx4 v40, s[16:19], s26 offen offset:2048 lds
v_mfma_f32_16x16x32_bf16 v[212:215], a[4:7], a[40:43], v[212:215]
ds_read_b128 a[136:139], v43 offset:2048
v_mfma_f32_16x16x32_bf16 v[136:139], a[20:23], a[8:11], v[136:139]
ds_read_b64_tr_b16 a[192:193], v48 offset:2048
ds_read_b64_tr_b16 a[194:195], v48 offset:2560
v_mfma_f32_16x16x32_bf16 v[140:143], a[24:27], a[8:11], v[140:143]
buffer_load_dwordx4 v41, s[16:19], s26 offen offset:3072 lds
v_mfma_f32_16x16x32_bf16 v[176:179], a[28:31], a[8:11], v[176:179]
ds_read_b128 a[140:143], v43 offset:4096
v_mfma_f32_16x16x32_bf16 v[180:183], a[32:35], a[8:11], v[180:183]
s_add_u32 m0, s48, 36864
ds_read_b64_tr_b16 a[196:197], v49 offset:4096
ds_read_b64_tr_b16 a[198:199], v49 offset:4608
v_mfma_f32_16x16x32_bf16 v[216:219], a[8:11], a[36:39], v[216:219]
buffer_load_dwordx4 v40, s[16:19], s47 offen offset:0 lds
v_mfma_f32_16x16x32_bf16 v[220:223], a[8:11], a[40:43], v[220:223]
ds_read_b128 a[160:163], v43 offset:6144
v_mfma_f32_16x16x32_bf16 v[144:147], a[20:23], a[12:15], v[144:147]
ds_read_b64_tr_b16 a[200:201], v50 offset:4096
ds_read_b64_tr_b16 a[202:203], v50 offset:4608
v_mfma_f32_16x16x32_bf16 v[148:151], a[24:27], a[12:15], v[148:151]
buffer_load_dwordx4 v41, s[16:19], s47 offen offset:1024 lds
v_mfma_f32_16x16x32_bf16 v[184:187], a[28:31], a[12:15], v[184:187]
v_mfma_f32_16x16x32_bf16 v[188:191], a[32:35], a[12:15], v[188:191]
v_mfma_f32_16x16x32_bf16 v[224:227], a[12:15], a[36:39], v[224:227]
buffer_load_dwordx4 v42, s[12:15], s25 offen offset:2048 lds
v_mfma_f32_16x16x32_bf16 v[228:231], a[12:15], a[40:43], v[228:231]
v_mfma_f32_16x16x32_bf16 v[152:155], a[20:23], a[16:19], v[152:155]
s_add_u32 s26, s26, 393216
s_add_u32 s47, s47, 393216
s_add_u32 s25, s25, 128
v_mfma_f32_16x16x32_bf16 v[156:159], a[24:27], a[16:19], v[156:159]
v_mfma_f32_16x16x32_bf16 v[192:195], a[28:31], a[16:19], v[192:195]
v_mfma_f32_16x16x32_bf16 v[196:199], a[32:35], a[16:19], v[196:199]
v_mfma_f32_16x16x32_bf16 v[232:235], a[16:19], a[36:39], v[232:235]
v_mfma_f32_16x16x32_bf16 v[236:239], a[16:19], a[40:43], v[236:239]
s_waitcnt lgkmcnt(0)
v_mfma_f32_16x16x32_bf16 v[160:163], a[172:175], a[128:131], v[160:163]
v_and_b32 v28, 15, v1
v_lshrrev_b32 v29, 4, v1
v_mfma_f32_16x16x32_bf16 v[164:167], a[192:195], a[128:131], v[164:167]
s_add_u32 m0, s91, 16384
ds_read_b64_tr_b16 a[20:21], v47 offset:14336
ds_read_b64_tr_b16 a[22:23], v47 offset:14848
v_mfma_f32_16x16x32_bf16 v[200:203], a[128:131], a[196:199], v[200:203]
buffer_load_dwordx4 v36, s[12:15], s24 offen offset:0 lds
v_lshlrev_b32 v29, 2, v29
v_mfma_f32_16x16x32_bf16 v[204:207], a[128:131], a[200:203], v[204:207]
ds_read_b128 a[0:3], v46
v_sub_u32 v28, v28, v29
v_mfma_f32_16x16x32_bf16 v[128:131], a[164:167], a[132:135], v[128:131]
ds_read_b64_tr_b16 a[24:25], v48 offset:14336
ds_read_b64_tr_b16 a[26:27], v48 offset:14848
v_mfma_f32_16x16x32_bf16 v[132:135], a[168:171], a[132:135], v[132:135]
v_mov_b32 v30, 0
v_add_u32 v29, 16, v28
v_mfma_f32_16x16x32_bf16 v[168:171], a[172:175], a[132:135], v[168:171]
ds_read_b128 a[4:7], v44 offset:0
v_and_b32 v29, 0xfffffff1, v29
v_mfma_f32_16x16x32_bf16 v[172:175], a[192:195], a[132:135], v[172:175]
ds_read_b64_tr_b16 a[28:29], v47 offset:16384
ds_read_b64_tr_b16 a[30:31], v47 offset:16896
v_mfma_f32_16x16x32_bf16 v[208:211], a[132:135], a[196:199], v[208:211]
buffer_load_dwordx4 v37, s[12:15], s24 offen offset:1024 lds
v_cmp_eq_u32 vcc, 0, v29
v_mfma_f32_16x16x32_bf16 v[212:215], a[132:135], a[200:203], v[212:215]
ds_read_b128 a[8:11], v44 offset:2048
v_nop
v_mfma_f32_16x16x32_bf16 v[136:139], a[164:167], a[136:139], v[136:139]
ds_read_b64_tr_b16 a[32:33], v48 offset:16384
ds_read_b64_tr_b16 a[34:35], v48 offset:16896
v_mfma_f32_16x16x32_bf16 v[140:143], a[168:171], a[136:139], v[140:143]
v_nop
v_cndmask_b32 v16, v34, v30, vcc
v_mfma_f32_16x16x32_bf16 v[176:179], a[172:175], a[136:139], v[176:179]
ds_read_b128 a[12:15], v44 offset:4096
v_add_u32 v29, 15, v28
v_mfma_f32_16x16x32_bf16 v[180:183], a[192:195], a[136:139], v[180:183]
ds_read_b64_tr_b16 a[36:37], v49 offset:18432
ds_read_b64_tr_b16 a[38:39], v49 offset:18944
v_mfma_f32_16x16x32_bf16 v[216:219], a[136:139], a[196:199], v[216:219]
buffer_load_dwordx4 v38, s[12:15], s24 offen offset:2048 lds
v_and_b32 v29, 0xfffffff1, v29
v_mfma_f32_16x16x32_bf16 v[220:223], a[136:139], a[200:203], v[220:223]
ds_read_b128 a[16:19], v44 offset:6144
v_cmp_eq_u32 vcc, 0, v29
v_mfma_f32_16x16x32_bf16 v[144:147], a[164:167], a[140:143], v[144:147]
ds_read_b64_tr_b16 a[40:41], v50 offset:18432
ds_read_b64_tr_b16 a[42:43], v50 offset:18944
v_mfma_f32_16x16x32_bf16 v[148:151], a[168:171], a[140:143], v[148:151]
s_cmp_eq_u32 s39, 1
s_mov_b32 s28, 16384
s_cselect_b32 s28, 4294934528, s28
v_mfma_f32_16x16x32_bf16 v[184:187], a[172:175], a[140:143], v[184:187]
v_add_u32 v43, 16384, v43
v_add_u32 v44, 16384, v44
v_mfma_f32_16x16x32_bf16 v[188:191], a[192:195], a[140:143], v[188:191]
v_add_u32 v47, 4294934528, v47
v_add_u32 v48, 4294934528, v48
v_mfma_f32_16x16x32_bf16 v[224:227], a[140:143], a[196:199], v[224:227]
buffer_load_dwordx4 v39, s[12:15], s24 offen offset:3072 lds
v_add_u32 v49, 4294934528, v49
v_add_u32 v50, 4294934528, v50
v_mfma_f32_16x16x32_bf16 v[228:231], a[140:143], a[200:203], v[228:231]
v_add_u32 v45, s28, v45
v_add_u32 v46, s28, v46
v_mfma_f32_16x16x32_bf16 v[152:155], a[164:167], a[160:163], v[152:155]
v_nop
v_nop
v_mfma_f32_16x16x32_bf16 v[156:159], a[168:171], a[160:163], v[156:159]
s_add_u32 s24, s24, 128
v_cndmask_b32 v17, v34, v30, vcc
v_add_u32 v29, 14, v28
v_mfma_f32_16x16x32_bf16 v[192:195], a[172:175], a[160:163], v[192:195]
v_and_b32 v29, 0xfffffff1, v29
v_cmp_eq_u32 vcc, 0, v29
v_mfma_f32_16x16x32_bf16 v[196:199], a[192:195], a[160:163], v[196:199]
v_nop
v_nop
v_mfma_f32_16x16x32_bf16 v[232:235], a[160:163], a[196:199], v[232:235]
buffer_load_dword v250, v249, s[60:63], s90 offen sc1
v_cndmask_b32 v18, v34, v30, vcc
v_add_u32 v29, 13, v28
v_mfma_f32_16x16x32_bf16 v[236:239], a[160:163], a[200:203], v[236:239]
s_waitcnt vmcnt(18) lgkmcnt(0)
s_barrier
v_and_b32 v29, 0xfffffff1, v29
v_mfma_f32_16x16x32_bf16 v[160:163], a[28:31], a[0:3], v[160:163]
v_cmp_eq_u32 vcc, 0, v29
v_nop
v_mfma_f32_16x16x32_bf16 v[164:167], a[32:35], a[0:3], v[164:167]
s_add_u32 m0, s48, 131072
ds_read_b64_tr_b16 a[164:165], v47 offset:0
ds_read_b64_tr_b16 a[166:167], v47 offset:512
v_mfma_f32_16x16x32_bf16 v[200:203], a[0:3], a[36:39], v[200:203]
buffer_load_dwordx4 v40, s[16:19], s26 offen offset:0 lds
v_nop
v_mfma_f32_16x16x32_bf16 v[204:207], a[0:3], a[40:43], v[204:207]
ds_read_b128 a[128:131], v45
v_cndmask_b32 v19, v34, v30, vcc
v_mfma_f32_16x16x32_bf16 v[128:131], a[20:23], a[4:7], v[128:131]
ds_read_b64_tr_b16 a[168:169], v48 offset:0
ds_read_b64_tr_b16 a[170:171], v48 offset:512
v_mfma_f32_16x16x32_bf16 v[132:135], a[24:27], a[4:7], v[132:135]
buffer_load_dwordx4 v41, s[16:19], s26 offen offset:1024 lds
v_add_u32 v29, 0, v28
v_mfma_f32_16x16x32_bf16 v[168:171], a[28:31], a[4:7], v[168:171]
ds_read_b128 a[132:135], v43 offset:0
v_and_b32 v29, 0xfffffff1, v29
v_mfma_f32_16x16x32_bf16 v[172:175], a[32:35], a[4:7], v[172:175]
ds_read_b64_tr_b16 a[172:173], v47 offset:2048
ds_read_b64_tr_b16 a[174:175], v47 offset:2560
v_mfma_f32_16x16x32_bf16 v[208:211], a[4:7], a[36:39], v[208:211]
buffer_load_dwordx4 v40, s[16:19], s26 offen offset:2048 lds
v_cmp_eq_u32 vcc, 0, v29
v_mfma_f32_16x16x32_bf16 v[212:215], a[4:7], a[40:43], v[212:215]
ds_read_b128 a[136:139], v43 offset:2048
v_nop
v_mfma_f32_16x16x32_bf16 v[136:139], a[20:23], a[8:11], v[136:139]
ds_read_b64_tr_b16 a[192:193], v48 offset:2048
ds_read_b64_tr_b16 a[194:195], v48 offset:2560
v_mfma_f32_16x16x32_bf16 v[140:143], a[24:27], a[8:11], v[140:143]
buffer_load_dwordx4 v41, s[16:19], s26 offen offset:3072 lds
v_nop
v_mfma_f32_16x16x32_bf16 v[176:179], a[28:31], a[8:11], v[176:179]
ds_read_b128 a[140:143], v43 offset:4096
v_cndmask_b32 v20, v34, v30, vcc
v_mfma_f32_16x16x32_bf16 v[180:183], a[32:35], a[8:11], v[180:183]
s_add_u32 m0, s48, 135168
ds_read_b64_tr_b16 a[196:197], v49 offset:4096
ds_read_b64_tr_b16 a[198:199], v49 offset:4608
v_mfma_f32_16x16x32_bf16 v[216:219], a[8:11], a[36:39], v[216:219]
buffer_load_dwordx4 v40, s[16:19], s47 offen offset:0 lds
v_add_u32 v29, -1, v28
v_mfma_f32_16x16x32_bf16 v[220:223], a[8:11], a[40:43], v[220:223]
ds_read_b128 a[160:163], v43 offset:6144
v_and_b32 v29, 0xfffffff1, v29
v_mfma_f32_16x16x32_bf16 v[144:147], a[20:23], a[12:15], v[144:147]
ds_read_b64_tr_b16 a[200:201], v50 offset:4096
ds_read_b64_tr_b16 a[202:203], v50 offset:4608
v_mfma_f32_16x16x32_bf16 v[148:151], a[24:27], a[12:15], v[148:151]
buffer_load_dwordx4 v41, s[16:19], s47 offen offset:1024 lds
v_cmp_eq_u32 vcc, 0, v29
v_mfma_f32_16x16x32_bf16 v[184:187], a[28:31], a[12:15], v[184:187]
v_nop
v_nop
v_mfma_f32_16x16x32_bf16 v[188:191], a[32:35], a[12:15], v[188:191]
v_cndmask_b32 v21, v34, v30, vcc
v_add_u32 v29, -2, v28
v_mfma_f32_16x16x32_bf16 v[224:227], a[12:15], a[36:39], v[224:227]
buffer_load_dwordx4 v42, s[12:15], s25 offen offset:2048 lds
v_and_b32 v29, 0xfffffff1, v29
v_mfma_f32_16x16x32_bf16 v[228:231], a[12:15], a[40:43], v[228:231]
v_cmp_eq_u32 vcc, 0, v29
v_nop
v_mfma_f32_16x16x32_bf16 v[152:155], a[20:23], a[16:19], v[152:155]
s_add_u32 s26, s26, 393216
s_add_u32 s47, s47, 393216
s_add_u32 s25, s25, 128
v_mfma_f32_16x16x32_bf16 v[156:159], a[24:27], a[16:19], v[156:159]
v_nop
v_cndmask_b32 v22, v34, v30, vcc
v_mfma_f32_16x16x32_bf16 v[192:195], a[28:31], a[16:19], v[192:195]
v_add_u32 v29, -3, v28
v_and_b32 v29, 0xfffffff1, v29
v_mfma_f32_16x16x32_bf16 v[196:199], a[32:35], a[16:19], v[196:199]
v_cmp_eq_u32 vcc, 0, v29
v_nop
v_mfma_f32_16x16x32_bf16 v[232:235], a[16:19], a[36:39], v[232:235]
v_nop
v_cndmask_b32 v23, v34, v30, vcc
v_mfma_f32_16x16x32_bf16 v[236:239], a[16:19], a[40:43], v[236:239]
s_waitcnt lgkmcnt(0)
s_lshr_b32 s29, s3, 1
s_or_b32 s29, s29, s36
v_mfma_f32_16x16x32_bf16 v[160:163], a[172:175], a[128:131], v[160:163]
s_cmp_eq_u32 s29, 0
s_cselect_b64 s[32:33], -1, 0
v_mfma_f32_16x16x32_bf16 v[164:167], a[192:195], a[128:131], v[164:167]
ds_read_b64_tr_b16 a[20:21], v47 offset:14336
ds_read_b64_tr_b16 a[22:23], v47 offset:14848
v_mfma_f32_16x16x32_bf16 v[200:203], a[128:131], a[196:199], v[200:203]
v_cndmask_b32 v24, v16, v34, s[32:33]
v_cndmask_b32 v25, v17, v34, s[32:33]
v_mfma_f32_16x16x32_bf16 v[204:207], a[128:131], a[200:203], v[204:207]
ds_read_b128 a[0:3], v46
v_cndmask_b32 v26, v18, v34, s[32:33]
v_mfma_f32_16x16x32_bf16 v[128:131], a[164:167], a[132:135], v[128:131]
ds_read_b64_tr_b16 a[24:25], v48 offset:14336
ds_read_b64_tr_b16 a[26:27], v48 offset:14848
v_mfma_f32_16x16x32_bf16 v[132:135], a[168:171], a[132:135], v[132:135]
v_cndmask_b32 v27, v19, v34, s[32:33]
v_mfma_f32_16x16x32_bf16 v[168:171], a[172:175], a[132:135], v[168:171]
ds_read_b128 a[4:7], v44 offset:0
v_mfma_f32_16x16x32_bf16 v[172:175], a[192:195], a[132:135], v[172:175]
ds_read_b64_tr_b16 a[28:29], v47 offset:16384
ds_read_b64_tr_b16 a[30:31], v47 offset:16896
v_mfma_f32_16x16x32_bf16 v[208:211], a[132:135], a[196:199], v[208:211]
v_mfma_f32_16x16x32_bf16 v[212:215], a[132:135], a[200:203], v[212:215]
ds_read_b128 a[8:11], v44 offset:2048
v_mfma_f32_16x16x32_bf16 v[136:139], a[164:167], a[136:139], v[136:139]
ds_read_b64_tr_b16 a[32:33], v48 offset:16384
ds_read_b64_tr_b16 a[34:35], v48 offset:16896
v_mfma_f32_16x16x32_bf16 v[140:143], a[168:171], a[136:139], v[140:143]
v_mfma_f32_16x16x32_bf16 v[176:179], a[172:175], a[136:139], v[176:179]
ds_read_b128 a[12:15], v44 offset:4096
v_mfma_f32_16x16x32_bf16 v[180:183], a[192:195], a[136:139], v[180:183]
ds_read_b64_tr_b16 a[36:37], v49 offset:18432
ds_read_b64_tr_b16 a[38:39], v49 offset:18944
v_mfma_f32_16x16x32_bf16 v[216:219], a[136:139], a[196:199], v[216:219]
v_mfma_f32_16x16x32_bf16 v[220:223], a[136:139], a[200:203], v[220:223]
ds_read_b128 a[16:19], v44 offset:6144
v_mfma_f32_16x16x32_bf16 v[144:147], a[164:167], a[140:143], v[144:147]
ds_read_b64_tr_b16 a[40:41], v50 offset:18432
ds_read_b64_tr_b16 a[42:43], v50 offset:18944
v_mfma_f32_16x16x32_bf16 v[148:151], a[168:171], a[140:143], v[148:151]
s_cmp_eq_u32 s39, 1
s_mov_b32 s28, 4294885376
s_cselect_b32 s28, 4294901760, s28
v_mfma_f32_16x16x32_bf16 v[184:187], a[172:175], a[140:143], v[184:187]
v_add_u32 v43, 4294885376, v43
v_add_u32 v44, 4294885376, v44
v_mfma_f32_16x16x32_bf16 v[188:191], a[192:195], a[140:143], v[188:191]
v_add_u32 v47, 4294901760, v47
v_add_u32 v48, 4294901760, v48
v_mfma_f32_16x16x32_bf16 v[224:227], a[140:143], a[196:199], v[224:227]
s_waitcnt vmcnt(7)
v_add_u32 v49, 4294901760, v49
v_add_u32 v50, 4294901760, v50
v_and_b32 v250, 7, v250
v_mfma_f32_16x16x32_bf16 v[228:231], a[140:143], a[200:203], v[228:231]
v_add_u32 v45, s28, v45
v_add_u32 v46, s28, v46
v_mfma_f32_16x16x32_bf16 v[152:155], a[164:167], a[160:163], v[152:155]
v_cmp_gt_u32 vcc, 3, v250
v_mfma_f32_16x16x32_bf16 v[156:159], a[168:171], a[160:163], v[156:159]
v_mfma_f32_16x16x32_bf16 v[192:195], a[172:175], a[160:163], v[192:195]
s_cmp_lg_u64 vcc, 0
s_cbranch_scc1 .Lspin3
.Lspin3_ret:
v_mfma_f32_16x16x32_bf16 v[196:199], a[192:195], a[160:163], v[196:199]
v_mfma_f32_16x16x32_bf16 v[232:235], a[160:163], a[196:199], v[232:235]
s_lshl_b32 s54, s51, 19
s_and_b32 s56, s3, 1
s_lshl_b32 s56, s56, 18
v_mfma_f32_16x16x32_bf16 v[236:239], a[160:163], a[200:203], v[236:239]
s_waitcnt vmcnt(12) lgkmcnt(0)
s_barrier
s_add_u32 s54, s54, s56
v_mfma_f32_16x16x32_bf16 v[160:163], a[28:31], a[0:3], v[160:163]
s_add_u32 s54, s54, s52
s_lshr_b32 s56, s3, 1
s_lshl_b32 s56, s56, 7
v_mfma_f32_16x16x32_bf16 v[164:167], a[32:35], a[0:3], v[164:167]
s_add_u32 m0, s91, 114688
ds_read_b64_tr_b16 a[164:165], v47 offset:0
ds_read_b64_tr_b16 a[166:167], v47 offset:512
v_mfma_f32_16x16x32_bf16 v[200:203], a[0:3], a[36:39], v[200:203]
s_add_u32 s54, s54, s56
buffer_load_dwordx4 v240, s[8:11], s45 offen offset:0 lds
v_mfma_f32_16x16x32_bf16 v[204:207], a[0:3], a[40:43], v[204:207]
s_xor_b32 s56, s55, 1
ds_read_b128 a[128:131], v45
v_mfma_f32_16x16x32_bf16 v[128:131], a[20:23], a[4:7], v[128:131]
ds_read_b64_tr_b16 a[168:169], v48 offset:0
ds_read_b64_tr_b16 a[170:171], v48 offset:512
v_mfma_f32_16x16x32_bf16 v[132:135], a[24:27], a[4:7], v[132:135]
s_lshl_b32 s56, s56, 11
buffer_load_dwordx4 v241, s[8:11], s45 offen offset:1024 lds
v_mfma_f32_16x16x32_bf16 v[168:171], a[28:31], a[4:7], v[168:171]
s_add_u32 s54, s54, s56
ds_read_b128 a[132:135], v43 offset:0
v_mfma_f32_16x16x32_bf16 v[172:175], a[32:35], a[4:7], v[172:175]
ds_read_b64_tr_b16 a[172:173], v47 offset:2048
ds_read_b64_tr_b16 a[174:175], v47 offset:2560
v_mfma_f32_16x16x32_bf16 v[208:211], a[4:7], a[36:39], v[208:211]
s_sub_u32 s18, 0x800000, s54
buffer_load_dwordx4 v242, s[8:11], s45 offen offset:2048 lds
v_mfma_f32_16x16x32_bf16 v[212:215], a[4:7], a[40:43], v[212:215]
ds_read_b128 a[136:139], v43 offset:2048
v_mfma_f32_16x16x32_bf16 v[136:139], a[20:23], a[8:11], v[136:139]
ds_read_b64_tr_b16 a[192:193], v48 offset:2048
ds_read_b64_tr_b16 a[194:195], v48 offset:2560
v_mfma_f32_16x16x32_bf16 v[140:143], a[24:27], a[8:11], v[140:143]
buffer_load_dwordx4 v243, s[8:11], s45 offen offset:3072 lds
v_mfma_f32_16x16x32_bf16 v[176:179], a[28:31], a[8:11], v[176:179]
s_add_u32 s45, s45, 131072
ds_read_b128 a[140:143], v43 offset:4096
v_mfma_f32_16x16x32_bf16 v[180:183], a[32:35], a[8:11], v[180:183]
s_add_u32 m0, s91, 98304
ds_read_b64_tr_b16 a[196:197], v49 offset:4096
ds_read_b64_tr_b16 a[198:199], v49 offset:4608
v_mfma_f32_16x16x32_bf16 v[216:219], a[8:11], a[36:39], v[216:219]
buffer_load_dwordx4 v7, s[4:7], s44 offen offset:0 sc1 lds
v_mfma_f32_16x16x32_bf16 v[220:223], a[8:11], a[40:43], v[220:223]
ds_read_b128 a[160:163], v43 offset:6144
v_mfma_f32_16x16x32_bf16 v[144:147], a[20:23], a[12:15], v[144:147]
ds_read_b64_tr_b16 a[200:201], v50 offset:4096
ds_read_b64_tr_b16 a[202:203], v50 offset:4608
v_mfma_f32_16x16x32_bf16 v[148:151], a[24:27], a[12:15], v[148:151]
buffer_load_dwordx4 v7, s[4:7], s44 offen offset:1024 sc1 lds
v_mfma_f32_16x16x32_bf16 v[184:187], a[28:31], a[12:15], v[184:187]
s_add_u32 s16, s82, s54
s_addc_u32 s17, s83, 0
s_and_b32 s17, s17, 0xffff
v_mfma_f32_16x16x32_bf16 v[188:191], a[32:35], a[12:15], v[188:191]
s_lshr_b32 s54, s2, 1
s_lshl_b32 s54, s54, 16
s_lshl_b32 s56, s3, 14
v_mfma_f32_16x16x32_bf16 v[224:227], a[12:15], a[36:39], v[224:227]
s_add_u32 s54, s54, s56
buffer_load_dwordx4 v7, s[4:7], s44 offen offset:2048 sc1 lds
v_mfma_f32_16x16x32_bf16 v[228:231], a[12:15], a[40:43], v[228:231]
s_add_u32 s54, s54, 54525952
s_add_u32 s12, s84, s54
s_addc_u32 s13, s85, 0
v_mfma_f32_16x16x32_bf16 v[152:155], a[20:23], a[16:19], v[152:155]
s_and_b32 s13, s13, 0xffff
s_mov_b32 s14, 16384
s_mov_b32 s56, 131072
v_mfma_f32_16x16x32_bf16 v[156:159], a[24:27], a[16:19], v[156:159]
s_mov_b32 s57, -32768
buffer_load_dwordx4 v7, s[4:7], s44 offen offset:3072 sc1 lds
v_mfma_f32_16x16x32_bf16 v[192:195], a[28:31], a[16:19], v[192:195]
s_add_u32 s44, s44, 4096
s_mov_b32 s46, 98304
s_lshr_b32 s30, s51, 3
v_mfma_f32_16x16x32_bf16 v[196:199], a[32:35], a[16:19], v[196:199]
s_and_b32 s31, s51, 7
s_lshl_b32 s31, s31, 3
s_or_b32 s30, s30, s31
v_mfma_f32_16x16x32_bf16 v[232:235], a[16:19], a[36:39], v[232:235]
s_add_u32 m0, s91, 81920
s_xor_b32 s31, s2, 1
v_mfma_f32_16x16x32_bf16 v[236:239], a[16:19], a[40:43], v[236:239]
s_waitcnt lgkmcnt(0)
buffer_load_dwordx4 v240, s[8:11], s45 offen offset:0 lds
v_mfma_f32_16x16x32_bf16 v[160:163], a[172:175], a[128:131], v[160:163]
s_lshl_b32 s34, s36, 18
s_lshr_b32 s35, s3, 1
s_lshl_b32 s35, s35, 17
v_mfma_f32_16x16x32_bf16 v[164:167], a[192:195], a[128:131], v[164:167]
ds_read_b64_tr_b16 a[20:21], v47 offset:14336
ds_read_b64_tr_b16 a[22:23], v47 offset:14848
v_mfma_f32_16x16x32_bf16 v[200:203], a[128:131], a[196:199], v[200:203]
s_add_u32 s34, s34, s35
s_lshl_b32 s35, s37, 12
s_add_u32 s34, s34, s35
buffer_load_dwordx4 v241, s[8:11], s45 offen offset:1024 lds
v_mfma_f32_16x16x32_bf16 v[204:207], a[128:131], a[200:203], v[204:207]
s_and_b32 s35, s3, 1
ds_read_b128 a[0:3], v46
v_mfma_f32_16x16x32_bf16 v[128:131], a[164:167], a[132:135], v[128:131]
ds_read_b64_tr_b16 a[24:25], v48 offset:14336
ds_read_b64_tr_b16 a[26:27], v48 offset:14848
v_mfma_f32_16x16x32_bf16 v[132:135], a[168:171], a[132:135], v[132:135]
s_lshl_b32 s35, s35, 11
s_add_u32 s34, s34, s35
s_add_u32 s34, s34, 50331648
buffer_load_dwordx4 v242, s[8:11], s45 offen offset:2048 lds
v_mfma_f32_16x16x32_bf16 v[168:171], a[172:175], a[132:135], v[168:171]
ds_read_b128 a[4:7], v44 offset:0
v_mfma_f32_16x16x32_bf16 v[172:175], a[192:195], a[132:135], v[172:175]
ds_read_b64_tr_b16 a[28:29], v47 offset:16384
ds_read_b64_tr_b16 a[30:31], v47 offset:16896
v_mfma_f32_16x16x32_bf16 v[208:211], a[132:135], a[196:199], v[208:211]
s_add_u32 s20, s84, s34
s_addc_u32 s21, s85, 0
s_and_b32 s21, s21, 0xffff
buffer_load_dwordx4 v243, s[8:11], s45 offen offset:3072 lds
v_mfma_f32_16x16x32_bf16 v[212:215], a[132:135], a[200:203], v[212:215]
s_mov_b32 s22, 131072
s_add_u32 s45, s45, 131072
ds_read_b128 a[8:11], v44 offset:2048
v_mfma_f32_16x16x32_bf16 v[136:139], a[164:167], a[136:139], v[136:139]
s_add_u32 m0, s91, 65536
ds_read_b64_tr_b16 a[32:33], v48 offset:16384
ds_read_b64_tr_b16 a[34:35], v48 offset:16896
v_mfma_f32_16x16x32_bf16 v[140:143], a[168:171], a[136:139], v[140:143]
s_mov_b32 s40, 0
s_mov_b32 s41, 65536
buffer_load_dwordx4 v7, s[4:7], s44 offen offset:0 sc1 lds
v_mfma_f32_16x16x32_bf16 v[176:179], a[172:175], a[136:139], v[176:179]
ds_read_b128 a[12:15], v44 offset:4096
v_mfma_f32_16x16x32_bf16 v[180:183], a[192:195], a[136:139], v[180:183]
ds_read_b64_tr_b16 a[36:37], v49 offset:18432
ds_read_b64_tr_b16 a[38:39], v49 offset:18944
v_mfma_f32_16x16x32_bf16 v[216:219], a[136:139], a[196:199], v[216:219]
buffer_load_dwordx4 v7, s[4:7], s44 offen offset:1024 sc1 lds
v_mfma_f32_16x16x32_bf16 v[220:223], a[136:139], a[200:203], v[220:223]
ds_read_b128 a[16:19], v44 offset:6144
v_mfma_f32_16x16x32_bf16 v[144:147], a[164:167], a[140:143], v[144:147]
ds_read_b64_tr_b16 a[40:41], v50 offset:18432
ds_read_b64_tr_b16 a[42:43], v50 offset:18944
v_mfma_f32_16x16x32_bf16 v[148:151], a[168:171], a[140:143], v[148:151]
s_cmp_eq_u32 s39, 1
s_mov_b32 s28, 16384
s_cselect_b32 s28, 98304, s28
buffer_load_dwordx4 v7, s[4:7], s44 offen offset:2048 sc1 lds
v_mfma_f32_16x16x32_bf16 v[184:187], a[172:175], a[140:143], v[184:187]
v_add_u32 v43, 16384, v43
v_add_u32 v44, 16384, v44
v_mfma_f32_16x16x32_bf16 v[188:191], a[192:195], a[140:143], v[188:191]
v_add_u32 v47, 98304, v47
v_add_u32 v48, 98304, v48
v_mfma_f32_16x16x32_bf16 v[224:227], a[140:143], a[196:199], v[224:227]
buffer_load_dwordx4 v7, s[4:7], s44 offen offset:3072 sc1 lds
v_add_u32 v49, 98304, v49
v_add_u32 v50, 98304, v50
v_mfma_f32_16x16x32_bf16 v[228:231], a[140:143], a[200:203], v[228:231]
s_add_u32 s44, s44, 4096
v_add_u32 v45, s28, v45
v_add_u32 v46, s28, v46
v_mfma_f32_16x16x32_bf16 v[152:155], a[164:167], a[160:163], v[152:155]
s_cmp_eq_u32 s55, 0
s_cselect_b64 s[58:59], -1, 0
s_cselect_b32 s50, 28, 6
v_mfma_f32_16x16x32_bf16 v[156:159], a[168:171], a[160:163], v[156:159]
v_and_b32 v252, 7, v1
v_bfe_u32 v253, v1, 3, 1
v_mfma_f32_16x16x32_bf16 v[192:195], a[172:175], a[160:163], v[192:195]
v_bfe_u32 v254, v1, 4, 1
v_lshrrev_b32 v255, 5, v1
v_mfma_f32_16x16x32_bf16 v[196:199], a[192:195], a[160:163], v[196:199]
v_lshlrev_b32 v252, 12, v252
v_lshl_add_u32 v252, v253, 6, v252
v_mfma_f32_16x16x32_bf16 v[232:235], a[160:163], a[196:199], v[232:235]
.Lpad17:
s_cbranch_execz .Ltch8
v_lshl_add_u32 v252, v254, 5, v252
v_lshl_add_u32 v251, v255, 4, v252
v_mfma_f32_16x16x32_bf16 v[236:239], a[160:163], a[200:203], v[236:239]
s_waitcnt vmcnt(16) lgkmcnt(0)
s_barrier
v_bfe_u32 v252, v1, 2, 2
v_mfma_f32_16x16x32_bf16 v[160:163], a[28:31], a[0:3], v[160:163]
v_and_b32 v253, 3, v1
v_lshlrev_b32 v252, 1, v252
v_mfma_f32_16x16x32_bf16 v[164:167], a[32:35], a[0:3], v[164:167]
s_add_u32 m0, s91, 49152
ds_read_b64_tr_b16 a[164:165], v47 offset:0
ds_read_b64_tr_b16 a[166:167], v47 offset:512
v_mfma_f32_16x16x32_bf16 v[200:203], a[0:3], a[36:39], v[200:203]
buffer_load_dwordx4 v240, s[8:11], s45 offen offset:0 lds
v_lshl_or_b32 v252, v253, 6, v252
v_mfma_f32_16x16x32_bf16 v[204:207], a[0:3], a[40:43], v[204:207]
ds_read_b128 a[128:131], v45
v_or_b32 v252, s30, v252
v_mfma_f32_16x16x32_bf16 v[128:131], a[20:23], a[4:7], v[128:131]
ds_read_b64_tr_b16 a[168:169], v48 offset:0
ds_read_b64_tr_b16 a[170:171], v48 offset:512
v_mfma_f32_16x16x32_bf16 v[132:135], a[24:27], a[4:7], v[132:135]
v_mov_b32 v253, s31
v_cndmask_b32 v252, v252, v253, s[58:59]
v_mfma_f32_16x16x32_bf16 v[168:171], a[28:31], a[4:7], v[168:171]
ds_read_b128 a[132:135], v43 offset:0
v_lshlrev_b32 v249, 5, v252
v_mfma_f32_16x16x32_bf16 v[172:175], a[32:35], a[4:7], v[172:175]
buffer_load_dwordx4 v241, s[8:11], s45 offen offset:1024 lds
ds_read_b64_tr_b16 a[172:173], v47 offset:2048
ds_read_b64_tr_b16 a[174:175], v47 offset:2560
v_mfma_f32_16x16x32_bf16 v[208:211], a[4:7], a[36:39], v[208:211]
v_mfma_f32_16x16x32_bf16 v[212:215], a[4:7], a[40:43], v[212:215]
ds_read_b128 a[136:139], v43 offset:2048
v_mfma_f32_16x16x32_bf16 v[136:139], a[20:23], a[8:11], v[136:139]
ds_read_b64_tr_b16 a[192:193], v48 offset:2048
ds_read_b64_tr_b16 a[194:195], v48 offset:2560
v_mfma_f32_16x16x32_bf16 v[140:143], a[24:27], a[8:11], v[140:143]
v_mfma_f32_16x16x32_bf16 v[176:179], a[28:31], a[8:11], v[176:179]
buffer_load_dwordx4 v242, s[8:11], s45 offen offset:2048 lds
ds_read_b128 a[140:143], v43 offset:4096
v_mfma_f32_16x16x32_bf16 v[180:183], a[32:35], a[8:11], v[180:183]
ds_read_b64_tr_b16 a[196:197], v49 offset:4096
ds_read_b64_tr_b16 a[198:199], v49 offset:4608
v_mfma_f32_16x16x32_bf16 v[216:219], a[8:11], a[36:39], v[216:219]
v_mfma_f32_16x16x32_bf16 v[220:223], a[8:11], a[40:43], v[220:223]
ds_read_b128 a[160:163], v43 offset:6144
v_mfma_f32_16x16x32_bf16 v[144:147], a[20:23], a[12:15], v[144:147]
ds_read_b64_tr_b16 a[200:201], v50 offset:4096
ds_read_b64_tr_b16 a[202:203], v50 offset:4608
v_mfma_f32_16x16x32_bf16 v[148:151], a[24:27], a[12:15], v[148:151]
buffer_load_dwordx4 v243, s[8:11], s45 offen offset:3072 lds
v_mfma_f32_16x16x32_bf16 v[184:187], a[28:31], a[12:15], v[184:187]
s_add_u32 s45, s45, 131072
v_mfma_f32_16x16x32_bf16 v[188:191], a[32:35], a[12:15], v[188:191]
v_mfma_f32_16x16x32_bf16 v[224:227], a[12:15], a[36:39], v[224:227]
v_mfma_f32_16x16x32_bf16 v[228:231], a[12:15], a[40:43], v[228:231]
s_add_u32 m0, s91, 32768
v_mfma_f32_16x16x32_bf16 v[152:155], a[20:23], a[16:19], v[152:155]
buffer_load_dwordx4 v7, s[4:7], s44 offen offset:0 sc1 lds
v_mfma_f32_16x16x32_bf16 v[156:159], a[24:27], a[16:19], v[156:159]
v_mfma_f32_16x16x32_bf16 v[192:195], a[28:31], a[16:19], v[192:195]
v_mfma_f32_16x16x32_bf16 v[196:199], a[32:35], a[16:19], v[196:199]
v_mfma_f32_16x16x32_bf16 v[232:235], a[16:19], a[36:39], v[232:235]
v_mfma_f32_16x16x32_bf16 v[236:239], a[16:19], a[40:43], v[236:239]
s_waitcnt lgkmcnt(0)
buffer_load_dwordx4 v7, s[4:7], s44 offen offset:1024 sc1 lds
v_mfma_f32_16x16x32_bf16 v[160:163], a[172:175], a[128:131], v[160:163]
v_mfma_f32_16x16x32_bf16 v[164:167], a[192:195], a[128:131], v[164:167]
ds_read_b64_tr_b16 a[20:21], v47 offset:14336
ds_read_b64_tr_b16 a[22:23], v47 offset:14848
v_mfma_f32_16x16x32_bf16 v[200:203], a[128:131], a[196:199], v[200:203]
v_mfma_f32_16x16x32_bf16 v[204:207], a[128:131], a[200:203], v[204:207]
ds_read_b128 a[0:3], v46
v_mfma_f32_16x16x32_bf16 v[128:131], a[164:167], a[132:135], v[128:131]
buffer_load_dwordx4 v7, s[4:7], s44 offen offset:2048 sc1 lds
ds_read_b64_tr_b16 a[24:25], v48 offset:14336
ds_read_b64_tr_b16 a[26:27], v48 offset:14848
v_mfma_f32_16x16x32_bf16 v[132:135], a[168:171], a[132:135], v[132:135]
v_mfma_f32_16x16x32_bf16 v[168:171], a[172:175], a[132:135], v[168:171]
ds_read_b128 a[4:7], v44 offset:0
v_mfma_f32_16x16x32_bf16 v[172:175], a[192:195], a[132:135], v[172:175]
ds_read_b64_tr_b16 a[28:29], v47 offset:16384
ds_read_b64_tr_b16 a[30:31], v47 offset:16896
v_mfma_f32_16x16x32_bf16 v[208:211], a[132:135], a[196:199], v[208:211]
v_mfma_f32_16x16x32_bf16 v[212:215], a[132:135], a[200:203], v[212:215]
buffer_load_dwordx4 v7, s[4:7], s44 offen offset:3072 sc1 lds
ds_read_b128 a[8:11], v44 offset:2048
v_mfma_f32_16x16x32_bf16 v[136:139], a[164:167], a[136:139], v[136:139]
s_add_u32 s44, s44, 4096
ds_read_b64_tr_b16 a[32:33], v48 offset:16384
ds_read_b64_tr_b16 a[34:35], v48 offset:16896
v_mfma_f32_16x16x32_bf16 v[140:143], a[168:171], a[136:139], v[140:143]
v_mfma_f32_16x16x32_bf16 v[176:179], a[172:175], a[136:139], v[176:179]
ds_read_b128 a[12:15], v44 offset:4096
v_mfma_f32_16x16x32_bf16 v[180:183], a[192:195], a[136:139], v[180:183]
ds_read_b64_tr_b16 a[36:37], v49 offset:18432
ds_read_b64_tr_b16 a[38:39], v49 offset:18944
v_mfma_f32_16x16x32_bf16 v[216:219], a[136:139], a[196:199], v[216:219]
v_mfma_f32_16x16x32_bf16 v[220:223], a[136:139], a[200:203], v[220:223]
ds_read_b128 a[16:19], v44 offset:6144
v_mfma_f32_16x16x32_bf16 v[144:147], a[164:167], a[140:143], v[144:147]
ds_read_b64_tr_b16 a[40:41], v50 offset:18432
ds_read_b64_tr_b16 a[42:43], v50 offset:18944
v_mfma_f32_16x16x32_bf16 v[148:151], a[168:171], a[140:143], v[148:151]
v_mfma_f32_16x16x32_bf16 v[184:187], a[172:175], a[140:143], v[184:187]
v_mfma_f32_16x16x32_bf16 v[188:191], a[192:195], a[140:143], v[188:191]
v_mfma_f32_16x16x32_bf16 v[224:227], a[140:143], a[196:199], v[224:227]
v_mfma_f32_16x16x32_bf16 v[228:231], a[140:143], a[200:203], v[228:231]
v_mfma_f32_16x16x32_bf16 v[152:155], a[164:167], a[160:163], v[152:155]
v_mfma_f32_16x16x32_bf16 v[156:159], a[168:171], a[160:163], v[156:159]
v_mfma_f32_16x16x32_bf16 v[192:195], a[172:175], a[160:163], v[192:195]
v_mfma_f32_16x16x32_bf16 v[196:199], a[192:195], a[160:163], v[196:199]
v_mfma_f32_16x16x32_bf16 v[232:235], a[160:163], a[196:199], v[232:235]
v_mfma_f32_16x16x32_bf16 v[236:239], a[160:163], a[200:203], v[236:239]
s_waitcnt vmcnt(16)
s_waitcnt lgkmcnt(0)
s_barrier
v_mfma_f32_16x16x32_bf16 v[160:163], a[28:31], a[0:3], v[160:163]
v_mfma_f32_16x16x32_bf16 v[164:167], a[32:35], a[0:3], v[164:167]
s_add_u32 m0, s91, 16384
ds_read_b64_tr_b16 a[80:81], v245 offset:0
ds_read_b64_tr_b16 a[82:83], v245 offset:2048
v_mfma_f32_16x16x32_bf16 v[200:203], a[0:3], a[36:39], v[200:203]
buffer_load_dwordx4 v240, s[8:11], s45 offen offset:0 lds
v_mfma_f32_16x16x32_bf16 v[204:207], a[0:3], a[40:43], v[204:207]
v_mfma_f32_16x16x32_bf16 v[128:131], a[20:23], a[4:7], v[128:131]
ds_read_b128 a[64:67], v244 offset:0
ds_read_b128 a[68:71], v244 offset:256
v_mfma_f32_16x16x32_bf16 v[132:135], a[24:27], a[4:7], v[132:135]
buffer_load_dwordx4 v241, s[8:11], s45 offen offset:1024 lds
v_mfma_f32_16x16x32_bf16 v[168:171], a[28:31], a[4:7], v[168:171]
v_mfma_f32_16x16x32_bf16 v[172:175], a[32:35], a[4:7], v[172:175]
ds_read_b64_tr_b16 a[84:85], v246 offset:0
ds_read_b64_tr_b16 a[86:87], v246 offset:2048
v_mfma_f32_16x16x32_bf16 v[208:211], a[4:7], a[36:39], v[208:211]
buffer_load_dwordx4 v242, s[8:11], s45 offen offset:2048 lds
v_mfma_f32_16x16x32_bf16 v[212:215], a[4:7], a[40:43], v[212:215]
v_mfma_f32_16x16x32_bf16 v[136:139], a[20:23], a[8:11], v[136:139]
ds_read_b64_tr_b16 a[88:89], v247 offset:0
ds_read_b64_tr_b16 a[90:91], v247 offset:2048
v_mfma_f32_16x16x32_bf16 v[140:143], a[24:27], a[8:11], v[140:143]
buffer_load_dwordx4 v243, s[8:11], s45 offen offset:3072 lds
v_mfma_f32_16x16x32_bf16 v[176:179], a[28:31], a[8:11], v[176:179]
s_add_u32 s45, s45, 131072
v_mfma_f32_16x16x32_bf16 v[180:183], a[32:35], a[8:11], v[180:183]
s_add_u32 m0, s91, 0
ds_read_b128 a[72:75], v244 offset:4096
ds_read_b128 a[76:79], v244 offset:4352
v_mfma_f32_16x16x32_bf16 v[216:219], a[8:11], a[36:39], v[216:219]
buffer_load_dwordx4 v7, s[4:7], s44 offen offset:0 sc1 lds
v_mfma_f32_16x16x32_bf16 v[220:223], a[8:11], a[40:43], v[220:223]
v_mfma_f32_16x16x32_bf16 v[144:147], a[20:23], a[12:15], v[144:147]
ds_read_b64_tr_b16 a[92:93], v248 offset:0
ds_read_b64_tr_b16 a[94:95], v248 offset:2048
v_mfma_f32_16x16x32_bf16 v[148:151], a[24:27], a[12:15], v[148:151]
buffer_load_dwordx4 v7, s[4:7], s44 offen offset:1024 sc1 lds
v_mfma_f32_16x16x32_bf16 v[184:187], a[28:31], a[12:15], v[184:187]
v_mfma_f32_16x16x32_bf16 v[188:191], a[32:35], a[12:15], v[188:191]
v_mfma_f32_16x16x32_bf16 v[224:227], a[12:15], a[36:39], v[224:227]
buffer_load_dwordx4 v7, s[4:7], s44 offen offset:2048 sc1 lds
v_mfma_f32_16x16x32_bf16 v[228:231], a[12:15], a[40:43], v[228:231]
v_mfma_f32_16x16x32_bf16 v[152:155], a[20:23], a[16:19], v[152:155]
v_mfma_f32_16x16x32_bf16 v[156:159], a[24:27], a[16:19], v[156:159]
buffer_load_dwordx4 v7, s[4:7], s44 offen offset:3072 sc1 lds
v_mfma_f32_16x16x32_bf16 v[192:195], a[28:31], a[16:19], v[192:195]
s_add_u32 s44, s44, 4096
v_mfma_f32_16x16x32_bf16 v[196:199], a[32:35], a[16:19], v[196:199]
v_mfma_f32_16x16x32_bf16 v[232:235], a[16:19], a[36:39], v[232:235]
v_mfma_f32_16x16x32_bf16 v[236:239], a[16:19], a[40:43], v[236:239]
s_mov_b32 s39, 1048744942
s_waitcnt lgkmcnt(0)
v_mfma_f32_16x16x32_bf16 v[64:67], a[80:83], a[64:67], 0
s_add_u32 m0, s91, 147456
ds_read_b64_tr_b16 a[112:113], v245 offset:8192
ds_read_b64_tr_b16 a[114:115], v245 offset:10240
v_mfma_f32_16x16x32_bf16 v[68:71], a[84:87], a[64:67], 0
buffer_load_dwordx4 v240, s[8:11], s45 offen offset:0 lds
v_mfma_f32_16x16x32_bf16 v[72:75], a[88:91], a[64:67], 0
ds_read_b128 a[96:99], v244 offset:2048
ds_read_b128 a[100:103], v244 offset:2304
v_mfma_f32_16x16x32_bf16 v[76:79], a[92:95], a[64:67], 0
buffer_load_dwordx4 v241, s[8:11], s45 offen offset:1024 lds
ds_read_b64_tr_b16 a[116:117], v246 offset:8192
ds_read_b64_tr_b16 a[118:119], v246 offset:10240
v_mfma_f32_16x16x32_bf16 v[80:83], a[80:83], a[68:71], 0
v_cvt_pk_bf16_f32 v160, v160, v161
v_cvt_pk_bf16_f32 v161, v162, v163
v_cvt_pk_bf16_f32 v162, v164, v165
v_cvt_pk_bf16_f32 v163, v166, v167
v_mfma_f32_16x16x32_bf16 v[84:87], a[84:87], a[68:71], 0
buffer_load_dwordx4 v242, s[8:11], s45 offen offset:2048 lds
ds_read_b64_tr_b16 a[120:121], v247 offset:8192
ds_read_b64_tr_b16 a[122:123], v247 offset:10240
v_mfma_f32_16x16x32_bf16 v[88:91], a[88:91], a[68:71], 0
ds_read_b128 a[104:107], v244 offset:6144
ds_read_b128 a[108:111], v244 offset:6400
v_cvt_pk_bf16_f32 v36, v200, v201
v_cvt_pk_bf16_f32 v37, v202, v203
v_mfma_f32_16x16x32_bf16 v[92:95], a[92:95], a[68:71], 0
buffer_load_dwordx4 v243, s[8:11], s45 offen offset:3072 lds
v_cvt_pk_bf16_f32 v46, v204, v205
v_cvt_pk_bf16_f32 v47, v206, v207
v_cvt_pk_bf16_f32 v168, v168, v169
v_mfma_f32_16x16x32_bf16 v[96:99], a[80:83], a[72:75], 0
s_cmp_eq_u32 s46, 0
s_cselect_b32 s53, s56, s57
s_add_u32 s45, s45, 131072
s_add_u32 m0, s91, 131072
ds_read_b64_tr_b16 a[124:125], v248 offset:8192
ds_read_b64_tr_b16 a[126:127], v248 offset:10240
v_mfma_f32_16x16x32_bf16 v[100:103], a[84:87], a[72:75], 0
buffer_load_dwordx4 v7, s[4:7], s44 offen offset:0 sc1 lds
v_cvt_pk_bf16_f32 v169, v170, v171
v_cvt_pk_bf16_f32 v170, v172, v173
v_cvt_pk_bf16_f32 v171, v174, v175
v_mfma_f32_16x16x32_bf16 v[104:107], a[88:91], a[72:75], 0
s_add_u32 s49, s46, s91
s_add_u32 s46, s46, s53
v_add_u32 v244, s53, v244
v_cvt_pk_bf16_f32 v128, v128, v129
v_cvt_pk_bf16_f32 v129, v130, v131
v_mfma_f32_16x16x32_bf16 v[108:111], a[92:95], a[72:75], 0
buffer_load_dwordx4 v7, s[4:7], s44 offen offset:1024 sc1 lds
v_cvt_pk_bf16_f32 v130, v132, v133
v_cvt_pk_bf16_f32 v131, v134, v135
v_cvt_pk_bf16_f32 v38, v208, v209
v_mfma_f32_16x16x32_bf16 v[112:115], a[80:83], a[76:79], 0
v_add_u32 v245, s53, v245
v_add_u32 v246, s53, v246
v_cvt_pk_bf16_f32 v39, v210, v211
v_cvt_pk_bf16_f32 v48, v212, v213
v_mfma_f32_16x16x32_bf16 v[116:119], a[84:87], a[76:79], 0
buffer_load_dwordx4 v7, s[4:7], s44 offen offset:2048 sc1 lds
v_cvt_pk_bf16_f32 v49, v214, v215
v_cvt_pk_bf16_f32 v176, v176, v177
v_cvt_pk_bf16_f32 v177, v178, v179
v_mfma_f32_16x16x32_bf16 v[120:123], a[88:91], a[76:79], 0
buffer_load_dwordx4 v7, s[4:7], s44 offen offset:3072 sc1 lds
v_add_u32 v247, s53, v247
v_add_u32 v248, s53, v248
v_cvt_pk_bf16_f32 v178, v180, v181
v_mfma_f32_16x16x32_bf16 v[124:127], a[92:95], a[76:79], 0
s_waitcnt vmcnt(24) lgkmcnt(0)
s_barrier
v_cvt_pk_bf16_f32 v179, v182, v183
v_cvt_pk_bf16_f32 v136, v136, v137
v_cvt_pk_bf16_f32 v137, v138, v139
v_mfma_f32_16x16x32_bf16 v[64:67], a[112:115], a[96:99], v[64:67]
s_add_u32 m0, s49, 16384
s_add_u32 s44, s44, 4096
ds_read_b64_tr_b16 a[80:81], v245 offset:0
ds_read_b64_tr_b16 a[82:83], v245 offset:2048
v_mfma_f32_16x16x32_bf16 v[68:71], a[116:119], a[96:99], v[68:71]
buffer_load_dwordx4 v240, s[8:11], s45 offen offset:0 lds
v_cvt_pk_bf16_f32 v138, v140, v141
v_cvt_pk_bf16_f32 v139, v142, v143
v_cvt_pk_bf16_f32 v40, v216, v217
v_mfma_f32_16x16x32_bf16 v[72:75], a[120:123], a[96:99], v[72:75]
ds_read_b128 a[64:67], v244 offset:0
ds_read_b128 a[68:71], v244 offset:256
v_cvt_pk_bf16_f32 v41, v218, v219
v_cvt_pk_bf16_f32 v50, v220, v221
v_mfma_f32_16x16x32_bf16 v[76:79], a[124:127], a[96:99], v[76:79]
ds_read_b64_tr_b16 a[84:85], v246 offset:0
ds_read_b64_tr_b16 a[86:87], v246 offset:2048
v_cvt_pk_bf16_f32 v51, v222, v223
v_cvt_pk_bf16_f32 v184, v184, v185
v_mfma_f32_16x16x32_bf16 v[80:83], a[112:115], a[100:103], v[80:83]
buffer_load_dwordx4 v241, s[8:11], s45 offen offset:1024 lds
v_cvt_pk_bf16_f32 v185, v186, v187
v_cvt_pk_bf16_f32 v186, v188, v189
v_cvt_pk_bf16_f32 v187, v190, v191
v_mfma_f32_16x16x32_bf16 v[84:87], a[116:119], a[100:103], v[84:87]
ds_read_b64_tr_b16 a[88:89], v247 offset:0
ds_read_b64_tr_b16 a[90:91], v247 offset:2048
v_cvt_pk_bf16_f32 v144, v144, v145
v_cvt_pk_bf16_f32 v145, v146, v147
v_mfma_f32_16x16x32_bf16 v[88:91], a[120:123], a[100:103], v[88:91]
ds_read_b128 a[72:75], v244 offset:4096
ds_read_b128 a[76:79], v244 offset:4352
v_cvt_pk_bf16_f32 v146, v148, v149
v_cvt_pk_bf16_f32 v147, v150, v151
v_mfma_f32_16x16x32_bf16 v[92:95], a[124:127], a[100:103], v[92:95]
v_cvt_pk_bf16_f32 v42, v224, v225
v_cvt_pk_bf16_f32 v43, v226, v227
v_cvt_pk_bf16_f32 v52, v228, v229
v_cvt_pk_bf16_f32 v53, v230, v231
v_mfma_f32_16x16x32_bf16 v[96:99], a[112:115], a[104:107], v[96:99]
ds_read_b64_tr_b16 a[92:93], v248 offset:0
ds_read_b64_tr_b16 a[94:95], v248 offset:2048
v_cvt_pk_bf16_f32 v192, v192, v193
v_cvt_pk_bf16_f32 v193, v194, v195
v_mfma_f32_16x16x32_bf16 v[100:103], a[116:119], a[104:107], v[100:103]
buffer_load_dwordx4 v242, s[8:11], s45 offen offset:2048 lds
v_cvt_pk_bf16_f32 v194, v196, v197
v_cvt_pk_bf16_f32 v195, v198, v199
v_cvt_pk_bf16_f32 v152, v152, v153
v_mfma_f32_16x16x32_bf16 v[104:107], a[120:123], a[104:107], v[104:107]
v_cvt_pk_bf16_f32 v153, v154, v155
v_cvt_pk_bf16_f32 v154, v156, v157
v_cvt_pk_bf16_f32 v155, v158, v159
v_cvt_pk_bf16_f32 v44, v232, v233
v_mfma_f32_16x16x32_bf16 v[108:111], a[124:127], a[104:107], v[108:111]
v_cvt_pk_bf16_f32 v45, v234, v235
v_cvt_pk_bf16_f32 v54, v236, v237
v_cvt_pk_bf16_f32 v55, v238, v239
v_mfma_f32_16x16x32_bf16 v[200:203], v[160:163], v[128:131], v[24:27]
v_mfma_f32_16x16x32_bf16 v[112:115], a[112:115], a[108:111], v[112:115]
buffer_load_dwordx4 v243, s[8:11], s45 offen offset:3072 lds
v_mfma_f32_16x16x32_bf16 v[204:207], v[168:171], v[128:131], v[20:23]
v_mfma_f32_16x16x32_bf16 v[116:119], a[116:119], a[108:111], v[116:119]
s_add_u32 s45, s45, 131072
v_mfma_f32_16x16x32_bf16 v[208:211], v[168:171], v[136:139], v[16:19]
v_mfma_f32_16x16x32_bf16 v[120:123], a[120:123], a[108:111], v[120:123]
v_mfma_f32_16x16x32_bf16 v[212:215], v[176:179], v[136:139], v[20:23]
v_mfma_f32_16x16x32_bf16 v[124:127], a[124:127], a[108:111], v[124:127]
s_waitcnt lgkmcnt(0)
v_mfma_f32_16x16x32_bf16 v[216:219], v[176:179], v[144:147], v[16:19]
v_mfma_f32_16x16x32_bf16 v[64:67], a[80:83], a[64:67], v[64:67]
s_mov_b32 m0, s49
ds_read_b64_tr_b16 a[112:113], v245 offset:8192
ds_read_b64_tr_b16 a[114:115], v245 offset:10240
v_mfma_f32_16x16x32_bf16 v[220:223], v[184:187], v[144:147], v[20:23]
v_mfma_f32_16x16x32_bf16 v[68:71], a[84:87], a[64:67], v[68:71]
buffer_load_dwordx4 v7, s[4:7], s44 offen offset:0 sc1 lds
v_mfma_f32_16x16x32_bf16 v[224:227], v[184:187], v[152:155], v[16:19]
v_mfma_f32_16x16x32_bf16 v[72:75], a[88:91], a[64:67], v[72:75]
ds_read_b128 a[96:99], v244 offset:2048
ds_read_b128 a[100:103], v244 offset:2304
v_mfma_f32_16x16x32_bf16 v[228:231], v[192:195], v[152:155], v[20:23]
v_mfma_f32_16x16x32_bf16 v[76:79], a[92:95], a[64:67], v[76:79]
ds_read_b64_tr_b16 a[116:117], v246 offset:8192
ds_read_b64_tr_b16 a[118:119], v246 offset:10240
v_max3_f32 v56, v200, v201, v202
v_max3_f32 v58, v208, v209, v210
v_mfma_f32_16x16x32_bf16 v[80:83], a[80:83], a[68:71], v[80:83]
buffer_load_dwordx4 v7, s[4:7], s44 offen offset:1024 sc1 lds
v_max3_f32 v60, v216, v217, v218
v_max3_f32 v62, v224, v225, v226
v_max3_f32 v57, v203, v204, v205
v_mfma_f32_16x16x32_bf16 v[84:87], a[84:87], a[68:71], v[84:87]
ds_read_b64_tr_b16 a[120:121], v247 offset:8192
ds_read_b64_tr_b16 a[122:123], v247 offset:10240
v_max3_f32 v59, v211, v212, v213
v_max3_f32 v61, v219, v220, v221
v_mfma_f32_16x16x32_bf16 v[88:91], a[88:91], a[68:71], v[88:91]
ds_read_b128 a[104:107], v244 offset:6144
ds_read_b128 a[108:111], v244 offset:6400
v_max3_f32 v63, v227, v228, v229
v_max3_f32 v56, v56, v206, v207
v_mfma_f32_16x16x32_bf16 v[92:95], a[92:95], a[68:71], v[92:95]
v_max3_f32 v58, v58, v214, v215
v_max3_f32 v60, v60, v222, v223
v_max3_f32 v62, v62, v230, v231
v_max_f32 v56, v56, v57
v_mfma_f32_16x16x32_bf16 v[96:99], a[80:83], a[72:75], v[96:99]
s_cmp_eq_u32 s46, 0
s_cselect_b32 s53, s56, s57
ds_read_b64_tr_b16 a[124:125], v248 offset:8192
ds_read_b64_tr_b16 a[126:127], v248 offset:10240
v_mfma_f32_16x16x32_bf16 v[100:103], a[84:87], a[72:75], v[100:103]
v_max_f32 v58, v58, v59
v_max_f32 v60, v60, v61
v_max_f32 v62, v62, v63
v_mov_b32 v57, v56
v_mfma_f32_16x16x32_bf16 v[104:107], a[88:91], a[72:75], v[104:107]
s_add_u32 s49, s46, s91
s_add_u32 s46, s46, s53
v_add_u32 v244, s53, v244
v_mov_b32 v59, v58
v_mov_b32 v61, v60
v_mfma_f32_16x16x32_bf16 v[108:111], a[92:95], a[72:75], v[108:111]
buffer_load_dwordx4 v7, s[4:7], s44 offen offset:2048 sc1 lds
v_mov_b32 v63, v62
v_permlane16_swap_b32 v56, v57
v_mfma_f32_16x16x32_bf16 v[112:115], a[80:83], a[76:79], v[112:115]
v_add_u32 v245, s53, v245
v_add_u32 v246, s53, v246
v_permlane16_swap_b32 v58, v59
v_mfma_f32_16x16x32_bf16 v[116:119], a[84:87], a[76:79], v[116:119]
buffer_load_dwordx4 v7, s[4:7], s44 offen offset:3072 sc1 lds
v_permlane16_swap_b32 v60, v61
v_mfma_f32_16x16x32_bf16 v[120:123], a[88:91], a[76:79], v[120:123]
v_add_u32 v247, s53, v247
v_add_u32 v248, s53, v248
v_permlane16_swap_b32 v62, v63
v_mfma_f32_16x16x32_bf16 v[124:127], a[92:95], a[76:79], v[124:127]
s_waitcnt vmcnt(24) lgkmcnt(0)
s_barrier
v_max_f32 v56, v56, v57
v_max_f32 v58, v58, v59
v_max_f32 v60, v60, v61
v_mfma_f32_16x16x32_bf16 v[64:67], a[112:115], a[96:99], v[64:67]
s_add_u32 m0, s49, 16384
s_add_u32 s44, s44, 4096
ds_read_b64_tr_b16 a[80:81], v245 offset:0
ds_read_b64_tr_b16 a[82:83], v245 offset:2048
v_mfma_f32_16x16x32_bf16 v[68:71], a[116:119], a[96:99], v[68:71]
buffer_load_dwordx4 v240, s[8:11], s45 offen offset:0 lds
v_max_f32 v62, v62, v63
v_mov_b32 v57, v56
v_mov_b32 v59, v58
v_mfma_f32_16x16x32_bf16 v[72:75], a[120:123], a[96:99], v[72:75]
ds_read_b128 a[64:67], v244 offset:0
ds_read_b128 a[68:71], v244 offset:256
v_mov_b32 v61, v60
v_mov_b32 v63, v62
v_mfma_f32_16x16x32_bf16 v[76:79], a[124:127], a[96:99], v[76:79]
ds_read_b64_tr_b16 a[84:85], v246 offset:0
ds_read_b64_tr_b16 a[86:87], v246 offset:2048
v_permlane32_swap_b32 v56, v57
v_mfma_f32_16x16x32_bf16 v[80:83], a[112:115], a[100:103], v[80:83]
buffer_load_dwordx4 v241, s[8:11], s45 offen offset:1024 lds
v_permlane32_swap_b32 v58, v59
v_mfma_f32_16x16x32_bf16 v[84:87], a[116:119], a[100:103], v[84:87]
ds_read_b64_tr_b16 a[88:89], v247 offset:0
ds_read_b64_tr_b16 a[90:91], v247 offset:2048
v_permlane32_swap_b32 v60, v61
v_mfma_f32_16x16x32_bf16 v[88:91], a[120:123], a[100:103], v[88:91]
ds_read_b128 a[72:75], v244 offset:4096
ds_read_b128 a[76:79], v244 offset:4352
v_permlane32_swap_b32 v62, v63
v_mfma_f32_16x16x32_bf16 v[92:95], a[124:127], a[100:103], v[92:95]
v_max_f32 v56, v56, v57
v_max_f32 v58, v58, v59
v_max_f32 v60, v60, v61
v_max_f32 v62, v62, v63
v_mfma_f32_16x16x32_bf16 v[96:99], a[112:115], a[104:107], v[96:99]
ds_read_b64_tr_b16 a[92:93], v248 offset:0
ds_read_b64_tr_b16 a[94:95], v248 offset:2048
v_mul_f32 v56, s39, v56
v_mul_f32 v58, s39, v58
v_mfma_f32_16x16x32_bf16 v[100:103], a[116:119], a[104:107], v[100:103]
buffer_load_dwordx4 v242, s[8:11], s45 offen offset:2048 lds
v_mul_f32 v60, s39, v60
v_mul_f32 v62, s39, v62
v_fma_f32 v200, v200, s39, -v56
v_mfma_f32_16x16x32_bf16 v[104:107], a[120:123], a[104:107], v[104:107]
v_fma_f32 v208, v208, s39, -v58
v_fma_f32 v216, v216, s39, -v60
v_fma_f32 v224, v224, s39, -v62
v_fma_f32 v201, v201, s39, -v56
v_mfma_f32_16x16x32_bf16 v[108:111], a[124:127], a[104:107], v[108:111]
v_fma_f32 v209, v209, s39, -v58
v_fma_f32 v217, v217, s39, -v60
v_fma_f32 v225, v225, s39, -v62
v_fma_f32 v202, v202, s39, -v56
v_mfma_f32_16x16x32_bf16 v[112:115], a[112:115], a[108:111], v[112:115]
buffer_load_dwordx4 v243, s[8:11], s45 offen offset:3072 lds
v_fma_f32 v210, v210, s39, -v58
v_fma_f32 v218, v218, s39, -v60
v_fma_f32 v226, v226, s39, -v62
v_mfma_f32_16x16x32_bf16 v[116:119], a[116:119], a[108:111], v[116:119]
s_add_u32 s45, s45, 131072
v_fma_f32 v203, v203, s39, -v56
v_fma_f32 v211, v211, s39, -v58
v_fma_f32 v219, v219, s39, -v60
v_fma_f32 v227, v227, s39, -v62
v_mfma_f32_16x16x32_bf16 v[120:123], a[120:123], a[108:111], v[120:123]
v_fma_f32 v204, v204, s39, -v56
v_fma_f32 v212, v212, s39, -v58
v_fma_f32 v220, v220, s39, -v60
v_fma_f32 v228, v228, s39, -v62
v_mfma_f32_16x16x32_bf16 v[124:127], a[124:127], a[108:111], v[124:127]
s_waitcnt lgkmcnt(0)
v_fma_f32 v205, v205, s39, -v56
v_fma_f32 v213, v213, s39, -v58
v_fma_f32 v221, v221, s39, -v60
v_fma_f32 v229, v229, s39, -v62
v_mfma_f32_16x16x32_bf16 v[64:67], a[80:83], a[64:67], v[64:67]
s_mov_b32 m0, s49
ds_read_b64_tr_b16 a[112:113], v245 offset:8192
ds_read_b64_tr_b16 a[114:115], v245 offset:10240
v_fma_f32 v206, v206, s39, -v56
v_mfma_f32_16x16x32_bf16 v[68:71], a[84:87], a[64:67], v[68:71]
buffer_load_dwordx4 v7, s[4:7], s44 offen offset:0 sc1 lds
v_fma_f32 v214, v214, s39, -v58
v_fma_f32 v222, v222, s39, -v60
v_fma_f32 v230, v230, s39, -v62
v_mfma_f32_16x16x32_bf16 v[72:75], a[88:91], a[64:67], v[72:75]
ds_read_b128 a[96:99], v244 offset:2048
ds_read_b128 a[100:103], v244 offset:2304
v_fma_f32 v207, v207, s39, -v56
v_fma_f32 v215, v215, s39, -v58
v_mfma_f32_16x16x32_bf16 v[76:79], a[92:95], a[64:67], v[76:79]
ds_read_b64_tr_b16 a[116:117], v246 offset:8192
ds_read_b64_tr_b16 a[118:119], v246 offset:10240
v_fma_f32 v223, v223, s39, -v60
v_fma_f32 v231, v231, s39, -v62
v_mfma_f32_16x16x32_bf16 v[80:83], a[80:83], a[68:71], v[80:83]
buffer_load_dwordx4 v7, s[4:7], s44 offen offset:1024 sc1 lds
v_exp_f32 v200, v200
v_mfma_f32_16x16x32_bf16 v[84:87], a[84:87], a[68:71], v[84:87]
ds_read_b64_tr_b16 a[120:121], v247 offset:8192
ds_read_b64_tr_b16 a[122:123], v247 offset:10240
v_exp_f32 v208, v208
v_mfma_f32_16x16x32_bf16 v[88:91], a[88:91], a[68:71], v[88:91]
ds_read_b128 a[104:107], v244 offset:6144
ds_read_b128 a[108:111], v244 offset:6400
v_exp_f32 v216, v216
v_mfma_f32_16x16x32_bf16 v[92:95], a[92:95], a[68:71], v[92:95]
v_exp_f32 v224, v224
v_exp_f32 v201, v201
v_mfma_f32_16x16x32_bf16 v[96:99], a[80:83], a[72:75], v[96:99]
s_cmp_eq_u32 s46, 0
s_cselect_b32 s53, s56, s57
ds_read_b64_tr_b16 a[124:125], v248 offset:8192
ds_read_b64_tr_b16 a[126:127], v248 offset:10240
v_mfma_f32_16x16x32_bf16 v[100:103], a[84:87], a[72:75], v[100:103]
v_exp_f32 v209, v209
v_exp_f32 v217, v217
v_mfma_f32_16x16x32_bf16 v[104:107], a[88:91], a[72:75], v[104:107]
s_add_u32 s49, s46, s91
s_add_u32 s46, s46, s53
v_add_u32 v244, s53, v244
v_exp_f32 v225, v225
v_mfma_f32_16x16x32_bf16 v[108:111], a[92:95], a[72:75], v[108:111]
buffer_load_dwordx4 v7, s[4:7], s44 offen offset:2048 sc1 lds
v_exp_f32 v202, v202
v_mfma_f32_16x16x32_bf16 v[112:115], a[80:83], a[76:79], v[112:115]
v_add_u32 v245, s53, v245
v_add_u32 v246, s53, v246
v_exp_f32 v210, v210
v_mfma_f32_16x16x32_bf16 v[116:119], a[84:87], a[76:79], v[116:119]
buffer_load_dwordx4 v7, s[4:7], s44 offen offset:3072 sc1 lds
v_exp_f32 v218, v218
v_mfma_f32_16x16x32_bf16 v[120:123], a[88:91], a[76:79], v[120:123]
v_add_u32 v247, s53, v247
v_add_u32 v248, s53, v248
v_exp_f32 v226, v226
v_mfma_f32_16x16x32_bf16 v[124:127], a[92:95], a[76:79], v[124:127]
s_waitcnt vmcnt(24) lgkmcnt(0)
s_barrier
v_exp_f32 v203, v203
v_mfma_f32_16x16x32_bf16 v[64:67], a[112:115], a[96:99], v[64:67]
s_add_u32 m0, s49, 16384
s_add_u32 s44, s44, 4096
ds_read_b64_tr_b16 a[80:81], v245 offset:0
ds_read_b64_tr_b16 a[82:83], v245 offset:2048
v_mfma_f32_16x16x32_bf16 v[68:71], a[116:119], a[96:99], v[68:71]
buffer_load_dwordx4 v240, s[8:11], s45 offen offset:0 lds
v_exp_f32 v211, v211
v_mfma_f32_16x16x32_bf16 v[72:75], a[120:123], a[96:99], v[72:75]
ds_read_b128 a[64:67], v244 offset:0
ds_read_b128 a[68:71], v244 offset:256
v_exp_f32 v219, v219
v_mfma_f32_16x16x32_bf16 v[76:79], a[124:127], a[96:99], v[76:79]
ds_read_b64_tr_b16 a[84:85], v246 offset:0
ds_read_b64_tr_b16 a[86:87], v246 offset:2048
v_exp_f32 v227, v227
v_mfma_f32_16x16x32_bf16 v[80:83], a[112:115], a[100:103], v[80:83]
buffer_load_dwordx4 v241, s[8:11], s45 offen offset:1024 lds
v_exp_f32 v204, v204
v_mfma_f32_16x16x32_bf16 v[84:87], a[116:119], a[100:103], v[84:87]
ds_read_b64_tr_b16 a[88:89], v247 offset:0
ds_read_b64_tr_b16 a[90:91], v247 offset:2048
.Lpad18:
s_cbranch_execz .Ltch6
v_exp_f32 v212, v212
v_mfma_f32_16x16x32_bf16 v[88:91], a[120:123], a[100:103], v[88:91]
ds_read_b128 a[72:75], v244 offset:4096
ds_read_b128 a[76:79], v244 offset:4352
v_exp_f32 v220, v220
v_mfma_f32_16x16x32_bf16 v[92:95], a[124:127], a[100:103], v[92:95]
v_exp_f32 v228, v228
v_exp_f32 v205, v205
v_mfma_f32_16x16x32_bf16 v[96:99], a[112:115], a[104:107], v[96:99]
ds_read_b64_tr_b16 a[92:93], v248 offset:0
ds_read_b64_tr_b16 a[94:95], v248 offset:2048
v_exp_f32 v213, v213
v_mfma_f32_16x16x32_bf16 v[100:103], a[116:119], a[104:107], v[100:103]
buffer_load_dwordx4 v242, s[8:11], s45 offen offset:2048 lds
v_exp_f32 v221, v221
v_mfma_f32_16x16x32_bf16 v[104:107], a[120:123], a[104:107], v[104:107]
v_exp_f32 v229, v229
v_exp_f32 v206, v206
v_mfma_f32_16x16x32_bf16 v[108:111], a[124:127], a[104:107], v[108:111]
v_exp_f32 v214, v214
v_exp_f32 v222, v222
v_mfma_f32_16x16x32_bf16 v[112:115], a[112:115], a[108:111], v[112:115]
buffer_load_dwordx4 v243, s[8:11], s45 offen offset:3072 lds
v_exp_f32 v230, v230
v_mfma_f32_16x16x32_bf16 v[116:119], a[116:119], a[108:111], v[116:119]
s_add_u32 s45, s45, 131072
v_exp_f32 v207, v207
v_exp_f32 v215, v215
v_mfma_f32_16x16x32_bf16 v[120:123], a[120:123], a[108:111], v[120:123]
v_exp_f32 v223, v223
v_exp_f32 v231, v231
v_mfma_f32_16x16x32_bf16 v[124:127], a[124:127], a[108:111], v[124:127]
s_waitcnt lgkmcnt(0)
v_cvt_pk_bf16_f32 v132, v200, v201
v_cvt_pk_bf16_f32 v140, v208, v209
v_cvt_pk_bf16_f32 v148, v216, v217
v_cvt_pk_bf16_f32 v156, v224, v225
v_mfma_f32_16x16x32_bf16 v[64:67], a[80:83], a[64:67], v[64:67]
s_mov_b32 m0, s49
ds_read_b64_tr_b16 a[112:113], v245 offset:8192
ds_read_b64_tr_b16 a[114:115], v245 offset:10240
v_cvt_pk_bf16_f32 v133, v202, v203
v_mfma_f32_16x16x32_bf16 v[68:71], a[84:87], a[64:67], v[68:71]
buffer_load_dwordx4 v7, s[4:7], s44 offen offset:0 sc1 lds
v_cvt_pk_bf16_f32 v141, v210, v211
v_cvt_pk_bf16_f32 v149, v218, v219
v_cvt_pk_bf16_f32 v157, v226, v227
v_mfma_f32_16x16x32_bf16 v[72:75], a[88:91], a[64:67], v[72:75]
ds_read_b128 a[96:99], v244 offset:2048
ds_read_b128 a[100:103], v244 offset:2304
v_cvt_pk_bf16_f32 v134, v204, v205
v_cvt_pk_bf16_f32 v142, v212, v213
v_mfma_f32_16x16x32_bf16 v[76:79], a[92:95], a[64:67], v[76:79]
ds_read_b64_tr_b16 a[116:117], v246 offset:8192
ds_read_b64_tr_b16 a[118:119], v246 offset:10240
v_cvt_pk_bf16_f32 v150, v220, v221
v_cvt_pk_bf16_f32 v158, v228, v229
v_mfma_f32_16x16x32_bf16 v[80:83], a[80:83], a[68:71], v[80:83]
buffer_load_dwordx4 v7, s[4:7], s44 offen offset:1024 sc1 lds
v_cvt_pk_bf16_f32 v135, v206, v207
v_cvt_pk_bf16_f32 v143, v214, v215
v_cvt_pk_bf16_f32 v151, v222, v223
v_mfma_f32_16x16x32_bf16 v[84:87], a[84:87], a[68:71], v[84:87]
ds_read_b64_tr_b16 a[120:121], v247 offset:8192
ds_read_b64_tr_b16 a[122:123], v247 offset:10240
v_cvt_pk_bf16_f32 v159, v230, v231
v_add_f32 v56, v200, v201
v_mfma_f32_16x16x32_bf16 v[88:91], a[88:91], a[68:71], v[88:91]
ds_read_b128 a[104:107], v244 offset:6144
ds_read_b128 a[108:111], v244 offset:6400
v_add_f32 v58, v208, v209
v_add_f32 v60, v216, v217
v_mfma_f32_16x16x32_bf16 v[92:95], a[92:95], a[68:71], v[92:95]
v_add_f32 v62, v224, v225
v_mfma_f32_16x16x32_bf16 v[160:163], v[36:39], v[132:135], 0
v_mfma_f32_16x16x32_bf16 v[96:99], a[80:83], a[72:75], v[96:99]
s_cmp_eq_u32 s46, 0
s_cselect_b32 s53, s56, s57
ds_read_b64_tr_b16 a[124:125], v248 offset:8192
ds_read_b64_tr_b16 a[126:127], v248 offset:10240
v_mfma_f32_16x16x32_bf16 v[164:167], v[46:49], v[132:135], 0
v_mfma_f32_16x16x32_bf16 v[100:103], a[84:87], a[72:75], v[100:103]
v_mfma_f32_16x16x32_bf16 v[168:171], v[38:41], v[140:143], 0
v_mfma_f32_16x16x32_bf16 v[104:107], a[88:91], a[72:75], v[104:107]
s_add_u32 s49, s46, s91
s_add_u32 s46, s46, s53
v_add_u32 v244, s53, v244
v_mfma_f32_16x16x32_bf16 v[172:175], v[48:51], v[140:143], 0
v_mfma_f32_16x16x32_bf16 v[108:111], a[92:95], a[72:75], v[108:111]
buffer_load_dwordx4 v7, s[4:7], s44 offen offset:2048 sc1 lds
v_mfma_f32_16x16x32_bf16 v[176:179], v[40:43], v[148:151], 0
v_mfma_f32_16x16x32_bf16 v[112:115], a[80:83], a[76:79], v[112:115]
v_add_u32 v245, s53, v245
v_add_u32 v246, s53, v246
v_mfma_f32_16x16x32_bf16 v[180:183], v[50:53], v[148:151], 0
v_mfma_f32_16x16x32_bf16 v[116:119], a[84:87], a[76:79], v[116:119]
buffer_load_dwordx4 v7, s[4:7], s44 offen offset:3072 sc1 lds
v_mfma_f32_16x16x32_bf16 v[184:187], v[42:45], v[156:159], 0
v_mfma_f32_16x16x32_bf16 v[120:123], a[88:91], a[76:79], v[120:123]
v_add_u32 v247, s53, v247
v_add_u32 v248, s53, v248
v_mfma_f32_16x16x32_bf16 v[188:191], v[52:55], v[156:159], 0
v_mfma_f32_16x16x32_bf16 v[124:127], a[92:95], a[76:79], v[124:127]
s_waitcnt vmcnt(24) lgkmcnt(0)
s_barrier
v_add_f32 v57, v202, v203
v_add_f32 v59, v210, v211
v_add_f32 v61, v218, v219
v_mfma_f32_16x16x32_bf16 v[64:67], a[112:115], a[96:99], v[64:67]
s_add_u32 m0, s49, 16384
s_add_u32 s44, s44, 4096
ds_read_b64_tr_b16 a[80:81], v245 offset:0
ds_read_b64_tr_b16 a[82:83], v245 offset:2048
v_mfma_f32_16x16x32_bf16 v[68:71], a[116:119], a[96:99], v[68:71]
buffer_load_dwordx4 v240, s[8:11], s45 offen offset:0 lds
v_add_f32 v63, v226, v227
v_add_f32 v56, v56, v204
v_add_f32 v58, v58, v212
v_mfma_f32_16x16x32_bf16 v[72:75], a[120:123], a[96:99], v[72:75]
ds_read_b128 a[64:67], v244 offset:0
ds_read_b128 a[68:71], v244 offset:256
v_add_f32 v60, v60, v220
v_add_f32 v62, v62, v228
v_mfma_f32_16x16x32_bf16 v[76:79], a[124:127], a[96:99], v[76:79]
ds_read_b64_tr_b16 a[84:85], v246 offset:0
ds_read_b64_tr_b16 a[86:87], v246 offset:2048
v_add_f32 v57, v57, v205
v_add_f32 v59, v59, v213
v_mfma_f32_16x16x32_bf16 v[80:83], a[112:115], a[100:103], v[80:83]
buffer_load_dwordx4 v241, s[8:11], s45 offen offset:1024 lds
v_add_f32 v61, v61, v221
v_add_f32 v63, v63, v229
v_add_f32 v56, v56, v206
v_mfma_f32_16x16x32_bf16 v[84:87], a[116:119], a[100:103], v[84:87]
ds_read_b64_tr_b16 a[88:89], v247 offset:0
ds_read_b64_tr_b16 a[90:91], v247 offset:2048
v_add_f32 v58, v58, v214
v_add_f32 v60, v60, v222
v_mfma_f32_16x16x32_bf16 v[88:91], a[120:123], a[100:103], v[88:91]
ds_read_b128 a[72:75], v244 offset:4096
ds_read_b128 a[76:79], v244 offset:4352
v_add_f32 v62, v62, v230
v_add_f32 v57, v57, v207
v_mfma_f32_16x16x32_bf16 v[92:95], a[124:127], a[100:103], v[92:95]
v_add_f32 v59, v59, v215
v_add_f32 v61, v61, v223
v_add_f32 v63, v63, v231
v_add_f32 v56, v56, v57
v_mfma_f32_16x16x32_bf16 v[96:99], a[112:115], a[104:107], v[96:99]
ds_read_b64_tr_b16 a[92:93], v248 offset:0
ds_read_b64_tr_b16 a[94:95], v248 offset:2048
v_add_f32 v58, v58, v59
v_add_f32 v60, v60, v61
v_mfma_f32_16x16x32_bf16 v[100:103], a[116:119], a[104:107], v[100:103]
buffer_load_dwordx4 v242, s[8:11], s45 offen offset:2048 lds
v_add_f32 v62, v62, v63
v_mov_b32 v57, v56
v_mov_b32 v59, v58
v_mfma_f32_16x16x32_bf16 v[104:107], a[120:123], a[104:107], v[104:107]
v_mov_b32 v61, v60
v_mov_b32 v63, v62
v_permlane16_swap_b32 v56, v57
v_mfma_f32_16x16x32_bf16 v[108:111], a[124:127], a[104:107], v[108:111]
v_permlane16_swap_b32 v58, v59
v_permlane16_swap_b32 v60, v61
v_mfma_f32_16x16x32_bf16 v[112:115], a[112:115], a[108:111], v[112:115]
buffer_load_dwordx4 v243, s[8:11], s45 offen offset:3072 lds
v_permlane16_swap_b32 v62, v63
v_add_f32 v56, v56, v57
v_mfma_f32_16x16x32_bf16 v[116:119], a[116:119], a[108:111], v[116:119]
s_add_u32 s45, s45, 131072
v_add_f32 v58, v58, v59
v_add_f32 v60, v60, v61
v_add_f32 v62, v62, v63
v_mov_b32 v57, v56
v_mfma_f32_16x16x32_bf16 v[120:123], a[120:123], a[108:111], v[120:123]
v_mov_b32 v59, v58
v_mov_b32 v61, v60
v_mov_b32 v63, v62
v_mfma_f32_16x16x32_bf16 v[124:127], a[124:127], a[108:111], v[124:127]
s_waitcnt lgkmcnt(0)
v_permlane32_swap_b32 v56, v57
v_permlane32_swap_b32 v58, v59
v_mfma_f32_16x16x32_bf16 v[64:67], a[80:83], a[64:67], v[64:67]
s_mov_b32 m0, s49
ds_read_b64_tr_b16 a[112:113], v245 offset:8192
ds_read_b64_tr_b16 a[114:115], v245 offset:10240
v_mfma_f32_16x16x32_bf16 v[68:71], a[84:87], a[64:67], v[68:71]
buffer_load_dwordx4 v7, s[4:7], s44 offen offset:0 sc1 lds
v_permlane32_swap_b32 v60, v61
v_mfma_f32_16x16x32_bf16 v[72:75], a[88:91], a[64:67], v[72:75]
ds_read_b128 a[96:99], v244 offset:2048
ds_read_b128 a[100:103], v244 offset:2304
v_permlane32_swap_b32 v62, v63
v_mfma_f32_16x16x32_bf16 v[76:79], a[92:95], a[64:67], v[76:79]
ds_read_b64_tr_b16 a[116:117], v246 offset:8192
ds_read_b64_tr_b16 a[118:119], v246 offset:10240
v_add_f32 v56, v56, v57
v_add_f32 v58, v58, v59
v_mfma_f32_16x16x32_bf16 v[80:83], a[80:83], a[68:71], v[80:83]
buffer_load_dwordx4 v7, s[4:7], s44 offen offset:1024 sc1 lds
v_add_f32 v60, v60, v61
v_add_f32 v62, v62, v63
v_mfma_f32_16x16x32_bf16 v[84:87], a[84:87], a[68:71], v[84:87]
ds_read_b64_tr_b16 a[120:121], v247 offset:8192
ds_read_b64_tr_b16 a[122:123], v247 offset:10240
v_rcp_f32 v56, v56
v_mfma_f32_16x16x32_bf16 v[88:91], a[88:91], a[68:71], v[88:91]
ds_read_b128 a[104:107], v244 offset:6144
ds_read_b128 a[108:111], v244 offset:6400
v_rcp_f32 v58, v58
v_mfma_f32_16x16x32_bf16 v[92:95], a[92:95], a[68:71], v[92:95]
v_rcp_f32 v60, v60
v_rcp_f32 v62, v62
v_mfma_f32_16x16x32_bf16 v[96:99], a[80:83], a[72:75], v[96:99]
s_cmp_eq_u32 s46, 0
s_cselect_b32 s53, s56, s57
ds_read_b64_tr_b16 a[124:125], v248 offset:8192
ds_read_b64_tr_b16 a[126:127], v248 offset:10240
v_mfma_f32_16x16x32_bf16 v[100:103], a[84:87], a[72:75], v[100:103]
v_mul_f32 v160, v56, v160
v_mul_f32 v161, v56, v161
v_mul_f32 v162, v56, v162
v_mul_f32 v163, v56, v163
v_mfma_f32_16x16x32_bf16 v[104:107], a[88:91], a[72:75], v[104:107]
s_add_u32 s49, s46, s91
s_add_u32 s46, s46, s53
v_add_u32 v244, s53, v244
v_mul_f32 v168, v58, v168
v_mul_f32 v169, v58, v169
v_mfma_f32_16x16x32_bf16 v[108:111], a[92:95], a[72:75], v[108:111]
buffer_load_dwordx4 v7, s[4:7], s44 offen offset:2048 sc1 lds
v_mul_f32 v170, v58, v170
v_mul_f32 v171, v58, v171
v_cvt_pk_bf16_f32 v128, v160, v161
v_mfma_f32_16x16x32_bf16 v[112:115], a[80:83], a[76:79], v[112:115]
v_add_u32 v245, s53, v245
v_add_u32 v246, s53, v246
v_cvt_pk_bf16_f32 v130, v168, v169
v_cvt_pk_bf16_f32 v129, v162, v163
v_mfma_f32_16x16x32_bf16 v[116:119], a[84:87], a[76:79], v[116:119]
buffer_load_dwordx4 v7, s[4:7], s44 offen offset:3072 sc1 lds
v_cvt_pk_bf16_f32 v131, v170, v171
v_nop
v_mfma_f32_16x16x32_bf16 v[120:123], a[88:91], a[76:79], v[120:123]
v_add_u32 v247, s53, v247
v_add_u32 v248, s53, v248
v_permlane16_swap_b32 v128, v130
v_mfma_f32_16x16x32_bf16 v[124:127], a[92:95], a[76:79], v[124:127]
s_waitcnt vmcnt(24) lgkmcnt(0)
s_barrier
v_permlane16_swap_b32 v129, v131
v_mfma_f32_16x16x32_bf16 v[64:67], a[112:115], a[96:99], v[64:67]
s_add_u32 m0, s49, 16384
s_add_u32 s44, s44, 4096
ds_read_b64_tr_b16 a[80:81], v245 offset:0
ds_read_b64_tr_b16 a[82:83], v245 offset:2048
v_mfma_f32_16x16x32_bf16 v[68:71], a[116:119], a[96:99], v[68:71]
buffer_load_dwordx4 v240, s[8:11], s45 offen offset:0 lds
buffer_store_dwordx4 v[128:131], v7, s[20:23], s40 offen offset:0 sc1
v_mul_f32 v164, v56, v164
v_mfma_f32_16x16x32_bf16 v[72:75], a[120:123], a[96:99], v[72:75]
ds_read_b128 a[64:67], v244 offset:0
ds_read_b128 a[68:71], v244 offset:256
v_mul_f32 v165, v56, v165
v_mul_f32 v166, v56, v166
v_mfma_f32_16x16x32_bf16 v[76:79], a[124:127], a[96:99], v[76:79]
ds_read_b64_tr_b16 a[84:85], v246 offset:0
ds_read_b64_tr_b16 a[86:87], v246 offset:2048
v_mul_f32 v167, v56, v167
v_mul_f32 v172, v58, v172
v_mfma_f32_16x16x32_bf16 v[80:83], a[112:115], a[100:103], v[80:83]
buffer_load_dwordx4 v241, s[8:11], s45 offen offset:1024 lds
v_mul_f32 v173, v58, v173
v_mul_f32 v174, v58, v174
v_mul_f32 v175, v58, v175
v_mfma_f32_16x16x32_bf16 v[84:87], a[116:119], a[100:103], v[84:87]
ds_read_b64_tr_b16 a[88:89], v247 offset:0
ds_read_b64_tr_b16 a[90:91], v247 offset:2048
v_cvt_pk_bf16_f32 v136, v164, v165
v_cvt_pk_bf16_f32 v138, v172, v173
v_mfma_f32_16x16x32_bf16 v[88:91], a[120:123], a[100:103], v[88:91]
ds_read_b128 a[72:75], v244 offset:4096
ds_read_b128 a[76:79], v244 offset:4352
v_cvt_pk_bf16_f32 v137, v166, v167
v_cvt_pk_bf16_f32 v139, v174, v175
v_mfma_f32_16x16x32_bf16 v[92:95], a[124:127], a[100:103], v[92:95]
v_nop
v_permlane16_swap_b32 v136, v138
v_mfma_f32_16x16x32_bf16 v[96:99], a[112:115], a[104:107], v[96:99]
ds_read_b64_tr_b16 a[92:93], v248 offset:0
ds_read_b64_tr_b16 a[94:95], v248 offset:2048
v_permlane16_swap_b32 v137, v139
v_mfma_f32_16x16x32_bf16 v[100:103], a[116:119], a[104:107], v[100:103]
buffer_load_dwordx4 v242, s[8:11], s45 offen offset:2048 lds
buffer_store_dwordx4 v[136:139], v7, s[20:23], s40 offen offset:1024 sc1
v_mul_f32 v176, v60, v176
v_mfma_f32_16x16x32_bf16 v[104:107], a[120:123], a[104:107], v[104:107]
v_mul_f32 v177, v60, v177
v_mul_f32 v178, v60, v178
v_mul_f32 v179, v60, v179
v_mul_f32 v184, v62, v184
v_mfma_f32_16x16x32_bf16 v[108:111], a[124:127], a[104:107], v[108:111]
v_mul_f32 v185, v62, v185
v_mul_f32 v186, v62, v186
v_mul_f32 v187, v62, v187
v_cvt_pk_bf16_f32 v144, v176, v177
v_mfma_f32_16x16x32_bf16 v[112:115], a[112:115], a[108:111], v[112:115]
buffer_load_dwordx4 v243, s[8:11], s45 offen offset:3072 lds
v_cvt_pk_bf16_f32 v146, v184, v185
v_cvt_pk_bf16_f32 v145, v178, v179
v_cvt_pk_bf16_f32 v147, v186, v187
v_mfma_f32_16x16x32_bf16 v[116:119], a[116:119], a[108:111], v[116:119]
s_add_u32 s45, s45, 131072
v_nop
v_permlane16_swap_b32 v144, v146
v_mfma_f32_16x16x32_bf16 v[120:123], a[120:123], a[108:111], v[120:123]
v_permlane16_swap_b32 v145, v147
v_mfma_f32_16x16x32_bf16 v[124:127], a[124:127], a[108:111], v[124:127]
s_waitcnt lgkmcnt(0)
buffer_store_dwordx4 v[144:147], v7, s[20:23], s41 offen offset:0 sc1
v_mul_f32 v180, v60, v180
v_mul_f32 v181, v60, v181
v_mfma_f32_16x16x32_bf16 v[64:67], a[80:83], a[64:67], v[64:67]
s_mov_b32 m0, s49
ds_read_b64_tr_b16 a[112:113], v245 offset:8192
ds_read_b64_tr_b16 a[114:115], v245 offset:10240
v_mul_f32 v182, v60, v182
v_mfma_f32_16x16x32_bf16 v[68:71], a[84:87], a[64:67], v[68:71]
buffer_load_dwordx4 v7, s[4:7], s44 offen offset:0 sc1 lds
v_mul_f32 v183, v60, v183
v_mul_f32 v188, v62, v188
v_mul_f32 v189, v62, v189
v_mfma_f32_16x16x32_bf16 v[72:75], a[88:91], a[64:67], v[72:75]
ds_read_b128 a[96:99], v244 offset:2048
ds_read_b128 a[100:103], v244 offset:2304
v_mul_f32 v190, v62, v190
v_mul_f32 v191, v62, v191
v_mfma_f32_16x16x32_bf16 v[76:79], a[92:95], a[64:67], v[76:79]
ds_read_b64_tr_b16 a[116:117], v246 offset:8192
ds_read_b64_tr_b16 a[118:119], v246 offset:10240
v_cvt_pk_bf16_f32 v152, v180, v181
v_cvt_pk_bf16_f32 v154, v188, v189
v_mfma_f32_16x16x32_bf16 v[80:83], a[80:83], a[68:71], v[80:83]
buffer_load_dwordx4 v7, s[4:7], s44 offen offset:1024 sc1 lds
v_cvt_pk_bf16_f32 v153, v182, v183
v_cvt_pk_bf16_f32 v155, v190, v191
v_nop
v_mfma_f32_16x16x32_bf16 v[84:87], a[84:87], a[68:71], v[84:87]
ds_read_b64_tr_b16 a[120:121], v247 offset:8192
ds_read_b64_tr_b16 a[122:123], v247 offset:10240
v_permlane16_swap_b32 v152, v154
v_mfma_f32_16x16x32_bf16 v[88:91], a[88:91], a[68:71], v[88:91]
ds_read_b128 a[104:107], v244 offset:6144
ds_read_b128 a[108:111], v244 offset:6400
v_permlane16_swap_b32 v153, v155
v_mfma_f32_16x16x32_bf16 v[92:95], a[92:95], a[68:71], v[92:95]
buffer_store_dwordx4 v[152:155], v7, s[20:23], s41 offen offset:1024 sc1
v_mfma_f32_16x16x32_bf16 v[96:99], a[80:83], a[72:75], v[96:99]
s_cmp_eq_u32 s46, 0
s_cselect_b32 s53, s56, s57
ds_read_b64_tr_b16 a[124:125], v248 offset:8192
ds_read_b64_tr_b16 a[126:127], v248 offset:10240
v_mfma_f32_16x16x32_bf16 v[100:103], a[84:87], a[72:75], v[100:103]
v_mfma_f32_16x16x32_bf16 v[104:107], a[88:91], a[72:75], v[104:107]
s_add_u32 s49, s46, s91
s_add_u32 s46, s46, s53
v_add_u32 v244, s53, v244
v_mfma_f32_16x16x32_bf16 v[108:111], a[92:95], a[72:75], v[108:111]
buffer_load_dwordx4 v7, s[4:7], s44 offen offset:2048 sc1 lds
v_mfma_f32_16x16x32_bf16 v[112:115], a[80:83], a[76:79], v[112:115]
v_add_u32 v245, s53, v245
v_add_u32 v246, s53, v246
v_mfma_f32_16x16x32_bf16 v[116:119], a[84:87], a[76:79], v[116:119]
buffer_load_dwordx4 v7, s[4:7], s44 offen offset:3072 sc1 lds
v_mfma_f32_16x16x32_bf16 v[120:123], a[88:91], a[76:79], v[120:123]
v_add_u32 v247, s53, v247
v_add_u32 v248, s53, v248
v_mfma_f32_16x16x32_bf16 v[124:127], a[92:95], a[76:79], v[124:127]
s_waitcnt vmcnt(28) lgkmcnt(0)
s_barrier
v_mfma_f32_16x16x32_bf16 v[64:67], a[112:115], a[96:99], v[64:67]
s_add_u32 m0, s49, 16384
s_add_u32 s44, s44, 4096
ds_read_b64_tr_b16 a[80:81], v245 offset:0
ds_read_b64_tr_b16 a[82:83], v245 offset:2048
v_mfma_f32_16x16x32_bf16 v[68:71], a[116:119], a[96:99], v[68:71]
buffer_load_dwordx4 v240, s[8:11], s45 offen offset:0 lds
v_mfma_f32_16x16x32_bf16 v[72:75], a[120:123], a[96:99], v[72:75]
ds_read_b128 a[64:67], v244 offset:0
ds_read_b128 a[68:71], v244 offset:256
v_mfma_f32_16x16x32_bf16 v[76:79], a[124:127], a[96:99], v[76:79]
ds_read_b64_tr_b16 a[84:85], v246 offset:0
ds_read_b64_tr_b16 a[86:87], v246 offset:2048
v_mfma_f32_16x16x32_bf16 v[80:83], a[112:115], a[100:103], v[80:83]
buffer_load_dwordx4 v241, s[8:11], s45 offen offset:1024 lds
v_mfma_f32_16x16x32_bf16 v[84:87], a[116:119], a[100:103], v[84:87]
ds_read_b64_tr_b16 a[88:89], v247 offset:0
ds_read_b64_tr_b16 a[90:91], v247 offset:2048
v_mfma_f32_16x16x32_bf16 v[88:91], a[120:123], a[100:103], v[88:91]
ds_read_b128 a[72:75], v244 offset:4096
ds_read_b128 a[76:79], v244 offset:4352
v_mfma_f32_16x16x32_bf16 v[92:95], a[124:127], a[100:103], v[92:95]
v_mfma_f32_16x16x32_bf16 v[96:99], a[112:115], a[104:107], v[96:99]
ds_read_b64_tr_b16 a[92:93], v248 offset:0
ds_read_b64_tr_b16 a[94:95], v248 offset:2048
v_mfma_f32_16x16x32_bf16 v[100:103], a[116:119], a[104:107], v[100:103]
buffer_load_dwordx4 v242, s[8:11], s45 offen offset:2048 lds
v_mfma_f32_16x16x32_bf16 v[104:107], a[120:123], a[104:107], v[104:107]
v_mfma_f32_16x16x32_bf16 v[108:111], a[124:127], a[104:107], v[108:111]
v_mfma_f32_16x16x32_bf16 v[112:115], a[112:115], a[108:111], v[112:115]
buffer_load_dwordx4 v243, s[8:11], s45 offen offset:3072 lds
v_mfma_f32_16x16x32_bf16 v[116:119], a[116:119], a[108:111], v[116:119]
s_add_u32 s45, s45, 131072
v_mfma_f32_16x16x32_bf16 v[120:123], a[120:123], a[108:111], v[120:123]
v_mfma_f32_16x16x32_bf16 v[124:127], a[124:127], a[108:111], v[124:127]
s_waitcnt lgkmcnt(0)
v_mfma_f32_16x16x32_bf16 v[64:67], a[80:83], a[64:67], v[64:67]
s_mov_b32 m0, s49
ds_read_b64_tr_b16 a[112:113], v245 offset:8192
ds_read_b64_tr_b16 a[114:115], v245 offset:10240
v_mfma_f32_16x16x32_bf16 v[68:71], a[84:87], a[64:67], v[68:71]
buffer_load_dwordx4 v7, s[4:7], s44 offen offset:0 sc1 lds
v_mfma_f32_16x16x32_bf16 v[72:75], a[88:91], a[64:67], v[72:75]
ds_read_b128 a[96:99], v244 offset:2048
ds_read_b128 a[100:103], v244 offset:2304
v_mfma_f32_16x16x32_bf16 v[76:79], a[92:95], a[64:67], v[76:79]
ds_read_b64_tr_b16 a[116:117], v246 offset:8192
ds_read_b64_tr_b16 a[118:119], v246 offset:10240
v_mfma_f32_16x16x32_bf16 v[80:83], a[80:83], a[68:71], v[80:83]
buffer_load_dwordx4 v7, s[4:7], s44 offen offset:1024 sc1 lds
v_mfma_f32_16x16x32_bf16 v[84:87], a[84:87], a[68:71], v[84:87]
ds_read_b64_tr_b16 a[120:121], v247 offset:8192
ds_read_b64_tr_b16 a[122:123], v247 offset:10240
v_mfma_f32_16x16x32_bf16 v[88:91], a[88:91], a[68:71], v[88:91]
ds_read_b128 a[104:107], v244 offset:6144
ds_read_b128 a[108:111], v244 offset:6400
v_mfma_f32_16x16x32_bf16 v[92:95], a[92:95], a[68:71], v[92:95]
v_mfma_f32_16x16x32_bf16 v[96:99], a[80:83], a[72:75], v[96:99]
s_cmp_eq_u32 s46, 0
s_cselect_b32 s53, s56, s57
ds_read_b64_tr_b16 a[124:125], v248 offset:8192
ds_read_b64_tr_b16 a[126:127], v248 offset:10240
v_mfma_f32_16x16x32_bf16 v[100:103], a[84:87], a[72:75], v[100:103]
v_mfma_f32_16x16x32_bf16 v[104:107], a[88:91], a[72:75], v[104:107]
s_add_u32 s49, s46, s91
s_add_u32 s46, s46, s53
v_add_u32 v244, s53, v244
v_mfma_f32_16x16x32_bf16 v[108:111], a[92:95], a[72:75], v[108:111]
buffer_load_dwordx4 v7, s[4:7], s44 offen offset:2048 sc1 lds
v_mfma_f32_16x16x32_bf16 v[112:115], a[80:83], a[76:79], v[112:115]
v_add_u32 v245, s53, v245
v_add_u32 v246, s53, v246
v_mfma_f32_16x16x32_bf16 v[116:119], a[84:87], a[76:79], v[116:119]
buffer_load_dwordx4 v7, s[4:7], s44 offen offset:3072 sc1 lds
v_mfma_f32_16x16x32_bf16 v[120:123], a[88:91], a[76:79], v[120:123]
v_add_u32 v247, s53, v247
v_add_u32 v248, s53, v248
v_mfma_f32_16x16x32_bf16 v[124:127], a[92:95], a[76:79], v[124:127]
s_waitcnt vmcnt(28) lgkmcnt(0)
s_barrier
v_mfma_f32_16x16x32_bf16 v[64:67], a[112:115], a[96:99], v[64:67]
s_add_u32 m0, s49, 16384
s_add_u32 s44, s44, 4096
ds_read_b64_tr_b16 a[80:81], v245 offset:0
ds_read_b64_tr_b16 a[82:83], v245 offset:2048
v_mfma_f32_16x16x32_bf16 v[68:71], a[116:119], a[96:99], v[68:71]
buffer_load_dwordx4 v240, s[8:11], s45 offen offset:0 lds
v_mfma_f32_16x16x32_bf16 v[72:75], a[120:123], a[96:99], v[72:75]
ds_read_b128 a[64:67], v244 offset:0
ds_read_b128 a[68:71], v244 offset:256
v_mfma_f32_16x16x32_bf16 v[76:79], a[124:127], a[96:99], v[76:79]
ds_read_b64_tr_b16 a[84:85], v246 offset:0
ds_read_b64_tr_b16 a[86:87], v246 offset:2048
v_mfma_f32_16x16x32_bf16 v[80:83], a[112:115], a[100:103], v[80:83]
buffer_load_dwordx4 v241, s[8:11], s45 offen offset:1024 lds
v_mfma_f32_16x16x32_bf16 v[84:87], a[116:119], a[100:103], v[84:87]
ds_read_b64_tr_b16 a[88:89], v247 offset:0
ds_read_b64_tr_b16 a[90:91], v247 offset:2048
v_mfma_f32_16x16x32_bf16 v[88:91], a[120:123], a[100:103], v[88:91]
ds_read_b128 a[72:75], v244 offset:4096
ds_read_b128 a[76:79], v244 offset:4352
v_mfma_f32_16x16x32_bf16 v[92:95], a[124:127], a[100:103], v[92:95]
v_mfma_f32_16x16x32_bf16 v[96:99], a[112:115], a[104:107], v[96:99]
ds_read_b64_tr_b16 a[92:93], v248 offset:0
ds_read_b64_tr_b16 a[94:95], v248 offset:2048
v_mfma_f32_16x16x32_bf16 v[100:103], a[116:119], a[104:107], v[100:103]
buffer_load_dwordx4 v242, s[8:11], s45 offen offset:2048 lds
v_mfma_f32_16x16x32_bf16 v[104:107], a[120:123], a[104:107], v[104:107]
v_mfma_f32_16x16x32_bf16 v[108:111], a[124:127], a[104:107], v[108:111]
v_mfma_f32_16x16x32_bf16 v[112:115], a[112:115], a[108:111], v[112:115]
buffer_load_dwordx4 v243, s[8:11], s45 offen offset:3072 lds
v_mfma_f32_16x16x32_bf16 v[116:119], a[116:119], a[108:111], v[116:119]
s_add_u32 s45, s45, 131072
v_mfma_f32_16x16x32_bf16 v[120:123], a[120:123], a[108:111], v[120:123]
v_mfma_f32_16x16x32_bf16 v[124:127], a[124:127], a[108:111], v[124:127]
s_waitcnt lgkmcnt(0)
v_mfma_f32_16x16x32_bf16 v[64:67], a[80:83], a[64:67], v[64:67]
s_mov_b32 m0, s49
ds_read_b64_tr_b16 a[112:113], v245 offset:8192
ds_read_b64_tr_b16 a[114:115], v245 offset:10240
v_mfma_f32_16x16x32_bf16 v[68:71], a[84:87], a[64:67], v[68:71]
buffer_load_dwordx4 v7, s[4:7], s44 offen offset:0 sc1 lds
v_mfma_f32_16x16x32_bf16 v[72:75], a[88:91], a[64:67], v[72:75]
ds_read_b128 a[96:99], v244 offset:2048
ds_read_b128 a[100:103], v244 offset:2304
v_mfma_f32_16x16x32_bf16 v[76:79], a[92:95], a[64:67], v[76:79]
ds_read_b64_tr_b16 a[116:117], v246 offset:8192
ds_read_b64_tr_b16 a[118:119], v246 offset:10240
v_mfma_f32_16x16x32_bf16 v[80:83], a[80:83], a[68:71], v[80:83]
buffer_load_dwordx4 v7, s[4:7], s44 offen offset:1024 sc1 lds
v_mfma_f32_16x16x32_bf16 v[84:87], a[84:87], a[68:71], v[84:87]
ds_read_b64_tr_b16 a[120:121], v247 offset:8192
ds_read_b64_tr_b16 a[122:123], v247 offset:10240
v_mfma_f32_16x16x32_bf16 v[88:91], a[88:91], a[68:71], v[88:91]
ds_read_b128 a[104:107], v244 offset:6144
ds_read_b128 a[108:111], v244 offset:6400
v_mfma_f32_16x16x32_bf16 v[92:95], a[92:95], a[68:71], v[92:95]
v_mfma_f32_16x16x32_bf16 v[96:99], a[80:83], a[72:75], v[96:99]
s_cmp_eq_u32 s46, 0
s_cselect_b32 s53, s56, s57
ds_read_b64_tr_b16 a[124:125], v248 offset:8192
ds_read_b64_tr_b16 a[126:127], v248 offset:10240
v_mfma_f32_16x16x32_bf16 v[100:103], a[84:87], a[72:75], v[100:103]
v_mfma_f32_16x16x32_bf16 v[104:107], a[88:91], a[72:75], v[104:107]
s_add_u32 s49, s46, s91
s_add_u32 s46, s46, s53
v_add_u32 v244, s53, v244
v_mfma_f32_16x16x32_bf16 v[108:111], a[92:95], a[72:75], v[108:111]
buffer_load_dwordx4 v7, s[4:7], s44 offen offset:2048 sc1 lds
v_mfma_f32_16x16x32_bf16 v[112:115], a[80:83], a[76:79], v[112:115]
v_add_u32 v245, s53, v245
v_add_u32 v246, s53, v246
v_mfma_f32_16x16x32_bf16 v[116:119], a[84:87], a[76:79], v[116:119]
buffer_load_dwordx4 v7, s[4:7], s44 offen offset:3072 sc1 lds
v_mfma_f32_16x16x32_bf16 v[120:123], a[88:91], a[76:79], v[120:123]
v_add_u32 v247, s53, v247
v_add_u32 v248, s53, v248
v_mfma_f32_16x16x32_bf16 v[124:127], a[92:95], a[76:79], v[124:127]
s_waitcnt vmcnt(18)
s_waitcnt vmcnt(28) lgkmcnt(0)
s_barrier
v_mfma_f32_16x16x32_bf16 v[64:67], a[112:115], a[96:99], v[64:67]
s_add_u32 m0, s49, 16384
s_add_u32 s44, s44, 4096
ds_read_b64_tr_b16 a[80:81], v245 offset:0
ds_read_b64_tr_b16 a[82:83], v245 offset:2048
v_mfma_f32_16x16x32_bf16 v[68:71], a[116:119], a[96:99], v[68:71]
buffer_load_dwordx4 v240, s[8:11], s45 offen offset:0 lds
v_mfma_f32_16x16x32_bf16 v[72:75], a[120:123], a[96:99], v[72:75]
ds_read_b128 a[64:67], v244 offset:0
ds_read_b128 a[68:71], v244 offset:256
v_mfma_f32_16x16x32_bf16 v[76:79], a[124:127], a[96:99], v[76:79]
ds_read_b64_tr_b16 a[84:85], v246 offset:0
ds_read_b64_tr_b16 a[86:87], v246 offset:2048
v_mfma_f32_16x16x32_bf16 v[80:83], a[112:115], a[100:103], v[80:83]
buffer_load_dwordx4 v241, s[8:11], s45 offen offset:1024 lds
v_mfma_f32_16x16x32_bf16 v[84:87], a[116:119], a[100:103], v[84:87]
ds_read_b64_tr_b16 a[88:89], v247 offset:0
ds_read_b64_tr_b16 a[90:91], v247 offset:2048
v_mfma_f32_16x16x32_bf16 v[88:91], a[120:123], a[100:103], v[88:91]
ds_read_b128 a[72:75], v244 offset:4096
ds_read_b128 a[76:79], v244 offset:4352
v_mfma_f32_16x16x32_bf16 v[92:95], a[124:127], a[100:103], v[92:95]
v_mfma_f32_16x16x32_bf16 v[96:99], a[112:115], a[104:107], v[96:99]
ds_read_b64_tr_b16 a[92:93], v248 offset:0
ds_read_b64_tr_b16 a[94:95], v248 offset:2048
v_mfma_f32_16x16x32_bf16 v[100:103], a[116:119], a[104:107], v[100:103]
buffer_load_dwordx4 v242, s[8:11], s45 offen offset:2048 lds
v_mfma_f32_16x16x32_bf16 v[104:107], a[120:123], a[104:107], v[104:107]
s_nop 0
v_or_b32 v0, 4, v11
v_mfma_f32_16x16x32_bf16 v[108:111], a[124:127], a[104:107], v[108:111]
v_mfma_f32_16x16x32_bf16 v[112:115], a[112:115], a[108:111], v[112:115]
buffer_load_dwordx4 v243, s[8:11], s45 offen offset:3072 lds
v_mfma_f32_16x16x32_bf16 v[116:119], a[116:119], a[108:111], v[116:119]
s_add_u32 s45, s45, 131072
v_mfma_f32_16x16x32_bf16 v[120:123], a[120:123], a[108:111], v[120:123]
buffer_store_dword v0, v6, s[60:63], 0 offen sc1
v_mfma_f32_16x16x32_bf16 v[124:127], a[124:127], a[108:111], v[124:127]
s_waitcnt lgkmcnt(0)
v_mfma_f32_16x16x32_bf16 v[64:67], a[80:83], a[64:67], v[64:67]
s_mov_b32 m0, s49
ds_read_b64_tr_b16 a[112:113], v245 offset:8192
ds_read_b64_tr_b16 a[114:115], v245 offset:10240
v_mfma_f32_16x16x32_bf16 v[68:71], a[84:87], a[64:67], v[68:71]
buffer_load_dwordx4 v7, s[4:7], s44 offen offset:0 sc1 lds
v_mfma_f32_16x16x32_bf16 v[72:75], a[88:91], a[64:67], v[72:75]
ds_read_b128 a[96:99], v244 offset:2048
ds_read_b128 a[100:103], v244 offset:2304
v_mfma_f32_16x16x32_bf16 v[76:79], a[92:95], a[64:67], v[76:79]
ds_read_b64_tr_b16 a[116:117], v246 offset:8192
ds_read_b64_tr_b16 a[118:119], v246 offset:10240
v_mfma_f32_16x16x32_bf16 v[80:83], a[80:83], a[68:71], v[80:83]
buffer_load_dwordx4 v7, s[4:7], s44 offen offset:1024 sc1 lds
v_mfma_f32_16x16x32_bf16 v[84:87], a[84:87], a[68:71], v[84:87]
ds_read_b64_tr_b16 a[120:121], v247 offset:8192
ds_read_b64_tr_b16 a[122:123], v247 offset:10240
v_mfma_f32_16x16x32_bf16 v[88:91], a[88:91], a[68:71], v[88:91]
ds_read_b128 a[104:107], v244 offset:6144
ds_read_b128 a[108:111], v244 offset:6400
v_mfma_f32_16x16x32_bf16 v[92:95], a[92:95], a[68:71], v[92:95]
.Lpad19:
s_cbranch_execz .Ltch9
v_mfma_f32_16x16x32_bf16 v[96:99], a[80:83], a[72:75], v[96:99]
s_cmp_eq_u32 s46, 0
s_cselect_b32 s53, s56, s57
buffer_load_dword v35, v4, s[96:99], 0 offen
ds_read_b64_tr_b16 a[124:125], v248 offset:8192
ds_read_b64_tr_b16 a[126:127], v248 offset:10240
v_mfma_f32_16x16x32_bf16 v[100:103], a[84:87], a[72:75], v[100:103]
v_mfma_f32_16x16x32_bf16 v[104:107], a[88:91], a[72:75], v[104:107]
s_add_u32 s49, s46, s91
s_add_u32 s46, s46, s53
v_add_u32 v244, s53, v244
v_mfma_f32_16x16x32_bf16 v[108:111], a[92:95], a[72:75], v[108:111]
buffer_load_dwordx4 v7, s[4:7], s44 offen offset:2048 sc1 lds
v_mfma_f32_16x16x32_bf16 v[112:115], a[80:83], a[76:79], v[112:115]
v_add_u32 v245, s53, v245
v_add_u32 v246, s53, v246
v_mfma_f32_16x16x32_bf16 v[116:119], a[84:87], a[76:79], v[116:119]
buffer_load_dwordx4 v7, s[4:7], s44 offen offset:3072 sc1 lds
v_mfma_f32_16x16x32_bf16 v[120:123], a[88:91], a[76:79], v[120:123]
v_add_u32 v247, s53, v247
v_add_u32 v248, s53, v248
v_mfma_f32_16x16x32_bf16 v[124:127], a[92:95], a[76:79], v[124:127]
s_waitcnt vmcnt(26) lgkmcnt(0)
s_barrier
s_mov_b64 exec, 0
s_cmp_eq_u32 s3, 0
s_cbranch_scc1 .Lpad20
s_cmp_eq_u32 s3, 1
s_cbranch_scc1 .L5odd_pad
s_cmp_eq_u32 s3, 2
s_cbranch_scc1 .Ltch10
s_branch .Lpad21
.Ltch10:
s_mov_b64 exec, -1
.L5b:
v_mfma_f32_16x16x32_bf16 v[64:67], a[112:115], a[96:99], v[64:67]
s_add_u32 m0, s49, 16384
s_add_u32 s44, s44, 4096
ds_read_b64_tr_b16 a[80:81], v245 offset:0
ds_read_b64_tr_b16 a[82:83], v245 offset:2048
v_mfma_f32_16x16x32_bf16 v[68:71], a[116:119], a[96:99], v[68:71]
buffer_load_dwordx4 v240, s[8:11], s45 offen offset:0 lds
v_mfma_f32_16x16x32_bf16 v[72:75], a[120:123], a[96:99], v[72:75]
ds_read_b128 a[64:67], v244 offset:0
ds_read_b128 a[68:71], v244 offset:256
v_mfma_f32_16x16x32_bf16 v[76:79], a[124:127], a[96:99], v[76:79]
ds_read_b64_tr_b16 a[84:85], v246 offset:0
ds_read_b64_tr_b16 a[86:87], v246 offset:2048
v_mfma_f32_16x16x32_bf16 v[80:83], a[112:115], a[100:103], v[80:83]
buffer_load_dwordx4 v241, s[8:11], s45 offen offset:1024 lds
v_mfma_f32_16x16x32_bf16 v[84:87], a[116:119], a[100:103], v[84:87]
ds_read_b64_tr_b16 a[88:89], v247 offset:0
ds_read_b64_tr_b16 a[90:91], v247 offset:2048
v_mfma_f32_16x16x32_bf16 v[88:91], a[120:123], a[100:103], v[88:91]
ds_read_b128 a[72:75], v244 offset:4096
ds_read_b128 a[76:79], v244 offset:4352
v_mfma_f32_16x16x32_bf16 v[92:95], a[124:127], a[100:103], v[92:95]
v_mfma_f32_16x16x32_bf16 v[96:99], a[112:115], a[104:107], v[96:99]
ds_read_b64_tr_b16 a[92:93], v248 offset:0
ds_read_b64_tr_b16 a[94:95], v248 offset:2048
v_mfma_f32_16x16x32_bf16 v[100:103], a[116:119], a[104:107], v[100:103]
buffer_load_dwordx4 v242, s[8:11], s45 offen offset:2048 lds
v_mfma_f32_16x16x32_bf16 v[104:107], a[120:123], a[104:107], v[104:107]
v_mfma_f32_16x16x32_bf16 v[108:111], a[124:127], a[104:107], v[108:111]
v_mfma_f32_16x16x32_bf16 v[112:115], a[112:115], a[108:111], v[112:115]
buffer_load_dwordx4 v243, s[8:11], s45 offen offset:3072 lds
v_mfma_f32_16x16x32_bf16 v[116:119], a[116:119], a[108:111], v[116:119]
s_add_u32 s45, s45, 131072
v_mfma_f32_16x16x32_bf16 v[120:123], a[120:123], a[108:111], v[120:123]
v_mfma_f32_16x16x32_bf16 v[124:127], a[124:127], a[108:111], v[124:127]
s_waitcnt lgkmcnt(0)
v_mfma_f32_16x16x32_bf16 v[64:67], a[80:83], a[64:67], v[64:67]
s_mov_b32 m0, s49
ds_read_b64_tr_b16 a[112:113], v245 offset:8192
ds_read_b64_tr_b16 a[114:115], v245 offset:10240
v_mfma_f32_16x16x32_bf16 v[68:71], a[84:87], a[64:67], v[68:71]
buffer_load_dwordx4 v7, s[4:7], s44 offen offset:0 sc1 lds
v_mfma_f32_16x16x32_bf16 v[72:75], a[88:91], a[64:67], v[72:75]
ds_read_b128 a[96:99], v244 offset:2048
ds_read_b128 a[100:103], v244 offset:2304
v_mfma_f32_16x16x32_bf16 v[76:79], a[92:95], a[64:67], v[76:79]
ds_read_b64_tr_b16 a[116:117], v246 offset:8192
ds_read_b64_tr_b16 a[118:119], v246 offset:10240
v_mfma_f32_16x16x32_bf16 v[80:83], a[80:83], a[68:71], v[80:83]
buffer_load_dwordx4 v7, s[4:7], s44 offen offset:1024 sc1 lds
v_mfma_f32_16x16x32_bf16 v[84:87], a[84:87], a[68:71], v[84:87]
ds_read_b64_tr_b16 a[120:121], v247 offset:8192
ds_read_b64_tr_b16 a[122:123], v247 offset:10240
v_mfma_f32_16x16x32_bf16 v[88:91], a[88:91], a[68:71], v[88:91]
ds_read_b128 a[104:107], v244 offset:6144
ds_read_b128 a[108:111], v244 offset:6400
v_mfma_f32_16x16x32_bf16 v[92:95], a[92:95], a[68:71], v[92:95]
v_mfma_f32_16x16x32_bf16 v[96:99], a[80:83], a[72:75], v[96:99]
s_cmp_eq_u32 s46, 0
s_cselect_b32 s53, s56, s57
ds_read_b64_tr_b16 a[124:125], v248 offset:8192
ds_read_b64_tr_b16 a[126:127], v248 offset:10240
v_mfma_f32_16x16x32_bf16 v[100:103], a[84:87], a[72:75], v[100:103]
v_mfma_f32_16x16x32_bf16 v[104:107], a[88:91], a[72:75], v[104:107]
s_add_u32 s49, s46, s91
s_add_u32 s46, s46, s53
v_add_u32 v244, s53, v244
v_mfma_f32_16x16x32_bf16 v[108:111], a[92:95], a[72:75], v[108:111]
buffer_load_dwordx4 v7, s[4:7], s44 offen offset:2048 sc1 lds
v_mfma_f32_16x16x32_bf16 v[112:115], a[80:83], a[76:79], v[112:115]
s_sub_u32 s50, s50, 1
v_add_u32 v245, s53, v245
v_add_u32 v246, s53, v246
v_mfma_f32_16x16x32_bf16 v[116:119], a[84:87], a[76:79], v[116:119]
buffer_load_dwordx4 v7, s[4:7], s44 offen offset:3072 sc1 lds
v_mfma_f32_16x16x32_bf16 v[120:123], a[88:91], a[76:79], v[120:123]
v_add_u32 v247, s53, v247
v_add_u32 v248, s53, v248
v_mfma_f32_16x16x32_bf16 v[124:127], a[92:95], a[76:79], v[124:127]
s_waitcnt vmcnt(24) lgkmcnt(0)
s_barrier
s_cbranch_scc0 .L5b
s_cmp_eq_u32 s55, 0
s_cbranch_scc0 .L5odd
v_mfma_f32_16x16x32_bf16 v[64:67], a[112:115], a[96:99], v[64:67]
ds_read_b64_tr_b16 a[80:81], v245 offset:0
ds_read_b64_tr_b16 a[82:83], v245 offset:2048
v_mfma_f32_16x16x32_bf16 v[68:71], a[116:119], a[96:99], v[68:71]
buffer_load_dword v250, v249, s[60:63], s90 offen sc1
v_and_b32 v168, 15, v1
v_mfma_f32_16x16x32_bf16 v[72:75], a[120:123], a[96:99], v[72:75]
ds_read_b128 a[64:67], v244 offset:0
ds_read_b128 a[68:71], v244 offset:256
v_mfma_f32_16x16x32_bf16 v[76:79], a[124:127], a[96:99], v[76:79]
ds_read_b64_tr_b16 a[84:85], v246 offset:0
ds_read_b64_tr_b16 a[86:87], v246 offset:2048
v_mfma_f32_16x16x32_bf16 v[80:83], a[112:115], a[100:103], v[80:83]
s_mov_b32 s32, 4096
v_lshrrev_b32 v169, 4, v1
v_lshlrev_b32 v169, 2, v169
v_mfma_f32_16x16x32_bf16 v[84:87], a[116:119], a[100:103], v[84:87]
ds_read_b64_tr_b16 a[88:89], v247 offset:0
ds_read_b64_tr_b16 a[90:91], v247 offset:2048
v_mfma_f32_16x16x32_bf16 v[88:91], a[120:123], a[100:103], v[88:91]
ds_read_b128 a[72:75], v244 offset:4096
ds_read_b128 a[76:79], v244 offset:4352
v_mfma_f32_16x16x32_bf16 v[92:95], a[124:127], a[100:103], v[92:95]
v_sub_u32 v168, v168, v169
v_mov_b32 v169, 0x3f80
v_mfma_f32_16x16x32_bf16 v[96:99], a[112:115], a[104:107], v[96:99]
ds_read_b64_tr_b16 a[92:93], v248 offset:0
ds_read_b64_tr_b16 a[94:95], v248 offset:2048
v_mfma_f32_16x16x32_bf16 v[100:103], a[116:119], a[104:107], v[100:103]
v_mov_b32 v170, 0x3f800000
v_cmp_eq_u32 s[24:25], 0, v168
v_mfma_f32_16x16x32_bf16 v[104:107], a[120:123], a[104:107], v[104:107]
v_cmp_eq_u32 s[26:27], 1, v168
v_cmp_eq_u32 s[28:29], 2, v168
v_mfma_f32_16x16x32_bf16 v[108:111], a[124:127], a[104:107], v[108:111]
v_cmp_eq_u32 s[30:31], 3, v168
v_cndmask_b32 v160, 0, v169, s[24:25]
v_mfma_f32_16x16x32_bf16 v[112:115], a[112:115], a[108:111], v[112:115]
v_cndmask_b32 v161, 0, v169, s[28:29]
v_cndmask_b32 v160, v160, v170, s[26:27]
v_mfma_f32_16x16x32_bf16 v[116:119], a[116:119], a[108:111], v[116:119]
v_cndmask_b32 v161, v161, v170, s[30:31]
v_mov_b32 v162, 0
v_mfma_f32_16x16x32_bf16 v[120:123], a[120:123], a[108:111], v[120:123]
v_mov_b32 v163, 0
v_mov_b32 v164, 0
v_mfma_f32_16x16x32_bf16 v[124:127], a[124:127], a[108:111], v[124:127]
s_waitcnt lgkmcnt(0)
v_mov_b32 v165, 0
v_mov_b32 v166, v160
v_mfma_f32_16x16x32_bf16 v[64:67], a[80:83], a[64:67], v[64:67]
ds_read_b64_tr_b16 a[112:113], v245 offset:8192
ds_read_b64_tr_b16 a[114:115], v245 offset:10240
v_mfma_f32_16x16x32_bf16 v[68:71], a[84:87], a[64:67], v[68:71]
v_mov_b32 v167, v161
v_mfma_f32_16x16x32_bf16 v[72:75], a[88:91], a[64:67], v[72:75]
ds_read_b128 a[96:99], v244 offset:2048
ds_read_b128 a[100:103], v244 offset:2304
v_mfma_f32_16x16x32_bf16 v[76:79], a[92:95], a[64:67], v[76:79]
ds_read_b64_tr_b16 a[116:117], v246 offset:8192
ds_read_b64_tr_b16 a[118:119], v246 offset:10240
v_mfma_f32_16x16x32_bf16 v[80:83], a[80:83], a[68:71], v[80:83]
v_mfma_f32_16x16x32_bf16 v[84:87], a[84:87], a[68:71], v[84:87]
ds_read_b64_tr_b16 a[120:121], v247 offset:8192
ds_read_b64_tr_b16 a[122:123], v247 offset:10240
v_mfma_f32_16x16x32_bf16 v[88:91], a[88:91], a[68:71], v[88:91]
ds_read_b128 a[104:107], v244 offset:6144
ds_read_b128 a[108:111], v244 offset:6400
v_mfma_f32_16x16x32_bf16 v[92:95], a[92:95], a[68:71], v[92:95]
v_mfma_f32_16x16x32_bf16 v[96:99], a[80:83], a[72:75], v[96:99]
s_cmp_eq_u32 s46, 0
s_cselect_b32 s53, s56, s57
ds_read_b64_tr_b16 a[124:125], v248 offset:8192
ds_read_b64_tr_b16 a[126:127], v248 offset:10240
v_mfma_f32_16x16x32_bf16 v[100:103], a[84:87], a[72:75], v[100:103]
v_mfma_f32_16x16x32_bf16 v[104:107], a[88:91], a[72:75], v[104:107]
s_add_u32 s49, s46, s91
s_add_u32 s46, s46, s53
v_add_u32 v244, s53, v244
v_mfma_f32_16x16x32_bf16 v[108:111], a[92:95], a[72:75], v[108:111]
v_mfma_f32_16x16x32_bf16 v[112:115], a[80:83], a[76:79], v[112:115]
v_add_u32 v245, s53, v245
v_add_u32 v246, s53, v246
v_mfma_f32_16x16x32_bf16 v[116:119], a[84:87], a[76:79], v[116:119]
v_mfma_f32_16x16x32_bf16 v[120:123], a[88:91], a[76:79], v[120:123]
v_add_u32 v247, s53, v247
v_add_u32 v248, s53, v248
v_mfma_f32_16x16x32_bf16 v[124:127], a[92:95], a[76:79], v[124:127]
s_waitcnt vmcnt(17) lgkmcnt(0)
s_barrier
v_mfma_f32_16x16x32_bf16 v[64:67], a[112:115], a[96:99], v[64:67]
ds_read_b64_tr_b16 a[80:81], v245 offset:0
ds_read_b64_tr_b16 a[82:83], v245 offset:2048
v_mfma_f32_16x16x32_bf16 v[68:71], a[116:119], a[96:99], v[68:71]
v_mfma_f32_16x16x32_bf16 v[72:75], a[120:123], a[96:99], v[72:75]
ds_read_b128 a[64:67], v244 offset:0
ds_read_b128 a[68:71], v244 offset:256
v_mfma_f32_16x16x32_bf16 v[76:79], a[124:127], a[96:99], v[76:79]
ds_read_b64_tr_b16 a[84:85], v246 offset:0
ds_read_b64_tr_b16 a[86:87], v246 offset:2048
v_mfma_f32_16x16x32_bf16 v[80:83], a[112:115], a[100:103], v[80:83]
v_mfma_f32_16x16x32_bf16 v[84:87], a[116:119], a[100:103], v[84:87]
ds_read_b64_tr_b16 a[88:89], v247 offset:0
ds_read_b64_tr_b16 a[90:91], v247 offset:2048
v_mfma_f32_16x16x32_bf16 v[88:91], a[120:123], a[100:103], v[88:91]
ds_read_b128 a[72:75], v244 offset:4096
ds_read_b128 a[76:79], v244 offset:4352
v_mfma_f32_16x16x32_bf16 v[92:95], a[124:127], a[100:103], v[92:95]
v_mfma_f32_16x16x32_bf16 v[96:99], a[112:115], a[104:107], v[96:99]
ds_read_b64_tr_b16 a[92:93], v248 offset:0
ds_read_b64_tr_b16 a[94:95], v248 offset:2048
v_mfma_f32_16x16x32_bf16 v[100:103], a[116:119], a[104:107], v[100:103]
v_mfma_f32_16x16x32_bf16 v[104:107], a[120:123], a[104:107], v[104:107]
s_waitcnt vmcnt(0)
v_and_b32 v250, 7, v250
v_mfma_f32_16x16x32_bf16 v[108:111], a[124:127], a[104:107], v[108:111]
v_cmp_gt_u32 vcc, 5, v250
v_mfma_f32_16x16x32_bf16 v[112:115], a[112:115], a[108:111], v[112:115]
v_mfma_f32_16x16x32_bf16 v[116:119], a[116:119], a[108:111], v[116:119]
s_cmp_lg_u64 vcc, 0
s_cbranch_scc1 .Lspin5
.Lspin5_ret:
v_mfma_f32_16x16x32_bf16 v[120:123], a[120:123], a[108:111], v[120:123]
v_mfma_f32_16x16x32_bf16 v[124:127], a[124:127], a[108:111], v[124:127]
s_waitcnt lgkmcnt(0)
v_mfma_f32_16x16x32_bf16 v[64:67], a[80:83], a[64:67], v[64:67]
ds_read_b64_tr_b16 a[112:113], v245 offset:8192
ds_read_b64_tr_b16 a[114:115], v245 offset:10240
v_mfma_f32_16x16x32_bf16 v[68:71], a[84:87], a[64:67], v[68:71]
buffer_load_dwordx4 v[128:131], v7, s[12:15], 0 offen offset:0 sc1
v_mfma_f32_16x16x32_bf16 v[72:75], a[88:91], a[64:67], v[72:75]
ds_read_b128 a[96:99], v244 offset:2048
ds_read_b128 a[100:103], v244 offset:2304
v_mfma_f32_16x16x32_bf16 v[76:79], a[92:95], a[64:67], v[76:79]
ds_read_b64_tr_b16 a[116:117], v246 offset:8192
ds_read_b64_tr_b16 a[118:119], v246 offset:10240
v_mfma_f32_16x16x32_bf16 v[80:83], a[80:83], a[68:71], v[80:83]
buffer_load_dwordx4 v[132:135], v7, s[12:15], 0 offen offset:1024 sc1
v_mfma_f32_16x16x32_bf16 v[84:87], a[84:87], a[68:71], v[84:87]
ds_read_b64_tr_b16 a[120:121], v247 offset:8192
ds_read_b64_tr_b16 a[122:123], v247 offset:10240
v_mfma_f32_16x16x32_bf16 v[88:91], a[88:91], a[68:71], v[88:91]
ds_read_b128 a[104:107], v244 offset:6144
ds_read_b128 a[108:111], v244 offset:6400
v_mfma_f32_16x16x32_bf16 v[92:95], a[92:95], a[68:71], v[92:95]
buffer_load_dwordx4 v[136:139], v7, s[12:15], 0 offen offset:2048 sc1
v_mfma_f32_16x16x32_bf16 v[96:99], a[80:83], a[72:75], v[96:99]
s_cmp_eq_u32 s46, 0
s_cselect_b32 s53, s56, s57
ds_read_b64_tr_b16 a[124:125], v248 offset:8192
ds_read_b64_tr_b16 a[126:127], v248 offset:10240
v_mfma_f32_16x16x32_bf16 v[100:103], a[84:87], a[72:75], v[100:103]
v_mfma_f32_16x16x32_bf16 v[104:107], a[88:91], a[72:75], v[104:107]
s_add_u32 s49, s46, s91
s_add_u32 s46, s46, s53
buffer_load_dwordx4 v[140:143], v7, s[12:15], 0 offen offset:3072 sc1
v_add_u32 v244, s53, v244
v_mfma_f32_16x16x32_bf16 v[108:111], a[92:95], a[72:75], v[108:111]
v_mfma_f32_16x16x32_bf16 v[112:115], a[80:83], a[76:79], v[112:115]
v_add_u32 v245, s53, v245
v_add_u32 v246, s53, v246
v_mfma_f32_16x16x32_bf16 v[116:119], a[84:87], a[76:79], v[116:119]
v_mfma_f32_16x16x32_bf16 v[120:123], a[88:91], a[76:79], v[120:123]
v_add_u32 v247, s53, v247
v_add_u32 v248, s53, v248
v_mfma_f32_16x16x32_bf16 v[124:127], a[92:95], a[76:79], v[124:127]
s_waitcnt vmcnt(13) lgkmcnt(0)
s_barrier
v_mfma_f32_16x16x32_bf16 v[64:67], a[112:115], a[96:99], v[64:67]
ds_read_b64_tr_b16 a[80:81], v245 offset:0
ds_read_b64_tr_b16 a[82:83], v245 offset:2048
v_mfma_f32_16x16x32_bf16 v[68:71], a[116:119], a[96:99], v[68:71]
buffer_load_dwordx4 v[144:147], v7, s[12:15], s32 offen offset:0 sc1
v_mfma_f32_16x16x32_bf16 v[72:75], a[120:123], a[96:99], v[72:75]
ds_read_b128 a[64:67], v244 offset:0
ds_read_b128 a[68:71], v244 offset:256
v_mfma_f32_16x16x32_bf16 v[76:79], a[124:127], a[96:99], v[76:79]
ds_read_b64_tr_b16 a[84:85], v246 offset:0
ds_read_b64_tr_b16 a[86:87], v246 offset:2048
v_mfma_f32_16x16x32_bf16 v[80:83], a[112:115], a[100:103], v[80:83]
buffer_load_dwordx4 v[148:151], v7, s[12:15], s32 offen offset:1024 sc1
v_mfma_f32_16x16x32_bf16 v[84:87], a[116:119], a[100:103], v[84:87]
ds_read_b64_tr_b16 a[88:89], v247 offset:0
ds_read_b64_tr_b16 a[90:91], v247 offset:2048
v_mfma_f32_16x16x32_bf16 v[88:91], a[120:123], a[100:103], v[88:91]
ds_read_b128 a[72:75], v244 offset:4096
ds_read_b128 a[76:79], v244 offset:4352
v_mfma_f32_16x16x32_bf16 v[92:95], a[124:127], a[100:103], v[92:95]
v_mfma_f32_16x16x32_bf16 v[96:99], a[112:115], a[104:107], v[96:99]
ds_read_b64_tr_b16 a[92:93], v248 offset:0
ds_read_b64_tr_b16 a[94:95], v248 offset:2048
v_mfma_f32_16x16x32_bf16 v[100:103], a[116:119], a[104:107], v[100:103]
buffer_load_dwordx4 v[152:155], v7, s[12:15], s32 offen offset:2048 sc1
v_mfma_f32_16x16x32_bf16 v[104:107], a[120:123], a[104:107], v[104:107]
v_mfma_f32_16x16x32_bf16 v[108:111], a[124:127], a[104:107], v[108:111]
v_mfma_f32_16x16x32_bf16 v[112:115], a[112:115], a[108:111], v[112:115]
buffer_load_dwordx4 v[156:159], v7, s[12:15], s32 offen offset:3072 sc1
v_mfma_f32_16x16x32_bf16 v[116:119], a[116:119], a[108:111], v[116:119]
v_mfma_f32_16x16x32_bf16 v[120:123], a[120:123], a[108:111], v[120:123]
v_mfma_f32_16x16x32_bf16 v[124:127], a[124:127], a[108:111], v[124:127]
s_waitcnt lgkmcnt(0)
v_mfma_f32_16x16x32_bf16 v[64:67], a[80:83], a[64:67], v[64:67]
ds_read_b64_tr_b16 a[112:113], v245 offset:8192
ds_read_b64_tr_b16 a[114:115], v245 offset:10240
v_mfma_f32_16x16x32_bf16 v[68:71], a[84:87], a[64:67], v[68:71]
v_mfma_f32_16x16x32_bf16 v[72:75], a[88:91], a[64:67], v[72:75]
ds_read_b128 a[96:99], v244 offset:2048
ds_read_b128 a[100:103], v244 offset:2304
v_mfma_f32_16x16x32_bf16 v[76:79], a[92:95], a[64:67], v[76:79]
ds_read_b64_tr_b16 a[116:117], v246 offset:8192
ds_read_b64_tr_b16 a[118:119], v246 offset:10240
v_mfma_f32_16x16x32_bf16 v[80:83], a[80:83], a[68:71], v[80:83]
v_mfma_f32_16x16x32_bf16 v[84:87], a[84:87], a[68:71], v[84:87]
ds_read_b64_tr_b16 a[120:121], v247 offset:8192
ds_read_b64_tr_b16 a[122:123], v247 offset:10240
v_mfma_f32_16x16x32_bf16 v[88:91], a[88:91], a[68:71], v[88:91]
ds_read_b128 a[104:107], v244 offset:6144
ds_read_b128 a[108:111], v244 offset:6400
v_mfma_f32_16x16x32_bf16 v[92:95], a[92:95], a[68:71], v[92:95]
v_mfma_f32_16x16x32_bf16 v[96:99], a[80:83], a[72:75], v[96:99]
s_cmp_eq_u32 s46, 0
s_cselect_b32 s53, s56, s57
ds_read_b64_tr_b16 a[124:125], v248 offset:8192
ds_read_b64_tr_b16 a[126:127], v248 offset:10240
v_mfma_f32_16x16x32_bf16 v[100:103], a[84:87], a[72:75], v[100:103]
v_mfma_f32_16x16x32_bf16 v[104:107], a[88:91], a[72:75], v[104:107]
s_add_u32 s49, s46, s91
s_add_u32 s46, s46, s53
v_add_u32 v244, s53, v244
v_mfma_f32_16x16x32_bf16 v[108:111], a[92:95], a[72:75], v[108:111]
v_mfma_f32_16x16x32_bf16 v[112:115], a[80:83], a[76:79], v[112:115]
v_add_u32 v245, s53, v245
v_add_u32 v246, s53, v246
v_mfma_f32_16x16x32_bf16 v[116:119], a[84:87], a[76:79], v[116:119]
v_mfma_f32_16x16x32_bf16 v[120:123], a[88:91], a[76:79], v[120:123]
v_add_u32 v247, s53, v247
v_add_u32 v248, s53, v248
v_mfma_f32_16x16x32_bf16 v[124:127], a[92:95], a[76:79], v[124:127]
s_waitcnt vmcnt(9) lgkmcnt(0)
s_barrier
v_mfma_f32_16x16x32_bf16 v[64:67], a[112:115], a[96:99], v[64:67]
ds_read_b64_tr_b16 a[80:81], v245 offset:0
ds_read_b64_tr_b16 a[82:83], v245 offset:2048
v_mfma_f32_16x16x32_bf16 v[68:71], a[116:119], a[96:99], v[68:71]
ds_read_b128 a[64:67], v244 offset:0
ds_read_b128 a[68:71], v244 offset:256
v_mfma_f32_16x16x32_bf16 v[72:75], a[120:123], a[96:99], v[72:75]
ds_read_b64_tr_b16 a[84:85], v246 offset:0
ds_read_b64_tr_b16 a[86:87], v246 offset:2048
v_mfma_f32_16x16x32_bf16 v[76:79], a[124:127], a[96:99], v[76:79]
ds_read_b64_tr_b16 a[88:89], v247 offset:0
ds_read_b64_tr_b16 a[90:91], v247 offset:2048
v_mfma_f32_16x16x32_bf16 v[80:83], a[112:115], a[100:103], v[80:83]
ds_read_b64_tr_b16 a[92:93], v248 offset:0
ds_read_b64_tr_b16 a[94:95], v248 offset:2048
v_mfma_f32_16x16x32_bf16 v[84:87], a[116:119], a[100:103], v[84:87]
ds_read_b64_tr_b16 a[144:145], v245 offset:8192
ds_read_b64_tr_b16 a[146:147], v245 offset:10240
v_mfma_f32_16x16x32_bf16 v[88:91], a[120:123], a[100:103], v[88:91]
ds_read_b128 a[128:131], v244 offset:2048
ds_read_b128 a[132:135], v244 offset:2304
v_mfma_f32_16x16x32_bf16 v[92:95], a[124:127], a[100:103], v[92:95]
ds_read_b64_tr_b16 a[148:149], v246 offset:8192
ds_read_b64_tr_b16 a[150:151], v246 offset:10240
v_mfma_f32_16x16x32_bf16 v[96:99], a[112:115], a[104:107], v[96:99]
ds_read_b64_tr_b16 a[152:153], v247 offset:8192
ds_read_b64_tr_b16 a[154:155], v247 offset:10240
v_mfma_f32_16x16x32_bf16 v[100:103], a[116:119], a[104:107], v[100:103]
ds_read_b64_tr_b16 a[156:157], v248 offset:8192
ds_read_b64_tr_b16 a[158:159], v248 offset:10240
v_mfma_f32_16x16x32_bf16 v[104:107], a[120:123], a[104:107], v[104:107]
ds_read_b128 a[72:75], v244 offset:4096
ds_read_b128 a[76:79], v244 offset:4352
v_mfma_f32_16x16x32_bf16 v[108:111], a[124:127], a[104:107], v[108:111]
ds_read_b128 a[136:139], v244 offset:6144
ds_read_b128 a[140:143], v244 offset:6400
v_mfma_f32_16x16x32_bf16 v[112:115], a[112:115], a[108:111], v[112:115]
s_mov_b32 s24, 0
s_mov_b32 s25, 32768
v_mfma_f32_16x16x32_bf16 v[116:119], a[116:119], a[108:111], v[116:119]
s_mov_b32 s26, 65536
s_mov_b32 s27, 98304
v_mfma_f32_16x16x32_bf16 v[120:123], a[120:123], a[108:111], v[120:123]
s_mov_b32 s28, 131072
s_mov_b32 s29, 163840
v_mfma_f32_16x16x32_bf16 v[124:127], a[124:127], a[108:111], v[124:127]
s_mov_b32 s30, 196608
s_mov_b32 s31, 229376
s_waitcnt vmcnt(0) lgkmcnt(0)
v_mfma_f32_16x16x32_bf16 v[64:67], v[160:163], v[128:131], v[64:67]
v_mfma_f32_16x16x32_bf16 v[68:71], v[164:167], v[128:131], v[68:71]
v_mfma_f32_16x16x32_bf16 v[72:75], v[160:163], v[132:135], v[72:75]
v_mfma_f32_16x16x32_bf16 v[76:79], v[164:167], v[132:135], v[76:79]
v_mfma_f32_16x16x32_bf16 v[64:67], a[80:83], a[64:67], v[64:67]
v_mfma_f32_16x16x32_bf16 v[68:71], a[84:87], a[64:67], v[68:71]
v_mfma_f32_16x16x32_bf16 v[72:75], a[88:91], a[64:67], v[72:75]
v_mfma_f32_16x16x32_bf16 v[76:79], a[92:95], a[64:67], v[76:79]
v_mfma_f32_16x16x32_bf16 v[64:67], a[144:147], a[128:131], v[64:67]
v_mfma_f32_16x16x32_bf16 v[68:71], a[148:151], a[128:131], v[68:71]
v_mfma_f32_16x16x32_bf16 v[72:75], a[152:155], a[128:131], v[72:75]
v_mfma_f32_16x16x32_bf16 v[76:79], a[156:159], a[128:131], v[76:79]
v_mfma_f32_16x16x32_bf16 v[80:83], v[160:163], v[136:139], v[80:83]
v_mfma_f32_16x16x32_bf16 v[84:87], v[164:167], v[136:139], v[84:87]
v_mfma_f32_16x16x32_bf16 v[88:91], v[160:163], v[140:143], v[88:91]
s_nop 1
v_cvt_pk_bf16_f32 v192, v64, v65
v_cvt_pk_bf16_f32 v194, v68, v69
v_cvt_pk_bf16_f32 v193, v66, v67
v_mfma_f32_16x16x32_bf16 v[92:95], v[164:167], v[140:143], v[92:95]
v_cvt_pk_bf16_f32 v195, v70, v71
v_cvt_pk_bf16_f32 v196, v72, v73
v_cvt_pk_bf16_f32 v198, v76, v77
v_mfma_f32_16x16x32_bf16 v[80:83], a[80:83], a[68:71], v[80:83]
v_cvt_pk_bf16_f32 v197, v74, v75
v_cvt_pk_bf16_f32 v199, v78, v79
v_mfma_f32_16x16x32_bf16 v[84:87], a[84:87], a[68:71], v[84:87]
v_permlane16_swap_b32 v192, v194
v_mfma_f32_16x16x32_bf16 v[88:91], a[88:91], a[68:71], v[88:91]
v_permlane16_swap_b32 v193, v195
v_mfma_f32_16x16x32_bf16 v[92:95], a[92:95], a[68:71], v[92:95]
v_permlane16_swap_b32 v196, v198
v_mfma_f32_16x16x32_bf16 v[80:83], a[144:147], a[132:135], v[80:83]
v_permlane16_swap_b32 v197, v199
v_mov_b32 v200, v192
v_mfma_f32_16x16x32_bf16 v[84:87], a[148:151], a[132:135], v[84:87]
v_mov_b32 v201, v193
v_mov_b32 v202, v194
v_mov_b32 v203, v195
v_mfma_f32_16x16x32_bf16 v[88:91], a[152:155], a[132:135], v[88:91]
v_mov_b32_dpp v192, v196 row_ror:8 row_mask:0xf bank_mask:0xc
v_mov_b32_dpp v193, v197 row_ror:8 row_mask:0xf bank_mask:0xc
v_mov_b32_dpp v194, v198 row_ror:8 row_mask:0xf bank_mask:0xc
v_mfma_f32_16x16x32_bf16 v[92:95], a[156:159], a[132:135], v[92:95]
v_mov_b32_dpp v195, v199 row_ror:8 row_mask:0xf bank_mask:0xc
v_mfma_f32_16x16x32_bf16 v[96:99], v[160:163], v[144:147], v[96:99]
buffer_store_dwordx4 v[192:195], v251, s[16:19], s24 offen nt
v_mov_b32_dpp v196, v200 row_ror:8 row_mask:0xf bank_mask:0x3
v_mov_b32_dpp v197, v201 row_ror:8 row_mask:0xf bank_mask:0x3
v_mfma_f32_16x16x32_bf16 v[100:103], v[164:167], v[144:147], v[100:103]
v_mov_b32_dpp v198, v202 row_ror:8 row_mask:0xf bank_mask:0x3
v_mov_b32_dpp v199, v203 row_ror:8 row_mask:0xf bank_mask:0x3
v_mfma_f32_16x16x32_bf16 v[104:107], v[160:163], v[148:151], v[104:107]
buffer_store_dwordx4 v[196:199], v251, s[16:19], s25 offen nt
v_cvt_pk_bf16_f32 v204, v80, v81
v_cvt_pk_bf16_f32 v206, v84, v85
v_mfma_f32_16x16x32_bf16 v[108:111], v[164:167], v[148:151], v[108:111]
v_cvt_pk_bf16_f32 v205, v82, v83
v_cvt_pk_bf16_f32 v207, v86, v87
v_cvt_pk_bf16_f32 v208, v88, v89
v_mfma_f32_16x16x32_bf16 v[96:99], a[80:83], a[72:75], v[96:99]
v_cvt_pk_bf16_f32 v210, v92, v93
v_cvt_pk_bf16_f32 v209, v90, v91
v_cvt_pk_bf16_f32 v211, v94, v95
v_mfma_f32_16x16x32_bf16 v[100:103], a[84:87], a[72:75], v[100:103]
v_permlane16_swap_b32 v204, v206
v_mfma_f32_16x16x32_bf16 v[104:107], a[88:91], a[72:75], v[104:107]
v_permlane16_swap_b32 v205, v207
v_mfma_f32_16x16x32_bf16 v[108:111], a[92:95], a[72:75], v[108:111]
v_permlane16_swap_b32 v208, v210
v_mfma_f32_16x16x32_bf16 v[96:99], a[144:147], a[136:139], v[96:99]
v_permlane16_swap_b32 v209, v211
v_mov_b32 v212, v204
v_mfma_f32_16x16x32_bf16 v[100:103], a[148:151], a[136:139], v[100:103]
v_mov_b32 v213, v205
v_mov_b32 v214, v206
v_mov_b32 v215, v207
v_mfma_f32_16x16x32_bf16 v[104:107], a[152:155], a[136:139], v[104:107]
v_mov_b32_dpp v204, v208 row_ror:8 row_mask:0xf bank_mask:0xc
v_mov_b32_dpp v205, v209 row_ror:8 row_mask:0xf bank_mask:0xc
v_mov_b32_dpp v206, v210 row_ror:8 row_mask:0xf bank_mask:0xc
v_mfma_f32_16x16x32_bf16 v[108:111], a[156:159], a[136:139], v[108:111]
v_mov_b32_dpp v207, v211 row_ror:8 row_mask:0xf bank_mask:0xc
v_mfma_f32_16x16x32_bf16 v[112:115], v[160:163], v[152:155], v[112:115]
buffer_store_dwordx4 v[204:207], v251, s[16:19], s26 offen nt
v_mov_b32_dpp v208, v212 row_ror:8 row_mask:0xf bank_mask:0x3
v_mov_b32_dpp v209, v213 row_ror:8 row_mask:0xf bank_mask:0x3
v_mfma_f32_16x16x32_bf16 v[116:119], v[164:167], v[152:155], v[116:119]
v_mov_b32_dpp v210, v214 row_ror:8 row_mask:0xf bank_mask:0x3
v_mov_b32_dpp v211, v215 row_ror:8 row_mask:0xf bank_mask:0x3
v_mfma_f32_16x16x32_bf16 v[120:123], v[160:163], v[156:159], v[120:123]
buffer_store_dwordx4 v[208:211], v251, s[16:19], s27 offen nt
v_cvt_pk_bf16_f32 v192, v96, v97
v_cvt_pk_bf16_f32 v194, v100, v101
v_mfma_f32_16x16x32_bf16 v[124:127], v[164:167], v[156:159], v[124:127]
v_cvt_pk_bf16_f32 v193, v98, v99
v_cvt_pk_bf16_f32 v195, v102, v103
v_cvt_pk_bf16_f32 v196, v104, v105
v_mfma_f32_16x16x32_bf16 v[112:115], a[80:83], a[76:79], v[112:115]
v_cvt_pk_bf16_f32 v198, v108, v109
v_cvt_pk_bf16_f32 v197, v106, v107
v_cvt_pk_bf16_f32 v199, v110, v111
v_mfma_f32_16x16x32_bf16 v[116:119], a[84:87], a[76:79], v[116:119]
v_permlane16_swap_b32 v192, v194
v_mfma_f32_16x16x32_bf16 v[120:123], a[88:91], a[76:79], v[120:123]
v_permlane16_swap_b32 v193, v195
v_mfma_f32_16x16x32_bf16 v[124:127], a[92:95], a[76:79], v[124:127]
v_permlane16_swap_b32 v196, v198
v_mfma_f32_16x16x32_bf16 v[112:115], a[144:147], a[140:143], v[112:115]
v_permlane16_swap_b32 v197, v199
v_mov_b32 v200, v192
v_mfma_f32_16x16x32_bf16 v[116:119], a[148:151], a[140:143], v[116:119]
v_mov_b32 v201, v193
v_mov_b32 v202, v194
v_mov_b32 v203, v195
v_mfma_f32_16x16x32_bf16 v[120:123], a[152:155], a[140:143], v[120:123]
v_mov_b32_dpp v192, v196 row_ror:8 row_mask:0xf bank_mask:0xc
v_mov_b32_dpp v193, v197 row_ror:8 row_mask:0xf bank_mask:0xc
v_mov_b32_dpp v194, v198 row_ror:8 row_mask:0xf bank_mask:0xc
v_mfma_f32_16x16x32_bf16 v[124:127], a[156:159], a[140:143], v[124:127]
v_mov_b32_dpp v195, v199 row_ror:8 row_mask:0xf bank_mask:0xc
buffer_store_dwordx4 v[192:195], v251, s[16:19], s28 offen nt
v_mov_b32_dpp v196, v200 row_ror:8 row_mask:0xf bank_mask:0x3
v_mov_b32_dpp v197, v201 row_ror:8 row_mask:0xf bank_mask:0x3
v_mov_b32_dpp v198, v202 row_ror:8 row_mask:0xf bank_mask:0x3
v_mov_b32_dpp v199, v203 row_ror:8 row_mask:0xf bank_mask:0x3
buffer_store_dwordx4 v[196:199], v251, s[16:19], s29 offen nt
v_cvt_pk_bf16_f32 v204, v112, v113
v_cvt_pk_bf16_f32 v206, v116, v117
v_cvt_pk_bf16_f32 v205, v114, v115
v_cvt_pk_bf16_f32 v207, v118, v119
v_cvt_pk_bf16_f32 v208, v120, v121
v_cvt_pk_bf16_f32 v210, v124, v125
v_cvt_pk_bf16_f32 v209, v122, v123
v_cvt_pk_bf16_f32 v211, v126, v127
v_permlane16_swap_b32 v204, v206
v_permlane16_swap_b32 v205, v207
v_permlane16_swap_b32 v208, v210
v_permlane16_swap_b32 v209, v211
v_mov_b32 v212, v204
v_mov_b32 v213, v205
v_mov_b32 v214, v206
v_mov_b32 v215, v207
v_mov_b32_dpp v204, v208 row_ror:8 row_mask:0xf bank_mask:0xc
v_mov_b32_dpp v205, v209 row_ror:8 row_mask:0xf bank_mask:0xc
v_mov_b32_dpp v206, v210 row_ror:8 row_mask:0xf bank_mask:0xc
v_mov_b32_dpp v207, v211 row_ror:8 row_mask:0xf bank_mask:0xc
buffer_store_dwordx4 v[204:207], v251, s[16:19], s30 offen nt
v_mov_b32_dpp v208, v212 row_ror:8 row_mask:0xf bank_mask:0x3
v_mov_b32_dpp v209, v213 row_ror:8 row_mask:0xf bank_mask:0x3
v_mov_b32_dpp v210, v214 row_ror:8 row_mask:0xf bank_mask:0x3
v_mov_b32_dpp v211, v215 row_ror:8 row_mask:0xf bank_mask:0x3
buffer_store_dwordx4 v[208:211], v251, s[16:19], s31 offen nt
s_branch .Lend
.L5odd_pad:
s_cbranch_execz .Ltch10
.L5odd:
v_mfma_f32_16x16x32_bf16 v[64:67], a[112:115], a[96:99], v[64:67]
s_add_u32 m0, s49, 16384
s_add_u32 s44, s44, 4096
ds_read_b64_tr_b16 a[80:81], v245 offset:0
ds_read_b64_tr_b16 a[82:83], v245 offset:2048
v_mfma_f32_16x16x32_bf16 v[68:71], a[116:119], a[96:99], v[68:71]
buffer_load_dwordx4 v240, s[8:11], s45 offen offset:0 lds
v_mfma_f32_16x16x32_bf16 v[72:75], a[120:123], a[96:99], v[72:75]
ds_read_b128 a[64:67], v244 offset:0
ds_read_b128 a[68:71], v244 offset:256
v_mfma_f32_16x16x32_bf16 v[76:79], a[124:127], a[96:99], v[76:79]
ds_read_b64_tr_b16 a[84:85], v246 offset:0
ds_read_b64_tr_b16 a[86:87], v246 offset:2048
v_mfma_f32_16x16x32_bf16 v[80:83], a[112:115], a[100:103], v[80:83]
buffer_load_dwordx4 v241, s[8:11], s45 offen offset:1024 lds
v_mfma_f32_16x16x32_bf16 v[84:87], a[116:119], a[100:103], v[84:87]
ds_read_b64_tr_b16 a[88:89], v247 offset:0
ds_read_b64_tr_b16 a[90:91], v247 offset:2048
.Lpad20:
s_cbranch_execz .Ltch10
v_mfma_f32_16x16x32_bf16 v[88:91], a[120:123], a[100:103], v[88:91]
ds_read_b128 a[72:75], v244 offset:4096
ds_read_b128 a[76:79], v244 offset:4352
v_mfma_f32_16x16x32_bf16 v[92:95], a[124:127], a[100:103], v[92:95]
buffer_load_dword v250, v249, s[60:63], s90 offen sc1
v_mfma_f32_16x16x32_bf16 v[96:99], a[112:115], a[104:107], v[96:99]
ds_read_b64_tr_b16 a[92:93], v248 offset:0
ds_read_b64_tr_b16 a[94:95], v248 offset:2048
v_mfma_f32_16x16x32_bf16 v[100:103], a[116:119], a[104:107], v[100:103]
buffer_load_dwordx4 v242, s[8:11], s45 offen offset:2048 lds
v_mfma_f32_16x16x32_bf16 v[104:107], a[120:123], a[104:107], v[104:107]
v_mfma_f32_16x16x32_bf16 v[108:111], a[124:127], a[104:107], v[108:111]
v_mfma_f32_16x16x32_bf16 v[112:115], a[112:115], a[108:111], v[112:115]
buffer_load_dwordx4 v243, s[8:11], s45 offen offset:3072 lds
v_mfma_f32_16x16x32_bf16 v[116:119], a[116:119], a[108:111], v[116:119]
s_add_u32 s45, s45, 131072
v_mfma_f32_16x16x32_bf16 v[120:123], a[120:123], a[108:111], v[120:123]
v_mfma_f32_16x16x32_bf16 v[124:127], a[124:127], a[108:111], v[124:127]
s_waitcnt lgkmcnt(0)
v_mfma_f32_16x16x32_bf16 v[64:67], a[80:83], a[64:67], v[64:67]
s_mov_b32 m0, s49
ds_read_b64_tr_b16 a[112:113], v245 offset:8192
ds_read_b64_tr_b16 a[114:115], v245 offset:10240
v_mfma_f32_16x16x32_bf16 v[68:71], a[84:87], a[64:67], v[68:71]
buffer_load_dwordx4 v7, s[4:7], s44 offen offset:0 sc1 lds
v_mfma_f32_16x16x32_bf16 v[72:75], a[88:91], a[64:67], v[72:75]
ds_read_b128 a[96:99], v244 offset:2048
ds_read_b128 a[100:103], v244 offset:2304
v_mfma_f32_16x16x32_bf16 v[76:79], a[92:95], a[64:67], v[76:79]
ds_read_b64_tr_b16 a[116:117], v246 offset:8192
ds_read_b64_tr_b16 a[118:119], v246 offset:10240
v_mfma_f32_16x16x32_bf16 v[80:83], a[80:83], a[68:71], v[80:83]
buffer_load_dwordx4 v7, s[4:7], s44 offen offset:1024 sc1 lds
v_mfma_f32_16x16x32_bf16 v[84:87], a[84:87], a[68:71], v[84:87]
ds_read_b64_tr_b16 a[120:121], v247 offset:8192
ds_read_b64_tr_b16 a[122:123], v247 offset:10240
v_mfma_f32_16x16x32_bf16 v[88:91], a[88:91], a[68:71], v[88:91]
ds_read_b128 a[104:107], v244 offset:6144
ds_read_b128 a[108:111], v244 offset:6400
v_mfma_f32_16x16x32_bf16 v[92:95], a[92:95], a[68:71], v[92:95]
v_mfma_f32_16x16x32_bf16 v[96:99], a[80:83], a[72:75], v[96:99]
s_cmp_eq_u32 s46, 0
s_cselect_b32 s53, s56, s57
ds_read_b64_tr_b16 a[124:125], v248 offset:8192
ds_read_b64_tr_b16 a[126:127], v248 offset:10240
v_mfma_f32_16x16x32_bf16 v[100:103], a[84:87], a[72:75], v[100:103]
v_mfma_f32_16x16x32_bf16 v[104:107], a[88:91], a[72:75], v[104:107]
s_add_u32 s49, s46, s91
s_add_u32 s46, s46, s53
v_add_u32 v244, s53, v244
v_mfma_f32_16x16x32_bf16 v[108:111], a[92:95], a[72:75], v[108:111]
buffer_load_dwordx4 v7, s[4:7], s44 offen offset:2048 sc1 lds
v_mfma_f32_16x16x32_bf16 v[112:115], a[80:83], a[76:79], v[112:115]
v_add_u32 v245, s53, v245
v_add_u32 v246, s53, v246
v_mfma_f32_16x16x32_bf16 v[116:119], a[84:87], a[76:79], v[116:119]
buffer_load_dwordx4 v7, s[4:7], s44 offen offset:3072 sc1 lds
v_mfma_f32_16x16x32_bf16 v[120:123], a[88:91], a[76:79], v[120:123]
v_add_u32 v247, s53, v247
v_add_u32 v248, s53, v248
v_mfma_f32_16x16x32_bf16 v[124:127], a[92:95], a[76:79], v[124:127]
s_waitcnt vmcnt(25) lgkmcnt(0)
s_barrier
v_mfma_f32_16x16x32_bf16 v[64:67], a[112:115], a[96:99], v[64:67]
s_add_u32 m0, s49, 16384
s_add_u32 s44, s44, 4096
ds_read_b64_tr_b16 a[80:81], v245 offset:0
ds_read_b64_tr_b16 a[82:83], v245 offset:2048
v_mfma_f32_16x16x32_bf16 v[68:71], a[116:119], a[96:99], v[68:71]
buffer_load_dwordx4 v240, s[8:11], s45 offen offset:0 lds
v_mfma_f32_16x16x32_bf16 v[72:75], a[120:123], a[96:99], v[72:75]
ds_read_b128 a[64:67], v244 offset:0
ds_read_b128 a[68:71], v244 offset:256
v_mfma_f32_16x16x32_bf16 v[76:79], a[124:127], a[96:99], v[76:79]
ds_read_b64_tr_b16 a[84:85], v246 offset:0
ds_read_b64_tr_b16 a[86:87], v246 offset:2048
v_mfma_f32_16x16x32_bf16 v[80:83], a[112:115], a[100:103], v[80:83]
buffer_load_dwordx4 v241, s[8:11], s45 offen offset:1024 lds
v_mfma_f32_16x16x32_bf16 v[84:87], a[116:119], a[100:103], v[84:87]
ds_read_b64_tr_b16 a[88:89], v247 offset:0
ds_read_b64_tr_b16 a[90:91], v247 offset:2048
v_mfma_f32_16x16x32_bf16 v[88:91], a[120:123], a[100:103], v[88:91]
ds_read_b128 a[72:75], v244 offset:4096
ds_read_b128 a[76:79], v244 offset:4352
v_mfma_f32_16x16x32_bf16 v[92:95], a[124:127], a[100:103], v[92:95]
v_mfma_f32_16x16x32_bf16 v[96:99], a[112:115], a[104:107], v[96:99]
ds_read_b64_tr_b16 a[92:93], v248 offset:0
ds_read_b64_tr_b16 a[94:95], v248 offset:2048
v_mfma_f32_16x16x32_bf16 v[100:103], a[116:119], a[104:107], v[100:103]
buffer_load_dwordx4 v242, s[8:11], s45 offen offset:2048 lds
v_mfma_f32_16x16x32_bf16 v[104:107], a[120:123], a[104:107], v[104:107]
v_mfma_f32_16x16x32_bf16 v[108:111], a[124:127], a[104:107], v[108:111]
v_mfma_f32_16x16x32_bf16 v[112:115], a[112:115], a[108:111], v[112:115]
buffer_load_dwordx4 v243, s[8:11], s45 offen offset:3072 lds
v_mfma_f32_16x16x32_bf16 v[116:119], a[116:119], a[108:111], v[116:119]
s_add_u32 s45, s45, 131072
v_mfma_f32_16x16x32_bf16 v[120:123], a[120:123], a[108:111], v[120:123]
v_mfma_f32_16x16x32_bf16 v[124:127], a[124:127], a[108:111], v[124:127]
s_waitcnt lgkmcnt(0)
v_mfma_f32_16x16x32_bf16 v[64:67], a[80:83], a[64:67], v[64:67]
s_mov_b32 m0, s49
ds_read_b64_tr_b16 a[112:113], v245 offset:8192
ds_read_b64_tr_b16 a[114:115], v245 offset:10240
v_mfma_f32_16x16x32_bf16 v[68:71], a[84:87], a[64:67], v[68:71]
buffer_load_dwordx4 v7, s[4:7], s44 offen offset:0 sc1 lds
v_mfma_f32_16x16x32_bf16 v[72:75], a[88:91], a[64:67], v[72:75]
ds_read_b128 a[96:99], v244 offset:2048
ds_read_b128 a[100:103], v244 offset:2304
v_mfma_f32_16x16x32_bf16 v[76:79], a[92:95], a[64:67], v[76:79]
ds_read_b64_tr_b16 a[116:117], v246 offset:8192
ds_read_b64_tr_b16 a[118:119], v246 offset:10240
v_mfma_f32_16x16x32_bf16 v[80:83], a[80:83], a[68:71], v[80:83]
buffer_load_dwordx4 v7, s[4:7], s44 offen offset:1024 sc1 lds
v_mfma_f32_16x16x32_bf16 v[84:87], a[84:87], a[68:71], v[84:87]
ds_read_b64_tr_b16 a[120:121], v247 offset:8192
ds_read_b64_tr_b16 a[122:123], v247 offset:10240
v_mfma_f32_16x16x32_bf16 v[88:91], a[88:91], a[68:71], v[88:91]
ds_read_b128 a[104:107], v244 offset:6144
ds_read_b128 a[108:111], v244 offset:6400
v_mfma_f32_16x16x32_bf16 v[92:95], a[92:95], a[68:71], v[92:95]
v_mfma_f32_16x16x32_bf16 v[96:99], a[80:83], a[72:75], v[96:99]
s_cmp_eq_u32 s46, 0
s_cselect_b32 s53, s56, s57
ds_read_b64_tr_b16 a[124:125], v248 offset:8192
ds_read_b64_tr_b16 a[126:127], v248 offset:10240
v_mfma_f32_16x16x32_bf16 v[100:103], a[84:87], a[72:75], v[100:103]
v_mfma_f32_16x16x32_bf16 v[104:107], a[88:91], a[72:75], v[104:107]
s_add_u32 s49, s46, s91
s_add_u32 s46, s46, s53
v_add_u32 v244, s53, v244
v_mfma_f32_16x16x32_bf16 v[108:111], a[92:95], a[72:75], v[108:111]
buffer_load_dwordx4 v7, s[4:7], s44 offen offset:2048 sc1 lds
v_mfma_f32_16x16x32_bf16 v[112:115], a[80:83], a[76:79], v[112:115]
v_add_u32 v245, s53, v245
v_add_u32 v246, s53, v246
v_mfma_f32_16x16x32_bf16 v[116:119], a[84:87], a[76:79], v[116:119]
buffer_load_dwordx4 v7, s[4:7], s44 offen offset:3072 sc1 lds
v_mfma_f32_16x16x32_bf16 v[120:123], a[88:91], a[76:79], v[120:123]
v_add_u32 v247, s53, v247
v_add_u32 v248, s53, v248
v_mfma_f32_16x16x32_bf16 v[124:127], a[92:95], a[76:79], v[124:127]
s_waitcnt vmcnt(25) lgkmcnt(0)
s_barrier
s_lshl_b32 s54, s51, 2
s_add_u32 s54, s54, s3
s_lshl_b32 s54, s54, 16
s_add_u32 s54, s54, 50331648
s_add_u32 s4, s84, s54
s_addc_u32 s5, s85, 0
s_and_b32 s5, s5, 0xffff
s_mov_b32 s6, 65536
s_lshl_b32 s54, s3, 15
s_add_u32 s54, s54, s52
s_sub_u32 s28, 0x200000, s54
s_add_u32 s8, s68, s54
s_addc_u32 s9, s69, 0
s_and_b32 s9, s9, 0xffff
s_mov_b32 s10, s28
s_mov_b32 s44, -4096
s_mov_b32 s45, 0
v_mfma_f32_16x16x32_bf16 v[64:67], a[112:115], a[96:99], v[64:67]
s_add_u32 m0, s49, 16384
s_add_u32 s44, s44, 4096
ds_read_b64_tr_b16 a[80:81], v245 offset:0
ds_read_b64_tr_b16 a[82:83], v245 offset:2048
v_mfma_f32_16x16x32_bf16 v[68:71], a[116:119], a[96:99], v[68:71]
buffer_load_dwordx4 v240, s[8:11], s45 offen offset:0 lds
v_mfma_f32_16x16x32_bf16 v[72:75], a[120:123], a[96:99], v[72:75]
ds_read_b128 a[64:67], v244 offset:0
ds_read_b128 a[68:71], v244 offset:256
v_mfma_f32_16x16x32_bf16 v[76:79], a[124:127], a[96:99], v[76:79]
ds_read_b64_tr_b16 a[84:85], v246 offset:0
ds_read_b64_tr_b16 a[86:87], v246 offset:2048
v_mfma_f32_16x16x32_bf16 v[80:83], a[112:115], a[100:103], v[80:83]
buffer_load_dwordx4 v241, s[8:11], s45 offen offset:1024 lds
v_mfma_f32_16x16x32_bf16 v[84:87], a[116:119], a[100:103], v[84:87]
ds_read_b64_tr_b16 a[88:89], v247 offset:0
ds_read_b64_tr_b16 a[90:91], v247 offset:2048
v_mfma_f32_16x16x32_bf16 v[88:91], a[120:123], a[100:103], v[88:91]
ds_read_b128 a[72:75], v244 offset:4096
ds_read_b128 a[76:79], v244 offset:4352
v_mfma_f32_16x16x32_bf16 v[92:95], a[124:127], a[100:103], v[92:95]
v_mfma_f32_16x16x32_bf16 v[96:99], a[112:115], a[104:107], v[96:99]
ds_read_b64_tr_b16 a[92:93], v248 offset:0
ds_read_b64_tr_b16 a[94:95], v248 offset:2048
v_mfma_f32_16x16x32_bf16 v[100:103], a[116:119], a[104:107], v[100:103]
buffer_load_dwordx4 v242, s[8:11], s45 offen offset:2048 lds
v_mfma_f32_16x16x32_bf16 v[104:107], a[120:123], a[104:107], v[104:107]
s_waitcnt vmcnt(17)
v_and_b32 v250, 7, v250
v_mfma_f32_16x16x32_bf16 v[108:111], a[124:127], a[104:107], v[108:111]
v_cmp_gt_u32 vcc, 4, v250
v_mfma_f32_16x16x32_bf16 v[112:115], a[112:115], a[108:111], v[112:115]
buffer_load_dwordx4 v243, s[8:11], s45 offen offset:3072 lds
v_mfma_f32_16x16x32_bf16 v[116:119], a[116:119], a[108:111], v[116:119]
s_add_u32 s45, s45, 131072
s_cmp_lg_u64 vcc, 0
s_cbranch_scc1 .Lspin4
.Lspin4_ret:
v_mfma_f32_16x16x32_bf16 v[120:123], a[120:123], a[108:111], v[120:123]
v_mfma_f32_16x16x32_bf16 v[124:127], a[124:127], a[108:111], v[124:127]
s_waitcnt lgkmcnt(0)
v_mfma_f32_16x16x32_bf16 v[64:67], a[80:83], a[64:67], v[64:67]
s_mov_b32 m0, s49
ds_read_b64_tr_b16 a[112:113], v245 offset:8192
ds_read_b64_tr_b16 a[114:115], v245 offset:10240
v_mfma_f32_16x16x32_bf16 v[68:71], a[84:87], a[64:67], v[68:71]
buffer_load_dwordx4 v7, s[4:7], s44 offen offset:0 sc1 lds
v_mfma_f32_16x16x32_bf16 v[72:75], a[88:91], a[64:67], v[72:75]
ds_read_b128 a[96:99], v244 offset:2048
ds_read_b128 a[100:103], v244 offset:2304
v_mfma_f32_16x16x32_bf16 v[76:79], a[92:95], a[64:67], v[76:79]
ds_read_b64_tr_b16 a[116:117], v246 offset:8192
ds_read_b64_tr_b16 a[118:119], v246 offset:10240
v_mfma_f32_16x16x32_bf16 v[80:83], a[80:83], a[68:71], v[80:83]
buffer_load_dwordx4 v7, s[4:7], s44 offen offset:1024 sc1 lds
v_mfma_f32_16x16x32_bf16 v[84:87], a[84:87], a[68:71], v[84:87]
ds_read_b64_tr_b16 a[120:121], v247 offset:8192
ds_read_b64_tr_b16 a[122:123], v247 offset:10240
v_mfma_f32_16x16x32_bf16 v[88:91], a[88:91], a[68:71], v[88:91]
ds_read_b128 a[104:107], v244 offset:6144
ds_read_b128 a[108:111], v244 offset:6400
v_mfma_f32_16x16x32_bf16 v[92:95], a[92:95], a[68:71], v[92:95]
v_mfma_f32_16x16x32_bf16 v[96:99], a[80:83], a[72:75], v[96:99]
s_cmp_eq_u32 s46, 0
s_cselect_b32 s53, s56, s57
ds_read_b64_tr_b16 a[124:125], v248 offset:8192
ds_read_b64_tr_b16 a[126:127], v248 offset:10240
v_mfma_f32_16x16x32_bf16 v[100:103], a[84:87], a[72:75], v[100:103]
v_mfma_f32_16x16x32_bf16 v[104:107], a[88:91], a[72:75], v[104:107]
s_add_u32 s49, s46, s91
s_add_u32 s46, s46, s53
v_add_u32 v244, s53, v244
v_mfma_f32_16x16x32_bf16 v[108:111], a[92:95], a[72:75], v[108:111]
buffer_load_dwordx4 v7, s[4:7], s44 offen offset:2048 sc1 lds
v_mfma_f32_16x16x32_bf16 v[112:115], a[80:83], a[76:79], v[112:115]
v_add_u32 v245, s53, v245
v_add_u32 v246, s53, v246
v_mfma_f32_16x16x32_bf16 v[116:119], a[84:87], a[76:79], v[116:119]
buffer_load_dwordx4 v7, s[4:7], s44 offen offset:3072 sc1 lds
v_mfma_f32_16x16x32_bf16 v[120:123], a[88:91], a[76:79], v[120:123]
v_add_u32 v247, s53, v247
v_add_u32 v248, s53, v248
v_mfma_f32_16x16x32_bf16 v[124:127], a[92:95], a[76:79], v[124:127]
s_waitcnt vmcnt(25) lgkmcnt(0)
s_barrier
v_mfma_f32_16x16x32_bf16 v[64:67], a[112:115], a[96:99], v[64:67]
s_add_u32 m0, s49, 16384
s_add_u32 s44, s44, 4096
ds_read_b64_tr_b16 a[80:81], v245 offset:0
ds_read_b64_tr_b16 a[82:83], v245 offset:2048
v_mfma_f32_16x16x32_bf16 v[68:71], a[116:119], a[96:99], v[68:71]
buffer_load_dwordx4 v240, s[8:11], s45 offen offset:0 lds
v_mfma_f32_16x16x32_bf16 v[72:75], a[120:123], a[96:99], v[72:75]
ds_read_b128 a[64:67], v244 offset:0
ds_read_b128 a[68:71], v244 offset:256
v_mfma_f32_16x16x32_bf16 v[76:79], a[124:127], a[96:99], v[76:79]
ds_read_b64_tr_b16 a[84:85], v246 offset:0
ds_read_b64_tr_b16 a[86:87], v246 offset:2048
v_mfma_f32_16x16x32_bf16 v[80:83], a[112:115], a[100:103], v[80:83]
buffer_load_dwordx4 v241, s[8:11], s45 offen offset:1024 lds
v_mfma_f32_16x16x32_bf16 v[84:87], a[116:119], a[100:103], v[84:87]
ds_read_b64_tr_b16 a[88:89], v247 offset:0
ds_read_b64_tr_b16 a[90:91], v247 offset:2048
v_mfma_f32_16x16x32_bf16 v[88:91], a[120:123], a[100:103], v[88:91]
ds_read_b128 a[72:75], v244 offset:4096
ds_read_b128 a[76:79], v244 offset:4352
v_mfma_f32_16x16x32_bf16 v[92:95], a[124:127], a[100:103], v[92:95]
v_mfma_f32_16x16x32_bf16 v[96:99], a[112:115], a[104:107], v[96:99]
ds_read_b64_tr_b16 a[92:93], v248 offset:0
ds_read_b64_tr_b16 a[94:95], v248 offset:2048
v_mfma_f32_16x16x32_bf16 v[100:103], a[116:119], a[104:107], v[100:103]
buffer_load_dwordx4 v242, s[8:11], s45 offen offset:2048 lds
v_mfma_f32_16x16x32_bf16 v[104:107], a[120:123], a[104:107], v[104:107]
v_mfma_f32_16x16x32_bf16 v[108:111], a[124:127], a[104:107], v[108:111]
v_mfma_f32_16x16x32_bf16 v[112:115], a[112:115], a[108:111], v[112:115]
buffer_load_dwordx4 v243, s[8:11], s45 offen offset:3072 lds
v_mfma_f32_16x16x32_bf16 v[116:119], a[116:119], a[108:111], v[116:119]
s_add_u32 s45, s45, 131072
v_mfma_f32_16x16x32_bf16 v[120:123], a[120:123], a[108:111], v[120:123]
v_mfma_f32_16x16x32_bf16 v[124:127], a[124:127], a[108:111], v[124:127]
s_waitcnt lgkmcnt(0)
v_mfma_f32_16x16x32_bf16 v[64:67], a[80:83], a[64:67], v[64:67]
s_mov_b32 m0, s49
ds_read_b64_tr_b16 a[112:113], v245 offset:8192
ds_read_b64_tr_b16 a[114:115], v245 offset:10240
v_mfma_f32_16x16x32_bf16 v[68:71], a[84:87], a[64:67], v[68:71]
buffer_load_dwordx4 v7, s[4:7], s44 offen offset:0 sc1 lds
v_mfma_f32_16x16x32_bf16 v[72:75], a[88:91], a[64:67], v[72:75]
ds_read_b128 a[96:99], v244 offset:2048
ds_read_b128 a[100:103], v244 offset:2304
v_mfma_f32_16x16x32_bf16 v[76:79], a[92:95], a[64:67], v[76:79]
ds_read_b64_tr_b16 a[116:117], v246 offset:8192
ds_read_b64_tr_b16 a[118:119], v246 offset:10240
v_mfma_f32_16x16x32_bf16 v[80:83], a[80:83], a[68:71], v[80:83]
buffer_load_dwordx4 v7, s[4:7], s44 offen offset:1024 sc1 lds
v_mfma_f32_16x16x32_bf16 v[84:87], a[84:87], a[68:71], v[84:87]
ds_read_b64_tr_b16 a[120:121], v247 offset:8192
ds_read_b64_tr_b16 a[122:123], v247 offset:10240
v_mfma_f32_16x16x32_bf16 v[88:91], a[88:91], a[68:71], v[88:91]
ds_read_b128 a[104:107], v244 offset:6144
ds_read_b128 a[108:111], v244 offset:6400
v_mfma_f32_16x16x32_bf16 v[92:95], a[92:95], a[68:71], v[92:95]
v_mfma_f32_16x16x32_bf16 v[96:99], a[80:83], a[72:75], v[96:99]
s_cmp_eq_u32 s46, 0
s_cselect_b32 s53, s56, s57
ds_read_b64_tr_b16 a[124:125], v248 offset:8192
ds_read_b64_tr_b16 a[126:127], v248 offset:10240
v_mfma_f32_16x16x32_bf16 v[100:103], a[84:87], a[72:75], v[100:103]
v_mfma_f32_16x16x32_bf16 v[104:107], a[88:91], a[72:75], v[104:107]
s_add_u32 s49, s46, s91
s_add_u32 s46, s46, s53
v_add_u32 v244, s53, v244
v_mfma_f32_16x16x32_bf16 v[108:111], a[92:95], a[72:75], v[108:111]
buffer_load_dwordx4 v7, s[4:7], s44 offen offset:2048 sc1 lds
v_mfma_f32_16x16x32_bf16 v[112:115], a[80:83], a[76:79], v[112:115]
v_add_u32 v245, s53, v245
v_add_u32 v246, s53, v246
v_mfma_f32_16x16x32_bf16 v[116:119], a[84:87], a[76:79], v[116:119]
buffer_load_dwordx4 v7, s[4:7], s44 offen offset:3072 sc1 lds
v_mfma_f32_16x16x32_bf16 v[120:123], a[88:91], a[76:79], v[120:123]
v_add_u32 v247, s53, v247
v_add_u32 v248, s53, v248
v_mfma_f32_16x16x32_bf16 v[124:127], a[92:95], a[76:79], v[124:127]
s_waitcnt vmcnt(24) lgkmcnt(0)
s_barrier
v_mfma_f32_16x16x32_bf16 v[64:67], a[112:115], a[96:99], v[64:67]
s_add_u32 m0, s49, 16384
s_add_u32 s44, s44, 4096
ds_read_b64_tr_b16 a[80:81], v245 offset:0
ds_read_b64_tr_b16 a[82:83], v245 offset:2048
v_mfma_f32_16x16x32_bf16 v[68:71], a[116:119], a[96:99], v[68:71]
buffer_load_dwordx4 v240, s[8:11], s45 offen offset:0 lds
v_mfma_f32_16x16x32_bf16 v[72:75], a[120:123], a[96:99], v[72:75]
ds_read_b128 a[64:67], v244 offset:0
ds_read_b128 a[68:71], v244 offset:256
v_mfma_f32_16x16x32_bf16 v[76:79], a[124:127], a[96:99], v[76:79]
ds_read_b64_tr_b16 a[84:85], v246 offset:0
ds_read_b64_tr_b16 a[86:87], v246 offset:2048
v_mfma_f32_16x16x32_bf16 v[80:83], a[112:115], a[100:103], v[80:83]
buffer_load_dwordx4 v241, s[8:11], s45 offen offset:1024 lds
v_mfma_f32_16x16x32_bf16 v[84:87], a[116:119], a[100:103], v[84:87]
ds_read_b64_tr_b16 a[88:89], v247 offset:0
ds_read_b64_tr_b16 a[90:91], v247 offset:2048
v_mfma_f32_16x16x32_bf16 v[88:91], a[120:123], a[100:103], v[88:91]
ds_read_b128 a[72:75], v244 offset:4096
ds_read_b128 a[76:79], v244 offset:4352
v_mfma_f32_16x16x32_bf16 v[92:95], a[124:127], a[100:103], v[92:95]
v_mfma_f32_16x16x32_bf16 v[96:99], a[112:115], a[104:107], v[96:99]
ds_read_b64_tr_b16 a[92:93], v248 offset:0
ds_read_b64_tr_b16 a[94:95], v248 offset:2048
v_mfma_f32_16x16x32_bf16 v[100:103], a[116:119], a[104:107], v[100:103]
buffer_load_dwordx4 v242, s[8:11], s45 offen offset:2048 lds
v_mfma_f32_16x16x32_bf16 v[104:107], a[120:123], a[104:107], v[104:107]
v_mfma_f32_16x16x32_bf16 v[108:111], a[124:127], a[104:107], v[108:111]
v_mfma_f32_16x16x32_bf16 v[112:115], a[112:115], a[108:111], v[112:115]
buffer_load_dwordx4 v243, s[8:11], s45 offen offset:3072 lds
v_mfma_f32_16x16x32_bf16 v[116:119], a[116:119], a[108:111], v[116:119]
s_add_u32 s45, s45, 131072
v_mfma_f32_16x16x32_bf16 v[120:123], a[120:123], a[108:111], v[120:123]
v_mfma_f32_16x16x32_bf16 v[124:127], a[124:127], a[108:111], v[124:127]
s_waitcnt lgkmcnt(0)
v_mfma_f32_16x16x32_bf16 v[64:67], a[80:83], a[64:67], v[64:67]
s_mov_b32 m0, s49
ds_read_b64_tr_b16 a[112:113], v245 offset:8192
ds_read_b64_tr_b16 a[114:115], v245 offset:10240
v_mfma_f32_16x16x32_bf16 v[68:71], a[84:87], a[64:67], v[68:71]
buffer_load_dwordx4 v7, s[4:7], s44 offen offset:0 sc1 lds
v_mfma_f32_16x16x32_bf16 v[72:75], a[88:91], a[64:67], v[72:75]
ds_read_b128 a[96:99], v244 offset:2048
ds_read_b128 a[100:103], v244 offset:2304
v_mfma_f32_16x16x32_bf16 v[76:79], a[92:95], a[64:67], v[76:79]
ds_read_b64_tr_b16 a[116:117], v246 offset:8192
ds_read_b64_tr_b16 a[118:119], v246 offset:10240
v_mfma_f32_16x16x32_bf16 v[80:83], a[80:83], a[68:71], v[80:83]
buffer_load_dwordx4 v7, s[4:7], s44 offen offset:1024 sc1 lds
v_mfma_f32_16x16x32_bf16 v[84:87], a[84:87], a[68:71], v[84:87]
ds_read_b64_tr_b16 a[120:121], v247 offset:8192
ds_read_b64_tr_b16 a[122:123], v247 offset:10240
v_mfma_f32_16x16x32_bf16 v[88:91], a[88:91], a[68:71], v[88:91]
ds_read_b128 a[104:107], v244 offset:6144
ds_read_b128 a[108:111], v244 offset:6400
v_mfma_f32_16x16x32_bf16 v[92:95], a[92:95], a[68:71], v[92:95]
v_mfma_f32_16x16x32_bf16 v[96:99], a[80:83], a[72:75], v[96:99]
s_cmp_eq_u32 s46, 0
s_cselect_b32 s53, s56, s57
ds_read_b64_tr_b16 a[124:125], v248 offset:8192
ds_read_b64_tr_b16 a[126:127], v248 offset:10240
v_mfma_f32_16x16x32_bf16 v[100:103], a[84:87], a[72:75], v[100:103]
v_mfma_f32_16x16x32_bf16 v[104:107], a[88:91], a[72:75], v[104:107]
s_add_u32 s49, s46, s91
s_add_u32 s46, s46, s53
v_add_u32 v244, s53, v244
v_mfma_f32_16x16x32_bf16 v[108:111], a[92:95], a[72:75], v[108:111]
buffer_load_dwordx4 v7, s[4:7], s44 offen offset:2048 sc1 lds
v_mfma_f32_16x16x32_bf16 v[112:115], a[80:83], a[76:79], v[112:115]
v_add_u32 v245, s53, v245
v_add_u32 v246, s53, v246
v_mfma_f32_16x16x32_bf16 v[116:119], a[84:87], a[76:79], v[116:119]
buffer_load_dwordx4 v7, s[4:7], s44 offen offset:3072 sc1 lds
v_mfma_f32_16x16x32_bf16 v[120:123], a[88:91], a[76:79], v[120:123]
v_add_u32 v247, s53, v247
v_add_u32 v248, s53, v248
v_mfma_f32_16x16x32_bf16 v[124:127], a[92:95], a[76:79], v[124:127]
s_waitcnt vmcnt(24) lgkmcnt(0)
s_barrier
v_mfma_f32_16x16x32_bf16 v[64:67], a[112:115], a[96:99], v[64:67]
s_add_u32 m0, s49, 16384
s_add_u32 s44, s44, 4096
ds_read_b64_tr_b16 a[80:81], v245 offset:0
ds_read_b64_tr_b16 a[82:83], v245 offset:2048
v_mfma_f32_16x16x32_bf16 v[68:71], a[116:119], a[96:99], v[68:71]
buffer_load_dwordx4 v240, s[8:11], s45 offen offset:0 lds
v_mfma_f32_16x16x32_bf16 v[72:75], a[120:123], a[96:99], v[72:75]
ds_read_b128 a[64:67], v244 offset:0
ds_read_b128 a[68:71], v244 offset:256
v_mfma_f32_16x16x32_bf16 v[76:79], a[124:127], a[96:99], v[76:79]
ds_read_b64_tr_b16 a[84:85], v246 offset:0
ds_read_b64_tr_b16 a[86:87], v246 offset:2048
v_mfma_f32_16x16x32_bf16 v[80:83], a[112:115], a[100:103], v[80:83]
buffer_load_dwordx4 v241, s[8:11], s45 offen offset:1024 lds
v_mfma_f32_16x16x32_bf16 v[84:87], a[116:119], a[100:103], v[84:87]
ds_read_b64_tr_b16 a[88:89], v247 offset:0
ds_read_b64_tr_b16 a[90:91], v247 offset:2048
v_mfma_f32_16x16x32_bf16 v[88:91], a[120:123], a[100:103], v[88:91]
ds_read_b128 a[72:75], v244 offset:4096
ds_read_b128 a[76:79], v244 offset:4352
v_mfma_f32_16x16x32_bf16 v[92:95], a[124:127], a[100:103], v[92:95]
v_mfma_f32_16x16x32_bf16 v[96:99], a[112:115], a[104:107], v[96:99]
ds_read_b64_tr_b16 a[92:93], v248 offset:0
ds_read_b64_tr_b16 a[94:95], v248 offset:2048
v_mfma_f32_16x16x32_bf16 v[100:103], a[116:119], a[104:107], v[100:103]
buffer_load_dwordx4 v242, s[8:11], s45 offen offset:2048 lds
v_mfma_f32_16x16x32_bf16 v[104:107], a[120:123], a[104:107], v[104:107]
v_mfma_f32_16x16x32_bf16 v[108:111], a[124:127], a[104:107], v[108:111]
v_mfma_f32_16x16x32_bf16 v[112:115], a[112:115], a[108:111], v[112:115]
buffer_load_dwordx4 v243, s[8:11], s45 offen offset:3072 lds
v_mfma_f32_16x16x32_bf16 v[116:119], a[116:119], a[108:111], v[116:119]
s_add_u32 s45, s45, 131072
v_mfma_f32_16x16x32_bf16 v[120:123], a[120:123], a[108:111], v[120:123]
v_mfma_f32_16x16x32_bf16 v[124:127], a[124:127], a[108:111], v[124:127]
s_waitcnt lgkmcnt(0)
v_mfma_f32_16x16x32_bf16 v[64:67], a[80:83], a[64:67], v[64:67]
s_mov_b32 m0, s49
ds_read_b64_tr_b16 a[112:113], v245 offset:8192
ds_read_b64_tr_b16 a[114:115], v245 offset:10240
v_mfma_f32_16x16x32_bf16 v[68:71], a[84:87], a[64:67], v[68:71]
buffer_load_dwordx4 v7, s[4:7], s44 offen offset:0 sc1 lds
v_mfma_f32_16x16x32_bf16 v[72:75], a[88:91], a[64:67], v[72:75]
ds_read_b128 a[96:99], v244 offset:2048
ds_read_b128 a[100:103], v244 offset:2304
v_mfma_f32_16x16x32_bf16 v[76:79], a[92:95], a[64:67], v[76:79]
ds_read_b64_tr_b16 a[116:117], v246 offset:8192
ds_read_b64_tr_b16 a[118:119], v246 offset:10240
v_mfma_f32_16x16x32_bf16 v[80:83], a[80:83], a[68:71], v[80:83]
buffer_load_dwordx4 v7, s[4:7], s44 offen offset:1024 sc1 lds
v_mfma_f32_16x16x32_bf16 v[84:87], a[84:87], a[68:71], v[84:87]
ds_read_b64_tr_b16 a[120:121], v247 offset:8192
ds_read_b64_tr_b16 a[122:123], v247 offset:10240
v_mfma_f32_16x16x32_bf16 v[88:91], a[88:91], a[68:71], v[88:91]
ds_read_b128 a[104:107], v244 offset:6144
ds_read_b128 a[108:111], v244 offset:6400
v_mfma_f32_16x16x32_bf16 v[92:95], a[92:95], a[68:71], v[92:95]
v_mfma_f32_16x16x32_bf16 v[96:99], a[80:83], a[72:75], v[96:99]
s_cmp_eq_u32 s46, 0
s_cselect_b32 s53, s56, s57
ds_read_b64_tr_b16 a[124:125], v248 offset:8192
ds_read_b64_tr_b16 a[126:127], v248 offset:10240
v_mfma_f32_16x16x32_bf16 v[100:103], a[84:87], a[72:75], v[100:103]
v_mfma_f32_16x16x32_bf16 v[104:107], a[88:91], a[72:75], v[104:107]
s_add_u32 s49, s46, s91
s_add_u32 s46, s46, s53
v_add_u32 v244, s53, v244
v_mfma_f32_16x16x32_bf16 v[108:111], a[92:95], a[72:75], v[108:111]
buffer_load_dwordx4 v7, s[4:7], s44 offen offset:2048 sc1 lds
v_mfma_f32_16x16x32_bf16 v[112:115], a[80:83], a[76:79], v[112:115]
v_add_u32 v245, s53, v245
v_add_u32 v246, s53, v246
v_mfma_f32_16x16x32_bf16 v[116:119], a[84:87], a[76:79], v[116:119]
buffer_load_dwordx4 v7, s[4:7], s44 offen offset:3072 sc1 lds
v_mfma_f32_16x16x32_bf16 v[120:123], a[88:91], a[76:79], v[120:123]
v_add_u32 v247, s53, v247
v_add_u32 v248, s53, v248
v_mfma_f32_16x16x32_bf16 v[124:127], a[92:95], a[76:79], v[124:127]
s_waitcnt vmcnt(24) lgkmcnt(0)
s_barrier
v_mfma_f32_16x16x32_bf16 v[64:67], a[112:115], a[96:99], v[64:67]
s_add_u32 m0, s49, 16384
s_add_u32 s44, s44, 4096
ds_read_b64_tr_b16 a[80:81], v245 offset:0
ds_read_b64_tr_b16 a[82:83], v245 offset:2048
v_mfma_f32_16x16x32_bf16 v[68:71], a[116:119], a[96:99], v[68:71]
buffer_load_dwordx4 v240, s[8:11], s45 offen offset:0 lds
v_mfma_f32_16x16x32_bf16 v[72:75], a[120:123], a[96:99], v[72:75]
ds_read_b128 a[64:67], v244 offset:0
ds_read_b128 a[68:71], v244 offset:256
v_mfma_f32_16x16x32_bf16 v[76:79], a[124:127], a[96:99], v[76:79]
ds_read_b64_tr_b16 a[84:85], v246 offset:0
ds_read_b64_tr_b16 a[86:87], v246 offset:2048
v_mfma_f32_16x16x32_bf16 v[80:83], a[112:115], a[100:103], v[80:83]
buffer_load_dwordx4 v241, s[8:11], s45 offen offset:1024 lds
v_mfma_f32_16x16x32_bf16 v[84:87], a[116:119], a[100:103], v[84:87]
ds_read_b64_tr_b16 a[88:89], v247 offset:0
ds_read_b64_tr_b16 a[90:91], v247 offset:2048
v_mfma_f32_16x16x32_bf16 v[88:91], a[120:123], a[100:103], v[88:91]
ds_read_b128 a[72:75], v244 offset:4096
ds_read_b128 a[76:79], v244 offset:4352
v_mfma_f32_16x16x32_bf16 v[92:95], a[124:127], a[100:103], v[92:95]
v_mfma_f32_16x16x32_bf16 v[96:99], a[112:115], a[104:107], v[96:99]
ds_read_b64_tr_b16 a[92:93], v248 offset:0
ds_read_b64_tr_b16 a[94:95], v248 offset:2048
v_mfma_f32_16x16x32_bf16 v[100:103], a[116:119], a[104:107], v[100:103]
buffer_load_dwordx4 v242, s[8:11], s45 offen offset:2048 lds
v_mfma_f32_16x16x32_bf16 v[104:107], a[120:123], a[104:107], v[104:107]
v_mfma_f32_16x16x32_bf16 v[108:111], a[124:127], a[104:107], v[108:111]
v_mfma_f32_16x16x32_bf16 v[112:115], a[112:115], a[108:111], v[112:115]
buffer_load_dwordx4 v243, s[8:11], s45 offen offset:3072 lds
v_mfma_f32_16x16x32_bf16 v[116:119], a[116:119], a[108:111], v[116:119]
s_add_u32 s45, s45, 131072
v_mfma_f32_16x16x32_bf16 v[120:123], a[120:123], a[108:111], v[120:123]
v_mfma_f32_16x16x32_bf16 v[124:127], a[124:127], a[108:111], v[124:127]
s_waitcnt lgkmcnt(0)
v_mfma_f32_16x16x32_bf16 v[128:131], a[80:83], a[64:67], 0
s_mov_b32 m0, s49
ds_read_b64_tr_b16 a[112:113], v245 offset:8192
ds_read_b64_tr_b16 a[114:115], v245 offset:10240
v_mfma_f32_16x16x32_bf16 v[132:135], a[84:87], a[64:67], 0
buffer_load_dwordx4 v7, s[4:7], s44 offen offset:0 sc1 lds
v_mfma_f32_16x16x32_bf16 v[136:139], a[88:91], a[64:67], 0
ds_read_b128 a[96:99], v244 offset:2048
ds_read_b128 a[100:103], v244 offset:2304
v_mfma_f32_16x16x32_bf16 v[140:143], a[92:95], a[64:67], 0
ds_read_b64_tr_b16 a[116:117], v246 offset:8192
ds_read_b64_tr_b16 a[118:119], v246 offset:10240
v_mfma_f32_16x16x32_bf16 v[144:147], a[80:83], a[68:71], 0
buffer_load_dwordx4 v7, s[4:7], s44 offen offset:1024 sc1 lds
v_mfma_f32_16x16x32_bf16 v[148:151], a[84:87], a[68:71], 0
ds_read_b64_tr_b16 a[120:121], v247 offset:8192
ds_read_b64_tr_b16 a[122:123], v247 offset:10240
v_mfma_f32_16x16x32_bf16 v[152:155], a[88:91], a[68:71], 0
ds_read_b128 a[104:107], v244 offset:6144
ds_read_b128 a[108:111], v244 offset:6400
v_mfma_f32_16x16x32_bf16 v[156:159], a[92:95], a[68:71], 0
v_mfma_f32_16x16x32_bf16 v[160:163], a[80:83], a[72:75], 0
s_cmp_eq_u32 s46, 0
s_cselect_b32 s53, s56, s57
ds_read_b64_tr_b16 a[124:125], v248 offset:8192
ds_read_b64_tr_b16 a[126:127], v248 offset:10240
v_mfma_f32_16x16x32_bf16 v[164:167], a[84:87], a[72:75], 0
v_mfma_f32_16x16x32_bf16 v[168:171], a[88:91], a[72:75], 0
s_add_u32 s49, s46, s91
s_add_u32 s46, s46, s53
v_add_u32 v244, s53, v244
v_mfma_f32_16x16x32_bf16 v[172:175], a[92:95], a[72:75], 0
buffer_load_dwordx4 v7, s[4:7], s44 offen offset:2048 sc1 lds
v_mfma_f32_16x16x32_bf16 v[176:179], a[80:83], a[76:79], 0
v_add_u32 v245, s53, v245
v_add_u32 v246, s53, v246
v_mfma_f32_16x16x32_bf16 v[180:183], a[84:87], a[76:79], 0
buffer_load_dwordx4 v7, s[4:7], s44 offen offset:3072 sc1 lds
v_mfma_f32_16x16x32_bf16 v[184:187], a[88:91], a[76:79], 0
v_add_u32 v247, s53, v247
v_add_u32 v248, s53, v248
v_mfma_f32_16x16x32_bf16 v[188:191], a[92:95], a[76:79], 0
s_waitcnt vmcnt(24) lgkmcnt(0)
s_barrier
s_mov_b32 s32, 4096
v_mfma_f32_16x16x32_bf16 v[128:131], a[112:115], a[96:99], v[128:131]
.Lpad21:
s_cbranch_execz .Ltch10
s_add_u32 m0, s49, 16384
s_add_u32 s44, s44, 4096
ds_read_b64_tr_b16 a[80:81], v245 offset:0
ds_read_b64_tr_b16 a[82:83], v245 offset:2048
v_mfma_f32_16x16x32_bf16 v[132:135], a[116:119], a[96:99], v[132:135]
buffer_load_dwordx4 v240, s[8:11], s45 offen offset:0 lds
v_cvt_pk_bf16_f32 v64, v64, v65
v_mfma_f32_16x16x32_bf16 v[136:139], a[120:123], a[96:99], v[136:139]
ds_read_b128 a[64:67], v244 offset:0
ds_read_b128 a[68:71], v244 offset:256
v_mfma_f32_16x16x32_bf16 v[140:143], a[124:127], a[96:99], v[140:143]
ds_read_b64_tr_b16 a[84:85], v246 offset:0
ds_read_b64_tr_b16 a[86:87], v246 offset:2048
v_mfma_f32_16x16x32_bf16 v[144:147], a[112:115], a[100:103], v[144:147]
buffer_load_dwordx4 v241, s[8:11], s45 offen offset:1024 lds
v_cvt_pk_bf16_f32 v65, v66, v67
v_mfma_f32_16x16x32_bf16 v[148:151], a[116:119], a[100:103], v[148:151]
ds_read_b64_tr_b16 a[88:89], v247 offset:0
ds_read_b64_tr_b16 a[90:91], v247 offset:2048
v_cvt_pk_bf16_f32 v66, v68, v69
v_mfma_f32_16x16x32_bf16 v[152:155], a[120:123], a[100:103], v[152:155]
ds_read_b128 a[72:75], v244 offset:4096
ds_read_b128 a[76:79], v244 offset:4352
v_cvt_pk_bf16_f32 v67, v70, v71
v_mfma_f32_16x16x32_bf16 v[156:159], a[124:127], a[100:103], v[156:159]
buffer_store_dwordx4 v[64:67], v7, s[12:15], 0 offen offset:0 sc1
v_mfma_f32_16x16x32_bf16 v[160:163], a[112:115], a[104:107], v[160:163]
ds_read_b64_tr_b16 a[92:93], v248 offset:0
ds_read_b64_tr_b16 a[94:95], v248 offset:2048
v_mfma_f32_16x16x32_bf16 v[164:167], a[116:119], a[104:107], v[164:167]
buffer_load_dwordx4 v242, s[8:11], s45 offen offset:2048 lds
v_mfma_f32_16x16x32_bf16 v[168:171], a[120:123], a[104:107], v[168:171]
v_mfma_f32_16x16x32_bf16 v[172:175], a[124:127], a[104:107], v[172:175]
v_mfma_f32_16x16x32_bf16 v[176:179], a[112:115], a[108:111], v[176:179]
buffer_load_dwordx4 v243, s[8:11], s45 offen offset:3072 lds
v_mfma_f32_16x16x32_bf16 v[180:183], a[116:119], a[108:111], v[180:183]
s_add_u32 s45, s45, 131072
v_mfma_f32_16x16x32_bf16 v[184:187], a[120:123], a[108:111], v[184:187]
v_mfma_f32_16x16x32_bf16 v[188:191], a[124:127], a[108:111], v[188:191]
s_waitcnt lgkmcnt(0)
v_mfma_f32_16x16x32_bf16 v[128:131], a[80:83], a[64:67], v[128:131]
s_mov_b32 m0, s49
ds_read_b64_tr_b16 a[112:113], v245 offset:8192
ds_read_b64_tr_b16 a[114:115], v245 offset:10240
v_mfma_f32_16x16x32_bf16 v[132:135], a[84:87], a[64:67], v[132:135]
buffer_load_dwordx4 v7, s[4:7], s44 offen offset:0 sc1 lds
v_cvt_pk_bf16_f32 v72, v72, v73
v_mfma_f32_16x16x32_bf16 v[136:139], a[88:91], a[64:67], v[136:139]
ds_read_b128 a[96:99], v244 offset:2048
ds_read_b128 a[100:103], v244 offset:2304
v_mfma_f32_16x16x32_bf16 v[140:143], a[92:95], a[64:67], v[140:143]
ds_read_b64_tr_b16 a[116:117], v246 offset:8192
ds_read_b64_tr_b16 a[118:119], v246 offset:10240
v_mfma_f32_16x16x32_bf16 v[144:147], a[80:83], a[68:71], v[144:147]
buffer_load_dwordx4 v7, s[4:7], s44 offen offset:1024 sc1 lds
v_cvt_pk_bf16_f32 v73, v74, v75
v_mfma_f32_16x16x32_bf16 v[148:151], a[84:87], a[68:71], v[148:151]
ds_read_b64_tr_b16 a[120:121], v247 offset:8192
ds_read_b64_tr_b16 a[122:123], v247 offset:10240
v_cvt_pk_bf16_f32 v74, v76, v77
v_mfma_f32_16x16x32_bf16 v[152:155], a[88:91], a[68:71], v[152:155]
ds_read_b128 a[104:107], v244 offset:6144
ds_read_b128 a[108:111], v244 offset:6400
v_cvt_pk_bf16_f32 v75, v78, v79
v_mfma_f32_16x16x32_bf16 v[156:159], a[92:95], a[68:71], v[156:159]
buffer_store_dwordx4 v[72:75], v7, s[12:15], 0 offen offset:1024 sc1
v_mfma_f32_16x16x32_bf16 v[160:163], a[80:83], a[72:75], v[160:163]
s_cmp_eq_u32 s46, 0
s_cselect_b32 s53, s56, s57
ds_read_b64_tr_b16 a[124:125], v248 offset:8192
ds_read_b64_tr_b16 a[126:127], v248 offset:10240
v_mfma_f32_16x16x32_bf16 v[164:167], a[84:87], a[72:75], v[164:167]
v_mfma_f32_16x16x32_bf16 v[168:171], a[88:91], a[72:75], v[168:171]
s_add_u32 s49, s46, s91
s_add_u32 s46, s46, s53
v_add_u32 v244, s53, v244
v_mfma_f32_16x16x32_bf16 v[172:175], a[92:95], a[72:75], v[172:175]
buffer_load_dwordx4 v7, s[4:7], s44 offen offset:2048 sc1 lds
v_mfma_f32_16x16x32_bf16 v[176:179], a[80:83], a[76:79], v[176:179]
v_add_u32 v245, s53, v245
v_add_u32 v246, s53, v246
v_mfma_f32_16x16x32_bf16 v[180:183], a[84:87], a[76:79], v[180:183]
buffer_load_dwordx4 v7, s[4:7], s44 offen offset:3072 sc1 lds
v_mfma_f32_16x16x32_bf16 v[184:187], a[88:91], a[76:79], v[184:187]
v_add_u32 v247, s53, v247
v_add_u32 v248, s53, v248
v_mfma_f32_16x16x32_bf16 v[188:191], a[92:95], a[76:79], v[188:191]
s_waitcnt vmcnt(26) lgkmcnt(0)
s_barrier
v_mfma_f32_16x16x32_bf16 v[128:131], a[112:115], a[96:99], v[128:131]
s_add_u32 m0, s49, 16384
s_add_u32 s44, s44, 4096
ds_read_b64_tr_b16 a[80:81], v245 offset:0
ds_read_b64_tr_b16 a[82:83], v245 offset:2048
v_mfma_f32_16x16x32_bf16 v[132:135], a[116:119], a[96:99], v[132:135]
buffer_load_dwordx4 v240, s[8:11], s45 offen offset:0 lds
v_cvt_pk_bf16_f32 v80, v80, v81
v_mfma_f32_16x16x32_bf16 v[136:139], a[120:123], a[96:99], v[136:139]
ds_read_b128 a[64:67], v244 offset:0
ds_read_b128 a[68:71], v244 offset:256
v_mfma_f32_16x16x32_bf16 v[140:143], a[124:127], a[96:99], v[140:143]
ds_read_b64_tr_b16 a[84:85], v246 offset:0
ds_read_b64_tr_b16 a[86:87], v246 offset:2048
v_mfma_f32_16x16x32_bf16 v[144:147], a[112:115], a[100:103], v[144:147]
buffer_load_dwordx4 v241, s[8:11], s45 offen offset:1024 lds
v_cvt_pk_bf16_f32 v81, v82, v83
v_mfma_f32_16x16x32_bf16 v[148:151], a[116:119], a[100:103], v[148:151]
ds_read_b64_tr_b16 a[88:89], v247 offset:0
ds_read_b64_tr_b16 a[90:91], v247 offset:2048
v_cvt_pk_bf16_f32 v82, v84, v85
v_mfma_f32_16x16x32_bf16 v[152:155], a[120:123], a[100:103], v[152:155]
ds_read_b128 a[72:75], v244 offset:4096
ds_read_b128 a[76:79], v244 offset:4352
v_cvt_pk_bf16_f32 v83, v86, v87
v_mfma_f32_16x16x32_bf16 v[156:159], a[124:127], a[100:103], v[156:159]
buffer_store_dwordx4 v[80:83], v7, s[12:15], 0 offen offset:2048 sc1
v_mfma_f32_16x16x32_bf16 v[160:163], a[112:115], a[104:107], v[160:163]
ds_read_b64_tr_b16 a[92:93], v248 offset:0
ds_read_b64_tr_b16 a[94:95], v248 offset:2048
v_mfma_f32_16x16x32_bf16 v[164:167], a[116:119], a[104:107], v[164:167]
buffer_load_dwordx4 v242, s[8:11], s45 offen offset:2048 lds
v_mfma_f32_16x16x32_bf16 v[168:171], a[120:123], a[104:107], v[168:171]
v_mfma_f32_16x16x32_bf16 v[172:175], a[124:127], a[104:107], v[172:175]
v_mfma_f32_16x16x32_bf16 v[176:179], a[112:115], a[108:111], v[176:179]
buffer_load_dwordx4 v243, s[8:11], s45 offen offset:3072 lds
v_mfma_f32_16x16x32_bf16 v[180:183], a[116:119], a[108:111], v[180:183]
s_add_u32 s45, s45, 131072
v_mfma_f32_16x16x32_bf16 v[184:187], a[120:123], a[108:111], v[184:187]
v_mfma_f32_16x16x32_bf16 v[188:191], a[124:127], a[108:111], v[188:191]
s_waitcnt lgkmcnt(0)
v_mfma_f32_16x16x32_bf16 v[128:131], a[80:83], a[64:67], v[128:131]
s_mov_b32 m0, s49
ds_read_b64_tr_b16 a[112:113], v245 offset:8192
ds_read_b64_tr_b16 a[114:115], v245 offset:10240
v_mfma_f32_16x16x32_bf16 v[132:135], a[84:87], a[64:67], v[132:135]
buffer_load_dwordx4 v7, s[4:7], s44 offen offset:0 sc1 lds
v_cvt_pk_bf16_f32 v88, v88, v89
v_mfma_f32_16x16x32_bf16 v[136:139], a[88:91], a[64:67], v[136:139]
ds_read_b128 a[96:99], v244 offset:2048
ds_read_b128 a[100:103], v244 offset:2304
v_mfma_f32_16x16x32_bf16 v[140:143], a[92:95], a[64:67], v[140:143]
ds_read_b64_tr_b16 a[116:117], v246 offset:8192
ds_read_b64_tr_b16 a[118:119], v246 offset:10240
v_mfma_f32_16x16x32_bf16 v[144:147], a[80:83], a[68:71], v[144:147]
buffer_load_dwordx4 v7, s[4:7], s44 offen offset:1024 sc1 lds
v_cvt_pk_bf16_f32 v89, v90, v91
v_mfma_f32_16x16x32_bf16 v[148:151], a[84:87], a[68:71], v[148:151]
ds_read_b64_tr_b16 a[120:121], v247 offset:8192
ds_read_b64_tr_b16 a[122:123], v247 offset:10240
v_cvt_pk_bf16_f32 v90, v92, v93
v_mfma_f32_16x16x32_bf16 v[152:155], a[88:91], a[68:71], v[152:155]
ds_read_b128 a[104:107], v244 offset:6144
ds_read_b128 a[108:111], v244 offset:6400
v_cvt_pk_bf16_f32 v91, v94, v95
v_mfma_f32_16x16x32_bf16 v[156:159], a[92:95], a[68:71], v[156:159]
buffer_store_dwordx4 v[88:91], v7, s[12:15], 0 offen offset:3072 sc1
v_mfma_f32_16x16x32_bf16 v[160:163], a[80:83], a[72:75], v[160:163]
s_cmp_eq_u32 s46, 0
s_cselect_b32 s53, s56, s57
ds_read_b64_tr_b16 a[124:125], v248 offset:8192
ds_read_b64_tr_b16 a[126:127], v248 offset:10240
v_mfma_f32_16x16x32_bf16 v[164:167], a[84:87], a[72:75], v[164:167]
v_mfma_f32_16x16x32_bf16 v[168:171], a[88:91], a[72:75], v[168:171]
s_add_u32 s49, s46, s91
s_add_u32 s46, s46, s53
v_add_u32 v244, s53, v244
v_mfma_f32_16x16x32_bf16 v[172:175], a[92:95], a[72:75], v[172:175]
buffer_load_dwordx4 v7, s[4:7], s44 offen offset:2048 sc1 lds
v_mfma_f32_16x16x32_bf16 v[176:179], a[80:83], a[76:79], v[176:179]
v_add_u32 v245, s53, v245
v_add_u32 v246, s53, v246
v_mfma_f32_16x16x32_bf16 v[180:183], a[84:87], a[76:79], v[180:183]
buffer_load_dwordx4 v7, s[4:7], s44 offen offset:3072 sc1 lds
v_mfma_f32_16x16x32_bf16 v[184:187], a[88:91], a[76:79], v[184:187]
v_add_u32 v247, s53, v247
v_add_u32 v248, s53, v248
v_mfma_f32_16x16x32_bf16 v[188:191], a[92:95], a[76:79], v[188:191]
s_waitcnt vmcnt(28) lgkmcnt(0)
s_barrier
v_mfma_f32_16x16x32_bf16 v[128:131], a[112:115], a[96:99], v[128:131]
s_add_u32 m0, s49, 16384
s_add_u32 s44, s44, 4096
ds_read_b64_tr_b16 a[80:81], v245 offset:0
ds_read_b64_tr_b16 a[82:83], v245 offset:2048
v_mfma_f32_16x16x32_bf16 v[132:135], a[116:119], a[96:99], v[132:135]
buffer_load_dwordx4 v240, s[8:11], s45 offen offset:0 lds
v_cvt_pk_bf16_f32 v96, v96, v97
v_mfma_f32_16x16x32_bf16 v[136:139], a[120:123], a[96:99], v[136:139]
ds_read_b128 a[64:67], v244 offset:0
ds_read_b128 a[68:71], v244 offset:256
v_mfma_f32_16x16x32_bf16 v[140:143], a[124:127], a[96:99], v[140:143]
ds_read_b64_tr_b16 a[84:85], v246 offset:0
ds_read_b64_tr_b16 a[86:87], v246 offset:2048
v_mfma_f32_16x16x32_bf16 v[144:147], a[112:115], a[100:103], v[144:147]
buffer_load_dwordx4 v241, s[8:11], s45 offen offset:1024 lds
v_cvt_pk_bf16_f32 v97, v98, v99
v_mfma_f32_16x16x32_bf16 v[148:151], a[116:119], a[100:103], v[148:151]
ds_read_b64_tr_b16 a[88:89], v247 offset:0
ds_read_b64_tr_b16 a[90:91], v247 offset:2048
v_cvt_pk_bf16_f32 v98, v100, v101
v_mfma_f32_16x16x32_bf16 v[152:155], a[120:123], a[100:103], v[152:155]
ds_read_b128 a[72:75], v244 offset:4096
ds_read_b128 a[76:79], v244 offset:4352
v_cvt_pk_bf16_f32 v99, v102, v103
v_mfma_f32_16x16x32_bf16 v[156:159], a[124:127], a[100:103], v[156:159]
buffer_store_dwordx4 v[96:99], v7, s[12:15], s32 offen offset:0 sc1
v_mfma_f32_16x16x32_bf16 v[160:163], a[112:115], a[104:107], v[160:163]
ds_read_b64_tr_b16 a[92:93], v248 offset:0
ds_read_b64_tr_b16 a[94:95], v248 offset:2048
v_mfma_f32_16x16x32_bf16 v[164:167], a[116:119], a[104:107], v[164:167]
buffer_load_dwordx4 v242, s[8:11], s45 offen offset:2048 lds
v_mfma_f32_16x16x32_bf16 v[168:171], a[120:123], a[104:107], v[168:171]
v_mfma_f32_16x16x32_bf16 v[172:175], a[124:127], a[104:107], v[172:175]
v_mfma_f32_16x16x32_bf16 v[176:179], a[112:115], a[108:111], v[176:179]
buffer_load_dwordx4 v243, s[8:11], s45 offen offset:3072 lds
v_mfma_f32_16x16x32_bf16 v[180:183], a[116:119], a[108:111], v[180:183]
s_add_u32 s45, s45, 131072
v_mfma_f32_16x16x32_bf16 v[184:187], a[120:123], a[108:111], v[184:187]
v_mfma_f32_16x16x32_bf16 v[188:191], a[124:127], a[108:111], v[188:191]
s_waitcnt lgkmcnt(0)
v_mfma_f32_16x16x32_bf16 v[128:131], a[80:83], a[64:67], v[128:131]
s_mov_b32 m0, s49
ds_read_b64_tr_b16 a[112:113], v245 offset:8192
ds_read_b64_tr_b16 a[114:115], v245 offset:10240
v_mfma_f32_16x16x32_bf16 v[132:135], a[84:87], a[64:67], v[132:135]
buffer_load_dwordx4 v7, s[4:7], s44 offen offset:0 sc1 lds
v_cvt_pk_bf16_f32 v104, v104, v105
v_mfma_f32_16x16x32_bf16 v[136:139], a[88:91], a[64:67], v[136:139]
ds_read_b128 a[96:99], v244 offset:2048
ds_read_b128 a[100:103], v244 offset:2304
v_mfma_f32_16x16x32_bf16 v[140:143], a[92:95], a[64:67], v[140:143]
ds_read_b64_tr_b16 a[116:117], v246 offset:8192
ds_read_b64_tr_b16 a[118:119], v246 offset:10240
v_mfma_f32_16x16x32_bf16 v[144:147], a[80:83], a[68:71], v[144:147]
buffer_load_dwordx4 v7, s[4:7], s44 offen offset:1024 sc1 lds
v_cvt_pk_bf16_f32 v105, v106, v107
v_mfma_f32_16x16x32_bf16 v[148:151], a[84:87], a[68:71], v[148:151]
ds_read_b64_tr_b16 a[120:121], v247 offset:8192
ds_read_b64_tr_b16 a[122:123], v247 offset:10240
v_cvt_pk_bf16_f32 v106, v108, v109
v_mfma_f32_16x16x32_bf16 v[152:155], a[88:91], a[68:71], v[152:155]
ds_read_b128 a[104:107], v244 offset:6144
ds_read_b128 a[108:111], v244 offset:6400
v_cvt_pk_bf16_f32 v107, v110, v111
v_mfma_f32_16x16x32_bf16 v[156:159], a[92:95], a[68:71], v[156:159]
buffer_store_dwordx4 v[104:107], v7, s[12:15], s32 offen offset:1024 sc1
v_mfma_f32_16x16x32_bf16 v[160:163], a[80:83], a[72:75], v[160:163]
s_cmp_eq_u32 s46, 0
s_cselect_b32 s53, s56, s57
ds_read_b64_tr_b16 a[124:125], v248 offset:8192
ds_read_b64_tr_b16 a[126:127], v248 offset:10240
v_mfma_f32_16x16x32_bf16 v[164:167], a[84:87], a[72:75], v[164:167]
v_mfma_f32_16x16x32_bf16 v[168:171], a[88:91], a[72:75], v[168:171]
s_add_u32 s49, s46, s91
s_add_u32 s46, s46, s53
v_add_u32 v244, s53, v244
v_mfma_f32_16x16x32_bf16 v[172:175], a[92:95], a[72:75], v[172:175]
buffer_load_dwordx4 v7, s[4:7], s44 offen offset:2048 sc1 lds
v_mfma_f32_16x16x32_bf16 v[176:179], a[80:83], a[76:79], v[176:179]
v_add_u32 v245, s53, v245
v_add_u32 v246, s53, v246
v_mfma_f32_16x16x32_bf16 v[180:183], a[84:87], a[76:79], v[180:183]
buffer_load_dwordx4 v7, s[4:7], s44 offen offset:3072 sc1 lds
v_mfma_f32_16x16x32_bf16 v[184:187], a[88:91], a[76:79], v[184:187]
v_add_u32 v247, s53, v247
v_add_u32 v248, s53, v248
v_mfma_f32_16x16x32_bf16 v[188:191], a[92:95], a[76:79], v[188:191]
s_waitcnt vmcnt(30) lgkmcnt(0)
s_barrier
v_mfma_f32_16x16x32_bf16 v[128:131], a[112:115], a[96:99], v[128:131]
s_add_u32 m0, s49, 16384
s_add_u32 s44, s44, 4096
ds_read_b64_tr_b16 a[80:81], v245 offset:0
ds_read_b64_tr_b16 a[82:83], v245 offset:2048
v_mfma_f32_16x16x32_bf16 v[132:135], a[116:119], a[96:99], v[132:135]
buffer_load_dwordx4 v240, s[8:11], s45 offen offset:0 lds
v_cvt_pk_bf16_f32 v112, v112, v113
v_mfma_f32_16x16x32_bf16 v[136:139], a[120:123], a[96:99], v[136:139]
ds_read_b128 a[64:67], v244 offset:0
ds_read_b128 a[68:71], v244 offset:256
v_mfma_f32_16x16x32_bf16 v[140:143], a[124:127], a[96:99], v[140:143]
ds_read_b64_tr_b16 a[84:85], v246 offset:0
ds_read_b64_tr_b16 a[86:87], v246 offset:2048
v_mfma_f32_16x16x32_bf16 v[144:147], a[112:115], a[100:103], v[144:147]
buffer_load_dwordx4 v241, s[8:11], s45 offen offset:1024 lds
v_cvt_pk_bf16_f32 v113, v114, v115
v_mfma_f32_16x16x32_bf16 v[148:151], a[116:119], a[100:103], v[148:151]
ds_read_b64_tr_b16 a[88:89], v247 offset:0
ds_read_b64_tr_b16 a[90:91], v247 offset:2048
v_cvt_pk_bf16_f32 v114, v116, v117
v_mfma_f32_16x16x32_bf16 v[152:155], a[120:123], a[100:103], v[152:155]
ds_read_b128 a[72:75], v244 offset:4096
ds_read_b128 a[76:79], v244 offset:4352
v_cvt_pk_bf16_f32 v115, v118, v119
v_mfma_f32_16x16x32_bf16 v[156:159], a[124:127], a[100:103], v[156:159]
buffer_store_dwordx4 v[112:115], v7, s[12:15], s32 offen offset:2048 sc1
v_mfma_f32_16x16x32_bf16 v[160:163], a[112:115], a[104:107], v[160:163]
ds_read_b64_tr_b16 a[92:93], v248 offset:0
ds_read_b64_tr_b16 a[94:95], v248 offset:2048
v_mfma_f32_16x16x32_bf16 v[164:167], a[116:119], a[104:107], v[164:167]
buffer_load_dwordx4 v242, s[8:11], s45 offen offset:2048 lds
v_mfma_f32_16x16x32_bf16 v[168:171], a[120:123], a[104:107], v[168:171]
v_mfma_f32_16x16x32_bf16 v[172:175], a[124:127], a[104:107], v[172:175]
v_mfma_f32_16x16x32_bf16 v[176:179], a[112:115], a[108:111], v[176:179]
buffer_load_dwordx4 v243, s[8:11], s45 offen offset:3072 lds
v_mfma_f32_16x16x32_bf16 v[180:183], a[116:119], a[108:111], v[180:183]
s_add_u32 s45, s45, 131072
v_mfma_f32_16x16x32_bf16 v[184:187], a[120:123], a[108:111], v[184:187]
v_mfma_f32_16x16x32_bf16 v[188:191], a[124:127], a[108:111], v[188:191]
s_waitcnt lgkmcnt(0)
v_mfma_f32_16x16x32_bf16 v[128:131], a[80:83], a[64:67], v[128:131]
s_mov_b32 m0, s49
ds_read_b64_tr_b16 a[112:113], v245 offset:8192
ds_read_b64_tr_b16 a[114:115], v245 offset:10240
v_mfma_f32_16x16x32_bf16 v[132:135], a[84:87], a[64:67], v[132:135]
buffer_load_dwordx4 v7, s[4:7], s44 offen offset:0 sc1 lds
v_cvt_pk_bf16_f32 v120, v120, v121
v_mfma_f32_16x16x32_bf16 v[136:139], a[88:91], a[64:67], v[136:139]
ds_read_b128 a[96:99], v244 offset:2048
ds_read_b128 a[100:103], v244 offset:2304
v_mfma_f32_16x16x32_bf16 v[140:143], a[92:95], a[64:67], v[140:143]
ds_read_b64_tr_b16 a[116:117], v246 offset:8192
ds_read_b64_tr_b16 a[118:119], v246 offset:10240
v_mfma_f32_16x16x32_bf16 v[144:147], a[80:83], a[68:71], v[144:147]
buffer_load_dwordx4 v7, s[4:7], s44 offen offset:1024 sc1 lds
v_cvt_pk_bf16_f32 v121, v122, v123
v_mfma_f32_16x16x32_bf16 v[148:151], a[84:87], a[68:71], v[148:151]
ds_read_b64_tr_b16 a[120:121], v247 offset:8192
ds_read_b64_tr_b16 a[122:123], v247 offset:10240
v_cvt_pk_bf16_f32 v122, v124, v125
v_mfma_f32_16x16x32_bf16 v[152:155], a[88:91], a[68:71], v[152:155]
ds_read_b128 a[104:107], v244 offset:6144
ds_read_b128 a[108:111], v244 offset:6400
v_cvt_pk_bf16_f32 v123, v126, v127
v_mfma_f32_16x16x32_bf16 v[156:159], a[92:95], a[68:71], v[156:159]
buffer_store_dwordx4 v[120:123], v7, s[12:15], s32 offen offset:3072 sc1
v_mfma_f32_16x16x32_bf16 v[160:163], a[80:83], a[72:75], v[160:163]
s_cmp_eq_u32 s46, 0
s_cselect_b32 s53, s56, s57
ds_read_b64_tr_b16 a[124:125], v248 offset:8192
ds_read_b64_tr_b16 a[126:127], v248 offset:10240
v_mfma_f32_16x16x32_bf16 v[164:167], a[84:87], a[72:75], v[164:167]
v_mfma_f32_16x16x32_bf16 v[168:171], a[88:91], a[72:75], v[168:171]
s_add_u32 s49, s46, s91
s_add_u32 s46, s46, s53
v_add_u32 v244, s53, v244
v_mfma_f32_16x16x32_bf16 v[172:175], a[92:95], a[72:75], v[172:175]
buffer_load_dwordx4 v7, s[4:7], s44 offen offset:2048 sc1 lds
v_mfma_f32_16x16x32_bf16 v[176:179], a[80:83], a[76:79], v[176:179]
v_add_u32 v245, s53, v245
v_add_u32 v246, s53, v246
v_mfma_f32_16x16x32_bf16 v[180:183], a[84:87], a[76:79], v[180:183]
buffer_load_dwordx4 v7, s[4:7], s44 offen offset:3072 sc1 lds
v_mfma_f32_16x16x32_bf16 v[184:187], a[88:91], a[76:79], v[184:187]
v_add_u32 v247, s53, v247
v_add_u32 v248, s53, v248
v_mfma_f32_16x16x32_bf16 v[188:191], a[92:95], a[76:79], v[188:191]
s_waitcnt vmcnt(30) lgkmcnt(0)
s_barrier
v_mfma_f32_16x16x32_bf16 v[128:131], a[112:115], a[96:99], v[128:131]
s_add_u32 m0, s49, 16384
s_add_u32 s44, s44, 4096
ds_read_b64_tr_b16 a[80:81], v245 offset:0
ds_read_b64_tr_b16 a[82:83], v245 offset:2048
v_mfma_f32_16x16x32_bf16 v[132:135], a[116:119], a[96:99], v[132:135]
buffer_load_dwordx4 v240, s[8:11], s45 offen offset:0 lds
v_mfma_f32_16x16x32_bf16 v[136:139], a[120:123], a[96:99], v[136:139]
ds_read_b128 a[64:67], v244 offset:0
ds_read_b128 a[68:71], v244 offset:256
v_mfma_f32_16x16x32_bf16 v[140:143], a[124:127], a[96:99], v[140:143]
ds_read_b64_tr_b16 a[84:85], v246 offset:0
ds_read_b64_tr_b16 a[86:87], v246 offset:2048
v_mfma_f32_16x16x32_bf16 v[144:147], a[112:115], a[100:103], v[144:147]
buffer_load_dwordx4 v241, s[8:11], s45 offen offset:1024 lds
v_mfma_f32_16x16x32_bf16 v[148:151], a[116:119], a[100:103], v[148:151]
ds_read_b64_tr_b16 a[88:89], v247 offset:0
ds_read_b64_tr_b16 a[90:91], v247 offset:2048
v_mfma_f32_16x16x32_bf16 v[152:155], a[120:123], a[100:103], v[152:155]
ds_read_b128 a[72:75], v244 offset:4096
ds_read_b128 a[76:79], v244 offset:4352
v_mfma_f32_16x16x32_bf16 v[156:159], a[124:127], a[100:103], v[156:159]
v_mfma_f32_16x16x32_bf16 v[160:163], a[112:115], a[104:107], v[160:163]
ds_read_b64_tr_b16 a[92:93], v248 offset:0
ds_read_b64_tr_b16 a[94:95], v248 offset:2048
v_mfma_f32_16x16x32_bf16 v[164:167], a[116:119], a[104:107], v[164:167]
buffer_load_dwordx4 v242, s[8:11], s45 offen offset:2048 lds
v_mfma_f32_16x16x32_bf16 v[168:171], a[120:123], a[104:107], v[168:171]
v_mfma_f32_16x16x32_bf16 v[172:175], a[124:127], a[104:107], v[172:175]
v_mfma_f32_16x16x32_bf16 v[176:179], a[112:115], a[108:111], v[176:179]
buffer_load_dwordx4 v243, s[8:11], s45 offen offset:3072 lds
v_mfma_f32_16x16x32_bf16 v[180:183], a[116:119], a[108:111], v[180:183]
s_add_u32 s45, s45, 131072
v_mfma_f32_16x16x32_bf16 v[184:187], a[120:123], a[108:111], v[184:187]
v_mfma_f32_16x16x32_bf16 v[188:191], a[124:127], a[108:111], v[188:191]
s_waitcnt lgkmcnt(0)
v_mfma_f32_16x16x32_bf16 v[128:131], a[80:83], a[64:67], v[128:131]
s_mov_b32 m0, s49
ds_read_b64_tr_b16 a[112:113], v245 offset:8192
ds_read_b64_tr_b16 a[114:115], v245 offset:10240
v_mfma_f32_16x16x32_bf16 v[132:135], a[84:87], a[64:67], v[132:135]
buffer_load_dwordx4 v7, s[4:7], s44 offen offset:0 sc1 lds
v_mfma_f32_16x16x32_bf16 v[136:139], a[88:91], a[64:67], v[136:139]
ds_read_b128 a[96:99], v244 offset:2048
ds_read_b128 a[100:103], v244 offset:2304
v_mfma_f32_16x16x32_bf16 v[140:143], a[92:95], a[64:67], v[140:143]
ds_read_b64_tr_b16 a[116:117], v246 offset:8192
ds_read_b64_tr_b16 a[118:119], v246 offset:10240
v_mfma_f32_16x16x32_bf16 v[144:147], a[80:83], a[68:71], v[144:147]
buffer_load_dwordx4 v7, s[4:7], s44 offen offset:1024 sc1 lds
v_mfma_f32_16x16x32_bf16 v[148:151], a[84:87], a[68:71], v[148:151]
ds_read_b64_tr_b16 a[120:121], v247 offset:8192
ds_read_b64_tr_b16 a[122:123], v247 offset:10240
v_mfma_f32_16x16x32_bf16 v[152:155], a[88:91], a[68:71], v[152:155]
ds_read_b128 a[104:107], v244 offset:6144
ds_read_b128 a[108:111], v244 offset:6400
v_mfma_f32_16x16x32_bf16 v[156:159], a[92:95], a[68:71], v[156:159]
v_mfma_f32_16x16x32_bf16 v[160:163], a[80:83], a[72:75], v[160:163]
s_cmp_eq_u32 s46, 0
s_cselect_b32 s53, s56, s57
ds_read_b64_tr_b16 a[124:125], v248 offset:8192
ds_read_b64_tr_b16 a[126:127], v248 offset:10240
v_mfma_f32_16x16x32_bf16 v[164:167], a[84:87], a[72:75], v[164:167]
v_mfma_f32_16x16x32_bf16 v[168:171], a[88:91], a[72:75], v[168:171]
s_add_u32 s49, s46, s91
s_add_u32 s46, s46, s53
v_add_u32 v244, s53, v244
v_mfma_f32_16x16x32_bf16 v[172:175], a[92:95], a[72:75], v[172:175]
buffer_load_dwordx4 v7, s[4:7], s44 offen offset:2048 sc1 lds
v_mfma_f32_16x16x32_bf16 v[176:179], a[80:83], a[76:79], v[176:179]
v_add_u32 v245, s53, v245
v_add_u32 v246, s53, v246
v_mfma_f32_16x16x32_bf16 v[180:183], a[84:87], a[76:79], v[180:183]
buffer_load_dwordx4 v7, s[4:7], s44 offen offset:3072 sc1 lds
v_mfma_f32_16x16x32_bf16 v[184:187], a[88:91], a[76:79], v[184:187]
v_add_u32 v247, s53, v247
v_add_u32 v248, s53, v248
v_mfma_f32_16x16x32_bf16 v[188:191], a[92:95], a[76:79], v[188:191]
s_waitcnt vmcnt(28) lgkmcnt(0)
s_barrier
v_mfma_f32_16x16x32_bf16 v[128:131], a[112:115], a[96:99], v[128:131]
s_add_u32 m0, s49, 16384
s_add_u32 s44, s44, 4096
ds_read_b64_tr_b16 a[80:81], v245 offset:0
ds_read_b64_tr_b16 a[82:83], v245 offset:2048
v_mfma_f32_16x16x32_bf16 v[132:135], a[116:119], a[96:99], v[132:135]
buffer_load_dwordx4 v240, s[8:11], s45 offen offset:0 lds
v_mfma_f32_16x16x32_bf16 v[136:139], a[120:123], a[96:99], v[136:139]
ds_read_b128 a[64:67], v244 offset:0
ds_read_b128 a[68:71], v244 offset:256
v_mfma_f32_16x16x32_bf16 v[140:143], a[124:127], a[96:99], v[140:143]
ds_read_b64_tr_b16 a[84:85], v246 offset:0
ds_read_b64_tr_b16 a[86:87], v246 offset:2048
v_mfma_f32_16x16x32_bf16 v[144:147], a[112:115], a[100:103], v[144:147]
buffer_load_dwordx4 v241, s[8:11], s45 offen offset:1024 lds
v_mfma_f32_16x16x32_bf16 v[148:151], a[116:119], a[100:103], v[148:151]
ds_read_b64_tr_b16 a[88:89], v247 offset:0
ds_read_b64_tr_b16 a[90:91], v247 offset:2048
v_mfma_f32_16x16x32_bf16 v[152:155], a[120:123], a[100:103], v[152:155]
ds_read_b128 a[72:75], v244 offset:4096
ds_read_b128 a[76:79], v244 offset:4352
v_mfma_f32_16x16x32_bf16 v[156:159], a[124:127], a[100:103], v[156:159]
v_mfma_f32_16x16x32_bf16 v[160:163], a[112:115], a[104:107], v[160:163]
ds_read_b64_tr_b16 a[92:93], v248 offset:0
ds_read_b64_tr_b16 a[94:95], v248 offset:2048
v_mfma_f32_16x16x32_bf16 v[164:167], a[116:119], a[104:107], v[164:167]
buffer_load_dwordx4 v242, s[8:11], s45 offen offset:2048 lds
v_mfma_f32_16x16x32_bf16 v[168:171], a[120:123], a[104:107], v[168:171]
v_mfma_f32_16x16x32_bf16 v[172:175], a[124:127], a[104:107], v[172:175]
v_mfma_f32_16x16x32_bf16 v[176:179], a[112:115], a[108:111], v[176:179]
buffer_load_dwordx4 v243, s[8:11], s45 offen offset:3072 lds
v_mfma_f32_16x16x32_bf16 v[180:183], a[116:119], a[108:111], v[180:183]
s_add_u32 s45, s45, 131072
v_mfma_f32_16x16x32_bf16 v[184:187], a[120:123], a[108:111], v[184:187]
v_mfma_f32_16x16x32_bf16 v[188:191], a[124:127], a[108:111], v[188:191]
s_waitcnt lgkmcnt(0)
v_mfma_f32_16x16x32_bf16 v[128:131], a[80:83], a[64:67], v[128:131]
s_mov_b32 m0, s49
ds_read_b64_tr_b16 a[112:113], v245 offset:8192
ds_read_b64_tr_b16 a[114:115], v245 offset:10240
v_mfma_f32_16x16x32_bf16 v[132:135], a[84:87], a[64:67], v[132:135]
buffer_load_dwordx4 v7, s[4:7], s44 offen offset:0 sc1 lds
v_mfma_f32_16x16x32_bf16 v[136:139], a[88:91], a[64:67], v[136:139]
ds_read_b128 a[96:99], v244 offset:2048
ds_read_b128 a[100:103], v244 offset:2304
v_mfma_f32_16x16x32_bf16 v[140:143], a[92:95], a[64:67], v[140:143]
ds_read_b64_tr_b16 a[116:117], v246 offset:8192
ds_read_b64_tr_b16 a[118:119], v246 offset:10240
v_mfma_f32_16x16x32_bf16 v[144:147], a[80:83], a[68:71], v[144:147]
buffer_load_dwordx4 v7, s[4:7], s44 offen offset:1024 sc1 lds
v_mfma_f32_16x16x32_bf16 v[148:151], a[84:87], a[68:71], v[148:151]
ds_read_b64_tr_b16 a[120:121], v247 offset:8192
ds_read_b64_tr_b16 a[122:123], v247 offset:10240
v_mfma_f32_16x16x32_bf16 v[152:155], a[88:91], a[68:71], v[152:155]
ds_read_b128 a[104:107], v244 offset:6144
ds_read_b128 a[108:111], v244 offset:6400
v_mfma_f32_16x16x32_bf16 v[156:159], a[92:95], a[68:71], v[156:159]
v_mfma_f32_16x16x32_bf16 v[160:163], a[80:83], a[72:75], v[160:163]
s_cmp_eq_u32 s46, 0
s_cselect_b32 s53, s56, s57
ds_read_b64_tr_b16 a[124:125], v248 offset:8192
ds_read_b64_tr_b16 a[126:127], v248 offset:10240
v_mfma_f32_16x16x32_bf16 v[164:167], a[84:87], a[72:75], v[164:167]
v_mfma_f32_16x16x32_bf16 v[168:171], a[88:91], a[72:75], v[168:171]
s_add_u32 s49, s46, s91
s_add_u32 s46, s46, s53
v_add_u32 v244, s53, v244
v_mfma_f32_16x16x32_bf16 v[172:175], a[92:95], a[72:75], v[172:175]
buffer_load_dwordx4 v7, s[4:7], s44 offen offset:2048 sc1 lds
v_mfma_f32_16x16x32_bf16 v[176:179], a[80:83], a[76:79], v[176:179]
v_add_u32 v245, s53, v245
v_add_u32 v246, s53, v246
v_mfma_f32_16x16x32_bf16 v[180:183], a[84:87], a[76:79], v[180:183]
buffer_load_dwordx4 v7, s[4:7], s44 offen offset:3072 sc1 lds
v_mfma_f32_16x16x32_bf16 v[184:187], a[88:91], a[76:79], v[184:187]
v_add_u32 v247, s53, v247
v_add_u32 v248, s53, v248
v_mfma_f32_16x16x32_bf16 v[188:191], a[92:95], a[76:79], v[188:191]
s_waitcnt vmcnt(18)
s_waitcnt vmcnt(26) lgkmcnt(0)
s_barrier
v_mfma_f32_16x16x32_bf16 v[128:131], a[112:115], a[96:99], v[128:131]
s_add_u32 m0, s49, 16384
s_add_u32 s44, s44, 4096
ds_read_b64_tr_b16 a[80:81], v245 offset:0
ds_read_b64_tr_b16 a[82:83], v245 offset:2048
v_mfma_f32_16x16x32_bf16 v[132:135], a[116:119], a[96:99], v[132:135]
buffer_load_dwordx4 v240, s[8:11], s45 offen offset:0 lds
v_mfma_f32_16x16x32_bf16 v[136:139], a[120:123], a[96:99], v[136:139]
ds_read_b128 a[64:67], v244 offset:0
ds_read_b128 a[68:71], v244 offset:256
v_mfma_f32_16x16x32_bf16 v[140:143], a[124:127], a[96:99], v[140:143]
ds_read_b64_tr_b16 a[84:85], v246 offset:0
ds_read_b64_tr_b16 a[86:87], v246 offset:2048
v_mfma_f32_16x16x32_bf16 v[144:147], a[112:115], a[100:103], v[144:147]
buffer_load_dwordx4 v241, s[8:11], s45 offen offset:1024 lds
v_mfma_f32_16x16x32_bf16 v[148:151], a[116:119], a[100:103], v[148:151]
ds_read_b64_tr_b16 a[88:89], v247 offset:0
ds_read_b64_tr_b16 a[90:91], v247 offset:2048
v_mfma_f32_16x16x32_bf16 v[152:155], a[120:123], a[100:103], v[152:155]
ds_read_b128 a[72:75], v244 offset:4096
ds_read_b128 a[76:79], v244 offset:4352
v_mfma_f32_16x16x32_bf16 v[156:159], a[124:127], a[100:103], v[156:159]
v_mfma_f32_16x16x32_bf16 v[160:163], a[112:115], a[104:107], v[160:163]
ds_read_b64_tr_b16 a[92:93], v248 offset:0
ds_read_b64_tr_b16 a[94:95], v248 offset:2048
v_mfma_f32_16x16x32_bf16 v[164:167], a[116:119], a[104:107], v[164:167]
buffer_load_dwordx4 v242, s[8:11], s45 offen offset:2048 lds
v_mfma_f32_16x16x32_bf16 v[168:171], a[120:123], a[104:107], v[168:171]
s_nop 0
v_or_b32 v0, 5, v11
v_mfma_f32_16x16x32_bf16 v[172:175], a[124:127], a[104:107], v[172:175]
.Lpad22:
s_nop 0
v_mfma_f32_16x16x32_bf16 v[176:179], a[112:115], a[108:111], v[176:179]
buffer_load_dwordx4 v243, s[8:11], s45 offen offset:3072 lds
v_mfma_f32_16x16x32_bf16 v[180:183], a[116:119], a[108:111], v[180:183]
s_add_u32 s45, s45, 131072
v_mfma_f32_16x16x32_bf16 v[184:187], a[120:123], a[108:111], v[184:187]
buffer_store_dword v0, v6, s[60:63], 0 offen sc1
v_mfma_f32_16x16x32_bf16 v[188:191], a[124:127], a[108:111], v[188:191]
s_waitcnt lgkmcnt(0)
v_mfma_f32_16x16x32_bf16 v[128:131], a[80:83], a[64:67], v[128:131]
s_mov_b32 m0, s49
ds_read_b64_tr_b16 a[112:113], v245 offset:8192
ds_read_b64_tr_b16 a[114:115], v245 offset:10240
v_mfma_f32_16x16x32_bf16 v[132:135], a[84:87], a[64:67], v[132:135]
buffer_load_dwordx4 v7, s[4:7], s44 offen offset:0 sc1 lds
v_mfma_f32_16x16x32_bf16 v[136:139], a[88:91], a[64:67], v[136:139]
ds_read_b128 a[96:99], v244 offset:2048
ds_read_b128 a[100:103], v244 offset:2304
v_mfma_f32_16x16x32_bf16 v[140:143], a[92:95], a[64:67], v[140:143]
ds_read_b64_tr_b16 a[116:117], v246 offset:8192
ds_read_b64_tr_b16 a[118:119], v246 offset:10240
v_mfma_f32_16x16x32_bf16 v[144:147], a[80:83], a[68:71], v[144:147]
buffer_load_dwordx4 v7, s[4:7], s44 offen offset:1024 sc1 lds
v_mfma_f32_16x16x32_bf16 v[148:151], a[84:87], a[68:71], v[148:151]
ds_read_b64_tr_b16 a[120:121], v247 offset:8192
ds_read_b64_tr_b16 a[122:123], v247 offset:10240
v_mfma_f32_16x16x32_bf16 v[152:155], a[88:91], a[68:71], v[152:155]
ds_read_b128 a[104:107], v244 offset:6144
ds_read_b128 a[108:111], v244 offset:6400
v_mfma_f32_16x16x32_bf16 v[156:159], a[92:95], a[68:71], v[156:159]
v_mfma_f32_16x16x32_bf16 v[160:163], a[80:83], a[72:75], v[160:163]
s_cmp_eq_u32 s46, 0
s_cselect_b32 s53, s56, s57
ds_read_b64_tr_b16 a[124:125], v248 offset:8192
ds_read_b64_tr_b16 a[126:127], v248 offset:10240
v_mfma_f32_16x16x32_bf16 v[164:167], a[84:87], a[72:75], v[164:167]
v_mfma_f32_16x16x32_bf16 v[168:171], a[88:91], a[72:75], v[168:171]
s_add_u32 s49, s46, s91
s_add_u32 s46, s46, s53
v_add_u32 v244, s53, v244
v_mfma_f32_16x16x32_bf16 v[172:175], a[92:95], a[72:75], v[172:175]
buffer_load_dwordx4 v7, s[4:7], s44 offen offset:2048 sc1 lds
v_mfma_f32_16x16x32_bf16 v[176:179], a[80:83], a[76:79], v[176:179]
v_add_u32 v245, s53, v245
v_add_u32 v246, s53, v246
v_mfma_f32_16x16x32_bf16 v[180:183], a[84:87], a[76:79], v[180:183]
buffer_load_dwordx4 v7, s[4:7], s44 offen offset:3072 sc1 lds
v_mfma_f32_16x16x32_bf16 v[184:187], a[88:91], a[76:79], v[184:187]
v_add_u32 v247, s53, v247
v_add_u32 v248, s53, v248
v_mfma_f32_16x16x32_bf16 v[188:191], a[92:95], a[76:79], v[188:191]
s_waitcnt vmcnt(25) lgkmcnt(0)
s_barrier
v_mfma_f32_16x16x32_bf16 v[128:131], a[112:115], a[96:99], v[128:131]
s_add_u32 m0, s49, 16384
s_add_u32 s44, s44, 4096
ds_read_b64_tr_b16 a[80:81], v245 offset:0
ds_read_b64_tr_b16 a[82:83], v245 offset:2048
v_mfma_f32_16x16x32_bf16 v[132:135], a[116:119], a[96:99], v[132:135]
buffer_load_dwordx4 v240, s[8:11], s45 offen offset:0 lds
v_mfma_f32_16x16x32_bf16 v[136:139], a[120:123], a[96:99], v[136:139]
ds_read_b128 a[64:67], v244 offset:0
ds_read_b128 a[68:71], v244 offset:256
v_mfma_f32_16x16x32_bf16 v[140:143], a[124:127], a[96:99], v[140:143]
ds_read_b64_tr_b16 a[84:85], v246 offset:0
ds_read_b64_tr_b16 a[86:87], v246 offset:2048
v_mfma_f32_16x16x32_bf16 v[144:147], a[112:115], a[100:103], v[144:147]
buffer_load_dwordx4 v241, s[8:11], s45 offen offset:1024 lds
v_mfma_f32_16x16x32_bf16 v[148:151], a[116:119], a[100:103], v[148:151]
ds_read_b64_tr_b16 a[88:89], v247 offset:0
ds_read_b64_tr_b16 a[90:91], v247 offset:2048
v_mfma_f32_16x16x32_bf16 v[152:155], a[120:123], a[100:103], v[152:155]
ds_read_b128 a[72:75], v244 offset:4096
ds_read_b128 a[76:79], v244 offset:4352
v_mfma_f32_16x16x32_bf16 v[156:159], a[124:127], a[100:103], v[156:159]
v_mfma_f32_16x16x32_bf16 v[160:163], a[112:115], a[104:107], v[160:163]
ds_read_b64_tr_b16 a[92:93], v248 offset:0
ds_read_b64_tr_b16 a[94:95], v248 offset:2048
v_mfma_f32_16x16x32_bf16 v[164:167], a[116:119], a[104:107], v[164:167]
buffer_load_dwordx4 v242, s[8:11], s45 offen offset:2048 lds
v_mfma_f32_16x16x32_bf16 v[168:171], a[120:123], a[104:107], v[168:171]
v_mfma_f32_16x16x32_bf16 v[172:175], a[124:127], a[104:107], v[172:175]
v_mfma_f32_16x16x32_bf16 v[176:179], a[112:115], a[108:111], v[176:179]
buffer_load_dwordx4 v243, s[8:11], s45 offen offset:3072 lds
v_mfma_f32_16x16x32_bf16 v[180:183], a[116:119], a[108:111], v[180:183]
s_add_u32 s45, s45, 131072
v_mfma_f32_16x16x32_bf16 v[184:187], a[120:123], a[108:111], v[184:187]
v_mfma_f32_16x16x32_bf16 v[188:191], a[124:127], a[108:111], v[188:191]
s_waitcnt lgkmcnt(0)
v_mfma_f32_16x16x32_bf16 v[128:131], a[80:83], a[64:67], v[128:131]
s_mov_b32 m0, s49
ds_read_b64_tr_b16 a[112:113], v245 offset:8192
ds_read_b64_tr_b16 a[114:115], v245 offset:10240
v_mfma_f32_16x16x32_bf16 v[132:135], a[84:87], a[64:67], v[132:135]
buffer_load_dwordx4 v7, s[4:7], s44 offen offset:0 sc1 lds
v_mfma_f32_16x16x32_bf16 v[136:139], a[88:91], a[64:67], v[136:139]
ds_read_b128 a[96:99], v244 offset:2048
ds_read_b128 a[100:103], v244 offset:2304
v_mfma_f32_16x16x32_bf16 v[140:143], a[92:95], a[64:67], v[140:143]
ds_read_b64_tr_b16 a[116:117], v246 offset:8192
ds_read_b64_tr_b16 a[118:119], v246 offset:10240
v_mfma_f32_16x16x32_bf16 v[144:147], a[80:83], a[68:71], v[144:147]
buffer_load_dwordx4 v7, s[4:7], s44 offen offset:1024 sc1 lds
v_mfma_f32_16x16x32_bf16 v[148:151], a[84:87], a[68:71], v[148:151]
ds_read_b64_tr_b16 a[120:121], v247 offset:8192
ds_read_b64_tr_b16 a[122:123], v247 offset:10240
v_mfma_f32_16x16x32_bf16 v[152:155], a[88:91], a[68:71], v[152:155]
ds_read_b128 a[104:107], v244 offset:6144
ds_read_b128 a[108:111], v244 offset:6400
v_mfma_f32_16x16x32_bf16 v[156:159], a[92:95], a[68:71], v[156:159]
v_mfma_f32_16x16x32_bf16 v[160:163], a[80:83], a[72:75], v[160:163]
s_cmp_eq_u32 s46, 0
s_cselect_b32 s53, s56, s57
ds_read_b64_tr_b16 a[124:125], v248 offset:8192
ds_read_b64_tr_b16 a[126:127], v248 offset:10240
v_mfma_f32_16x16x32_bf16 v[164:167], a[84:87], a[72:75], v[164:167]
v_mfma_f32_16x16x32_bf16 v[168:171], a[88:91], a[72:75], v[168:171]
s_add_u32 s49, s46, s91
s_add_u32 s46, s46, s53
v_add_u32 v244, s53, v244
v_mfma_f32_16x16x32_bf16 v[172:175], a[92:95], a[72:75], v[172:175]
buffer_load_dwordx4 v7, s[4:7], s44 offen offset:2048 sc1 lds
v_mfma_f32_16x16x32_bf16 v[176:179], a[80:83], a[76:79], v[176:179]
v_add_u32 v245, s53, v245
v_add_u32 v246, s53, v246
v_mfma_f32_16x16x32_bf16 v[180:183], a[84:87], a[76:79], v[180:183]
buffer_load_dwordx4 v7, s[4:7], s44 offen offset:3072 sc1 lds
v_mfma_f32_16x16x32_bf16 v[184:187], a[88:91], a[76:79], v[184:187]
v_add_u32 v247, s53, v247
v_add_u32 v248, s53, v248
v_mfma_f32_16x16x32_bf16 v[188:191], a[92:95], a[76:79], v[188:191]
s_waitcnt vmcnt(25) lgkmcnt(0)
s_barrier
v_mfma_f32_16x16x32_bf16 v[128:131], a[112:115], a[96:99], v[128:131]
s_add_u32 m0, s49, 16384
s_add_u32 s44, s44, 4096
ds_read_b64_tr_b16 a[80:81], v245 offset:0
ds_read_b64_tr_b16 a[82:83], v245 offset:2048
v_mfma_f32_16x16x32_bf16 v[132:135], a[116:119], a[96:99], v[132:135]
buffer_load_dwordx4 v240, s[8:11], s45 offen offset:0 lds
v_mfma_f32_16x16x32_bf16 v[136:139], a[120:123], a[96:99], v[136:139]
ds_read_b128 a[64:67], v244 offset:0
ds_read_b128 a[68:71], v244 offset:256
v_mfma_f32_16x16x32_bf16 v[140:143], a[124:127], a[96:99], v[140:143]
ds_read_b64_tr_b16 a[84:85], v246 offset:0
ds_read_b64_tr_b16 a[86:87], v246 offset:2048
v_mfma_f32_16x16x32_bf16 v[144:147], a[112:115], a[100:103], v[144:147]
buffer_load_dwordx4 v241, s[8:11], s45 offen offset:1024 lds
v_mfma_f32_16x16x32_bf16 v[148:151], a[116:119], a[100:103], v[148:151]
ds_read_b64_tr_b16 a[88:89], v247 offset:0
ds_read_b64_tr_b16 a[90:91], v247 offset:2048
v_mfma_f32_16x16x32_bf16 v[152:155], a[120:123], a[100:103], v[152:155]
ds_read_b128 a[72:75], v244 offset:4096
ds_read_b128 a[76:79], v244 offset:4352
v_mfma_f32_16x16x32_bf16 v[156:159], a[124:127], a[100:103], v[156:159]
v_mfma_f32_16x16x32_bf16 v[160:163], a[112:115], a[104:107], v[160:163]
ds_read_b64_tr_b16 a[92:93], v248 offset:0
ds_read_b64_tr_b16 a[94:95], v248 offset:2048
v_mfma_f32_16x16x32_bf16 v[164:167], a[116:119], a[104:107], v[164:167]
buffer_load_dwordx4 v242, s[8:11], s45 offen offset:2048 lds
v_mfma_f32_16x16x32_bf16 v[168:171], a[120:123], a[104:107], v[168:171]
v_mfma_f32_16x16x32_bf16 v[172:175], a[124:127], a[104:107], v[172:175]
v_mfma_f32_16x16x32_bf16 v[176:179], a[112:115], a[108:111], v[176:179]
buffer_load_dwordx4 v243, s[8:11], s45 offen offset:3072 lds
v_mfma_f32_16x16x32_bf16 v[180:183], a[116:119], a[108:111], v[180:183]
s_add_u32 s45, s45, 131072
v_mfma_f32_16x16x32_bf16 v[184:187], a[120:123], a[108:111], v[184:187]
v_mfma_f32_16x16x32_bf16 v[188:191], a[124:127], a[108:111], v[188:191]
s_waitcnt lgkmcnt(0)
v_mfma_f32_16x16x32_bf16 v[128:131], a[80:83], a[64:67], v[128:131]
s_mov_b32 m0, s49
ds_read_b64_tr_b16 a[112:113], v245 offset:8192
ds_read_b64_tr_b16 a[114:115], v245 offset:10240
v_mfma_f32_16x16x32_bf16 v[132:135], a[84:87], a[64:67], v[132:135]
buffer_load_dwordx4 v7, s[4:7], s44 offen offset:0 sc1 lds
v_mfma_f32_16x16x32_bf16 v[136:139], a[88:91], a[64:67], v[136:139]
ds_read_b128 a[96:99], v244 offset:2048
ds_read_b128 a[100:103], v244 offset:2304
v_mfma_f32_16x16x32_bf16 v[140:143], a[92:95], a[64:67], v[140:143]
ds_read_b64_tr_b16 a[116:117], v246 offset:8192
ds_read_b64_tr_b16 a[118:119], v246 offset:10240
v_mfma_f32_16x16x32_bf16 v[144:147], a[80:83], a[68:71], v[144:147]
buffer_load_dwordx4 v7, s[4:7], s44 offen offset:1024 sc1 lds
v_mfma_f32_16x16x32_bf16 v[148:151], a[84:87], a[68:71], v[148:151]
ds_read_b64_tr_b16 a[120:121], v247 offset:8192
ds_read_b64_tr_b16 a[122:123], v247 offset:10240
v_mfma_f32_16x16x32_bf16 v[152:155], a[88:91], a[68:71], v[152:155]
ds_read_b128 a[104:107], v244 offset:6144
ds_read_b128 a[108:111], v244 offset:6400
v_mfma_f32_16x16x32_bf16 v[156:159], a[92:95], a[68:71], v[156:159]
v_mfma_f32_16x16x32_bf16 v[160:163], a[80:83], a[72:75], v[160:163]
s_cmp_eq_u32 s46, 0
s_cselect_b32 s53, s56, s57
ds_read_b64_tr_b16 a[124:125], v248 offset:8192
ds_read_b64_tr_b16 a[126:127], v248 offset:10240
v_mfma_f32_16x16x32_bf16 v[164:167], a[84:87], a[72:75], v[164:167]
v_mfma_f32_16x16x32_bf16 v[168:171], a[88:91], a[72:75], v[168:171]
s_add_u32 s49, s46, s91
s_add_u32 s46, s46, s53
v_add_u32 v244, s53, v244
v_mfma_f32_16x16x32_bf16 v[172:175], a[92:95], a[72:75], v[172:175]
buffer_load_dwordx4 v7, s[4:7], s44 offen offset:2048 sc1 lds
v_mfma_f32_16x16x32_bf16 v[176:179], a[80:83], a[76:79], v[176:179]
v_add_u32 v245, s53, v245
v_add_u32 v246, s53, v246
v_mfma_f32_16x16x32_bf16 v[180:183], a[84:87], a[76:79], v[180:183]
buffer_load_dwordx4 v7, s[4:7], s44 offen offset:3072 sc1 lds
v_mfma_f32_16x16x32_bf16 v[184:187], a[88:91], a[76:79], v[184:187]
v_add_u32 v247, s53, v247
v_add_u32 v248, s53, v248
v_mfma_f32_16x16x32_bf16 v[188:191], a[92:95], a[76:79], v[188:191]
s_waitcnt vmcnt(25) lgkmcnt(0)
s_barrier
v_mfma_f32_16x16x32_bf16 v[128:131], a[112:115], a[96:99], v[128:131]
s_add_u32 m0, s49, 16384
s_add_u32 s44, s44, 4096
ds_read_b64_tr_b16 a[80:81], v245 offset:0
ds_read_b64_tr_b16 a[82:83], v245 offset:2048
v_mfma_f32_16x16x32_bf16 v[132:135], a[116:119], a[96:99], v[132:135]
buffer_load_dwordx4 v240, s[8:11], s45 offen offset:0 lds
v_mfma_f32_16x16x32_bf16 v[136:139], a[120:123], a[96:99], v[136:139]
ds_read_b128 a[64:67], v244 offset:0
ds_read_b128 a[68:71], v244 offset:256
v_mfma_f32_16x16x32_bf16 v[140:143], a[124:127], a[96:99], v[140:143]
ds_read_b64_tr_b16 a[84:85], v246 offset:0
ds_read_b64_tr_b16 a[86:87], v246 offset:2048
v_mfma_f32_16x16x32_bf16 v[144:147], a[112:115], a[100:103], v[144:147]
buffer_load_dwordx4 v241, s[8:11], s45 offen offset:1024 lds
v_mfma_f32_16x16x32_bf16 v[148:151], a[116:119], a[100:103], v[148:151]
ds_read_b64_tr_b16 a[88:89], v247 offset:0
ds_read_b64_tr_b16 a[90:91], v247 offset:2048
v_mfma_f32_16x16x32_bf16 v[152:155], a[120:123], a[100:103], v[152:155]
ds_read_b128 a[72:75], v244 offset:4096
ds_read_b128 a[76:79], v244 offset:4352
v_mfma_f32_16x16x32_bf16 v[156:159], a[124:127], a[100:103], v[156:159]
v_mfma_f32_16x16x32_bf16 v[160:163], a[112:115], a[104:107], v[160:163]
ds_read_b64_tr_b16 a[92:93], v248 offset:0
ds_read_b64_tr_b16 a[94:95], v248 offset:2048
v_mfma_f32_16x16x32_bf16 v[164:167], a[116:119], a[104:107], v[164:167]
buffer_load_dwordx4 v242, s[8:11], s45 offen offset:2048 lds
v_mfma_f32_16x16x32_bf16 v[168:171], a[120:123], a[104:107], v[168:171]
v_mfma_f32_16x16x32_bf16 v[172:175], a[124:127], a[104:107], v[172:175]
v_mfma_f32_16x16x32_bf16 v[176:179], a[112:115], a[108:111], v[176:179]
buffer_load_dwordx4 v243, s[8:11], s45 offen offset:3072 lds
v_mfma_f32_16x16x32_bf16 v[180:183], a[116:119], a[108:111], v[180:183]
s_add_u32 s45, s45, 131072
v_mfma_f32_16x16x32_bf16 v[184:187], a[120:123], a[108:111], v[184:187]
v_mfma_f32_16x16x32_bf16 v[188:191], a[124:127], a[108:111], v[188:191]
s_waitcnt lgkmcnt(0)
v_mfma_f32_16x16x32_bf16 v[128:131], a[80:83], a[64:67], v[128:131]
s_mov_b32 m0, s49
ds_read_b64_tr_b16 a[112:113], v245 offset:8192
ds_read_b64_tr_b16 a[114:115], v245 offset:10240
v_mfma_f32_16x16x32_bf16 v[132:135], a[84:87], a[64:67], v[132:135]
buffer_load_dwordx4 v7, s[4:7], s44 offen offset:0 sc1 lds
v_mfma_f32_16x16x32_bf16 v[136:139], a[88:91], a[64:67], v[136:139]
ds_read_b128 a[96:99], v244 offset:2048
ds_read_b128 a[100:103], v244 offset:2304
v_mfma_f32_16x16x32_bf16 v[140:143], a[92:95], a[64:67], v[140:143]
ds_read_b64_tr_b16 a[116:117], v246 offset:8192
ds_read_b64_tr_b16 a[118:119], v246 offset:10240
v_mfma_f32_16x16x32_bf16 v[144:147], a[80:83], a[68:71], v[144:147]
buffer_load_dwordx4 v7, s[4:7], s44 offen offset:1024 sc1 lds
v_mfma_f32_16x16x32_bf16 v[148:151], a[84:87], a[68:71], v[148:151]
ds_read_b64_tr_b16 a[120:121], v247 offset:8192
ds_read_b64_tr_b16 a[122:123], v247 offset:10240
v_mfma_f32_16x16x32_bf16 v[152:155], a[88:91], a[68:71], v[152:155]
ds_read_b128 a[104:107], v244 offset:6144
ds_read_b128 a[108:111], v244 offset:6400
v_mfma_f32_16x16x32_bf16 v[156:159], a[92:95], a[68:71], v[156:159]
v_mfma_f32_16x16x32_bf16 v[160:163], a[80:83], a[72:75], v[160:163]
s_cmp_eq_u32 s46, 0
s_cselect_b32 s53, s56, s57
ds_read_b64_tr_b16 a[124:125], v248 offset:8192
ds_read_b64_tr_b16 a[126:127], v248 offset:10240
v_mfma_f32_16x16x32_bf16 v[164:167], a[84:87], a[72:75], v[164:167]
v_mfma_f32_16x16x32_bf16 v[168:171], a[88:91], a[72:75], v[168:171]
s_add_u32 s49, s46, s91
s_add_u32 s46, s46, s53
v_add_u32 v244, s53, v244
v_mfma_f32_16x16x32_bf16 v[172:175], a[92:95], a[72:75], v[172:175]
buffer_load_dwordx4 v7, s[4:7], s44 offen offset:2048 sc1 lds
v_mfma_f32_16x16x32_bf16 v[176:179], a[80:83], a[76:79], v[176:179]
v_add_u32 v245, s53, v245
v_add_u32 v246, s53, v246
v_mfma_f32_16x16x32_bf16 v[180:183], a[84:87], a[76:79], v[180:183]
buffer_load_dwordx4 v7, s[4:7], s44 offen offset:3072 sc1 lds
v_mfma_f32_16x16x32_bf16 v[184:187], a[88:91], a[76:79], v[184:187]
v_add_u32 v247, s53, v247
v_add_u32 v248, s53, v248
v_mfma_f32_16x16x32_bf16 v[188:191], a[92:95], a[76:79], v[188:191]
s_waitcnt vmcnt(24) lgkmcnt(0)
s_barrier
v_mfma_f32_16x16x32_bf16 v[128:131], a[112:115], a[96:99], v[128:131]
s_add_u32 m0, s49, 16384
s_add_u32 s44, s44, 4096
ds_read_b64_tr_b16 a[80:81], v245 offset:0
ds_read_b64_tr_b16 a[82:83], v245 offset:2048
v_mfma_f32_16x16x32_bf16 v[132:135], a[116:119], a[96:99], v[132:135]
buffer_load_dwordx4 v240, s[8:11], s45 offen offset:0 lds
v_mfma_f32_16x16x32_bf16 v[136:139], a[120:123], a[96:99], v[136:139]
ds_read_b128 a[64:67], v244 offset:0
ds_read_b128 a[68:71], v244 offset:256
v_mfma_f32_16x16x32_bf16 v[140:143], a[124:127], a[96:99], v[140:143]
ds_read_b64_tr_b16 a[84:85], v246 offset:0
ds_read_b64_tr_b16 a[86:87], v246 offset:2048
v_mfma_f32_16x16x32_bf16 v[144:147], a[112:115], a[100:103], v[144:147]
buffer_load_dwordx4 v241, s[8:11], s45 offen offset:1024 lds
v_mfma_f32_16x16x32_bf16 v[148:151], a[116:119], a[100:103], v[148:151]
ds_read_b64_tr_b16 a[88:89], v247 offset:0
ds_read_b64_tr_b16 a[90:91], v247 offset:2048
v_mfma_f32_16x16x32_bf16 v[152:155], a[120:123], a[100:103], v[152:155]
ds_read_b128 a[72:75], v244 offset:4096
ds_read_b128 a[76:79], v244 offset:4352
v_mfma_f32_16x16x32_bf16 v[156:159], a[124:127], a[100:103], v[156:159]
v_mfma_f32_16x16x32_bf16 v[160:163], a[112:115], a[104:107], v[160:163]
ds_read_b64_tr_b16 a[92:93], v248 offset:0
ds_read_b64_tr_b16 a[94:95], v248 offset:2048
v_mfma_f32_16x16x32_bf16 v[164:167], a[116:119], a[104:107], v[164:167]
buffer_load_dwordx4 v242, s[8:11], s45 offen offset:2048 lds
v_mfma_f32_16x16x32_bf16 v[168:171], a[120:123], a[104:107], v[168:171]
v_mfma_f32_16x16x32_bf16 v[172:175], a[124:127], a[104:107], v[172:175]
v_mfma_f32_16x16x32_bf16 v[176:179], a[112:115], a[108:111], v[176:179]
buffer_load_dwordx4 v243, s[8:11], s45 offen offset:3072 lds
v_mfma_f32_16x16x32_bf16 v[180:183], a[116:119], a[108:111], v[180:183]
s_add_u32 s45, s45, 131072
v_mfma_f32_16x16x32_bf16 v[184:187], a[120:123], a[108:111], v[184:187]
v_mfma_f32_16x16x32_bf16 v[188:191], a[124:127], a[108:111], v[188:191]
s_waitcnt lgkmcnt(0)
v_mfma_f32_16x16x32_bf16 v[128:131], a[80:83], a[64:67], v[128:131]
s_mov_b32 m0, s49
ds_read_b64_tr_b16 a[112:113], v245 offset:8192
ds_read_b64_tr_b16 a[114:115], v245 offset:10240
v_mfma_f32_16x16x32_bf16 v[132:135], a[84:87], a[64:67], v[132:135]
buffer_load_dwordx4 v7, s[4:7], s44 offen offset:0 sc1 lds
v_mfma_f32_16x16x32_bf16 v[136:139], a[88:91], a[64:67], v[136:139]
ds_read_b128 a[96:99], v244 offset:2048
ds_read_b128 a[100:103], v244 offset:2304
v_mfma_f32_16x16x32_bf16 v[140:143], a[92:95], a[64:67], v[140:143]
ds_read_b64_tr_b16 a[116:117], v246 offset:8192
ds_read_b64_tr_b16 a[118:119], v246 offset:10240
v_mfma_f32_16x16x32_bf16 v[144:147], a[80:83], a[68:71], v[144:147]
buffer_load_dwordx4 v7, s[4:7], s44 offen offset:1024 sc1 lds
v_mfma_f32_16x16x32_bf16 v[148:151], a[84:87], a[68:71], v[148:151]
ds_read_b64_tr_b16 a[120:121], v247 offset:8192
ds_read_b64_tr_b16 a[122:123], v247 offset:10240
v_mfma_f32_16x16x32_bf16 v[152:155], a[88:91], a[68:71], v[152:155]
ds_read_b128 a[104:107], v244 offset:6144
ds_read_b128 a[108:111], v244 offset:6400
v_mfma_f32_16x16x32_bf16 v[156:159], a[92:95], a[68:71], v[156:159]
v_mfma_f32_16x16x32_bf16 v[160:163], a[80:83], a[72:75], v[160:163]
s_cmp_eq_u32 s46, 0
s_cselect_b32 s53, s56, s57
ds_read_b64_tr_b16 a[124:125], v248 offset:8192
ds_read_b64_tr_b16 a[126:127], v248 offset:10240
v_mfma_f32_16x16x32_bf16 v[164:167], a[84:87], a[72:75], v[164:167]
v_mfma_f32_16x16x32_bf16 v[168:171], a[88:91], a[72:75], v[168:171]
s_add_u32 s49, s46, s91
s_add_u32 s46, s46, s53
v_add_u32 v244, s53, v244
v_mfma_f32_16x16x32_bf16 v[172:175], a[92:95], a[72:75], v[172:175]
buffer_load_dwordx4 v7, s[4:7], s44 offen offset:2048 sc1 lds
v_mfma_f32_16x16x32_bf16 v[176:179], a[80:83], a[76:79], v[176:179]
v_add_u32 v245, s53, v245
v_add_u32 v246, s53, v246
v_mfma_f32_16x16x32_bf16 v[180:183], a[84:87], a[76:79], v[180:183]
buffer_load_dwordx4 v7, s[4:7], s44 offen offset:3072 sc1 lds
v_mfma_f32_16x16x32_bf16 v[184:187], a[88:91], a[76:79], v[184:187]
v_add_u32 v247, s53, v247
v_add_u32 v248, s53, v248
v_mfma_f32_16x16x32_bf16 v[188:191], a[92:95], a[76:79], v[188:191]
s_waitcnt vmcnt(24) lgkmcnt(0)
s_barrier
v_mfma_f32_16x16x32_bf16 v[128:131], a[112:115], a[96:99], v[128:131]
ds_read_b64_tr_b16 a[80:81], v245 offset:0
ds_read_b64_tr_b16 a[82:83], v245 offset:2048
v_mfma_f32_16x16x32_bf16 v[132:135], a[116:119], a[96:99], v[132:135]
v_mfma_f32_16x16x32_bf16 v[136:139], a[120:123], a[96:99], v[136:139]
ds_read_b128 a[64:67], v244 offset:0
ds_read_b128 a[68:71], v244 offset:256
v_mfma_f32_16x16x32_bf16 v[140:143], a[124:127], a[96:99], v[140:143]
ds_read_b64_tr_b16 a[84:85], v246 offset:0
ds_read_b64_tr_b16 a[86:87], v246 offset:2048
v_mfma_f32_16x16x32_bf16 v[144:147], a[112:115], a[100:103], v[144:147]
v_mfma_f32_16x16x32_bf16 v[148:151], a[116:119], a[100:103], v[148:151]
ds_read_b64_tr_b16 a[88:89], v247 offset:0
ds_read_b64_tr_b16 a[90:91], v247 offset:2048
v_mfma_f32_16x16x32_bf16 v[152:155], a[120:123], a[100:103], v[152:155]
ds_read_b128 a[72:75], v244 offset:4096
ds_read_b128 a[76:79], v244 offset:4352
v_mfma_f32_16x16x32_bf16 v[156:159], a[124:127], a[100:103], v[156:159]
v_mfma_f32_16x16x32_bf16 v[160:163], a[112:115], a[104:107], v[160:163]
ds_read_b64_tr_b16 a[92:93], v248 offset:0
ds_read_b64_tr_b16 a[94:95], v248 offset:2048
v_mfma_f32_16x16x32_bf16 v[164:167], a[116:119], a[104:107], v[164:167]
v_mfma_f32_16x16x32_bf16 v[168:171], a[120:123], a[104:107], v[168:171]
v_mfma_f32_16x16x32_bf16 v[172:175], a[124:127], a[104:107], v[172:175]
v_mfma_f32_16x16x32_bf16 v[176:179], a[112:115], a[108:111], v[176:179]
v_mfma_f32_16x16x32_bf16 v[180:183], a[116:119], a[108:111], v[180:183]
v_mfma_f32_16x16x32_bf16 v[184:187], a[120:123], a[108:111], v[184:187]
v_mfma_f32_16x16x32_bf16 v[188:191], a[124:127], a[108:111], v[188:191]
s_waitcnt lgkmcnt(0)
v_mfma_f32_16x16x32_bf16 v[128:131], a[80:83], a[64:67], v[128:131]
ds_read_b64_tr_b16 a[112:113], v245 offset:8192
ds_read_b64_tr_b16 a[114:115], v245 offset:10240
v_mfma_f32_16x16x32_bf16 v[132:135], a[84:87], a[64:67], v[132:135]
v_mfma_f32_16x16x32_bf16 v[136:139], a[88:91], a[64:67], v[136:139]
ds_read_b128 a[96:99], v244 offset:2048
ds_read_b128 a[100:103], v244 offset:2304
v_mfma_f32_16x16x32_bf16 v[140:143], a[92:95], a[64:67], v[140:143]
ds_read_b64_tr_b16 a[116:117], v246 offset:8192
ds_read_b64_tr_b16 a[118:119], v246 offset:10240
v_mfma_f32_16x16x32_bf16 v[144:147], a[80:83], a[68:71], v[144:147]
v_mfma_f32_16x16x32_bf16 v[148:151], a[84:87], a[68:71], v[148:151]
ds_read_b64_tr_b16 a[120:121], v247 offset:8192
ds_read_b64_tr_b16 a[122:123], v247 offset:10240
v_mfma_f32_16x16x32_bf16 v[152:155], a[88:91], a[68:71], v[152:155]
ds_read_b128 a[104:107], v244 offset:6144
ds_read_b128 a[108:111], v244 offset:6400
v_mfma_f32_16x16x32_bf16 v[156:159], a[92:95], a[68:71], v[156:159]
v_mfma_f32_16x16x32_bf16 v[160:163], a[80:83], a[72:75], v[160:163]
s_cmp_eq_u32 s46, 0
s_cselect_b32 s53, s56, s57
ds_read_b64_tr_b16 a[124:125], v248 offset:8192
ds_read_b64_tr_b16 a[126:127], v248 offset:10240
v_mfma_f32_16x16x32_bf16 v[164:167], a[84:87], a[72:75], v[164:167]
v_mfma_f32_16x16x32_bf16 v[168:171], a[88:91], a[72:75], v[168:171]
s_add_u32 s49, s46, s91
s_add_u32 s46, s46, s53
v_add_u32 v244, s53, v244
v_mfma_f32_16x16x32_bf16 v[172:175], a[92:95], a[72:75], v[172:175]
v_mfma_f32_16x16x32_bf16 v[176:179], a[80:83], a[76:79], v[176:179]
v_add_u32 v245, s53, v245
v_add_u32 v246, s53, v246
v_mfma_f32_16x16x32_bf16 v[180:183], a[84:87], a[76:79], v[180:183]
v_mfma_f32_16x16x32_bf16 v[184:187], a[88:91], a[76:79], v[184:187]
v_add_u32 v247, s53, v247
v_add_u32 v248, s53, v248
v_mfma_f32_16x16x32_bf16 v[188:191], a[92:95], a[76:79], v[188:191]
s_waitcnt vmcnt(16) lgkmcnt(0)
s_barrier
v_mfma_f32_16x16x32_bf16 v[128:131], a[112:115], a[96:99], v[128:131]
ds_read_b64_tr_b16 a[80:81], v245 offset:0
ds_read_b64_tr_b16 a[82:83], v245 offset:2048
v_mfma_f32_16x16x32_bf16 v[132:135], a[116:119], a[96:99], v[132:135]
v_mfma_f32_16x16x32_bf16 v[136:139], a[120:123], a[96:99], v[136:139]
ds_read_b128 a[64:67], v244 offset:0
ds_read_b128 a[68:71], v244 offset:256
v_mfma_f32_16x16x32_bf16 v[140:143], a[124:127], a[96:99], v[140:143]
ds_read_b64_tr_b16 a[84:85], v246 offset:0
ds_read_b64_tr_b16 a[86:87], v246 offset:2048
v_mfma_f32_16x16x32_bf16 v[144:147], a[112:115], a[100:103], v[144:147]
v_mfma_f32_16x16x32_bf16 v[148:151], a[116:119], a[100:103], v[148:151]
ds_read_b64_tr_b16 a[88:89], v247 offset:0
ds_read_b64_tr_b16 a[90:91], v247 offset:2048
v_mfma_f32_16x16x32_bf16 v[152:155], a[120:123], a[100:103], v[152:155]
ds_read_b128 a[72:75], v244 offset:4096
ds_read_b128 a[76:79], v244 offset:4352
v_mfma_f32_16x16x32_bf16 v[156:159], a[124:127], a[100:103], v[156:159]
v_mfma_f32_16x16x32_bf16 v[160:163], a[112:115], a[104:107], v[160:163]
ds_read_b64_tr_b16 a[92:93], v248 offset:0
ds_read_b64_tr_b16 a[94:95], v248 offset:2048
v_mfma_f32_16x16x32_bf16 v[164:167], a[116:119], a[104:107], v[164:167]
v_mfma_f32_16x16x32_bf16 v[168:171], a[120:123], a[104:107], v[168:171]
v_mfma_f32_16x16x32_bf16 v[172:175], a[124:127], a[104:107], v[172:175]
v_mfma_f32_16x16x32_bf16 v[176:179], a[112:115], a[108:111], v[176:179]
v_mfma_f32_16x16x32_bf16 v[180:183], a[116:119], a[108:111], v[180:183]
v_mfma_f32_16x16x32_bf16 v[184:187], a[120:123], a[108:111], v[184:187]
v_mfma_f32_16x16x32_bf16 v[188:191], a[124:127], a[108:111], v[188:191]
s_waitcnt lgkmcnt(0)
v_mfma_f32_16x16x32_bf16 v[128:131], a[80:83], a[64:67], v[128:131]
ds_read_b64_tr_b16 a[112:113], v245 offset:8192
ds_read_b64_tr_b16 a[114:115], v245 offset:10240
v_mfma_f32_16x16x32_bf16 v[132:135], a[84:87], a[64:67], v[132:135]
v_mfma_f32_16x16x32_bf16 v[136:139], a[88:91], a[64:67], v[136:139]
ds_read_b128 a[96:99], v244 offset:2048
ds_read_b128 a[100:103], v244 offset:2304
v_mfma_f32_16x16x32_bf16 v[140:143], a[92:95], a[64:67], v[140:143]
ds_read_b64_tr_b16 a[116:117], v246 offset:8192
ds_read_b64_tr_b16 a[118:119], v246 offset:10240
v_mfma_f32_16x16x32_bf16 v[144:147], a[80:83], a[68:71], v[144:147]
v_mfma_f32_16x16x32_bf16 v[148:151], a[84:87], a[68:71], v[148:151]
ds_read_b64_tr_b16 a[120:121], v247 offset:8192
ds_read_b64_tr_b16 a[122:123], v247 offset:10240
v_mfma_f32_16x16x32_bf16 v[152:155], a[88:91], a[68:71], v[152:155]
ds_read_b128 a[104:107], v244 offset:6144
ds_read_b128 a[108:111], v244 offset:6400
v_mfma_f32_16x16x32_bf16 v[156:159], a[92:95], a[68:71], v[156:159]
v_mfma_f32_16x16x32_bf16 v[160:163], a[80:83], a[72:75], v[160:163]
s_cmp_eq_u32 s46, 0
s_cselect_b32 s53, s56, s57
ds_read_b64_tr_b16 a[124:125], v248 offset:8192
ds_read_b64_tr_b16 a[126:127], v248 offset:10240
v_mfma_f32_16x16x32_bf16 v[164:167], a[84:87], a[72:75], v[164:167]
v_mfma_f32_16x16x32_bf16 v[168:171], a[88:91], a[72:75], v[168:171]
s_add_u32 s49, s46, s91
s_add_u32 s46, s46, s53
v_add_u32 v244, s53, v244
v_mfma_f32_16x16x32_bf16 v[172:175], a[92:95], a[72:75], v[172:175]
v_mfma_f32_16x16x32_bf16 v[176:179], a[80:83], a[76:79], v[176:179]
v_add_u32 v245, s53, v245
v_add_u32 v246, s53, v246
v_mfma_f32_16x16x32_bf16 v[180:183], a[84:87], a[76:79], v[180:183]
v_mfma_f32_16x16x32_bf16 v[184:187], a[88:91], a[76:79], v[184:187]
v_add_u32 v247, s53, v247
v_add_u32 v248, s53, v248
v_mfma_f32_16x16x32_bf16 v[188:191], a[92:95], a[76:79], v[188:191]
s_waitcnt vmcnt(8) lgkmcnt(0)
s_barrier
v_mfma_f32_16x16x32_bf16 v[128:131], a[112:115], a[96:99], v[128:131]
ds_read_b64_tr_b16 a[80:81], v245 offset:0
ds_read_b64_tr_b16 a[82:83], v245 offset:2048
v_mfma_f32_16x16x32_bf16 v[132:135], a[116:119], a[96:99], v[132:135]
v_mfma_f32_16x16x32_bf16 v[136:139], a[120:123], a[96:99], v[136:139]
ds_read_b128 a[64:67], v244 offset:0
ds_read_b128 a[68:71], v244 offset:256
v_mfma_f32_16x16x32_bf16 v[140:143], a[124:127], a[96:99], v[140:143]
ds_read_b64_tr_b16 a[84:85], v246 offset:0
ds_read_b64_tr_b16 a[86:87], v246 offset:2048
v_mfma_f32_16x16x32_bf16 v[144:147], a[112:115], a[100:103], v[144:147]
v_mfma_f32_16x16x32_bf16 v[148:151], a[116:119], a[100:103], v[148:151]
ds_read_b64_tr_b16 a[88:89], v247 offset:0
ds_read_b64_tr_b16 a[90:91], v247 offset:2048
v_mfma_f32_16x16x32_bf16 v[152:155], a[120:123], a[100:103], v[152:155]
ds_read_b128 a[72:75], v244 offset:4096
ds_read_b128 a[76:79], v244 offset:4352
v_mfma_f32_16x16x32_bf16 v[156:159], a[124:127], a[100:103], v[156:159]
v_mfma_f32_16x16x32_bf16 v[160:163], a[112:115], a[104:107], v[160:163]
ds_read_b64_tr_b16 a[92:93], v248 offset:0
ds_read_b64_tr_b16 a[94:95], v248 offset:2048
v_mfma_f32_16x16x32_bf16 v[164:167], a[116:119], a[104:107], v[164:167]
v_mfma_f32_16x16x32_bf16 v[168:171], a[120:123], a[104:107], v[168:171]
v_mfma_f32_16x16x32_bf16 v[172:175], a[124:127], a[104:107], v[172:175]
v_mfma_f32_16x16x32_bf16 v[176:179], a[112:115], a[108:111], v[176:179]
v_mfma_f32_16x16x32_bf16 v[180:183], a[116:119], a[108:111], v[180:183]
v_mfma_f32_16x16x32_bf16 v[184:187], a[120:123], a[108:111], v[184:187]
v_mfma_f32_16x16x32_bf16 v[188:191], a[124:127], a[108:111], v[188:191]
s_waitcnt lgkmcnt(0)
v_mfma_f32_16x16x32_bf16 v[128:131], a[80:83], a[64:67], v[128:131]
ds_read_b64_tr_b16 a[112:113], v245 offset:8192
ds_read_b64_tr_b16 a[114:115], v245 offset:10240
v_mfma_f32_16x16x32_bf16 v[132:135], a[84:87], a[64:67], v[132:135]
v_mfma_f32_16x16x32_bf16 v[136:139], a[88:91], a[64:67], v[136:139]
ds_read_b128 a[96:99], v244 offset:2048
ds_read_b128 a[100:103], v244 offset:2304
v_mfma_f32_16x16x32_bf16 v[140:143], a[92:95], a[64:67], v[140:143]
ds_read_b64_tr_b16 a[116:117], v246 offset:8192
ds_read_b64_tr_b16 a[118:119], v246 offset:10240
v_mfma_f32_16x16x32_bf16 v[144:147], a[80:83], a[68:71], v[144:147]
v_mfma_f32_16x16x32_bf16 v[148:151], a[84:87], a[68:71], v[148:151]
ds_read_b64_tr_b16 a[120:121], v247 offset:8192
ds_read_b64_tr_b16 a[122:123], v247 offset:10240
v_mfma_f32_16x16x32_bf16 v[152:155], a[88:91], a[68:71], v[152:155]
ds_read_b128 a[104:107], v244 offset:6144
ds_read_b128 a[108:111], v244 offset:6400
.Lpad23:
s_nop 0
v_mfma_f32_16x16x32_bf16 v[156:159], a[92:95], a[68:71], v[156:159]
v_mfma_f32_16x16x32_bf16 v[160:163], a[80:83], a[72:75], v[160:163]
s_cmp_eq_u32 s46, 0
s_cselect_b32 s53, s56, s57
ds_read_b64_tr_b16 a[124:125], v248 offset:8192
ds_read_b64_tr_b16 a[126:127], v248 offset:10240
v_mfma_f32_16x16x32_bf16 v[164:167], a[84:87], a[72:75], v[164:167]
v_mfma_f32_16x16x32_bf16 v[168:171], a[88:91], a[72:75], v[168:171]
s_add_u32 s49, s46, s91
s_add_u32 s46, s46, s53
v_add_u32 v244, s53, v244
v_mfma_f32_16x16x32_bf16 v[172:175], a[92:95], a[72:75], v[172:175]
v_mfma_f32_16x16x32_bf16 v[176:179], a[80:83], a[76:79], v[176:179]
v_add_u32 v245, s53, v245
v_add_u32 v246, s53, v246
v_mfma_f32_16x16x32_bf16 v[180:183], a[84:87], a[76:79], v[180:183]
v_mfma_f32_16x16x32_bf16 v[184:187], a[88:91], a[76:79], v[184:187]
v_add_u32 v247, s53, v247
v_add_u32 v248, s53, v248
v_mfma_f32_16x16x32_bf16 v[188:191], a[92:95], a[76:79], v[188:191]
s_waitcnt vmcnt(0) lgkmcnt(0)
s_barrier
v_mfma_f32_16x16x32_bf16 v[128:131], a[112:115], a[96:99], v[128:131]
ds_read_b64_tr_b16 a[80:81], v245 offset:0
ds_read_b64_tr_b16 a[82:83], v245 offset:2048
v_mfma_f32_16x16x32_bf16 v[132:135], a[116:119], a[96:99], v[132:135]
ds_read_b128 a[64:67], v244 offset:0
ds_read_b128 a[68:71], v244 offset:256
v_mfma_f32_16x16x32_bf16 v[136:139], a[120:123], a[96:99], v[136:139]
ds_read_b64_tr_b16 a[84:85], v246 offset:0
ds_read_b64_tr_b16 a[86:87], v246 offset:2048
v_mfma_f32_16x16x32_bf16 v[140:143], a[124:127], a[96:99], v[140:143]
ds_read_b64_tr_b16 a[88:89], v247 offset:0
ds_read_b64_tr_b16 a[90:91], v247 offset:2048
v_mfma_f32_16x16x32_bf16 v[144:147], a[112:115], a[100:103], v[144:147]
ds_read_b64_tr_b16 a[92:93], v248 offset:0
ds_read_b64_tr_b16 a[94:95], v248 offset:2048
v_mfma_f32_16x16x32_bf16 v[148:151], a[116:119], a[100:103], v[148:151]
ds_read_b64_tr_b16 a[144:145], v245 offset:8192
ds_read_b64_tr_b16 a[146:147], v245 offset:10240
v_mfma_f32_16x16x32_bf16 v[152:155], a[120:123], a[100:103], v[152:155]
ds_read_b128 a[128:131], v244 offset:2048
ds_read_b128 a[132:135], v244 offset:2304
v_mfma_f32_16x16x32_bf16 v[156:159], a[124:127], a[100:103], v[156:159]
ds_read_b64_tr_b16 a[148:149], v246 offset:8192
ds_read_b64_tr_b16 a[150:151], v246 offset:10240
v_mfma_f32_16x16x32_bf16 v[160:163], a[112:115], a[104:107], v[160:163]
ds_read_b64_tr_b16 a[152:153], v247 offset:8192
ds_read_b64_tr_b16 a[154:155], v247 offset:10240
v_mfma_f32_16x16x32_bf16 v[164:167], a[116:119], a[104:107], v[164:167]
ds_read_b64_tr_b16 a[156:157], v248 offset:8192
ds_read_b64_tr_b16 a[158:159], v248 offset:10240
v_mfma_f32_16x16x32_bf16 v[168:171], a[120:123], a[104:107], v[168:171]
ds_read_b128 a[72:75], v244 offset:4096
ds_read_b128 a[76:79], v244 offset:4352
v_mfma_f32_16x16x32_bf16 v[172:175], a[124:127], a[104:107], v[172:175]
ds_read_b128 a[136:139], v244 offset:6144
ds_read_b128 a[140:143], v244 offset:6400
v_mfma_f32_16x16x32_bf16 v[176:179], a[112:115], a[108:111], v[176:179]
s_mov_b32 s24, 0
s_mov_b32 s25, 32768
v_mfma_f32_16x16x32_bf16 v[180:183], a[116:119], a[108:111], v[180:183]
s_mov_b32 s26, 65536
s_mov_b32 s27, 98304
v_mfma_f32_16x16x32_bf16 v[184:187], a[120:123], a[108:111], v[184:187]
s_mov_b32 s28, 131072
s_mov_b32 s29, 163840
v_mfma_f32_16x16x32_bf16 v[188:191], a[124:127], a[108:111], v[188:191]
s_mov_b32 s30, 196608
s_mov_b32 s31, 229376
s_waitcnt lgkmcnt(0)
v_mfma_f32_16x16x32_bf16 v[128:131], a[80:83], a[64:67], v[128:131]
v_mfma_f32_16x16x32_bf16 v[132:135], a[84:87], a[64:67], v[132:135]
v_mfma_f32_16x16x32_bf16 v[136:139], a[88:91], a[64:67], v[136:139]
v_mfma_f32_16x16x32_bf16 v[140:143], a[92:95], a[64:67], v[140:143]
v_mfma_f32_16x16x32_bf16 v[128:131], a[144:147], a[128:131], v[128:131]
v_mfma_f32_16x16x32_bf16 v[132:135], a[148:151], a[128:131], v[132:135]
v_mfma_f32_16x16x32_bf16 v[136:139], a[152:155], a[128:131], v[136:139]
v_mfma_f32_16x16x32_bf16 v[140:143], a[156:159], a[128:131], v[140:143]
v_mfma_f32_16x16x32_bf16 v[144:147], a[80:83], a[68:71], v[144:147]
v_mfma_f32_16x16x32_bf16 v[148:151], a[84:87], a[68:71], v[148:151]
v_mfma_f32_16x16x32_bf16 v[152:155], a[88:91], a[68:71], v[152:155]
s_nop 1
v_cvt_pk_bf16_f32 v192, v128, v129
v_cvt_pk_bf16_f32 v194, v132, v133
v_cvt_pk_bf16_f32 v193, v130, v131
v_mfma_f32_16x16x32_bf16 v[156:159], a[92:95], a[68:71], v[156:159]
v_cvt_pk_bf16_f32 v195, v134, v135
v_cvt_pk_bf16_f32 v196, v136, v137
v_cvt_pk_bf16_f32 v198, v140, v141
v_mfma_f32_16x16x32_bf16 v[144:147], a[144:147], a[132:135], v[144:147]
v_cvt_pk_bf16_f32 v197, v138, v139
v_cvt_pk_bf16_f32 v199, v142, v143
v_mfma_f32_16x16x32_bf16 v[148:151], a[148:151], a[132:135], v[148:151]
v_permlane16_swap_b32 v192, v194
v_mfma_f32_16x16x32_bf16 v[152:155], a[152:155], a[132:135], v[152:155]
v_permlane16_swap_b32 v193, v195
v_mfma_f32_16x16x32_bf16 v[156:159], a[156:159], a[132:135], v[156:159]
v_permlane16_swap_b32 v196, v198
v_mfma_f32_16x16x32_bf16 v[160:163], a[80:83], a[72:75], v[160:163]
v_permlane16_swap_b32 v197, v199
v_mov_b32 v200, v192
v_mfma_f32_16x16x32_bf16 v[164:167], a[84:87], a[72:75], v[164:167]
v_mov_b32 v201, v193
v_mov_b32 v202, v194
v_mov_b32 v203, v195
v_mfma_f32_16x16x32_bf16 v[168:171], a[88:91], a[72:75], v[168:171]
v_mov_b32_dpp v192, v196 row_ror:8 row_mask:0xf bank_mask:0xc
v_mov_b32_dpp v193, v197 row_ror:8 row_mask:0xf bank_mask:0xc
v_mov_b32_dpp v194, v198 row_ror:8 row_mask:0xf bank_mask:0xc
v_mfma_f32_16x16x32_bf16 v[172:175], a[92:95], a[72:75], v[172:175]
v_mov_b32_dpp v195, v199 row_ror:8 row_mask:0xf bank_mask:0xc
v_mfma_f32_16x16x32_bf16 v[160:163], a[144:147], a[136:139], v[160:163]
buffer_store_dwordx4 v[192:195], v251, s[16:19], s24 offen nt
v_mov_b32_dpp v196, v200 row_ror:8 row_mask:0xf bank_mask:0x3
v_mov_b32_dpp v197, v201 row_ror:8 row_mask:0xf bank_mask:0x3
v_mfma_f32_16x16x32_bf16 v[164:167], a[148:151], a[136:139], v[164:167]
v_mov_b32_dpp v198, v202 row_ror:8 row_mask:0xf bank_mask:0x3
v_mov_b32_dpp v199, v203 row_ror:8 row_mask:0xf bank_mask:0x3
v_mfma_f32_16x16x32_bf16 v[168:171], a[152:155], a[136:139], v[168:171]
buffer_store_dwordx4 v[196:199], v251, s[16:19], s25 offen nt
v_cvt_pk_bf16_f32 v204, v144, v145
v_cvt_pk_bf16_f32 v206, v148, v149
v_mfma_f32_16x16x32_bf16 v[172:175], a[156:159], a[136:139], v[172:175]
v_cvt_pk_bf16_f32 v205, v146, v147
v_cvt_pk_bf16_f32 v207, v150, v151
v_cvt_pk_bf16_f32 v208, v152, v153
v_mfma_f32_16x16x32_bf16 v[176:179], a[80:83], a[76:79], v[176:179]
v_cvt_pk_bf16_f32 v210, v156, v157
v_cvt_pk_bf16_f32 v209, v154, v155
v_cvt_pk_bf16_f32 v211, v158, v159
v_mfma_f32_16x16x32_bf16 v[180:183], a[84:87], a[76:79], v[180:183]
v_permlane16_swap_b32 v204, v206
v_mfma_f32_16x16x32_bf16 v[184:187], a[88:91], a[76:79], v[184:187]
v_permlane16_swap_b32 v205, v207
v_mfma_f32_16x16x32_bf16 v[188:191], a[92:95], a[76:79], v[188:191]
v_permlane16_swap_b32 v208, v210
v_mfma_f32_16x16x32_bf16 v[176:179], a[144:147], a[140:143], v[176:179]
v_permlane16_swap_b32 v209, v211
v_mov_b32 v212, v204
v_mfma_f32_16x16x32_bf16 v[180:183], a[148:151], a[140:143], v[180:183]
v_mov_b32 v213, v205
v_mov_b32 v214, v206
v_mov_b32 v215, v207
v_mfma_f32_16x16x32_bf16 v[184:187], a[152:155], a[140:143], v[184:187]
v_mov_b32_dpp v204, v208 row_ror:8 row_mask:0xf bank_mask:0xc
v_mov_b32_dpp v205, v209 row_ror:8 row_mask:0xf bank_mask:0xc
v_mov_b32_dpp v206, v210 row_ror:8 row_mask:0xf bank_mask:0xc
v_mfma_f32_16x16x32_bf16 v[188:191], a[156:159], a[140:143], v[188:191]
v_mov_b32_dpp v207, v211 row_ror:8 row_mask:0xf bank_mask:0xc
buffer_store_dwordx4 v[204:207], v251, s[16:19], s26 offen nt
v_mov_b32_dpp v208, v212 row_ror:8 row_mask:0xf bank_mask:0x3
v_mov_b32_dpp v209, v213 row_ror:8 row_mask:0xf bank_mask:0x3
v_mov_b32_dpp v210, v214 row_ror:8 row_mask:0xf bank_mask:0x3
v_mov_b32_dpp v211, v215 row_ror:8 row_mask:0xf bank_mask:0x3
buffer_store_dwordx4 v[208:211], v251, s[16:19], s27 offen nt
v_cvt_pk_bf16_f32 v192, v160, v161
v_cvt_pk_bf16_f32 v194, v164, v165
v_cvt_pk_bf16_f32 v193, v162, v163
v_cvt_pk_bf16_f32 v195, v166, v167
v_cvt_pk_bf16_f32 v196, v168, v169
v_cvt_pk_bf16_f32 v198, v172, v173
v_cvt_pk_bf16_f32 v197, v170, v171
v_cvt_pk_bf16_f32 v199, v174, v175
v_permlane16_swap_b32 v192, v194
v_permlane16_swap_b32 v193, v195
v_permlane16_swap_b32 v196, v198
v_permlane16_swap_b32 v197, v199
v_mov_b32 v200, v192
v_mov_b32 v201, v193
v_mov_b32 v202, v194
v_mov_b32 v203, v195
v_mov_b32_dpp v192, v196 row_ror:8 row_mask:0xf bank_mask:0xc
v_mov_b32_dpp v193, v197 row_ror:8 row_mask:0xf bank_mask:0xc
v_mov_b32_dpp v194, v198 row_ror:8 row_mask:0xf bank_mask:0xc
v_mov_b32_dpp v195, v199 row_ror:8 row_mask:0xf bank_mask:0xc
buffer_store_dwordx4 v[192:195], v251, s[16:19], s28 offen nt
v_mov_b32_dpp v196, v200 row_ror:8 row_mask:0xf bank_mask:0x3
v_mov_b32_dpp v197, v201 row_ror:8 row_mask:0xf bank_mask:0x3
v_mov_b32_dpp v198, v202 row_ror:8 row_mask:0xf bank_mask:0x3
v_mov_b32_dpp v199, v203 row_ror:8 row_mask:0xf bank_mask:0x3
buffer_store_dwordx4 v[196:199], v251, s[16:19], s29 offen nt
v_cvt_pk_bf16_f32 v204, v176, v177
v_cvt_pk_bf16_f32 v206, v180, v181
v_cvt_pk_bf16_f32 v205, v178, v179
v_cvt_pk_bf16_f32 v207, v182, v183
v_cvt_pk_bf16_f32 v208, v184, v185
v_cvt_pk_bf16_f32 v210, v188, v189
v_cvt_pk_bf16_f32 v209, v186, v187
v_cvt_pk_bf16_f32 v211, v190, v191
v_permlane16_swap_b32 v204, v206
v_permlane16_swap_b32 v205, v207
v_permlane16_swap_b32 v208, v210
v_permlane16_swap_b32 v209, v211
v_mov_b32 v212, v204
v_mov_b32 v213, v205
v_mov_b32 v214, v206
v_mov_b32 v215, v207
v_mov_b32_dpp v204, v208 row_ror:8 row_mask:0xf bank_mask:0xc
v_mov_b32_dpp v205, v209 row_ror:8 row_mask:0xf bank_mask:0xc
v_mov_b32_dpp v206, v210 row_ror:8 row_mask:0xf bank_mask:0xc
v_mov_b32_dpp v207, v211 row_ror:8 row_mask:0xf bank_mask:0xc
buffer_store_dwordx4 v[204:207], v251, s[16:19], s30 offen nt
v_mov_b32_dpp v208, v212 row_ror:8 row_mask:0xf bank_mask:0x3
v_mov_b32_dpp v209, v213 row_ror:8 row_mask:0xf bank_mask:0x3
v_mov_b32_dpp v210, v214 row_ror:8 row_mask:0xf bank_mask:0x3
v_mov_b32_dpp v211, v215 row_ror:8 row_mask:0xf bank_mask:0x3
buffer_store_dwordx4 v[208:211], v251, s[16:19], s31 offen nt
.Lend:
s_endpgm
.Lspin1:
s_mov_b32 s93, 0
.Lspin1_l:
s_sleep 4
buffer_load_dword v8, v3, s[60:63], s90 offen sc1
s_waitcnt vmcnt(0)
v_and_b32 v0, 7, v8
v_cmp_lt_u32 s[28:29], v0, v14
v_cmp_eq_u32 s[30:31], v8, v2
s_nop 0
s_or_b64 s[28:29], s[28:29], s[30:31]
s_cbranch_scc0 .Lspin1_ret
s_add_u32 s93, s93, 1
s_cmp_lt_u32 s93, 0x100000
s_cbranch_scc1 .Lspin1_l
s_branch .Lspin1_ret
.Lj0a:
s_lshl_b32 s28, s3, 5
v_add_u32 v13, s28, v8
v_cmp_gt_i32 s[32:33], 0, v13
v_nop
v_cmp_gt_i32 s[34:35], 1, v13
v_cndmask_b32 v160, v160, v34, s[32:33]
v_cmp_gt_i32 s[32:33], 2, v13
v_cndmask_b32 v161, v161, v34, s[34:35]
v_cmp_gt_i32 s[34:35], 3, v13
v_cndmask_b32 v162, v162, v34, s[32:33]
v_cmp_gt_i32 s[32:33], 8, v13
v_cndmask_b32 v163, v163, v34, s[34:35]
v_cmp_gt_i32 s[34:35], 9, v13
v_cndmask_b32 v164, v164, v34, s[32:33]
v_cmp_gt_i32 s[32:33], 10, v13
v_cndmask_b32 v165, v165, v34, s[34:35]
v_cmp_gt_i32 s[34:35], 11, v13
v_cndmask_b32 v166, v166, v34, s[32:33]
v_cmp_gt_i32 s[32:33], 16, v13
v_cndmask_b32 v167, v167, v34, s[34:35]
v_cmp_gt_i32 s[34:35], 17, v13
v_cndmask_b32 v168, v168, v34, s[32:33]
v_cmp_gt_i32 s[32:33], 18, v13
v_cndmask_b32 v169, v169, v34, s[34:35]
v_cmp_gt_i32 s[34:35], 19, v13
v_cndmask_b32 v170, v170, v34, s[32:33]
v_cmp_gt_i32 s[32:33], 24, v13
v_cndmask_b32 v171, v171, v34, s[34:35]
v_cmp_gt_i32 s[34:35], 25, v13
v_cndmask_b32 v172, v172, v34, s[32:33]
v_cmp_gt_i32 s[32:33], 26, v13
v_cndmask_b32 v173, v173, v34, s[34:35]
v_cmp_gt_i32 s[34:35], 27, v13
v_cndmask_b32 v174, v174, v34, s[32:33]
v_nop
v_cndmask_b32 v175, v175, v34, s[34:35]
s_cmp_eq_u32 s89, 0
s_branch .Lj0a_ret
.Lj0b:
v_mov_b32 v14, v208
v_subrev_u32 v13, 32, v13
v_cmp_gt_i32 s[32:33], 0, v13
v_nop
v_cmp_gt_i32 s[34:35], 1, v13
v_cndmask_b32 v208, v208, v34, s[32:33]
v_cmp_gt_i32 s[32:33], 2, v13
v_cndmask_b32 v209, v209, v34, s[34:35]
v_cmp_gt_i32 s[34:35], 3, v13
v_cndmask_b32 v210, v210, v34, s[32:33]
v_cmp_gt_i32 s[32:33], 8, v13
v_cndmask_b32 v211, v211, v34, s[34:35]
v_cmp_gt_i32 s[34:35], 9, v13
v_cndmask_b32 v212, v212, v34, s[32:33]
v_cmp_gt_i32 s[32:33], 10, v13
v_cndmask_b32 v213, v213, v34, s[34:35]
v_cmp_gt_i32 s[34:35], 11, v13
v_cndmask_b32 v214, v214, v34, s[32:33]
v_cmp_gt_i32 s[32:33], 16, v13
v_cndmask_b32 v215, v215, v34, s[34:35]
v_cmp_gt_i32 s[34:35], 17, v13
v_cndmask_b32 v216, v216, v34, s[32:33]
v_cmp_gt_i32 s[32:33], 18, v13
v_cndmask_b32 v217, v217, v34, s[34:35]
v_cmp_gt_i32 s[34:35], 19, v13
v_cndmask_b32 v218, v218, v34, s[32:33]
v_cmp_gt_i32 s[32:33], 24, v13
v_cndmask_b32 v219, v219, v34, s[34:35]
v_cmp_gt_i32 s[34:35], 25, v13
v_cndmask_b32 v220, v220, v34, s[32:33]
v_cmp_gt_i32 s[32:33], 26, v13
v_cndmask_b32 v221, v221, v34, s[34:35]
v_cmp_gt_i32 s[34:35], 27, v13
v_cndmask_b32 v222, v222, v34, s[32:33]
v_nop
v_cndmask_b32 v223, v223, v34, s[34:35]
s_branch .Lj0b_ret
.Lj0c:
v_subrev_u32 v13, 32, v13
v_cmp_gt_i32 s[32:33], 0, v13
v_nop
v_cmp_gt_i32 s[34:35], 1, v13
v_cndmask_b32 v208, v208, v34, s[32:33]
v_cmp_gt_i32 s[32:33], 2, v13
v_cndmask_b32 v209, v209, v34, s[34:35]
v_cmp_gt_i32 s[34:35], 3, v13
v_cndmask_b32 v210, v210, v34, s[32:33]
v_cmp_gt_i32 s[32:33], 8, v13
v_cndmask_b32 v211, v211, v34, s[34:35]
v_cmp_gt_i32 s[34:35], 9, v13
v_cndmask_b32 v212, v212, v34, s[32:33]
v_cmp_gt_i32 s[32:33], 10, v13
v_cndmask_b32 v213, v213, v34, s[34:35]
v_cmp_gt_i32 s[34:35], 11, v13
v_cndmask_b32 v214, v214, v34, s[32:33]
v_cmp_gt_i32 s[32:33], 16, v13
v_cndmask_b32 v215, v215, v34, s[34:35]
v_cmp_gt_i32 s[34:35], 17, v13
v_cndmask_b32 v216, v216, v34, s[32:33]
v_cmp_gt_i32 s[32:33], 18, v13
v_cndmask_b32 v217, v217, v34, s[34:35]
v_cmp_gt_i32 s[34:35], 19, v13
v_cndmask_b32 v218, v218, v34, s[32:33]
v_cmp_gt_i32 s[32:33], 24, v13
v_cndmask_b32 v219, v219, v34, s[34:35]
v_cmp_gt_i32 s[34:35], 25, v13
v_cndmask_b32 v220, v220, v34, s[32:33]
v_cmp_gt_i32 s[32:33], 26, v13
v_cndmask_b32 v221, v221, v34, s[34:35]
v_cmp_gt_i32 s[34:35], 27, v13
v_cndmask_b32 v222, v222, v34, s[32:33]
v_nop
v_cndmask_b32 v223, v223, v34, s[34:35]
v_subrev_u32 v13, 32, v13
s_branch .Lj0c_ret
.Lool_pad:
s_nop 0
.Lev_d1:
s_cmp_eq_u32 s43, s42
s_cbranch_scc1 .LT0
s_swappc_b64 s[30:31], s[58:59]
.Lev_d1_r:
s_branch .LD0
.Lev_s1:
s_cmp_eq_u32 s43, s42
s_cbranch_scc1 .LE0
s_swappc_b64 s[30:31], s[58:59]
.Lev_s1_r:
s_branch .LS0
.Lspin21:
s_mov_b32 s93, 0
.Lspin21_l:
s_sleep 4
buffer_load_dword v9, v10, s[60:63], 0 offen sc1
s_waitcnt vmcnt(0)
v_cmp_gt_u32 vcc, 1, v9
s_cmp_lg_u64 vcc, 0
s_cbranch_scc0 .Lspin21_ret
s_add_u32 s93, s93, 1
s_cmp_lt_u32 s93, 0x100000
s_cbranch_scc1 .Lspin21_l
s_branch .Lspin21_ret
.Ljob0:
s_waitcnt vmcnt(8)
v_mov_b32 v0, 1
buffer_store_dword v0, v5, s[60:63], 0 offen sc1
global_load_dword v35, v[2:3], off
v_add_co_u32 v2, vcc, 0x20000, v2
s_nop 1
v_addc_co_u32 v3, vcc, 0, v3, vcc
s_mov_b32 s57, 4
s_add_u32 s93, s43, 1
s_max_u32 s43, s57, s93
s_min_u32 s43, s43, s42
s_sub_u32 s55, s43, s93
s_swappc_b64 s[58:59], s[30:31]
.Ljob1:
global_load_dword v35, v[2:3], off
v_add_co_u32 v2, vcc, 0x20000, v2
s_nop 1
v_addc_co_u32 v3, vcc, 0, v3, vcc
s_mov_b32 s57, 6
s_add_u32 s93, s43, 1
s_max_u32 s43, s57, s93
s_min_u32 s43, s43, s42
s_sub_u32 s55, s43, s93
s_swappc_b64 s[58:59], s[30:31]
.Ljob2:
global_load_dword v35, v[2:3], off
v_add_co_u32 v2, vcc, 0x20000, v2
s_nop 1
v_addc_co_u32 v3, vcc, 0, v3, vcc
s_mov_b32 s57, 8
s_add_u32 s93, s43, 1
s_max_u32 s43, s57, s93
s_min_u32 s43, s43, s42
s_sub_u32 s55, s43, s93
s_swappc_b64 s[58:59], s[30:31]
.Ljob3:
global_load_dword v35, v[2:3], off
v_add_co_u32 v2, vcc, 0x20000, v2
s_nop 1
v_addc_co_u32 v3, vcc, 0, v3, vcc
s_mov_b32 s57, 10
s_add_u32 s93, s43, 1
s_max_u32 s43, s57, s93
s_min_u32 s43, s43, s42
s_sub_u32 s55, s43, s93
s_swappc_b64 s[58:59], s[30:31]
.Ljob4:
v_and_b32 v10, 7, v1
v_and_b32 v9, 8, v1
v_lshl_or_b32 v10, v9, 1, v10
s_and_b32 s93, s87, 2
s_lshl_b32 s93, s93, 2
v_or_b32 v10, s93, v10
v_lshlrev_b32 v10, 9, v10
v_bfe_u32 v9, v1, 4, 1
v_lshl_or_b32 v10, v9, 2, v10
s_lshl_b32 s93, s86, 6
v_add_u32 v10, s93, v10
s_and_b32 s93, s87, 1
s_lshl_b32 s93, s93, 3
s_add_u32 s93, s93, 0x10000
v_add_u32 v10, s93, v10
buffer_load_dword v9, v10, s[60:63], 0 offen sc1
global_load_dword v35, v[2:3], off
v_add_co_u32 v2, vcc, 0x20000, v2
s_nop 1
v_addc_co_u32 v3, vcc, 0, v3, vcc
s_mov_b32 s57, 12
s_add_u32 s93, s43, 1
s_max_u32 s43, s57, s93
s_min_u32 s43, s43, s42
s_sub_u32 s55, s43, s93
s_swappc_b64 s[58:59], s[30:31]
.Ljob5:
s_waitcnt vmcnt(5)
v_cmp_gt_u32 vcc, 1, v9
s_cmp_lg_u64 vcc, 0
s_cbranch_scc1 .Lspin21
.Lspin21_ret:
global_load_dword v35, v[2:3], off
v_add_co_u32 v2, vcc, 0x20000, v2
s_nop 1
v_addc_co_u32 v3, vcc, 0, v3, vcc
s_mov_b32 s57, 14
s_add_u32 s93, s43, 1
s_max_u32 s43, s57, s93
s_min_u32 s43, s43, s42
s_sub_u32 s55, s43, s93
s_swappc_b64 s[58:59], s[30:31]
.Ljob6:
global_load_dword v35, v[2:3], off
v_add_co_u32 v2, vcc, 0x20000, v2
s_nop 1
v_addc_co_u32 v3, vcc, 0, v3, vcc
s_mov_b32 s57, 16
s_add_u32 s93, s43, 1
s_max_u32 s43, s57, s93
s_min_u32 s43, s43, s42
s_sub_u32 s55, s43, s93
s_swappc_b64 s[58:59], s[30:31]
.Ljob7:
global_load_dword v35, v[2:3], off
v_add_co_u32 v2, vcc, 0x20000, v2
s_nop 1
v_addc_co_u32 v3, vcc, 0, v3, vcc
s_mov_b32 s57, 2147483647
s_add_u32 s93, s43, 1
s_max_u32 s43, s57, s93
s_min_u32 s43, s43, s42
s_sub_u32 s55, s43, s93
s_swappc_b64 s[58:59], s[30:31]
.Lx:
s_sub_u32 s4, s4, s91
s_subb_u32 s5, s5, 0
s_mov_b32 s6, 0x80000
s_sub_u32 s8, s8, s91
s_subb_u32 s9, s9, 0
s_mov_b32 s10, 0x84000
s_mov_b64 s[38:39], s[32:33]
v_add_u32 v222, 0xffff0000, v32
v_add_u32 v222, s91, v222
v_add_u32 v223, 0x4000, v33
v_add_u32 v223, s91, v223
s_cmp_eq_u64 s[30:31], 0
s_cbranch_scc1 .Lx_s0
v_mov_b32 v224, v64
v_mov_b32 v225, v65
v_mov_b32 v226, v66
v_mov_b32 v227, v67
v_mov_b32 v228, v68
v_mov_b32 v229, v69
v_mov_b32 v230, v70
v_mov_b32 v231, v71
v_mov_b32 v232, v72
v_mov_b32 v233, v73
v_mov_b32 v234, v74
v_mov_b32 v235, v75
v_mov_b32 v236, v76
v_mov_b32 v237, v77
v_mov_b32 v238, v78
v_mov_b32 v239, v79
v_mov_b32 v240, v80
v_mov_b32 v241, v81
v_mov_b32 v242, v82
v_mov_b32 v243, v83
v_mov_b32 v244, v84
v_mov_b32 v245, v85
v_mov_b32 v246, v86
v_mov_b32 v247, v87
v_mov_b32 v248, v88
v_mov_b32 v249, v89
v_mov_b32 v250, v90
v_mov_b32 v251, v91
v_mov_b32 v252, v92
v_mov_b32 v253, v93
v_mov_b32 v254, v94
v_mov_b32 v255, v95
s_sub_u32 s40, 15, s89
s_lshl_b32 s40, s40, 2
s_add_u32 s40, s40, s3
s_getpc_b64 s[42:43]
.Lx_p0:
s_add_u32 s42, s42, .Lx_r0-.Lx_p0
s_addc_u32 s43, s43, 0
s_branch .Lx_core
.Lx_r0:
v_accvgpr_write_b32 a64, v128
v_accvgpr_write_b32 a65, v129
v_accvgpr_write_b32 a66, v130
v_accvgpr_write_b32 a67, v131
v_accvgpr_write_b32 a68, v132
v_accvgpr_write_b32 a69, v133
v_accvgpr_write_b32 a70, v134
v_accvgpr_write_b32 a71, v135
v_accvgpr_write_b32 a72, v136
v_accvgpr_write_b32 a73, v137
v_accvgpr_write_b32 a74, v138
v_accvgpr_write_b32 a75, v139
v_accvgpr_write_b32 a76, v140
v_accvgpr_write_b32 a77, v141
v_accvgpr_write_b32 a78, v142
v_accvgpr_write_b32 a79, v143
v_accvgpr_write_b32 a80, v144
v_accvgpr_write_b32 a81, v145
v_accvgpr_write_b32 a82, v146
v_accvgpr_write_b32 a83, v147
v_accvgpr_write_b32 a84, v148
v_accvgpr_write_b32 a85, v149
v_accvgpr_write_b32 a86, v150
v_accvgpr_write_b32 a87, v151
v_accvgpr_write_b32 a88, v152
v_accvgpr_write_b32 a89, v153
v_accvgpr_write_b32 a90, v154
v_accvgpr_write_b32 a91, v155
v_accvgpr_write_b32 a92, v156
v_accvgpr_write_b32 a93, v157
v_accvgpr_write_b32 a94, v158
v_accvgpr_write_b32 a95, v159
v_accvgpr_write_b32 a112, v160
v_accvgpr_write_b32 a113, v161
v_accvgpr_write_b32 a114, v162
v_accvgpr_write_b32 a115, v163
v_accvgpr_write_b32 a116, v164
v_accvgpr_write_b32 a117, v165
v_accvgpr_write_b32 a118, v166
v_accvgpr_write_b32 a119, v167
v_accvgpr_write_b32 a120, v168
v_accvgpr_write_b32 a121, v169
v_accvgpr_write_b32 a122, v170
v_accvgpr_write_b32 a123, v171
v_accvgpr_write_b32 a124, v172
v_accvgpr_write_b32 a125, v173
v_accvgpr_write_b32 a126, v174
v_accvgpr_write_b32 a127, v175
v_accvgpr_write_b32 a144, v176
v_accvgpr_write_b32 a145, v177
v_accvgpr_write_b32 a146, v178
v_accvgpr_write_b32 a147, v179
v_accvgpr_write_b32 a148, v180
v_accvgpr_write_b32 a149, v181
v_accvgpr_write_b32 a150, v182
v_accvgpr_write_b32 a151, v183
v_accvgpr_write_b32 a152, v184
v_accvgpr_write_b32 a153, v185
v_accvgpr_write_b32 a154, v186
v_accvgpr_write_b32 a155, v187
v_accvgpr_write_b32 a156, v188
v_accvgpr_write_b32 a157, v189
v_accvgpr_write_b32 a158, v190
v_accvgpr_write_b32 a159, v191
v_mov_b32 v60, v220
.Lx_s0:
s_cmp_eq_u64 s[38:39], 0
s_cbranch_scc1 .Lx_s1
v_mov_b32 v224, v96
v_mov_b32 v225, v97
v_mov_b32 v226, v98
v_mov_b32 v227, v99
v_mov_b32 v228, v100
v_mov_b32 v229, v101
v_mov_b32 v230, v102
v_mov_b32 v231, v103
v_mov_b32 v232, v104
v_mov_b32 v233, v105
v_mov_b32 v234, v106
v_mov_b32 v235, v107
v_mov_b32 v236, v108
v_mov_b32 v237, v109
v_mov_b32 v238, v110
v_mov_b32 v239, v111
v_mov_b32 v240, v112
v_mov_b32 v241, v113
v_mov_b32 v242, v114
v_mov_b32 v243, v115
v_mov_b32 v244, v116
v_mov_b32 v245, v117
v_mov_b32 v246, v118
v_mov_b32 v247, v119
v_mov_b32 v248, v120
v_mov_b32 v249, v121
v_mov_b32 v250, v122
v_mov_b32 v251, v123
v_mov_b32 v252, v124
v_mov_b32 v253, v125
v_mov_b32 v254, v126
v_mov_b32 v255, v127
s_mov_b32 s40, s89
s_lshl_b32 s40, s40, 2
s_add_u32 s40, s40, s3
s_getpc_b64 s[42:43]
.Lx_p1:
s_add_u32 s42, s42, .Lx_r1-.Lx_p1
s_addc_u32 s43, s43, 0
s_branch .Lx_core
.Lx_r1:
v_accvgpr_write_b32 a176, v128
v_accvgpr_write_b32 a177, v129
v_accvgpr_write_b32 a178, v130
v_accvgpr_write_b32 a179, v131
v_accvgpr_write_b32 a180, v132
v_accvgpr_write_b32 a181, v133
v_accvgpr_write_b32 a182, v134
v_accvgpr_write_b32 a183, v135
v_accvgpr_write_b32 a184, v136
v_accvgpr_write_b32 a185, v137
v_accvgpr_write_b32 a186, v138
v_accvgpr_write_b32 a187, v139
v_accvgpr_write_b32 a188, v140
v_accvgpr_write_b32 a189, v141
v_accvgpr_write_b32 a190, v142
v_accvgpr_write_b32 a191, v143
v_accvgpr_write_b32 a208, v144
v_accvgpr_write_b32 a209, v145
v_accvgpr_write_b32 a210, v146
v_accvgpr_write_b32 a211, v147
v_accvgpr_write_b32 a212, v148
v_accvgpr_write_b32 a213, v149
v_accvgpr_write_b32 a214, v150
v_accvgpr_write_b32 a215, v151
v_accvgpr_write_b32 a216, v152
v_accvgpr_write_b32 a217, v153
v_accvgpr_write_b32 a218, v154
v_accvgpr_write_b32 a219, v155
.Lpad24:
s_nop 0
v_accvgpr_write_b32 a220, v156
v_accvgpr_write_b32 a221, v157
v_accvgpr_write_b32 a222, v158
v_accvgpr_write_b32 a223, v159
v_accvgpr_write_b32 a240, v160
v_accvgpr_write_b32 a241, v161
v_accvgpr_write_b32 a242, v162
v_accvgpr_write_b32 a243, v163
v_accvgpr_write_b32 a244, v164
v_accvgpr_write_b32 a245, v165
v_accvgpr_write_b32 a246, v166
v_accvgpr_write_b32 a247, v167
v_accvgpr_write_b32 a248, v168
v_accvgpr_write_b32 a249, v169
v_accvgpr_write_b32 a250, v170
v_accvgpr_write_b32 a251, v171
v_accvgpr_write_b32 a252, v172
v_accvgpr_write_b32 a253, v173
v_accvgpr_write_b32 a254, v174
v_accvgpr_write_b32 a255, v175
v_accvgpr_write_b32 a96, v176
v_accvgpr_write_b32 a97, v177
v_accvgpr_write_b32 a98, v178
v_accvgpr_write_b32 a99, v179
v_accvgpr_write_b32 a100, v180
v_accvgpr_write_b32 a101, v181
v_accvgpr_write_b32 a102, v182
v_accvgpr_write_b32 a103, v183
v_accvgpr_write_b32 a104, v184
v_accvgpr_write_b32 a105, v185
v_accvgpr_write_b32 a106, v186
v_accvgpr_write_b32 a107, v187
v_accvgpr_write_b32 a108, v188
v_accvgpr_write_b32 a109, v189
v_accvgpr_write_b32 a110, v190
v_accvgpr_write_b32 a111, v191
v_mov_b32 v61, v220
.Lx_s1:
s_branch .Lx_ret
.Lx_core:
s_lshl_b32 s41, s40, 5
v_and_b32 v221, 31, v1
v_lshrrev_b32 v218, 5, v1
v_lshlrev_b32 v218, 2, v218
v_sub_u32 v221, v221, v218
v_add_u32 v221, s41, v221
v_mov_b32 v216, v34
v_mov_b32 v220, 0
s_mov_b32 s44, 0
s_mov_b32 s45, 4096
s_mov_b32 s46, 0
v_mov_b32 v128, 0
v_mov_b32 v129, 0
v_mov_b32 v130, 0
v_mov_b32 v131, 0
v_mov_b32 v132, 0
v_mov_b32 v133, 0
v_mov_b32 v134, 0
v_mov_b32 v135, 0
v_mov_b32 v136, 0
v_mov_b32 v137, 0
v_mov_b32 v138, 0
v_mov_b32 v139, 0
v_mov_b32 v140, 0
v_mov_b32 v141, 0
v_mov_b32 v142, 0
v_mov_b32 v143, 0
v_mov_b32 v144, 0
v_mov_b32 v145, 0
v_mov_b32 v146, 0
v_mov_b32 v147, 0
v_mov_b32 v148, 0
v_mov_b32 v149, 0
v_mov_b32 v150, 0
v_mov_b32 v151, 0
v_mov_b32 v152, 0
v_mov_b32 v153, 0
v_mov_b32 v154, 0
v_mov_b32 v155, 0
v_mov_b32 v156, 0
v_mov_b32 v157, 0
v_mov_b32 v158, 0
v_mov_b32 v159, 0
v_mov_b32 v160, 0
v_mov_b32 v161, 0
v_mov_b32 v162, 0
v_mov_b32 v163, 0
v_mov_b32 v164, 0
v_mov_b32 v165, 0
v_mov_b32 v166, 0
v_mov_b32 v167, 0
v_mov_b32 v168, 0
v_mov_b32 v169, 0
v_mov_b32 v170, 0
v_mov_b32 v171, 0
v_mov_b32 v172, 0
v_mov_b32 v173, 0
v_mov_b32 v174, 0
v_mov_b32 v175, 0
v_mov_b32 v176, 0
v_mov_b32 v177, 0
v_mov_b32 v178, 0
v_mov_b32 v179, 0
v_mov_b32 v180, 0
v_mov_b32 v181, 0
v_mov_b32 v182, 0
v_mov_b32 v183, 0
v_mov_b32 v184, 0
v_mov_b32 v185, 0
v_mov_b32 v186, 0
v_mov_b32 v187, 0
v_mov_b32 v188, 0
v_mov_b32 v189, 0
v_mov_b32 v190, 0
v_mov_b32 v191, 0
.Lx_loop:
buffer_load_dwordx4 a[0:3], v222, s[4:7], s44 offen offset:0 sc1
buffer_load_dwordx4 a[4:7], v222, s[4:7], s44 offen offset:512 sc1
buffer_load_dwordx4 a[8:11], v222, s[4:7], s44 offen offset:1024 sc1
buffer_load_dwordx4 a[12:15], v222, s[4:7], s44 offen offset:1536 sc1
buffer_load_dwordx4 a[16:19], v222, s[4:7], s44 offen offset:2048 sc1
buffer_load_dwordx4 a[20:23], v222, s[4:7], s44 offen offset:2560 sc1
buffer_load_dwordx4 a[24:27], v222, s[4:7], s44 offen offset:3072 sc1
buffer_load_dwordx4 a[28:31], v222, s[4:7], s44 offen offset:3584 sc1
buffer_load_dwordx4 a[32:35], v223, s[8:11], s44 offen offset:0 sc1
buffer_load_dwordx4 a[36:39], v223, s[8:11], s44 offen offset:1024 sc1
buffer_load_dwordx4 a[40:43], v223, s[8:11], s44 offen offset:2048 sc1
buffer_load_dwordx4 a[44:47], v223, s[8:11], s44 offen offset:3072 sc1
buffer_load_dwordx4 a[48:51], v223, s[8:11], s45 offen offset:0 sc1
buffer_load_dwordx4 a[52:55], v223, s[8:11], s45 offen offset:1024 sc1
buffer_load_dwordx4 a[56:59], v223, s[8:11], s45 offen offset:2048 sc1
buffer_load_dwordx4 a[60:63], v223, s[8:11], s45 offen offset:3072 sc1
s_waitcnt vmcnt(8)
v_mfma_f32_32x32x16_bf16 v[192:207], a[0:3], v[224:227], 0
v_mfma_f32_32x32x16_bf16 v[192:207], a[4:7], v[228:231], v[192:207]
v_mfma_f32_32x32x16_bf16 v[192:207], a[8:11], v[232:235], v[192:207]
v_mfma_f32_32x32x16_bf16 v[192:207], a[12:15], v[236:239], v[192:207]
v_mfma_f32_32x32x16_bf16 v[192:207], a[16:19], v[240:243], v[192:207]
v_mfma_f32_32x32x16_bf16 v[192:207], a[20:23], v[244:247], v[192:207]
v_mfma_f32_32x32x16_bf16 v[192:207], a[24:27], v[248:251], v[192:207]
v_mfma_f32_32x32x16_bf16 v[192:207], a[28:31], v[252:255], v[192:207]
v_cmp_gt_i32 s[32:33], 0, v221
v_nop
v_cmp_gt_i32 s[34:35], 1, v221
s_nop 8
v_cndmask_b32 v192, v192, v34, s[32:33]
v_cmp_gt_i32 s[32:33], 2, v221
v_cndmask_b32 v193, v193, v34, s[34:35]
v_cmp_gt_i32 s[34:35], 3, v221
v_cndmask_b32 v194, v194, v34, s[32:33]
v_cmp_gt_i32 s[32:33], 8, v221
v_cndmask_b32 v195, v195, v34, s[34:35]
v_cmp_gt_i32 s[34:35], 9, v221
v_cndmask_b32 v196, v196, v34, s[32:33]
v_cmp_gt_i32 s[32:33], 10, v221
v_cndmask_b32 v197, v197, v34, s[34:35]
v_cmp_gt_i32 s[34:35], 11, v221
v_cndmask_b32 v198, v198, v34, s[32:33]
v_cmp_gt_i32 s[32:33], 16, v221
v_cndmask_b32 v199, v199, v34, s[34:35]
v_cmp_gt_i32 s[34:35], 17, v221
v_cndmask_b32 v200, v200, v34, s[32:33]
v_cmp_gt_i32 s[32:33], 18, v221
v_cndmask_b32 v201, v201, v34, s[34:35]
v_cmp_gt_i32 s[34:35], 19, v221
v_cndmask_b32 v202, v202, v34, s[32:33]
v_cmp_gt_i32 s[32:33], 24, v221
v_cndmask_b32 v203, v203, v34, s[34:35]
v_cmp_gt_i32 s[34:35], 25, v221
v_cndmask_b32 v204, v204, v34, s[32:33]
v_cmp_gt_i32 s[32:33], 26, v221
v_cndmask_b32 v205, v205, v34, s[34:35]
v_cmp_gt_i32 s[34:35], 27, v221
v_cndmask_b32 v206, v206, v34, s[32:33]
v_nop
v_cndmask_b32 v207, v207, v34, s[34:35]
v_max3_f32 v217, v192, v193, v216
v_max3_f32 v217, v217, v194, v195
v_max3_f32 v217, v217, v196, v197
v_max3_f32 v217, v217, v198, v199
v_max3_f32 v217, v217, v200, v201
v_max3_f32 v217, v217, v202, v203
v_max3_f32 v217, v217, v204, v205
v_max3_f32 v217, v217, v206, v207
v_mov_b32 v218, v217
s_nop 1
v_permlane32_swap_b32 v217, v218
v_max_f32 v217, v217, v218
v_sub_f32 v219, v216, v217
v_exp_f32 v219, v219
v_mov_b32 v216, v217
v_sub_f32 v192, v192, v217
v_sub_f32 v193, v193, v217
v_sub_f32 v194, v194, v217
v_sub_f32 v195, v195, v217
v_sub_f32 v196, v196, v217
v_sub_f32 v197, v197, v217
v_sub_f32 v198, v198, v217
v_sub_f32 v199, v199, v217
v_sub_f32 v200, v200, v217
v_sub_f32 v201, v201, v217
v_sub_f32 v202, v202, v217
v_sub_f32 v203, v203, v217
v_sub_f32 v204, v204, v217
v_sub_f32 v205, v205, v217
v_sub_f32 v206, v206, v217
v_sub_f32 v207, v207, v217
v_exp_f32 v192, v192
v_exp_f32 v193, v193
v_exp_f32 v194, v194
v_exp_f32 v195, v195
v_exp_f32 v196, v196
v_exp_f32 v197, v197
v_exp_f32 v198, v198
v_exp_f32 v199, v199
v_exp_f32 v200, v200
v_exp_f32 v201, v201
v_exp_f32 v202, v202
v_exp_f32 v203, v203
v_exp_f32 v204, v204
v_exp_f32 v205, v205
v_exp_f32 v206, v206
v_exp_f32 v207, v207
v_mul_f32 v220, v220, v219
v_add_f32 v220, v220, v192
v_add_f32 v220, v220, v193
v_add_f32 v220, v220, v194
v_add_f32 v220, v220, v195
v_add_f32 v220, v220, v196
v_add_f32 v220, v220, v197
v_add_f32 v220, v220, v198
v_add_f32 v220, v220, v199
v_add_f32 v220, v220, v200
v_add_f32 v220, v220, v201
v_add_f32 v220, v220, v202
v_add_f32 v220, v220, v203
v_add_f32 v220, v220, v204
v_add_f32 v220, v220, v205
v_add_f32 v220, v220, v206
v_add_f32 v220, v220, v207
v_cvt_pk_bf16_f32 v208, v192, v193
v_cvt_pk_bf16_f32 v209, v194, v195
v_cvt_pk_bf16_f32 v210, v196, v197
v_cvt_pk_bf16_f32 v211, v198, v199
v_cvt_pk_bf16_f32 v212, v200, v201
v_cvt_pk_bf16_f32 v213, v202, v203
v_cvt_pk_bf16_f32 v214, v204, v205
v_cvt_pk_bf16_f32 v215, v206, v207
v_mul_f32 v128, v219, v128
v_mul_f32 v129, v219, v129
v_mul_f32 v130, v219, v130
v_mul_f32 v131, v219, v131
v_mul_f32 v132, v219, v132
v_mul_f32 v133, v219, v133
v_mul_f32 v134, v219, v134
v_mul_f32 v135, v219, v135
v_mul_f32 v136, v219, v136
v_mul_f32 v137, v219, v137
v_mul_f32 v138, v219, v138
v_mul_f32 v139, v219, v139
v_mul_f32 v140, v219, v140
v_mul_f32 v141, v219, v141
v_mul_f32 v142, v219, v142
v_mul_f32 v143, v219, v143
v_mul_f32 v144, v219, v144
v_mul_f32 v145, v219, v145
v_mul_f32 v146, v219, v146
v_mul_f32 v147, v219, v147
v_mul_f32 v148, v219, v148
v_mul_f32 v149, v219, v149
v_mul_f32 v150, v219, v150
v_mul_f32 v151, v219, v151
v_mul_f32 v152, v219, v152
v_mul_f32 v153, v219, v153
v_mul_f32 v154, v219, v154
v_mul_f32 v155, v219, v155
v_mul_f32 v156, v219, v156
v_mul_f32 v157, v219, v157
v_mul_f32 v158, v219, v158
v_mul_f32 v159, v219, v159
v_mul_f32 v160, v219, v160
v_mul_f32 v161, v219, v161
v_mul_f32 v162, v219, v162
v_mul_f32 v163, v219, v163
v_mul_f32 v164, v219, v164
v_mul_f32 v165, v219, v165
v_mul_f32 v166, v219, v166
v_mul_f32 v167, v219, v167
v_mul_f32 v168, v219, v168
v_mul_f32 v169, v219, v169
v_mul_f32 v170, v219, v170
v_mul_f32 v171, v219, v171
v_mul_f32 v172, v219, v172
v_mul_f32 v173, v219, v173
v_mul_f32 v174, v219, v174
v_mul_f32 v175, v219, v175
v_mul_f32 v176, v219, v176
v_mul_f32 v177, v219, v177
v_mul_f32 v178, v219, v178
v_mul_f32 v179, v219, v179
v_mul_f32 v180, v219, v180
v_mul_f32 v181, v219, v181
v_mul_f32 v182, v219, v182
v_mul_f32 v183, v219, v183
v_mul_f32 v184, v219, v184
v_mul_f32 v185, v219, v185
v_mul_f32 v186, v219, v186
v_mul_f32 v187, v219, v187
v_mul_f32 v188, v219, v188
v_mul_f32 v189, v219, v189
v_mul_f32 v190, v219, v190
v_mul_f32 v191, v219, v191
s_waitcnt vmcnt(0)
v_mfma_f32_32x32x16_bf16 v[128:143], a[32:35], v[208:211], v[128:143]
v_mfma_f32_32x32x16_bf16 v[144:159], a[36:39], v[208:211], v[144:159]
v_mfma_f32_32x32x16_bf16 v[160:175], a[40:43], v[208:211], v[160:175]
v_mfma_f32_32x32x16_bf16 v[176:191], a[44:47], v[208:211], v[176:191]
v_mfma_f32_32x32x16_bf16 v[128:143], a[48:51], v[212:215], v[128:143]
v_mfma_f32_32x32x16_bf16 v[144:159], a[52:55], v[212:215], v[144:159]
v_mfma_f32_32x32x16_bf16 v[160:175], a[56:59], v[212:215], v[160:175]
v_mfma_f32_32x32x16_bf16 v[176:191], a[60:63], v[212:215], v[176:191]
v_subrev_u32 v221, 32, v221
s_add_u32 s44, s44, 8192
s_add_u32 s45, s45, 8192
s_add_u32 s46, s46, 1
s_cmp_le_u32 s46, s40
s_cbranch_scc1 .Lx_loop
v_mov_b32 v218, v220
s_nop 1
v_permlane32_swap_b32 v220, v218
v_add_f32 v220, v220, v218
s_setpc_b64 s[42:43]
.Lspin3:
s_mov_b32 s93, 0
.Lspin3_l:
s_sleep 4
buffer_load_dword v250, v249, s[60:63], s90 offen sc1
s_waitcnt vmcnt(0)
v_and_b32 v250, 7, v250
v_cmp_gt_u32 vcc, 3, v250
s_cmp_lg_u64 vcc, 0
s_cbranch_scc0 .Lspin3_ret
s_add_u32 s93, s93, 1
s_cmp_lt_u32 s93, 0x100000
s_cbranch_scc1 .Lspin3_l
s_branch .Lspin3_ret
.Lspin5:
s_mov_b32 s93, 0
.Lspin5_l:
s_sleep 4
buffer_load_dword v250, v249, s[60:63], s90 offen sc1
s_waitcnt vmcnt(0)
v_and_b32 v250, 7, v250
v_cmp_gt_u32 vcc, 5, v250
s_cmp_lg_u64 vcc, 0
s_cbranch_scc0 .Lspin5_ret
s_add_u32 s93, s93, 1
s_cmp_lt_u32 s93, 0x100000
s_cbranch_scc1 .Lspin5_l
s_branch .Lspin5_ret
.Lspin4:
s_mov_b32 s93, 0
.Lspin4_l:
s_sleep 4
buffer_load_dword v250, v249, s[60:63], s90 offen sc1
s_waitcnt vmcnt(0)
v_and_b32 v250, 7, v250
v_cmp_gt_u32 vcc, 4, v250
s_cmp_lg_u64 vcc, 0
s_cbranch_scc0 .Lspin4_ret
s_add_u32 s93, s93, 1
s_cmp_lt_u32 s93, 0x100000
s_cbranch_scc1 .Lspin4_l
s_branch .Lspin4_ret
.Lpret:
.Lcode1:
)ASM"
    :: "{s[0:1]}"(__builtin_amdgcn_kernarg_segment_ptr()), "{s2}"(blockIdx.x), "{v0}"(threadIdx.x),
       "{s3}"((unsigned)(size_t)(__attribute__((address_space(3))) char*)smem)
    : "v0", "v1", "v2", "v3", "v4", "v5", "v6", "v7", "v8", "v9", "v10", "v11", "v12", "v13", "v14", "v15", "v16", "v17", "v18", "v19", "v20", "v21", "v22", "v23", "v24", "v25", "v26", "v27", "v28", "v29", "v30", "v31", "v32", "v33", "v34", "v35", "v36", "v37", "v38", "v39", "v40", "v41", "v42", "v43", "v44", "v45", "v46", "v47", "v48", "v49", "v50", "v51", "v52", "v53", "v54", "v55", "v56", "v57", "v58", "v59", "v60", "v61", "v62", "v63", "v64", "v65", "v66", "v67", "v68", "v69", "v70", "v71", "v72", "v73", "v74", "v75", "v76", "v77", "v78", "v79", "v80", "v81", "v82", "v83", "v84", "v85", "v86", "v87", "v88", "v89", "v90", "v91", "v92", "v93", "v94", "v95", "v96", "v97", "v98", "v99", "v100", "v101", "v102", "v103", "v104", "v105", "v106", "v107", "v108", "v109", "v110", "v111", "v112", "v113", "v114", "v115", "v116", "v117", "v118", "v119", "v120", "v121", "v122", "v123", "v124", "v125", "v126", "v127", "v128", "v129", "v130", "v131", "v132", "v133", "v134", "v135", "v136", "v137", "v138", "v139", "v140", "v141", "v142", "v143", "v144", "v145", "v146", "v147", "v148", "v149", "v150", "v151", "v152", "v153", "v154", "v155", "v156", "v157", "v158", "v159", "v160", "v161", "v162", "v163", "v164", "v165", "v166", "v167", "v168", "v169", "v170", "v171", "v172", "v173", "v174", "v175", "v176", "v177", "v178", "v179", "v180", "v181", "v182", "v183", "v184", "v185", "v186", "v187", "v188", "v189", "v190", "v191", "v192", "v193", "v194", "v195", "v196", "v197", "v198", "v199", "v200", "v201", "v202", "v203", "v204", "v205", "v206", "v207", "v208", "v209", "v210", "v211", "v212", "v213", "v214", "v215", "v216", "v217", "v218", "v219", "v220", "v221", "v222", "v223", "v224", "v225", "v226", "v227", "v228", "v229", "v230", "v231", "v232", "v233", "v234", "v235", "v236", "v237", "v238", "v239", "v240", "v241", "v242", "v243", "v244", "v245", "v246", "v247", "v248", "v249", "v250", "v251", "v252", "v253", "v254", "v255", "a0", "a1", "a2", "a3", "a4", "a5", "a6", "a7", "a8", "a9", "a10", "a11", "a12", "a13", "a14", "a15", "a16", "a17", "a18", "a19", "a20", "a21", "a22", "a23", "a24", "a25", "a26", "a27", "a28", "a29", "a30", "a31", "a32", "a33", "a34", "a35", "a36", "a37", "a38", "a39", "a40", "a41", "a42", "a43", "a44", "a45", "a46", "a47", "a48", "a49", "a50", "a51", "a52", "a53", "a54", "a55", "a56", "a57", "a58", "a59", "a60", "a61", "a62", "a63", "a64", "a65", "a66", "a67", "a68", "a69", "a70", "a71", "a72", "a73", "a74", "a75", "a76", "a77", "a78", "a79", "a80", "a81", "a82", "a83", "a84", "a85", "a86", "a87", "a88", "a89", "a90", "a91", "a92", "a93", "a94", "a95", "a96", "a97", "a98", "a99", "a100", "a101", "a102", "a103", "a104", "a105", "a106", "a107", "a108", "a109", "a110", "a111", "a112", "a113", "a114", "a115", "a116", "a117", "a118", "a119", "a120", "a121", "a122", "a123", "a124", "a125", "a126", "a127", "a128", "a129", "a130", "a131", "a132", "a133", "a134", "a135", "a136", "a137", "a138", "a139", "a140", "a141", "a142", "a143", "a144", "a145", "a146", "a147", "a148", "a149", "a150", "a151", "a152", "a153", "a154", "a155", "a156", "a157", "a158", "a159", "a160", "a161", "a162", "a163", "a164", "a165", "a166", "a167", "a168", "a169", "a170", "a171", "a172", "a173", "a174", "a175", "a176", "a177", "a178", "a179", "a180", "a181", "a182", "a183", "a184", "a185", "a186", "a187", "a188", "a189", "a190", "a191", "a192", "a193", "a194", "a195", "a196", "a197", "a198", "a199", "a200", "a201", "a202", "a203", "a204", "a205", "a206", "a207", "a208", "a209", "a210", "a211", "a212", "a213", "a214", "a215", "a216", "a217", "a218", "a219", "a220", "a221", "a222", "a223", "a224", "a225", "a226", "a227", "a228", "a229", "a230", "a231", "a232", "a233", "a234", "a235", "a236", "a237", "a238", "a239", "a240", "a241", "a242", "a243", "a244", "a245", "a246", "a247", "a248", "a249", "a250", "a251", "a252", "a253", "a254", "a255", "s3", "s4", "s5", "s6", "s7", "s8", "s9", "s10", "s11", "s12", "s13", "s14", "s15", "s16", "s17", "s18", "s19", "s20", "s21", "s22", "s23", "s24", "s25", "s26", "s27", "s28", "s29", "s30", "s31", "s32", "s33", "s34", "s35", "s36", "s37", "s38", "s39", "s40", "s41", "s42", "s43", "s44", "s45", "s46", "s47", "s48", "s49", "s50", "s51", "s52", "s53", "s54", "s55", "s56", "s57", "s58", "s59", "s60", "s61", "s62", "s63", "s64", "s65", "s66", "s67", "s68", "s69", "s70", "s71", "s72", "s73", "s74", "s75", "s76", "s77", "s78", "s79", "s80", "s81", "s82", "s83", "s84", "s85", "s86", "s87", "s88", "s89", "s90", "s91", "s92", "s93", "s94", "s95", "s96", "s97", "s98", "s99", "s100", "s101", "vcc", "m0", "scc", "memory");
}
extern "C" void kernel_launch(void* const* d_in, const int* in_sizes, int n_in, void* d_out, int out_size,
                              void* d_ws, size_t ws_size, hipStream_t stream) {
  Args a;
  for (int i = 0; i < 9; ++i) a.p[i] = d_in[i];
  a.out = d_out; a.ws = (char*)d_ws + (0 ? ((ws_size - (72u << 20)) & ~(size_t)0xfffff) : 0);
  mega_kernel<<<256, 256, 0, stream>>>(a);
}
```

```cpp
#include <hip/hip_runtime.h>
#include <stdint.h>
struct Args { const void* p[9]; void* out; void* ws; };
__global__ __launch_bounds__(256, 1) __attribute__((aligned(4096))) void mega_kernel(Args args) {
  __shared__ __attribute__((aligned(16))) char smem[163840];
  asm volatile(R"ASM(
s_getpc_b64 s[96:97]
.Lcode0:
s_load_dwordx16 s[64:79], s[0:1], 0
s_load_dwordx4 s[80:83], s[0:1], 64
s_load_dwordx2 s[84:85], s[0:1], 80
v_and_b32 v1, 63, v0
v_lshrrev_b32 v16, 6, v0
v_lshlrev_b32 v7, 4, v1
v_readfirstlane_b32 s3, v16
s_and_b32 s86, s2, 7
s_lshr_b32 s87, s2, 3
s_and_b32 s4, s87, 3
s_lshl_b32 s88, s86, 2
s_add_u32 s88, s88, s4
s_lshr_b32 s89, s2, 5
s_lshl_b32 s91, s3, 12
s_lshl_b32 s92, s3, 13
s_and_b32 s97, s97, 0xffff
s_mov_b32 s98, .Lcode1-.Lcode0
s_mov_b32 s99, 0x20000
s_lshl_b32 s33, s87, 2
s_add_u32 s33, s33, s3
s_lshl_b32 s33, s33, 10
v_and_b32 v4, 7, v1
v_lshlrev_b32 v4, 7, v4
v_add_u32 v4, s33, v4
buffer_load_dword v35, v4, s[96:99], 0 offen
s_and_b32 s27, s87, 7
s_bfe_u32 s28, s87, 0x10003
s_lshl_b32 s29, s86, 1
s_add_u32 s28, s28, s29
v_lshrrev_b32 v40, 3, v1
v_and_b32 v41, 7, v1
v_xor_b32 v41, v41, v40
v_lshlrev_b32 v41, 4, v41
v_lshl_add_u32 v202, v40, 10, v41
v_add_u32 v203, 7168, v202
v_add_u32 v204, 260096, v202
v_add_u32 v205, 267264, v202
v_lshl_add_u32 v20, v40, 11, v41
v_add_u32 v21, 15360, v20
v_add_u32 v22, 30720, v20
v_add_u32 v23, 46080, v20
v_lshrrev_b32 v40, 4, v1
v_and_b32 v41, 15, v1
v_lshlrev_b32 v42, 1, v40
v_xor_b32 v41, v41, v42
v_lshlrev_b32 v42, 4, v41
v_lshl_add_u32 v16, v40, 13, v42
v_add_u32 v17, 31744, v16
v_xor_b32 v18, 0x80, v16
v_add_u32 v18, 63488, v18
v_add_u32 v19, 31744, v18
s_and_b32 s30, s3, 1
s_lshr_b32 s31, s3, 1
s_lshl_b32 s32, s30, 20
s_lshl_b32 s33, s27, 15
s_add_u32 s32, s32, s33
s_lshl_b32 s33, s31, 14
s_add_u32 s32, s32, s33
s_lshl_b32 s34, s28, 9
s_lshl_b32 s33, s31, 8
s_add_u32 s34, s34, s33
s_lshl_b32 s33, s30, 18
s_add_u32 s34, s34, s33
s_sub_u32 s35, 0x200000, s32
s_sub_u32 s36, 0x400000, s34
s_cmp_lt_u32 s87, 16
s_cselect_b32 s46, 1, 0
s_waitcnt lgkmcnt(0)
s_cselect_b32 s38, s76, s78
s_cselect_b32 s39, s77, s79
s_add_u32 s8, s38, s34
s_addc_u32 s9, s39, 0
s_and_b32 s9, s9, 0xffff
s_mov_b32 s10, s36
s_mov_b32 s11, 0x20000
s_add_u32 s4, s72, s32
s_addc_u32 s5, s73, 0
s_and_b32 s5, s5, 0xffff
s_mov_b32 s6, s35
s_mov_b32 s7, 0x20000
s_add_u32 s37, s92, 0
s_mov_b32 s42, 0
s_mov_b32 s43, 131072
s_mov_b32 m0, s37
s_nop 0
buffer_load_dwordx4 v16, s[8:11], s42 offen offset:0 nt lds
buffer_load_dwordx4 v17, s[8:11], s42 offen offset:1024 nt lds
buffer_load_dwordx4 v18, s[8:11], s42 offen offset:2048 nt lds
buffer_load_dwordx4 v19, s[8:11], s42 offen offset:3072 nt lds
s_add_u32 m0, s37, 4096
s_nop 0
buffer_load_dwordx4 v16, s[8:11], s43 offen offset:0 nt lds
buffer_load_dwordx4 v17, s[8:11], s43 offen offset:1024 nt lds
buffer_load_dwordx4 v18, s[8:11], s43 offen offset:2048 nt lds
buffer_load_dwordx4 v19, s[8:11], s43 offen offset:3072 nt lds
s_add_u32 s60, s84, 62914560
s_addc_u32 s61, s85, 0
s_and_b32 s61, s61, 0xffff
s_mov_b32 s62, 82944
s_mov_b32 s63, 0x20000
s_lshl_b32 s90, s86, 13
v_lshlrev_b32 v3, 5, v0
s_lshl_b32 s93, s2, 4
s_add_u32 s93, s93, s3
s_lshl_b32 s93, s93, 2
s_add_u32 s93, s93, 0x10000
s_add_u32 s37, s92, 32768
s_mov_b32 s40, 0
s_mov_b32 s41, 524288
s_mov_b32 m0, s37
s_nop 0
buffer_load_dwordx4 v202, s[4:7], s40 offen offset:0 lds
v_lshlrev_b32 v45, 20, v1
v_add_u32 v44, s93, v45
s_lshl_b32 s93, s87, 2
s_add_u32 s93, s93, s3
s_xor_b32 s93, s93, 32
s_lshl_b32 s93, s93, 20
s_lshl_b32 s0, s86, 2
s_add_u32 s93, s93, s0
s_add_u32 s93, s93, 0x14000
v_lshlrev_b32 v45, 7, v1
v_add_u32 v46, s93, v45
buffer_load_dwordx4 v203, s[4:7], s40 offen offset:1024 lds
v_mov_b32 v45, 0
s_lshl_b32 s33, s2, 5
v_lshlrev_b32 v41, 13, v0
v_lshrrev_b32 v42, 3, v0
v_lshl_add_u32 v41, v42, 20, v41
v_add_u32 v6, s33, v41
s_and_b32 s33, s87, 2
s_lshl_b32 s33, s33, 5
s_or_b32 s33, s33, s86
v_and_b32 v41, 0x47, v0
v_cmp_eq_u32 vcc, s33, v41
buffer_load_dwordx4 v204, s[4:7], s40 offen offset:2048 lds
s_nop 0
v_cndmask_b32 v14, 0, 1, vcc
s_lshl_b32 s33, s30, 14
s_add_u32 s33, s33, 32768
v_and_b32 v36, 15, v1
v_lshrrev_b32 v37, 4, v1
v_and_b32 v38, 7, v36
v_xor_b32 v38, v38, v37
v_lshlrev_b32 v38, 4, v38
v_lshl_add_u32 v192, v36, 7, v38
v_add_u32 v192, s33, v192
v_xor_b32 v193, 64, v192
buffer_load_dwordx4 v205, s[4:7], s40 offen offset:3072 lds
s_mov_b64 exec, 0
s_cmp_eq_u32 s3, 0
s_cbranch_scc1 .Lpad1
s_cmp_eq_u32 s3, 1
s_cbranch_scc1 .Lp1_v_pad
s_cmp_eq_u32 s3, 2
s_cbranch_scc1 .Lpad2
s_branch .Lpad3
.Ltch0:
s_mov_b64 exec, -1
s_lshl_b32 s33, s31, 14
v_and_b32 v36, 15, v1
v_lshrrev_b32 v37, 4, v1
v_lshrrev_b32 v38, 2, v36
v_and_b32 v39, 3, v36
v_lshrrev_b32 v40, 1, v39
v_xor_b32 v40, v40, v39
v_and_b32 v40, s46, v40
v_mul_u32_u24 v40, 3, v40
v_xor_b32 v39, v39, v40
v_lshl_add_u32 v40, v37, 3, v38
v_and_b32 v41, 1, v37
s_add_u32 m0, s37, 4096
s_nop 0
buffer_load_dwordx4 v202, s[4:7], s41 offen offset:0 lds
v_lshl_or_b32 v41, v41, 2, v38
v_lshlrev_b32 v40, 8, v40
v_lshl_add_u32 v40, v39, 3, v40
v_add_u32 v40, s33, v40
v_xor_b32 v36, 0, v41
v_lshl_add_u32 v194, v36, 5, v40
v_xor_b32 v36, 1, v41
v_lshl_add_u32 v195, v36, 5, v40
v_xor_b32 v36, 2, v41
v_lshl_add_u32 v196, v36, 5, v40
v_xor_b32 v36, 3, v41
buffer_load_dwordx4 v203, s[4:7], s41 offen offset:1024 lds
v_lshl_add_u32 v197, v36, 5, v40
v_xor_b32 v36, 4, v41
v_lshl_add_u32 v198, v36, 5, v40
v_xor_b32 v36, 5, v41
v_lshl_add_u32 v199, v36, 5, v40
v_xor_b32 v36, 6, v41
v_lshl_add_u32 v200, v36, 5, v40
v_xor_b32 v36, 7, v41
v_lshl_add_u32 v201, v36, 5, v40
s_and_b32 s95, s27, 3
s_lshl_b32 s33, s95, 6
buffer_load_dwordx4 v204, s[4:7], s41 offen offset:2048 lds
v_xor_b32 v194, s33, v194
v_xor_b32 v195, s33, v195
v_xor_b32 v196, s33, v196
v_xor_b32 v197, s33, v197
v_xor_b32 v198, s33, v198
v_xor_b32 v199, s33, v199
v_xor_b32 v200, s33, v200
v_xor_b32 v201, s33, v201
s_lshl_b32 s95, s95, 10
s_add_u32 s33, s92, 32768
v_and_b32 v36, 15, v1
buffer_load_dwordx4 v205, s[4:7], s41 offen offset:3072 lds
s_mov_b64 exec, 0
s_cmp_eq_u32 s3, 0
s_cbranch_scc1 .Lp2_pad
s_cmp_eq_u32 s3, 1
s_cbranch_scc1 .Lpad4
s_cmp_eq_u32 s3, 2
s_cbranch_scc1 .Lpad5
s_branch .Lpad6
.Ltch1:
s_mov_b64 exec, -1
v_lshrrev_b32 v37, 4, v1
v_and_b32 v38, 7, v36
v_xor_b32 v38, v38, v37
v_lshlrev_b32 v38, 4, v38
v_lshl_add_u32 v24, v36, 7, v38
v_add_u32 v24, s33, v24
v_xor_b32 v25, 64, v24
v_and_b32 v36, 15, v1
v_lshrrev_b32 v37, 4, v1
v_lshrrev_b32 v38, 2, v36
v_and_b32 v39, 3, v36
v_lshl_add_u32 v40, v37, 3, v38
s_add_u32 s37, s92, 65536
s_mov_b32 s42, 524288
s_mov_b32 s43, 655360
s_mov_b32 m0, s37
s_nop 0
buffer_load_dwordx4 v16, s[8:11], s42 offen offset:0 nt lds
v_and_b32 v41, 1, v37
v_lshl_or_b32 v41, v41, 2, v38
v_lshlrev_b32 v40, 8, v40
v_lshl_add_u32 v40, v39, 3, v40
v_add_u32 v40, 65536, v40
v_xor_b32 v36, 0, v41
v_lshl_add_u32 v26, v36, 5, v40
v_xor_b32 v36, 1, v41
v_lshl_add_u32 v27, v36, 5, v40
v_xor_b32 v36, 2, v41
v_lshl_add_u32 v28, v36, 5, v40
buffer_load_dwordx4 v17, s[8:11], s42 offen offset:1024 nt lds
v_xor_b32 v36, 3, v41
v_lshl_add_u32 v29, v36, 5, v40
v_xor_b32 v36, 4, v41
v_lshl_add_u32 v30, v36, 5, v40
v_xor_b32 v36, 5, v41
v_lshl_add_u32 v31, v36, 5, v40
v_xor_b32 v36, 6, v41
v_lshl_add_u32 v32, v36, 5, v40
v_xor_b32 v36, 7, v41
v_lshl_add_u32 v33, v36, 5, v40
s_add_u32 s12, s70, 0
s_addc_u32 s13, s71, 0
buffer_load_dwordx4 v18, s[8:11], s42 offen offset:2048 nt lds
s_and_b32 s13, s13, 0xffff
s_mov_b32 s14, 4194304
s_mov_b32 s15, 0x20000
s_lshl_b32 s33, s88, 8
s_lshl_b32 s34, s3, 17
s_add_u32 s33, s33, s34
s_sub_u32 s34, 0x800000, s33
s_add_u32 s16, s74, s33
s_addc_u32 s17, s75, 0
s_and_b32 s17, s17, 0xffff
buffer_load_dwordx4 v19, s[8:11], s42 offen offset:3072 nt lds
s_mov_b64 exec, 0
s_cmp_eq_u32 s3, 0
s_cbranch_scc1 .Lpad7
s_cmp_eq_u32 s3, 1
s_cbranch_scc1 .Lpad8
s_cmp_eq_u32 s3, 2
s_cbranch_scc1 .Lpad9
s_branch .Lj0_pad
.Ltch2:
s_mov_b64 exec, -1
s_mov_b32 s18, s34
s_mov_b32 s19, 0x20000
s_sub_u32 s24, 15, s89
s_lshl_b32 s24, s24, 18
s_lshl_b32 s25, s89, 18
s_lshl_b32 s33, s3, 16
s_add_u32 s94, s3, 1
s_and_b32 s94, s94, 3
s_add_u32 s24, s24, s33
s_lshl_b32 s33, s94, 16
s_add_u32 s25, s25, s33
s_mov_b32 s26, 0
s_add_u32 m0, s37, 4096
s_nop 0
buffer_load_dwordx4 v16, s[8:11], s43 offen offset:0 nt lds
s_lshl_b32 s33, s28, 1
s_add_u32 s33, s33, s31
s_lshl_b32 s33, s33, 19
s_lshl_b32 s34, s27, 1
s_add_u32 s34, s34, s30
s_lshl_b32 s34, s34, 12
s_add_u32 s33, s33, s34
s_xor_b32 s34, s46, 1
s_lshl_b32 s34, s34, 24
s_add_u32 s33, s33, s34
s_add_u32 s20, s84, s33
s_addc_u32 s21, s85, 0
buffer_load_dwordx4 v17, s[8:11], s43 offen offset:1024 nt lds
s_and_b32 s21, s21, 0xffff
s_mov_b32 s22, 524288
s_mov_b32 s23, 0x20000
s_cmp_eq_u32 s46, 1
s_cselect_b32 s47, 1040356334, 1.0
s_lshl_b32 s33, s87, 2
s_add_u32 s33, s33, s3
s_lshl_b32 s33, s33, 13
s_lshl_b32 s34, s86, 10
s_add_u32 s33, s33, s34
buffer_load_dwordx4 v18, s[8:11], s43 offen offset:2048 nt lds
v_and_b32 v36, 7, v1
v_lshlrev_b32 v36, 7, v36
v_add_u32 v206, s33, v36
s_lshl_b32 s33, s2, 2
s_add_u32 s33, s33, s3
s_lshl_b32 s33, s33, 12
v_bfe_u32 v37, v1, 1, 1
v_and_b32 v38, 1, v1
v_lshlrev_b32 v37, 11, v37
v_lshl_add_u32 v37, v38, 7, v37
v_add_u32 v207, s33, v37
buffer_load_dwordx4 v19, s[8:11], s43 offen offset:3072 nt lds
s_mov_b32 s56, s74
s_and_b32 s57, s75, 0xffff
s_mov_b32 s58, 0x800000
s_mov_b32 s59, 0x20000
s_mov_b32 s48, 0x100000
s_mov_b32 s49, 256
s_mov_b32 s35, s92
s_add_u32 s36, s92, 32768
s_add_u32 s39, s92, 163840
s_mov_b32 s54, 65536
s_mov_b32 s55, -98304
s_mov_b32 s38, 1
s_waitcnt vmcnt(8)
s_barrier
ds_read_b64_tr_b16 v[96:97], v194 offset:0
ds_read_b64_tr_b16 v[98:99], v194 offset:1024
ds_read_b128 v[64:67], v192 offset:0
ds_read_b64_tr_b16 v[100:101], v195 offset:0
ds_read_b64_tr_b16 v[102:103], v195 offset:1024
ds_read_b128 v[68:71], v192 offset:2048
ds_read_b64_tr_b16 v[104:105], v196 offset:0
ds_read_b64_tr_b16 v[106:107], v196 offset:1024
ds_read_b128 v[72:75], v192 offset:4096
ds_read_b64_tr_b16 v[108:109], v197 offset:0
ds_read_b64_tr_b16 v[110:111], v197 offset:1024
ds_read_b128 v[76:79], v192 offset:6144
s_waitcnt lgkmcnt(6)
ds_read_b64_tr_b16 v[112:113], v198 offset:0
ds_read_b64_tr_b16 v[114:115], v198 offset:1024
ds_read_b128 v[80:83], v192 offset:8192
ds_read_b64_tr_b16 v[116:117], v199 offset:0
ds_read_b64_tr_b16 v[118:119], v199 offset:1024
ds_read_b128 v[84:87], v192 offset:10240
ds_read_b64_tr_b16 v[120:121], v200 offset:0
ds_read_b64_tr_b16 v[122:123], v200 offset:1024
ds_read_b128 v[88:91], v192 offset:12288
ds_read_b64_tr_b16 v[124:125], v201 offset:0
ds_read_b64_tr_b16 v[126:127], v201 offset:1024
ds_read_b128 v[92:95], v192 offset:14336
s_cmp_eq_u32 s46, 1
s_waitcnt lgkmcnt(0)
s_cbranch_scc0 .Lp1_v
v_mfma_f32_16x16x32_bf16 a[0:3], v[96:99], v[64:67], 0
ds_read_b64_tr_b16 v[160:161], v194 offset:8192
v_mfma_f32_16x16x32_bf16 a[4:7], v[100:103], v[64:67], 0
s_add_u32 s37, s92, 98304
s_mov_b32 s40, 128
s_mov_b32 s41, 524416
v_mfma_f32_16x16x32_bf16 a[8:11], v[104:107], v[64:67], 0
s_mov_b32 m0, s37
ds_read_b64_tr_b16 v[162:163], v194 offset:9216
v_mfma_f32_16x16x32_bf16 a[12:15], v[108:111], v[64:67], 0
buffer_load_dwordx4 v202, s[4:7], s40 offen offset:0 lds
v_mfma_f32_16x16x32_bf16 a[16:19], v[112:115], v[64:67], 0
ds_read_b128 v[128:131], v193 offset:0
v_mfma_f32_16x16x32_bf16 a[20:23], v[116:119], v[64:67], 0
buffer_load_dwordx4 v203, s[4:7], s40 offen offset:1024 lds
v_mfma_f32_16x16x32_bf16 a[24:27], v[120:123], v[64:67], 0
ds_read_b64_tr_b16 v[164:165], v195 offset:8192
v_mfma_f32_16x16x32_bf16 a[28:31], v[124:127], v[64:67], 0
buffer_load_dwordx4 v204, s[4:7], s40 offen offset:2048 lds
v_mfma_f32_16x16x32_bf16 a[32:35], v[96:99], v[68:71], 0
ds_read_b64_tr_b16 v[166:167], v195 offset:9216
v_mfma_f32_16x16x32_bf16 a[36:39], v[100:103], v[68:71], 0
buffer_load_dwordx4 v205, s[4:7], s40 offen offset:3072 lds
v_mfma_f32_16x16x32_bf16 a[40:43], v[104:107], v[68:71], 0
s_add_u32 m0, s37, 4096
ds_read_b128 v[132:135], v193 offset:2048
v_mfma_f32_16x16x32_bf16 a[44:47], v[108:111], v[68:71], 0
buffer_load_dwordx4 v202, s[4:7], s41 offen offset:0 lds
v_mfma_f32_16x16x32_bf16 a[48:51], v[112:115], v[68:71], 0
ds_read_b64_tr_b16 v[168:169], v196 offset:8192
v_mfma_f32_16x16x32_bf16 a[52:55], v[116:119], v[68:71], 0
buffer_load_dwordx4 v203, s[4:7], s41 offen offset:1024 lds
v_mfma_f32_16x16x32_bf16 a[56:59], v[120:123], v[68:71], 0
ds_read_b64_tr_b16 v[170:171], v196 offset:9216
v_mfma_f32_16x16x32_bf16 a[60:63], v[124:127], v[68:71], 0
buffer_load_dwordx4 v204, s[4:7], s41 offen offset:2048 lds
v_mfma_f32_16x16x32_bf16 a[64:67], v[96:99], v[72:75], 0
ds_read_b128 v[136:139], v193 offset:4096
v_mfma_f32_16x16x32_bf16 a[68:71], v[100:103], v[72:75], 0
buffer_load_dwordx4 v205, s[4:7], s41 offen offset:3072 lds
v_mfma_f32_16x16x32_bf16 a[72:75], v[104:107], v[72:75], 0
ds_read_b64_tr_b16 v[172:173], v197 offset:8192
v_mfma_f32_16x16x32_bf16 a[76:79], v[108:111], v[72:75], 0
s_add_u32 s37, s92, 131072
s_mov_b32 s42, 1048576
s_mov_b32 s43, 1179648
v_mfma_f32_16x16x32_bf16 a[80:83], v[112:115], v[72:75], 0
s_mov_b32 m0, s37
ds_read_b64_tr_b16 v[174:175], v197 offset:9216
v_mfma_f32_16x16x32_bf16 a[84:87], v[116:119], v[72:75], 0
buffer_load_dwordx4 v16, s[8:11], s42 offen offset:0 nt lds
v_mfma_f32_16x16x32_bf16 a[88:91], v[120:123], v[72:75], 0
ds_read_b128 v[140:143], v193 offset:6144
v_mfma_f32_16x16x32_bf16 a[92:95], v[124:127], v[72:75], 0
buffer_load_dwordx4 v17, s[8:11], s42 offen offset:1024 nt lds
v_mfma_f32_16x16x32_bf16 a[96:99], v[96:99], v[76:79], 0
ds_read_b64_tr_b16 v[176:177], v198 offset:8192
v_mfma_f32_16x16x32_bf16 a[100:103], v[100:103], v[76:79], 0
buffer_load_dwordx4 v18, s[8:11], s42 offen offset:2048 nt lds
v_mfma_f32_16x16x32_bf16 a[104:107], v[104:107], v[76:79], 0
ds_read_b64_tr_b16 v[178:179], v198 offset:9216
v_mfma_f32_16x16x32_bf16 a[108:111], v[108:111], v[76:79], 0
buffer_load_dwordx4 v19, s[8:11], s42 offen offset:3072 nt lds
v_mfma_f32_16x16x32_bf16 a[112:115], v[112:115], v[76:79], 0
s_add_u32 m0, s37, 4096
ds_read_b128 v[144:147], v193 offset:8192
v_mfma_f32_16x16x32_bf16 a[116:119], v[116:119], v[76:79], 0
buffer_load_dwordx4 v16, s[8:11], s43 offen offset:0 nt lds
v_mfma_f32_16x16x32_bf16 a[120:123], v[120:123], v[76:79], 0
ds_read_b64_tr_b16 v[180:181], v199 offset:8192
v_mfma_f32_16x16x32_bf16 a[124:127], v[124:127], v[76:79], 0
buffer_load_dwordx4 v17, s[8:11], s43 offen offset:1024 nt lds
v_mfma_f32_16x16x32_bf16 a[128:131], v[96:99], v[80:83], 0
ds_read_b64_tr_b16 v[182:183], v199 offset:9216
v_mfma_f32_16x16x32_bf16 a[132:135], v[100:103], v[80:83], 0
buffer_load_dwordx4 v18, s[8:11], s43 offen offset:2048 nt lds
v_mfma_f32_16x16x32_bf16 a[136:139], v[104:107], v[80:83], 0
ds_read_b128 v[148:151], v193 offset:10240
v_mfma_f32_16x16x32_bf16 a[140:143], v[108:111], v[80:83], 0
buffer_load_dwordx4 v19, s[8:11], s43 offen offset:3072 nt lds
v_mfma_f32_16x16x32_bf16 a[144:147], v[112:115], v[80:83], 0
ds_read_b64_tr_b16 v[184:185], v200 offset:8192
v_mfma_f32_16x16x32_bf16 a[148:151], v[116:119], v[80:83], 0
v_mfma_f32_16x16x32_bf16 a[152:155], v[120:123], v[80:83], 0
ds_read_b64_tr_b16 v[186:187], v200 offset:9216
v_mfma_f32_16x16x32_bf16 a[156:159], v[124:127], v[80:83], 0
v_mfma_f32_16x16x32_bf16 a[160:163], v[96:99], v[84:87], 0
ds_read_b128 v[152:155], v193 offset:12288
v_mfma_f32_16x16x32_bf16 a[164:167], v[100:103], v[84:87], 0
v_mfma_f32_16x16x32_bf16 a[168:171], v[104:107], v[84:87], 0
ds_read_b64_tr_b16 v[188:189], v201 offset:8192
v_mfma_f32_16x16x32_bf16 a[172:175], v[108:111], v[84:87], 0
v_mfma_f32_16x16x32_bf16 a[176:179], v[112:115], v[84:87], 0
s_add_u32 s44, s35, 65536
ds_read_b64_tr_b16 v[190:191], v201 offset:9216
v_mfma_f32_16x16x32_bf16 a[180:183], v[116:119], v[84:87], 0
s_cmp_ge_u32 s44, s39
s_cselect_b32 s45, s55, s54
v_mfma_f32_16x16x32_bf16 a[184:187], v[120:123], v[84:87], 0
ds_read_b128 v[156:159], v193 offset:14336
v_mfma_f32_16x16x32_bf16 a[188:191], v[124:127], v[84:87], 0
s_add_u32 s44, s36, 65536
v_mfma_f32_16x16x32_bf16 a[192:195], v[96:99], v[88:91], 0
v_mfma_f32_16x16x32_bf16 a[196:199], v[100:103], v[88:91], 0
s_cmp_ge_u32 s44, s39
s_cselect_b32 s34, s55, s54
v_mfma_f32_16x16x32_bf16 a[200:203], v[104:107], v[88:91], 0
v_add_u32 v194, s45, v194
v_mfma_f32_16x16x32_bf16 a[204:207], v[108:111], v[88:91], 0
v_add_u32 v195, s45, v195
v_mfma_f32_16x16x32_bf16 a[208:211], v[112:115], v[88:91], 0
v_add_u32 v196, s45, v196
v_mfma_f32_16x16x32_bf16 a[212:215], v[116:119], v[88:91], 0
v_add_u32 v197, s45, v197
v_mfma_f32_16x16x32_bf16 a[216:219], v[120:123], v[88:91], 0
v_add_u32 v198, s45, v198
v_mfma_f32_16x16x32_bf16 a[220:223], v[124:127], v[88:91], 0
v_add_u32 v199, s45, v199
v_mfma_f32_16x16x32_bf16 a[224:227], v[96:99], v[92:95], 0
v_add_u32 v200, s45, v200
v_mfma_f32_16x16x32_bf16 a[228:231], v[100:103], v[92:95], 0
v_add_u32 v201, s45, v201
v_mfma_f32_16x16x32_bf16 a[232:235], v[104:107], v[92:95], 0
v_add_u32 v192, s34, v192
v_mfma_f32_16x16x32_bf16 a[236:239], v[108:111], v[92:95], 0
v_add_u32 v193, s34, v193
v_mfma_f32_16x16x32_bf16 a[240:243], v[112:115], v[92:95], 0
v_mfma_f32_16x16x32_bf16 a[244:247], v[116:119], v[92:95], 0
s_mov_b32 s40, 256
v_mfma_f32_16x16x32_bf16 a[248:251], v[120:123], v[92:95], 0
s_mov_b32 s41, 524544
v_mfma_f32_16x16x32_bf16 a[252:255], v[124:127], v[92:95], 0
s_waitcnt vmcnt(8) lgkmcnt(0)
s_barrier
v_mfma_f32_16x16x32_bf16 a[0:3], v[160:163], v[128:131], a[0:3]
s_mov_b32 m0, s35
ds_read_b64_tr_b16 v[96:97], v194 offset:0
v_mfma_f32_16x16x32_bf16 a[4:7], v[164:167], v[128:131], a[4:7]
buffer_load_dwordx4 v202, s[4:7], s40 offen offset:0 lds
v_mfma_f32_16x16x32_bf16 a[8:11], v[168:171], v[128:131], a[8:11]
ds_read_b64_tr_b16 v[98:99], v194 offset:1024
v_mfma_f32_16x16x32_bf16 a[12:15], v[172:175], v[128:131], a[12:15]
v_mfma_f32_16x16x32_bf16 a[16:19], v[176:179], v[128:131], a[16:19]
ds_read_b128 v[64:67], v192 offset:0
v_mfma_f32_16x16x32_bf16 a[20:23], v[180:183], v[128:131], a[20:23]
buffer_load_dwordx4 v203, s[4:7], s40 offen offset:1024 lds
v_mfma_f32_16x16x32_bf16 a[24:27], v[184:187], v[128:131], a[24:27]
ds_read_b64_tr_b16 v[100:101], v195 offset:0
v_mfma_f32_16x16x32_bf16 a[28:31], v[188:191], v[128:131], a[28:31]
v_mfma_f32_16x16x32_bf16 a[32:35], v[160:163], v[132:135], a[32:35]
ds_read_b64_tr_b16 v[102:103], v195 offset:1024
v_mfma_f32_16x16x32_bf16 a[36:39], v[164:167], v[132:135], a[36:39]
buffer_load_dwordx4 v204, s[4:7], s40 offen offset:2048 lds
v_mfma_f32_16x16x32_bf16 a[40:43], v[168:171], v[132:135], a[40:43]
ds_read_b128 v[68:71], v192 offset:2048
v_mfma_f32_16x16x32_bf16 a[44:47], v[172:175], v[132:135], a[44:47]
v_mfma_f32_16x16x32_bf16 a[48:51], v[176:179], v[132:135], a[48:51]
ds_read_b64_tr_b16 v[104:105], v196 offset:0
v_mfma_f32_16x16x32_bf16 a[52:55], v[180:183], v[132:135], a[52:55]
buffer_load_dwordx4 v205, s[4:7], s40 offen offset:3072 lds
v_mfma_f32_16x16x32_bf16 a[56:59], v[184:187], v[132:135], a[56:59]
ds_read_b64_tr_b16 v[106:107], v196 offset:1024
v_mfma_f32_16x16x32_bf16 a[60:63], v[188:191], v[132:135], a[60:63]
v_mfma_f32_16x16x32_bf16 a[64:67], v[160:163], v[136:139], a[64:67]
s_add_u32 m0, s35, 4096
ds_read_b128 v[72:75], v192 offset:4096
v_mfma_f32_16x16x32_bf16 a[68:71], v[164:167], v[136:139], a[68:71]
buffer_load_dwordx4 v202, s[4:7], s41 offen offset:0 lds
v_mfma_f32_16x16x32_bf16 a[72:75], v[168:171], v[136:139], a[72:75]
ds_read_b64_tr_b16 v[108:109], v197 offset:0
v_mfma_f32_16x16x32_bf16 a[76:79], v[172:175], v[136:139], a[76:79]
v_mfma_f32_16x16x32_bf16 a[80:83], v[176:179], v[136:139], a[80:83]
ds_read_b64_tr_b16 v[110:111], v197 offset:1024
v_mfma_f32_16x16x32_bf16 a[84:87], v[180:183], v[136:139], a[84:87]
buffer_load_dwordx4 v203, s[4:7], s41 offen offset:1024 lds
v_mfma_f32_16x16x32_bf16 a[88:91], v[184:187], v[136:139], a[88:91]
ds_read_b128 v[76:79], v192 offset:6144
v_mfma_f32_16x16x32_bf16 a[92:95], v[188:191], v[136:139], a[92:95]
v_mfma_f32_16x16x32_bf16 a[96:99], v[160:163], v[140:143], a[96:99]
ds_read_b64_tr_b16 v[112:113], v198 offset:0
v_mfma_f32_16x16x32_bf16 a[100:103], v[164:167], v[140:143], a[100:103]
buffer_load_dwordx4 v204, s[4:7], s41 offen offset:2048 lds
v_mfma_f32_16x16x32_bf16 a[104:107], v[168:171], v[140:143], a[104:107]
ds_read_b64_tr_b16 v[114:115], v198 offset:1024
v_mfma_f32_16x16x32_bf16 a[108:111], v[172:175], v[140:143], a[108:111]
v_mfma_f32_16x16x32_bf16 a[112:115], v[176:179], v[140:143], a[112:115]
ds_read_b128 v[80:83], v192 offset:8192
v_mfma_f32_16x16x32_bf16 a[116:119], v[180:183], v[140:143], a[116:119]
buffer_load_dwordx4 v205, s[4:7], s41 offen offset:3072 lds
v_mfma_f32_16x16x32_bf16 a[120:123], v[184:187], v[140:143], a[120:123]
ds_read_b64_tr_b16 v[116:117], v199 offset:0
v_mfma_f32_16x16x32_bf16 a[124:127], v[188:191], v[140:143], a[124:127]
v_mfma_f32_16x16x32_bf16 a[128:131], v[160:163], v[144:147], a[128:131]
ds_read_b64_tr_b16 v[118:119], v199 offset:1024
v_mfma_f32_16x16x32_bf16 a[132:135], v[164:167], v[144:147], a[132:135]
buffer_load_dword v35, v206, s[56:59], s48 offen
v_mfma_f32_16x16x32_bf16 a[136:139], v[168:171], v[144:147], a[136:139]
ds_read_b128 v[84:87], v192 offset:10240
v_mfma_f32_16x16x32_bf16 a[140:143], v[172:175], v[144:147], a[140:143]
buffer_load_dword v2, v3, s[60:63], s90 offen sc1
v_mfma_f32_16x16x32_bf16 a[144:147], v[176:179], v[144:147], a[144:147]
s_mov_b32 s42, 1572864
ds_read_b64_tr_b16 v[120:121], v200 offset:0
v_mfma_f32_16x16x32_bf16 a[148:151], v[180:183], v[144:147], a[148:151]
s_mov_b32 s43, 1703936
buffer_load_dword v35, v207, s[12:15], s49 offen
v_mfma_f32_16x16x32_bf16 a[152:155], v[184:187], v[144:147], a[152:155]
ds_read_b64_tr_b16 v[122:123], v200 offset:1024
v_mfma_f32_16x16x32_bf16 a[156:159], v[188:191], v[144:147], a[156:159]
buffer_load_dword v11, v6, s[60:63], 0 offen sc1
v_mfma_f32_16x16x32_bf16 a[160:163], v[160:163], v[148:151], a[160:163]
s_add_u32 s40, s40, 128
ds_read_b128 v[88:91], v192 offset:12288
v_mfma_f32_16x16x32_bf16 a[164:167], v[164:167], v[148:151], a[164:167]
s_add_u32 s41, s41, 128
v_mfma_f32_16x16x32_bf16 a[168:171], v[168:171], v[148:151], a[168:171]
ds_read_b64_tr_b16 v[124:125], v201 offset:0
v_mfma_f32_16x16x32_bf16 a[172:175], v[172:175], v[148:151], a[172:175]
s_add_u32 s48, s48, 0x100000
v_mfma_f32_16x16x32_bf16 a[176:179], v[176:179], v[148:151], a[176:179]
ds_read_b64_tr_b16 v[126:127], v201 offset:1024
v_mfma_f32_16x16x32_bf16 a[180:183], v[180:183], v[148:151], a[180:183]
s_add_u32 s49, s49, 256
v_mfma_f32_16x16x32_bf16 a[184:187], v[184:187], v[148:151], a[184:187]
ds_read_b128 v[92:95], v192 offset:14336
v_mfma_f32_16x16x32_bf16 a[188:191], v[188:191], v[148:151], a[188:191]
buffer_store_dword v45, v44, s[60:63], 0 offen sc1
v_mfma_f32_16x16x32_bf16 a[192:195], v[160:163], v[152:155], a[192:195]
v_mfma_f32_16x16x32_bf16 a[196:199], v[164:167], v[152:155], a[196:199]
v_mfma_f32_16x16x32_bf16 a[200:203], v[168:171], v[152:155], a[200:203]
v_mfma_f32_16x16x32_bf16 a[204:207], v[172:175], v[152:155], a[204:207]
buffer_store_dword v45, v46, s[60:63], 0 offen sc1
v_mfma_f32_16x16x32_bf16 a[208:211], v[176:179], v[152:155], a[208:211]
v_mfma_f32_16x16x32_bf16 a[212:215], v[180:183], v[152:155], a[212:215]
v_mfma_f32_16x16x32_bf16 a[216:219], v[184:187], v[152:155], a[216:219]
v_mfma_f32_16x16x32_bf16 a[220:223], v[188:191], v[152:155], a[220:223]
v_mfma_f32_16x16x32_bf16 a[224:227], v[160:163], v[156:159], a[224:227]
v_mfma_f32_16x16x32_bf16 a[228:231], v[164:167], v[156:159], a[228:231]
s_mov_b32 s37, s36
v_mfma_f32_16x16x32_bf16 a[232:235], v[168:171], v[156:159], a[232:235]
s_add_u32 s35, s35, s45
v_mfma_f32_16x16x32_bf16 a[236:239], v[172:175], v[156:159], a[236:239]
s_add_u32 s36, s36, s34
v_mfma_f32_16x16x32_bf16 a[240:243], v[176:179], v[156:159], a[240:243]
v_mfma_f32_16x16x32_bf16 a[244:247], v[180:183], v[156:159], a[244:247]
v_mfma_f32_16x16x32_bf16 a[248:251], v[184:187], v[156:159], a[248:251]
v_mfma_f32_16x16x32_bf16 a[252:255], v[188:191], v[156:159], a[252:255]
s_waitcnt lgkmcnt(0)
s_nop 0
s_nop 0
.Lp1_kloop:
v_mfma_f32_16x16x32_bf16 a[0:3], v[96:99], v[64:67], a[0:3]
s_mov_b32 m0, s37
ds_read_b64_tr_b16 v[160:161], v194 offset:8192
v_mfma_f32_16x16x32_bf16 a[4:7], v[100:103], v[64:67], a[4:7]
buffer_load_dwordx4 v16, s[8:11], s42 offen offset:0 nt lds
v_mfma_f32_16x16x32_bf16 a[8:11], v[104:107], v[64:67], a[8:11]
ds_read_b64_tr_b16 v[162:163], v194 offset:9216
v_mfma_f32_16x16x32_bf16 a[12:15], v[108:111], v[64:67], a[12:15]
v_mfma_f32_16x16x32_bf16 a[16:19], v[112:115], v[64:67], a[16:19]
ds_read_b128 v[128:131], v193 offset:0
v_mfma_f32_16x16x32_bf16 a[20:23], v[116:119], v[64:67], a[20:23]
buffer_load_dwordx4 v17, s[8:11], s42 offen offset:1024 nt lds
v_mfma_f32_16x16x32_bf16 a[24:27], v[120:123], v[64:67], a[24:27]
ds_read_b64_tr_b16 v[164:165], v195 offset:8192
v_mfma_f32_16x16x32_bf16 a[28:31], v[124:127], v[64:67], a[28:31]
v_mfma_f32_16x16x32_bf16 a[32:35], v[96:99], v[68:71], a[32:35]
ds_read_b64_tr_b16 v[166:167], v195 offset:9216
.Lpad1:
s_cbranch_execz .Ltch0
v_mfma_f32_16x16x32_bf16 a[36:39], v[100:103], v[68:71], a[36:39]
buffer_load_dwordx4 v18, s[8:11], s42 offen offset:2048 nt lds
v_mfma_f32_16x16x32_bf16 a[40:43], v[104:107], v[68:71], a[40:43]
ds_read_b128 v[132:135], v193 offset:2048
v_mfma_f32_16x16x32_bf16 a[44:47], v[108:111], v[68:71], a[44:47]
v_mfma_f32_16x16x32_bf16 a[48:51], v[112:115], v[68:71], a[48:51]
ds_read_b64_tr_b16 v[168:169], v196 offset:8192
v_mfma_f32_16x16x32_bf16 a[52:55], v[116:119], v[68:71], a[52:55]
buffer_load_dwordx4 v19, s[8:11], s42 offen offset:3072 nt lds
v_mfma_f32_16x16x32_bf16 a[56:59], v[120:123], v[68:71], a[56:59]
ds_read_b64_tr_b16 v[170:171], v196 offset:9216
v_mfma_f32_16x16x32_bf16 a[60:63], v[124:127], v[68:71], a[60:63]
v_mfma_f32_16x16x32_bf16 a[64:67], v[96:99], v[72:75], a[64:67]
s_add_u32 m0, s37, 4096
ds_read_b128 v[136:139], v193 offset:4096
v_mfma_f32_16x16x32_bf16 a[68:71], v[100:103], v[72:75], a[68:71]
buffer_load_dwordx4 v16, s[8:11], s43 offen offset:0 nt lds
v_mfma_f32_16x16x32_bf16 a[72:75], v[104:107], v[72:75], a[72:75]
ds_read_b64_tr_b16 v[172:173], v197 offset:8192
v_mfma_f32_16x16x32_bf16 a[76:79], v[108:111], v[72:75], a[76:79]
v_mfma_f32_16x16x32_bf16 a[80:83], v[112:115], v[72:75], a[80:83]
ds_read_b64_tr_b16 v[174:175], v197 offset:9216
v_mfma_f32_16x16x32_bf16 a[84:87], v[116:119], v[72:75], a[84:87]
buffer_load_dwordx4 v17, s[8:11], s43 offen offset:1024 nt lds
v_mfma_f32_16x16x32_bf16 a[88:91], v[120:123], v[72:75], a[88:91]
ds_read_b128 v[140:143], v193 offset:6144
v_mfma_f32_16x16x32_bf16 a[92:95], v[124:127], v[72:75], a[92:95]
v_mfma_f32_16x16x32_bf16 a[96:99], v[96:99], v[76:79], a[96:99]
ds_read_b64_tr_b16 v[176:177], v198 offset:8192
v_mfma_f32_16x16x32_bf16 a[100:103], v[100:103], v[76:79], a[100:103]
buffer_load_dwordx4 v18, s[8:11], s43 offen offset:2048 nt lds
v_mfma_f32_16x16x32_bf16 a[104:107], v[104:107], v[76:79], a[104:107]
ds_read_b64_tr_b16 v[178:179], v198 offset:9216
v_mfma_f32_16x16x32_bf16 a[108:111], v[108:111], v[76:79], a[108:111]
v_mfma_f32_16x16x32_bf16 a[112:115], v[112:115], v[76:79], a[112:115]
ds_read_b128 v[144:147], v193 offset:8192
v_mfma_f32_16x16x32_bf16 a[116:119], v[116:119], v[76:79], a[116:119]
buffer_load_dwordx4 v19, s[8:11], s43 offen offset:3072 nt lds
v_mfma_f32_16x16x32_bf16 a[120:123], v[120:123], v[76:79], a[120:123]
ds_read_b64_tr_b16 v[180:181], v199 offset:8192
v_mfma_f32_16x16x32_bf16 a[124:127], v[124:127], v[76:79], a[124:127]
v_mfma_f32_16x16x32_bf16 a[128:131], v[96:99], v[80:83], a[128:131]
ds_read_b64_tr_b16 v[182:183], v199 offset:9216
v_mfma_f32_16x16x32_bf16 a[132:135], v[100:103], v[80:83], a[132:135]
v_mfma_f32_16x16x32_bf16 a[136:139], v[104:107], v[80:83], a[136:139]
ds_read_b128 v[148:151], v193 offset:10240
v_mfma_f32_16x16x32_bf16 a[140:143], v[108:111], v[80:83], a[140:143]
v_mfma_f32_16x16x32_bf16 a[144:147], v[112:115], v[80:83], a[144:147]
ds_read_b64_tr_b16 v[184:185], v200 offset:8192
v_mfma_f32_16x16x32_bf16 a[148:151], v[116:119], v[80:83], a[148:151]
v_mfma_f32_16x16x32_bf16 a[152:155], v[120:123], v[80:83], a[152:155]
ds_read_b64_tr_b16 v[186:187], v200 offset:9216
v_mfma_f32_16x16x32_bf16 a[156:159], v[124:127], v[80:83], a[156:159]
v_mfma_f32_16x16x32_bf16 a[160:163], v[96:99], v[84:87], a[160:163]
s_add_u32 s42, s42, 524288
ds_read_b128 v[152:155], v193 offset:12288
v_mfma_f32_16x16x32_bf16 a[164:167], v[100:103], v[84:87], a[164:167]
s_add_u32 s43, s43, 524288
v_mfma_f32_16x16x32_bf16 a[168:171], v[104:107], v[84:87], a[168:171]
ds_read_b64_tr_b16 v[188:189], v201 offset:8192
v_mfma_f32_16x16x32_bf16 a[172:175], v[108:111], v[84:87], a[172:175]
v_mfma_f32_16x16x32_bf16 a[176:179], v[112:115], v[84:87], a[176:179]
s_add_u32 s44, s35, 65536
ds_read_b64_tr_b16 v[190:191], v201 offset:9216
v_mfma_f32_16x16x32_bf16 a[180:183], v[116:119], v[84:87], a[180:183]
s_cmp_ge_u32 s44, s39
s_cselect_b32 s45, s55, s54
v_mfma_f32_16x16x32_bf16 a[184:187], v[120:123], v[84:87], a[184:187]
ds_read_b128 v[156:159], v193 offset:14336
v_mfma_f32_16x16x32_bf16 a[188:191], v[124:127], v[84:87], a[188:191]
s_add_u32 s44, s36, 65536
v_mfma_f32_16x16x32_bf16 a[192:195], v[96:99], v[88:91], a[192:195]
v_mfma_f32_16x16x32_bf16 a[196:199], v[100:103], v[88:91], a[196:199]
s_cmp_ge_u32 s44, s39
s_cselect_b32 s34, s55, s54
v_mfma_f32_16x16x32_bf16 a[200:203], v[104:107], v[88:91], a[200:203]
v_add_u32 v194, s45, v194
v_mfma_f32_16x16x32_bf16 a[204:207], v[108:111], v[88:91], a[204:207]
v_add_u32 v195, s45, v195
v_mfma_f32_16x16x32_bf16 a[208:211], v[112:115], v[88:91], a[208:211]
v_add_u32 v196, s45, v196
v_mfma_f32_16x16x32_bf16 a[212:215], v[116:119], v[88:91], a[212:215]
v_add_u32 v197, s45, v197
v_mfma_f32_16x16x32_bf16 a[216:219], v[120:123], v[88:91], a[216:219]
v_add_u32 v198, s45, v198
v_mfma_f32_16x16x32_bf16 a[220:223], v[124:127], v[88:91], a[220:223]
v_add_u32 v199, s45, v199
v_mfma_f32_16x16x32_bf16 a[224:227], v[96:99], v[92:95], a[224:227]
v_add_u32 v200, s45, v200
v_mfma_f32_16x16x32_bf16 a[228:231], v[100:103], v[92:95], a[228:231]
v_add_u32 v201, s45, v201
v_mfma_f32_16x16x32_bf16 a[232:235], v[104:107], v[92:95], a[232:235]
v_add_u32 v192, s34, v192
v_mfma_f32_16x16x32_bf16 a[236:239], v[108:111], v[92:95], a[236:239]
v_add_u32 v193, s34, v193
v_mfma_f32_16x16x32_bf16 a[240:243], v[112:115], v[92:95], a[240:243]
v_mfma_f32_16x16x32_bf16 a[244:247], v[116:119], v[92:95], a[244:247]
v_mfma_f32_16x16x32_bf16 a[248:251], v[120:123], v[92:95], a[248:251]
v_mfma_f32_16x16x32_bf16 a[252:255], v[124:127], v[92:95], a[252:255]
s_waitcnt vmcnt(10) lgkmcnt(0)
s_barrier
v_mfma_f32_16x16x32_bf16 a[0:3], v[160:163], v[128:131], a[0:3]
s_mov_b32 m0, s35
ds_read_b64_tr_b16 v[96:97], v194 offset:0
v_mfma_f32_16x16x32_bf16 a[4:7], v[164:167], v[128:131], a[4:7]
buffer_load_dwordx4 v202, s[4:7], s40 offen offset:0 lds
v_mfma_f32_16x16x32_bf16 a[8:11], v[168:171], v[128:131], a[8:11]
ds_read_b64_tr_b16 v[98:99], v194 offset:1024
v_mfma_f32_16x16x32_bf16 a[12:15], v[172:175], v[128:131], a[12:15]
v_mfma_f32_16x16x32_bf16 a[16:19], v[176:179], v[128:131], a[16:19]
ds_read_b128 v[64:67], v192 offset:0
v_mfma_f32_16x16x32_bf16 a[20:23], v[180:183], v[128:131], a[20:23]
buffer_load_dwordx4 v203, s[4:7], s40 offen offset:1024 lds
v_mfma_f32_16x16x32_bf16 a[24:27], v[184:187], v[128:131], a[24:27]
ds_read_b64_tr_b16 v[100:101], v195 offset:0
v_mfma_f32_16x16x32_bf16 a[28:31], v[188:191], v[128:131], a[28:31]
v_mfma_f32_16x16x32_bf16 a[32:35], v[160:163], v[132:135], a[32:35]
ds_read_b64_tr_b16 v[102:103], v195 offset:1024
v_mfma_f32_16x16x32_bf16 a[36:39], v[164:167], v[132:135], a[36:39]
buffer_load_dwordx4 v204, s[4:7], s40 offen offset:2048 lds
v_mfma_f32_16x16x32_bf16 a[40:43], v[168:171], v[132:135], a[40:43]
ds_read_b128 v[68:71], v192 offset:2048
v_mfma_f32_16x16x32_bf16 a[44:47], v[172:175], v[132:135], a[44:47]
v_mfma_f32_16x16x32_bf16 a[48:51], v[176:179], v[132:135], a[48:51]
ds_read_b64_tr_b16 v[104:105], v196 offset:0
v_mfma_f32_16x16x32_bf16 a[52:55], v[180:183], v[132:135], a[52:55]
buffer_load_dwordx4 v205, s[4:7], s40 offen offset:3072 lds
v_mfma_f32_16x16x32_bf16 a[56:59], v[184:187], v[132:135], a[56:59]
ds_read_b64_tr_b16 v[106:107], v196 offset:1024
v_mfma_f32_16x16x32_bf16 a[60:63], v[188:191], v[132:135], a[60:63]
v_mfma_f32_16x16x32_bf16 a[64:67], v[160:163], v[136:139], a[64:67]
s_add_u32 m0, s35, 4096
ds_read_b128 v[72:75], v192 offset:4096
v_mfma_f32_16x16x32_bf16 a[68:71], v[164:167], v[136:139], a[68:71]
buffer_load_dwordx4 v202, s[4:7], s41 offen offset:0 lds
v_mfma_f32_16x16x32_bf16 a[72:75], v[168:171], v[136:139], a[72:75]
ds_read_b64_tr_b16 v[108:109], v197 offset:0
v_mfma_f32_16x16x32_bf16 a[76:79], v[172:175], v[136:139], a[76:79]
v_mfma_f32_16x16x32_bf16 a[80:83], v[176:179], v[136:139], a[80:83]
ds_read_b64_tr_b16 v[110:111], v197 offset:1024
v_mfma_f32_16x16x32_bf16 a[84:87], v[180:183], v[136:139], a[84:87]
buffer_load_dwordx4 v203, s[4:7], s41 offen offset:1024 lds
v_mfma_f32_16x16x32_bf16 a[88:91], v[184:187], v[136:139], a[88:91]
ds_read_b128 v[76:79], v192 offset:6144
v_mfma_f32_16x16x32_bf16 a[92:95], v[188:191], v[136:139], a[92:95]
v_mfma_f32_16x16x32_bf16 a[96:99], v[160:163], v[140:143], a[96:99]
ds_read_b64_tr_b16 v[112:113], v198 offset:0
v_mfma_f32_16x16x32_bf16 a[100:103], v[164:167], v[140:143], a[100:103]
buffer_load_dwordx4 v204, s[4:7], s41 offen offset:2048 lds
v_mfma_f32_16x16x32_bf16 a[104:107], v[168:171], v[140:143], a[104:107]
ds_read_b64_tr_b16 v[114:115], v198 offset:1024
v_mfma_f32_16x16x32_bf16 a[108:111], v[172:175], v[140:143], a[108:111]
v_mfma_f32_16x16x32_bf16 a[112:115], v[176:179], v[140:143], a[112:115]
ds_read_b128 v[80:83], v192 offset:8192
v_mfma_f32_16x16x32_bf16 a[116:119], v[180:183], v[140:143], a[116:119]
buffer_load_dwordx4 v205, s[4:7], s41 offen offset:3072 lds
v_mfma_f32_16x16x32_bf16 a[120:123], v[184:187], v[140:143], a[120:123]
ds_read_b64_tr_b16 v[116:117], v199 offset:0
v_mfma_f32_16x16x32_bf16 a[124:127], v[188:191], v[140:143], a[124:127]
v_mfma_f32_16x16x32_bf16 a[128:131], v[160:163], v[144:147], a[128:131]
ds_read_b64_tr_b16 v[118:119], v199 offset:1024
v_mfma_f32_16x16x32_bf16 a[132:135], v[164:167], v[144:147], a[132:135]
buffer_load_dword v35, v206, s[56:59], s48 offen
v_mfma_f32_16x16x32_bf16 a[136:139], v[168:171], v[144:147], a[136:139]
ds_read_b128 v[84:87], v192 offset:10240
v_mfma_f32_16x16x32_bf16 a[140:143], v[172:175], v[144:147], a[140:143]
v_mfma_f32_16x16x32_bf16 a[144:147], v[176:179], v[144:147], a[144:147]
ds_read_b64_tr_b16 v[120:121], v200 offset:0
v_mfma_f32_16x16x32_bf16 a[148:151], v[180:183], v[144:147], a[148:151]
buffer_load_dword v35, v207, s[12:15], s49 offen
v_mfma_f32_16x16x32_bf16 a[152:155], v[184:187], v[144:147], a[152:155]
ds_read_b64_tr_b16 v[122:123], v200 offset:1024
v_mfma_f32_16x16x32_bf16 a[156:159], v[188:191], v[144:147], a[156:159]
v_mfma_f32_16x16x32_bf16 a[160:163], v[160:163], v[148:151], a[160:163]
s_add_u32 s40, s40, 128
ds_read_b128 v[88:91], v192 offset:12288
v_mfma_f32_16x16x32_bf16 a[164:167], v[164:167], v[148:151], a[164:167]
s_add_u32 s41, s41, 128
v_mfma_f32_16x16x32_bf16 a[168:171], v[168:171], v[148:151], a[168:171]
ds_read_b64_tr_b16 v[124:125], v201 offset:0
v_mfma_f32_16x16x32_bf16 a[172:175], v[172:175], v[148:151], a[172:175]
s_add_u32 s48, s48, 0x100000
v_mfma_f32_16x16x32_bf16 a[176:179], v[176:179], v[148:151], a[176:179]
ds_read_b64_tr_b16 v[126:127], v201 offset:1024
v_mfma_f32_16x16x32_bf16 a[180:183], v[180:183], v[148:151], a[180:183]
s_add_u32 s49, s49, 256
v_mfma_f32_16x16x32_bf16 a[184:187], v[184:187], v[148:151], a[184:187]
ds_read_b128 v[92:95], v192 offset:14336
v_mfma_f32_16x16x32_bf16 a[188:191], v[188:191], v[148:151], a[188:191]
v_mfma_f32_16x16x32_bf16 a[192:195], v[160:163], v[152:155], a[192:195]
v_mfma_f32_16x16x32_bf16 a[196:199], v[164:167], v[152:155], a[196:199]
v_mfma_f32_16x16x32_bf16 a[200:203], v[168:171], v[152:155], a[200:203]
v_mfma_f32_16x16x32_bf16 a[204:207], v[172:175], v[152:155], a[204:207]
v_mfma_f32_16x16x32_bf16 a[208:211], v[176:179], v[152:155], a[208:211]
v_mfma_f32_16x16x32_bf16 a[212:215], v[180:183], v[152:155], a[212:215]
v_mfma_f32_16x16x32_bf16 a[216:219], v[184:187], v[152:155], a[216:219]
v_mfma_f32_16x16x32_bf16 a[220:223], v[188:191], v[152:155], a[220:223]
v_mfma_f32_16x16x32_bf16 a[224:227], v[160:163], v[156:159], a[224:227]
v_mfma_f32_16x16x32_bf16 a[228:231], v[164:167], v[156:159], a[228:231]
s_mov_b32 s37, s36
v_mfma_f32_16x16x32_bf16 a[232:235], v[168:171], v[156:159], a[232:235]
s_add_u32 s35, s35, s45
v_mfma_f32_16x16x32_bf16 a[236:239], v[172:175], v[156:159], a[236:239]
s_add_u32 s36, s36, s34
v_mfma_f32_16x16x32_bf16 a[240:243], v[176:179], v[156:159], a[240:243]
s_add_u32 s38, s38, 1
v_mfma_f32_16x16x32_bf16 a[244:247], v[180:183], v[156:159], a[244:247]
s_cmp_lt_u32 s38, 6
v_mfma_f32_16x16x32_bf16 a[248:251], v[184:187], v[156:159], a[248:251]
v_mfma_f32_16x16x32_bf16 a[252:255], v[188:191], v[156:159], a[252:255]
s_waitcnt lgkmcnt(0)
s_cbranch_scc1 .Lp1_kloop
v_mfma_f32_16x16x32_bf16 a[0:3], v[96:99], v[64:67], a[0:3]
s_add_u32 m0, s92, 32768
ds_read_b64_tr_b16 v[160:161], v194 offset:8192
v_mfma_f32_16x16x32_bf16 a[4:7], v[100:103], v[64:67], a[4:7]
buffer_load_dwordx4 v20, s[12:15], s24 offen offset:0 lds
v_mfma_f32_16x16x32_bf16 a[8:11], v[104:107], v[64:67], a[8:11]
ds_read_b64_tr_b16 v[162:163], v194 offset:9216
v_mfma_f32_16x16x32_bf16 a[12:15], v[108:111], v[64:67], a[12:15]
v_mfma_f32_16x16x32_bf16 a[16:19], v[112:115], v[64:67], a[16:19]
ds_read_b128 v[128:131], v193 offset:0
v_mfma_f32_16x16x32_bf16 a[20:23], v[116:119], v[64:67], a[20:23]
buffer_load_dwordx4 v21, s[12:15], s24 offen offset:1024 lds
v_mfma_f32_16x16x32_bf16 a[24:27], v[120:123], v[64:67], a[24:27]
ds_read_b64_tr_b16 v[164:165], v195 offset:8192
v_mfma_f32_16x16x32_bf16 a[28:31], v[124:127], v[64:67], a[28:31]
v_mfma_f32_16x16x32_bf16 a[32:35], v[96:99], v[68:71], a[32:35]
ds_read_b64_tr_b16 v[166:167], v195 offset:9216
v_mfma_f32_16x16x32_bf16 a[36:39], v[100:103], v[68:71], a[36:39]
buffer_load_dwordx4 v22, s[12:15], s24 offen offset:2048 lds
v_mfma_f32_16x16x32_bf16 a[40:43], v[104:107], v[68:71], a[40:43]
ds_read_b128 v[132:135], v193 offset:2048
v_mfma_f32_16x16x32_bf16 a[44:47], v[108:111], v[68:71], a[44:47]
v_mfma_f32_16x16x32_bf16 a[48:51], v[112:115], v[68:71], a[48:51]
ds_read_b64_tr_b16 v[168:169], v196 offset:8192
v_mfma_f32_16x16x32_bf16 a[52:55], v[116:119], v[68:71], a[52:55]
buffer_load_dwordx4 v23, s[12:15], s24 offen offset:3072 lds
v_mfma_f32_16x16x32_bf16 a[56:59], v[120:123], v[68:71], a[56:59]
ds_read_b64_tr_b16 v[170:171], v196 offset:9216
v_mfma_f32_16x16x32_bf16 a[60:63], v[124:127], v[68:71], a[60:63]
v_mfma_f32_16x16x32_bf16 a[64:67], v[96:99], v[72:75], a[64:67]
s_add_u32 m0, s92, 36864
ds_read_b128 v[136:139], v193 offset:4096
v_mfma_f32_16x16x32_bf16 a[68:71], v[100:103], v[72:75], a[68:71]
buffer_load_dwordx4 v20, s[12:15], s25 offen offset:0 lds
v_mfma_f32_16x16x32_bf16 a[72:75], v[104:107], v[72:75], a[72:75]
ds_read_b64_tr_b16 v[172:173], v197 offset:8192
v_mfma_f32_16x16x32_bf16 a[76:79], v[108:111], v[72:75], a[76:79]
v_mfma_f32_16x16x32_bf16 a[80:83], v[112:115], v[72:75], a[80:83]
ds_read_b64_tr_b16 v[174:175], v197 offset:9216
v_mfma_f32_16x16x32_bf16 a[84:87], v[116:119], v[72:75], a[84:87]
buffer_load_dwordx4 v21, s[12:15], s25 offen offset:1024 lds
v_mfma_f32_16x16x32_bf16 a[88:91], v[120:123], v[72:75], a[88:91]
ds_read_b128 v[140:143], v193 offset:6144
v_mfma_f32_16x16x32_bf16 a[92:95], v[124:127], v[72:75], a[92:95]
v_mfma_f32_16x16x32_bf16 a[96:99], v[96:99], v[76:79], a[96:99]
ds_read_b64_tr_b16 v[176:177], v198 offset:8192
v_mfma_f32_16x16x32_bf16 a[100:103], v[100:103], v[76:79], a[100:103]
buffer_load_dwordx4 v22, s[12:15], s25 offen offset:2048 lds
v_mfma_f32_16x16x32_bf16 a[104:107], v[104:107], v[76:79], a[104:107]
ds_read_b64_tr_b16 v[178:179], v198 offset:9216
v_mfma_f32_16x16x32_bf16 a[108:111], v[108:111], v[76:79], a[108:111]
v_mfma_f32_16x16x32_bf16 a[112:115], v[112:115], v[76:79], a[112:115]
ds_read_b128 v[144:147], v193 offset:8192
v_mfma_f32_16x16x32_bf16 a[116:119], v[116:119], v[76:79], a[116:119]
buffer_load_dwordx4 v23, s[12:15], s25 offen offset:3072 lds
v_mfma_f32_16x16x32_bf16 a[120:123], v[120:123], v[76:79], a[120:123]
ds_read_b64_tr_b16 v[180:181], v199 offset:8192
v_mfma_f32_16x16x32_bf16 a[124:127], v[124:127], v[76:79], a[124:127]
v_mfma_f32_16x16x32_bf16 a[128:131], v[96:99], v[80:83], a[128:131]
ds_read_b64_tr_b16 v[182:183], v199 offset:9216
v_mfma_f32_16x16x32_bf16 a[132:135], v[100:103], v[80:83], a[132:135]
v_mfma_f32_16x16x32_bf16 a[136:139], v[104:107], v[80:83], a[136:139]
ds_read_b128 v[148:151], v193 offset:10240
v_mfma_f32_16x16x32_bf16 a[140:143], v[108:111], v[80:83], a[140:143]
v_mfma_f32_16x16x32_bf16 a[144:147], v[112:115], v[80:83], a[144:147]
ds_read_b64_tr_b16 v[184:185], v200 offset:8192
v_mfma_f32_16x16x32_bf16 a[148:151], v[116:119], v[80:83], a[148:151]
v_mfma_f32_16x16x32_bf16 a[152:155], v[120:123], v[80:83], a[152:155]
ds_read_b64_tr_b16 v[186:187], v200 offset:9216
v_mfma_f32_16x16x32_bf16 a[156:159], v[124:127], v[80:83], a[156:159]
v_mfma_f32_16x16x32_bf16 a[160:163], v[96:99], v[84:87], a[160:163]
s_add_u32 s24, s24, 128
s_add_u32 s25, s25, 128
ds_read_b128 v[152:155], v193 offset:12288
v_mfma_f32_16x16x32_bf16 a[164:167], v[100:103], v[84:87], a[164:167]
v_mfma_f32_16x16x32_bf16 a[168:171], v[104:107], v[84:87], a[168:171]
ds_read_b64_tr_b16 v[188:189], v201 offset:8192
v_mfma_f32_16x16x32_bf16 a[172:175], v[108:111], v[84:87], a[172:175]
v_mfma_f32_16x16x32_bf16 a[176:179], v[112:115], v[84:87], a[176:179]
s_add_u32 s44, s35, 65536
ds_read_b64_tr_b16 v[190:191], v201 offset:9216
v_mfma_f32_16x16x32_bf16 a[180:183], v[116:119], v[84:87], a[180:183]
s_cmp_ge_u32 s44, s39
s_cselect_b32 s45, s55, s54
v_mfma_f32_16x16x32_bf16 a[184:187], v[120:123], v[84:87], a[184:187]
ds_read_b128 v[156:159], v193 offset:14336
v_mfma_f32_16x16x32_bf16 a[188:191], v[124:127], v[84:87], a[188:191]
s_add_u32 s44, s36, 65536
v_mfma_f32_16x16x32_bf16 a[192:195], v[96:99], v[88:91], a[192:195]
v_mfma_f32_16x16x32_bf16 a[196:199], v[100:103], v[88:91], a[196:199]
s_cmp_ge_u32 s44, s39
s_cselect_b32 s34, s55, s54
v_mfma_f32_16x16x32_bf16 a[200:203], v[104:107], v[88:91], a[200:203]
v_add_u32 v194, s45, v194
v_mfma_f32_16x16x32_bf16 a[204:207], v[108:111], v[88:91], a[204:207]
v_add_u32 v195, s45, v195
v_mfma_f32_16x16x32_bf16 a[208:211], v[112:115], v[88:91], a[208:211]
v_add_u32 v196, s45, v196
v_mfma_f32_16x16x32_bf16 a[212:215], v[116:119], v[88:91], a[212:215]
v_add_u32 v197, s45, v197
v_mfma_f32_16x16x32_bf16 a[216:219], v[120:123], v[88:91], a[216:219]
v_add_u32 v198, s45, v198
v_mfma_f32_16x16x32_bf16 a[220:223], v[124:127], v[88:91], a[220:223]
v_add_u32 v199, s45, v199
v_mfma_f32_16x16x32_bf16 a[224:227], v[96:99], v[92:95], a[224:227]
v_add_u32 v200, s45, v200
v_mfma_f32_16x16x32_bf16 a[228:231], v[100:103], v[92:95], a[228:231]
v_add_u32 v201, s45, v201
v_mfma_f32_16x16x32_bf16 a[232:235], v[104:107], v[92:95], a[232:235]
v_add_u32 v192, s34, v192
v_mfma_f32_16x16x32_bf16 a[236:239], v[108:111], v[92:95], a[236:239]
v_add_u32 v193, s34, v193
v_mfma_f32_16x16x32_bf16 a[240:243], v[112:115], v[92:95], a[240:243]
v_mfma_f32_16x16x32_bf16 a[244:247], v[116:119], v[92:95], a[244:247]
v_mfma_f32_16x16x32_bf16 a[248:251], v[120:123], v[92:95], a[248:251]
v_mfma_f32_16x16x32_bf16 a[252:255], v[124:127], v[92:95], a[252:255]
s_waitcnt vmcnt(10) lgkmcnt(0)
s_barrier
v_mfma_f32_16x16x32_bf16 a[0:3], v[160:163], v[128:131], a[0:3]
s_add_u32 m0, s91, 65536
ds_read_b64_tr_b16 v[96:97], v194 offset:0
v_mfma_f32_16x16x32_bf16 a[4:7], v[164:167], v[128:131], a[4:7]
buffer_load_dwordx4 v16, s[16:19], s26 offen offset:0 lds
v_mfma_f32_16x16x32_bf16 a[8:11], v[168:171], v[128:131], a[8:11]
ds_read_b64_tr_b16 v[98:99], v194 offset:1024
v_mfma_f32_16x16x32_bf16 a[12:15], v[172:175], v[128:131], a[12:15]
v_mfma_f32_16x16x32_bf16 a[16:19], v[176:179], v[128:131], a[16:19]
ds_read_b128 v[64:67], v192 offset:0
v_mfma_f32_16x16x32_bf16 a[20:23], v[180:183], v[128:131], a[20:23]
buffer_load_dwordx4 v17, s[16:19], s26 offen offset:1024 lds
v_mfma_f32_16x16x32_bf16 a[24:27], v[184:187], v[128:131], a[24:27]
ds_read_b64_tr_b16 v[100:101], v195 offset:0
v_mfma_f32_16x16x32_bf16 a[28:31], v[188:191], v[128:131], a[28:31]
v_mfma_f32_16x16x32_bf16 a[32:35], v[160:163], v[132:135], a[32:35]
ds_read_b64_tr_b16 v[102:103], v195 offset:1024
v_mfma_f32_16x16x32_bf16 a[36:39], v[164:167], v[132:135], a[36:39]
buffer_load_dwordx4 v18, s[16:19], s26 offen offset:2048 lds
v_mfma_f32_16x16x32_bf16 a[40:43], v[168:171], v[132:135], a[40:43]
ds_read_b128 v[68:71], v192 offset:2048
v_mfma_f32_16x16x32_bf16 a[44:47], v[172:175], v[132:135], a[44:47]
v_mfma_f32_16x16x32_bf16 a[48:51], v[176:179], v[132:135], a[48:51]
ds_read_b64_tr_b16 v[104:105], v196 offset:0
v_mfma_f32_16x16x32_bf16 a[52:55], v[180:183], v[132:135], a[52:55]
buffer_load_dwordx4 v19, s[16:19], s26 offen offset:3072 lds
v_mfma_f32_16x16x32_bf16 a[56:59], v[184:187], v[132:135], a[56:59]
ds_read_b64_tr_b16 v[106:107], v196 offset:1024
v_mfma_f32_16x16x32_bf16 a[60:63], v[188:191], v[132:135], a[60:63]
s_add_u32 s26, s26, 524288
v_mfma_f32_16x16x32_bf16 a[64:67], v[160:163], v[136:139], a[64:67]
s_add_u32 m0, s91, 81920
ds_read_b128 v[72:75], v192 offset:4096
v_mfma_f32_16x16x32_bf16 a[68:71], v[164:167], v[136:139], a[68:71]
buffer_load_dwordx4 v16, s[16:19], s26 offen offset:0 lds
v_mfma_f32_16x16x32_bf16 a[72:75], v[168:171], v[136:139], a[72:75]
ds_read_b64_tr_b16 v[108:109], v197 offset:0
v_mfma_f32_16x16x32_bf16 a[76:79], v[172:175], v[136:139], a[76:79]
v_mfma_f32_16x16x32_bf16 a[80:83], v[176:179], v[136:139], a[80:83]
ds_read_b64_tr_b16 v[110:111], v197 offset:1024
v_mfma_f32_16x16x32_bf16 a[84:87], v[180:183], v[136:139], a[84:87]
buffer_load_dwordx4 v17, s[16:19], s26 offen offset:1024 lds
v_mfma_f32_16x16x32_bf16 a[88:91], v[184:187], v[136:139], a[88:91]
ds_read_b128 v[76:79], v192 offset:6144
v_mfma_f32_16x16x32_bf16 a[92:95], v[188:191], v[136:139], a[92:95]
v_mfma_f32_16x16x32_bf16 a[96:99], v[160:163], v[140:143], a[96:99]
ds_read_b64_tr_b16 v[112:113], v198 offset:0
v_mfma_f32_16x16x32_bf16 a[100:103], v[164:167], v[140:143], a[100:103]
buffer_load_dwordx4 v18, s[16:19], s26 offen offset:2048 lds
v_mfma_f32_16x16x32_bf16 a[104:107], v[168:171], v[140:143], a[104:107]
ds_read_b64_tr_b16 v[114:115], v198 offset:1024
v_mfma_f32_16x16x32_bf16 a[108:111], v[172:175], v[140:143], a[108:111]
v_mfma_f32_16x16x32_bf16 a[112:115], v[176:179], v[140:143], a[112:115]
ds_read_b128 v[80:83], v192 offset:8192
v_mfma_f32_16x16x32_bf16 a[116:119], v[180:183], v[140:143], a[116:119]
buffer_load_dwordx4 v19, s[16:19], s26 offen offset:3072 lds
v_mfma_f32_16x16x32_bf16 a[120:123], v[184:187], v[140:143], a[120:123]
ds_read_b64_tr_b16 v[116:117], v199 offset:0
v_mfma_f32_16x16x32_bf16 a[124:127], v[188:191], v[140:143], a[124:127]
s_add_u32 s26, s26, 524288
v_mfma_f32_16x16x32_bf16 a[128:131], v[160:163], v[144:147], a[128:131]
ds_read_b64_tr_b16 v[118:119], v199 offset:1024
v_mfma_f32_16x16x32_bf16 a[132:135], v[164:167], v[144:147], a[132:135]
buffer_load_dword v35, v206, s[56:59], s48 offen
v_mfma_f32_16x16x32_bf16 a[136:139], v[168:171], v[144:147], a[136:139]
ds_read_b128 v[84:87], v192 offset:10240
v_mfma_f32_16x16x32_bf16 a[140:143], v[172:175], v[144:147], a[140:143]
v_mfma_f32_16x16x32_bf16 a[144:147], v[176:179], v[144:147], a[144:147]
ds_read_b64_tr_b16 v[120:121], v200 offset:0
v_mfma_f32_16x16x32_bf16 a[148:151], v[180:183], v[144:147], a[148:151]
buffer_load_dword v35, v207, s[12:15], s49 offen
v_add_u32 v11, 8, v11
v_and_b32 v11, -8, v11
v_mfma_f32_16x16x32_bf16 a[152:155], v[184:187], v[144:147], a[152:155]
ds_read_b64_tr_b16 v[122:123], v200 offset:1024
v_mfma_f32_16x16x32_bf16 a[156:159], v[188:191], v[144:147], a[156:159]
v_mfma_f32_16x16x32_bf16 a[160:163], v[160:163], v[148:151], a[160:163]
ds_read_b128 v[88:91], v192 offset:12288
v_mfma_f32_16x16x32_bf16 a[164:167], v[164:167], v[148:151], a[164:167]
s_nop 0
v_or_b32 v0, 0, v11
v_mfma_f32_16x16x32_bf16 a[168:171], v[168:171], v[148:151], a[168:171]
ds_read_b64_tr_b16 v[124:125], v201 offset:0
v_mfma_f32_16x16x32_bf16 a[172:175], v[172:175], v[148:151], a[172:175]
s_add_u32 s48, s48, 0x100000
v_mfma_f32_16x16x32_bf16 a[176:179], v[176:179], v[148:151], a[176:179]
ds_read_b64_tr_b16 v[126:127], v201 offset:1024
v_mfma_f32_16x16x32_bf16 a[180:183], v[180:183], v[148:151], a[180:183]
s_add_u32 s49, s49, 256
v_mfma_f32_16x16x32_bf16 a[184:187], v[184:187], v[148:151], a[184:187]
ds_read_b128 v[92:95], v192 offset:14336
v_mfma_f32_16x16x32_bf16 a[188:191], v[188:191], v[148:151], a[188:191]
buffer_store_dword v0, v6, s[60:63], 0 offen sc1
v_mfma_f32_16x16x32_bf16 a[192:195], v[160:163], v[152:155], a[192:195]
v_mfma_f32_16x16x32_bf16 a[196:199], v[164:167], v[152:155], a[196:199]
v_mfma_f32_16x16x32_bf16 a[200:203], v[168:171], v[152:155], a[200:203]
v_mfma_f32_16x16x32_bf16 a[204:207], v[172:175], v[152:155], a[204:207]
buffer_load_dword v35, v4, s[96:99], 0 offen
v_mfma_f32_16x16x32_bf16 a[208:211], v[176:179], v[152:155], a[208:211]
v_mfma_f32_16x16x32_bf16 a[212:215], v[180:183], v[152:155], a[212:215]
v_mfma_f32_16x16x32_bf16 a[216:219], v[184:187], v[152:155], a[216:219]
v_mfma_f32_16x16x32_bf16 a[220:223], v[188:191], v[152:155], a[220:223]
v_mfma_f32_16x16x32_bf16 a[224:227], v[160:163], v[156:159], a[224:227]
v_mfma_f32_16x16x32_bf16 a[228:231], v[164:167], v[156:159], a[228:231]
s_mov_b32 s37, s36
v_mfma_f32_16x16x32_bf16 a[232:235], v[168:171], v[156:159], a[232:235]
s_add_u32 s35, s35, s45
v_mfma_f32_16x16x32_bf16 a[236:239], v[172:175], v[156:159], a[236:239]
s_add_u32 s36, s36, s34
v_mfma_f32_16x16x32_bf16 a[240:243], v[176:179], v[156:159], a[240:243]
v_mfma_f32_16x16x32_bf16 a[244:247], v[180:183], v[156:159], a[244:247]
v_mfma_f32_16x16x32_bf16 a[248:251], v[184:187], v[156:159], a[248:251]
v_mfma_f32_16x16x32_bf16 a[252:255], v[188:191], v[156:159], a[252:255]
s_waitcnt lgkmcnt(0)
v_mfma_f32_16x16x32_bf16 a[0:3], v[96:99], v[64:67], a[0:3]
s_add_u32 m0, s92, 98304
ds_read_b64_tr_b16 v[160:161], v194 offset:8192
v_mfma_f32_16x16x32_bf16 a[4:7], v[100:103], v[64:67], a[4:7]
buffer_load_dwordx4 v20, s[12:15], s24 offen offset:0 lds
v_mfma_f32_16x16x32_bf16 a[8:11], v[104:107], v[64:67], a[8:11]
ds_read_b64_tr_b16 v[162:163], v194 offset:9216
v_mfma_f32_16x16x32_bf16 a[12:15], v[108:111], v[64:67], a[12:15]
v_mfma_f32_16x16x32_bf16 a[16:19], v[112:115], v[64:67], a[16:19]
ds_read_b128 v[128:131], v193 offset:0
v_mfma_f32_16x16x32_bf16 a[20:23], v[116:119], v[64:67], a[20:23]
buffer_load_dwordx4 v21, s[12:15], s24 offen offset:1024 lds
v_mfma_f32_16x16x32_bf16 a[24:27], v[120:123], v[64:67], a[24:27]
ds_read_b64_tr_b16 v[164:165], v195 offset:8192
v_mfma_f32_16x16x32_bf16 a[28:31], v[124:127], v[64:67], a[28:31]
v_mfma_f32_16x16x32_bf16 a[32:35], v[96:99], v[68:71], a[32:35]
ds_read_b64_tr_b16 v[166:167], v195 offset:9216
v_mfma_f32_16x16x32_bf16 a[36:39], v[100:103], v[68:71], a[36:39]
buffer_load_dwordx4 v22, s[12:15], s24 offen offset:2048 lds
v_mfma_f32_16x16x32_bf16 a[40:43], v[104:107], v[68:71], a[40:43]
ds_read_b128 v[132:135], v193 offset:2048
v_mfma_f32_16x16x32_bf16 a[44:47], v[108:111], v[68:71], a[44:47]
v_mfma_f32_16x16x32_bf16 a[48:51], v[112:115], v[68:71], a[48:51]
ds_read_b64_tr_b16 v[168:169], v196 offset:8192
v_mfma_f32_16x16x32_bf16 a[52:55], v[116:119], v[68:71], a[52:55]
buffer_load_dwordx4 v23, s[12:15], s24 offen offset:3072 lds
v_mfma_f32_16x16x32_bf16 a[56:59], v[120:123], v[68:71], a[56:59]
ds_read_b64_tr_b16 v[170:171], v196 offset:9216
v_mfma_f32_16x16x32_bf16 a[60:63], v[124:127], v[68:71], a[60:63]
v_mfma_f32_16x16x32_bf16 a[64:67], v[96:99], v[72:75], a[64:67]
s_add_u32 m0, s92, 102400
ds_read_b128 v[136:139], v193 offset:4096
v_mfma_f32_16x16x32_bf16 a[68:71], v[100:103], v[72:75], a[68:71]
buffer_load_dwordx4 v20, s[12:15], s25 offen offset:0 lds
v_mfma_f32_16x16x32_bf16 a[72:75], v[104:107], v[72:75], a[72:75]
ds_read_b64_tr_b16 v[172:173], v197 offset:8192
v_mfma_f32_16x16x32_bf16 a[76:79], v[108:111], v[72:75], a[76:79]
v_mfma_f32_16x16x32_bf16 a[80:83], v[112:115], v[72:75], a[80:83]
ds_read_b64_tr_b16 v[174:175], v197 offset:9216
v_mfma_f32_16x16x32_bf16 a[84:87], v[116:119], v[72:75], a[84:87]
buffer_load_dwordx4 v21, s[12:15], s25 offen offset:1024 lds
v_mfma_f32_16x16x32_bf16 a[88:91], v[120:123], v[72:75], a[88:91]
ds_read_b128 v[140:143], v193 offset:6144
v_mfma_f32_16x16x32_bf16 a[92:95], v[124:127], v[72:75], a[92:95]
v_mfma_f32_16x16x32_bf16 a[96:99], v[96:99], v[76:79], a[96:99]
ds_read_b64_tr_b16 v[176:177], v198 offset:8192
v_mfma_f32_16x16x32_bf16 a[100:103], v[100:103], v[76:79], a[100:103]
buffer_load_dwordx4 v22, s[12:15], s25 offen offset:2048 lds
v_mfma_f32_16x16x32_bf16 a[104:107], v[104:107], v[76:79], a[104:107]
ds_read_b64_tr_b16 v[178:179], v198 offset:9216
v_mfma_f32_16x16x32_bf16 a[108:111], v[108:111], v[76:79], a[108:111]
v_mfma_f32_16x16x32_bf16 a[112:115], v[112:115], v[76:79], a[112:115]
ds_read_b128 v[144:147], v193 offset:8192
v_mfma_f32_16x16x32_bf16 a[116:119], v[116:119], v[76:79], a[116:119]
buffer_load_dwordx4 v23, s[12:15], s25 offen offset:3072 lds
v_mfma_f32_16x16x32_bf16 a[120:123], v[120:123], v[76:79], a[120:123]
ds_read_b64_tr_b16 v[180:181], v199 offset:8192
v_mfma_f32_16x16x32_bf16 a[124:127], v[124:127], v[76:79], a[124:127]
v_mfma_f32_16x16x32_bf16 a[128:131], v[96:99], v[80:83], a[128:131]
ds_read_b64_tr_b16 v[182:183], v199 offset:9216
v_mfma_f32_16x16x32_bf16 a[132:135], v[100:103], v[80:83], a[132:135]
v_mfma_f32_16x16x32_bf16 a[136:139], v[104:107], v[80:83], a[136:139]
ds_read_b128 v[148:151], v193 offset:10240
v_mfma_f32_16x16x32_bf16 a[140:143], v[108:111], v[80:83], a[140:143]
v_mfma_f32_16x16x32_bf16 a[144:147], v[112:115], v[80:83], a[144:147]
ds_read_b64_tr_b16 v[184:185], v200 offset:8192
v_mfma_f32_16x16x32_bf16 a[148:151], v[116:119], v[80:83], a[148:151]
v_mfma_f32_16x16x32_bf16 a[152:155], v[120:123], v[80:83], a[152:155]
ds_read_b64_tr_b16 v[186:187], v200 offset:9216
v_mfma_f32_16x16x32_bf16 a[156:159], v[124:127], v[80:83], a[156:159]
v_mfma_f32_16x16x32_bf16 a[160:163], v[96:99], v[84:87], a[160:163]
s_add_u32 s24, s24, 128
s_add_u32 s25, s25, 128
ds_read_b128 v[152:155], v193 offset:12288
v_mfma_f32_16x16x32_bf16 a[164:167], v[100:103], v[84:87], a[164:167]
v_mfma_f32_16x16x32_bf16 a[168:171], v[104:107], v[84:87], a[168:171]
ds_read_b64_tr_b16 v[188:189], v201 offset:8192
v_mfma_f32_16x16x32_bf16 a[172:175], v[108:111], v[84:87], a[172:175]
v_mfma_f32_16x16x32_bf16 a[176:179], v[112:115], v[84:87], a[176:179]
ds_read_b64_tr_b16 v[190:191], v201 offset:9216
v_mfma_f32_16x16x32_bf16 a[180:183], v[116:119], v[84:87], a[180:183]
v_mfma_f32_16x16x32_bf16 a[184:187], v[120:123], v[84:87], a[184:187]
ds_read_b128 v[156:159], v193 offset:14336
v_mfma_f32_16x16x32_bf16 a[188:191], v[124:127], v[84:87], a[188:191]
v_mfma_f32_16x16x32_bf16 a[192:195], v[96:99], v[88:91], a[192:195]
v_mfma_f32_16x16x32_bf16 a[196:199], v[100:103], v[88:91], a[196:199]
v_mfma_f32_16x16x32_bf16 a[200:203], v[104:107], v[88:91], a[200:203]
v_mfma_f32_16x16x32_bf16 a[204:207], v[108:111], v[88:91], a[204:207]
v_mfma_f32_16x16x32_bf16 a[208:211], v[112:115], v[88:91], a[208:211]
v_mfma_f32_16x16x32_bf16 a[212:215], v[116:119], v[88:91], a[212:215]
v_mfma_f32_16x16x32_bf16 a[216:219], v[120:123], v[88:91], a[216:219]
.Lpad2:
s_cbranch_execz .Ltch0
v_mfma_f32_16x16x32_bf16 a[220:223], v[124:127], v[88:91], a[220:223]
v_mfma_f32_16x16x32_bf16 a[224:227], v[96:99], v[92:95], a[224:227]
v_mfma_f32_16x16x32_bf16 a[228:231], v[100:103], v[92:95], a[228:231]
v_mfma_f32_16x16x32_bf16 a[232:235], v[104:107], v[92:95], a[232:235]
v_mfma_f32_16x16x32_bf16 a[236:239], v[108:111], v[92:95], a[236:239]
v_mfma_f32_16x16x32_bf16 a[240:243], v[112:115], v[92:95], a[240:243]
v_mfma_f32_16x16x32_bf16 a[244:247], v[116:119], v[92:95], a[244:247]
v_mfma_f32_16x16x32_bf16 a[248:251], v[120:123], v[92:95], a[248:251]
v_mfma_f32_16x16x32_bf16 a[252:255], v[124:127], v[92:95], a[252:255]
s_waitcnt vmcnt(16) lgkmcnt(0)
s_barrier
v_mfma_f32_16x16x32_bf16 a[0:3], v[160:163], v[128:131], a[0:3]
s_add_u32 m0, s91, 0
ds_read_b64_tr_b16 v[64:65], v26 offset:0
v_mfma_f32_16x16x32_bf16 a[4:7], v[164:167], v[128:131], a[4:7]
buffer_load_dwordx4 v16, s[16:19], s26 offen offset:0 lds
v_mfma_f32_16x16x32_bf16 a[8:11], v[168:171], v[128:131], a[8:11]
ds_read_b64_tr_b16 v[66:67], v26 offset:1024
v_mfma_f32_16x16x32_bf16 a[12:15], v[172:175], v[128:131], a[12:15]
v_mfma_f32_16x16x32_bf16 a[16:19], v[176:179], v[128:131], a[16:19]
ds_read_b64_tr_b16 v[68:69], v27 offset:0
v_mfma_f32_16x16x32_bf16 a[20:23], v[180:183], v[128:131], a[20:23]
buffer_load_dwordx4 v17, s[16:19], s26 offen offset:1024 lds
v_mfma_f32_16x16x32_bf16 a[24:27], v[184:187], v[128:131], a[24:27]
ds_read_b64_tr_b16 v[70:71], v27 offset:1024
v_mfma_f32_16x16x32_bf16 a[28:31], v[188:191], v[128:131], a[28:31]
v_mfma_f32_16x16x32_bf16 a[32:35], v[160:163], v[132:135], a[32:35]
ds_read_b64_tr_b16 v[72:73], v28 offset:0
v_mfma_f32_16x16x32_bf16 a[36:39], v[164:167], v[132:135], a[36:39]
buffer_load_dwordx4 v18, s[16:19], s26 offen offset:2048 lds
v_mfma_f32_16x16x32_bf16 a[40:43], v[168:171], v[132:135], a[40:43]
ds_read_b64_tr_b16 v[74:75], v28 offset:1024
v_mfma_f32_16x16x32_bf16 a[44:47], v[172:175], v[132:135], a[44:47]
v_mfma_f32_16x16x32_bf16 a[48:51], v[176:179], v[132:135], a[48:51]
ds_read_b64_tr_b16 v[76:77], v29 offset:0
v_mfma_f32_16x16x32_bf16 a[52:55], v[180:183], v[132:135], a[52:55]
buffer_load_dwordx4 v19, s[16:19], s26 offen offset:3072 lds
v_mfma_f32_16x16x32_bf16 a[56:59], v[184:187], v[132:135], a[56:59]
ds_read_b64_tr_b16 v[78:79], v29 offset:1024
v_mfma_f32_16x16x32_bf16 a[60:63], v[188:191], v[132:135], a[60:63]
s_add_u32 s26, s26, 524288
v_mfma_f32_16x16x32_bf16 a[64:67], v[160:163], v[136:139], a[64:67]
s_add_u32 m0, s92, 131072
ds_read_b64_tr_b16 v[80:81], v30 offset:0
v_mfma_f32_16x16x32_bf16 a[68:71], v[164:167], v[136:139], a[68:71]
buffer_load_dwordx4 v20, s[12:15], s24 offen offset:0 lds
v_mfma_f32_16x16x32_bf16 a[72:75], v[168:171], v[136:139], a[72:75]
ds_read_b64_tr_b16 v[82:83], v30 offset:1024
v_mfma_f32_16x16x32_bf16 a[76:79], v[172:175], v[136:139], a[76:79]
v_mfma_f32_16x16x32_bf16 a[80:83], v[176:179], v[136:139], a[80:83]
ds_read_b64_tr_b16 v[84:85], v31 offset:0
v_mfma_f32_16x16x32_bf16 a[84:87], v[180:183], v[136:139], a[84:87]
buffer_load_dwordx4 v21, s[12:15], s24 offen offset:1024 lds
v_mfma_f32_16x16x32_bf16 a[88:91], v[184:187], v[136:139], a[88:91]
ds_read_b64_tr_b16 v[86:87], v31 offset:1024
v_mfma_f32_16x16x32_bf16 a[92:95], v[188:191], v[136:139], a[92:95]
v_mfma_f32_16x16x32_bf16 a[96:99], v[160:163], v[140:143], a[96:99]
ds_read_b64_tr_b16 v[88:89], v32 offset:0
v_mfma_f32_16x16x32_bf16 a[100:103], v[164:167], v[140:143], a[100:103]
buffer_load_dwordx4 v22, s[12:15], s24 offen offset:2048 lds
v_mfma_f32_16x16x32_bf16 a[104:107], v[168:171], v[140:143], a[104:107]
ds_read_b64_tr_b16 v[90:91], v32 offset:1024
v_mfma_f32_16x16x32_bf16 a[108:111], v[172:175], v[140:143], a[108:111]
v_mfma_f32_16x16x32_bf16 a[112:115], v[176:179], v[140:143], a[112:115]
ds_read_b64_tr_b16 v[92:93], v33 offset:0
v_mfma_f32_16x16x32_bf16 a[116:119], v[180:183], v[140:143], a[116:119]
buffer_load_dwordx4 v23, s[12:15], s24 offen offset:3072 lds
v_mfma_f32_16x16x32_bf16 a[120:123], v[184:187], v[140:143], a[120:123]
ds_read_b64_tr_b16 v[94:95], v33 offset:1024
v_mfma_f32_16x16x32_bf16 a[124:127], v[188:191], v[140:143], a[124:127]
v_mfma_f32_16x16x32_bf16 a[128:131], v[160:163], v[144:147], a[128:131]
s_add_u32 m0, s92, 135168
v_mfma_f32_16x16x32_bf16 a[132:135], v[164:167], v[144:147], a[132:135]
buffer_load_dwordx4 v20, s[12:15], s25 offen offset:0 lds
v_mfma_f32_16x16x32_bf16 a[136:139], v[168:171], v[144:147], a[136:139]
ds_read_b128 v[48:51], v24 offset:0
v_mfma_f32_16x16x32_bf16 a[140:143], v[172:175], v[144:147], a[140:143]
v_mfma_f32_16x16x32_bf16 a[144:147], v[176:179], v[144:147], a[144:147]
ds_read_b128 v[52:55], v24 offset:2048
v_mfma_f32_16x16x32_bf16 a[148:151], v[180:183], v[144:147], a[148:151]
buffer_load_dwordx4 v21, s[12:15], s25 offen offset:1024 lds
v_mfma_f32_16x16x32_bf16 a[152:155], v[184:187], v[144:147], a[152:155]
ds_read_b128 v[56:59], v24 offset:4096
v_mfma_f32_16x16x32_bf16 a[156:159], v[188:191], v[144:147], a[156:159]
v_mfma_f32_16x16x32_bf16 a[160:163], v[160:163], v[148:151], a[160:163]
v_mfma_f32_16x16x32_bf16 a[164:167], v[164:167], v[148:151], a[164:167]
buffer_load_dwordx4 v22, s[12:15], s25 offen offset:2048 lds
v_mfma_f32_16x16x32_bf16 a[168:171], v[168:171], v[148:151], a[168:171]
v_mfma_f32_16x16x32_bf16 a[172:175], v[172:175], v[148:151], a[172:175]
v_mfma_f32_16x16x32_bf16 a[176:179], v[176:179], v[148:151], a[176:179]
v_mfma_f32_16x16x32_bf16 a[180:183], v[180:183], v[148:151], a[180:183]
buffer_load_dwordx4 v23, s[12:15], s25 offen offset:3072 lds
v_mfma_f32_16x16x32_bf16 a[184:187], v[184:187], v[148:151], a[184:187]
v_mfma_f32_16x16x32_bf16 a[188:191], v[188:191], v[148:151], a[188:191]
v_mfma_f32_16x16x32_bf16 a[192:195], v[160:163], v[152:155], a[192:195]
v_mfma_f32_16x16x32_bf16 a[196:199], v[164:167], v[152:155], a[196:199]
s_add_u32 s24, s24, 128
s_add_u32 s25, s25, 128
v_mfma_f32_16x16x32_bf16 a[200:203], v[168:171], v[152:155], a[200:203]
v_mfma_f32_16x16x32_bf16 a[204:207], v[172:175], v[152:155], a[204:207]
v_mfma_f32_16x16x32_bf16 a[208:211], v[176:179], v[152:155], a[208:211]
v_mfma_f32_16x16x32_bf16 a[212:215], v[180:183], v[152:155], a[212:215]
v_mfma_f32_16x16x32_bf16 a[216:219], v[184:187], v[152:155], a[216:219]
v_mfma_f32_16x16x32_bf16 a[220:223], v[188:191], v[152:155], a[220:223]
v_mfma_f32_16x16x32_bf16 a[224:227], v[160:163], v[156:159], a[224:227]
v_mfma_f32_16x16x32_bf16 a[228:231], v[164:167], v[156:159], a[228:231]
v_mfma_f32_16x16x32_bf16 a[232:235], v[168:171], v[156:159], a[232:235]
v_mfma_f32_16x16x32_bf16 a[236:239], v[172:175], v[156:159], a[236:239]
v_mfma_f32_16x16x32_bf16 a[240:243], v[176:179], v[156:159], a[240:243]
v_mfma_f32_16x16x32_bf16 a[244:247], v[180:183], v[156:159], a[244:247]
v_mfma_f32_16x16x32_bf16 a[248:251], v[184:187], v[156:159], a[248:251]
v_mfma_f32_16x16x32_bf16 a[252:255], v[188:191], v[156:159], a[252:255]
s_waitcnt lgkmcnt(0)
s_branch .Lp2
.Lp1_v_pad:
s_cbranch_execz .Ltch0
.Lp1_v:
v_mfma_f32_16x16x32_bf16 a[0:3], v[64:67], v[96:99], 0
ds_read_b64_tr_b16 v[160:161], v194 offset:8192
v_mfma_f32_16x16x32_bf16 a[4:7], v[64:67], v[100:103], 0
s_add_u32 s37, s92, 98304
s_mov_b32 s40, 128
s_mov_b32 s41, 524416
v_mfma_f32_16x16x32_bf16 a[8:11], v[64:67], v[104:107], 0
s_mov_b32 m0, s37
ds_read_b64_tr_b16 v[162:163], v194 offset:9216
v_mfma_f32_16x16x32_bf16 a[12:15], v[64:67], v[108:111], 0
buffer_load_dwordx4 v202, s[4:7], s40 offen offset:0 lds
v_mfma_f32_16x16x32_bf16 a[16:19], v[64:67], v[112:115], 0
ds_read_b128 v[128:131], v193 offset:0
v_mfma_f32_16x16x32_bf16 a[20:23], v[64:67], v[116:119], 0
buffer_load_dwordx4 v203, s[4:7], s40 offen offset:1024 lds
v_mfma_f32_16x16x32_bf16 a[24:27], v[64:67], v[120:123], 0
ds_read_b64_tr_b16 v[164:165], v195 offset:8192
v_mfma_f32_16x16x32_bf16 a[28:31], v[64:67], v[124:127], 0
buffer_load_dwordx4 v204, s[4:7], s40 offen offset:2048 lds
v_mfma_f32_16x16x32_bf16 a[32:35], v[68:71], v[96:99], 0
ds_read_b64_tr_b16 v[166:167], v195 offset:9216
v_mfma_f32_16x16x32_bf16 a[36:39], v[68:71], v[100:103], 0
buffer_load_dwordx4 v205, s[4:7], s40 offen offset:3072 lds
v_mfma_f32_16x16x32_bf16 a[40:43], v[68:71], v[104:107], 0
s_add_u32 m0, s37, 4096
ds_read_b128 v[132:135], v193 offset:2048
v_mfma_f32_16x16x32_bf16 a[44:47], v[68:71], v[108:111], 0
buffer_load_dwordx4 v202, s[4:7], s41 offen offset:0 lds
v_mfma_f32_16x16x32_bf16 a[48:51], v[68:71], v[112:115], 0
ds_read_b64_tr_b16 v[168:169], v196 offset:8192
v_mfma_f32_16x16x32_bf16 a[52:55], v[68:71], v[116:119], 0
buffer_load_dwordx4 v203, s[4:7], s41 offen offset:1024 lds
v_mfma_f32_16x16x32_bf16 a[56:59], v[68:71], v[120:123], 0
ds_read_b64_tr_b16 v[170:171], v196 offset:9216
v_mfma_f32_16x16x32_bf16 a[60:63], v[68:71], v[124:127], 0
buffer_load_dwordx4 v204, s[4:7], s41 offen offset:2048 lds
v_mfma_f32_16x16x32_bf16 a[64:67], v[72:75], v[96:99], 0
ds_read_b128 v[136:139], v193 offset:4096
v_mfma_f32_16x16x32_bf16 a[68:71], v[72:75], v[100:103], 0
buffer_load_dwordx4 v205, s[4:7], s41 offen offset:3072 lds
v_mfma_f32_16x16x32_bf16 a[72:75], v[72:75], v[104:107], 0
ds_read_b64_tr_b16 v[172:173], v197 offset:8192
v_mfma_f32_16x16x32_bf16 a[76:79], v[72:75], v[108:111], 0
s_add_u32 s37, s92, 131072
s_mov_b32 s42, 1048576
s_mov_b32 s43, 1179648
v_mfma_f32_16x16x32_bf16 a[80:83], v[72:75], v[112:115], 0
s_mov_b32 m0, s37
ds_read_b64_tr_b16 v[174:175], v197 offset:9216
v_mfma_f32_16x16x32_bf16 a[84:87], v[72:75], v[116:119], 0
buffer_load_dwordx4 v16, s[8:11], s42 offen offset:0 nt lds
v_mfma_f32_16x16x32_bf16 a[88:91], v[72:75], v[120:123], 0
ds_read_b128 v[140:143], v193 offset:6144
v_mfma_f32_16x16x32_bf16 a[92:95], v[72:75], v[124:127], 0
buffer_load_dwordx4 v17, s[8:11], s42 offen offset:1024 nt lds
v_mfma_f32_16x16x32_bf16 a[96:99], v[76:79], v[96:99], 0
ds_read_b64_tr_b16 v[176:177], v198 offset:8192
v_mfma_f32_16x16x32_bf16 a[100:103], v[76:79], v[100:103], 0
buffer_load_dwordx4 v18, s[8:11], s42 offen offset:2048 nt lds
v_mfma_f32_16x16x32_bf16 a[104:107], v[76:79], v[104:107], 0
ds_read_b64_tr_b16 v[178:179], v198 offset:9216
v_mfma_f32_16x16x32_bf16 a[108:111], v[76:79], v[108:111], 0
buffer_load_dwordx4 v19, s[8:11], s42 offen offset:3072 nt lds
v_mfma_f32_16x16x32_bf16 a[112:115], v[76:79], v[112:115], 0
s_add_u32 m0, s37, 4096
ds_read_b128 v[144:147], v193 offset:8192
v_mfma_f32_16x16x32_bf16 a[116:119], v[76:79], v[116:119], 0
buffer_load_dwordx4 v16, s[8:11], s43 offen offset:0 nt lds
v_mfma_f32_16x16x32_bf16 a[120:123], v[76:79], v[120:123], 0
ds_read_b64_tr_b16 v[180:181], v199 offset:8192
v_mfma_f32_16x16x32_bf16 a[124:127], v[76:79], v[124:127], 0
buffer_load_dwordx4 v17, s[8:11], s43 offen offset:1024 nt lds
v_mfma_f32_16x16x32_bf16 a[128:131], v[80:83], v[96:99], 0
ds_read_b64_tr_b16 v[182:183], v199 offset:9216
v_mfma_f32_16x16x32_bf16 a[132:135], v[80:83], v[100:103], 0
buffer_load_dwordx4 v18, s[8:11], s43 offen offset:2048 nt lds
v_mfma_f32_16x16x32_bf16 a[136:139], v[80:83], v[104:107], 0
ds_read_b128 v[148:151], v193 offset:10240
v_mfma_f32_16x16x32_bf16 a[140:143], v[80:83], v[108:111], 0
buffer_load_dwordx4 v19, s[8:11], s43 offen offset:3072 nt lds
v_mfma_f32_16x16x32_bf16 a[144:147], v[80:83], v[112:115], 0
ds_read_b64_tr_b16 v[184:185], v200 offset:8192
v_mfma_f32_16x16x32_bf16 a[148:151], v[80:83], v[116:119], 0
v_mfma_f32_16x16x32_bf16 a[152:155], v[80:83], v[120:123], 0
ds_read_b64_tr_b16 v[186:187], v200 offset:9216
v_mfma_f32_16x16x32_bf16 a[156:159], v[80:83], v[124:127], 0
v_mfma_f32_16x16x32_bf16 a[160:163], v[84:87], v[96:99], 0
ds_read_b128 v[152:155], v193 offset:12288
v_mfma_f32_16x16x32_bf16 a[164:167], v[84:87], v[100:103], 0
v_mfma_f32_16x16x32_bf16 a[168:171], v[84:87], v[104:107], 0
ds_read_b64_tr_b16 v[188:189], v201 offset:8192
v_mfma_f32_16x16x32_bf16 a[172:175], v[84:87], v[108:111], 0
v_mfma_f32_16x16x32_bf16 a[176:179], v[84:87], v[112:115], 0
s_add_u32 s44, s35, 65536
ds_read_b64_tr_b16 v[190:191], v201 offset:9216
v_mfma_f32_16x16x32_bf16 a[180:183], v[84:87], v[116:119], 0
s_cmp_ge_u32 s44, s39
s_cselect_b32 s45, s55, s54
v_mfma_f32_16x16x32_bf16 a[184:187], v[84:87], v[120:123], 0
ds_read_b128 v[156:159], v193 offset:14336
v_mfma_f32_16x16x32_bf16 a[188:191], v[84:87], v[124:127], 0
s_add_u32 s44, s36, 65536
v_mfma_f32_16x16x32_bf16 a[192:195], v[88:91], v[96:99], 0
v_mfma_f32_16x16x32_bf16 a[196:199], v[88:91], v[100:103], 0
s_cmp_ge_u32 s44, s39
s_cselect_b32 s34, s55, s54
v_mfma_f32_16x16x32_bf16 a[200:203], v[88:91], v[104:107], 0
v_add_u32 v194, s45, v194
v_mfma_f32_16x16x32_bf16 a[204:207], v[88:91], v[108:111], 0
v_add_u32 v195, s45, v195
v_mfma_f32_16x16x32_bf16 a[208:211], v[88:91], v[112:115], 0
v_add_u32 v196, s45, v196
v_mfma_f32_16x16x32_bf16 a[212:215], v[88:91], v[116:119], 0
v_add_u32 v197, s45, v197
v_mfma_f32_16x16x32_bf16 a[216:219], v[88:91], v[120:123], 0
v_add_u32 v198, s45, v198
v_mfma_f32_16x16x32_bf16 a[220:223], v[88:91], v[124:127], 0
v_add_u32 v199, s45, v199
v_mfma_f32_16x16x32_bf16 a[224:227], v[92:95], v[96:99], 0
v_add_u32 v200, s45, v200
v_mfma_f32_16x16x32_bf16 a[228:231], v[92:95], v[100:103], 0
v_add_u32 v201, s45, v201
v_mfma_f32_16x16x32_bf16 a[232:235], v[92:95], v[104:107], 0
v_add_u32 v192, s34, v192
v_mfma_f32_16x16x32_bf16 a[236:239], v[92:95], v[108:111], 0
v_add_u32 v193, s34, v193
v_mfma_f32_16x16x32_bf16 a[240:243], v[92:95], v[112:115], 0
v_mfma_f32_16x16x32_bf16 a[244:247], v[92:95], v[116:119], 0
s_mov_b32 s40, 256
v_mfma_f32_16x16x32_bf16 a[248:251], v[92:95], v[120:123], 0
s_mov_b32 s41, 524544
v_mfma_f32_16x16x32_bf16 a[252:255], v[92:95], v[124:127], 0
s_waitcnt vmcnt(8) lgkmcnt(0)
s_barrier
v_mfma_f32_16x16x32_bf16 a[0:3], v[128:131], v[160:163], a[0:3]
s_mov_b32 m0, s35
ds_read_b64_tr_b16 v[96:97], v194 offset:0
v_mfma_f32_16x16x32_bf16 a[4:7], v[128:131], v[164:167], a[4:7]
buffer_load_dwordx4 v202, s[4:7], s40 offen offset:0 lds
v_mfma_f32_16x16x32_bf16 a[8:11], v[128:131], v[168:171], a[8:11]
ds_read_b64_tr_b16 v[98:99], v194 offset:1024
v_mfma_f32_16x16x32_bf16 a[12:15], v[128:131], v[172:175], a[12:15]
v_mfma_f32_16x16x32_bf16 a[16:19], v[128:131], v[176:179], a[16:19]
ds_read_b128 v[64:67], v192 offset:0
v_mfma_f32_16x16x32_bf16 a[20:23], v[128:131], v[180:183], a[20:23]
buffer_load_dwordx4 v203, s[4:7], s40 offen offset:1024 lds
v_mfma_f32_16x16x32_bf16 a[24:27], v[128:131], v[184:187], a[24:27]
ds_read_b64_tr_b16 v[100:101], v195 offset:0
v_mfma_f32_16x16x32_bf16 a[28:31], v[128:131], v[188:191], a[28:31]
v_mfma_f32_16x16x32_bf16 a[32:35], v[132:135], v[160:163], a[32:35]
ds_read_b64_tr_b16 v[102:103], v195 offset:1024
v_mfma_f32_16x16x32_bf16 a[36:39], v[132:135], v[164:167], a[36:39]
buffer_load_dwordx4 v204, s[4:7], s40 offen offset:2048 lds
v_mfma_f32_16x16x32_bf16 a[40:43], v[132:135], v[168:171], a[40:43]
ds_read_b128 v[68:71], v192 offset:2048
v_mfma_f32_16x16x32_bf16 a[44:47], v[132:135], v[172:175], a[44:47]
v_mfma_f32_16x16x32_bf16 a[48:51], v[132:135], v[176:179], a[48:51]
ds_read_b64_tr_b16 v[104:105], v196 offset:0
v_mfma_f32_16x16x32_bf16 a[52:55], v[132:135], v[180:183], a[52:55]
buffer_load_dwordx4 v205, s[4:7], s40 offen offset:3072 lds
v_mfma_f32_16x16x32_bf16 a[56:59], v[132:135], v[184:187], a[56:59]
ds_read_b64_tr_b16 v[106:107], v196 offset:1024
v_mfma_f32_16x16x32_bf16 a[60:63], v[132:135], v[188:191], a[60:63]
v_mfma_f32_16x16x32_bf16 a[64:67], v[136:139], v[160:163], a[64:67]
s_add_u32 m0, s35, 4096
ds_read_b128 v[72:75], v192 offset:4096
v_mfma_f32_16x16x32_bf16 a[68:71], v[136:139], v[164:167], a[68:71]
buffer_load_dwordx4 v202, s[4:7], s41 offen offset:0 lds
v_mfma_f32_16x16x32_bf16 a[72:75], v[136:139], v[168:171], a[72:75]
ds_read_b64_tr_b16 v[108:109], v197 offset:0
v_mfma_f32_16x16x32_bf16 a[76:79], v[136:139], v[172:175], a[76:79]
v_mfma_f32_16x16x32_bf16 a[80:83], v[136:139], v[176:179], a[80:83]
ds_read_b64_tr_b16 v[110:111], v197 offset:1024
v_mfma_f32_16x16x32_bf16 a[84:87], v[136:139], v[180:183], a[84:87]
buffer_load_dwordx4 v203, s[4:7], s41 offen offset:1024 lds
v_mfma_f32_16x16x32_bf16 a[88:91], v[136:139], v[184:187], a[88:91]
ds_read_b128 v[76:79], v192 offset:6144
v_mfma_f32_16x16x32_bf16 a[92:95], v[136:139], v[188:191], a[92:95]
v_mfma_f32_16x16x32_bf16 a[96:99], v[140:143], v[160:163], a[96:99]
ds_read_b64_tr_b16 v[112:113], v198 offset:0
v_mfma_f32_16x16x32_bf16 a[100:103], v[140:143], v[164:167], a[100:103]
buffer_load_dwordx4 v204, s[4:7], s41 offen offset:2048 lds
v_mfma_f32_16x16x32_bf16 a[104:107], v[140:143], v[168:171], a[104:107]
ds_read_b64_tr_b16 v[114:115], v198 offset:1024
v_mfma_f32_16x16x32_bf16 a[108:111], v[140:143], v[172:175], a[108:111]
v_mfma_f32_16x16x32_bf16 a[112:115], v[140:143], v[176:179], a[112:115]
ds_read_b128 v[80:83], v192 offset:8192
v_mfma_f32_16x16x32_bf16 a[116:119], v[140:143], v[180:183], a[116:119]
buffer_load_dwordx4 v205, s[4:7], s41 offen offset:3072 lds
v_mfma_f32_16x16x32_bf16 a[120:123], v[140:143], v[184:187], a[120:123]
ds_read_b64_tr_b16 v[116:117], v199 offset:0
v_mfma_f32_16x16x32_bf16 a[124:127], v[140:143], v[188:191], a[124:127]
v_mfma_f32_16x16x32_bf16 a[128:131], v[144:147], v[160:163], a[128:131]
ds_read_b64_tr_b16 v[118:119], v199 offset:1024
v_mfma_f32_16x16x32_bf16 a[132:135], v[144:147], v[164:167], a[132:135]
buffer_load_dword v35, v206, s[56:59], s48 offen
v_mfma_f32_16x16x32_bf16 a[136:139], v[144:147], v[168:171], a[136:139]
ds_read_b128 v[84:87], v192 offset:10240
v_mfma_f32_16x16x32_bf16 a[140:143], v[144:147], v[172:175], a[140:143]
buffer_load_dword v2, v3, s[60:63], s90 offen sc1
v_mfma_f32_16x16x32_bf16 a[144:147], v[144:147], v[176:179], a[144:147]
s_mov_b32 s42, 1572864
ds_read_b64_tr_b16 v[120:121], v200 offset:0
v_mfma_f32_16x16x32_bf16 a[148:151], v[144:147], v[180:183], a[148:151]
s_mov_b32 s43, 1703936
buffer_load_dword v35, v207, s[12:15], s49 offen
v_mfma_f32_16x16x32_bf16 a[152:155], v[144:147], v[184:187], a[152:155]
ds_read_b64_tr_b16 v[122:123], v200 offset:1024
v_mfma_f32_16x16x32_bf16 a[156:159], v[144:147], v[188:191], a[156:159]
buffer_load_dword v11, v6, s[60:63], 0 offen sc1
v_mfma_f32_16x16x32_bf16 a[160:163], v[148:151], v[160:163], a[160:163]
s_add_u32 s40, s40, 128
ds_read_b128 v[88:91], v192 offset:12288
v_mfma_f32_16x16x32_bf16 a[164:167], v[148:151], v[164:167], a[164:167]
s_add_u32 s41, s41, 128
v_mfma_f32_16x16x32_bf16 a[168:171], v[148:151], v[168:171], a[168:171]
ds_read_b64_tr_b16 v[124:125], v201 offset:0
v_mfma_f32_16x16x32_bf16 a[172:175], v[148:151], v[172:175], a[172:175]
s_add_u32 s48, s48, 0x100000
v_mfma_f32_16x16x32_bf16 a[176:179], v[148:151], v[176:179], a[176:179]
ds_read_b64_tr_b16 v[126:127], v201 offset:1024
v_mfma_f32_16x16x32_bf16 a[180:183], v[148:151], v[180:183], a[180:183]
s_add_u32 s49, s49, 256
v_mfma_f32_16x16x32_bf16 a[184:187], v[148:151], v[184:187], a[184:187]
ds_read_b128 v[92:95], v192 offset:14336
v_mfma_f32_16x16x32_bf16 a[188:191], v[148:151], v[188:191], a[188:191]
buffer_store_dword v45, v44, s[60:63], 0 offen sc1
v_mfma_f32_16x16x32_bf16 a[192:195], v[152:155], v[160:163], a[192:195]
v_mfma_f32_16x16x32_bf16 a[196:199], v[152:155], v[164:167], a[196:199]
v_mfma_f32_16x16x32_bf16 a[200:203], v[152:155], v[168:171], a[200:203]
v_mfma_f32_16x16x32_bf16 a[204:207], v[152:155], v[172:175], a[204:207]
buffer_store_dword v45, v46, s[60:63], 0 offen sc1
v_mfma_f32_16x16x32_bf16 a[208:211], v[152:155], v[176:179], a[208:211]
v_mfma_f32_16x16x32_bf16 a[212:215], v[152:155], v[180:183], a[212:215]
v_mfma_f32_16x16x32_bf16 a[216:219], v[152:155], v[184:187], a[216:219]
v_mfma_f32_16x16x32_bf16 a[220:223], v[152:155], v[188:191], a[220:223]
v_mfma_f32_16x16x32_bf16 a[224:227], v[156:159], v[160:163], a[224:227]
v_mfma_f32_16x16x32_bf16 a[228:231], v[156:159], v[164:167], a[228:231]
s_mov_b32 s37, s36
v_mfma_f32_16x16x32_bf16 a[232:235], v[156:159], v[168:171], a[232:235]
s_add_u32 s35, s35, s45
v_mfma_f32_16x16x32_bf16 a[236:239], v[156:159], v[172:175], a[236:239]
s_add_u32 s36, s36, s34
v_mfma_f32_16x16x32_bf16 a[240:243], v[156:159], v[176:179], a[240:243]
v_mfma_f32_16x16x32_bf16 a[244:247], v[156:159], v[180:183], a[244:247]
v_mfma_f32_16x16x32_bf16 a[248:251], v[156:159], v[184:187], a[248:251]
v_mfma_f32_16x16x32_bf16 a[252:255], v[156:159], v[188:191], a[252:255]
s_waitcnt lgkmcnt(0)
s_nop 0
s_nop 0
s_nop 0
s_nop 0
s_nop 0
s_nop 0
.Lp1_vloop:
v_mfma_f32_16x16x32_bf16 a[0:3], v[64:67], v[96:99], a[0:3]
s_mov_b32 m0, s37
ds_read_b64_tr_b16 v[160:161], v194 offset:8192
v_mfma_f32_16x16x32_bf16 a[4:7], v[64:67], v[100:103], a[4:7]
buffer_load_dwordx4 v16, s[8:11], s42 offen offset:0 nt lds
v_mfma_f32_16x16x32_bf16 a[8:11], v[64:67], v[104:107], a[8:11]
ds_read_b64_tr_b16 v[162:163], v194 offset:9216
v_mfma_f32_16x16x32_bf16 a[12:15], v[64:67], v[108:111], a[12:15]
v_mfma_f32_16x16x32_bf16 a[16:19], v[64:67], v[112:115], a[16:19]
ds_read_b128 v[128:131], v193 offset:0
v_mfma_f32_16x16x32_bf16 a[20:23], v[64:67], v[116:119], a[20:23]
buffer_load_dwordx4 v17, s[8:11], s42 offen offset:1024 nt lds
v_mfma_f32_16x16x32_bf16 a[24:27], v[64:67], v[120:123], a[24:27]
ds_read_b64_tr_b16 v[164:165], v195 offset:8192
v_mfma_f32_16x16x32_bf16 a[28:31], v[64:67], v[124:127], a[28:31]
v_mfma_f32_16x16x32_bf16 a[32:35], v[68:71], v[96:99], a[32:35]
ds_read_b64_tr_b16 v[166:167], v195 offset:9216
v_mfma_f32_16x16x32_bf16 a[36:39], v[68:71], v[100:103], a[36:39]
buffer_load_dwordx4 v18, s[8:11], s42 offen offset:2048 nt lds
v_mfma_f32_16x16x32_bf16 a[40:43], v[68:71], v[104:107], a[40:43]
ds_read_b128 v[132:135], v193 offset:2048
v_mfma_f32_16x16x32_bf16 a[44:47], v[68:71], v[108:111], a[44:47]
v_mfma_f32_16x16x32_bf16 a[48:51], v[68:71], v[112:115], a[48:51]
ds_read_b64_tr_b16 v[168:169], v196 offset:8192
v_mfma_f32_16x16x32_bf16 a[52:55], v[68:71], v[116:119], a[52:55]
buffer_load_dwordx4 v19, s[8:11], s42 offen offset:3072 nt lds
v_mfma_f32_16x16x32_bf16 a[56:59], v[68:71], v[120:123], a[56:59]
ds_read_b64_tr_b16 v[170:171], v196 offset:9216
v_mfma_f32_16x16x32_bf16 a[60:63], v[68:71], v[124:127], a[60:63]
v_mfma_f32_16x16x32_bf16 a[64:67], v[72:75], v[96:99], a[64:67]
s_add_u32 m0, s37, 4096
ds_read_b128 v[136:139], v193 offset:4096
v_mfma_f32_16x16x32_bf16 a[68:71], v[72:75], v[100:103], a[68:71]
buffer_load_dwordx4 v16, s[8:11], s43 offen offset:0 nt lds
v_mfma_f32_16x16x32_bf16 a[72:75], v[72:75], v[104:107], a[72:75]
ds_read_b64_tr_b16 v[172:173], v197 offset:8192
v_mfma_f32_16x16x32_bf16 a[76:79], v[72:75], v[108:111], a[76:79]
v_mfma_f32_16x16x32_bf16 a[80:83], v[72:75], v[112:115], a[80:83]
ds_read_b64_tr_b16 v[174:175], v197 offset:9216
v_mfma_f32_16x16x32_bf16 a[84:87], v[72:75], v[116:119], a[84:87]
buffer_load_dwordx4 v17, s[8:11], s43 offen offset:1024 nt lds
v_mfma_f32_16x16x32_bf16 a[88:91], v[72:75], v[120:123], a[88:91]
ds_read_b128 v[140:143], v193 offset:6144
v_mfma_f32_16x16x32_bf16 a[92:95], v[72:75], v[124:127], a[92:95]
v_mfma_f32_16x16x32_bf16 a[96:99], v[76:79], v[96:99], a[96:99]
ds_read_b64_tr_b16 v[176:177], v198 offset:8192
v_mfma_f32_16x16x32_bf16 a[100:103], v[76:79], v[100:103], a[100:103]
buffer_load_dwordx4 v18, s[8:11], s43 offen offset:2048 nt lds
v_mfma_f32_16x16x32_bf16 a[104:107], v[76:79], v[104:107], a[104:107]
ds_read_b64_tr_b16 v[178:179], v198 offset:9216
v_mfma_f32_16x16x32_bf16 a[108:111], v[76:79], v[108:111], a[108:111]
v_mfma_f32_16x16x32_bf16 a[112:115], v[76:79], v[112:115], a[112:115]
ds_read_b128 v[144:147], v193 offset:8192
v_mfma_f32_16x16x32_bf16 a[116:119], v[76:79], v[116:119], a[116:119]
buffer_load_dwordx4 v19, s[8:11], s43 offen offset:3072 nt lds
v_mfma_f32_16x16x32_bf16 a[120:123], v[76:79], v[120:123], a[120:123]
ds_read_b64_tr_b16 v[180:181], v199 offset:8192
v_mfma_f32_16x16x32_bf16 a[124:127], v[76:79], v[124:127], a[124:127]
v_mfma_f32_16x16x32_bf16 a[128:131], v[80:83], v[96:99], a[128:131]
ds_read_b64_tr_b16 v[182:183], v199 offset:9216
v_mfma_f32_16x16x32_bf16 a[132:135], v[80:83], v[100:103], a[132:135]
v_mfma_f32_16x16x32_bf16 a[136:139], v[80:83], v[104:107], a[136:139]
ds_read_b128 v[148:151], v193 offset:10240
v_mfma_f32_16x16x32_bf16 a[140:143], v[80:83], v[108:111], a[140:143]
v_mfma_f32_16x16x32_bf16 a[144:147], v[80:83], v[112:115], a[144:147]
ds_read_b64_tr_b16 v[184:185], v200 offset:8192
v_mfma_f32_16x16x32_bf16 a[148:151], v[80:83], v[116:119], a[148:151]
v_mfma_f32_16x16x32_bf16 a[152:155], v[80:83], v[120:123], a[152:155]
ds_read_b64_tr_b16 v[186:187], v200 offset:9216
v_mfma_f32_16x16x32_bf16 a[156:159], v[80:83], v[124:127], a[156:159]
v_mfma_f32_16x16x32_bf16 a[160:163], v[84:87], v[96:99], a[160:163]
s_add_u32 s42, s42, 524288
ds_read_b128 v[152:155], v193 offset:12288
v_mfma_f32_16x16x32_bf16 a[164:167], v[84:87], v[100:103], a[164:167]
s_add_u32 s43, s43, 524288
v_mfma_f32_16x16x32_bf16 a[168:171], v[84:87], v[104:107], a[168:171]
ds_read_b64_tr_b16 v[188:189], v201 offset:8192
v_mfma_f32_16x16x32_bf16 a[172:175], v[84:87], v[108:111], a[172:175]
v_mfma_f32_16x16x32_bf16 a[176:179], v[84:87], v[112:115], a[176:179]
s_add_u32 s44, s35, 65536
ds_read_b64_tr_b16 v[190:191], v201 offset:9216
v_mfma_f32_16x16x32_bf16 a[180:183], v[84:87], v[116:119], a[180:183]
s_cmp_ge_u32 s44, s39
s_cselect_b32 s45, s55, s54
v_mfma_f32_16x16x32_bf16 a[184:187], v[84:87], v[120:123], a[184:187]
ds_read_b128 v[156:159], v193 offset:14336
v_mfma_f32_16x16x32_bf16 a[188:191], v[84:87], v[124:127], a[188:191]
s_add_u32 s44, s36, 65536
v_mfma_f32_16x16x32_bf16 a[192:195], v[88:91], v[96:99], a[192:195]
v_mfma_f32_16x16x32_bf16 a[196:199], v[88:91], v[100:103], a[196:199]
s_cmp_ge_u32 s44, s39
s_cselect_b32 s34, s55, s54
v_mfma_f32_16x16x32_bf16 a[200:203], v[88:91], v[104:107], a[200:203]
v_add_u32 v194, s45, v194
v_mfma_f32_16x16x32_bf16 a[204:207], v[88:91], v[108:111], a[204:207]
v_add_u32 v195, s45, v195
v_mfma_f32_16x16x32_bf16 a[208:211], v[88:91], v[112:115], a[208:211]
v_add_u32 v196, s45, v196
v_mfma_f32_16x16x32_bf16 a[212:215], v[88:91], v[116:119], a[212:215]
v_add_u32 v197, s45, v197
v_mfma_f32_16x16x32_bf16 a[216:219], v[88:91], v[120:123], a[216:219]
v_add_u32 v198, s45, v198
v_mfma_f32_16x16x32_bf16 a[220:223], v[88:91], v[124:127], a[220:223]
v_add_u32 v199, s45, v199
v_mfma_f32_16x16x32_bf16 a[224:227], v[92:95], v[96:99], a[224:227]
v_add_u32 v200, s45, v200
v_mfma_f32_16x16x32_bf16 a[228:231], v[92:95], v[100:103], a[228:231]
v_add_u32 v201, s45, v201
v_mfma_f32_16x16x32_bf16 a[232:235], v[92:95], v[104:107], a[232:235]
v_add_u32 v192, s34, v192
v_mfma_f32_16x16x32_bf16 a[236:239], v[92:95], v[108:111], a[236:239]
v_add_u32 v193, s34, v193
v_mfma_f32_16x16x32_bf16 a[240:243], v[92:95], v[112:115], a[240:243]
v_mfma_f32_16x16x32_bf16 a[244:247], v[92:95], v[116:119], a[244:247]
v_mfma_f32_16x16x32_bf16 a[248:251], v[92:95], v[120:123], a[248:251]
v_mfma_f32_16x16x32_bf16 a[252:255], v[92:95], v[124:127], a[252:255]
s_waitcnt vmcnt(10) lgkmcnt(0)
s_barrier
v_mfma_f32_16x16x32_bf16 a[0:3], v[128:131], v[160:163], a[0:3]
s_mov_b32 m0, s35
ds_read_b64_tr_b16 v[96:97], v194 offset:0
v_mfma_f32_16x16x32_bf16 a[4:7], v[128:131], v[164:167], a[4:7]
buffer_load_dwordx4 v202, s[4:7], s40 offen offset:0 lds
v_mfma_f32_16x16x32_bf16 a[8:11], v[128:131], v[168:171], a[8:11]
ds_read_b64_tr_b16 v[98:99], v194 offset:1024
v_mfma_f32_16x16x32_bf16 a[12:15], v[128:131], v[172:175], a[12:15]
v_mfma_f32_16x16x32_bf16 a[16:19], v[128:131], v[176:179], a[16:19]
ds_read_b128 v[64:67], v192 offset:0
v_mfma_f32_16x16x32_bf16 a[20:23], v[128:131], v[180:183], a[20:23]
buffer_load_dwordx4 v203, s[4:7], s40 offen offset:1024 lds
v_mfma_f32_16x16x32_bf16 a[24:27], v[128:131], v[184:187], a[24:27]
ds_read_b64_tr_b16 v[100:101], v195 offset:0
v_mfma_f32_16x16x32_bf16 a[28:31], v[128:131], v[188:191], a[28:31]
v_mfma_f32_16x16x32_bf16 a[32:35], v[132:135], v[160:163], a[32:35]
ds_read_b64_tr_b16 v[102:103], v195 offset:1024
v_mfma_f32_16x16x32_bf16 a[36:39], v[132:135], v[164:167], a[36:39]
buffer_load_dwordx4 v204, s[4:7], s40 offen offset:2048 lds
v_mfma_f32_16x16x32_bf16 a[40:43], v[132:135], v[168:171], a[40:43]
ds_read_b128 v[68:71], v192 offset:2048
v_mfma_f32_16x16x32_bf16 a[44:47], v[132:135], v[172:175], a[44:47]
v_mfma_f32_16x16x32_bf16 a[48:51], v[132:135], v[176:179], a[48:51]
ds_read_b64_tr_b16 v[104:105], v196 offset:0
v_mfma_f32_16x16x32_bf16 a[52:55], v[132:135], v[180:183], a[52:55]
buffer_load_dwordx4 v205, s[4:7], s40 offen offset:3072 lds
v_mfma_f32_16x16x32_bf16 a[56:59], v[132:135], v[184:187], a[56:59]
ds_read_b64_tr_b16 v[106:107], v196 offset:1024
v_mfma_f32_16x16x32_bf16 a[60:63], v[132:135], v[188:191], a[60:63]
v_mfma_f32_16x16x32_bf16 a[64:67], v[136:139], v[160:163], a[64:67]
s_add_u32 m0, s35, 4096
ds_read_b128 v[72:75], v192 offset:4096
v_mfma_f32_16x16x32_bf16 a[68:71], v[136:139], v[164:167], a[68:71]
buffer_load_dwordx4 v202, s[4:7], s41 offen offset:0 lds
v_mfma_f32_16x16x32_bf16 a[72:75], v[136:139], v[168:171], a[72:75]
ds_read_b64_tr_b16 v[108:109], v197 offset:0
v_mfma_f32_16x16x32_bf16 a[76:79], v[136:139], v[172:175], a[76:79]
v_mfma_f32_16x16x32_bf16 a[80:83], v[136:139], v[176:179], a[80:83]
ds_read_b64_tr_b16 v[110:111], v197 offset:1024
v_mfma_f32_16x16x32_bf16 a[84:87], v[136:139], v[180:183], a[84:87]
buffer_load_dwordx4 v203, s[4:7], s41 offen offset:1024 lds
v_mfma_f32_16x16x32_bf16 a[88:91], v[136:139], v[184:187], a[88:91]
ds_read_b128 v[76:79], v192 offset:6144
v_mfma_f32_16x16x32_bf16 a[92:95], v[136:139], v[188:191], a[92:95]
v_mfma_f32_16x16x32_bf16 a[96:99], v[140:143], v[160:163], a[96:99]
ds_read_b64_tr_b16 v[112:113], v198 offset:0
v_mfma_f32_16x16x32_bf16 a[100:103], v[140:143], v[164:167], a[100:103]
buffer_load_dwordx4 v204, s[4:7], s41 offen offset:2048 lds
v_mfma_f32_16x16x32_bf16 a[104:107], v[140:143], v[168:171], a[104:107]
ds_read_b64_tr_b16 v[114:115], v198 offset:1024
v_mfma_f32_16x16x32_bf16 a[108:111], v[140:143], v[172:175], a[108:111]
.Lpad3:
s_cbranch_execz .Ltch0
v_mfma_f32_16x16x32_bf16 a[112:115], v[140:143], v[176:179], a[112:115]
ds_read_b128 v[80:83], v192 offset:8192
v_mfma_f32_16x16x32_bf16 a[116:119], v[140:143], v[180:183], a[116:119]
buffer_load_dwordx4 v205, s[4:7], s41 offen offset:3072 lds
v_mfma_f32_16x16x32_bf16 a[120:123], v[140:143], v[184:187], a[120:123]
ds_read_b64_tr_b16 v[116:117], v199 offset:0
v_mfma_f32_16x16x32_bf16 a[124:127], v[140:143], v[188:191], a[124:127]
v_mfma_f32_16x16x32_bf16 a[128:131], v[144:147], v[160:163], a[128:131]
ds_read_b64_tr_b16 v[118:119], v199 offset:1024
v_mfma_f32_16x16x32_bf16 a[132:135], v[144:147], v[164:167], a[132:135]
buffer_load_dword v35, v206, s[56:59], s48 offen
v_mfma_f32_16x16x32_bf16 a[136:139], v[144:147], v[168:171], a[136:139]
ds_read_b128 v[84:87], v192 offset:10240
v_mfma_f32_16x16x32_bf16 a[140:143], v[144:147], v[172:175], a[140:143]
v_mfma_f32_16x16x32_bf16 a[144:147], v[144:147], v[176:179], a[144:147]
ds_read_b64_tr_b16 v[120:121], v200 offset:0
v_mfma_f32_16x16x32_bf16 a[148:151], v[144:147], v[180:183], a[148:151]
buffer_load_dword v35, v207, s[12:15], s49 offen
v_mfma_f32_16x16x32_bf16 a[152:155], v[144:147], v[184:187], a[152:155]
ds_read_b64_tr_b16 v[122:123], v200 offset:1024
v_mfma_f32_16x16x32_bf16 a[156:159], v[144:147], v[188:191], a[156:159]
v_mfma_f32_16x16x32_bf16 a[160:163], v[148:151], v[160:163], a[160:163]
s_add_u32 s40, s40, 128
ds_read_b128 v[88:91], v192 offset:12288
v_mfma_f32_16x16x32_bf16 a[164:167], v[148:151], v[164:167], a[164:167]
s_add_u32 s41, s41, 128
v_mfma_f32_16x16x32_bf16 a[168:171], v[148:151], v[168:171], a[168:171]
ds_read_b64_tr_b16 v[124:125], v201 offset:0
v_mfma_f32_16x16x32_bf16 a[172:175], v[148:151], v[172:175], a[172:175]
s_add_u32 s48, s48, 0x100000
v_mfma_f32_16x16x32_bf16 a[176:179], v[148:151], v[176:179], a[176:179]
ds_read_b64_tr_b16 v[126:127], v201 offset:1024
v_mfma_f32_16x16x32_bf16 a[180:183], v[148:151], v[180:183], a[180:183]
s_add_u32 s49, s49, 256
v_mfma_f32_16x16x32_bf16 a[184:187], v[148:151], v[184:187], a[184:187]
ds_read_b128 v[92:95], v192 offset:14336
v_mfma_f32_16x16x32_bf16 a[188:191], v[148:151], v[188:191], a[188:191]
v_mfma_f32_16x16x32_bf16 a[192:195], v[152:155], v[160:163], a[192:195]
v_mfma_f32_16x16x32_bf16 a[196:199], v[152:155], v[164:167], a[196:199]
v_mfma_f32_16x16x32_bf16 a[200:203], v[152:155], v[168:171], a[200:203]
v_mfma_f32_16x16x32_bf16 a[204:207], v[152:155], v[172:175], a[204:207]
v_mfma_f32_16x16x32_bf16 a[208:211], v[152:155], v[176:179], a[208:211]
v_mfma_f32_16x16x32_bf16 a[212:215], v[152:155], v[180:183], a[212:215]
v_mfma_f32_16x16x32_bf16 a[216:219], v[152:155], v[184:187], a[216:219]
v_mfma_f32_16x16x32_bf16 a[220:223], v[152:155], v[188:191], a[220:223]
v_mfma_f32_16x16x32_bf16 a[224:227], v[156:159], v[160:163], a[224:227]
v_mfma_f32_16x16x32_bf16 a[228:231], v[156:159], v[164:167], a[228:231]
s_mov_b32 s37, s36
v_mfma_f32_16x16x32_bf16 a[232:235], v[156:159], v[168:171], a[232:235]
s_add_u32 s35, s35, s45
v_mfma_f32_16x16x32_bf16 a[236:239], v[156:159], v[172:175], a[236:239]
s_add_u32 s36, s36, s34
v_mfma_f32_16x16x32_bf16 a[240:243], v[156:159], v[176:179], a[240:243]
s_add_u32 s38, s38, 1
v_mfma_f32_16x16x32_bf16 a[244:247], v[156:159], v[180:183], a[244:247]
s_cmp_lt_u32 s38, 6
v_mfma_f32_16x16x32_bf16 a[248:251], v[156:159], v[184:187], a[248:251]
v_mfma_f32_16x16x32_bf16 a[252:255], v[156:159], v[188:191], a[252:255]
s_waitcnt lgkmcnt(0)
s_cbranch_scc1 .Lp1_vloop
v_mfma_f32_16x16x32_bf16 a[0:3], v[64:67], v[96:99], a[0:3]
s_add_u32 m0, s92, 32768
ds_read_b64_tr_b16 v[160:161], v194 offset:8192
v_mfma_f32_16x16x32_bf16 a[4:7], v[64:67], v[100:103], a[4:7]
buffer_load_dwordx4 v20, s[12:15], s24 offen offset:0 lds
v_mfma_f32_16x16x32_bf16 a[8:11], v[64:67], v[104:107], a[8:11]
ds_read_b64_tr_b16 v[162:163], v194 offset:9216
v_mfma_f32_16x16x32_bf16 a[12:15], v[64:67], v[108:111], a[12:15]
v_mfma_f32_16x16x32_bf16 a[16:19], v[64:67], v[112:115], a[16:19]
ds_read_b128 v[128:131], v193 offset:0
v_mfma_f32_16x16x32_bf16 a[20:23], v[64:67], v[116:119], a[20:23]
buffer_load_dwordx4 v21, s[12:15], s24 offen offset:1024 lds
v_mfma_f32_16x16x32_bf16 a[24:27], v[64:67], v[120:123], a[24:27]
ds_read_b64_tr_b16 v[164:165], v195 offset:8192
v_mfma_f32_16x16x32_bf16 a[28:31], v[64:67], v[124:127], a[28:31]
v_mfma_f32_16x16x32_bf16 a[32:35], v[68:71], v[96:99], a[32:35]
ds_read_b64_tr_b16 v[166:167], v195 offset:9216
v_mfma_f32_16x16x32_bf16 a[36:39], v[68:71], v[100:103], a[36:39]
buffer_load_dwordx4 v22, s[12:15], s24 offen offset:2048 lds
v_mfma_f32_16x16x32_bf16 a[40:43], v[68:71], v[104:107], a[40:43]
ds_read_b128 v[132:135], v193 offset:2048
v_mfma_f32_16x16x32_bf16 a[44:47], v[68:71], v[108:111], a[44:47]
v_mfma_f32_16x16x32_bf16 a[48:51], v[68:71], v[112:115], a[48:51]
ds_read_b64_tr_b16 v[168:169], v196 offset:8192
v_mfma_f32_16x16x32_bf16 a[52:55], v[68:71], v[116:119], a[52:55]
buffer_load_dwordx4 v23, s[12:15], s24 offen offset:3072 lds
v_mfma_f32_16x16x32_bf16 a[56:59], v[68:71], v[120:123], a[56:59]
ds_read_b64_tr_b16 v[170:171], v196 offset:9216
v_mfma_f32_16x16x32_bf16 a[60:63], v[68:71], v[124:127], a[60:63]
v_mfma_f32_16x16x32_bf16 a[64:67], v[72:75], v[96:99], a[64:67]
s_add_u32 m0, s92, 36864
ds_read_b128 v[136:139], v193 offset:4096
v_mfma_f32_16x16x32_bf16 a[68:71], v[72:75], v[100:103], a[68:71]
buffer_load_dwordx4 v20, s[12:15], s25 offen offset:0 lds
v_mfma_f32_16x16x32_bf16 a[72:75], v[72:75], v[104:107], a[72:75]
ds_read_b64_tr_b16 v[172:173], v197 offset:8192
v_mfma_f32_16x16x32_bf16 a[76:79], v[72:75], v[108:111], a[76:79]
v_mfma_f32_16x16x32_bf16 a[80:83], v[72:75], v[112:115], a[80:83]
ds_read_b64_tr_b16 v[174:175], v197 offset:9216
v_mfma_f32_16x16x32_bf16 a[84:87], v[72:75], v[116:119], a[84:87]
buffer_load_dwordx4 v21, s[12:15], s25 offen offset:1024 lds
v_mfma_f32_16x16x32_bf16 a[88:91], v[72:75], v[120:123], a[88:91]
ds_read_b128 v[140:143], v193 offset:6144
v_mfma_f32_16x16x32_bf16 a[92:95], v[72:75], v[124:127], a[92:95]
v_mfma_f32_16x16x32_bf16 a[96:99], v[76:79], v[96:99], a[96:99]
ds_read_b64_tr_b16 v[176:177], v198 offset:8192
v_mfma_f32_16x16x32_bf16 a[100:103], v[76:79], v[100:103], a[100:103]
buffer_load_dwordx4 v22, s[12:15], s25 offen offset:2048 lds
v_mfma_f32_16x16x32_bf16 a[104:107], v[76:79], v[104:107], a[104:107]
ds_read_b64_tr_b16 v[178:179], v198 offset:9216
v_mfma_f32_16x16x32_bf16 a[108:111], v[76:79], v[108:111], a[108:111]
v_mfma_f32_16x16x32_bf16 a[112:115], v[76:79], v[112:115], a[112:115]
ds_read_b128 v[144:147], v193 offset:8192
v_mfma_f32_16x16x32_bf16 a[116:119], v[76:79], v[116:119], a[116:119]
buffer_load_dwordx4 v23, s[12:15], s25 offen offset:3072 lds
v_mfma_f32_16x16x32_bf16 a[120:123], v[76:79], v[120:123], a[120:123]
ds_read_b64_tr_b16 v[180:181], v199 offset:8192
v_mfma_f32_16x16x32_bf16 a[124:127], v[76:79], v[124:127], a[124:127]
v_mfma_f32_16x16x32_bf16 a[128:131], v[80:83], v[96:99], a[128:131]
ds_read_b64_tr_b16 v[182:183], v199 offset:9216
v_mfma_f32_16x16x32_bf16 a[132:135], v[80:83], v[100:103], a[132:135]
v_mfma_f32_16x16x32_bf16 a[136:139], v[80:83], v[104:107], a[136:139]
ds_read_b128 v[148:151], v193 offset:10240
v_mfma_f32_16x16x32_bf16 a[140:143], v[80:83], v[108:111], a[140:143]
v_mfma_f32_16x16x32_bf16 a[144:147], v[80:83], v[112:115], a[144:147]
ds_read_b64_tr_b16 v[184:185], v200 offset:8192
v_mfma_f32_16x16x32_bf16 a[148:151], v[80:83], v[116:119], a[148:151]
v_mfma_f32_16x16x32_bf16 a[152:155], v[80:83], v[120:123], a[152:155]
ds_read_b64_tr_b16 v[186:187], v200 offset:9216
v_mfma_f32_16x16x32_bf16 a[156:159], v[80:83], v[124:127], a[156:159]
v_mfma_f32_16x16x32_bf16 a[160:163], v[84:87], v[96:99], a[160:163]
s_add_u32 s24, s24, 128
s_add_u32 s25, s25, 128
ds_read_b128 v[152:155], v193 offset:12288
v_mfma_f32_16x16x32_bf16 a[164:167], v[84:87], v[100:103], a[164:167]
v_mfma_f32_16x16x32_bf16 a[168:171], v[84:87], v[104:107], a[168:171]
ds_read_b64_tr_b16 v[188:189], v201 offset:8192
v_mfma_f32_16x16x32_bf16 a[172:175], v[84:87], v[108:111], a[172:175]
v_mfma_f32_16x16x32_bf16 a[176:179], v[84:87], v[112:115], a[176:179]
s_add_u32 s44, s35, 65536
ds_read_b64_tr_b16 v[190:191], v201 offset:9216
v_mfma_f32_16x16x32_bf16 a[180:183], v[84:87], v[116:119], a[180:183]
s_cmp_ge_u32 s44, s39
s_cselect_b32 s45, s55, s54
v_mfma_f32_16x16x32_bf16 a[184:187], v[84:87], v[120:123], a[184:187]
ds_read_b128 v[156:159], v193 offset:14336
v_mfma_f32_16x16x32_bf16 a[188:191], v[84:87], v[124:127], a[188:191]
s_add_u32 s44, s36, 65536
v_mfma_f32_16x16x32_bf16 a[192:195], v[88:91], v[96:99], a[192:195]
v_mfma_f32_16x16x32_bf16 a[196:199], v[88:91], v[100:103], a[196:199]
s_cmp_ge_u32 s44, s39
s_cselect_b32 s34, s55, s54
v_mfma_f32_16x16x32_bf16 a[200:203], v[88:91], v[104:107], a[200:203]
v_add_u32 v194, s45, v194
v_mfma_f32_16x16x32_bf16 a[204:207], v[88:91], v[108:111], a[204:207]
v_add_u32 v195, s45, v195
v_mfma_f32_16x16x32_bf16 a[208:211], v[88:91], v[112:115], a[208:211]
v_add_u32 v196, s45, v196
v_mfma_f32_16x16x32_bf16 a[212:215], v[88:91], v[116:119], a[212:215]
v_add_u32 v197, s45, v197
v_mfma_f32_16x16x32_bf16 a[216:219], v[88:91], v[120:123], a[216:219]
v_add_u32 v198, s45, v198
v_mfma_f32_16x16x32_bf16 a[220:223], v[88:91], v[124:127], a[220:223]
v_add_u32 v199, s45, v199
v_mfma_f32_16x16x32_bf16 a[224:227], v[92:95], v[96:99], a[224:227]
v_add_u32 v200, s45, v200
v_mfma_f32_16x16x32_bf16 a[228:231], v[92:95], v[100:103], a[228:231]
v_add_u32 v201, s45, v201
v_mfma_f32_16x16x32_bf16 a[232:235], v[92:95], v[104:107], a[232:235]
v_add_u32 v192, s34, v192
v_mfma_f32_16x16x32_bf16 a[236:239], v[92:95], v[108:111], a[236:239]
v_add_u32 v193, s34, v193
v_mfma_f32_16x16x32_bf16 a[240:243], v[92:95], v[112:115], a[240:243]
v_mfma_f32_16x16x32_bf16 a[244:247], v[92:95], v[116:119], a[244:247]
v_mfma_f32_16x16x32_bf16 a[248:251], v[92:95], v[120:123], a[248:251]
v_mfma_f32_16x16x32_bf16 a[252:255], v[92:95], v[124:127], a[252:255]
s_waitcnt vmcnt(10) lgkmcnt(0)
s_barrier
v_mfma_f32_16x16x32_bf16 a[0:3], v[128:131], v[160:163], a[0:3]
s_add_u32 m0, s91, 65536
ds_read_b64_tr_b16 v[96:97], v194 offset:0
v_mfma_f32_16x16x32_bf16 a[4:7], v[128:131], v[164:167], a[4:7]
buffer_load_dwordx4 v16, s[16:19], s26 offen offset:0 lds
v_mfma_f32_16x16x32_bf16 a[8:11], v[128:131], v[168:171], a[8:11]
ds_read_b64_tr_b16 v[98:99], v194 offset:1024
v_mfma_f32_16x16x32_bf16 a[12:15], v[128:131], v[172:175], a[12:15]
v_mfma_f32_16x16x32_bf16 a[16:19], v[128:131], v[176:179], a[16:19]
ds_read_b128 v[64:67], v192 offset:0
v_mfma_f32_16x16x32_bf16 a[20:23], v[128:131], v[180:183], a[20:23]
buffer_load_dwordx4 v17, s[16:19], s26 offen offset:1024 lds
v_mfma_f32_16x16x32_bf16 a[24:27], v[128:131], v[184:187], a[24:27]
ds_read_b64_tr_b16 v[100:101], v195 offset:0
v_mfma_f32_16x16x32_bf16 a[28:31], v[128:131], v[188:191], a[28:31]
v_mfma_f32_16x16x32_bf16 a[32:35], v[132:135], v[160:163], a[32:35]
ds_read_b64_tr_b16 v[102:103], v195 offset:1024
v_mfma_f32_16x16x32_bf16 a[36:39], v[132:135], v[164:167], a[36:39]
buffer_load_dwordx4 v18, s[16:19], s26 offen offset:2048 lds
v_mfma_f32_16x16x32_bf16 a[40:43], v[132:135], v[168:171], a[40:43]
ds_read_b128 v[68:71], v192 offset:2048
v_mfma_f32_16x16x32_bf16 a[44:47], v[132:135], v[172:175], a[44:47]
v_mfma_f32_16x16x32_bf16 a[48:51], v[132:135], v[176:179], a[48:51]
ds_read_b64_tr_b16 v[104:105], v196 offset:0
v_mfma_f32_16x16x32_bf16 a[52:55], v[132:135], v[180:183], a[52:55]
buffer_load_dwordx4 v19, s[16:19], s26 offen offset:3072 lds
v_mfma_f32_16x16x32_bf16 a[56:59], v[132:135], v[184:187], a[56:59]
ds_read_b64_tr_b16 v[106:107], v196 offset:1024
v_mfma_f32_16x16x32_bf16 a[60:63], v[132:135], v[188:191], a[60:63]
s_add_u32 s26, s26, 524288
v_mfma_f32_16x16x32_bf16 a[64:67], v[136:139], v[160:163], a[64:67]
s_add_u32 m0, s91, 81920
ds_read_b128 v[72:75], v192 offset:4096
v_mfma_f32_16x16x32_bf16 a[68:71], v[136:139], v[164:167], a[68:71]
buffer_load_dwordx4 v16, s[16:19], s26 offen offset:0 lds
v_mfma_f32_16x16x32_bf16 a[72:75], v[136:139], v[168:171], a[72:75]
ds_read_b64_tr_b16 v[108:109], v197 offset:0
v_mfma_f32_16x16x32_bf16 a[76:79], v[136:139], v[172:175], a[76:79]
v_mfma_f32_16x16x32_bf16 a[80:83], v[136:139], v[176:179], a[80:83]
ds_read_b64_tr_b16 v[110:111], v197 offset:1024
v_mfma_f32_16x16x32_bf16 a[84:87], v[136:139], v[180:183], a[84:87]
buffer_load_dwordx4 v17, s[16:19], s26 offen offset:1024 lds
v_mfma_f32_16x16x32_bf16 a[88:91], v[136:139], v[184:187], a[88:91]
ds_read_b128 v[76:79], v192 offset:6144
v_mfma_f32_16x16x32_bf16 a[92:95], v[136:139], v[188:191], a[92:95]
v_mfma_f32_16x16x32_bf16 a[96:99], v[140:143], v[160:163], a[96:99]
ds_read_b64_tr_b16 v[112:113], v198 offset:0
v_mfma_f32_16x16x32_bf16 a[100:103], v[140:143], v[164:167], a[100:103]
buffer_load_dwordx4 v18, s[16:19], s26 offen offset:2048 lds
v_mfma_f32_16x16x32_bf16 a[104:107], v[140:143], v[168:171], a[104:107]
ds_read_b64_tr_b16 v[114:115], v198 offset:1024
v_mfma_f32_16x16x32_bf16 a[108:111], v[140:143], v[172:175], a[108:111]
v_mfma_f32_16x16x32_bf16 a[112:115], v[140:143], v[176:179], a[112:115]
ds_read_b128 v[80:83], v192 offset:8192
v_mfma_f32_16x16x32_bf16 a[116:119], v[140:143], v[180:183], a[116:119]
buffer_load_dwordx4 v19, s[16:19], s26 offen offset:3072 lds
v_mfma_f32_16x16x32_bf16 a[120:123], v[140:143], v[184:187], a[120:123]
ds_read_b64_tr_b16 v[116:117], v199 offset:0
v_mfma_f32_16x16x32_bf16 a[124:127], v[140:143], v[188:191], a[124:127]
s_add_u32 s26, s26, 524288
v_mfma_f32_16x16x32_bf16 a[128:131], v[144:147], v[160:163], a[128:131]
ds_read_b64_tr_b16 v[118:119], v199 offset:1024
v_mfma_f32_16x16x32_bf16 a[132:135], v[144:147], v[164:167], a[132:135]
buffer_load_dword v35, v206, s[56:59], s48 offen
v_mfma_f32_16x16x32_bf16 a[136:139], v[144:147], v[168:171], a[136:139]
ds_read_b128 v[84:87], v192 offset:10240
v_mfma_f32_16x16x32_bf16 a[140:143], v[144:147], v[172:175], a[140:143]
v_mfma_f32_16x16x32_bf16 a[144:147], v[144:147], v[176:179], a[144:147]
ds_read_b64_tr_b16 v[120:121], v200 offset:0
v_mfma_f32_16x16x32_bf16 a[148:151], v[144:147], v[180:183], a[148:151]
buffer_load_dword v35, v207, s[12:15], s49 offen
v_add_u32 v11, 8, v11
v_and_b32 v11, -8, v11
v_mfma_f32_16x16x32_bf16 a[152:155], v[144:147], v[184:187], a[152:155]
ds_read_b64_tr_b16 v[122:123], v200 offset:1024
v_mfma_f32_16x16x32_bf16 a[156:159], v[144:147], v[188:191], a[156:159]
v_mfma_f32_16x16x32_bf16 a[160:163], v[148:151], v[160:163], a[160:163]
ds_read_b128 v[88:91], v192 offset:12288
v_mfma_f32_16x16x32_bf16 a[164:167], v[148:151], v[164:167], a[164:167]
s_nop 0
v_or_b32 v0, 0, v11
v_mfma_f32_16x16x32_bf16 a[168:171], v[148:151], v[168:171], a[168:171]
ds_read_b64_tr_b16 v[124:125], v201 offset:0
v_mfma_f32_16x16x32_bf16 a[172:175], v[148:151], v[172:175], a[172:175]
s_add_u32 s48, s48, 0x100000
v_mfma_f32_16x16x32_bf16 a[176:179], v[148:151], v[176:179], a[176:179]
ds_read_b64_tr_b16 v[126:127], v201 offset:1024
v_mfma_f32_16x16x32_bf16 a[180:183], v[148:151], v[180:183], a[180:183]
s_add_u32 s49, s49, 256
v_mfma_f32_16x16x32_bf16 a[184:187], v[148:151], v[184:187], a[184:187]
ds_read_b128 v[92:95], v192 offset:14336
v_mfma_f32_16x16x32_bf16 a[188:191], v[148:151], v[188:191], a[188:191]
buffer_store_dword v0, v6, s[60:63], 0 offen sc1
v_mfma_f32_16x16x32_bf16 a[192:195], v[152:155], v[160:163], a[192:195]
v_mfma_f32_16x16x32_bf16 a[196:199], v[152:155], v[164:167], a[196:199]
v_mfma_f32_16x16x32_bf16 a[200:203], v[152:155], v[168:171], a[200:203]
v_mfma_f32_16x16x32_bf16 a[204:207], v[152:155], v[172:175], a[204:207]
buffer_load_dword v35, v4, s[96:99], 0 offen
v_mfma_f32_16x16x32_bf16 a[208:211], v[152:155], v[176:179], a[208:211]
v_mfma_f32_16x16x32_bf16 a[212:215], v[152:155], v[180:183], a[212:215]
v_mfma_f32_16x16x32_bf16 a[216:219], v[152:155], v[184:187], a[216:219]
v_mfma_f32_16x16x32_bf16 a[220:223], v[152:155], v[188:191], a[220:223]
v_mfma_f32_16x16x32_bf16 a[224:227], v[156:159], v[160:163], a[224:227]
v_mfma_f32_16x16x32_bf16 a[228:231], v[156:159], v[164:167], a[228:231]
s_mov_b32 s37, s36
v_mfma_f32_16x16x32_bf16 a[232:235], v[156:159], v[168:171], a[232:235]
s_add_u32 s35, s35, s45
v_mfma_f32_16x16x32_bf16 a[236:239], v[156:159], v[172:175], a[236:239]
s_add_u32 s36, s36, s34
v_mfma_f32_16x16x32_bf16 a[240:243], v[156:159], v[176:179], a[240:243]
v_mfma_f32_16x16x32_bf16 a[244:247], v[156:159], v[180:183], a[244:247]
v_mfma_f32_16x16x32_bf16 a[248:251], v[156:159], v[184:187], a[248:251]
v_mfma_f32_16x16x32_bf16 a[252:255], v[156:159], v[188:191], a[252:255]
s_waitcnt lgkmcnt(0)
v_mfma_f32_16x16x32_bf16 a[0:3], v[64:67], v[96:99], a[0:3]
s_add_u32 m0, s92, 98304
ds_read_b64_tr_b16 v[160:161], v194 offset:8192
v_mfma_f32_16x16x32_bf16 a[4:7], v[64:67], v[100:103], a[4:7]
buffer_load_dwordx4 v20, s[12:15], s24 offen offset:0 lds
v_mfma_f32_16x16x32_bf16 a[8:11], v[64:67], v[104:107], a[8:11]
ds_read_b64_tr_b16 v[162:163], v194 offset:9216
v_mfma_f32_16x16x32_bf16 a[12:15], v[64:67], v[108:111], a[12:15]
v_mfma_f32_16x16x32_bf16 a[16:19], v[64:67], v[112:115], a[16:19]
ds_read_b128 v[128:131], v193 offset:0
v_mfma_f32_16x16x32_bf16 a[20:23], v[64:67], v[116:119], a[20:23]
buffer_load_dwordx4 v21, s[12:15], s24 offen offset:1024 lds
v_mfma_f32_16x16x32_bf16 a[24:27], v[64:67], v[120:123], a[24:27]
ds_read_b64_tr_b16 v[164:165], v195 offset:8192
v_mfma_f32_16x16x32_bf16 a[28:31], v[64:67], v[124:127], a[28:31]
v_mfma_f32_16x16x32_bf16 a[32:35], v[68:71], v[96:99], a[32:35]
ds_read_b64_tr_b16 v[166:167], v195 offset:9216
v_mfma_f32_16x16x32_bf16 a[36:39], v[68:71], v[100:103], a[36:39]
buffer_load_dwordx4 v22, s[12:15], s24 offen offset:2048 lds
v_mfma_f32_16x16x32_bf16 a[40:43], v[68:71], v[104:107], a[40:43]
ds_read_b128 v[132:135], v193 offset:2048
v_mfma_f32_16x16x32_bf16 a[44:47], v[68:71], v[108:111], a[44:47]
v_mfma_f32_16x16x32_bf16 a[48:51], v[68:71], v[112:115], a[48:51]
ds_read_b64_tr_b16 v[168:169], v196 offset:8192
v_mfma_f32_16x16x32_bf16 a[52:55], v[68:71], v[116:119], a[52:55]
buffer_load_dwordx4 v23, s[12:15], s24 offen offset:3072 lds
v_mfma_f32_16x16x32_bf16 a[56:59], v[68:71], v[120:123], a[56:59]
ds_read_b64_tr_b16 v[170:171], v196 offset:9216
v_mfma_f32_16x16x32_bf16 a[60:63], v[68:71], v[124:127], a[60:63]
v_mfma_f32_16x16x32_bf16 a[64:67], v[72:75], v[96:99], a[64:67]
s_add_u32 m0, s92, 102400
ds_read_b128 v[136:139], v193 offset:4096
v_mfma_f32_16x16x32_bf16 a[68:71], v[72:75], v[100:103], a[68:71]
buffer_load_dwordx4 v20, s[12:15], s25 offen offset:0 lds
v_mfma_f32_16x16x32_bf16 a[72:75], v[72:75], v[104:107], a[72:75]
ds_read_b64_tr_b16 v[172:173], v197 offset:8192
v_mfma_f32_16x16x32_bf16 a[76:79], v[72:75], v[108:111], a[76:79]
v_mfma_f32_16x16x32_bf16 a[80:83], v[72:75], v[112:115], a[80:83]
ds_read_b64_tr_b16 v[174:175], v197 offset:9216
v_mfma_f32_16x16x32_bf16 a[84:87], v[72:75], v[116:119], a[84:87]
buffer_load_dwordx4 v21, s[12:15], s25 offen offset:1024 lds
v_mfma_f32_16x16x32_bf16 a[88:91], v[72:75], v[120:123], a[88:91]
ds_read_b128 v[140:143], v193 offset:6144
v_mfma_f32_16x16x32_bf16 a[92:95], v[72:75], v[124:127], a[92:95]
v_mfma_f32_16x16x32_bf16 a[96:99], v[76:79], v[96:99], a[96:99]
ds_read_b64_tr_b16 v[176:177], v198 offset:8192
v_mfma_f32_16x16x32_bf16 a[100:103], v[76:79], v[100:103], a[100:103]
buffer_load_dwordx4 v22, s[12:15], s25 offen offset:2048 lds
v_mfma_f32_16x16x32_bf16 a[104:107], v[76:79], v[104:107], a[104:107]
ds_read_b64_tr_b16 v[178:179], v198 offset:9216
v_mfma_f32_16x16x32_bf16 a[108:111], v[76:79], v[108:111], a[108:111]
v_mfma_f32_16x16x32_bf16 a[112:115], v[76:79], v[112:115], a[112:115]
ds_read_b128 v[144:147], v193 offset:8192
v_mfma_f32_16x16x32_bf16 a[116:119], v[76:79], v[116:119], a[116:119]
buffer_load_dwordx4 v23, s[12:15], s25 offen offset:3072 lds
v_mfma_f32_16x16x32_bf16 a[120:123], v[76:79], v[120:123], a[120:123]
ds_read_b64_tr_b16 v[180:181], v199 offset:8192
v_mfma_f32_16x16x32_bf16 a[124:127], v[76:79], v[124:127], a[124:127]
v_mfma_f32_16x16x32_bf16 a[128:131], v[80:83], v[96:99], a[128:131]
ds_read_b64_tr_b16 v[182:183], v199 offset:9216
v_mfma_f32_16x16x32_bf16 a[132:135], v[80:83], v[100:103], a[132:135]
v_mfma_f32_16x16x32_bf16 a[136:139], v[80:83], v[104:107], a[136:139]
ds_read_b128 v[148:151], v193 offset:10240
v_mfma_f32_16x16x32_bf16 a[140:143], v[80:83], v[108:111], a[140:143]
v_mfma_f32_16x16x32_bf16 a[144:147], v[80:83], v[112:115], a[144:147]
ds_read_b64_tr_b16 v[184:185], v200 offset:8192
v_mfma_f32_16x16x32_bf16 a[148:151], v[80:83], v[116:119], a[148:151]
v_mfma_f32_16x16x32_bf16 a[152:155], v[80:83], v[120:123], a[152:155]
ds_read_b64_tr_b16 v[186:187], v200 offset:9216
v_mfma_f32_16x16x32_bf16 a[156:159], v[80:83], v[124:127], a[156:159]
v_mfma_f32_16x16x32_bf16 a[160:163], v[84:87], v[96:99], a[160:163]
s_add_u32 s24, s24, 128
s_add_u32 s25, s25, 128
ds_read_b128 v[152:155], v193 offset:12288
v_mfma_f32_16x16x32_bf16 a[164:167], v[84:87], v[100:103], a[164:167]
v_mfma_f32_16x16x32_bf16 a[168:171], v[84:87], v[104:107], a[168:171]
ds_read_b64_tr_b16 v[188:189], v201 offset:8192
v_mfma_f32_16x16x32_bf16 a[172:175], v[84:87], v[108:111], a[172:175]
v_mfma_f32_16x16x32_bf16 a[176:179], v[84:87], v[112:115], a[176:179]
ds_read_b64_tr_b16 v[190:191], v201 offset:9216
v_mfma_f32_16x16x32_bf16 a[180:183], v[84:87], v[116:119], a[180:183]
v_mfma_f32_16x16x32_bf16 a[184:187], v[84:87], v[120:123], a[184:187]
ds_read_b128 v[156:159], v193 offset:14336
v_mfma_f32_16x16x32_bf16 a[188:191], v[84:87], v[124:127], a[188:191]
v_mfma_f32_16x16x32_bf16 a[192:195], v[88:91], v[96:99], a[192:195]
v_mfma_f32_16x16x32_bf16 a[196:199], v[88:91], v[100:103], a[196:199]
v_mfma_f32_16x16x32_bf16 a[200:203], v[88:91], v[104:107], a[200:203]
v_mfma_f32_16x16x32_bf16 a[204:207], v[88:91], v[108:111], a[204:207]
v_mfma_f32_16x16x32_bf16 a[208:211], v[88:91], v[112:115], a[208:211]
v_mfma_f32_16x16x32_bf16 a[212:215], v[88:91], v[116:119], a[212:215]
v_mfma_f32_16x16x32_bf16 a[216:219], v[88:91], v[120:123], a[216:219]
v_mfma_f32_16x16x32_bf16 a[220:223], v[88:91], v[124:127], a[220:223]
v_mfma_f32_16x16x32_bf16 a[224:227], v[92:95], v[96:99], a[224:227]
v_mfma_f32_16x16x32_bf16 a[228:231], v[92:95], v[100:103], a[228:231]
v_mfma_f32_16x16x32_bf16 a[232:235], v[92:95], v[104:107], a[232:235]
v_mfma_f32_16x16x32_bf16 a[236:239], v[92:95], v[108:111], a[236:239]
v_mfma_f32_16x16x32_bf16 a[240:243], v[92:95], v[112:115], a[240:243]
v_mfma_f32_16x16x32_bf16 a[244:247], v[92:95], v[116:119], a[244:247]
v_mfma_f32_16x16x32_bf16 a[248:251], v[92:95], v[120:123], a[248:251]
v_mfma_f32_16x16x32_bf16 a[252:255], v[92:95], v[124:127], a[252:255]
s_waitcnt vmcnt(16) lgkmcnt(0)
s_barrier
v_mfma_f32_16x16x32_bf16 a[0:3], v[128:131], v[160:163], a[0:3]
s_add_u32 m0, s91, 0
ds_read_b64_tr_b16 v[64:65], v26 offset:0
v_mfma_f32_16x16x32_bf16 a[4:7], v[128:131], v[164:167], a[4:7]
buffer_load_dwordx4 v16, s[16:19], s26 offen offset:0 lds
v_mfma_f32_16x16x32_bf16 a[8:11], v[128:131], v[168:171], a[8:11]
ds_read_b64_tr_b16 v[66:67], v26 offset:1024
v_mfma_f32_16x16x32_bf16 a[12:15], v[128:131], v[172:175], a[12:15]
v_mfma_f32_16x16x32_bf16 a[16:19], v[128:131], v[176:179], a[16:19]
ds_read_b64_tr_b16 v[68:69], v27 offset:0
v_mfma_f32_16x16x32_bf16 a[20:23], v[128:131], v[180:183], a[20:23]
buffer_load_dwordx4 v17, s[16:19], s26 offen offset:1024 lds
v_mfma_f32_16x16x32_bf16 a[24:27], v[128:131], v[184:187], a[24:27]
ds_read_b64_tr_b16 v[70:71], v27 offset:1024
v_mfma_f32_16x16x32_bf16 a[28:31], v[128:131], v[188:191], a[28:31]
v_mfma_f32_16x16x32_bf16 a[32:35], v[132:135], v[160:163], a[32:35]
ds_read_b64_tr_b16 v[72:73], v28 offset:0
v_mfma_f32_16x16x32_bf16 a[36:39], v[132:135], v[164:167], a[36:39]
buffer_load_dwordx4 v18, s[16:19], s26 offen offset:2048 lds
v_mfma_f32_16x16x32_bf16 a[40:43], v[132:135], v[168:171], a[40:43]
ds_read_b64_tr_b16 v[74:75], v28 offset:1024
v_mfma_f32_16x16x32_bf16 a[44:47], v[132:135], v[172:175], a[44:47]
v_mfma_f32_16x16x32_bf16 a[48:51], v[132:135], v[176:179], a[48:51]
ds_read_b64_tr_b16 v[76:77], v29 offset:0
v_mfma_f32_16x16x32_bf16 a[52:55], v[132:135], v[180:183], a[52:55]
buffer_load_dwordx4 v19, s[16:19], s26 offen offset:3072 lds
v_mfma_f32_16x16x32_bf16 a[56:59], v[132:135], v[184:187], a[56:59]
ds_read_b64_tr_b16 v[78:79], v29 offset:1024
v_mfma_f32_16x16x32_bf16 a[60:63], v[132:135], v[188:191], a[60:63]
s_add_u32 s26, s26, 524288
v_mfma_f32_16x16x32_bf16 a[64:67], v[136:139], v[160:163], a[64:67]
s_add_u32 m0, s92, 131072
ds_read_b64_tr_b16 v[80:81], v30 offset:0
v_mfma_f32_16x16x32_bf16 a[68:71], v[136:139], v[164:167], a[68:71]
buffer_load_dwordx4 v20, s[12:15], s24 offen offset:0 lds
v_mfma_f32_16x16x32_bf16 a[72:75], v[136:139], v[168:171], a[72:75]
ds_read_b64_tr_b16 v[82:83], v30 offset:1024
v_mfma_f32_16x16x32_bf16 a[76:79], v[136:139], v[172:175], a[76:79]
v_mfma_f32_16x16x32_bf16 a[80:83], v[136:139], v[176:179], a[80:83]
ds_read_b64_tr_b16 v[84:85], v31 offset:0
v_mfma_f32_16x16x32_bf16 a[84:87], v[136:139], v[180:183], a[84:87]
buffer_load_dwordx4 v21, s[12:15], s24 offen offset:1024 lds
v_mfma_f32_16x16x32_bf16 a[88:91], v[136:139], v[184:187], a[88:91]
ds_read_b64_tr_b16 v[86:87], v31 offset:1024
v_mfma_f32_16x16x32_bf16 a[92:95], v[136:139], v[188:191], a[92:95]
v_mfma_f32_16x16x32_bf16 a[96:99], v[140:143], v[160:163], a[96:99]
ds_read_b64_tr_b16 v[88:89], v32 offset:0
v_mfma_f32_16x16x32_bf16 a[100:103], v[140:143], v[164:167], a[100:103]
buffer_load_dwordx4 v22, s[12:15], s24 offen offset:2048 lds
v_mfma_f32_16x16x32_bf16 a[104:107], v[140:143], v[168:171], a[104:107]
ds_read_b64_tr_b16 v[90:91], v32 offset:1024
v_mfma_f32_16x16x32_bf16 a[108:111], v[140:143], v[172:175], a[108:111]
v_mfma_f32_16x16x32_bf16 a[112:115], v[140:143], v[176:179], a[112:115]
ds_read_b64_tr_b16 v[92:93], v33 offset:0
v_mfma_f32_16x16x32_bf16 a[116:119], v[140:143], v[180:183], a[116:119]
buffer_load_dwordx4 v23, s[12:15], s24 offen offset:3072 lds
v_mfma_f32_16x16x32_bf16 a[120:123], v[140:143], v[184:187], a[120:123]
ds_read_b64_tr_b16 v[94:95], v33 offset:1024
v_mfma_f32_16x16x32_bf16 a[124:127], v[140:143], v[188:191], a[124:127]
v_mfma_f32_16x16x32_bf16 a[128:131], v[144:147], v[160:163], a[128:131]
s_add_u32 m0, s92, 135168
v_mfma_f32_16x16x32_bf16 a[132:135], v[144:147], v[164:167], a[132:135]
buffer_load_dwordx4 v20, s[12:15], s25 offen offset:0 lds
v_mfma_f32_16x16x32_bf16 a[136:139], v[144:147], v[168:171], a[136:139]
ds_read_b128 v[48:51], v24 offset:0
v_mfma_f32_16x16x32_bf16 a[140:143], v[144:147], v[172:175], a[140:143]
v_mfma_f32_16x16x32_bf16 a[144:147], v[144:147], v[176:179], a[144:147]
ds_read_b128 v[52:55], v24 offset:2048
v_mfma_f32_16x16x32_bf16 a[148:151], v[144:147], v[180:183], a[148:151]
buffer_load_dwordx4 v21, s[12:15], s25 offen offset:1024 lds
v_mfma_f32_16x16x32_bf16 a[152:155], v[144:147], v[184:187], a[152:155]
ds_read_b128 v[56:59], v24 offset:4096
v_mfma_f32_16x16x32_bf16 a[156:159], v[144:147], v[188:191], a[156:159]
v_mfma_f32_16x16x32_bf16 a[160:163], v[148:151], v[160:163], a[160:163]
v_mfma_f32_16x16x32_bf16 a[164:167], v[148:151], v[164:167], a[164:167]
buffer_load_dwordx4 v22, s[12:15], s25 offen offset:2048 lds
v_mfma_f32_16x16x32_bf16 a[168:171], v[148:151], v[168:171], a[168:171]
v_mfma_f32_16x16x32_bf16 a[172:175], v[148:151], v[172:175], a[172:175]
v_mfma_f32_16x16x32_bf16 a[176:179], v[148:151], v[176:179], a[176:179]
v_mfma_f32_16x16x32_bf16 a[180:183], v[148:151], v[180:183], a[180:183]
buffer_load_dwordx4 v23, s[12:15], s25 offen offset:3072 lds
v_mfma_f32_16x16x32_bf16 a[184:187], v[148:151], v[184:187], a[184:187]
v_mfma_f32_16x16x32_bf16 a[188:191], v[148:151], v[188:191], a[188:191]
v_mfma_f32_16x16x32_bf16 a[192:195], v[152:155], v[160:163], a[192:195]
v_mfma_f32_16x16x32_bf16 a[196:199], v[152:155], v[164:167], a[196:199]
s_add_u32 s24, s24, 128
s_add_u32 s25, s25, 128
v_mfma_f32_16x16x32_bf16 a[200:203], v[152:155], v[168:171], a[200:203]
v_mfma_f32_16x16x32_bf16 a[204:207], v[152:155], v[172:175], a[204:207]
v_mfma_f32_16x16x32_bf16 a[208:211], v[152:155], v[176:179], a[208:211]
v_mfma_f32_16x16x32_bf16 a[212:215], v[152:155], v[180:183], a[212:215]
v_mfma_f32_16x16x32_bf16 a[216:219], v[152:155], v[184:187], a[216:219]
v_mfma_f32_16x16x32_bf16 a[220:223], v[152:155], v[188:191], a[220:223]
v_mfma_f32_16x16x32_bf16 a[224:227], v[156:159], v[160:163], a[224:227]
v_mfma_f32_16x16x32_bf16 a[228:231], v[156:159], v[164:167], a[228:231]
v_mfma_f32_16x16x32_bf16 a[232:235], v[156:159], v[168:171], a[232:235]
v_mfma_f32_16x16x32_bf16 a[236:239], v[156:159], v[172:175], a[236:239]
v_mfma_f32_16x16x32_bf16 a[240:243], v[156:159], v[176:179], a[240:243]
v_mfma_f32_16x16x32_bf16 a[244:247], v[156:159], v[180:183], a[244:247]
v_mfma_f32_16x16x32_bf16 a[248:251], v[156:159], v[184:187], a[248:251]
v_mfma_f32_16x16x32_bf16 a[252:255], v[156:159], v[188:191], a[252:255]
s_waitcnt lgkmcnt(0)
.Lp2_pad:
s_cbranch_execz .Ltch1
.Lp2:
v_mfma_f32_16x16x32_bf16 v[128:131], v[64:67], v[48:51], 0
ds_read_b128 v[60:63], v24 offset:6144
v_mfma_f32_16x16x32_bf16 v[132:135], v[68:71], v[48:51], 0
v_add_u32 v24, 65536, v24
v_accvgpr_read_b32 v36, a0
v_mfma_f32_16x16x32_bf16 v[136:139], v[72:75], v[48:51], 0
ds_read_b64_tr_b16 v[96:97], v26 offset:8192
ds_read_b64_tr_b16 v[98:99], v26 offset:9216
v_mfma_f32_16x16x32_bf16 v[140:143], v[76:79], v[48:51], 0
v_accvgpr_read_b32 v37, a1
v_mul_f32 v36, s47, v36
v_mfma_f32_16x16x32_bf16 v[144:147], v[80:83], v[48:51], 0
ds_read_b64_tr_b16 v[100:101], v27 offset:8192
ds_read_b64_tr_b16 v[102:103], v27 offset:9216
v_mfma_f32_16x16x32_bf16 v[148:151], v[84:87], v[48:51], 0
v_mul_f32 v37, s47, v37
v_cvt_pk_bf16_f32 v40, v36, v37
v_mfma_f32_16x16x32_bf16 v[152:155], v[88:91], v[48:51], 0
ds_read_b64_tr_b16 v[104:105], v28 offset:8192
ds_read_b64_tr_b16 v[106:107], v28 offset:9216
v_mfma_f32_16x16x32_bf16 v[156:159], v[92:95], v[48:51], 0
v_accvgpr_read_b32 v38, a2
v_accvgpr_read_b32 v39, a3
v_mfma_f32_16x16x32_bf16 v[160:163], v[64:67], v[52:55], 0
ds_read_b128 v[48:51], v25 offset:0
v_mul_f32 v38, s47, v38
v_mfma_f32_16x16x32_bf16 v[164:167], v[68:71], v[52:55], 0
v_mul_f32 v39, s47, v39
v_cvt_pk_bf16_f32 v41, v38, v39
v_mfma_f32_16x16x32_bf16 v[168:171], v[72:75], v[52:55], 0
s_add_u32 m0, s91, 16384
ds_read_b64_tr_b16 v[108:109], v29 offset:8192
ds_read_b64_tr_b16 v[110:111], v29 offset:9216
v_mfma_f32_16x16x32_bf16 v[172:175], v[76:79], v[52:55], 0
buffer_load_dwordx4 v16, s[16:19], s26 offen offset:0 lds
v_accvgpr_read_b32 v36, a4
v_mfma_f32_16x16x32_bf16 v[176:179], v[80:83], v[52:55], 0
ds_read_b64_tr_b16 v[112:113], v30 offset:8192
ds_read_b64_tr_b16 v[114:115], v30 offset:9216
.Lpad4:
s_cbranch_execz .Ltch1
v_mfma_f32_16x16x32_bf16 v[180:183], v[84:87], v[52:55], 0
v_accvgpr_read_b32 v37, a5
v_mul_f32 v36, s47, v36
v_mfma_f32_16x16x32_bf16 v[184:187], v[88:91], v[52:55], 0
ds_read_b64_tr_b16 v[116:117], v31 offset:8192
ds_read_b64_tr_b16 v[118:119], v31 offset:9216
v_mfma_f32_16x16x32_bf16 v[188:191], v[92:95], v[52:55], 0
buffer_load_dwordx4 v17, s[16:19], s26 offen offset:1024 lds
v_mul_f32 v37, s47, v37
v_mfma_f32_16x16x32_bf16 v[192:195], v[64:67], v[56:59], 0
ds_read_b128 v[52:55], v25 offset:2048
v_cvt_pk_bf16_f32 v42, v36, v37
v_mfma_f32_16x16x32_bf16 v[196:199], v[68:71], v[56:59], 0
v_accvgpr_read_b32 v38, a6
v_accvgpr_read_b32 v39, a7
v_mfma_f32_16x16x32_bf16 v[200:203], v[72:75], v[56:59], 0
ds_read_b64_tr_b16 v[120:121], v32 offset:8192
ds_read_b64_tr_b16 v[122:123], v32 offset:9216
v_mfma_f32_16x16x32_bf16 v[204:207], v[76:79], v[56:59], 0
buffer_load_dwordx4 v18, s[16:19], s26 offen offset:2048 lds
v_mul_f32 v38, s47, v38
v_mfma_f32_16x16x32_bf16 v[208:211], v[80:83], v[56:59], 0
ds_read_b64_tr_b16 v[124:125], v33 offset:8192
ds_read_b64_tr_b16 v[126:127], v33 offset:9216
v_mfma_f32_16x16x32_bf16 v[212:215], v[84:87], v[56:59], 0
v_mul_f32 v39, s47, v39
v_cvt_pk_bf16_f32 v43, v38, v39
v_mfma_f32_16x16x32_bf16 v[216:219], v[88:91], v[56:59], 0
s_waitcnt lgkmcnt(15)
v_permlane32_swap_b32 v40, v42
v_mfma_f32_16x16x32_bf16 v[220:223], v[92:95], v[56:59], 0
v_permlane32_swap_b32 v41, v43
v_mfma_f32_16x16x32_bf16 v[224:227], v[64:67], v[60:63], 0
s_xor_b32 s48, s95, 0
ds_read_b128 v[56:59], v25 offset:4096
v_mfma_f32_16x16x32_bf16 v[228:231], v[68:71], v[60:63], 0
buffer_load_dwordx4 v19, s[16:19], s26 offen offset:3072 lds
v_accvgpr_read_b32 v36, a128
v_mfma_f32_16x16x32_bf16 v[232:235], v[72:75], v[60:63], 0
buffer_store_dwordx4 v[40:43], v7, s[20:23], s48 offen sc1
v_accvgpr_read_b32 v37, a129
v_mfma_f32_16x16x32_bf16 v[236:239], v[76:79], v[60:63], 0
s_add_u32 s26, s26, 524288
v_mul_f32 v36, s47, v36
v_mul_f32 v37, s47, v37
v_mfma_f32_16x16x32_bf16 v[240:243], v[80:83], v[60:63], 0
v_cvt_pk_bf16_f32 v44, v36, v37
v_accvgpr_read_b32 v38, a130
v_mfma_f32_16x16x32_bf16 v[244:247], v[84:87], v[60:63], 0
v_accvgpr_read_b32 v39, a131
v_mul_f32 v38, s47, v38
v_mfma_f32_16x16x32_bf16 v[248:251], v[88:91], v[60:63], 0
v_mul_f32 v39, s47, v39
v_cvt_pk_bf16_f32 v45, v38, v39
v_mfma_f32_16x16x32_bf16 v[252:255], v[92:95], v[60:63], 0
s_waitcnt vmcnt(17) lgkmcnt(0)
s_barrier
v_accvgpr_read_b32 v36, a132
v_mfma_f32_16x16x32_bf16 v[128:131], v[96:99], v[48:51], v[128:131]
ds_read_b128 v[60:63], v25 offset:6144
v_accvgpr_read_b32 v37, a133
v_mfma_f32_16x16x32_bf16 v[132:135], v[100:103], v[48:51], v[132:135]
v_add_u32 v25, 65536, v25
v_mul_f32 v36, s47, v36
v_mfma_f32_16x16x32_bf16 v[136:139], v[104:107], v[48:51], v[136:139]
s_add_u32 m0, s92, 32768
ds_read_b64_tr_b16 v[64:65], v26 offset:16384
ds_read_b64_tr_b16 v[66:67], v26 offset:17408
v_mfma_f32_16x16x32_bf16 v[140:143], v[108:111], v[48:51], v[140:143]
buffer_load_dwordx4 v20, s[12:15], s24 offen offset:0 lds
v_mul_f32 v37, s47, v37
v_mfma_f32_16x16x32_bf16 v[144:147], v[112:115], v[48:51], v[144:147]
ds_read_b64_tr_b16 v[68:69], v27 offset:16384
ds_read_b64_tr_b16 v[70:71], v27 offset:17408
v_mfma_f32_16x16x32_bf16 v[148:151], v[116:119], v[48:51], v[148:151]
v_cvt_pk_bf16_f32 v46, v36, v37
v_accvgpr_read_b32 v38, a134
v_mfma_f32_16x16x32_bf16 v[152:155], v[120:123], v[48:51], v[152:155]
ds_read_b64_tr_b16 v[72:73], v28 offset:16384
ds_read_b64_tr_b16 v[74:75], v28 offset:17408
v_mfma_f32_16x16x32_bf16 v[156:159], v[124:127], v[48:51], v[156:159]
buffer_load_dwordx4 v21, s[12:15], s24 offen offset:1024 lds
v_accvgpr_read_b32 v39, a135
v_mfma_f32_16x16x32_bf16 v[160:163], v[96:99], v[52:55], v[160:163]
ds_read_b128 v[48:51], v24 offset:0
v_mul_f32 v38, s47, v38
v_mfma_f32_16x16x32_bf16 v[164:167], v[100:103], v[52:55], v[164:167]
v_mul_f32 v39, s47, v39
v_cvt_pk_bf16_f32 v47, v38, v39
v_mfma_f32_16x16x32_bf16 v[168:171], v[104:107], v[52:55], v[168:171]
ds_read_b64_tr_b16 v[76:77], v29 offset:16384
ds_read_b64_tr_b16 v[78:79], v29 offset:17408
v_mfma_f32_16x16x32_bf16 v[172:175], v[108:111], v[52:55], v[172:175]
buffer_load_dwordx4 v22, s[12:15], s24 offen offset:2048 lds
v_mfma_f32_16x16x32_bf16 v[176:179], v[112:115], v[52:55], v[176:179]
ds_read_b64_tr_b16 v[80:81], v30 offset:16384
ds_read_b64_tr_b16 v[82:83], v30 offset:17408
v_mfma_f32_16x16x32_bf16 v[180:183], v[116:119], v[52:55], v[180:183]
v_permlane32_swap_b32 v44, v46
v_mfma_f32_16x16x32_bf16 v[184:187], v[120:123], v[52:55], v[184:187]
ds_read_b64_tr_b16 v[84:85], v31 offset:16384
ds_read_b64_tr_b16 v[86:87], v31 offset:17408
v_mfma_f32_16x16x32_bf16 v[188:191], v[124:127], v[52:55], v[188:191]
buffer_load_dwordx4 v23, s[12:15], s24 offen offset:3072 lds
v_mfma_f32_16x16x32_bf16 v[192:195], v[96:99], v[56:59], v[192:195]
ds_read_b128 v[52:55], v24 offset:2048
v_mfma_f32_16x16x32_bf16 v[196:199], v[100:103], v[56:59], v[196:199]
v_permlane32_swap_b32 v45, v47
v_mfma_f32_16x16x32_bf16 v[200:203], v[104:107], v[56:59], v[200:203]
s_add_u32 m0, s92, 36864
ds_read_b64_tr_b16 v[88:89], v32 offset:16384
ds_read_b64_tr_b16 v[90:91], v32 offset:17408
v_mfma_f32_16x16x32_bf16 v[204:207], v[108:111], v[56:59], v[204:207]
buffer_load_dwordx4 v20, s[12:15], s25 offen offset:0 lds
v_accvgpr_write_b32 a128, v44
v_mfma_f32_16x16x32_bf16 v[208:211], v[112:115], v[56:59], v[208:211]
ds_read_b64_tr_b16 v[92:93], v33 offset:16384
ds_read_b64_tr_b16 v[94:95], v33 offset:17408
v_mfma_f32_16x16x32_bf16 v[212:215], v[116:119], v[56:59], v[212:215]
v_accvgpr_write_b32 a129, v45
v_accvgpr_write_b32 a130, v46
v_mfma_f32_16x16x32_bf16 v[216:219], v[120:123], v[56:59], v[216:219]
s_waitcnt lgkmcnt(15)
v_accvgpr_write_b32 a131, v47
v_accvgpr_read_b32 v36, a8
v_mfma_f32_16x16x32_bf16 v[220:223], v[124:127], v[56:59], v[220:223]
v_accvgpr_read_b32 v37, a9
v_mul_f32 v36, s47, v36
v_mfma_f32_16x16x32_bf16 v[224:227], v[96:99], v[60:63], v[224:227]
ds_read_b128 v[56:59], v24 offset:4096
v_mul_f32 v37, s47, v37
v_mfma_f32_16x16x32_bf16 v[228:231], v[100:103], v[60:63], v[228:231]
buffer_load_dwordx4 v21, s[12:15], s25 offen offset:1024 lds
v_cvt_pk_bf16_f32 v40, v36, v37
v_mfma_f32_16x16x32_bf16 v[232:235], v[104:107], v[60:63], v[232:235]
v_accvgpr_read_b32 v38, a10
v_accvgpr_read_b32 v39, a11
v_mfma_f32_16x16x32_bf16 v[236:239], v[108:111], v[60:63], v[236:239]
v_mul_f32 v38, s47, v38
v_mul_f32 v39, s47, v39
v_mfma_f32_16x16x32_bf16 v[240:243], v[112:115], v[60:63], v[240:243]
v_cvt_pk_bf16_f32 v41, v38, v39
v_accvgpr_read_b32 v36, a12
v_mfma_f32_16x16x32_bf16 v[244:247], v[116:119], v[60:63], v[244:247]
v_accvgpr_read_b32 v37, a13
v_mul_f32 v36, s47, v36
v_mfma_f32_16x16x32_bf16 v[248:251], v[120:123], v[60:63], v[248:251]
v_mul_f32 v37, s47, v37
v_cvt_pk_bf16_f32 v42, v36, v37
v_mfma_f32_16x16x32_bf16 v[252:255], v[124:127], v[60:63], v[252:255]
s_waitcnt lgkmcnt(0)
v_accvgpr_read_b32 v38, a14
v_accvgpr_read_b32 v39, a15
v_mfma_f32_16x16x32_bf16 v[128:131], v[64:67], v[48:51], v[128:131]
ds_read_b128 v[60:63], v24 offset:6144
v_mul_f32 v38, s47, v38
v_mfma_f32_16x16x32_bf16 v[132:135], v[68:71], v[48:51], v[132:135]
v_add_u32 v24, 32768, v24
v_mul_f32 v39, s47, v39
v_mfma_f32_16x16x32_bf16 v[136:139], v[72:75], v[48:51], v[136:139]
ds_read_b64_tr_b16 v[96:97], v26 offset:24576
ds_read_b64_tr_b16 v[98:99], v26 offset:25600
v_mfma_f32_16x16x32_bf16 v[140:143], v[76:79], v[48:51], v[140:143]
buffer_load_dwordx4 v22, s[12:15], s25 offen offset:2048 lds
v_cvt_pk_bf16_f32 v43, v38, v39
v_mfma_f32_16x16x32_bf16 v[144:147], v[80:83], v[48:51], v[144:147]
ds_read_b64_tr_b16 v[100:101], v27 offset:24576
ds_read_b64_tr_b16 v[102:103], v27 offset:25600
v_mfma_f32_16x16x32_bf16 v[148:151], v[84:87], v[48:51], v[148:151]
v_permlane32_swap_b32 v40, v42
v_mfma_f32_16x16x32_bf16 v[152:155], v[88:91], v[48:51], v[152:155]
ds_read_b64_tr_b16 v[104:105], v28 offset:24576
ds_read_b64_tr_b16 v[106:107], v28 offset:25600
v_mfma_f32_16x16x32_bf16 v[156:159], v[92:95], v[48:51], v[156:159]
buffer_load_dwordx4 v23, s[12:15], s25 offen offset:3072 lds
v_accvgpr_read_b32 v36, a136
v_mfma_f32_16x16x32_bf16 v[160:163], v[64:67], v[52:55], v[160:163]
ds_read_b128 v[48:51], v25 offset:0
v_accvgpr_read_b32 v37, a137
v_mfma_f32_16x16x32_bf16 v[164:167], v[68:71], v[52:55], v[164:167]
s_add_u32 s24, s24, 128
s_add_u32 s25, s25, 128
v_mul_f32 v36, s47, v36
v_mfma_f32_16x16x32_bf16 v[168:171], v[72:75], v[52:55], v[168:171]
s_add_u32 m0, s91, 65536
ds_read_b64_tr_b16 v[108:109], v29 offset:24576
ds_read_b64_tr_b16 v[110:111], v29 offset:25600
v_mfma_f32_16x16x32_bf16 v[172:175], v[76:79], v[52:55], v[172:175]
buffer_load_dwordx4 v16, s[16:19], s26 offen offset:0 lds
v_mul_f32 v37, s47, v37
v_mfma_f32_16x16x32_bf16 v[176:179], v[80:83], v[52:55], v[176:179]
ds_read_b64_tr_b16 v[112:113], v30 offset:24576
ds_read_b64_tr_b16 v[114:115], v30 offset:25600
v_mfma_f32_16x16x32_bf16 v[180:183], v[84:87], v[52:55], v[180:183]
v_permlane32_swap_b32 v41, v43
v_mfma_f32_16x16x32_bf16 v[184:187], v[88:91], v[52:55], v[184:187]
ds_read_b64_tr_b16 v[116:117], v31 offset:24576
ds_read_b64_tr_b16 v[118:119], v31 offset:25600
v_mfma_f32_16x16x32_bf16 v[188:191], v[92:95], v[52:55], v[188:191]
s_xor_b32 s48, s95, 1024
buffer_load_dwordx4 v17, s[16:19], s26 offen offset:1024 lds
v_mfma_f32_16x16x32_bf16 v[192:195], v[64:67], v[56:59], v[192:195]
ds_read_b128 v[52:55], v25 offset:2048
v_cvt_pk_bf16_f32 v44, v36, v37
v_mfma_f32_16x16x32_bf16 v[196:199], v[68:71], v[56:59], v[196:199]
buffer_store_dwordx4 v[40:43], v7, s[20:23], s48 offen sc1
v_accvgpr_read_b32 v38, a138
v_mfma_f32_16x16x32_bf16 v[200:203], v[72:75], v[56:59], v[200:203]
ds_read_b64_tr_b16 v[120:121], v32 offset:24576
ds_read_b64_tr_b16 v[122:123], v32 offset:25600
v_mfma_f32_16x16x32_bf16 v[204:207], v[76:79], v[56:59], v[204:207]
buffer_load_dwordx4 v18, s[16:19], s26 offen offset:2048 lds
v_accvgpr_read_b32 v39, a139
v_mfma_f32_16x16x32_bf16 v[208:211], v[80:83], v[56:59], v[208:211]
ds_read_b64_tr_b16 v[124:125], v33 offset:24576
ds_read_b64_tr_b16 v[126:127], v33 offset:25600
v_mfma_f32_16x16x32_bf16 v[212:215], v[84:87], v[56:59], v[212:215]
v_mul_f32 v38, s47, v38
v_mul_f32 v39, s47, v39
v_mfma_f32_16x16x32_bf16 v[216:219], v[88:91], v[56:59], v[216:219]
s_waitcnt lgkmcnt(15)
v_cvt_pk_bf16_f32 v45, v38, v39
v_accvgpr_read_b32 v36, a140
v_mfma_f32_16x16x32_bf16 v[220:223], v[92:95], v[56:59], v[220:223]
v_xor_b32 v26, 0x10000, v26
v_accvgpr_read_b32 v37, a141
v_mfma_f32_16x16x32_bf16 v[224:227], v[64:67], v[60:63], v[224:227]
ds_read_b128 v[56:59], v25 offset:4096
v_xor_b32 v27, 0x10000, v27
v_mfma_f32_16x16x32_bf16 v[228:231], v[68:71], v[60:63], v[228:231]
buffer_load_dwordx4 v19, s[16:19], s26 offen offset:3072 lds
v_xor_b32 v28, 0x10000, v28
v_mfma_f32_16x16x32_bf16 v[232:235], v[72:75], v[60:63], v[232:235]
v_xor_b32 v29, 0x10000, v29
v_mul_f32 v36, s47, v36
v_mfma_f32_16x16x32_bf16 v[236:239], v[76:79], v[60:63], v[236:239]
s_add_u32 s26, s26, 524288
v_xor_b32 v30, 0x10000, v30
v_mul_f32 v37, s47, v37
v_mfma_f32_16x16x32_bf16 v[240:243], v[80:83], v[60:63], v[240:243]
v_xor_b32 v31, 0x10000, v31
v_cvt_pk_bf16_f32 v46, v36, v37
v_mfma_f32_16x16x32_bf16 v[244:247], v[84:87], v[60:63], v[244:247]
v_xor_b32 v32, 0x10000, v32
v_accvgpr_read_b32 v38, a142
v_mfma_f32_16x16x32_bf16 v[248:251], v[88:91], v[60:63], v[248:251]
v_xor_b32 v33, 0x10000, v33
v_accvgpr_read_b32 v39, a143
v_mfma_f32_16x16x32_bf16 v[252:255], v[92:95], v[60:63], v[252:255]
s_waitcnt vmcnt(18) lgkmcnt(0)
s_barrier
v_mul_f32 v38, s47, v38
v_mfma_f32_16x16x32_bf16 v[128:131], v[96:99], v[48:51], v[128:131]
ds_read_b128 v[60:63], v25 offset:6144
v_mul_f32 v39, s47, v39
v_mfma_f32_16x16x32_bf16 v[132:135], v[100:103], v[48:51], v[132:135]
v_add_u32 v25, 32768, v25
v_cvt_pk_bf16_f32 v47, v38, v39
v_mfma_f32_16x16x32_bf16 v[136:139], v[104:107], v[48:51], v[136:139]
s_add_u32 m0, s92, 98304
ds_read_b64_tr_b16 v[64:65], v26 offset:0
ds_read_b64_tr_b16 v[66:67], v26 offset:1024
v_mfma_f32_16x16x32_bf16 v[140:143], v[108:111], v[48:51], v[140:143]
buffer_load_dwordx4 v20, s[12:15], s24 offen offset:0 lds
v_mfma_f32_16x16x32_bf16 v[144:147], v[112:115], v[48:51], v[144:147]
ds_read_b64_tr_b16 v[68:69], v27 offset:0
ds_read_b64_tr_b16 v[70:71], v27 offset:1024
v_mfma_f32_16x16x32_bf16 v[148:151], v[116:119], v[48:51], v[148:151]
v_permlane32_swap_b32 v44, v46
v_mfma_f32_16x16x32_bf16 v[152:155], v[120:123], v[48:51], v[152:155]
ds_read_b64_tr_b16 v[72:73], v28 offset:0
ds_read_b64_tr_b16 v[74:75], v28 offset:1024
v_mfma_f32_16x16x32_bf16 v[156:159], v[124:127], v[48:51], v[156:159]
buffer_load_dwordx4 v21, s[12:15], s24 offen offset:1024 lds
v_mfma_f32_16x16x32_bf16 v[160:163], v[96:99], v[52:55], v[160:163]
ds_read_b128 v[48:51], v24 offset:0
v_mfma_f32_16x16x32_bf16 v[164:167], v[100:103], v[52:55], v[164:167]
v_permlane32_swap_b32 v45, v47
v_mfma_f32_16x16x32_bf16 v[168:171], v[104:107], v[52:55], v[168:171]
ds_read_b64_tr_b16 v[76:77], v29 offset:0
ds_read_b64_tr_b16 v[78:79], v29 offset:1024
v_mfma_f32_16x16x32_bf16 v[172:175], v[108:111], v[52:55], v[172:175]
buffer_load_dwordx4 v22, s[12:15], s24 offen offset:2048 lds
v_accvgpr_write_b32 a132, v44
v_mfma_f32_16x16x32_bf16 v[176:179], v[112:115], v[52:55], v[176:179]
ds_read_b64_tr_b16 v[80:81], v30 offset:0
ds_read_b64_tr_b16 v[82:83], v30 offset:1024
v_mfma_f32_16x16x32_bf16 v[180:183], v[116:119], v[52:55], v[180:183]
v_accvgpr_write_b32 a133, v45
v_accvgpr_write_b32 a134, v46
v_mfma_f32_16x16x32_bf16 v[184:187], v[120:123], v[52:55], v[184:187]
ds_read_b64_tr_b16 v[84:85], v31 offset:0
ds_read_b64_tr_b16 v[86:87], v31 offset:1024
v_mfma_f32_16x16x32_bf16 v[188:191], v[124:127], v[52:55], v[188:191]
buffer_load_dwordx4 v23, s[12:15], s24 offen offset:3072 lds
v_accvgpr_write_b32 a135, v47
v_mfma_f32_16x16x32_bf16 v[192:195], v[96:99], v[56:59], v[192:195]
ds_read_b128 v[52:55], v24 offset:2048
v_accvgpr_read_b32 v36, a16
v_mfma_f32_16x16x32_bf16 v[196:199], v[100:103], v[56:59], v[196:199]
v_accvgpr_read_b32 v37, a17
v_mul_f32 v36, s47, v36
v_mfma_f32_16x16x32_bf16 v[200:203], v[104:107], v[56:59], v[200:203]
s_add_u32 m0, s92, 102400
ds_read_b64_tr_b16 v[88:89], v32 offset:0
ds_read_b64_tr_b16 v[90:91], v32 offset:1024
v_mfma_f32_16x16x32_bf16 v[204:207], v[108:111], v[56:59], v[204:207]
buffer_load_dwordx4 v20, s[12:15], s25 offen offset:0 lds
v_mul_f32 v37, s47, v37
v_mfma_f32_16x16x32_bf16 v[208:211], v[112:115], v[56:59], v[208:211]
ds_read_b64_tr_b16 v[92:93], v33 offset:0
ds_read_b64_tr_b16 v[94:95], v33 offset:1024
v_mfma_f32_16x16x32_bf16 v[212:215], v[116:119], v[56:59], v[212:215]
v_cvt_pk_bf16_f32 v40, v36, v37
v_accvgpr_read_b32 v38, a18
v_mfma_f32_16x16x32_bf16 v[216:219], v[120:123], v[56:59], v[216:219]
s_waitcnt lgkmcnt(15)
v_accvgpr_read_b32 v39, a19
v_mul_f32 v38, s47, v38
v_mfma_f32_16x16x32_bf16 v[220:223], v[124:127], v[56:59], v[220:223]
v_mul_f32 v39, s47, v39
v_cvt_pk_bf16_f32 v41, v38, v39
v_mfma_f32_16x16x32_bf16 v[224:227], v[96:99], v[60:63], v[224:227]
ds_read_b128 v[56:59], v24 offset:4096
v_accvgpr_read_b32 v36, a20
v_mfma_f32_16x16x32_bf16 v[228:231], v[100:103], v[60:63], v[228:231]
buffer_load_dwordx4 v21, s[12:15], s25 offen offset:1024 lds
v_accvgpr_read_b32 v37, a21
v_mfma_f32_16x16x32_bf16 v[232:235], v[104:107], v[60:63], v[232:235]
v_mul_f32 v36, s47, v36
v_mul_f32 v37, s47, v37
v_mfma_f32_16x16x32_bf16 v[236:239], v[108:111], v[60:63], v[236:239]
v_cvt_pk_bf16_f32 v42, v36, v37
v_accvgpr_read_b32 v38, a22
v_mfma_f32_16x16x32_bf16 v[240:243], v[112:115], v[60:63], v[240:243]
v_accvgpr_read_b32 v39, a23
v_mul_f32 v38, s47, v38
v_mfma_f32_16x16x32_bf16 v[244:247], v[116:119], v[60:63], v[244:247]
v_mul_f32 v39, s47, v39
v_cvt_pk_bf16_f32 v43, v38, v39
v_mfma_f32_16x16x32_bf16 v[248:251], v[120:123], v[60:63], v[248:251]
v_permlane32_swap_b32 v40, v42
v_mfma_f32_16x16x32_bf16 v[252:255], v[124:127], v[60:63], v[252:255]
s_waitcnt lgkmcnt(0)
v_permlane32_swap_b32 v41, v43
v_mfma_f32_16x16x32_bf16 v[128:131], v[64:67], v[48:51], v[128:131]
s_xor_b32 s48, s95, 2048
ds_read_b128 v[60:63], v24 offset:6144
v_mfma_f32_16x16x32_bf16 v[132:135], v[68:71], v[48:51], v[132:135]
buffer_store_dwordx4 v[40:43], v7, s[20:23], s48 offen sc1
v_add_u32 v24, 4294868992, v24
v_mfma_f32_16x16x32_bf16 v[136:139], v[72:75], v[48:51], v[136:139]
ds_read_b64_tr_b16 v[96:97], v26 offset:8192
ds_read_b64_tr_b16 v[98:99], v26 offset:9216
v_mfma_f32_16x16x32_bf16 v[140:143], v[76:79], v[48:51], v[140:143]
buffer_load_dwordx4 v22, s[12:15], s25 offen offset:2048 lds
v_accvgpr_read_b32 v36, a144
v_mfma_f32_16x16x32_bf16 v[144:147], v[80:83], v[48:51], v[144:147]
ds_read_b64_tr_b16 v[100:101], v27 offset:8192
ds_read_b64_tr_b16 v[102:103], v27 offset:9216
v_mfma_f32_16x16x32_bf16 v[148:151], v[84:87], v[48:51], v[148:151]
v_accvgpr_read_b32 v37, a145
v_mul_f32 v36, s47, v36
v_mfma_f32_16x16x32_bf16 v[152:155], v[88:91], v[48:51], v[152:155]
ds_read_b64_tr_b16 v[104:105], v28 offset:8192
ds_read_b64_tr_b16 v[106:107], v28 offset:9216
v_mfma_f32_16x16x32_bf16 v[156:159], v[92:95], v[48:51], v[156:159]
buffer_load_dwordx4 v23, s[12:15], s25 offen offset:3072 lds
v_mul_f32 v37, s47, v37
v_mfma_f32_16x16x32_bf16 v[160:163], v[64:67], v[52:55], v[160:163]
ds_read_b128 v[48:51], v25 offset:0
v_cvt_pk_bf16_f32 v44, v36, v37
v_mfma_f32_16x16x32_bf16 v[164:167], v[68:71], v[52:55], v[164:167]
s_add_u32 s24, s24, 128
s_add_u32 s25, s25, 128
v_accvgpr_read_b32 v38, a146
v_mfma_f32_16x16x32_bf16 v[168:171], v[72:75], v[52:55], v[168:171]
s_add_u32 m0, s91, 81920
ds_read_b64_tr_b16 v[108:109], v29 offset:8192
ds_read_b64_tr_b16 v[110:111], v29 offset:9216
v_mfma_f32_16x16x32_bf16 v[172:175], v[76:79], v[52:55], v[172:175]
buffer_load_dwordx4 v16, s[16:19], s26 offen offset:0 lds
v_accvgpr_read_b32 v39, a147
v_mfma_f32_16x16x32_bf16 v[176:179], v[80:83], v[52:55], v[176:179]
ds_read_b64_tr_b16 v[112:113], v30 offset:8192
ds_read_b64_tr_b16 v[114:115], v30 offset:9216
v_mfma_f32_16x16x32_bf16 v[180:183], v[84:87], v[52:55], v[180:183]
v_mul_f32 v38, s47, v38
v_mul_f32 v39, s47, v39
v_mfma_f32_16x16x32_bf16 v[184:187], v[88:91], v[52:55], v[184:187]
ds_read_b64_tr_b16 v[116:117], v31 offset:8192
ds_read_b64_tr_b16 v[118:119], v31 offset:9216
v_mfma_f32_16x16x32_bf16 v[188:191], v[92:95], v[52:55], v[188:191]
buffer_load_dwordx4 v17, s[16:19], s26 offen offset:1024 lds
v_cvt_pk_bf16_f32 v45, v38, v39
v_mfma_f32_16x16x32_bf16 v[192:195], v[64:67], v[56:59], v[192:195]
ds_read_b128 v[52:55], v25 offset:2048
v_accvgpr_read_b32 v36, a148
v_mfma_f32_16x16x32_bf16 v[196:199], v[68:71], v[56:59], v[196:199]
v_accvgpr_read_b32 v37, a149
v_mul_f32 v36, s47, v36
v_mfma_f32_16x16x32_bf16 v[200:203], v[72:75], v[56:59], v[200:203]
ds_read_b64_tr_b16 v[120:121], v32 offset:8192
ds_read_b64_tr_b16 v[122:123], v32 offset:9216
v_mfma_f32_16x16x32_bf16 v[204:207], v[76:79], v[56:59], v[204:207]
buffer_load_dwordx4 v18, s[16:19], s26 offen offset:2048 lds
v_mul_f32 v37, s47, v37
v_mfma_f32_16x16x32_bf16 v[208:211], v[80:83], v[56:59], v[208:211]
ds_read_b64_tr_b16 v[124:125], v33 offset:8192
ds_read_b64_tr_b16 v[126:127], v33 offset:9216
v_mfma_f32_16x16x32_bf16 v[212:215], v[84:87], v[56:59], v[212:215]
v_cvt_pk_bf16_f32 v46, v36, v37
v_accvgpr_read_b32 v38, a150
v_mfma_f32_16x16x32_bf16 v[216:219], v[88:91], v[56:59], v[216:219]
s_waitcnt lgkmcnt(15)
v_accvgpr_read_b32 v39, a151
v_mul_f32 v38, s47, v38
v_mfma_f32_16x16x32_bf16 v[220:223], v[92:95], v[56:59], v[220:223]
v_mul_f32 v39, s47, v39
v_cvt_pk_bf16_f32 v47, v38, v39
v_mfma_f32_16x16x32_bf16 v[224:227], v[64:67], v[60:63], v[224:227]
ds_read_b128 v[56:59], v25 offset:4096
v_mfma_f32_16x16x32_bf16 v[228:231], v[68:71], v[60:63], v[228:231]
buffer_load_dwordx4 v19, s[16:19], s26 offen offset:3072 lds
v_mfma_f32_16x16x32_bf16 v[232:235], v[72:75], v[60:63], v[232:235]
v_permlane32_swap_b32 v44, v46
v_mfma_f32_16x16x32_bf16 v[236:239], v[76:79], v[60:63], v[236:239]
s_add_u32 s26, s26, 524288
v_permlane32_swap_b32 v45, v47
v_mfma_f32_16x16x32_bf16 v[240:243], v[80:83], v[60:63], v[240:243]
v_accvgpr_write_b32 a136, v44
v_accvgpr_write_b32 a137, v45
v_mfma_f32_16x16x32_bf16 v[244:247], v[84:87], v[60:63], v[244:247]
v_accvgpr_write_b32 a138, v46
v_accvgpr_write_b32 a139, v47
v_mfma_f32_16x16x32_bf16 v[248:251], v[88:91], v[60:63], v[248:251]
v_accvgpr_read_b32 v36, a24
v_accvgpr_read_b32 v37, a25
v_mfma_f32_16x16x32_bf16 v[252:255], v[92:95], v[60:63], v[252:255]
s_waitcnt vmcnt(18) lgkmcnt(0)
s_barrier
v_mul_f32 v36, s47, v36
v_mfma_f32_16x16x32_bf16 v[128:131], v[96:99], v[48:51], v[128:131]
ds_read_b128 v[60:63], v25 offset:6144
v_mul_f32 v37, s47, v37
v_mfma_f32_16x16x32_bf16 v[132:135], v[100:103], v[48:51], v[132:135]
v_add_u32 v25, 4294868992, v25
v_cvt_pk_bf16_f32 v40, v36, v37
v_mfma_f32_16x16x32_bf16 v[136:139], v[104:107], v[48:51], v[136:139]
s_add_u32 m0, s92, 131072
ds_read_b64_tr_b16 v[64:65], v26 offset:16384
ds_read_b64_tr_b16 v[66:67], v26 offset:17408
v_mfma_f32_16x16x32_bf16 v[140:143], v[108:111], v[48:51], v[140:143]
buffer_load_dwordx4 v20, s[12:15], s24 offen offset:0 lds
v_accvgpr_read_b32 v38, a26
v_mfma_f32_16x16x32_bf16 v[144:147], v[112:115], v[48:51], v[144:147]
ds_read_b64_tr_b16 v[68:69], v27 offset:16384
ds_read_b64_tr_b16 v[70:71], v27 offset:17408
v_mfma_f32_16x16x32_bf16 v[148:151], v[116:119], v[48:51], v[148:151]
v_accvgpr_read_b32 v39, a27
v_mul_f32 v38, s47, v38
v_mfma_f32_16x16x32_bf16 v[152:155], v[120:123], v[48:51], v[152:155]
ds_read_b64_tr_b16 v[72:73], v28 offset:16384
ds_read_b64_tr_b16 v[74:75], v28 offset:17408
v_mfma_f32_16x16x32_bf16 v[156:159], v[124:127], v[48:51], v[156:159]
buffer_load_dwordx4 v21, s[12:15], s24 offen offset:1024 lds
v_mul_f32 v39, s47, v39
v_mfma_f32_16x16x32_bf16 v[160:163], v[96:99], v[52:55], v[160:163]
ds_read_b128 v[48:51], v24 offset:0
v_cvt_pk_bf16_f32 v41, v38, v39
v_mfma_f32_16x16x32_bf16 v[164:167], v[100:103], v[52:55], v[164:167]
v_accvgpr_read_b32 v36, a28
v_accvgpr_read_b32 v37, a29
v_mfma_f32_16x16x32_bf16 v[168:171], v[104:107], v[52:55], v[168:171]
ds_read_b64_tr_b16 v[76:77], v29 offset:16384
ds_read_b64_tr_b16 v[78:79], v29 offset:17408
v_mfma_f32_16x16x32_bf16 v[172:175], v[108:111], v[52:55], v[172:175]
buffer_load_dwordx4 v22, s[12:15], s24 offen offset:2048 lds
v_mul_f32 v36, s47, v36
v_mfma_f32_16x16x32_bf16 v[176:179], v[112:115], v[52:55], v[176:179]
ds_read_b64_tr_b16 v[80:81], v30 offset:16384
ds_read_b64_tr_b16 v[82:83], v30 offset:17408
v_mfma_f32_16x16x32_bf16 v[180:183], v[116:119], v[52:55], v[180:183]
v_mul_f32 v37, s47, v37
v_cvt_pk_bf16_f32 v42, v36, v37
v_mfma_f32_16x16x32_bf16 v[184:187], v[120:123], v[52:55], v[184:187]
ds_read_b64_tr_b16 v[84:85], v31 offset:16384
ds_read_b64_tr_b16 v[86:87], v31 offset:17408
v_mfma_f32_16x16x32_bf16 v[188:191], v[124:127], v[52:55], v[188:191]
buffer_load_dwordx4 v23, s[12:15], s24 offen offset:3072 lds
v_accvgpr_read_b32 v38, a30
v_mfma_f32_16x16x32_bf16 v[192:195], v[96:99], v[56:59], v[192:195]
ds_read_b128 v[52:55], v24 offset:2048
v_accvgpr_read_b32 v39, a31
v_mfma_f32_16x16x32_bf16 v[196:199], v[100:103], v[56:59], v[196:199]
v_mul_f32 v38, s47, v38
v_mul_f32 v39, s47, v39
v_mfma_f32_16x16x32_bf16 v[200:203], v[104:107], v[56:59], v[200:203]
s_add_u32 m0, s92, 135168
ds_read_b64_tr_b16 v[88:89], v32 offset:16384
ds_read_b64_tr_b16 v[90:91], v32 offset:17408
v_mfma_f32_16x16x32_bf16 v[204:207], v[108:111], v[56:59], v[204:207]
buffer_load_dwordx4 v20, s[12:15], s25 offen offset:0 lds
v_cvt_pk_bf16_f32 v43, v38, v39
v_mfma_f32_16x16x32_bf16 v[208:211], v[112:115], v[56:59], v[208:211]
ds_read_b64_tr_b16 v[92:93], v33 offset:16384
ds_read_b64_tr_b16 v[94:95], v33 offset:17408
v_mfma_f32_16x16x32_bf16 v[212:215], v[116:119], v[56:59], v[212:215]
v_permlane32_swap_b32 v40, v42
v_mfma_f32_16x16x32_bf16 v[216:219], v[120:123], v[56:59], v[216:219]
s_waitcnt lgkmcnt(15)
v_permlane32_swap_b32 v41, v43
v_mfma_f32_16x16x32_bf16 v[220:223], v[124:127], v[56:59], v[220:223]
s_xor_b32 s48, s95, 3072
buffer_store_dwordx4 v[40:43], v7, s[20:23], s48 offen sc1
v_mfma_f32_16x16x32_bf16 v[224:227], v[96:99], v[60:63], v[224:227]
ds_read_b128 v[56:59], v24 offset:4096
v_accvgpr_read_b32 v36, a152
v_mfma_f32_16x16x32_bf16 v[228:231], v[100:103], v[60:63], v[228:231]
buffer_load_dwordx4 v21, s[12:15], s25 offen offset:1024 lds
v_accvgpr_read_b32 v37, a153
v_mfma_f32_16x16x32_bf16 v[232:235], v[104:107], v[60:63], v[232:235]
v_mul_f32 v36, s47, v36
v_mul_f32 v37, s47, v37
v_mfma_f32_16x16x32_bf16 v[236:239], v[108:111], v[60:63], v[236:239]
v_cvt_pk_bf16_f32 v44, v36, v37
v_accvgpr_read_b32 v38, a154
v_mfma_f32_16x16x32_bf16 v[240:243], v[112:115], v[60:63], v[240:243]
v_accvgpr_read_b32 v39, a155
v_mul_f32 v38, s47, v38
v_mfma_f32_16x16x32_bf16 v[244:247], v[116:119], v[60:63], v[244:247]
v_mul_f32 v39, s47, v39
v_cvt_pk_bf16_f32 v45, v38, v39
v_mfma_f32_16x16x32_bf16 v[248:251], v[120:123], v[60:63], v[248:251]
v_accvgpr_read_b32 v36, a156
v_accvgpr_read_b32 v37, a157
v_mfma_f32_16x16x32_bf16 v[252:255], v[124:127], v[60:63], v[252:255]
s_waitcnt lgkmcnt(0)
v_mul_f32 v36, s47, v36
v_mul_f32 v37, s47, v37
v_mfma_f32_16x16x32_bf16 v[128:131], v[64:67], v[48:51], v[128:131]
ds_read_b128 v[60:63], v24 offset:6144
v_cvt_pk_bf16_f32 v46, v36, v37
v_mfma_f32_16x16x32_bf16 v[132:135], v[68:71], v[48:51], v[132:135]
v_add_u32 v24, 65536, v24
v_accvgpr_read_b32 v38, a158
v_mfma_f32_16x16x32_bf16 v[136:139], v[72:75], v[48:51], v[136:139]
ds_read_b64_tr_b16 v[96:97], v26 offset:24576
ds_read_b64_tr_b16 v[98:99], v26 offset:25600
v_mfma_f32_16x16x32_bf16 v[140:143], v[76:79], v[48:51], v[140:143]
buffer_load_dwordx4 v22, s[12:15], s25 offen offset:2048 lds
v_accvgpr_read_b32 v39, a159
v_mfma_f32_16x16x32_bf16 v[144:147], v[80:83], v[48:51], v[144:147]
ds_read_b64_tr_b16 v[100:101], v27 offset:24576
ds_read_b64_tr_b16 v[102:103], v27 offset:25600
.Lpad5:
s_cbranch_execz .Ltch1
v_mfma_f32_16x16x32_bf16 v[148:151], v[84:87], v[48:51], v[148:151]
v_mul_f32 v38, s47, v38
v_mul_f32 v39, s47, v39
v_mfma_f32_16x16x32_bf16 v[152:155], v[88:91], v[48:51], v[152:155]
ds_read_b64_tr_b16 v[104:105], v28 offset:24576
ds_read_b64_tr_b16 v[106:107], v28 offset:25600
v_mfma_f32_16x16x32_bf16 v[156:159], v[92:95], v[48:51], v[156:159]
buffer_load_dwordx4 v23, s[12:15], s25 offen offset:3072 lds
v_cvt_pk_bf16_f32 v47, v38, v39
v_mfma_f32_16x16x32_bf16 v[160:163], v[64:67], v[52:55], v[160:163]
ds_read_b128 v[48:51], v25 offset:0
v_mfma_f32_16x16x32_bf16 v[164:167], v[68:71], v[52:55], v[164:167]
s_add_u32 s24, s24, 128
s_add_u32 s25, s25, 128
v_mfma_f32_16x16x32_bf16 v[168:171], v[72:75], v[52:55], v[168:171]
s_add_u32 m0, s91, 0
ds_read_b64_tr_b16 v[108:109], v29 offset:24576
ds_read_b64_tr_b16 v[110:111], v29 offset:25600
v_mfma_f32_16x16x32_bf16 v[172:175], v[76:79], v[52:55], v[172:175]
buffer_load_dwordx4 v16, s[16:19], s26 offen offset:0 lds
v_mfma_f32_16x16x32_bf16 v[176:179], v[80:83], v[52:55], v[176:179]
ds_read_b64_tr_b16 v[112:113], v30 offset:24576
ds_read_b64_tr_b16 v[114:115], v30 offset:25600
v_mfma_f32_16x16x32_bf16 v[180:183], v[84:87], v[52:55], v[180:183]
v_permlane32_swap_b32 v44, v46
v_mfma_f32_16x16x32_bf16 v[184:187], v[88:91], v[52:55], v[184:187]
ds_read_b64_tr_b16 v[116:117], v31 offset:24576
ds_read_b64_tr_b16 v[118:119], v31 offset:25600
v_mfma_f32_16x16x32_bf16 v[188:191], v[92:95], v[52:55], v[188:191]
buffer_load_dwordx4 v17, s[16:19], s26 offen offset:1024 lds
v_mfma_f32_16x16x32_bf16 v[192:195], v[64:67], v[56:59], v[192:195]
ds_read_b128 v[52:55], v25 offset:2048
v_mfma_f32_16x16x32_bf16 v[196:199], v[68:71], v[56:59], v[196:199]
v_permlane32_swap_b32 v45, v47
v_mfma_f32_16x16x32_bf16 v[200:203], v[72:75], v[56:59], v[200:203]
ds_read_b64_tr_b16 v[120:121], v32 offset:24576
ds_read_b64_tr_b16 v[122:123], v32 offset:25600
v_mfma_f32_16x16x32_bf16 v[204:207], v[76:79], v[56:59], v[204:207]
buffer_load_dwordx4 v18, s[16:19], s26 offen offset:2048 lds
v_accvgpr_write_b32 a140, v44
v_mfma_f32_16x16x32_bf16 v[208:211], v[80:83], v[56:59], v[208:211]
ds_read_b64_tr_b16 v[124:125], v33 offset:24576
ds_read_b64_tr_b16 v[126:127], v33 offset:25600
v_mfma_f32_16x16x32_bf16 v[212:215], v[84:87], v[56:59], v[212:215]
v_accvgpr_write_b32 a141, v45
v_accvgpr_write_b32 a142, v46
v_mfma_f32_16x16x32_bf16 v[216:219], v[88:91], v[56:59], v[216:219]
s_waitcnt lgkmcnt(15)
v_accvgpr_write_b32 a143, v47
v_accvgpr_read_b32 v36, a32
v_mfma_f32_16x16x32_bf16 v[220:223], v[92:95], v[56:59], v[220:223]
v_xor_b32 v26, 0x10000, v26
v_accvgpr_read_b32 v37, a33
v_mfma_f32_16x16x32_bf16 v[224:227], v[64:67], v[60:63], v[224:227]
ds_read_b128 v[56:59], v25 offset:4096
v_xor_b32 v27, 0x10000, v27
v_mfma_f32_16x16x32_bf16 v[228:231], v[68:71], v[60:63], v[228:231]
buffer_load_dwordx4 v19, s[16:19], s26 offen offset:3072 lds
v_xor_b32 v28, 0x10000, v28
v_mfma_f32_16x16x32_bf16 v[232:235], v[72:75], v[60:63], v[232:235]
v_xor_b32 v29, 0x10000, v29
v_mul_f32 v36, s47, v36
v_mfma_f32_16x16x32_bf16 v[236:239], v[76:79], v[60:63], v[236:239]
s_add_u32 s26, s26, 524288
v_xor_b32 v30, 0x10000, v30
v_mul_f32 v37, s47, v37
v_mfma_f32_16x16x32_bf16 v[240:243], v[80:83], v[60:63], v[240:243]
v_xor_b32 v31, 0x10000, v31
v_cvt_pk_bf16_f32 v40, v36, v37
v_mfma_f32_16x16x32_bf16 v[244:247], v[84:87], v[60:63], v[244:247]
v_xor_b32 v32, 0x10000, v32
v_accvgpr_read_b32 v38, a34
v_mfma_f32_16x16x32_bf16 v[248:251], v[88:91], v[60:63], v[248:251]
v_xor_b32 v33, 0x10000, v33
v_accvgpr_read_b32 v39, a35
v_mfma_f32_16x16x32_bf16 v[252:255], v[92:95], v[60:63], v[252:255]
s_waitcnt vmcnt(17) lgkmcnt(0)
s_barrier
v_mul_f32 v38, s47, v38
v_mfma_f32_16x16x32_bf16 v[128:131], v[96:99], v[48:51], v[128:131]
ds_read_b128 v[60:63], v25 offset:6144
v_mul_f32 v39, s47, v39
v_mfma_f32_16x16x32_bf16 v[132:135], v[100:103], v[48:51], v[132:135]
v_add_u32 v25, 65536, v25
v_cvt_pk_bf16_f32 v41, v38, v39
v_mfma_f32_16x16x32_bf16 v[136:139], v[104:107], v[48:51], v[136:139]
s_add_u32 m0, s92, 32768
ds_read_b64_tr_b16 v[64:65], v26 offset:0
ds_read_b64_tr_b16 v[66:67], v26 offset:1024
v_mfma_f32_16x16x32_bf16 v[140:143], v[108:111], v[48:51], v[140:143]
buffer_load_dwordx4 v20, s[12:15], s24 offen offset:0 lds
v_accvgpr_read_b32 v36, a36
v_mfma_f32_16x16x32_bf16 v[144:147], v[112:115], v[48:51], v[144:147]
ds_read_b64_tr_b16 v[68:69], v27 offset:0
ds_read_b64_tr_b16 v[70:71], v27 offset:1024
v_mfma_f32_16x16x32_bf16 v[148:151], v[116:119], v[48:51], v[148:151]
v_accvgpr_read_b32 v37, a37
v_mul_f32 v36, s47, v36
v_mfma_f32_16x16x32_bf16 v[152:155], v[120:123], v[48:51], v[152:155]
ds_read_b64_tr_b16 v[72:73], v28 offset:0
ds_read_b64_tr_b16 v[74:75], v28 offset:1024
v_mfma_f32_16x16x32_bf16 v[156:159], v[124:127], v[48:51], v[156:159]
buffer_load_dwordx4 v21, s[12:15], s24 offen offset:1024 lds
v_mul_f32 v37, s47, v37
v_mfma_f32_16x16x32_bf16 v[160:163], v[96:99], v[52:55], v[160:163]
ds_read_b128 v[48:51], v24 offset:0
v_cvt_pk_bf16_f32 v42, v36, v37
v_mfma_f32_16x16x32_bf16 v[164:167], v[100:103], v[52:55], v[164:167]
v_accvgpr_read_b32 v38, a38
v_accvgpr_read_b32 v39, a39
v_mfma_f32_16x16x32_bf16 v[168:171], v[104:107], v[52:55], v[168:171]
ds_read_b64_tr_b16 v[76:77], v29 offset:0
ds_read_b64_tr_b16 v[78:79], v29 offset:1024
v_mfma_f32_16x16x32_bf16 v[172:175], v[108:111], v[52:55], v[172:175]
buffer_load_dwordx4 v22, s[12:15], s24 offen offset:2048 lds
v_mul_f32 v38, s47, v38
v_mfma_f32_16x16x32_bf16 v[176:179], v[112:115], v[52:55], v[176:179]
ds_read_b64_tr_b16 v[80:81], v30 offset:0
ds_read_b64_tr_b16 v[82:83], v30 offset:1024
v_mfma_f32_16x16x32_bf16 v[180:183], v[116:119], v[52:55], v[180:183]
v_mul_f32 v39, s47, v39
v_cvt_pk_bf16_f32 v43, v38, v39
v_mfma_f32_16x16x32_bf16 v[184:187], v[120:123], v[52:55], v[184:187]
ds_read_b64_tr_b16 v[84:85], v31 offset:0
ds_read_b64_tr_b16 v[86:87], v31 offset:1024
v_mfma_f32_16x16x32_bf16 v[188:191], v[124:127], v[52:55], v[188:191]
buffer_load_dwordx4 v23, s[12:15], s24 offen offset:3072 lds
v_accvgpr_read_b32 v36, a160
v_mfma_f32_16x16x32_bf16 v[192:195], v[96:99], v[56:59], v[192:195]
ds_read_b128 v[52:55], v24 offset:2048
v_accvgpr_read_b32 v37, a161
v_mfma_f32_16x16x32_bf16 v[196:199], v[100:103], v[56:59], v[196:199]
v_permlane32_swap_b32 v40, v42
v_mfma_f32_16x16x32_bf16 v[200:203], v[104:107], v[56:59], v[200:203]
s_add_u32 m0, s92, 36864
ds_read_b64_tr_b16 v[88:89], v32 offset:0
ds_read_b64_tr_b16 v[90:91], v32 offset:1024
v_mfma_f32_16x16x32_bf16 v[204:207], v[108:111], v[56:59], v[204:207]
buffer_load_dwordx4 v20, s[12:15], s25 offen offset:0 lds
v_mul_f32 v36, s47, v36
v_mfma_f32_16x16x32_bf16 v[208:211], v[112:115], v[56:59], v[208:211]
ds_read_b64_tr_b16 v[92:93], v33 offset:0
ds_read_b64_tr_b16 v[94:95], v33 offset:1024
v_mfma_f32_16x16x32_bf16 v[212:215], v[116:119], v[56:59], v[212:215]
v_permlane32_swap_b32 v41, v43
v_mfma_f32_16x16x32_bf16 v[216:219], v[120:123], v[56:59], v[216:219]
s_waitcnt lgkmcnt(15)
s_xor_b32 s48, s95, 65536
v_mul_f32 v37, s47, v37
v_mfma_f32_16x16x32_bf16 v[220:223], v[124:127], v[56:59], v[220:223]
buffer_store_dwordx4 v[40:43], v7, s[20:23], s48 offen sc1
v_cvt_pk_bf16_f32 v44, v36, v37
v_mfma_f32_16x16x32_bf16 v[224:227], v[96:99], v[60:63], v[224:227]
ds_read_b128 v[56:59], v24 offset:4096
v_accvgpr_read_b32 v38, a162
v_mfma_f32_16x16x32_bf16 v[228:231], v[100:103], v[60:63], v[228:231]
buffer_load_dwordx4 v21, s[12:15], s25 offen offset:1024 lds
v_accvgpr_read_b32 v39, a163
v_mfma_f32_16x16x32_bf16 v[232:235], v[104:107], v[60:63], v[232:235]
v_mul_f32 v38, s47, v38
v_mul_f32 v39, s47, v39
v_mfma_f32_16x16x32_bf16 v[236:239], v[108:111], v[60:63], v[236:239]
v_cvt_pk_bf16_f32 v45, v38, v39
v_accvgpr_read_b32 v36, a164
v_mfma_f32_16x16x32_bf16 v[240:243], v[112:115], v[60:63], v[240:243]
v_accvgpr_read_b32 v37, a165
v_mul_f32 v36, s47, v36
v_mfma_f32_16x16x32_bf16 v[244:247], v[116:119], v[60:63], v[244:247]
v_mul_f32 v37, s47, v37
v_cvt_pk_bf16_f32 v46, v36, v37
v_mfma_f32_16x16x32_bf16 v[248:251], v[120:123], v[60:63], v[248:251]
v_accvgpr_read_b32 v38, a166
v_accvgpr_read_b32 v39, a167
v_mfma_f32_16x16x32_bf16 v[252:255], v[124:127], v[60:63], v[252:255]
s_waitcnt lgkmcnt(0)
v_mul_f32 v38, s47, v38
v_mul_f32 v39, s47, v39
v_mfma_f32_16x16x32_bf16 v[128:131], v[64:67], v[48:51], v[128:131]
ds_read_b128 v[60:63], v24 offset:6144
v_cvt_pk_bf16_f32 v47, v38, v39
v_mfma_f32_16x16x32_bf16 v[132:135], v[68:71], v[48:51], v[132:135]
v_add_u32 v24, 32768, v24
v_mfma_f32_16x16x32_bf16 v[136:139], v[72:75], v[48:51], v[136:139]
ds_read_b64_tr_b16 v[96:97], v26 offset:8192
ds_read_b64_tr_b16 v[98:99], v26 offset:9216
v_mfma_f32_16x16x32_bf16 v[140:143], v[76:79], v[48:51], v[140:143]
buffer_load_dwordx4 v22, s[12:15], s25 offen offset:2048 lds
v_mfma_f32_16x16x32_bf16 v[144:147], v[80:83], v[48:51], v[144:147]
ds_read_b64_tr_b16 v[100:101], v27 offset:8192
ds_read_b64_tr_b16 v[102:103], v27 offset:9216
v_mfma_f32_16x16x32_bf16 v[148:151], v[84:87], v[48:51], v[148:151]
v_permlane32_swap_b32 v44, v46
v_mfma_f32_16x16x32_bf16 v[152:155], v[88:91], v[48:51], v[152:155]
ds_read_b64_tr_b16 v[104:105], v28 offset:8192
ds_read_b64_tr_b16 v[106:107], v28 offset:9216
v_mfma_f32_16x16x32_bf16 v[156:159], v[92:95], v[48:51], v[156:159]
buffer_load_dwordx4 v23, s[12:15], s25 offen offset:3072 lds
v_mfma_f32_16x16x32_bf16 v[160:163], v[64:67], v[52:55], v[160:163]
ds_read_b128 v[48:51], v25 offset:0
v_mfma_f32_16x16x32_bf16 v[164:167], v[68:71], v[52:55], v[164:167]
s_add_u32 s24, s24, 128
s_add_u32 s25, s25, 128
buffer_load_dword v35, v4, s[96:99], 0 offen
v_mfma_f32_16x16x32_bf16 v[168:171], v[72:75], v[52:55], v[168:171]
s_add_u32 m0, s91, 16384
ds_read_b64_tr_b16 v[108:109], v29 offset:8192
ds_read_b64_tr_b16 v[110:111], v29 offset:9216
v_mfma_f32_16x16x32_bf16 v[172:175], v[76:79], v[52:55], v[172:175]
buffer_load_dwordx4 v16, s[16:19], s26 offen offset:0 lds
v_mfma_f32_16x16x32_bf16 v[176:179], v[80:83], v[52:55], v[176:179]
ds_read_b64_tr_b16 v[112:113], v30 offset:8192
ds_read_b64_tr_b16 v[114:115], v30 offset:9216
v_mfma_f32_16x16x32_bf16 v[180:183], v[84:87], v[52:55], v[180:183]
v_permlane32_swap_b32 v45, v47
v_mfma_f32_16x16x32_bf16 v[184:187], v[88:91], v[52:55], v[184:187]
ds_read_b64_tr_b16 v[116:117], v31 offset:8192
ds_read_b64_tr_b16 v[118:119], v31 offset:9216
v_mfma_f32_16x16x32_bf16 v[188:191], v[92:95], v[52:55], v[188:191]
buffer_load_dwordx4 v17, s[16:19], s26 offen offset:1024 lds
v_accvgpr_write_b32 a160, v44
v_mfma_f32_16x16x32_bf16 v[192:195], v[64:67], v[56:59], v[192:195]
ds_read_b128 v[52:55], v25 offset:2048
v_accvgpr_write_b32 a161, v45
v_mfma_f32_16x16x32_bf16 v[196:199], v[68:71], v[56:59], v[196:199]
v_accvgpr_write_b32 a162, v46
v_accvgpr_write_b32 a163, v47
v_mfma_f32_16x16x32_bf16 v[200:203], v[72:75], v[56:59], v[200:203]
ds_read_b64_tr_b16 v[120:121], v32 offset:8192
ds_read_b64_tr_b16 v[122:123], v32 offset:9216
v_mfma_f32_16x16x32_bf16 v[204:207], v[76:79], v[56:59], v[204:207]
buffer_load_dwordx4 v18, s[16:19], s26 offen offset:2048 lds
v_accvgpr_read_b32 v36, a40
v_mfma_f32_16x16x32_bf16 v[208:211], v[80:83], v[56:59], v[208:211]
ds_read_b64_tr_b16 v[124:125], v33 offset:8192
ds_read_b64_tr_b16 v[126:127], v33 offset:9216
v_mfma_f32_16x16x32_bf16 v[212:215], v[84:87], v[56:59], v[212:215]
v_accvgpr_read_b32 v37, a41
v_mul_f32 v36, s47, v36
v_mfma_f32_16x16x32_bf16 v[216:219], v[88:91], v[56:59], v[216:219]
s_waitcnt lgkmcnt(15)
v_mul_f32 v37, s47, v37
v_cvt_pk_bf16_f32 v40, v36, v37
v_mfma_f32_16x16x32_bf16 v[220:223], v[92:95], v[56:59], v[220:223]
v_accvgpr_read_b32 v38, a42
v_accvgpr_read_b32 v39, a43
v_mfma_f32_16x16x32_bf16 v[224:227], v[64:67], v[60:63], v[224:227]
ds_read_b128 v[56:59], v25 offset:4096
v_mul_f32 v38, s47, v38
v_mfma_f32_16x16x32_bf16 v[228:231], v[68:71], v[60:63], v[228:231]
buffer_load_dwordx4 v19, s[16:19], s26 offen offset:3072 lds
v_mul_f32 v39, s47, v39
v_mfma_f32_16x16x32_bf16 v[232:235], v[72:75], v[60:63], v[232:235]
v_cvt_pk_bf16_f32 v41, v38, v39
v_accvgpr_read_b32 v36, a44
v_mfma_f32_16x16x32_bf16 v[236:239], v[76:79], v[60:63], v[236:239]
s_add_u32 s26, s26, 524288
v_accvgpr_read_b32 v37, a45
v_mul_f32 v36, s47, v36
v_mfma_f32_16x16x32_bf16 v[240:243], v[80:83], v[60:63], v[240:243]
v_mul_f32 v37, s47, v37
v_cvt_pk_bf16_f32 v42, v36, v37
v_mfma_f32_16x16x32_bf16 v[244:247], v[84:87], v[60:63], v[244:247]
v_accvgpr_read_b32 v38, a46
v_accvgpr_read_b32 v39, a47
v_mfma_f32_16x16x32_bf16 v[248:251], v[88:91], v[60:63], v[248:251]
v_mul_f32 v38, s47, v38
v_mul_f32 v39, s47, v39
v_mfma_f32_16x16x32_bf16 v[252:255], v[92:95], v[60:63], v[252:255]
s_waitcnt vmcnt(18) lgkmcnt(0)
s_barrier
v_cvt_pk_bf16_f32 v43, v38, v39
v_mfma_f32_16x16x32_bf16 v[128:131], v[96:99], v[48:51], v[128:131]
ds_read_b128 v[60:63], v25 offset:6144
v_accvgpr_read_b32 v36, a168
v_mfma_f32_16x16x32_bf16 v[132:135], v[100:103], v[48:51], v[132:135]
v_add_u32 v25, 32768, v25
v_accvgpr_read_b32 v37, a169
v_mfma_f32_16x16x32_bf16 v[136:139], v[104:107], v[48:51], v[136:139]
s_add_u32 m0, s92, 98304
ds_read_b64_tr_b16 v[64:65], v26 offset:16384
ds_read_b64_tr_b16 v[66:67], v26 offset:17408
v_mfma_f32_16x16x32_bf16 v[140:143], v[108:111], v[48:51], v[140:143]
buffer_load_dwordx4 v20, s[12:15], s24 offen offset:0 lds
v_mul_f32 v36, s47, v36
v_mfma_f32_16x16x32_bf16 v[144:147], v[112:115], v[48:51], v[144:147]
ds_read_b64_tr_b16 v[68:69], v27 offset:16384
ds_read_b64_tr_b16 v[70:71], v27 offset:17408
v_mfma_f32_16x16x32_bf16 v[148:151], v[116:119], v[48:51], v[148:151]
v_permlane32_swap_b32 v40, v42
v_mfma_f32_16x16x32_bf16 v[152:155], v[120:123], v[48:51], v[152:155]
ds_read_b64_tr_b16 v[72:73], v28 offset:16384
ds_read_b64_tr_b16 v[74:75], v28 offset:17408
v_mfma_f32_16x16x32_bf16 v[156:159], v[124:127], v[48:51], v[156:159]
buffer_load_dwordx4 v21, s[12:15], s24 offen offset:1024 lds
v_mul_f32 v37, s47, v37
v_mfma_f32_16x16x32_bf16 v[160:163], v[96:99], v[52:55], v[160:163]
ds_read_b128 v[48:51], v24 offset:0
v_cvt_pk_bf16_f32 v44, v36, v37
v_mfma_f32_16x16x32_bf16 v[164:167], v[100:103], v[52:55], v[164:167]
v_permlane32_swap_b32 v41, v43
v_mfma_f32_16x16x32_bf16 v[168:171], v[104:107], v[52:55], v[168:171]
ds_read_b64_tr_b16 v[76:77], v29 offset:16384
ds_read_b64_tr_b16 v[78:79], v29 offset:17408
v_mfma_f32_16x16x32_bf16 v[172:175], v[108:111], v[52:55], v[172:175]
s_xor_b32 s48, s95, 66560
buffer_load_dwordx4 v22, s[12:15], s24 offen offset:2048 lds
v_mfma_f32_16x16x32_bf16 v[176:179], v[112:115], v[52:55], v[176:179]
ds_read_b64_tr_b16 v[80:81], v30 offset:16384
ds_read_b64_tr_b16 v[82:83], v30 offset:17408
v_mfma_f32_16x16x32_bf16 v[180:183], v[116:119], v[52:55], v[180:183]
buffer_store_dwordx4 v[40:43], v7, s[20:23], s48 offen sc1
v_accvgpr_read_b32 v38, a170
v_mfma_f32_16x16x32_bf16 v[184:187], v[120:123], v[52:55], v[184:187]
ds_read_b64_tr_b16 v[84:85], v31 offset:16384
ds_read_b64_tr_b16 v[86:87], v31 offset:17408
v_mfma_f32_16x16x32_bf16 v[188:191], v[124:127], v[52:55], v[188:191]
buffer_load_dwordx4 v23, s[12:15], s24 offen offset:3072 lds
v_accvgpr_read_b32 v39, a171
v_mfma_f32_16x16x32_bf16 v[192:195], v[96:99], v[56:59], v[192:195]
ds_read_b128 v[52:55], v24 offset:2048
v_mul_f32 v38, s47, v38
v_mfma_f32_16x16x32_bf16 v[196:199], v[100:103], v[56:59], v[196:199]
v_mul_f32 v39, s47, v39
v_cvt_pk_bf16_f32 v45, v38, v39
v_mfma_f32_16x16x32_bf16 v[200:203], v[104:107], v[56:59], v[200:203]
s_add_u32 m0, s92, 102400
ds_read_b64_tr_b16 v[88:89], v32 offset:16384
ds_read_b64_tr_b16 v[90:91], v32 offset:17408
v_mfma_f32_16x16x32_bf16 v[204:207], v[108:111], v[56:59], v[204:207]
buffer_load_dwordx4 v20, s[12:15], s25 offen offset:0 lds
v_accvgpr_read_b32 v36, a172
v_mfma_f32_16x16x32_bf16 v[208:211], v[112:115], v[56:59], v[208:211]
ds_read_b64_tr_b16 v[92:93], v33 offset:16384
ds_read_b64_tr_b16 v[94:95], v33 offset:17408
v_mfma_f32_16x16x32_bf16 v[212:215], v[116:119], v[56:59], v[212:215]
v_accvgpr_read_b32 v37, a173
v_mul_f32 v36, s47, v36
v_mfma_f32_16x16x32_bf16 v[216:219], v[120:123], v[56:59], v[216:219]
s_waitcnt lgkmcnt(15)
v_mul_f32 v37, s47, v37
v_cvt_pk_bf16_f32 v46, v36, v37
v_mfma_f32_16x16x32_bf16 v[220:223], v[124:127], v[56:59], v[220:223]
v_accvgpr_read_b32 v38, a174
v_accvgpr_read_b32 v39, a175
v_mfma_f32_16x16x32_bf16 v[224:227], v[96:99], v[60:63], v[224:227]
ds_read_b128 v[56:59], v24 offset:4096
v_mul_f32 v38, s47, v38
v_mfma_f32_16x16x32_bf16 v[228:231], v[100:103], v[60:63], v[228:231]
buffer_load_dwordx4 v21, s[12:15], s25 offen offset:1024 lds
v_mul_f32 v39, s47, v39
v_mfma_f32_16x16x32_bf16 v[232:235], v[104:107], v[60:63], v[232:235]
v_cvt_pk_bf16_f32 v47, v38, v39
v_mfma_f32_16x16x32_bf16 v[236:239], v[108:111], v[60:63], v[236:239]
v_permlane32_swap_b32 v44, v46
v_mfma_f32_16x16x32_bf16 v[240:243], v[112:115], v[60:63], v[240:243]
v_permlane32_swap_b32 v45, v47
v_mfma_f32_16x16x32_bf16 v[244:247], v[116:119], v[60:63], v[244:247]
v_accvgpr_write_b32 a164, v44
v_accvgpr_write_b32 a165, v45
v_mfma_f32_16x16x32_bf16 v[248:251], v[120:123], v[60:63], v[248:251]
v_accvgpr_write_b32 a166, v46
v_accvgpr_write_b32 a167, v47
v_mfma_f32_16x16x32_bf16 v[252:255], v[124:127], v[60:63], v[252:255]
s_waitcnt lgkmcnt(0)
v_accvgpr_read_b32 v36, a48
v_accvgpr_read_b32 v37, a49
v_mfma_f32_16x16x32_bf16 v[128:131], v[64:67], v[48:51], v[128:131]
ds_read_b128 v[60:63], v24 offset:6144
v_mul_f32 v36, s47, v36
v_mfma_f32_16x16x32_bf16 v[132:135], v[68:71], v[48:51], v[132:135]
v_add_u32 v24, 4294868992, v24
v_mul_f32 v37, s47, v37
v_mfma_f32_16x16x32_bf16 v[136:139], v[72:75], v[48:51], v[136:139]
ds_read_b64_tr_b16 v[96:97], v26 offset:24576
ds_read_b64_tr_b16 v[98:99], v26 offset:25600
v_mfma_f32_16x16x32_bf16 v[140:143], v[76:79], v[48:51], v[140:143]
buffer_load_dwordx4 v22, s[12:15], s25 offen offset:2048 lds
v_cvt_pk_bf16_f32 v40, v36, v37
v_mfma_f32_16x16x32_bf16 v[144:147], v[80:83], v[48:51], v[144:147]
ds_read_b64_tr_b16 v[100:101], v27 offset:24576
ds_read_b64_tr_b16 v[102:103], v27 offset:25600
v_mfma_f32_16x16x32_bf16 v[148:151], v[84:87], v[48:51], v[148:151]
v_accvgpr_read_b32 v38, a50
v_accvgpr_read_b32 v39, a51
v_mfma_f32_16x16x32_bf16 v[152:155], v[88:91], v[48:51], v[152:155]
ds_read_b64_tr_b16 v[104:105], v28 offset:24576
ds_read_b64_tr_b16 v[106:107], v28 offset:25600
v_mfma_f32_16x16x32_bf16 v[156:159], v[92:95], v[48:51], v[156:159]
buffer_load_dwordx4 v23, s[12:15], s25 offen offset:3072 lds
v_mul_f32 v38, s47, v38
v_mfma_f32_16x16x32_bf16 v[160:163], v[64:67], v[52:55], v[160:163]
ds_read_b128 v[48:51], v25 offset:0
v_mul_f32 v39, s47, v39
v_mfma_f32_16x16x32_bf16 v[164:167], v[68:71], v[52:55], v[164:167]
s_add_u32 s24, s24, 128
s_add_u32 s25, s25, 128
v_cvt_pk_bf16_f32 v41, v38, v39
v_mfma_f32_16x16x32_bf16 v[168:171], v[72:75], v[52:55], v[168:171]
s_add_u32 m0, s91, 65536
ds_read_b64_tr_b16 v[108:109], v29 offset:24576
ds_read_b64_tr_b16 v[110:111], v29 offset:25600
v_mfma_f32_16x16x32_bf16 v[172:175], v[76:79], v[52:55], v[172:175]
buffer_load_dwordx4 v16, s[16:19], s26 offen offset:0 lds
v_accvgpr_read_b32 v36, a52
v_mfma_f32_16x16x32_bf16 v[176:179], v[80:83], v[52:55], v[176:179]
ds_read_b64_tr_b16 v[112:113], v30 offset:24576
ds_read_b64_tr_b16 v[114:115], v30 offset:25600
v_mfma_f32_16x16x32_bf16 v[180:183], v[84:87], v[52:55], v[180:183]
v_accvgpr_read_b32 v37, a53
v_mul_f32 v36, s47, v36
v_mfma_f32_16x16x32_bf16 v[184:187], v[88:91], v[52:55], v[184:187]
ds_read_b64_tr_b16 v[116:117], v31 offset:24576
ds_read_b64_tr_b16 v[118:119], v31 offset:25600
v_mfma_f32_16x16x32_bf16 v[188:191], v[92:95], v[52:55], v[188:191]
buffer_load_dwordx4 v17, s[16:19], s26 offen offset:1024 lds
v_mul_f32 v37, s47, v37
v_mfma_f32_16x16x32_bf16 v[192:195], v[64:67], v[56:59], v[192:195]
ds_read_b128 v[52:55], v25 offset:2048
v_cvt_pk_bf16_f32 v42, v36, v37
v_mfma_f32_16x16x32_bf16 v[196:199], v[68:71], v[56:59], v[196:199]
v_accvgpr_read_b32 v38, a54
v_accvgpr_read_b32 v39, a55
v_mfma_f32_16x16x32_bf16 v[200:203], v[72:75], v[56:59], v[200:203]
ds_read_b64_tr_b16 v[120:121], v32 offset:24576
ds_read_b64_tr_b16 v[122:123], v32 offset:25600
v_mfma_f32_16x16x32_bf16 v[204:207], v[76:79], v[56:59], v[204:207]
buffer_load_dwordx4 v18, s[16:19], s26 offen offset:2048 lds
v_mul_f32 v38, s47, v38
v_mfma_f32_16x16x32_bf16 v[208:211], v[80:83], v[56:59], v[208:211]
ds_read_b64_tr_b16 v[124:125], v33 offset:24576
ds_read_b64_tr_b16 v[126:127], v33 offset:25600
v_mfma_f32_16x16x32_bf16 v[212:215], v[84:87], v[56:59], v[212:215]
v_mul_f32 v39, s47, v39
v_cvt_pk_bf16_f32 v43, v38, v39
v_mfma_f32_16x16x32_bf16 v[216:219], v[88:91], v[56:59], v[216:219]
s_waitcnt lgkmcnt(15)
v_permlane32_swap_b32 v40, v42
v_mfma_f32_16x16x32_bf16 v[220:223], v[92:95], v[56:59], v[220:223]
v_xor_b32 v26, 0x10000, v26
v_mfma_f32_16x16x32_bf16 v[224:227], v[64:67], v[60:63], v[224:227]
ds_read_b128 v[56:59], v25 offset:4096
v_xor_b32 v27, 0x10000, v27
v_mfma_f32_16x16x32_bf16 v[228:231], v[68:71], v[60:63], v[228:231]
buffer_load_dwordx4 v19, s[16:19], s26 offen offset:3072 lds
v_xor_b32 v28, 0x10000, v28
v_mfma_f32_16x16x32_bf16 v[232:235], v[72:75], v[60:63], v[232:235]
v_xor_b32 v29, 0x10000, v29
v_mfma_f32_16x16x32_bf16 v[236:239], v[76:79], v[60:63], v[236:239]
s_add_u32 s26, s26, 524288
v_xor_b32 v30, 0x10000, v30
v_mfma_f32_16x16x32_bf16 v[240:243], v[80:83], v[60:63], v[240:243]
v_xor_b32 v31, 0x10000, v31
v_mfma_f32_16x16x32_bf16 v[244:247], v[84:87], v[60:63], v[244:247]
v_xor_b32 v32, 0x10000, v32
v_mfma_f32_16x16x32_bf16 v[248:251], v[88:91], v[60:63], v[248:251]
v_xor_b32 v33, 0x10000, v33
v_mfma_f32_16x16x32_bf16 v[252:255], v[92:95], v[60:63], v[252:255]
s_waitcnt vmcnt(18) lgkmcnt(0)
s_barrier
v_accvgpr_read_b32 v36, a56
v_mfma_f32_16x16x32_bf16 v[128:131], v[96:99], v[48:51], v[128:131]
ds_read_b128 v[60:63], v25 offset:6144
v_accvgpr_read_b32 v37, a57
v_mfma_f32_16x16x32_bf16 v[132:135], v[100:103], v[48:51], v[132:135]
v_add_u32 v25, 4294868992, v25
v_mul_f32 v36, s47, v36
v_mfma_f32_16x16x32_bf16 v[136:139], v[104:107], v[48:51], v[136:139]
s_add_u32 m0, s92, 131072
ds_read_b64_tr_b16 v[64:65], v26 offset:0
ds_read_b64_tr_b16 v[66:67], v26 offset:1024
v_mfma_f32_16x16x32_bf16 v[140:143], v[108:111], v[48:51], v[140:143]
buffer_load_dwordx4 v20, s[12:15], s24 offen offset:0 lds
v_mul_f32 v37, s47, v37
v_mfma_f32_16x16x32_bf16 v[144:147], v[112:115], v[48:51], v[144:147]
ds_read_b64_tr_b16 v[68:69], v27 offset:0
ds_read_b64_tr_b16 v[70:71], v27 offset:1024
v_mfma_f32_16x16x32_bf16 v[148:151], v[116:119], v[48:51], v[148:151]
v_permlane32_swap_b32 v41, v43
v_mfma_f32_16x16x32_bf16 v[152:155], v[120:123], v[48:51], v[152:155]
ds_read_b64_tr_b16 v[72:73], v28 offset:0
ds_read_b64_tr_b16 v[74:75], v28 offset:1024
v_mfma_f32_16x16x32_bf16 v[156:159], v[124:127], v[48:51], v[156:159]
s_xor_b32 s48, s95, 67584
buffer_load_dwordx4 v21, s[12:15], s24 offen offset:1024 lds
v_mfma_f32_16x16x32_bf16 v[160:163], v[96:99], v[52:55], v[160:163]
ds_read_b128 v[48:51], v24 offset:0
v_cvt_pk_bf16_f32 v44, v36, v37
v_mfma_f32_16x16x32_bf16 v[164:167], v[100:103], v[52:55], v[164:167]
buffer_store_dwordx4 v[40:43], v7, s[20:23], s48 offen sc1
v_accvgpr_read_b32 v38, a58
v_mfma_f32_16x16x32_bf16 v[168:171], v[104:107], v[52:55], v[168:171]
ds_read_b64_tr_b16 v[76:77], v29 offset:0
ds_read_b64_tr_b16 v[78:79], v29 offset:1024
v_mfma_f32_16x16x32_bf16 v[172:175], v[108:111], v[52:55], v[172:175]
buffer_load_dwordx4 v22, s[12:15], s24 offen offset:2048 lds
v_accvgpr_read_b32 v39, a59
v_mfma_f32_16x16x32_bf16 v[176:179], v[112:115], v[52:55], v[176:179]
ds_read_b64_tr_b16 v[80:81], v30 offset:0
ds_read_b64_tr_b16 v[82:83], v30 offset:1024
v_mfma_f32_16x16x32_bf16 v[180:183], v[116:119], v[52:55], v[180:183]
v_mul_f32 v38, s47, v38
v_mul_f32 v39, s47, v39
v_mfma_f32_16x16x32_bf16 v[184:187], v[120:123], v[52:55], v[184:187]
ds_read_b64_tr_b16 v[84:85], v31 offset:0
ds_read_b64_tr_b16 v[86:87], v31 offset:1024
v_mfma_f32_16x16x32_bf16 v[188:191], v[124:127], v[52:55], v[188:191]
buffer_load_dwordx4 v23, s[12:15], s24 offen offset:3072 lds
v_cvt_pk_bf16_f32 v45, v38, v39
v_mfma_f32_16x16x32_bf16 v[192:195], v[96:99], v[56:59], v[192:195]
ds_read_b128 v[52:55], v24 offset:2048
v_accvgpr_read_b32 v36, a60
v_mfma_f32_16x16x32_bf16 v[196:199], v[100:103], v[56:59], v[196:199]
v_accvgpr_read_b32 v37, a61
v_mul_f32 v36, s47, v36
v_mfma_f32_16x16x32_bf16 v[200:203], v[104:107], v[56:59], v[200:203]
s_add_u32 m0, s92, 135168
ds_read_b64_tr_b16 v[88:89], v32 offset:0
ds_read_b64_tr_b16 v[90:91], v32 offset:1024
v_mfma_f32_16x16x32_bf16 v[204:207], v[108:111], v[56:59], v[204:207]
buffer_load_dwordx4 v20, s[12:15], s25 offen offset:0 lds
v_mul_f32 v37, s47, v37
v_mfma_f32_16x16x32_bf16 v[208:211], v[112:115], v[56:59], v[208:211]
ds_read_b64_tr_b16 v[92:93], v33 offset:0
ds_read_b64_tr_b16 v[94:95], v33 offset:1024
v_mfma_f32_16x16x32_bf16 v[212:215], v[116:119], v[56:59], v[212:215]
v_cvt_pk_bf16_f32 v46, v36, v37
v_accvgpr_read_b32 v38, a62
v_mfma_f32_16x16x32_bf16 v[216:219], v[120:123], v[56:59], v[216:219]
s_waitcnt lgkmcnt(15)
v_accvgpr_read_b32 v39, a63
v_mul_f32 v38, s47, v38
v_mfma_f32_16x16x32_bf16 v[220:223], v[124:127], v[56:59], v[220:223]
v_mul_f32 v39, s47, v39
v_cvt_pk_bf16_f32 v47, v38, v39
v_mfma_f32_16x16x32_bf16 v[224:227], v[96:99], v[60:63], v[224:227]
ds_read_b128 v[56:59], v24 offset:4096
v_accvgpr_read_b32 v36, a176
v_mfma_f32_16x16x32_bf16 v[228:231], v[100:103], v[60:63], v[228:231]
buffer_load_dwordx4 v21, s[12:15], s25 offen offset:1024 lds
v_accvgpr_read_b32 v37, a177
v_mfma_f32_16x16x32_bf16 v[232:235], v[104:107], v[60:63], v[232:235]
v_permlane32_swap_b32 v44, v46
v_mfma_f32_16x16x32_bf16 v[236:239], v[108:111], v[60:63], v[236:239]
v_permlane32_swap_b32 v45, v47
v_mfma_f32_16x16x32_bf16 v[240:243], v[112:115], v[60:63], v[240:243]
s_xor_b32 s48, s95, 68608
buffer_store_dwordx4 v[44:47], v7, s[20:23], s48 offen sc1
v_mfma_f32_16x16x32_bf16 v[244:247], v[116:119], v[60:63], v[244:247]
v_mul_f32 v36, s47, v36
v_mul_f32 v37, s47, v37
v_mfma_f32_16x16x32_bf16 v[248:251], v[120:123], v[60:63], v[248:251]
v_cvt_pk_bf16_f32 v40, v36, v37
v_accvgpr_read_b32 v38, a178
.Lpad6:
s_cbranch_execz .Ltch1
v_mfma_f32_16x16x32_bf16 v[252:255], v[124:127], v[60:63], v[252:255]
s_waitcnt lgkmcnt(0)
v_accvgpr_read_b32 v39, a179
v_mul_f32 v38, s47, v38
v_mfma_f32_16x16x32_bf16 v[128:131], v[64:67], v[48:51], v[128:131]
ds_read_b128 v[60:63], v24 offset:6144
v_mul_f32 v39, s47, v39
v_mfma_f32_16x16x32_bf16 v[132:135], v[68:71], v[48:51], v[132:135]
v_add_u32 v24, 65536, v24
v_cvt_pk_bf16_f32 v41, v38, v39
v_mfma_f32_16x16x32_bf16 v[136:139], v[72:75], v[48:51], v[136:139]
ds_read_b64_tr_b16 v[96:97], v26 offset:8192
ds_read_b64_tr_b16 v[98:99], v26 offset:9216
v_mfma_f32_16x16x32_bf16 v[140:143], v[76:79], v[48:51], v[140:143]
buffer_load_dwordx4 v22, s[12:15], s25 offen offset:2048 lds
v_accvgpr_read_b32 v36, a180
v_mfma_f32_16x16x32_bf16 v[144:147], v[80:83], v[48:51], v[144:147]
ds_read_b64_tr_b16 v[100:101], v27 offset:8192
ds_read_b64_tr_b16 v[102:103], v27 offset:9216
v_mfma_f32_16x16x32_bf16 v[148:151], v[84:87], v[48:51], v[148:151]
v_accvgpr_read_b32 v37, a181
v_mul_f32 v36, s47, v36
v_mfma_f32_16x16x32_bf16 v[152:155], v[88:91], v[48:51], v[152:155]
ds_read_b64_tr_b16 v[104:105], v28 offset:8192
ds_read_b64_tr_b16 v[106:107], v28 offset:9216
v_mfma_f32_16x16x32_bf16 v[156:159], v[92:95], v[48:51], v[156:159]
buffer_load_dwordx4 v23, s[12:15], s25 offen offset:3072 lds
v_mul_f32 v37, s47, v37
v_mfma_f32_16x16x32_bf16 v[160:163], v[64:67], v[52:55], v[160:163]
ds_read_b128 v[48:51], v25 offset:0
v_cvt_pk_bf16_f32 v42, v36, v37
v_mfma_f32_16x16x32_bf16 v[164:167], v[68:71], v[52:55], v[164:167]
s_add_u32 s24, s24, 128
s_add_u32 s25, s25, 128
v_accvgpr_read_b32 v38, a182
v_mfma_f32_16x16x32_bf16 v[168:171], v[72:75], v[52:55], v[168:171]
s_add_u32 m0, s91, 81920
ds_read_b64_tr_b16 v[108:109], v29 offset:8192
ds_read_b64_tr_b16 v[110:111], v29 offset:9216
v_mfma_f32_16x16x32_bf16 v[172:175], v[76:79], v[52:55], v[172:175]
buffer_load_dwordx4 v16, s[16:19], s26 offen offset:0 lds
v_accvgpr_read_b32 v39, a183
v_mfma_f32_16x16x32_bf16 v[176:179], v[80:83], v[52:55], v[176:179]
ds_read_b64_tr_b16 v[112:113], v30 offset:8192
ds_read_b64_tr_b16 v[114:115], v30 offset:9216
v_mfma_f32_16x16x32_bf16 v[180:183], v[84:87], v[52:55], v[180:183]
v_mul_f32 v38, s47, v38
v_mul_f32 v39, s47, v39
v_mfma_f32_16x16x32_bf16 v[184:187], v[88:91], v[52:55], v[184:187]
ds_read_b64_tr_b16 v[116:117], v31 offset:8192
ds_read_b64_tr_b16 v[118:119], v31 offset:9216
v_mfma_f32_16x16x32_bf16 v[188:191], v[92:95], v[52:55], v[188:191]
buffer_load_dwordx4 v17, s[16:19], s26 offen offset:1024 lds
v_cvt_pk_bf16_f32 v43, v38, v39
v_mfma_f32_16x16x32_bf16 v[192:195], v[64:67], v[56:59], v[192:195]
ds_read_b128 v[52:55], v25 offset:2048
v_mfma_f32_16x16x32_bf16 v[196:199], v[68:71], v[56:59], v[196:199]
v_permlane32_swap_b32 v40, v42
v_mfma_f32_16x16x32_bf16 v[200:203], v[72:75], v[56:59], v[200:203]
ds_read_b64_tr_b16 v[120:121], v32 offset:8192
ds_read_b64_tr_b16 v[122:123], v32 offset:9216
v_mfma_f32_16x16x32_bf16 v[204:207], v[76:79], v[56:59], v[204:207]
buffer_load_dwordx4 v18, s[16:19], s26 offen offset:2048 lds
v_mfma_f32_16x16x32_bf16 v[208:211], v[80:83], v[56:59], v[208:211]
ds_read_b64_tr_b16 v[124:125], v33 offset:8192
ds_read_b64_tr_b16 v[126:127], v33 offset:9216
v_mfma_f32_16x16x32_bf16 v[212:215], v[84:87], v[56:59], v[212:215]
v_permlane32_swap_b32 v41, v43
v_mfma_f32_16x16x32_bf16 v[216:219], v[88:91], v[56:59], v[216:219]
s_waitcnt lgkmcnt(15)
v_accvgpr_write_b32 a168, v40
v_accvgpr_write_b32 a169, v41
v_mfma_f32_16x16x32_bf16 v[220:223], v[92:95], v[56:59], v[220:223]
v_accvgpr_write_b32 a170, v42
v_accvgpr_write_b32 a171, v43
v_mfma_f32_16x16x32_bf16 v[224:227], v[64:67], v[60:63], v[224:227]
ds_read_b128 v[56:59], v25 offset:4096
v_accvgpr_read_b32 v36, a64
v_mfma_f32_16x16x32_bf16 v[228:231], v[68:71], v[60:63], v[228:231]
buffer_load_dwordx4 v19, s[16:19], s26 offen offset:3072 lds
v_accvgpr_read_b32 v37, a65
v_mfma_f32_16x16x32_bf16 v[232:235], v[72:75], v[60:63], v[232:235]
v_mul_f32 v36, s47, v36
v_mul_f32 v37, s47, v37
v_mfma_f32_16x16x32_bf16 v[236:239], v[76:79], v[60:63], v[236:239]
s_add_u32 s26, s26, 524288
v_cvt_pk_bf16_f32 v44, v36, v37
v_accvgpr_read_b32 v38, a66
v_mfma_f32_16x16x32_bf16 v[240:243], v[80:83], v[60:63], v[240:243]
v_accvgpr_read_b32 v39, a67
v_mul_f32 v38, s47, v38
v_mfma_f32_16x16x32_bf16 v[244:247], v[84:87], v[60:63], v[244:247]
v_mul_f32 v39, s47, v39
v_cvt_pk_bf16_f32 v45, v38, v39
v_mfma_f32_16x16x32_bf16 v[248:251], v[88:91], v[60:63], v[248:251]
v_accvgpr_read_b32 v36, a68
v_accvgpr_read_b32 v37, a69
v_mfma_f32_16x16x32_bf16 v[252:255], v[92:95], v[60:63], v[252:255]
s_waitcnt vmcnt(18) lgkmcnt(0)
s_barrier
v_mul_f32 v36, s47, v36
v_mfma_f32_16x16x32_bf16 v[128:131], v[96:99], v[48:51], v[128:131]
ds_read_b128 v[60:63], v25 offset:6144
v_mul_f32 v37, s47, v37
v_mfma_f32_16x16x32_bf16 v[132:135], v[100:103], v[48:51], v[132:135]
v_add_u32 v25, 65536, v25
v_cvt_pk_bf16_f32 v46, v36, v37
v_mfma_f32_16x16x32_bf16 v[136:139], v[104:107], v[48:51], v[136:139]
s_add_u32 m0, s92, 32768
ds_read_b64_tr_b16 v[64:65], v26 offset:16384
ds_read_b64_tr_b16 v[66:67], v26 offset:17408
v_mfma_f32_16x16x32_bf16 v[140:143], v[108:111], v[48:51], v[140:143]
buffer_load_dwordx4 v20, s[12:15], s24 offen offset:0 lds
v_accvgpr_read_b32 v38, a70
v_mfma_f32_16x16x32_bf16 v[144:147], v[112:115], v[48:51], v[144:147]
ds_read_b64_tr_b16 v[68:69], v27 offset:16384
ds_read_b64_tr_b16 v[70:71], v27 offset:17408
v_mfma_f32_16x16x32_bf16 v[148:151], v[116:119], v[48:51], v[148:151]
v_accvgpr_read_b32 v39, a71
v_mul_f32 v38, s47, v38
v_mfma_f32_16x16x32_bf16 v[152:155], v[120:123], v[48:51], v[152:155]
ds_read_b64_tr_b16 v[72:73], v28 offset:16384
ds_read_b64_tr_b16 v[74:75], v28 offset:17408
v_mfma_f32_16x16x32_bf16 v[156:159], v[124:127], v[48:51], v[156:159]
buffer_load_dwordx4 v21, s[12:15], s24 offen offset:1024 lds
v_mul_f32 v39, s47, v39
v_mfma_f32_16x16x32_bf16 v[160:163], v[96:99], v[52:55], v[160:163]
ds_read_b128 v[48:51], v24 offset:0
v_cvt_pk_bf16_f32 v47, v38, v39
v_mfma_f32_16x16x32_bf16 v[164:167], v[100:103], v[52:55], v[164:167]
v_permlane32_swap_b32 v44, v46
v_mfma_f32_16x16x32_bf16 v[168:171], v[104:107], v[52:55], v[168:171]
ds_read_b64_tr_b16 v[76:77], v29 offset:16384
ds_read_b64_tr_b16 v[78:79], v29 offset:17408
v_mfma_f32_16x16x32_bf16 v[172:175], v[108:111], v[52:55], v[172:175]
buffer_load_dwordx4 v22, s[12:15], s24 offen offset:2048 lds
v_accvgpr_read_b32 v36, a184
v_mfma_f32_16x16x32_bf16 v[176:179], v[112:115], v[52:55], v[176:179]
ds_read_b64_tr_b16 v[80:81], v30 offset:16384
ds_read_b64_tr_b16 v[82:83], v30 offset:17408
v_mfma_f32_16x16x32_bf16 v[180:183], v[116:119], v[52:55], v[180:183]
v_permlane32_swap_b32 v45, v47
v_mfma_f32_16x16x32_bf16 v[184:187], v[120:123], v[52:55], v[184:187]
ds_read_b64_tr_b16 v[84:85], v31 offset:16384
ds_read_b64_tr_b16 v[86:87], v31 offset:17408
v_mfma_f32_16x16x32_bf16 v[188:191], v[124:127], v[52:55], v[188:191]
s_xor_b32 s48, s95, 131072
buffer_load_dwordx4 v23, s[12:15], s24 offen offset:3072 lds
v_mfma_f32_16x16x32_bf16 v[192:195], v[96:99], v[56:59], v[192:195]
ds_read_b128 v[52:55], v24 offset:2048
v_accvgpr_read_b32 v37, a185
v_mfma_f32_16x16x32_bf16 v[196:199], v[100:103], v[56:59], v[196:199]
buffer_store_dwordx4 v[44:47], v7, s[20:23], s48 offen sc1
v_mul_f32 v36, s47, v36
v_mfma_f32_16x16x32_bf16 v[200:203], v[104:107], v[56:59], v[200:203]
s_add_u32 m0, s92, 36864
ds_read_b64_tr_b16 v[88:89], v32 offset:16384
ds_read_b64_tr_b16 v[90:91], v32 offset:17408
v_mfma_f32_16x16x32_bf16 v[204:207], v[108:111], v[56:59], v[204:207]
buffer_load_dwordx4 v20, s[12:15], s25 offen offset:0 lds
v_mul_f32 v37, s47, v37
v_mfma_f32_16x16x32_bf16 v[208:211], v[112:115], v[56:59], v[208:211]
ds_read_b64_tr_b16 v[92:93], v33 offset:16384
ds_read_b64_tr_b16 v[94:95], v33 offset:17408
v_mfma_f32_16x16x32_bf16 v[212:215], v[116:119], v[56:59], v[212:215]
v_cvt_pk_bf16_f32 v40, v36, v37
v_accvgpr_read_b32 v38, a186
v_mfma_f32_16x16x32_bf16 v[216:219], v[120:123], v[56:59], v[216:219]
s_waitcnt lgkmcnt(15)
v_accvgpr_read_b32 v39, a187
v_mul_f32 v38, s47, v38
v_mfma_f32_16x16x32_bf16 v[220:223], v[124:127], v[56:59], v[220:223]
v_mul_f32 v39, s47, v39
v_cvt_pk_bf16_f32 v41, v38, v39
v_mfma_f32_16x16x32_bf16 v[224:227], v[96:99], v[60:63], v[224:227]
ds_read_b128 v[56:59], v24 offset:4096
v_accvgpr_read_b32 v36, a188
v_mfma_f32_16x16x32_bf16 v[228:231], v[100:103], v[60:63], v[228:231]
buffer_load_dwordx4 v21, s[12:15], s25 offen offset:1024 lds
v_accvgpr_read_b32 v37, a189
v_mfma_f32_16x16x32_bf16 v[232:235], v[104:107], v[60:63], v[232:235]
v_mul_f32 v36, s47, v36
v_mul_f32 v37, s47, v37
v_mfma_f32_16x16x32_bf16 v[236:239], v[108:111], v[60:63], v[236:239]
v_cvt_pk_bf16_f32 v42, v36, v37
v_accvgpr_read_b32 v38, a190
v_mfma_f32_16x16x32_bf16 v[240:243], v[112:115], v[60:63], v[240:243]
v_accvgpr_read_b32 v39, a191
v_mul_f32 v38, s47, v38
v_mfma_f32_16x16x32_bf16 v[244:247], v[116:119], v[60:63], v[244:247]
v_mul_f32 v39, s47, v39
v_cvt_pk_bf16_f32 v43, v38, v39
v_mfma_f32_16x16x32_bf16 v[248:251], v[120:123], v[60:63], v[248:251]
v_permlane32_swap_b32 v40, v42
v_mfma_f32_16x16x32_bf16 v[252:255], v[124:127], v[60:63], v[252:255]
s_waitcnt lgkmcnt(0)
v_permlane32_swap_b32 v41, v43
v_mfma_f32_16x16x32_bf16 v[128:131], v[64:67], v[48:51], v[128:131]
ds_read_b128 v[60:63], v24 offset:6144
v_accvgpr_write_b32 a172, v40
v_mfma_f32_16x16x32_bf16 v[132:135], v[68:71], v[48:51], v[132:135]
v_add_u32 v24, 32768, v24
v_accvgpr_write_b32 a173, v41
v_mfma_f32_16x16x32_bf16 v[136:139], v[72:75], v[48:51], v[136:139]
ds_read_b64_tr_b16 v[96:97], v26 offset:24576
ds_read_b64_tr_b16 v[98:99], v26 offset:25600
v_mfma_f32_16x16x32_bf16 v[140:143], v[76:79], v[48:51], v[140:143]
buffer_load_dwordx4 v22, s[12:15], s25 offen offset:2048 lds
v_accvgpr_write_b32 a174, v42
v_mfma_f32_16x16x32_bf16 v[144:147], v[80:83], v[48:51], v[144:147]
ds_read_b64_tr_b16 v[100:101], v27 offset:24576
ds_read_b64_tr_b16 v[102:103], v27 offset:25600
v_mfma_f32_16x16x32_bf16 v[148:151], v[84:87], v[48:51], v[148:151]
v_accvgpr_write_b32 a175, v43
v_accvgpr_read_b32 v36, a72
v_mfma_f32_16x16x32_bf16 v[152:155], v[88:91], v[48:51], v[152:155]
ds_read_b64_tr_b16 v[104:105], v28 offset:24576
ds_read_b64_tr_b16 v[106:107], v28 offset:25600
v_mfma_f32_16x16x32_bf16 v[156:159], v[92:95], v[48:51], v[156:159]
buffer_load_dwordx4 v23, s[12:15], s25 offen offset:3072 lds
v_accvgpr_read_b32 v37, a73
v_mfma_f32_16x16x32_bf16 v[160:163], v[64:67], v[52:55], v[160:163]
ds_read_b128 v[48:51], v25 offset:0
v_mul_f32 v36, s47, v36
v_mfma_f32_16x16x32_bf16 v[164:167], v[68:71], v[52:55], v[164:167]
s_add_u32 s24, s24, 128
s_add_u32 s25, s25, 128
v_mul_f32 v37, s47, v37
v_mfma_f32_16x16x32_bf16 v[168:171], v[72:75], v[52:55], v[168:171]
s_add_u32 m0, s91, 0
ds_read_b64_tr_b16 v[108:109], v29 offset:24576
ds_read_b64_tr_b16 v[110:111], v29 offset:25600
v_mfma_f32_16x16x32_bf16 v[172:175], v[76:79], v[52:55], v[172:175]
buffer_load_dwordx4 v16, s[16:19], s26 offen offset:0 lds
v_cvt_pk_bf16_f32 v44, v36, v37
v_mfma_f32_16x16x32_bf16 v[176:179], v[80:83], v[52:55], v[176:179]
ds_read_b64_tr_b16 v[112:113], v30 offset:24576
ds_read_b64_tr_b16 v[114:115], v30 offset:25600
v_mfma_f32_16x16x32_bf16 v[180:183], v[84:87], v[52:55], v[180:183]
v_accvgpr_read_b32 v38, a74
v_accvgpr_read_b32 v39, a75
v_mfma_f32_16x16x32_bf16 v[184:187], v[88:91], v[52:55], v[184:187]
ds_read_b64_tr_b16 v[116:117], v31 offset:24576
ds_read_b64_tr_b16 v[118:119], v31 offset:25600
v_mfma_f32_16x16x32_bf16 v[188:191], v[92:95], v[52:55], v[188:191]
buffer_load_dwordx4 v17, s[16:19], s26 offen offset:1024 lds
v_mul_f32 v38, s47, v38
v_mfma_f32_16x16x32_bf16 v[192:195], v[64:67], v[56:59], v[192:195]
ds_read_b128 v[52:55], v25 offset:2048
v_mul_f32 v39, s47, v39
v_mfma_f32_16x16x32_bf16 v[196:199], v[68:71], v[56:59], v[196:199]
v_cvt_pk_bf16_f32 v45, v38, v39
v_accvgpr_read_b32 v36, a76
v_mfma_f32_16x16x32_bf16 v[200:203], v[72:75], v[56:59], v[200:203]
ds_read_b64_tr_b16 v[120:121], v32 offset:24576
ds_read_b64_tr_b16 v[122:123], v32 offset:25600
v_mfma_f32_16x16x32_bf16 v[204:207], v[76:79], v[56:59], v[204:207]
buffer_load_dwordx4 v18, s[16:19], s26 offen offset:2048 lds
v_accvgpr_read_b32 v37, a77
v_mfma_f32_16x16x32_bf16 v[208:211], v[80:83], v[56:59], v[208:211]
ds_read_b64_tr_b16 v[124:125], v33 offset:24576
ds_read_b64_tr_b16 v[126:127], v33 offset:25600
v_mfma_f32_16x16x32_bf16 v[212:215], v[84:87], v[56:59], v[212:215]
v_mul_f32 v36, s47, v36
v_mul_f32 v37, s47, v37
v_mfma_f32_16x16x32_bf16 v[216:219], v[88:91], v[56:59], v[216:219]
s_waitcnt lgkmcnt(15)
v_cvt_pk_bf16_f32 v46, v36, v37
v_accvgpr_read_b32 v38, a78
v_mfma_f32_16x16x32_bf16 v[220:223], v[92:95], v[56:59], v[220:223]
v_xor_b32 v26, 0x10000, v26
v_accvgpr_read_b32 v39, a79
v_mfma_f32_16x16x32_bf16 v[224:227], v[64:67], v[60:63], v[224:227]
ds_read_b128 v[56:59], v25 offset:4096
v_xor_b32 v27, 0x10000, v27
v_mfma_f32_16x16x32_bf16 v[228:231], v[68:71], v[60:63], v[228:231]
buffer_load_dwordx4 v19, s[16:19], s26 offen offset:3072 lds
v_xor_b32 v28, 0x10000, v28
v_mfma_f32_16x16x32_bf16 v[232:235], v[72:75], v[60:63], v[232:235]
v_xor_b32 v29, 0x10000, v29
v_mul_f32 v38, s47, v38
v_mfma_f32_16x16x32_bf16 v[236:239], v[76:79], v[60:63], v[236:239]
s_add_u32 s26, s26, 524288
v_xor_b32 v30, 0x10000, v30
v_mul_f32 v39, s47, v39
v_mfma_f32_16x16x32_bf16 v[240:243], v[80:83], v[60:63], v[240:243]
v_xor_b32 v31, 0x10000, v31
v_cvt_pk_bf16_f32 v47, v38, v39
v_mfma_f32_16x16x32_bf16 v[244:247], v[84:87], v[60:63], v[244:247]
v_xor_b32 v32, 0x10000, v32
v_accvgpr_read_b32 v36, a192
v_mfma_f32_16x16x32_bf16 v[248:251], v[88:91], v[60:63], v[248:251]
v_xor_b32 v33, 0x10000, v33
v_accvgpr_read_b32 v37, a193
v_mfma_f32_16x16x32_bf16 v[252:255], v[92:95], v[60:63], v[252:255]
s_waitcnt vmcnt(17) lgkmcnt(0)
s_barrier
v_mul_f32 v36, s47, v36
v_mfma_f32_16x16x32_bf16 v[128:131], v[96:99], v[48:51], v[128:131]
ds_read_b128 v[60:63], v25 offset:6144
v_mul_f32 v37, s47, v37
v_mfma_f32_16x16x32_bf16 v[132:135], v[100:103], v[48:51], v[132:135]
v_add_u32 v25, 32768, v25
v_cvt_pk_bf16_f32 v40, v36, v37
v_mfma_f32_16x16x32_bf16 v[136:139], v[104:107], v[48:51], v[136:139]
s_add_u32 m0, s92, 98304
ds_read_b64_tr_b16 v[64:65], v26 offset:0
ds_read_b64_tr_b16 v[66:67], v26 offset:1024
v_mfma_f32_16x16x32_bf16 v[140:143], v[108:111], v[48:51], v[140:143]
buffer_load_dwordx4 v20, s[12:15], s24 offen offset:0 lds
v_accvgpr_read_b32 v38, a194
v_mfma_f32_16x16x32_bf16 v[144:147], v[112:115], v[48:51], v[144:147]
ds_read_b64_tr_b16 v[68:69], v27 offset:0
ds_read_b64_tr_b16 v[70:71], v27 offset:1024
v_mfma_f32_16x16x32_bf16 v[148:151], v[116:119], v[48:51], v[148:151]
v_permlane32_swap_b32 v44, v46
v_mfma_f32_16x16x32_bf16 v[152:155], v[120:123], v[48:51], v[152:155]
ds_read_b64_tr_b16 v[72:73], v28 offset:0
ds_read_b64_tr_b16 v[74:75], v28 offset:1024
v_mfma_f32_16x16x32_bf16 v[156:159], v[124:127], v[48:51], v[156:159]
buffer_load_dwordx4 v21, s[12:15], s24 offen offset:1024 lds
v_accvgpr_read_b32 v39, a195
v_mfma_f32_16x16x32_bf16 v[160:163], v[96:99], v[52:55], v[160:163]
ds_read_b128 v[48:51], v24 offset:0
v_mul_f32 v38, s47, v38
v_mfma_f32_16x16x32_bf16 v[164:167], v[100:103], v[52:55], v[164:167]
v_permlane32_swap_b32 v45, v47
v_mfma_f32_16x16x32_bf16 v[168:171], v[104:107], v[52:55], v[168:171]
ds_read_b64_tr_b16 v[76:77], v29 offset:0
ds_read_b64_tr_b16 v[78:79], v29 offset:1024
v_mfma_f32_16x16x32_bf16 v[172:175], v[108:111], v[52:55], v[172:175]
s_xor_b32 s48, s95, 132096
buffer_load_dwordx4 v22, s[12:15], s24 offen offset:2048 lds
v_mfma_f32_16x16x32_bf16 v[176:179], v[112:115], v[52:55], v[176:179]
ds_read_b64_tr_b16 v[80:81], v30 offset:0
ds_read_b64_tr_b16 v[82:83], v30 offset:1024
v_mfma_f32_16x16x32_bf16 v[180:183], v[116:119], v[52:55], v[180:183]
buffer_store_dwordx4 v[44:47], v7, s[20:23], s48 offen sc1
v_mul_f32 v39, s47, v39
v_mfma_f32_16x16x32_bf16 v[184:187], v[120:123], v[52:55], v[184:187]
ds_read_b64_tr_b16 v[84:85], v31 offset:0
ds_read_b64_tr_b16 v[86:87], v31 offset:1024
v_mfma_f32_16x16x32_bf16 v[188:191], v[124:127], v[52:55], v[188:191]
buffer_load_dwordx4 v23, s[12:15], s24 offen offset:3072 lds
v_cvt_pk_bf16_f32 v41, v38, v39
v_mfma_f32_16x16x32_bf16 v[192:195], v[96:99], v[56:59], v[192:195]
ds_read_b128 v[52:55], v24 offset:2048
v_accvgpr_read_b32 v36, a196
v_mfma_f32_16x16x32_bf16 v[196:199], v[100:103], v[56:59], v[196:199]
v_accvgpr_read_b32 v37, a197
v_mul_f32 v36, s47, v36
v_mfma_f32_16x16x32_bf16 v[200:203], v[104:107], v[56:59], v[200:203]
s_add_u32 m0, s92, 102400
ds_read_b64_tr_b16 v[88:89], v32 offset:0
ds_read_b64_tr_b16 v[90:91], v32 offset:1024
v_mfma_f32_16x16x32_bf16 v[204:207], v[108:111], v[56:59], v[204:207]
buffer_load_dwordx4 v20, s[12:15], s25 offen offset:0 lds
v_mul_f32 v37, s47, v37
v_mfma_f32_16x16x32_bf16 v[208:211], v[112:115], v[56:59], v[208:211]
ds_read_b64_tr_b16 v[92:93], v33 offset:0
ds_read_b64_tr_b16 v[94:95], v33 offset:1024
v_mfma_f32_16x16x32_bf16 v[212:215], v[116:119], v[56:59], v[212:215]
v_cvt_pk_bf16_f32 v42, v36, v37
v_accvgpr_read_b32 v38, a198
v_mfma_f32_16x16x32_bf16 v[216:219], v[120:123], v[56:59], v[216:219]
s_waitcnt lgkmcnt(15)
v_accvgpr_read_b32 v39, a199
v_mul_f32 v38, s47, v38
v_mfma_f32_16x16x32_bf16 v[220:223], v[124:127], v[56:59], v[220:223]
v_mul_f32 v39, s47, v39
v_cvt_pk_bf16_f32 v43, v38, v39
v_mfma_f32_16x16x32_bf16 v[224:227], v[96:99], v[60:63], v[224:227]
ds_read_b128 v[56:59], v24 offset:4096
v_mfma_f32_16x16x32_bf16 v[228:231], v[100:103], v[60:63], v[228:231]
buffer_load_dwordx4 v21, s[12:15], s25 offen offset:1024 lds
v_mfma_f32_16x16x32_bf16 v[232:235], v[104:107], v[60:63], v[232:235]
v_permlane32_swap_b32 v40, v42
v_mfma_f32_16x16x32_bf16 v[236:239], v[108:111], v[60:63], v[236:239]
v_permlane32_swap_b32 v41, v43
v_mfma_f32_16x16x32_bf16 v[240:243], v[112:115], v[60:63], v[240:243]
v_accvgpr_write_b32 a192, v40
v_accvgpr_write_b32 a193, v41
v_mfma_f32_16x16x32_bf16 v[244:247], v[116:119], v[60:63], v[244:247]
v_accvgpr_write_b32 a194, v42
v_accvgpr_write_b32 a195, v43
v_mfma_f32_16x16x32_bf16 v[248:251], v[120:123], v[60:63], v[248:251]
v_accvgpr_read_b32 v36, a80
v_accvgpr_read_b32 v37, a81
v_mfma_f32_16x16x32_bf16 v[252:255], v[124:127], v[60:63], v[252:255]
s_waitcnt lgkmcnt(0)
v_mul_f32 v36, s47, v36
v_mul_f32 v37, s47, v37
v_mfma_f32_16x16x32_bf16 v[128:131], v[64:67], v[48:51], v[128:131]
ds_read_b128 v[60:63], v24 offset:6144
v_cvt_pk_bf16_f32 v44, v36, v37
v_mfma_f32_16x16x32_bf16 v[132:135], v[68:71], v[48:51], v[132:135]
v_add_u32 v24, 4294868992, v24
v_accvgpr_read_b32 v38, a82
v_mfma_f32_16x16x32_bf16 v[136:139], v[72:75], v[48:51], v[136:139]
ds_read_b64_tr_b16 v[96:97], v26 offset:8192
ds_read_b64_tr_b16 v[98:99], v26 offset:9216
v_mfma_f32_16x16x32_bf16 v[140:143], v[76:79], v[48:51], v[140:143]
s_lshl_b32 s28, s88, 19
buffer_load_dwordx4 v22, s[12:15], s25 offen offset:2048 lds
v_mfma_f32_16x16x32_bf16 v[144:147], v[80:83], v[48:51], v[144:147]
ds_read_b64_tr_b16 v[100:101], v27 offset:8192
ds_read_b64_tr_b16 v[102:103], v27 offset:9216
v_mfma_f32_16x16x32_bf16 v[148:151], v[84:87], v[48:51], v[148:151]
s_add_u32 s28, s28, s91
v_accvgpr_read_b32 v39, a83
v_mul_f32 v38, s47, v38
v_mfma_f32_16x16x32_bf16 v[152:155], v[88:91], v[48:51], v[152:155]
ds_read_b64_tr_b16 v[104:105], v28 offset:8192
ds_read_b64_tr_b16 v[106:107], v28 offset:9216
v_mfma_f32_16x16x32_bf16 v[156:159], v[92:95], v[48:51], v[156:159]
s_lshl_b32 s29, s89, 15
buffer_load_dwordx4 v23, s[12:15], s25 offen offset:3072 lds
v_mfma_f32_16x16x32_bf16 v[160:163], v[64:67], v[52:55], v[160:163]
s_sub_u32 s54, 0x80000, s29
ds_read_b128 v[48:51], v25 offset:0
v_mfma_f32_16x16x32_bf16 v[164:167], v[68:71], v[52:55], v[164:167]
s_add_u32 s24, s24, 128
s_add_u32 s25, s25, 128
v_mul_f32 v39, s47, v39
v_mfma_f32_16x16x32_bf16 v[168:171], v[72:75], v[52:55], v[168:171]
s_add_u32 m0, s91, 16384
ds_read_b64_tr_b16 v[108:109], v29 offset:8192
ds_read_b64_tr_b16 v[110:111], v29 offset:9216
v_mfma_f32_16x16x32_bf16 v[172:175], v[76:79], v[52:55], v[172:175]
s_sub_u32 s29, s54, s91
buffer_load_dwordx4 v16, s[16:19], s26 offen offset:0 lds
v_mfma_f32_16x16x32_bf16 v[176:179], v[80:83], v[52:55], v[176:179]
ds_read_b64_tr_b16 v[112:113], v30 offset:8192
ds_read_b64_tr_b16 v[114:115], v30 offset:9216
v_mfma_f32_16x16x32_bf16 v[180:183], v[84:87], v[52:55], v[180:183]
s_add_u32 s4, s84, s28
s_addc_u32 s5, s85, 0
v_cvt_pk_bf16_f32 v45, v38, v39
v_mfma_f32_16x16x32_bf16 v[184:187], v[88:91], v[52:55], v[184:187]
ds_read_b64_tr_b16 v[116:117], v31 offset:8192
ds_read_b64_tr_b16 v[118:119], v31 offset:9216
v_mfma_f32_16x16x32_bf16 v[188:191], v[92:95], v[52:55], v[188:191]
s_and_b32 s5, s5, 0xffff
buffer_load_dwordx4 v17, s[16:19], s26 offen offset:1024 lds
v_mfma_f32_16x16x32_bf16 v[192:195], v[64:67], v[56:59], v[192:195]
s_mov_b32 s6, s29
ds_read_b128 v[52:55], v25 offset:2048
v_mfma_f32_16x16x32_bf16 v[196:199], v[68:71], v[56:59], v[196:199]
s_mov_b32 s7, 0x20000
v_accvgpr_read_b32 v36, a84
v_accvgpr_read_b32 v37, a85
v_mfma_f32_16x16x32_bf16 v[200:203], v[72:75], v[56:59], v[200:203]
ds_read_b64_tr_b16 v[120:121], v32 offset:8192
ds_read_b64_tr_b16 v[122:123], v32 offset:9216
v_mfma_f32_16x16x32_bf16 v[204:207], v[76:79], v[56:59], v[204:207]
s_add_u32 s28, s28, 16760832
buffer_load_dwordx4 v18, s[16:19], s26 offen offset:2048 lds
v_mfma_f32_16x16x32_bf16 v[208:211], v[80:83], v[56:59], v[208:211]
ds_read_b64_tr_b16 v[124:125], v33 offset:8192
ds_read_b64_tr_b16 v[126:127], v33 offset:9216
v_mfma_f32_16x16x32_bf16 v[212:215], v[84:87], v[56:59], v[212:215]
s_add_u32 s29, s29, 16384
v_mul_f32 v36, s47, v36
v_mul_f32 v37, s47, v37
v_mfma_f32_16x16x32_bf16 v[216:219], v[88:91], v[56:59], v[216:219]
s_waitcnt lgkmcnt(15)
s_add_u32 s45, s54, 16384
v_cvt_pk_bf16_f32 v46, v36, v37
v_mfma_f32_16x16x32_bf16 v[220:223], v[92:95], v[56:59], v[220:223]
s_add_u32 s8, s84, s28
s_addc_u32 s9, s85, 0
v_accvgpr_read_b32 v38, a86
v_mfma_f32_16x16x32_bf16 v[224:227], v[64:67], v[60:63], v[224:227]
s_and_b32 s9, s9, 0xffff
ds_read_b128 v[56:59], v25 offset:4096
v_mfma_f32_16x16x32_bf16 v[228:231], v[68:71], v[60:63], v[228:231]
s_mov_b32 s10, s29
buffer_load_dwordx4 v19, s[16:19], s26 offen offset:3072 lds
v_mfma_f32_16x16x32_bf16 v[232:235], v[72:75], v[60:63], v[232:235]
s_mov_b32 s11, 0x20000
v_accvgpr_read_b32 v39, a87
v_mul_f32 v38, s47, v38
v_mfma_f32_16x16x32_bf16 v[236:239], v[76:79], v[60:63], v[236:239]
s_add_u32 s26, s26, 524288
v_mul_f32 v39, s47, v39
v_cvt_pk_bf16_f32 v47, v38, v39
v_mfma_f32_16x16x32_bf16 v[240:243], v[80:83], v[60:63], v[240:243]
s_and_b32 s46, s89, 1
v_permlane32_swap_b32 v44, v46
v_mfma_f32_16x16x32_bf16 v[244:247], v[84:87], v[60:63], v[244:247]
s_lshl_b32 s46, s46, 15
v_permlane32_swap_b32 v45, v47
v_mfma_f32_16x16x32_bf16 v[248:251], v[88:91], v[60:63], v[248:251]
s_add_u32 s46, s46, s91
s_xor_b32 s48, s95, 133120
v_accvgpr_read_b32 v36, a200
v_mfma_f32_16x16x32_bf16 v[252:255], v[92:95], v[60:63], v[252:255]
s_waitcnt vmcnt(17) lgkmcnt(0)
s_barrier
s_mov_b32 s39, s46
v_mfma_f32_16x16x32_bf16 v[128:131], v[96:99], v[48:51], v[128:131]
s_mov_b32 s27, 0x30000
ds_read_b128 v[60:63], v25 offset:6144
v_mfma_f32_16x16x32_bf16 v[132:135], v[100:103], v[48:51], v[132:135]
buffer_store_dwordx4 v[44:47], v7, s[20:23], s48 offen sc1
v_add_u32 v25, 4294868992, v25
v_mfma_f32_16x16x32_bf16 v[136:139], v[104:107], v[48:51], v[136:139]
s_add_u32 m0, s92, 131072
ds_read_b64_tr_b16 v[64:65], v26 offset:16384
ds_read_b64_tr_b16 v[66:67], v26 offset:17408
v_mfma_f32_16x16x32_bf16 v[140:143], v[108:111], v[48:51], v[140:143]
buffer_load_dwordx4 v20, s[12:15], s24 offen offset:0 lds
v_accvgpr_read_b32 v37, a201
v_mfma_f32_16x16x32_bf16 v[144:147], v[112:115], v[48:51], v[144:147]
ds_read_b64_tr_b16 v[68:69], v27 offset:16384
ds_read_b64_tr_b16 v[70:71], v27 offset:17408
v_mfma_f32_16x16x32_bf16 v[148:151], v[116:119], v[48:51], v[148:151]
v_mul_f32 v36, s47, v36
v_mul_f32 v37, s47, v37
v_mfma_f32_16x16x32_bf16 v[152:155], v[120:123], v[48:51], v[152:155]
ds_read_b64_tr_b16 v[72:73], v28 offset:16384
ds_read_b64_tr_b16 v[74:75], v28 offset:17408
v_mfma_f32_16x16x32_bf16 v[156:159], v[124:127], v[48:51], v[156:159]
buffer_load_dwordx4 v21, s[12:15], s24 offen offset:1024 lds
v_cvt_pk_bf16_f32 v40, v36, v37
v_mfma_f32_16x16x32_bf16 v[160:163], v[96:99], v[52:55], v[160:163]
ds_read_b128 v[48:51], v24 offset:0
v_accvgpr_read_b32 v38, a202
v_mfma_f32_16x16x32_bf16 v[164:167], v[100:103], v[52:55], v[164:167]
v_accvgpr_read_b32 v39, a203
v_mul_f32 v38, s47, v38
v_mfma_f32_16x16x32_bf16 v[168:171], v[104:107], v[52:55], v[168:171]
ds_read_b64_tr_b16 v[76:77], v29 offset:16384
ds_read_b64_tr_b16 v[78:79], v29 offset:17408
v_mfma_f32_16x16x32_bf16 v[172:175], v[108:111], v[52:55], v[172:175]
buffer_load_dwordx4 v22, s[12:15], s24 offen offset:2048 lds
v_mul_f32 v39, s47, v39
v_mfma_f32_16x16x32_bf16 v[176:179], v[112:115], v[52:55], v[176:179]
ds_read_b64_tr_b16 v[80:81], v30 offset:16384
ds_read_b64_tr_b16 v[82:83], v30 offset:17408
v_mfma_f32_16x16x32_bf16 v[180:183], v[116:119], v[52:55], v[180:183]
v_cvt_pk_bf16_f32 v41, v38, v39
v_accvgpr_read_b32 v36, a204
v_mfma_f32_16x16x32_bf16 v[184:187], v[120:123], v[52:55], v[184:187]
ds_read_b64_tr_b16 v[84:85], v31 offset:16384
ds_read_b64_tr_b16 v[86:87], v31 offset:17408
v_mfma_f32_16x16x32_bf16 v[188:191], v[124:127], v[52:55], v[188:191]
buffer_load_dwordx4 v23, s[12:15], s24 offen offset:3072 lds
v_accvgpr_read_b32 v37, a205
v_mfma_f32_16x16x32_bf16 v[192:195], v[96:99], v[56:59], v[192:195]
ds_read_b128 v[52:55], v24 offset:2048
v_mul_f32 v36, s47, v36
v_mfma_f32_16x16x32_bf16 v[196:199], v[100:103], v[56:59], v[196:199]
v_mul_f32 v37, s47, v37
v_cvt_pk_bf16_f32 v42, v36, v37
v_mfma_f32_16x16x32_bf16 v[200:203], v[104:107], v[56:59], v[200:203]
s_add_u32 m0, s92, 135168
ds_read_b64_tr_b16 v[88:89], v32 offset:16384
.Lpad7:
s_cbranch_execz .Ltch2
ds_read_b64_tr_b16 v[90:91], v32 offset:17408
v_mfma_f32_16x16x32_bf16 v[204:207], v[108:111], v[56:59], v[204:207]
buffer_load_dwordx4 v20, s[12:15], s25 offen offset:0 lds
v_accvgpr_read_b32 v38, a206
v_mfma_f32_16x16x32_bf16 v[208:211], v[112:115], v[56:59], v[208:211]
ds_read_b64_tr_b16 v[92:93], v33 offset:16384
ds_read_b64_tr_b16 v[94:95], v33 offset:17408
v_mfma_f32_16x16x32_bf16 v[212:215], v[116:119], v[56:59], v[212:215]
v_accvgpr_read_b32 v39, a207
v_mul_f32 v38, s47, v38
v_mfma_f32_16x16x32_bf16 v[216:219], v[120:123], v[56:59], v[216:219]
s_waitcnt lgkmcnt(15)
v_mul_f32 v39, s47, v39
v_cvt_pk_bf16_f32 v43, v38, v39
v_mfma_f32_16x16x32_bf16 v[220:223], v[124:127], v[56:59], v[220:223]
v_permlane32_swap_b32 v40, v42
v_mfma_f32_16x16x32_bf16 v[224:227], v[96:99], v[60:63], v[224:227]
ds_read_b128 v[56:59], v24 offset:4096
v_mfma_f32_16x16x32_bf16 v[228:231], v[100:103], v[60:63], v[228:231]
buffer_load_dwordx4 v21, s[12:15], s25 offen offset:1024 lds
v_mfma_f32_16x16x32_bf16 v[232:235], v[104:107], v[60:63], v[232:235]
v_permlane32_swap_b32 v41, v43
v_mfma_f32_16x16x32_bf16 v[236:239], v[108:111], v[60:63], v[236:239]
v_accvgpr_write_b32 a196, v40
v_accvgpr_write_b32 a197, v41
v_mfma_f32_16x16x32_bf16 v[240:243], v[112:115], v[60:63], v[240:243]
v_accvgpr_write_b32 a198, v42
v_accvgpr_write_b32 a199, v43
v_mfma_f32_16x16x32_bf16 v[244:247], v[116:119], v[60:63], v[244:247]
v_accvgpr_read_b32 v36, a88
v_accvgpr_read_b32 v37, a89
v_mfma_f32_16x16x32_bf16 v[248:251], v[120:123], v[60:63], v[248:251]
v_mul_f32 v36, s47, v36
v_mul_f32 v37, s47, v37
v_mfma_f32_16x16x32_bf16 v[252:255], v[124:127], v[60:63], v[252:255]
s_waitcnt lgkmcnt(0)
v_cvt_pk_bf16_f32 v44, v36, v37
v_accvgpr_read_b32 v38, a90
v_mfma_f32_16x16x32_bf16 v[128:131], v[64:67], v[48:51], v[128:131]
ds_read_b128 v[60:63], v24 offset:6144
v_accvgpr_read_b32 v39, a91
v_mfma_f32_16x16x32_bf16 v[132:135], v[68:71], v[48:51], v[132:135]
v_add_u32 v24, 65536, v24
v_mul_f32 v38, s47, v38
v_mfma_f32_16x16x32_bf16 v[136:139], v[72:75], v[48:51], v[136:139]
ds_read_b64_tr_b16 v[96:97], v26 offset:24576
ds_read_b64_tr_b16 v[98:99], v26 offset:25600
v_mfma_f32_16x16x32_bf16 v[140:143], v[76:79], v[48:51], v[140:143]
buffer_load_dwordx4 v22, s[12:15], s25 offen offset:2048 lds
v_mul_f32 v39, s47, v39
v_mfma_f32_16x16x32_bf16 v[144:147], v[80:83], v[48:51], v[144:147]
ds_read_b64_tr_b16 v[100:101], v27 offset:24576
ds_read_b64_tr_b16 v[102:103], v27 offset:25600
v_mfma_f32_16x16x32_bf16 v[148:151], v[84:87], v[48:51], v[148:151]
v_cvt_pk_bf16_f32 v45, v38, v39
v_accvgpr_read_b32 v36, a92
v_mfma_f32_16x16x32_bf16 v[152:155], v[88:91], v[48:51], v[152:155]
ds_read_b64_tr_b16 v[104:105], v28 offset:24576
ds_read_b64_tr_b16 v[106:107], v28 offset:25600
v_mfma_f32_16x16x32_bf16 v[156:159], v[92:95], v[48:51], v[156:159]
buffer_load_dwordx4 v23, s[12:15], s25 offen offset:3072 lds
v_accvgpr_read_b32 v37, a93
v_mfma_f32_16x16x32_bf16 v[160:163], v[64:67], v[52:55], v[160:163]
ds_read_b128 v[48:51], v25 offset:0
v_mul_f32 v36, s47, v36
v_mfma_f32_16x16x32_bf16 v[164:167], v[68:71], v[52:55], v[164:167]
s_add_u32 s24, s24, 128
s_add_u32 s25, s25, 128
buffer_load_dword v35, v4, s[96:99], 0 offen
v_mfma_f32_16x16x32_bf16 v[168:171], v[72:75], v[52:55], v[168:171]
s_add_u32 m0, s91, 65536
ds_read_b64_tr_b16 v[108:109], v29 offset:24576
ds_read_b64_tr_b16 v[110:111], v29 offset:25600
v_mfma_f32_16x16x32_bf16 v[172:175], v[76:79], v[52:55], v[172:175]
buffer_load_dwordx4 v16, s[16:19], s26 offen offset:0 lds
v_mul_f32 v37, s47, v37
v_mfma_f32_16x16x32_bf16 v[176:179], v[80:83], v[52:55], v[176:179]
ds_read_b64_tr_b16 v[112:113], v30 offset:24576
ds_read_b64_tr_b16 v[114:115], v30 offset:25600
v_mfma_f32_16x16x32_bf16 v[180:183], v[84:87], v[52:55], v[180:183]
v_cvt_pk_bf16_f32 v46, v36, v37
v_accvgpr_read_b32 v38, a94
v_mfma_f32_16x16x32_bf16 v[184:187], v[88:91], v[52:55], v[184:187]
ds_read_b64_tr_b16 v[116:117], v31 offset:24576
ds_read_b64_tr_b16 v[118:119], v31 offset:25600
v_mfma_f32_16x16x32_bf16 v[188:191], v[92:95], v[52:55], v[188:191]
buffer_load_dwordx4 v17, s[16:19], s26 offen offset:1024 lds
v_accvgpr_read_b32 v39, a95
v_mfma_f32_16x16x32_bf16 v[192:195], v[64:67], v[56:59], v[192:195]
ds_read_b128 v[52:55], v25 offset:2048
v_mul_f32 v38, s47, v38
v_mfma_f32_16x16x32_bf16 v[196:199], v[68:71], v[56:59], v[196:199]
v_mul_f32 v39, s47, v39
v_cvt_pk_bf16_f32 v47, v38, v39
v_mfma_f32_16x16x32_bf16 v[200:203], v[72:75], v[56:59], v[200:203]
ds_read_b64_tr_b16 v[120:121], v32 offset:24576
ds_read_b64_tr_b16 v[122:123], v32 offset:25600
v_mfma_f32_16x16x32_bf16 v[204:207], v[76:79], v[56:59], v[204:207]
buffer_load_dwordx4 v18, s[16:19], s26 offen offset:2048 lds
v_mfma_f32_16x16x32_bf16 v[208:211], v[80:83], v[56:59], v[208:211]
ds_read_b64_tr_b16 v[124:125], v33 offset:24576
ds_read_b64_tr_b16 v[126:127], v33 offset:25600
v_mfma_f32_16x16x32_bf16 v[212:215], v[84:87], v[56:59], v[212:215]
v_permlane32_swap_b32 v44, v46
v_mfma_f32_16x16x32_bf16 v[216:219], v[88:91], v[56:59], v[216:219]
s_waitcnt lgkmcnt(15)
v_permlane32_swap_b32 v45, v47
v_mfma_f32_16x16x32_bf16 v[220:223], v[92:95], v[56:59], v[220:223]
s_xor_b32 s48, s95, 134144
v_xor_b32 v26, 0x10000, v26
v_mfma_f32_16x16x32_bf16 v[224:227], v[64:67], v[60:63], v[224:227]
ds_read_b128 v[56:59], v25 offset:4096
v_xor_b32 v27, 0x10000, v27
v_mfma_f32_16x16x32_bf16 v[228:231], v[68:71], v[60:63], v[228:231]
buffer_load_dwordx4 v19, s[16:19], s26 offen offset:3072 lds
v_xor_b32 v28, 0x10000, v28
v_mfma_f32_16x16x32_bf16 v[232:235], v[72:75], v[60:63], v[232:235]
buffer_store_dwordx4 v[44:47], v7, s[20:23], s48 offen sc1
v_xor_b32 v29, 0x10000, v29
v_mfma_f32_16x16x32_bf16 v[236:239], v[76:79], v[60:63], v[236:239]
s_add_u32 s26, s26, 524288
v_xor_b32 v30, 0x10000, v30
v_accvgpr_read_b32 v36, a96
v_mfma_f32_16x16x32_bf16 v[240:243], v[80:83], v[60:63], v[240:243]
v_xor_b32 v31, 0x10000, v31
v_accvgpr_read_b32 v37, a97
v_mfma_f32_16x16x32_bf16 v[244:247], v[84:87], v[60:63], v[244:247]
v_xor_b32 v32, 0x10000, v32
v_mul_f32 v36, s47, v36
v_mfma_f32_16x16x32_bf16 v[248:251], v[88:91], v[60:63], v[248:251]
v_xor_b32 v33, 0x10000, v33
v_mul_f32 v37, s47, v37
v_mfma_f32_16x16x32_bf16 v[252:255], v[92:95], v[60:63], v[252:255]
s_waitcnt vmcnt(19) lgkmcnt(0)
s_barrier
v_cvt_pk_bf16_f32 v40, v36, v37
v_mfma_f32_16x16x32_bf16 v[128:131], v[96:99], v[48:51], v[128:131]
ds_read_b128 v[60:63], v25 offset:6144
v_accvgpr_read_b32 v38, a98
v_mfma_f32_16x16x32_bf16 v[132:135], v[100:103], v[48:51], v[132:135]
v_add_u32 v25, 65536, v25
v_accvgpr_read_b32 v39, a99
v_mfma_f32_16x16x32_bf16 v[136:139], v[104:107], v[48:51], v[136:139]
s_add_u32 m0, s92, 32768
ds_read_b64_tr_b16 v[64:65], v26 offset:0
ds_read_b64_tr_b16 v[66:67], v26 offset:1024
v_mfma_f32_16x16x32_bf16 v[140:143], v[108:111], v[48:51], v[140:143]
buffer_load_dwordx4 v20, s[12:15], s24 offen offset:0 lds
v_mul_f32 v38, s47, v38
v_mfma_f32_16x16x32_bf16 v[144:147], v[112:115], v[48:51], v[144:147]
ds_read_b64_tr_b16 v[68:69], v27 offset:0
ds_read_b64_tr_b16 v[70:71], v27 offset:1024
v_mfma_f32_16x16x32_bf16 v[148:151], v[116:119], v[48:51], v[148:151]
v_mul_f32 v39, s47, v39
v_cvt_pk_bf16_f32 v41, v38, v39
v_mfma_f32_16x16x32_bf16 v[152:155], v[120:123], v[48:51], v[152:155]
ds_read_b64_tr_b16 v[72:73], v28 offset:0
ds_read_b64_tr_b16 v[74:75], v28 offset:1024
v_mfma_f32_16x16x32_bf16 v[156:159], v[124:127], v[48:51], v[156:159]
buffer_load_dwordx4 v21, s[12:15], s24 offen offset:1024 lds
v_accvgpr_read_b32 v36, a100
v_mfma_f32_16x16x32_bf16 v[160:163], v[96:99], v[52:55], v[160:163]
ds_read_b128 v[48:51], v24 offset:0
v_accvgpr_read_b32 v37, a101
v_mfma_f32_16x16x32_bf16 v[164:167], v[100:103], v[52:55], v[164:167]
v_mul_f32 v36, s47, v36
v_mul_f32 v37, s47, v37
v_mfma_f32_16x16x32_bf16 v[168:171], v[104:107], v[52:55], v[168:171]
ds_read_b64_tr_b16 v[76:77], v29 offset:0
ds_read_b64_tr_b16 v[78:79], v29 offset:1024
v_mfma_f32_16x16x32_bf16 v[172:175], v[108:111], v[52:55], v[172:175]
buffer_load_dwordx4 v22, s[12:15], s24 offen offset:2048 lds
v_cvt_pk_bf16_f32 v42, v36, v37
v_mfma_f32_16x16x32_bf16 v[176:179], v[112:115], v[52:55], v[176:179]
ds_read_b64_tr_b16 v[80:81], v30 offset:0
ds_read_b64_tr_b16 v[82:83], v30 offset:1024
v_mfma_f32_16x16x32_bf16 v[180:183], v[116:119], v[52:55], v[180:183]
v_accvgpr_read_b32 v38, a102
v_accvgpr_read_b32 v39, a103
v_mfma_f32_16x16x32_bf16 v[184:187], v[120:123], v[52:55], v[184:187]
ds_read_b64_tr_b16 v[84:85], v31 offset:0
ds_read_b64_tr_b16 v[86:87], v31 offset:1024
v_mfma_f32_16x16x32_bf16 v[188:191], v[124:127], v[52:55], v[188:191]
buffer_load_dwordx4 v23, s[12:15], s24 offen offset:3072 lds
v_mul_f32 v38, s47, v38
v_mfma_f32_16x16x32_bf16 v[192:195], v[96:99], v[56:59], v[192:195]
ds_read_b128 v[52:55], v24 offset:2048
v_mul_f32 v39, s47, v39
v_mfma_f32_16x16x32_bf16 v[196:199], v[100:103], v[56:59], v[196:199]
v_cvt_pk_bf16_f32 v43, v38, v39
v_mfma_f32_16x16x32_bf16 v[200:203], v[104:107], v[56:59], v[200:203]
s_add_u32 m0, s92, 36864
ds_read_b64_tr_b16 v[88:89], v32 offset:0
ds_read_b64_tr_b16 v[90:91], v32 offset:1024
v_mfma_f32_16x16x32_bf16 v[204:207], v[108:111], v[56:59], v[204:207]
buffer_load_dwordx4 v20, s[12:15], s25 offen offset:0 lds
v_accvgpr_read_b32 v36, a208
v_mfma_f32_16x16x32_bf16 v[208:211], v[112:115], v[56:59], v[208:211]
ds_read_b64_tr_b16 v[92:93], v33 offset:0
ds_read_b64_tr_b16 v[94:95], v33 offset:1024
v_mfma_f32_16x16x32_bf16 v[212:215], v[116:119], v[56:59], v[212:215]
v_permlane32_swap_b32 v40, v42
v_mfma_f32_16x16x32_bf16 v[216:219], v[120:123], v[56:59], v[216:219]
s_waitcnt lgkmcnt(15)
v_permlane32_swap_b32 v41, v43
v_mfma_f32_16x16x32_bf16 v[220:223], v[124:127], v[56:59], v[220:223]
s_xor_b32 s48, s95, 196608
buffer_store_dwordx4 v[40:43], v7, s[20:23], s48 offen sc1
v_mfma_f32_16x16x32_bf16 v[224:227], v[96:99], v[60:63], v[224:227]
ds_read_b128 v[56:59], v24 offset:4096
v_accvgpr_read_b32 v37, a209
v_mfma_f32_16x16x32_bf16 v[228:231], v[100:103], v[60:63], v[228:231]
buffer_load_dwordx4 v21, s[12:15], s25 offen offset:1024 lds
v_mul_f32 v36, s47, v36
v_mfma_f32_16x16x32_bf16 v[232:235], v[104:107], v[60:63], v[232:235]
v_mul_f32 v37, s47, v37
v_cvt_pk_bf16_f32 v44, v36, v37
v_mfma_f32_16x16x32_bf16 v[236:239], v[108:111], v[60:63], v[236:239]
v_accvgpr_read_b32 v38, a210
v_accvgpr_read_b32 v39, a211
v_mfma_f32_16x16x32_bf16 v[240:243], v[112:115], v[60:63], v[240:243]
v_mul_f32 v38, s47, v38
v_mul_f32 v39, s47, v39
v_mfma_f32_16x16x32_bf16 v[244:247], v[116:119], v[60:63], v[244:247]
v_cvt_pk_bf16_f32 v45, v38, v39
v_accvgpr_read_b32 v36, a212
v_mfma_f32_16x16x32_bf16 v[248:251], v[120:123], v[60:63], v[248:251]
v_accvgpr_read_b32 v37, a213
v_mul_f32 v36, s47, v36
v_mfma_f32_16x16x32_bf16 v[252:255], v[124:127], v[60:63], v[252:255]
s_waitcnt lgkmcnt(0)
v_mul_f32 v37, s47, v37
v_cvt_pk_bf16_f32 v46, v36, v37
v_mfma_f32_16x16x32_bf16 v[128:131], v[64:67], v[48:51], v[128:131]
ds_read_b128 v[60:63], v24 offset:6144
v_accvgpr_read_b32 v38, a214
v_mfma_f32_16x16x32_bf16 v[132:135], v[68:71], v[48:51], v[132:135]
v_add_u32 v24, 32768, v24
v_accvgpr_read_b32 v39, a215
v_mfma_f32_16x16x32_bf16 v[136:139], v[72:75], v[48:51], v[136:139]
ds_read_b64_tr_b16 v[96:97], v26 offset:8192
ds_read_b64_tr_b16 v[98:99], v26 offset:9216
v_mfma_f32_16x16x32_bf16 v[140:143], v[76:79], v[48:51], v[140:143]
buffer_load_dwordx4 v22, s[12:15], s25 offen offset:2048 lds
v_mul_f32 v38, s47, v38
v_mfma_f32_16x16x32_bf16 v[144:147], v[80:83], v[48:51], v[144:147]
ds_read_b64_tr_b16 v[100:101], v27 offset:8192
ds_read_b64_tr_b16 v[102:103], v27 offset:9216
v_mfma_f32_16x16x32_bf16 v[148:151], v[84:87], v[48:51], v[148:151]
v_mul_f32 v39, s47, v39
v_cvt_pk_bf16_f32 v47, v38, v39
v_mfma_f32_16x16x32_bf16 v[152:155], v[88:91], v[48:51], v[152:155]
ds_read_b64_tr_b16 v[104:105], v28 offset:8192
ds_read_b64_tr_b16 v[106:107], v28 offset:9216
v_mfma_f32_16x16x32_bf16 v[156:159], v[92:95], v[48:51], v[156:159]
buffer_load_dwordx4 v23, s[12:15], s25 offen offset:3072 lds
v_mfma_f32_16x16x32_bf16 v[160:163], v[64:67], v[52:55], v[160:163]
ds_read_b128 v[48:51], v25 offset:0
v_mfma_f32_16x16x32_bf16 v[164:167], v[68:71], v[52:55], v[164:167]
s_add_u32 s24, s24, 128
s_add_u32 s25, s25, 128
v_mfma_f32_16x16x32_bf16 v[168:171], v[72:75], v[52:55], v[168:171]
s_add_u32 m0, s91, 81920
ds_read_b64_tr_b16 v[108:109], v29 offset:8192
ds_read_b64_tr_b16 v[110:111], v29 offset:9216
v_mfma_f32_16x16x32_bf16 v[172:175], v[76:79], v[52:55], v[172:175]
buffer_load_dwordx4 v16, s[16:19], s26 offen offset:0 lds
v_mfma_f32_16x16x32_bf16 v[176:179], v[80:83], v[52:55], v[176:179]
ds_read_b64_tr_b16 v[112:113], v30 offset:8192
ds_read_b64_tr_b16 v[114:115], v30 offset:9216
v_mfma_f32_16x16x32_bf16 v[180:183], v[84:87], v[52:55], v[180:183]
v_permlane32_swap_b32 v44, v46
v_mfma_f32_16x16x32_bf16 v[184:187], v[88:91], v[52:55], v[184:187]
ds_read_b64_tr_b16 v[116:117], v31 offset:8192
ds_read_b64_tr_b16 v[118:119], v31 offset:9216
v_mfma_f32_16x16x32_bf16 v[188:191], v[92:95], v[52:55], v[188:191]
buffer_load_dwordx4 v17, s[16:19], s26 offen offset:1024 lds
v_mfma_f32_16x16x32_bf16 v[192:195], v[64:67], v[56:59], v[192:195]
ds_read_b128 v[52:55], v25 offset:2048
v_mfma_f32_16x16x32_bf16 v[196:199], v[68:71], v[56:59], v[196:199]
v_permlane32_swap_b32 v45, v47
v_mfma_f32_16x16x32_bf16 v[200:203], v[72:75], v[56:59], v[200:203]
ds_read_b64_tr_b16 v[120:121], v32 offset:8192
ds_read_b64_tr_b16 v[122:123], v32 offset:9216
v_mfma_f32_16x16x32_bf16 v[204:207], v[76:79], v[56:59], v[204:207]
buffer_load_dwordx4 v18, s[16:19], s26 offen offset:2048 lds
v_accvgpr_write_b32 a200, v44
v_mfma_f32_16x16x32_bf16 v[208:211], v[80:83], v[56:59], v[208:211]
ds_read_b64_tr_b16 v[124:125], v33 offset:8192
ds_read_b64_tr_b16 v[126:127], v33 offset:9216
v_mfma_f32_16x16x32_bf16 v[212:215], v[84:87], v[56:59], v[212:215]
v_accvgpr_write_b32 a201, v45
v_accvgpr_write_b32 a202, v46
v_mfma_f32_16x16x32_bf16 v[216:219], v[88:91], v[56:59], v[216:219]
s_waitcnt lgkmcnt(15)
v_accvgpr_write_b32 a203, v47
v_accvgpr_read_b32 v36, a104
v_mfma_f32_16x16x32_bf16 v[220:223], v[92:95], v[56:59], v[220:223]
v_accvgpr_read_b32 v37, a105
v_mul_f32 v36, s47, v36
v_mfma_f32_16x16x32_bf16 v[224:227], v[64:67], v[60:63], v[224:227]
ds_read_b128 v[56:59], v25 offset:4096
v_mul_f32 v37, s47, v37
v_mfma_f32_16x16x32_bf16 v[228:231], v[68:71], v[60:63], v[228:231]
buffer_load_dwordx4 v19, s[16:19], s26 offen offset:3072 lds
v_cvt_pk_bf16_f32 v40, v36, v37
v_mfma_f32_16x16x32_bf16 v[232:235], v[72:75], v[60:63], v[232:235]
v_accvgpr_read_b32 v38, a106
v_accvgpr_read_b32 v39, a107
v_mfma_f32_16x16x32_bf16 v[236:239], v[76:79], v[60:63], v[236:239]
s_add_u32 s26, s26, 524288
v_mul_f32 v38, s47, v38
v_mul_f32 v39, s47, v39
v_mfma_f32_16x16x32_bf16 v[240:243], v[80:83], v[60:63], v[240:243]
v_cvt_pk_bf16_f32 v41, v38, v39
v_accvgpr_read_b32 v36, a108
v_mfma_f32_16x16x32_bf16 v[244:247], v[84:87], v[60:63], v[244:247]
v_accvgpr_read_b32 v37, a109
v_mul_f32 v36, s47, v36
v_mfma_f32_16x16x32_bf16 v[248:251], v[88:91], v[60:63], v[248:251]
v_mul_f32 v37, s47, v37
v_cvt_pk_bf16_f32 v42, v36, v37
v_mfma_f32_16x16x32_bf16 v[252:255], v[92:95], v[60:63], v[252:255]
s_waitcnt vmcnt(19) lgkmcnt(0)
s_barrier
v_accvgpr_read_b32 v38, a110
v_mfma_f32_16x16x32_bf16 v[128:131], v[96:99], v[48:51], v[128:131]
ds_read_b128 v[60:63], v25 offset:6144
v_accvgpr_read_b32 v39, a111
v_mfma_f32_16x16x32_bf16 v[132:135], v[100:103], v[48:51], v[132:135]
v_add_u32 v25, 32768, v25
v_mul_f32 v38, s47, v38
v_mfma_f32_16x16x32_bf16 v[136:139], v[104:107], v[48:51], v[136:139]
s_add_u32 m0, s92, 98304
ds_read_b64_tr_b16 v[64:65], v26 offset:16384
ds_read_b64_tr_b16 v[66:67], v26 offset:17408
v_mfma_f32_16x16x32_bf16 v[140:143], v[108:111], v[48:51], v[140:143]
buffer_load_dwordx4 v20, s[12:15], s24 offen offset:0 lds
v_mul_f32 v39, s47, v39
v_mfma_f32_16x16x32_bf16 v[144:147], v[112:115], v[48:51], v[144:147]
ds_read_b64_tr_b16 v[68:69], v27 offset:16384
ds_read_b64_tr_b16 v[70:71], v27 offset:17408
v_mfma_f32_16x16x32_bf16 v[148:151], v[116:119], v[48:51], v[148:151]
v_cvt_pk_bf16_f32 v43, v38, v39
v_mfma_f32_16x16x32_bf16 v[152:155], v[120:123], v[48:51], v[152:155]
ds_read_b64_tr_b16 v[72:73], v28 offset:16384
ds_read_b64_tr_b16 v[74:75], v28 offset:17408
v_mfma_f32_16x16x32_bf16 v[156:159], v[124:127], v[48:51], v[156:159]
buffer_load_dwordx4 v21, s[12:15], s24 offen offset:1024 lds
v_accvgpr_read_b32 v36, a216
v_mfma_f32_16x16x32_bf16 v[160:163], v[96:99], v[52:55], v[160:163]
ds_read_b128 v[48:51], v24 offset:0
v_accvgpr_read_b32 v37, a217
v_mfma_f32_16x16x32_bf16 v[164:167], v[100:103], v[52:55], v[164:167]
v_permlane32_swap_b32 v40, v42
v_mfma_f32_16x16x32_bf16 v[168:171], v[104:107], v[52:55], v[168:171]
ds_read_b64_tr_b16 v[76:77], v29 offset:16384
ds_read_b64_tr_b16 v[78:79], v29 offset:17408
v_mfma_f32_16x16x32_bf16 v[172:175], v[108:111], v[52:55], v[172:175]
buffer_load_dwordx4 v22, s[12:15], s24 offen offset:2048 lds
v_mul_f32 v36, s47, v36
v_mfma_f32_16x16x32_bf16 v[176:179], v[112:115], v[52:55], v[176:179]
ds_read_b64_tr_b16 v[80:81], v30 offset:16384
ds_read_b64_tr_b16 v[82:83], v30 offset:17408
v_mfma_f32_16x16x32_bf16 v[180:183], v[116:119], v[52:55], v[180:183]
v_permlane32_swap_b32 v41, v43
v_mfma_f32_16x16x32_bf16 v[184:187], v[120:123], v[52:55], v[184:187]
ds_read_b64_tr_b16 v[84:85], v31 offset:16384
ds_read_b64_tr_b16 v[86:87], v31 offset:17408
v_mfma_f32_16x16x32_bf16 v[188:191], v[124:127], v[52:55], v[188:191]
s_xor_b32 s48, s95, 197632
buffer_load_dwordx4 v23, s[12:15], s24 offen offset:3072 lds
v_mfma_f32_16x16x32_bf16 v[192:195], v[96:99], v[56:59], v[192:195]
ds_read_b128 v[52:55], v24 offset:2048
v_mul_f32 v37, s47, v37
v_mfma_f32_16x16x32_bf16 v[196:199], v[100:103], v[56:59], v[196:199]
buffer_store_dwordx4 v[40:43], v7, s[20:23], s48 offen sc1
v_cvt_pk_bf16_f32 v44, v36, v37
v_mfma_f32_16x16x32_bf16 v[200:203], v[104:107], v[56:59], v[200:203]
s_add_u32 m0, s92, 102400
ds_read_b64_tr_b16 v[88:89], v32 offset:16384
ds_read_b64_tr_b16 v[90:91], v32 offset:17408
v_mfma_f32_16x16x32_bf16 v[204:207], v[108:111], v[56:59], v[204:207]
buffer_load_dwordx4 v20, s[12:15], s25 offen offset:0 lds
v_accvgpr_read_b32 v38, a218
v_mfma_f32_16x16x32_bf16 v[208:211], v[112:115], v[56:59], v[208:211]
ds_read_b64_tr_b16 v[92:93], v33 offset:16384
ds_read_b64_tr_b16 v[94:95], v33 offset:17408
v_mfma_f32_16x16x32_bf16 v[212:215], v[116:119], v[56:59], v[212:215]
v_accvgpr_read_b32 v39, a219
v_mul_f32 v38, s47, v38
v_mfma_f32_16x16x32_bf16 v[216:219], v[120:123], v[56:59], v[216:219]
s_waitcnt lgkmcnt(15)
v_mul_f32 v39, s47, v39
v_cvt_pk_bf16_f32 v45, v38, v39
v_mfma_f32_16x16x32_bf16 v[220:223], v[124:127], v[56:59], v[220:223]
v_accvgpr_read_b32 v36, a220
v_accvgpr_read_b32 v37, a221
v_mfma_f32_16x16x32_bf16 v[224:227], v[96:99], v[60:63], v[224:227]
ds_read_b128 v[56:59], v24 offset:4096
v_mul_f32 v36, s47, v36
v_mfma_f32_16x16x32_bf16 v[228:231], v[100:103], v[60:63], v[228:231]
buffer_load_dwordx4 v21, s[12:15], s25 offen offset:1024 lds
v_mul_f32 v37, s47, v37
v_mfma_f32_16x16x32_bf16 v[232:235], v[104:107], v[60:63], v[232:235]
v_cvt_pk_bf16_f32 v46, v36, v37
v_accvgpr_read_b32 v38, a222
v_mfma_f32_16x16x32_bf16 v[236:239], v[108:111], v[60:63], v[236:239]
v_accvgpr_read_b32 v39, a223
v_mul_f32 v38, s47, v38
v_mfma_f32_16x16x32_bf16 v[240:243], v[112:115], v[60:63], v[240:243]
v_mul_f32 v39, s47, v39
v_cvt_pk_bf16_f32 v47, v38, v39
v_mfma_f32_16x16x32_bf16 v[244:247], v[116:119], v[60:63], v[244:247]
v_permlane32_swap_b32 v44, v46
v_mfma_f32_16x16x32_bf16 v[248:251], v[120:123], v[60:63], v[248:251]
v_permlane32_swap_b32 v45, v47
v_mfma_f32_16x16x32_bf16 v[252:255], v[124:127], v[60:63], v[252:255]
s_waitcnt lgkmcnt(0)
v_accvgpr_write_b32 a204, v44
v_accvgpr_write_b32 a205, v45
v_mfma_f32_16x16x32_bf16 v[128:131], v[64:67], v[48:51], v[128:131]
ds_read_b128 v[60:63], v24 offset:6144
v_accvgpr_write_b32 a206, v46
v_mfma_f32_16x16x32_bf16 v[132:135], v[68:71], v[48:51], v[132:135]
v_add_u32 v24, 4294868992, v24
v_accvgpr_write_b32 a207, v47
v_mfma_f32_16x16x32_bf16 v[136:139], v[72:75], v[48:51], v[136:139]
ds_read_b64_tr_b16 v[96:97], v26 offset:24576
ds_read_b64_tr_b16 v[98:99], v26 offset:25600
v_mfma_f32_16x16x32_bf16 v[140:143], v[76:79], v[48:51], v[140:143]
buffer_load_dwordx4 v22, s[12:15], s25 offen offset:2048 lds
v_accvgpr_read_b32 v36, a112
v_mfma_f32_16x16x32_bf16 v[144:147], v[80:83], v[48:51], v[144:147]
ds_read_b64_tr_b16 v[100:101], v27 offset:24576
ds_read_b64_tr_b16 v[102:103], v27 offset:25600
v_mfma_f32_16x16x32_bf16 v[148:151], v[84:87], v[48:51], v[148:151]
v_accvgpr_read_b32 v37, a113
v_mul_f32 v36, s47, v36
v_mfma_f32_16x16x32_bf16 v[152:155], v[88:91], v[48:51], v[152:155]
ds_read_b64_tr_b16 v[104:105], v28 offset:24576
ds_read_b64_tr_b16 v[106:107], v28 offset:25600
v_mfma_f32_16x16x32_bf16 v[156:159], v[92:95], v[48:51], v[156:159]
buffer_load_dwordx4 v23, s[12:15], s25 offen offset:3072 lds
v_mul_f32 v37, s47, v37
v_mfma_f32_16x16x32_bf16 v[160:163], v[64:67], v[52:55], v[160:163]
ds_read_b128 v[48:51], v25 offset:0
v_cvt_pk_bf16_f32 v40, v36, v37
v_mfma_f32_16x16x32_bf16 v[164:167], v[68:71], v[52:55], v[164:167]
s_add_u32 s24, s24, 128
s_add_u32 s25, s25, 128
v_accvgpr_read_b32 v38, a114
v_mfma_f32_16x16x32_bf16 v[168:171], v[72:75], v[52:55], v[168:171]
s_add_u32 m0, s91, 0
ds_read_b64_tr_b16 v[108:109], v29 offset:24576
ds_read_b64_tr_b16 v[110:111], v29 offset:25600
v_mfma_f32_16x16x32_bf16 v[172:175], v[76:79], v[52:55], v[172:175]
buffer_load_dwordx4 v16, s[16:19], s26 offen offset:0 lds
v_accvgpr_read_b32 v39, a115
v_mfma_f32_16x16x32_bf16 v[176:179], v[80:83], v[52:55], v[176:179]
ds_read_b64_tr_b16 v[112:113], v30 offset:24576
ds_read_b64_tr_b16 v[114:115], v30 offset:25600
v_mfma_f32_16x16x32_bf16 v[180:183], v[84:87], v[52:55], v[180:183]
v_mul_f32 v38, s47, v38
v_mul_f32 v39, s47, v39
v_mfma_f32_16x16x32_bf16 v[184:187], v[88:91], v[52:55], v[184:187]
ds_read_b64_tr_b16 v[116:117], v31 offset:24576
ds_read_b64_tr_b16 v[118:119], v31 offset:25600
v_mfma_f32_16x16x32_bf16 v[188:191], v[92:95], v[52:55], v[188:191]
buffer_load_dwordx4 v17, s[16:19], s26 offen offset:1024 lds
v_cvt_pk_bf16_f32 v41, v38, v39
v_mfma_f32_16x16x32_bf16 v[192:195], v[64:67], v[56:59], v[192:195]
ds_read_b128 v[52:55], v25 offset:2048
v_accvgpr_read_b32 v36, a116
v_mfma_f32_16x16x32_bf16 v[196:199], v[68:71], v[56:59], v[196:199]
v_accvgpr_read_b32 v37, a117
v_mul_f32 v36, s47, v36
v_mfma_f32_16x16x32_bf16 v[200:203], v[72:75], v[56:59], v[200:203]
ds_read_b64_tr_b16 v[120:121], v32 offset:24576
ds_read_b64_tr_b16 v[122:123], v32 offset:25600
v_mfma_f32_16x16x32_bf16 v[204:207], v[76:79], v[56:59], v[204:207]
buffer_load_dwordx4 v18, s[16:19], s26 offen offset:2048 lds
v_mul_f32 v37, s47, v37
v_mfma_f32_16x16x32_bf16 v[208:211], v[80:83], v[56:59], v[208:211]
ds_read_b64_tr_b16 v[124:125], v33 offset:24576
ds_read_b64_tr_b16 v[126:127], v33 offset:25600
v_mfma_f32_16x16x32_bf16 v[212:215], v[84:87], v[56:59], v[212:215]
v_cvt_pk_bf16_f32 v42, v36, v37
v_accvgpr_read_b32 v38, a118
v_mfma_f32_16x16x32_bf16 v[216:219], v[88:91], v[56:59], v[216:219]
s_waitcnt lgkmcnt(15)
v_accvgpr_read_b32 v39, a119
v_mul_f32 v38, s47, v38
v_mfma_f32_16x16x32_bf16 v[220:223], v[92:95], v[56:59], v[220:223]
v_xor_b32 v26, 0x10000, v26
v_mul_f32 v39, s47, v39
v_mfma_f32_16x16x32_bf16 v[224:227], v[64:67], v[60:63], v[224:227]
ds_read_b128 v[56:59], v25 offset:4096
v_xor_b32 v27, 0x10000, v27
v_mfma_f32_16x16x32_bf16 v[228:231], v[68:71], v[60:63], v[228:231]
buffer_load_dwordx4 v19, s[16:19], s26 offen offset:3072 lds
v_xor_b32 v28, 0x10000, v28
v_mfma_f32_16x16x32_bf16 v[232:235], v[72:75], v[60:63], v[232:235]
v_xor_b32 v29, 0x10000, v29
v_cvt_pk_bf16_f32 v43, v38, v39
v_mfma_f32_16x16x32_bf16 v[236:239], v[76:79], v[60:63], v[236:239]
s_add_u32 s26, s26, 524288
v_xor_b32 v30, 0x10000, v30
v_accvgpr_read_b32 v36, a224
v_mfma_f32_16x16x32_bf16 v[240:243], v[80:83], v[60:63], v[240:243]
v_xor_b32 v31, 0x10000, v31
v_accvgpr_read_b32 v37, a225
v_mfma_f32_16x16x32_bf16 v[244:247], v[84:87], v[60:63], v[244:247]
v_xor_b32 v32, 0x10000, v32
v_mul_f32 v36, s47, v36
v_mfma_f32_16x16x32_bf16 v[248:251], v[88:91], v[60:63], v[248:251]
v_xor_b32 v33, 0x10000, v33
v_mul_f32 v37, s47, v37
v_mfma_f32_16x16x32_bf16 v[252:255], v[92:95], v[60:63], v[252:255]
s_waitcnt vmcnt(26)
s_waitcnt vmcnt(17) lgkmcnt(0)
s_barrier
v_cvt_pk_bf16_f32 v44, v36, v37
v_mfma_f32_16x16x32_bf16 v[128:131], v[96:99], v[48:51], v[128:131]
ds_read_b128 v[60:63], v25 offset:6144
v_accvgpr_read_b32 v38, a226
v_mfma_f32_16x16x32_bf16 v[132:135], v[100:103], v[48:51], v[132:135]
s_nop 0
v_add_u32 v25, 4294868992, v25
v_accvgpr_read_b32 v39, a227
v_or_b32 v0, 1, v11
v_mfma_f32_16x16x32_bf16 v[136:139], v[104:107], v[48:51], v[136:139]
s_add_u32 m0, s92, 131072
ds_read_b64_tr_b16 v[64:65], v26 offset:0
ds_read_b64_tr_b16 v[66:67], v26 offset:1024
v_mfma_f32_16x16x32_bf16 v[140:143], v[108:111], v[48:51], v[140:143]
buffer_load_dwordx4 v20, s[12:15], s24 offen offset:0 lds
v_mul_f32 v38, s47, v38
v_mfma_f32_16x16x32_bf16 v[144:147], v[112:115], v[48:51], v[144:147]
ds_read_b64_tr_b16 v[68:69], v27 offset:0
ds_read_b64_tr_b16 v[70:71], v27 offset:1024
v_mfma_f32_16x16x32_bf16 v[148:151], v[116:119], v[48:51], v[148:151]
buffer_store_dword v0, v6, s[60:63], 0 offen sc1
v_permlane32_swap_b32 v40, v42
v_mfma_f32_16x16x32_bf16 v[152:155], v[120:123], v[48:51], v[152:155]
ds_read_b64_tr_b16 v[72:73], v28 offset:0
ds_read_b64_tr_b16 v[74:75], v28 offset:1024
v_mfma_f32_16x16x32_bf16 v[156:159], v[124:127], v[48:51], v[156:159]
buffer_load_dwordx4 v21, s[12:15], s24 offen offset:1024 lds
v_mul_f32 v39, s47, v39
v_mfma_f32_16x16x32_bf16 v[160:163], v[96:99], v[52:55], v[160:163]
ds_read_b128 v[48:51], v24 offset:0
.Lpad8:
s_cbranch_execz .Ltch2
v_cvt_pk_bf16_f32 v45, v38, v39
v_mfma_f32_16x16x32_bf16 v[164:167], v[100:103], v[52:55], v[164:167]
v_permlane32_swap_b32 v41, v43
v_mfma_f32_16x16x32_bf16 v[168:171], v[104:107], v[52:55], v[168:171]
ds_read_b64_tr_b16 v[76:77], v29 offset:0
ds_read_b64_tr_b16 v[78:79], v29 offset:1024
v_mfma_f32_16x16x32_bf16 v[172:175], v[108:111], v[52:55], v[172:175]
s_xor_b32 s48, s95, 198656
buffer_load_dwordx4 v22, s[12:15], s24 offen offset:2048 lds
v_mfma_f32_16x16x32_bf16 v[176:179], v[112:115], v[52:55], v[176:179]
ds_read_b64_tr_b16 v[80:81], v30 offset:0
ds_read_b64_tr_b16 v[82:83], v30 offset:1024
v_mfma_f32_16x16x32_bf16 v[180:183], v[116:119], v[52:55], v[180:183]
buffer_store_dwordx4 v[40:43], v7, s[20:23], s48 offen sc1
v_accvgpr_read_b32 v36, a228
v_mfma_f32_16x16x32_bf16 v[184:187], v[120:123], v[52:55], v[184:187]
ds_read_b64_tr_b16 v[84:85], v31 offset:0
ds_read_b64_tr_b16 v[86:87], v31 offset:1024
v_mfma_f32_16x16x32_bf16 v[188:191], v[124:127], v[52:55], v[188:191]
buffer_load_dwordx4 v23, s[12:15], s24 offen offset:3072 lds
v_accvgpr_read_b32 v37, a229
v_mfma_f32_16x16x32_bf16 v[192:195], v[96:99], v[56:59], v[192:195]
ds_read_b128 v[52:55], v24 offset:2048
v_mul_f32 v36, s47, v36
v_mfma_f32_16x16x32_bf16 v[196:199], v[100:103], v[56:59], v[196:199]
v_mul_f32 v37, s47, v37
v_cvt_pk_bf16_f32 v46, v36, v37
v_mfma_f32_16x16x32_bf16 v[200:203], v[104:107], v[56:59], v[200:203]
s_add_u32 m0, s92, 135168
ds_read_b64_tr_b16 v[88:89], v32 offset:0
ds_read_b64_tr_b16 v[90:91], v32 offset:1024
v_mfma_f32_16x16x32_bf16 v[204:207], v[108:111], v[56:59], v[204:207]
buffer_load_dwordx4 v20, s[12:15], s25 offen offset:0 lds
v_accvgpr_read_b32 v38, a230
v_mfma_f32_16x16x32_bf16 v[208:211], v[112:115], v[56:59], v[208:211]
ds_read_b64_tr_b16 v[92:93], v33 offset:0
ds_read_b64_tr_b16 v[94:95], v33 offset:1024
v_mfma_f32_16x16x32_bf16 v[212:215], v[116:119], v[56:59], v[212:215]
v_accvgpr_read_b32 v39, a231
v_mul_f32 v38, s47, v38
v_mfma_f32_16x16x32_bf16 v[216:219], v[120:123], v[56:59], v[216:219]
s_waitcnt lgkmcnt(15)
v_mul_f32 v39, s47, v39
v_cvt_pk_bf16_f32 v47, v38, v39
v_mfma_f32_16x16x32_bf16 v[220:223], v[124:127], v[56:59], v[220:223]
v_permlane32_swap_b32 v44, v46
v_mfma_f32_16x16x32_bf16 v[224:227], v[96:99], v[60:63], v[224:227]
ds_read_b128 v[56:59], v24 offset:4096
v_mfma_f32_16x16x32_bf16 v[228:231], v[100:103], v[60:63], v[228:231]
buffer_load_dwordx4 v21, s[12:15], s25 offen offset:1024 lds
v_mfma_f32_16x16x32_bf16 v[232:235], v[104:107], v[60:63], v[232:235]
v_permlane32_swap_b32 v45, v47
v_mfma_f32_16x16x32_bf16 v[236:239], v[108:111], v[60:63], v[236:239]
v_accvgpr_write_b32 a224, v44
v_accvgpr_write_b32 a225, v45
v_mfma_f32_16x16x32_bf16 v[240:243], v[112:115], v[60:63], v[240:243]
v_accvgpr_write_b32 a226, v46
v_accvgpr_write_b32 a227, v47
v_mfma_f32_16x16x32_bf16 v[244:247], v[116:119], v[60:63], v[244:247]
v_accvgpr_read_b32 v36, a120
v_accvgpr_read_b32 v37, a121
v_mfma_f32_16x16x32_bf16 v[248:251], v[120:123], v[60:63], v[248:251]
v_mul_f32 v36, s47, v36
v_mul_f32 v37, s47, v37
v_mfma_f32_16x16x32_bf16 v[252:255], v[124:127], v[60:63], v[252:255]
s_waitcnt lgkmcnt(0)
v_cvt_pk_bf16_f32 v40, v36, v37
v_accvgpr_read_b32 v38, a122
v_mfma_f32_16x16x32_bf16 v[128:131], v[64:67], v[48:51], v[128:131]
ds_read_b128 v[60:63], v24 offset:6144
v_accvgpr_read_b32 v39, a123
v_mfma_f32_16x16x32_bf16 v[132:135], v[68:71], v[48:51], v[132:135]
v_add_u32 v24, 65536, v24
v_mul_f32 v38, s47, v38
v_mfma_f32_16x16x32_bf16 v[136:139], v[72:75], v[48:51], v[136:139]
ds_read_b64_tr_b16 v[96:97], v26 offset:8192
ds_read_b64_tr_b16 v[98:99], v26 offset:9216
v_mfma_f32_16x16x32_bf16 v[140:143], v[76:79], v[48:51], v[140:143]
buffer_load_dwordx4 v22, s[12:15], s25 offen offset:2048 lds
v_mul_f32 v39, s47, v39
v_mfma_f32_16x16x32_bf16 v[144:147], v[80:83], v[48:51], v[144:147]
ds_read_b64_tr_b16 v[100:101], v27 offset:8192
ds_read_b64_tr_b16 v[102:103], v27 offset:9216
v_mfma_f32_16x16x32_bf16 v[148:151], v[84:87], v[48:51], v[148:151]
v_cvt_pk_bf16_f32 v41, v38, v39
v_accvgpr_read_b32 v36, a124
v_mfma_f32_16x16x32_bf16 v[152:155], v[88:91], v[48:51], v[152:155]
ds_read_b64_tr_b16 v[104:105], v28 offset:8192
ds_read_b64_tr_b16 v[106:107], v28 offset:9216
v_mfma_f32_16x16x32_bf16 v[156:159], v[92:95], v[48:51], v[156:159]
buffer_load_dwordx4 v23, s[12:15], s25 offen offset:3072 lds
v_accvgpr_read_b32 v37, a125
v_mfma_f32_16x16x32_bf16 v[160:163], v[64:67], v[52:55], v[160:163]
ds_read_b128 v[48:51], v25 offset:0
v_mul_f32 v36, s47, v36
v_mfma_f32_16x16x32_bf16 v[164:167], v[68:71], v[52:55], v[164:167]
s_add_u32 s24, s24, 128
s_add_u32 s25, s25, 128
v_mul_f32 v37, s47, v37
v_mfma_f32_16x16x32_bf16 v[168:171], v[72:75], v[52:55], v[168:171]
s_add_u32 m0, s91, 16384
ds_read_b64_tr_b16 v[108:109], v29 offset:8192
ds_read_b64_tr_b16 v[110:111], v29 offset:9216
v_mfma_f32_16x16x32_bf16 v[172:175], v[76:79], v[52:55], v[172:175]
buffer_load_dwordx4 v16, s[16:19], s26 offen offset:0 lds
v_cvt_pk_bf16_f32 v42, v36, v37
v_mfma_f32_16x16x32_bf16 v[176:179], v[80:83], v[52:55], v[176:179]
ds_read_b64_tr_b16 v[112:113], v30 offset:8192
ds_read_b64_tr_b16 v[114:115], v30 offset:9216
v_mfma_f32_16x16x32_bf16 v[180:183], v[84:87], v[52:55], v[180:183]
v_accvgpr_read_b32 v38, a126
v_accvgpr_read_b32 v39, a127
v_mfma_f32_16x16x32_bf16 v[184:187], v[88:91], v[52:55], v[184:187]
ds_read_b64_tr_b16 v[116:117], v31 offset:8192
ds_read_b64_tr_b16 v[118:119], v31 offset:9216
v_mfma_f32_16x16x32_bf16 v[188:191], v[92:95], v[52:55], v[188:191]
buffer_load_dwordx4 v17, s[16:19], s26 offen offset:1024 lds
v_mul_f32 v38, s47, v38
v_mfma_f32_16x16x32_bf16 v[192:195], v[64:67], v[56:59], v[192:195]
ds_read_b128 v[52:55], v25 offset:2048
v_mul_f32 v39, s47, v39
v_mfma_f32_16x16x32_bf16 v[196:199], v[68:71], v[56:59], v[196:199]
v_cvt_pk_bf16_f32 v43, v38, v39
v_mfma_f32_16x16x32_bf16 v[200:203], v[72:75], v[56:59], v[200:203]
ds_read_b64_tr_b16 v[120:121], v32 offset:8192
ds_read_b64_tr_b16 v[122:123], v32 offset:9216
v_mfma_f32_16x16x32_bf16 v[204:207], v[76:79], v[56:59], v[204:207]
buffer_load_dwordx4 v18, s[16:19], s26 offen offset:2048 lds
v_accvgpr_read_b32 v36, a232
v_mfma_f32_16x16x32_bf16 v[208:211], v[80:83], v[56:59], v[208:211]
ds_read_b64_tr_b16 v[124:125], v33 offset:8192
ds_read_b64_tr_b16 v[126:127], v33 offset:9216
v_mfma_f32_16x16x32_bf16 v[212:215], v[84:87], v[56:59], v[212:215]
v_permlane32_swap_b32 v40, v42
v_mfma_f32_16x16x32_bf16 v[216:219], v[88:91], v[56:59], v[216:219]
s_waitcnt lgkmcnt(15)
v_permlane32_swap_b32 v41, v43
v_mfma_f32_16x16x32_bf16 v[220:223], v[92:95], v[56:59], v[220:223]
s_xor_b32 s48, s95, 199680
buffer_store_dwordx4 v[40:43], v7, s[20:23], s48 offen sc1
v_mfma_f32_16x16x32_bf16 v[224:227], v[64:67], v[60:63], v[224:227]
ds_read_b128 v[56:59], v25 offset:4096
v_accvgpr_read_b32 v37, a233
v_mfma_f32_16x16x32_bf16 v[228:231], v[68:71], v[60:63], v[228:231]
buffer_load_dwordx4 v19, s[16:19], s26 offen offset:3072 lds
v_mul_f32 v36, s47, v36
v_mfma_f32_16x16x32_bf16 v[232:235], v[72:75], v[60:63], v[232:235]
v_mul_f32 v37, s47, v37
v_cvt_pk_bf16_f32 v44, v36, v37
v_mfma_f32_16x16x32_bf16 v[236:239], v[76:79], v[60:63], v[236:239]
s_add_u32 s26, s26, 524288
v_accvgpr_read_b32 v38, a234
v_accvgpr_read_b32 v39, a235
v_mfma_f32_16x16x32_bf16 v[240:243], v[80:83], v[60:63], v[240:243]
v_mul_f32 v38, s47, v38
v_mul_f32 v39, s47, v39
v_mfma_f32_16x16x32_bf16 v[244:247], v[84:87], v[60:63], v[244:247]
buffer_load_dword v8, v3, s[60:63], s90 offen sc1
v_cvt_pk_bf16_f32 v45, v38, v39
v_mfma_f32_16x16x32_bf16 v[248:251], v[88:91], v[60:63], v[248:251]
v_accvgpr_read_b32 v36, a236
v_accvgpr_read_b32 v37, a237
v_mfma_f32_16x16x32_bf16 v[252:255], v[92:95], v[60:63], v[252:255]
s_waitcnt vmcnt(20) lgkmcnt(0)
s_barrier
v_mul_f32 v36, s47, v36
v_mfma_f32_16x16x32_bf16 v[128:131], v[96:99], v[48:51], v[128:131]
ds_read_b128 v[60:63], v25 offset:6144
v_mul_f32 v37, s47, v37
v_mfma_f32_16x16x32_bf16 v[132:135], v[100:103], v[48:51], v[132:135]
v_add_u32 v25, 65536, v25
v_cvt_pk_bf16_f32 v46, v36, v37
v_mfma_f32_16x16x32_bf16 v[136:139], v[104:107], v[48:51], v[136:139]
s_add_u32 m0, s92, 32768
ds_read_b64_tr_b16 v[64:65], v26 offset:16384
ds_read_b64_tr_b16 v[66:67], v26 offset:17408
v_mfma_f32_16x16x32_bf16 v[140:143], v[108:111], v[48:51], v[140:143]
buffer_load_dwordx4 v20, s[12:15], s24 offen offset:0 lds
v_accvgpr_read_b32 v38, a238
v_mfma_f32_16x16x32_bf16 v[144:147], v[112:115], v[48:51], v[144:147]
ds_read_b64_tr_b16 v[68:69], v27 offset:16384
ds_read_b64_tr_b16 v[70:71], v27 offset:17408
v_mfma_f32_16x16x32_bf16 v[148:151], v[116:119], v[48:51], v[148:151]
v_accvgpr_read_b32 v39, a239
v_mul_f32 v38, s47, v38
v_mfma_f32_16x16x32_bf16 v[152:155], v[120:123], v[48:51], v[152:155]
ds_read_b64_tr_b16 v[72:73], v28 offset:16384
ds_read_b64_tr_b16 v[74:75], v28 offset:17408
v_mfma_f32_16x16x32_bf16 v[156:159], v[124:127], v[48:51], v[156:159]
buffer_load_dwordx4 v21, s[12:15], s24 offen offset:1024 lds
v_mul_f32 v39, s47, v39
v_mfma_f32_16x16x32_bf16 v[160:163], v[96:99], v[52:55], v[160:163]
ds_read_b128 v[48:51], v24 offset:0
v_cvt_pk_bf16_f32 v47, v38, v39
v_mfma_f32_16x16x32_bf16 v[164:167], v[100:103], v[52:55], v[164:167]
v_permlane32_swap_b32 v44, v46
v_mfma_f32_16x16x32_bf16 v[168:171], v[104:107], v[52:55], v[168:171]
ds_read_b64_tr_b16 v[76:77], v29 offset:16384
ds_read_b64_tr_b16 v[78:79], v29 offset:17408
v_mfma_f32_16x16x32_bf16 v[172:175], v[108:111], v[52:55], v[172:175]
buffer_load_dwordx4 v22, s[12:15], s24 offen offset:2048 lds
v_mfma_f32_16x16x32_bf16 v[176:179], v[112:115], v[52:55], v[176:179]
ds_read_b64_tr_b16 v[80:81], v30 offset:16384
ds_read_b64_tr_b16 v[82:83], v30 offset:17408
v_mfma_f32_16x16x32_bf16 v[180:183], v[116:119], v[52:55], v[180:183]
v_permlane32_swap_b32 v45, v47
v_mfma_f32_16x16x32_bf16 v[184:187], v[120:123], v[52:55], v[184:187]
ds_read_b64_tr_b16 v[84:85], v31 offset:16384
ds_read_b64_tr_b16 v[86:87], v31 offset:17408
v_mfma_f32_16x16x32_bf16 v[188:191], v[124:127], v[52:55], v[188:191]
buffer_load_dwordx4 v23, s[12:15], s24 offen offset:3072 lds
v_accvgpr_write_b32 a228, v44
v_mfma_f32_16x16x32_bf16 v[192:195], v[96:99], v[56:59], v[192:195]
ds_read_b128 v[52:55], v24 offset:2048
v_accvgpr_write_b32 a229, v45
v_mfma_f32_16x16x32_bf16 v[196:199], v[100:103], v[56:59], v[196:199]
v_accvgpr_write_b32 a230, v46
v_accvgpr_write_b32 a231, v47
v_mfma_f32_16x16x32_bf16 v[200:203], v[104:107], v[56:59], v[200:203]
s_add_u32 m0, s92, 36864
ds_read_b64_tr_b16 v[88:89], v32 offset:16384
ds_read_b64_tr_b16 v[90:91], v32 offset:17408
v_mfma_f32_16x16x32_bf16 v[204:207], v[108:111], v[56:59], v[204:207]
buffer_load_dwordx4 v20, s[12:15], s25 offen offset:0 lds
v_accvgpr_read_b32 v36, a240
v_mfma_f32_16x16x32_bf16 v[208:211], v[112:115], v[56:59], v[208:211]
ds_read_b64_tr_b16 v[92:93], v33 offset:16384
ds_read_b64_tr_b16 v[94:95], v33 offset:17408
v_mfma_f32_16x16x32_bf16 v[212:215], v[116:119], v[56:59], v[212:215]
v_accvgpr_read_b32 v37, a241
v_mul_f32 v36, s47, v36
v_mfma_f32_16x16x32_bf16 v[216:219], v[120:123], v[56:59], v[216:219]
s_waitcnt lgkmcnt(15)
v_mul_f32 v37, s47, v37
v_cvt_pk_bf16_f32 v40, v36, v37
v_mfma_f32_16x16x32_bf16 v[220:223], v[124:127], v[56:59], v[220:223]
v_accvgpr_read_b32 v38, a242
v_accvgpr_read_b32 v39, a243
v_mfma_f32_16x16x32_bf16 v[224:227], v[96:99], v[60:63], v[224:227]
ds_read_b128 v[56:59], v24 offset:4096
v_mul_f32 v38, s47, v38
v_mfma_f32_16x16x32_bf16 v[228:231], v[100:103], v[60:63], v[228:231]
buffer_load_dwordx4 v21, s[12:15], s25 offen offset:1024 lds
v_mul_f32 v39, s47, v39
v_mfma_f32_16x16x32_bf16 v[232:235], v[104:107], v[60:63], v[232:235]
v_cvt_pk_bf16_f32 v41, v38, v39
v_accvgpr_read_b32 v36, a244
v_mfma_f32_16x16x32_bf16 v[236:239], v[108:111], v[60:63], v[236:239]
v_accvgpr_read_b32 v37, a245
v_mul_f32 v36, s47, v36
v_mfma_f32_16x16x32_bf16 v[240:243], v[112:115], v[60:63], v[240:243]
v_mul_f32 v37, s47, v37
v_cvt_pk_bf16_f32 v42, v36, v37
v_mfma_f32_16x16x32_bf16 v[244:247], v[116:119], v[60:63], v[244:247]
v_accvgpr_read_b32 v38, a246
v_accvgpr_read_b32 v39, a247
v_mfma_f32_16x16x32_bf16 v[248:251], v[120:123], v[60:63], v[248:251]
v_mul_f32 v38, s47, v38
v_mul_f32 v39, s47, v39
v_mfma_f32_16x16x32_bf16 v[252:255], v[124:127], v[60:63], v[252:255]
s_waitcnt lgkmcnt(0)
v_cvt_pk_bf16_f32 v43, v38, v39
v_mfma_f32_16x16x32_bf16 v[128:131], v[64:67], v[48:51], v[128:131]
ds_read_b128 v[60:63], v24 offset:6144
v_mfma_f32_16x16x32_bf16 v[132:135], v[68:71], v[48:51], v[132:135]
v_add_u32 v24, 32768, v24
v_mfma_f32_16x16x32_bf16 v[136:139], v[72:75], v[48:51], v[136:139]
ds_read_b64_tr_b16 v[96:97], v26 offset:24576
ds_read_b64_tr_b16 v[98:99], v26 offset:25600
v_mfma_f32_16x16x32_bf16 v[140:143], v[76:79], v[48:51], v[140:143]
buffer_load_dwordx4 v22, s[12:15], s25 offen offset:2048 lds
v_mfma_f32_16x16x32_bf16 v[144:147], v[80:83], v[48:51], v[144:147]
ds_read_b64_tr_b16 v[100:101], v27 offset:24576
ds_read_b64_tr_b16 v[102:103], v27 offset:25600
v_mfma_f32_16x16x32_bf16 v[148:151], v[84:87], v[48:51], v[148:151]
v_permlane32_swap_b32 v40, v42
v_mfma_f32_16x16x32_bf16 v[152:155], v[88:91], v[48:51], v[152:155]
ds_read_b64_tr_b16 v[104:105], v28 offset:24576
ds_read_b64_tr_b16 v[106:107], v28 offset:25600
v_mfma_f32_16x16x32_bf16 v[156:159], v[92:95], v[48:51], v[156:159]
buffer_load_dwordx4 v23, s[12:15], s25 offen offset:3072 lds
v_mfma_f32_16x16x32_bf16 v[160:163], v[64:67], v[52:55], v[160:163]
ds_read_b128 v[48:51], v25 offset:0
v_mfma_f32_16x16x32_bf16 v[164:167], v[68:71], v[52:55], v[164:167]
s_add_u32 s24, s24, 128
s_add_u32 s25, s25, 128
v_mfma_f32_16x16x32_bf16 v[168:171], v[72:75], v[52:55], v[168:171]
ds_read_b64_tr_b16 v[108:109], v29 offset:24576
ds_read_b64_tr_b16 v[110:111], v29 offset:25600
v_mfma_f32_16x16x32_bf16 v[172:175], v[76:79], v[52:55], v[172:175]
v_permlane32_swap_b32 v41, v43
v_mfma_f32_16x16x32_bf16 v[176:179], v[80:83], v[52:55], v[176:179]
ds_read_b64_tr_b16 v[112:113], v30 offset:24576
ds_read_b64_tr_b16 v[114:115], v30 offset:25600
v_mfma_f32_16x16x32_bf16 v[180:183], v[84:87], v[52:55], v[180:183]
v_accvgpr_write_b32 a232, v40
v_accvgpr_write_b32 a233, v41
v_mfma_f32_16x16x32_bf16 v[184:187], v[88:91], v[52:55], v[184:187]
ds_read_b64_tr_b16 v[116:117], v31 offset:24576
ds_read_b64_tr_b16 v[118:119], v31 offset:25600
v_mfma_f32_16x16x32_bf16 v[188:191], v[92:95], v[52:55], v[188:191]
v_accvgpr_write_b32 a234, v42
v_accvgpr_write_b32 a235, v43
v_mfma_f32_16x16x32_bf16 v[192:195], v[64:67], v[56:59], v[192:195]
ds_read_b128 v[52:55], v25 offset:2048
v_accvgpr_read_b32 v36, a248
v_mfma_f32_16x16x32_bf16 v[196:199], v[68:71], v[56:59], v[196:199]
v_accvgpr_read_b32 v37, a249
v_mul_f32 v36, s47, v36
v_mfma_f32_16x16x32_bf16 v[200:203], v[72:75], v[56:59], v[200:203]
ds_read_b64_tr_b16 v[120:121], v32 offset:24576
ds_read_b64_tr_b16 v[122:123], v32 offset:25600
v_mfma_f32_16x16x32_bf16 v[204:207], v[76:79], v[56:59], v[204:207]
v_mul_f32 v37, s47, v37
v_cvt_pk_bf16_f32 v44, v36, v37
v_mfma_f32_16x16x32_bf16 v[208:211], v[80:83], v[56:59], v[208:211]
ds_read_b64_tr_b16 v[124:125], v33 offset:24576
ds_read_b64_tr_b16 v[126:127], v33 offset:25600
v_mfma_f32_16x16x32_bf16 v[212:215], v[84:87], v[56:59], v[212:215]
s_waitcnt vmcnt(8)
v_and_b32 v0, 7, v8
v_cmp_lt_u32 s[28:29], v0, v14
v_mfma_f32_16x16x32_bf16 v[216:219], v[88:91], v[56:59], v[216:219]
s_waitcnt lgkmcnt(15)
v_cmp_eq_u32 s[30:31], v8, v2
v_accvgpr_read_b32 v38, a250
v_mfma_f32_16x16x32_bf16 v[220:223], v[92:95], v[56:59], v[220:223]
v_xor_b32 v26, 0x10000, v26
v_accvgpr_read_b32 v39, a251
v_mfma_f32_16x16x32_bf16 v[224:227], v[64:67], v[60:63], v[224:227]
ds_read_b128 v[56:59], v25 offset:4096
v_xor_b32 v27, 0x10000, v27
v_mfma_f32_16x16x32_bf16 v[228:231], v[68:71], v[60:63], v[228:231]
v_xor_b32 v28, 0x10000, v28
v_mul_f32 v38, s47, v38
v_mfma_f32_16x16x32_bf16 v[232:235], v[72:75], v[60:63], v[232:235]
ds_read_b128 v[20:23], v25 offset:6144
v_xor_b32 v29, 0x10000, v29
v_mfma_f32_16x16x32_bf16 v[236:239], v[76:79], v[60:63], v[236:239]
v_xor_b32 v30, 0x10000, v30
v_mul_f32 v39, s47, v39
v_mfma_f32_16x16x32_bf16 v[240:243], v[80:83], v[60:63], v[240:243]
v_xor_b32 v31, 0x10000, v31
v_cvt_pk_bf16_f32 v45, v38, v39
v_mfma_f32_16x16x32_bf16 v[244:247], v[84:87], v[60:63], v[244:247]
v_xor_b32 v32, 0x10000, v32
v_accvgpr_read_b32 v36, a252
v_mfma_f32_16x16x32_bf16 v[248:251], v[88:91], v[60:63], v[248:251]
s_or_b64 s[28:29], s[28:29], s[30:31]
s_cbranch_scc1 .Lspin1
.Lspin1_ret:
s_add_u32 s28, s39, 0
s_and_b32 s28, s28, 0xffff
v_xor_b32 v33, 0x10000, v33
v_mfma_f32_16x16x32_bf16 v[252:255], v[92:95], v[60:63], v[252:255]
s_waitcnt vmcnt(14) lgkmcnt(0)
s_barrier
s_mov_b32 s40, 0
v_mfma_f32_16x16x32_bf16 v[128:131], v[96:99], v[48:51], v[128:131]
s_add_u32 m0, s28, 65536
v_accvgpr_read_b32 v37, a253
v_mul_f32 v36, s47, v36
v_mfma_f32_16x16x32_bf16 v[132:135], v[100:103], v[48:51], v[132:135]
buffer_load_dwordx4 v7, s[4:7], s40 offen offset:0 sc1 lds
v_add_u32 v25, 32768, v25
v_mfma_f32_16x16x32_bf16 v[136:139], v[104:107], v[48:51], v[136:139]
ds_read_b64_tr_b16 v[64:65], v26 offset:0
ds_read_b64_tr_b16 v[66:67], v26 offset:1024
v_mfma_f32_16x16x32_bf16 v[140:143], v[108:111], v[48:51], v[140:143]
v_mul_f32 v37, s47, v37
v_cvt_pk_bf16_f32 v46, v36, v37
v_mfma_f32_16x16x32_bf16 v[144:147], v[112:115], v[48:51], v[144:147]
ds_read_b64_tr_b16 v[68:69], v27 offset:0
ds_read_b64_tr_b16 v[70:71], v27 offset:1024
v_mfma_f32_16x16x32_bf16 v[148:151], v[116:119], v[48:51], v[148:151]
buffer_load_dwordx4 v7, s[4:7], s40 offen offset:1024 sc1 lds
v_accvgpr_read_b32 v38, a254
v_mfma_f32_16x16x32_bf16 v[152:155], v[120:123], v[48:51], v[152:155]
ds_read_b64_tr_b16 v[72:73], v28 offset:0
ds_read_b64_tr_b16 v[74:75], v28 offset:1024
v_mfma_f32_16x16x32_bf16 v[156:159], v[124:127], v[48:51], v[156:159]
v_accvgpr_read_b32 v39, a255
v_mul_f32 v38, s47, v38
v_mfma_f32_16x16x32_bf16 v[160:163], v[96:99], v[52:55], v[160:163]
ds_read_b128 v[48:51], v24 offset:0
v_mul_f32 v39, s47, v39
v_mfma_f32_16x16x32_bf16 v[164:167], v[100:103], v[52:55], v[164:167]
buffer_load_dwordx4 v7, s[4:7], s40 offen offset:2048 sc1 lds
v_cvt_pk_bf16_f32 v47, v38, v39
v_mfma_f32_16x16x32_bf16 v[168:171], v[104:107], v[52:55], v[168:171]
ds_read_b64_tr_b16 v[76:77], v29 offset:0
ds_read_b64_tr_b16 v[78:79], v29 offset:1024
v_mfma_f32_16x16x32_bf16 v[172:175], v[108:111], v[52:55], v[172:175]
v_permlane32_swap_b32 v44, v46
v_mfma_f32_16x16x32_bf16 v[176:179], v[112:115], v[52:55], v[176:179]
ds_read_b64_tr_b16 v[80:81], v30 offset:0
ds_read_b64_tr_b16 v[82:83], v30 offset:1024
v_mfma_f32_16x16x32_bf16 v[180:183], v[116:119], v[52:55], v[180:183]
buffer_load_dwordx4 v7, s[4:7], s40 offen offset:3072 sc1 lds
v_mfma_f32_16x16x32_bf16 v[184:187], v[120:123], v[52:55], v[184:187]
ds_read_b64_tr_b16 v[84:85], v31 offset:0
ds_read_b64_tr_b16 v[86:87], v31 offset:1024
v_mfma_f32_16x16x32_bf16 v[188:191], v[124:127], v[52:55], v[188:191]
s_add_u32 s28, s39, 16384
s_and_b32 s28, s28, 0xffff
v_mfma_f32_16x16x32_bf16 v[192:195], v[96:99], v[56:59], v[192:195]
s_mov_b32 s41, 16384
ds_read_b128 v[52:55], v24 offset:2048
v_mfma_f32_16x16x32_bf16 v[196:199], v[100:103], v[56:59], v[196:199]
s_add_u32 m0, s28, 65536
v_permlane32_swap_b32 v45, v47
v_mfma_f32_16x16x32_bf16 v[200:203], v[104:107], v[56:59], v[200:203]
buffer_load_dwordx4 v7, s[4:7], s41 offen offset:0 sc1 lds
ds_read_b64_tr_b16 v[88:89], v32 offset:0
ds_read_b64_tr_b16 v[90:91], v32 offset:1024
v_mfma_f32_16x16x32_bf16 v[204:207], v[108:111], v[56:59], v[204:207]
v_accvgpr_write_b32 a236, v44
v_accvgpr_write_b32 a237, v45
v_mfma_f32_16x16x32_bf16 v[208:211], v[112:115], v[56:59], v[208:211]
ds_read_b64_tr_b16 v[92:93], v33 offset:0
ds_read_b64_tr_b16 v[94:95], v33 offset:1024
v_mfma_f32_16x16x32_bf16 v[212:215], v[116:119], v[56:59], v[212:215]
v_accvgpr_write_b32 a238, v46
v_accvgpr_write_b32 a239, v47
v_mfma_f32_16x16x32_bf16 v[216:219], v[120:123], v[56:59], v[216:219]
buffer_load_dwordx4 v7, s[4:7], s41 offen offset:1024 sc1 lds
v_mfma_f32_16x16x32_bf16 v[220:223], v[124:127], v[56:59], v[220:223]
v_mfma_f32_16x16x32_bf16 v[224:227], v[96:99], v[20:23], v[224:227]
ds_read_b128 v[56:59], v24 offset:4096
v_mfma_f32_16x16x32_bf16 v[228:231], v[100:103], v[20:23], v[228:231]
v_mfma_f32_16x16x32_bf16 v[232:235], v[104:107], v[20:23], v[232:235]
buffer_load_dwordx4 v7, s[4:7], s41 offen offset:2048 sc1 lds
v_mfma_f32_16x16x32_bf16 v[236:239], v[108:111], v[20:23], v[236:239]
v_mfma_f32_16x16x32_bf16 v[240:243], v[112:115], v[20:23], v[240:243]
v_mfma_f32_16x16x32_bf16 v[244:247], v[116:119], v[20:23], v[244:247]
v_mfma_f32_16x16x32_bf16 v[248:251], v[120:123], v[20:23], v[248:251]
buffer_load_dwordx4 v7, s[4:7], s41 offen offset:3072 sc1 lds
v_mfma_f32_16x16x32_bf16 v[252:255], v[124:127], v[20:23], v[252:255]
s_waitcnt lgkmcnt(0)
v_mfma_f32_16x16x32_bf16 v[128:131], v[64:67], v[48:51], v[128:131]
s_add_u32 s28, s39, 32768
s_and_b32 s28, s28, 0xffff
ds_read_b128 v[60:63], v24 offset:6144
v_mfma_f32_16x16x32_bf16 v[132:135], v[68:71], v[48:51], v[132:135]
s_mov_b32 s40, 32768
v_add_u32 v24, 4294868992, v24
v_mfma_f32_16x16x32_bf16 v[136:139], v[72:75], v[48:51], v[136:139]
s_add_u32 m0, s28, 65536
ds_read_b64_tr_b16 v[96:97], v26 offset:8192
ds_read_b64_tr_b16 v[98:99], v26 offset:9216
v_mfma_f32_16x16x32_bf16 v[140:143], v[76:79], v[48:51], v[140:143]
buffer_load_dwordx4 v7, s[4:7], s40 offen offset:0 sc1 lds
v_mfma_f32_16x16x32_bf16 v[144:147], v[80:83], v[48:51], v[144:147]
ds_read_b64_tr_b16 v[100:101], v27 offset:8192
ds_read_b64_tr_b16 v[102:103], v27 offset:9216
v_mfma_f32_16x16x32_bf16 v[148:151], v[84:87], v[48:51], v[148:151]
s_sub_u32 s32, s66, 4194304
s_subb_u32 s33, s67, 0
v_mfma_f32_16x16x32_bf16 v[152:155], v[88:91], v[48:51], v[152:155]
ds_read_b64_tr_b16 v[104:105], v28 offset:8192
ds_read_b64_tr_b16 v[106:107], v28 offset:9216
v_mfma_f32_16x16x32_bf16 v[156:159], v[92:95], v[48:51], v[156:159]
buffer_load_dwordx4 v7, s[4:7], s40 offen offset:1024 sc1 lds
v_mfma_f32_16x16x32_bf16 v[160:163], v[64:67], v[52:55], v[160:163]
ds_read_b128 v[48:51], v25 offset:0
v_mfma_f32_16x16x32_bf16 v[164:167], v[68:71], v[52:55], v[164:167]
s_sub_u32 s34, s80, 10485760
s_subb_u32 s35, s81, 0
v_mfma_f32_16x16x32_bf16 v[168:171], v[72:75], v[52:55], v[168:171]
ds_read_b64_tr_b16 v[108:109], v29 offset:8192
ds_read_b64_tr_b16 v[110:111], v29 offset:9216
v_mfma_f32_16x16x32_bf16 v[172:175], v[76:79], v[52:55], v[172:175]
buffer_load_dwordx4 v7, s[4:7], s40 offen offset:2048 sc1 lds
v_mfma_f32_16x16x32_bf16 v[176:179], v[80:83], v[52:55], v[176:179]
ds_read_b64_tr_b16 v[112:113], v30 offset:8192
ds_read_b64_tr_b16 v[114:115], v30 offset:9216
v_mfma_f32_16x16x32_bf16 v[180:183], v[84:87], v[52:55], v[180:183]
s_sub_u32 s36, s68, 18874368
s_subb_u32 s37, s69, 0
s_max_u32 s38, s89, 4
v_mfma_f32_16x16x32_bf16 v[184:187], v[88:91], v[52:55], v[184:187]
ds_read_b64_tr_b16 v[116:117], v31 offset:8192
ds_read_b64_tr_b16 v[118:119], v31 offset:9216
v_mfma_f32_16x16x32_bf16 v[188:191], v[92:95], v[52:55], v[188:191]
s_sub_u32 s38, s38, 4
buffer_load_dwordx4 v7, s[4:7], s40 offen offset:3072 sc1 lds
v_mfma_f32_16x16x32_bf16 v[192:195], v[64:67], v[56:59], v[192:195]
s_lshl_b32 s38, s38, 3
ds_read_b128 v[52:55], v25 offset:2048
v_mfma_f32_16x16x32_bf16 v[196:199], v[68:71], v[56:59], v[196:199]
s_add_u32 s28, s39, 49152
s_and_b32 s28, s28, 0xffff
s_lshr_b32 s29, 319291392, s38
v_mfma_f32_16x16x32_bf16 v[200:203], v[72:75], v[56:59], v[200:203]
s_mov_b32 s41, 49152
ds_read_b64_tr_b16 v[120:121], v32 offset:8192
ds_read_b64_tr_b16 v[122:123], v32 offset:9216
v_mfma_f32_16x16x32_bf16 v[204:207], v[76:79], v[56:59], v[204:207]
s_add_u32 m0, s28, 65536
s_and_b32 s29, s29, 0xff
s_lshl_b32 s29, s29, 7
v_mfma_f32_16x16x32_bf16 v[208:211], v[80:83], v[56:59], v[208:211]
buffer_load_dwordx4 v7, s[4:7], s41 offen offset:0 sc1 lds
ds_read_b64_tr_b16 v[124:125], v33 offset:8192
ds_read_b64_tr_b16 v[126:127], v33 offset:9216
v_mfma_f32_16x16x32_bf16 v[212:215], v[84:87], v[56:59], v[212:215]
s_lshr_b32 s52, 218826752, s38
s_and_b32 s52, s52, 0xff
v_mfma_f32_16x16x32_bf16 v[216:219], v[88:91], v[56:59], v[216:219]
s_waitcnt lgkmcnt(15)
s_cmp_lt_u32 s89, 4
s_cselect_b32 s52, 0, s52
v_mfma_f32_16x16x32_bf16 v[220:223], v[92:95], v[56:59], v[220:223]
s_sub_u32 s52, s52, 1
s_cselect_b32 s52, 0, s52
s_cselect_b64 s[30:31], -1, 0
v_mfma_f32_16x16x32_bf16 v[224:227], v[64:67], v[60:63], v[224:227]
buffer_load_dwordx4 v7, s[4:7], s41 offen offset:1024 sc1 lds
ds_read_b128 v[56:59], v25 offset:4096
v_mfma_f32_16x16x32_bf16 v[228:231], v[68:71], v[60:63], v[228:231]
s_and_b32 s38, s2, 31
s_lshl_b32 s38, s38, 2
s_add_u32 s38, s38, s3
v_mfma_f32_16x16x32_bf16 v[232:235], v[72:75], v[60:63], v[232:235]
s_add_u32 s38, s38, s29
s_lshl_b32 s38, s38, 8
v_mfma_f32_16x16x32_bf16 v[236:239], v[76:79], v[60:63], v[236:239]
v_lshrrev_b32 v9, 1, v1
v_min_u32 v9, 19, v9
v_mfma_f32_16x16x32_bf16 v[240:243], v[80:83], v[60:63], v[240:243]
buffer_load_dwordx4 v7, s[4:7], s41 offen offset:2048 sc1 lds
v_mov_b32 v2, s64
v_mfma_f32_16x16x32_bf16 v[244:247], v[84:87], v[60:63], v[244:247]
v_mov_b32 v3, s65
v_cmp_le_u32 vcc, 4, v9
v_mfma_f32_16x16x32_bf16 v[248:251], v[88:91], v[60:63], v[248:251]
v_mov_b32 v5, s32
v_mov_b32 v10, s33
v_mfma_f32_16x16x32_bf16 v[252:255], v[92:95], v[60:63], v[252:255]
s_waitcnt vmcnt(15) lgkmcnt(0)
s_barrier
v_cndmask_b32 v2, v2, v5, vcc
v_mfma_f32_16x16x32_bf16 v[128:131], v[96:99], v[48:51], v[128:131]
buffer_load_dwordx4 v7, s[4:7], s41 offen offset:3072 sc1 lds
ds_read_b128 v[60:63], v25 offset:6144
v_mfma_f32_16x16x32_bf16 v[132:135], v[100:103], v[48:51], v[132:135]
v_add_u32 v25, 4294868992, v25
v_or_b32 v0, 2, v11
v_mfma_f32_16x16x32_bf16 v[136:139], v[104:107], v[48:51], v[136:139]
ds_read_b64_tr_b16 v[64:65], v26 offset:16384
ds_read_b64_tr_b16 v[66:67], v26 offset:17408
v_mfma_f32_16x16x32_bf16 v[140:143], v[108:111], v[48:51], v[140:143]
buffer_store_dword v0, v6, s[60:63], 0 offen sc1
v_cndmask_b32 v3, v3, v10, vcc
v_mfma_f32_16x16x32_bf16 v[144:147], v[112:115], v[48:51], v[144:147]
ds_read_b64_tr_b16 v[68:69], v27 offset:16384
ds_read_b64_tr_b16 v[70:71], v27 offset:17408
.Lpad9:
s_cbranch_execz .Ltch2
v_mfma_f32_16x16x32_bf16 v[148:151], v[116:119], v[48:51], v[148:151]
v_cmp_le_u32 vcc, 10, v9
v_mov_b32 v5, s34
v_mfma_f32_16x16x32_bf16 v[152:155], v[120:123], v[48:51], v[152:155]
ds_read_b64_tr_b16 v[72:73], v28 offset:16384
ds_read_b64_tr_b16 v[74:75], v28 offset:17408
v_mfma_f32_16x16x32_bf16 v[156:159], v[124:127], v[48:51], v[156:159]
v_mov_b32 v10, s35
v_cndmask_b32 v2, v2, v5, vcc
v_mfma_f32_16x16x32_bf16 v[160:163], v[96:99], v[52:55], v[160:163]
s_xor_b32 s48, s95, 262144
ds_read_b128 v[48:51], v24 offset:0
v_mfma_f32_16x16x32_bf16 v[164:167], v[100:103], v[52:55], v[164:167]
buffer_store_dwordx4 a[128:131], v7, s[20:23], s48 offen sc1
v_cndmask_b32 v3, v3, v10, vcc
v_mfma_f32_16x16x32_bf16 v[168:171], v[104:107], v[52:55], v[168:171]
ds_read_b64_tr_b16 v[76:77], v29 offset:16384
ds_read_b64_tr_b16 v[78:79], v29 offset:17408
v_mfma_f32_16x16x32_bf16 v[172:175], v[108:111], v[52:55], v[172:175]
v_cmp_le_u32 vcc, 18, v9
v_mov_b32 v5, s36
v_mfma_f32_16x16x32_bf16 v[176:179], v[112:115], v[52:55], v[176:179]
ds_read_b64_tr_b16 v[80:81], v30 offset:16384
ds_read_b64_tr_b16 v[82:83], v30 offset:17408
v_mfma_f32_16x16x32_bf16 v[180:183], v[116:119], v[52:55], v[180:183]
v_mov_b32 v10, s37
v_cndmask_b32 v2, v2, v5, vcc
v_mfma_f32_16x16x32_bf16 v[184:187], v[120:123], v[52:55], v[184:187]
ds_read_b64_tr_b16 v[84:85], v31 offset:16384
ds_read_b64_tr_b16 v[86:87], v31 offset:17408
v_mfma_f32_16x16x32_bf16 v[188:191], v[124:127], v[52:55], v[188:191]
v_cndmask_b32 v3, v3, v10, vcc
v_lshlrev_b32 v9, 20, v9
v_mfma_f32_16x16x32_bf16 v[192:195], v[96:99], v[56:59], v[192:195]
ds_read_b128 v[52:55], v24 offset:2048
v_add_u32 v9, s38, v9
v_mfma_f32_16x16x32_bf16 v[196:199], v[100:103], v[56:59], v[196:199]
v_and_b32 v5, 1, v1
v_lshl_add_u32 v9, v5, 7, v9
v_mfma_f32_16x16x32_bf16 v[200:203], v[104:107], v[56:59], v[200:203]
ds_read_b64_tr_b16 v[88:89], v32 offset:16384
ds_read_b64_tr_b16 v[90:91], v32 offset:17408
v_mfma_f32_16x16x32_bf16 v[204:207], v[108:111], v[56:59], v[204:207]
v_add_co_u32 v2, vcc, v2, v9
v_nop
v_mfma_f32_16x16x32_bf16 v[208:211], v[112:115], v[56:59], v[208:211]
ds_read_b64_tr_b16 v[92:93], v33 offset:16384
ds_read_b64_tr_b16 v[94:95], v33 offset:17408
v_mfma_f32_16x16x32_bf16 v[212:215], v[116:119], v[56:59], v[212:215]
v_nop
v_addc_co_u32 v3, vcc, 0, v3, vcc
v_mfma_f32_16x16x32_bf16 v[216:219], v[120:123], v[56:59], v[216:219]
s_waitcnt lgkmcnt(15)
v_mov_b32 v5, s4
v_mov_b32 v10, s5
v_mfma_f32_16x16x32_bf16 v[220:223], v[124:127], v[56:59], v[220:223]
v_cndmask_b32 v2, v2, v5, s[30:31]
v_cndmask_b32 v3, v3, v10, s[30:31]
v_mfma_f32_16x16x32_bf16 v[224:227], v[96:99], v[60:63], v[224:227]
s_lshl_b32 s93, s2, 4
ds_read_b128 v[56:59], v24 offset:4096
v_mfma_f32_16x16x32_bf16 v[228:231], v[100:103], v[60:63], v[228:231]
s_xor_b32 s48, s95, 263168
s_add_u32 s93, s93, s3
s_lshl_b32 s93, s93, 2
v_mfma_f32_16x16x32_bf16 v[232:235], v[104:107], v[60:63], v[232:235]
s_add_u32 s93, s93, 0x10000
buffer_store_dwordx4 a[132:135], v7, s[20:23], s48 offen sc1
v_mfma_f32_16x16x32_bf16 v[236:239], v[108:111], v[60:63], v[236:239]
v_lshlrev_b32 v9, 20, v1
v_add_u32 v5, s93, v9
v_mfma_f32_16x16x32_bf16 v[240:243], v[112:115], v[60:63], v[240:243]
v_mfma_f32_16x16x32_bf16 v[244:247], v[116:119], v[60:63], v[244:247]
v_mfma_f32_16x16x32_bf16 v[248:251], v[120:123], v[60:63], v[248:251]
v_mfma_f32_16x16x32_bf16 v[252:255], v[124:127], v[60:63], v[252:255]
s_waitcnt lgkmcnt(0)
v_mfma_f32_16x16x32_bf16 v[128:131], v[64:67], v[48:51], v[128:131]
ds_read_b128 v[60:63], v24 offset:6144
v_mfma_f32_16x16x32_bf16 v[132:135], v[68:71], v[48:51], v[132:135]
v_mfma_f32_16x16x32_bf16 v[136:139], v[72:75], v[48:51], v[136:139]
ds_read_b64_tr_b16 v[96:97], v26 offset:24576
ds_read_b64_tr_b16 v[98:99], v26 offset:25600
v_mfma_f32_16x16x32_bf16 v[140:143], v[76:79], v[48:51], v[140:143]
v_mfma_f32_16x16x32_bf16 v[144:147], v[80:83], v[48:51], v[144:147]
ds_read_b64_tr_b16 v[100:101], v27 offset:24576
ds_read_b64_tr_b16 v[102:103], v27 offset:25600
v_mfma_f32_16x16x32_bf16 v[148:151], v[84:87], v[48:51], v[148:151]
v_mfma_f32_16x16x32_bf16 v[152:155], v[88:91], v[48:51], v[152:155]
ds_read_b64_tr_b16 v[104:105], v28 offset:24576
ds_read_b64_tr_b16 v[106:107], v28 offset:25600
v_mfma_f32_16x16x32_bf16 v[156:159], v[92:95], v[48:51], v[156:159]
v_mfma_f32_16x16x32_bf16 v[160:163], v[64:67], v[52:55], v[160:163]
ds_read_b128 v[48:51], v25 offset:0
v_mfma_f32_16x16x32_bf16 v[164:167], v[68:71], v[52:55], v[164:167]
v_mfma_f32_16x16x32_bf16 v[168:171], v[72:75], v[52:55], v[168:171]
s_xor_b32 s48, s95, 264192
ds_read_b64_tr_b16 v[108:109], v29 offset:24576
ds_read_b64_tr_b16 v[110:111], v29 offset:25600
v_mfma_f32_16x16x32_bf16 v[172:175], v[76:79], v[52:55], v[172:175]
buffer_store_dwordx4 a[136:139], v7, s[20:23], s48 offen sc1
v_mfma_f32_16x16x32_bf16 v[176:179], v[80:83], v[52:55], v[176:179]
ds_read_b64_tr_b16 v[112:113], v30 offset:24576
ds_read_b64_tr_b16 v[114:115], v30 offset:25600
v_mfma_f32_16x16x32_bf16 v[180:183], v[84:87], v[52:55], v[180:183]
v_mfma_f32_16x16x32_bf16 v[184:187], v[88:91], v[52:55], v[184:187]
ds_read_b64_tr_b16 v[116:117], v31 offset:24576
ds_read_b64_tr_b16 v[118:119], v31 offset:25600
v_mfma_f32_16x16x32_bf16 v[188:191], v[92:95], v[52:55], v[188:191]
v_mfma_f32_16x16x32_bf16 v[192:195], v[64:67], v[56:59], v[192:195]
ds_read_b128 v[52:55], v25 offset:2048
v_mfma_f32_16x16x32_bf16 v[196:199], v[68:71], v[56:59], v[196:199]
v_mfma_f32_16x16x32_bf16 v[200:203], v[72:75], v[56:59], v[200:203]
ds_read_b64_tr_b16 v[120:121], v32 offset:24576
ds_read_b64_tr_b16 v[122:123], v32 offset:25600
v_mfma_f32_16x16x32_bf16 v[204:207], v[76:79], v[56:59], v[204:207]
v_mfma_f32_16x16x32_bf16 v[208:211], v[80:83], v[56:59], v[208:211]
ds_read_b64_tr_b16 v[124:125], v33 offset:24576
ds_read_b64_tr_b16 v[126:127], v33 offset:25600
v_mfma_f32_16x16x32_bf16 v[212:215], v[84:87], v[56:59], v[212:215]
v_mfma_f32_16x16x32_bf16 v[216:219], v[88:91], v[56:59], v[216:219]
s_waitcnt lgkmcnt(15)
v_and_b32 v8, 31, v1
v_lshrrev_b32 v9, 5, v1
v_mfma_f32_16x16x32_bf16 v[220:223], v[92:95], v[56:59], v[220:223]
v_lshrrev_b32 v10, 4, v8
v_and_b32 v13, 15, v1
v_mfma_f32_16x16x32_bf16 v[224:227], v[64:67], v[60:63], v[224:227]
ds_read_b128 v[56:59], v25 offset:4096
v_lshlrev_b32 v13, 4, v13
v_mfma_f32_16x16x32_bf16 v[228:231], v[68:71], v[60:63], v[228:231]
v_lshl_add_u32 v13, v9, 8, v13
v_subrev_u32 v13, s91, v13
v_mfma_f32_16x16x32_bf16 v[232:235], v[72:75], v[60:63], v[232:235]
ds_read_b128 v[20:23], v25 offset:6144
v_lshl_add_u32 v32, v10, 12, v13
v_mfma_f32_16x16x32_bf16 v[236:239], v[76:79], v[60:63], v[236:239]
v_add_u32 v32, 0x10000, v32
v_lshl_add_u32 v33, v10, 9, v13
v_mfma_f32_16x16x32_bf16 v[240:243], v[80:83], v[60:63], v[240:243]
v_lshlrev_b32 v9, 2, v9
v_sub_u32 v8, v8, v9
v_mfma_f32_16x16x32_bf16 v[244:247], v[84:87], v[60:63], v[244:247]
v_mov_b32 v34, 0xff800000
v_add_u32 v12, s46, v32
v_mfma_f32_16x16x32_bf16 v[248:251], v[88:91], v[60:63], v[248:251]
s_add_u32 s28, s39, 0
s_and_b32 s28, s28, 0xffff
s_add_u32 s49, s39, 16384
v_mfma_f32_16x16x32_bf16 v[252:255], v[92:95], v[60:63], v[252:255]
s_waitcnt  lgkmcnt(0)
s_waitcnt vmcnt(4)
s_barrier
s_mov_b32 s40, 16384
v_mfma_f32_16x16x32_bf16 v[128:131], v[96:99], v[48:51], v[128:131]
s_add_u32 m0, s28, 0
s_xor_b32 s46, s39, 0x8000
s_mov_b32 s44, 65536
v_mfma_f32_16x16x32_bf16 v[132:135], v[100:103], v[48:51], v[132:135]
s_xor_b32 s50, s46, 0x18000
buffer_load_dwordx4 v7, s[8:11], s40 offen offset:0 sc1 lds
v_mfma_f32_16x16x32_bf16 v[136:139], v[104:107], v[48:51], v[136:139]
s_lshl_b32 s30, s89, 2
s_add_u32 s51, s30, s94
s_max_u32 s29, s51, 3
v_mfma_f32_16x16x32_bf16 v[140:143], v[108:111], v[48:51], v[140:143]
s_sub_u32 s42, s29, 3
s_sub_u32 s56, 57, s30
s_add_u32 s56, s56, s3
v_mfma_f32_16x16x32_bf16 v[144:147], v[112:115], v[48:51], v[144:147]
ds_read_b128 a[0:3], v12 offset:0
ds_read_b128 a[32:35], v12 offset:8192
v_mfma_f32_16x16x32_bf16 v[148:151], v[116:119], v[48:51], v[148:151]
s_mov_b32 s57, 2
buffer_load_dwordx4 v7, s[8:11], s40 offen offset:1024 sc1 lds
v_mfma_f32_16x16x32_bf16 v[152:155], v[120:123], v[48:51], v[152:155]
ds_read_b128 a[4:7], v12 offset:512
ds_read_b128 a[36:39], v12 offset:8704
v_mfma_f32_16x16x32_bf16 v[156:159], v[124:127], v[48:51], v[156:159]
s_min_u32 s43, s42, s57
s_sub_u32 s55, s43, 1
s_sub_u32 s31, s51, s29
v_mfma_f32_16x16x32_bf16 v[160:163], v[96:99], v[52:55], v[160:163]
ds_read_b128 a[8:11], v12 offset:1024
ds_read_b128 a[40:43], v12 offset:9216
v_mfma_f32_16x16x32_bf16 v[164:167], v[100:103], v[52:55], v[164:167]
s_lshl_b32 s31, s31, 5
buffer_load_dwordx4 v7, s[8:11], s40 offen offset:2048 sc1 lds
v_mfma_f32_16x16x32_bf16 v[168:171], v[104:107], v[52:55], v[168:171]
ds_read_b128 a[12:15], v12 offset:1536
ds_read_b128 a[44:47], v12 offset:9728
v_mfma_f32_16x16x32_bf16 v[172:175], v[108:111], v[52:55], v[172:175]
v_mfma_f32_16x16x32_bf16 v[176:179], v[112:115], v[52:55], v[176:179]
ds_read_b128 a[16:19], v12 offset:2048
ds_read_b128 a[48:51], v12 offset:10240
v_mfma_f32_16x16x32_bf16 v[180:183], v[116:119], v[52:55], v[180:183]
buffer_load_dwordx4 v7, s[8:11], s40 offen offset:3072 sc1 lds
v_mfma_f32_16x16x32_bf16 v[184:187], v[120:123], v[52:55], v[184:187]
ds_read_b128 a[20:23], v12 offset:2560
ds_read_b128 a[52:55], v12 offset:10752
v_mfma_f32_16x16x32_bf16 v[188:191], v[124:127], v[52:55], v[188:191]
v_mfma_f32_16x16x32_bf16 v[192:195], v[96:99], v[56:59], v[192:195]
ds_read_b128 a[24:27], v12 offset:3072
ds_read_b128 a[56:59], v12 offset:11264
v_mfma_f32_16x16x32_bf16 v[196:199], v[100:103], v[56:59], v[196:199]
v_mfma_f32_16x16x32_bf16 v[200:203], v[104:107], v[56:59], v[200:203]
ds_read_b128 a[28:31], v12 offset:3584
ds_read_b128 a[60:63], v12 offset:11776
v_mfma_f32_16x16x32_bf16 v[204:207], v[108:111], v[56:59], v[204:207]
v_cvt_pk_bf16_f32 v64, v128, v129
v_cvt_pk_bf16_f32 v66, v160, v161
v_mfma_f32_16x16x32_bf16 v[208:211], v[112:115], v[56:59], v[208:211]
v_cvt_pk_bf16_f32 v65, v130, v131
v_cvt_pk_bf16_f32 v67, v162, v163
v_mfma_f32_16x16x32_bf16 v[212:215], v[116:119], v[56:59], v[212:215]
v_cvt_pk_bf16_f32 v68, v132, v133
v_cvt_pk_bf16_f32 v70, v164, v165
v_mfma_f32_16x16x32_bf16 v[216:219], v[120:123], v[56:59], v[216:219]
v_cvt_pk_bf16_f32 v69, v134, v135
v_cvt_pk_bf16_f32 v71, v166, v167
v_mfma_f32_16x16x32_bf16 v[220:223], v[124:127], v[56:59], v[220:223]
v_permlane16_swap_b32 v64, v66
v_mfma_f32_16x16x32_bf16 v[224:227], v[96:99], v[20:23], v[224:227]
v_permlane16_swap_b32 v65, v67
v_mfma_f32_16x16x32_bf16 v[228:231], v[100:103], v[20:23], v[228:231]
v_cvt_pk_bf16_f32 v72, v136, v137
v_cvt_pk_bf16_f32 v74, v168, v169
v_mfma_f32_16x16x32_bf16 v[232:235], v[104:107], v[20:23], v[232:235]
v_cvt_pk_bf16_f32 v73, v138, v139
v_cvt_pk_bf16_f32 v75, v170, v171
v_mfma_f32_16x16x32_bf16 v[236:239], v[108:111], v[20:23], v[236:239]
v_permlane16_swap_b32 v68, v70
v_mfma_f32_16x16x32_bf16 v[240:243], v[112:115], v[20:23], v[240:243]
v_permlane16_swap_b32 v69, v71
v_mfma_f32_16x16x32_bf16 v[244:247], v[116:119], v[20:23], v[244:247]
v_cvt_pk_bf16_f32 v76, v140, v141
v_cvt_pk_bf16_f32 v78, v172, v173
v_mfma_f32_16x16x32_bf16 v[248:251], v[120:123], v[20:23], v[248:251]
s_getpc_b64 s[58:59]
.Lpc0:
s_add_u32 s58, s58, .Ljob0-.Lpc0
s_addc_u32 s59, s59, 0
v_mfma_f32_16x16x32_bf16 v[252:255], v[124:127], v[20:23], v[252:255]
s_waitcnt lgkmcnt(0)
v_cvt_pk_bf16_f32 v77, v142, v143
v_cvt_pk_bf16_f32 v79, v174, v175
v_permlane16_swap_b32 v72, v74
v_permlane16_swap_b32 v73, v75
v_cvt_pk_bf16_f32 v80, v144, v145
v_cvt_pk_bf16_f32 v82, v176, v177
v_cvt_pk_bf16_f32 v81, v146, v147
v_cvt_pk_bf16_f32 v83, v178, v179
v_permlane16_swap_b32 v76, v78
v_permlane16_swap_b32 v77, v79
v_cvt_pk_bf16_f32 v84, v148, v149
v_cvt_pk_bf16_f32 v86, v180, v181
v_cvt_pk_bf16_f32 v85, v150, v151
v_cvt_pk_bf16_f32 v87, v182, v183
v_permlane16_swap_b32 v80, v82
v_permlane16_swap_b32 v81, v83
v_cvt_pk_bf16_f32 v88, v152, v153
v_cvt_pk_bf16_f32 v90, v184, v185
v_cvt_pk_bf16_f32 v89, v154, v155
v_cvt_pk_bf16_f32 v91, v186, v187
v_permlane16_swap_b32 v84, v86
v_permlane16_swap_b32 v85, v87
v_cvt_pk_bf16_f32 v92, v156, v157
v_cvt_pk_bf16_f32 v94, v188, v189
v_cvt_pk_bf16_f32 v93, v158, v159
v_cvt_pk_bf16_f32 v95, v190, v191
v_permlane16_swap_b32 v88, v90
v_permlane16_swap_b32 v89, v91
v_permlane16_swap_b32 v92, v94
v_permlane16_swap_b32 v93, v95
v_add_u32 v62, s49, v32
v_add_u32 v63, s39, v33
v_add_u32 v13, s31, v8
v_mfma_f32_32x32x16_bf16 v[128:143], a[0:3], v[64:67], 0
s_add_u32 s28, s39, 16384
s_and_b32 s28, s28, 0xffff
s_mov_b32 s41, 32768
s_add_u32 m0, s28, 0
v_cvt_pk_bf16_f32 v96, v192, v193
v_cvt_pk_bf16_f32 v98, v224, v225
v_cvt_pk_bf16_f32 v97, v194, v195
v_mfma_f32_32x32x16_bf16 v[128:143], a[4:7], v[68:71], v[128:143]
buffer_load_dwordx4 v7, s[8:11], s41 offen offset:0 sc1 lds
v_cvt_pk_bf16_f32 v99, v226, v227
v_cvt_pk_bf16_f32 v100, v196, v197
v_cvt_pk_bf16_f32 v102, v228, v229
v_cvt_pk_bf16_f32 v101, v198, v199
v_cvt_pk_bf16_f32 v103, v230, v231
v_mfma_f32_32x32x16_bf16 v[128:143], a[8:11], v[72:75], v[128:143]
buffer_load_dwordx4 v7, s[8:11], s41 offen offset:1024 sc1 lds
v_permlane16_swap_b32 v96, v98
v_permlane16_swap_b32 v97, v99
v_cvt_pk_bf16_f32 v104, v200, v201
v_mfma_f32_32x32x16_bf16 v[128:143], a[12:15], v[76:79], v[128:143]
buffer_load_dwordx4 v7, s[8:11], s41 offen offset:2048 sc1 lds
v_cvt_pk_bf16_f32 v106, v232, v233
v_cvt_pk_bf16_f32 v105, v202, v203
v_cvt_pk_bf16_f32 v107, v234, v235
v_permlane16_swap_b32 v100, v102
v_mfma_f32_32x32x16_bf16 v[128:143], a[16:19], v[80:83], v[128:143]
buffer_load_dwordx4 v7, s[8:11], s41 offen offset:3072 sc1 lds
v_permlane16_swap_b32 v101, v103
v_cvt_pk_bf16_f32 v108, v204, v205
v_cvt_pk_bf16_f32 v110, v236, v237
v_cvt_pk_bf16_f32 v109, v206, v207
v_mfma_f32_32x32x16_bf16 v[128:143], a[20:23], v[84:87], v[128:143]
s_add_u32 s28, s39, 32768
s_and_b32 s28, s28, 0xffff
v_cvt_pk_bf16_f32 v111, v238, v239
v_permlane16_swap_b32 v104, v106
v_permlane16_swap_b32 v105, v107
v_mfma_f32_32x32x16_bf16 v[128:143], a[24:27], v[88:91], v[128:143]
s_mov_b32 s40, 49152
v_cvt_pk_bf16_f32 v112, v208, v209
v_cvt_pk_bf16_f32 v114, v240, v241
v_cvt_pk_bf16_f32 v113, v210, v211
v_cvt_pk_bf16_f32 v115, v242, v243
v_permlane16_swap_b32 v108, v110
v_mfma_f32_32x32x16_bf16 v[128:143], a[28:31], v[92:95], v[128:143]
s_add_u32 m0, s28, 0
v_permlane16_swap_b32 v109, v111
v_cvt_pk_bf16_f32 v116, v212, v213
v_cvt_pk_bf16_f32 v118, v244, v245
v_cvt_pk_bf16_f32 v117, v214, v215
v_cvt_pk_bf16_f32 v119, v246, v247
v_permlane16_swap_b32 v112, v114
v_permlane16_swap_b32 v113, v115
v_cvt_pk_bf16_f32 v120, v216, v217
v_cvt_pk_bf16_f32 v122, v248, v249
v_cvt_pk_bf16_f32 v121, v218, v219
v_cvt_pk_bf16_f32 v123, v250, v251
v_permlane16_swap_b32 v116, v118
v_permlane16_swap_b32 v117, v119
v_mfma_f32_32x32x16_bf16 v[160:175], a[0:3], v[96:99], 0
buffer_load_dwordx4 v7, s[8:11], s40 offen offset:0 sc1 lds
v_cvt_pk_bf16_f32 v124, v220, v221
v_cvt_pk_bf16_f32 v126, v252, v253
v_cvt_pk_bf16_f32 v125, v222, v223
v_cvt_pk_bf16_f32 v127, v254, v255
v_mfma_f32_32x32x16_bf16 v[160:175], a[4:7], v[100:103], v[160:175]
ds_read_b128 a[0:3], v62 offset:0
ds_read_b128 a[4:7], v62 offset:512
v_permlane16_swap_b32 v120, v122
v_permlane16_swap_b32 v121, v123
v_mfma_f32_32x32x16_bf16 v[160:175], a[8:11], v[104:107], v[160:175]
buffer_load_dwordx4 v7, s[8:11], s40 offen offset:1024 sc1 lds
v_permlane16_swap_b32 v124, v126
v_permlane16_swap_b32 v125, v127
v_max3_f32 v52, v128, v129, v130
v_mfma_f32_32x32x16_bf16 v[160:175], a[12:15], v[108:111], v[160:175]
ds_read_b128 a[8:11], v62 offset:1024
ds_read_b128 a[12:15], v62 offset:1536
v_max3_f32 v53, v131, v132, v133
v_max3_f32 v52, v52, v134, v135
v_max3_f32 v53, v53, v136, v137
v_max3_f32 v52, v52, v138, v139
v_mfma_f32_32x32x16_bf16 v[160:175], a[16:19], v[112:115], v[160:175]
buffer_load_dwordx4 v7, s[8:11], s40 offen offset:2048 sc1 lds
v_max3_f32 v53, v53, v140, v141
v_max3_f32 v52, v52, v142, v143
v_max_f32 v52, v52, v53
v_mov_b32 v53, v52
v_nop
v_mfma_f32_32x32x16_bf16 v[160:175], a[20:23], v[116:119], v[160:175]
ds_read_b128 a[16:19], v62 offset:2048
ds_read_b128 a[20:23], v62 offset:2560
v_permlane32_swap_b32 v52, v53
v_max_f32 v52, v52, v53
v_sub_f32 v192, 0, v52
v_mfma_f32_32x32x16_bf16 v[160:175], a[24:27], v[120:123], v[160:175]
buffer_load_dwordx4 v7, s[8:11], s40 offen offset:3072 sc1 lds
v_sub_f32 v193, 0, v52
v_sub_f32 v194, 0, v52
v_sub_f32 v195, 0, v52
v_sub_f32 v196, 0, v52
v_sub_f32 v197, 0, v52
v_mfma_f32_32x32x16_bf16 v[160:175], a[28:31], v[124:127], v[160:175]
s_xor_b32 s48, s95, 265216
ds_read_b128 a[24:27], v62 offset:3072
ds_read_b128 a[28:31], v62 offset:3584
v_sub_f32 v198, 0, v52
v_sub_f32 v199, 0, v52
v_sub_f32 v200, 0, v52
v_sub_f32 v201, 0, v52
v_sub_f32 v202, 0, v52
v_sub_f32 v203, 0, v52
v_sub_f32 v204, 0, v52
v_sub_f32 v205, 0, v52
v_sub_f32 v206, 0, v52
v_sub_f32 v207, 0, v52
s_nop 1
v_mfma_f32_32x32x16_bf16 v[144:159], a[32:35], v[64:67], v[192:207]
buffer_store_dwordx4 a[140:143], v7, s[20:23], s48 offen sc1
v_add_f32 v128, v128, v192
v_add_f32 v129, v129, v192
v_add_f32 v130, v130, v192
v_add_f32 v131, v131, v192
v_mfma_f32_32x32x16_bf16 v[144:159], a[36:39], v[68:71], v[144:159]
s_xor_b32 s48, s95, 327680
v_add_f32 v132, v132, v192
v_add_f32 v133, v133, v192
v_add_f32 v134, v134, v192
v_add_f32 v135, v135, v192
v_mfma_f32_32x32x16_bf16 v[144:159], a[40:43], v[72:75], v[144:159]
s_cmp_eq_u32 s51, 0
s_cbranch_scc1 .Lj0a
.Lj0a_ret:
buffer_store_dwordx4 a[160:163], v7, s[20:23], s48 offen sc1
v_max3_f32 v56, v160, v161, v162
v_max3_f32 v57, v163, v164, v165
v_mfma_f32_32x32x16_bf16 v[144:159], a[44:47], v[76:79], v[144:159]
v_max3_f32 v56, v56, v166, v167
v_max3_f32 v57, v57, v168, v169
v_max3_f32 v56, v56, v170, v171
v_max3_f32 v57, v57, v172, v173
v_max3_f32 v56, v56, v174, v175
v_max_f32 v56, v56, v57
v_mfma_f32_32x32x16_bf16 v[144:159], a[48:51], v[80:83], v[144:159]
s_xor_b32 s48, s95, 328704
v_mov_b32 v57, v56
v_nop
s_nop 0
v_permlane32_swap_b32 v56, v57
v_max_f32 v56, v56, v57
v_sub_f32 v208, 0, v56
v_mfma_f32_32x32x16_bf16 v[144:159], a[52:55], v[84:87], v[144:159]
buffer_store_dwordx4 a[164:167], v7, s[20:23], s48 offen sc1
v_sub_f32 v209, 0, v56
v_sub_f32 v210, 0, v56
v_sub_f32 v211, 0, v56
v_sub_f32 v212, 0, v56
v_sub_f32 v213, 0, v56
v_mfma_f32_32x32x16_bf16 v[144:159], a[56:59], v[88:91], v[144:159]
v_sub_f32 v214, 0, v56
v_sub_f32 v215, 0, v56
v_sub_f32 v216, 0, v56
v_sub_f32 v217, 0, v56
v_sub_f32 v218, 0, v56
v_sub_f32 v219, 0, v56
v_sub_f32 v220, 0, v56
v_sub_f32 v221, 0, v56
v_sub_f32 v222, 0, v56
v_sub_f32 v223, 0, v56
v_mov_b32 v14, v208
v_mfma_f32_32x32x16_bf16 v[144:159], a[60:63], v[92:95], v[144:159]
s_xor_b32 s48, s95, 329728
s_cmp_lt_u32 s51, 2
s_cbranch_scc1 .Lj0b
.Lj0b_ret:
v_add_f32 v136, v136, v192
v_add_f32 v137, v137, v192
v_add_f32 v138, v138, v192
v_mfma_f32_32x32x16_bf16 v[176:191], a[32:35], v[96:99], v[208:223]
buffer_store_dwordx4 a[168:171], v7, s[20:23], s48 offen sc1
v_add_f32 v139, v139, v192
v_add_f32 v140, v140, v192
v_add_f32 v141, v141, v192
v_mfma_f32_32x32x16_bf16 v[176:191], a[36:39], v[100:103], v[176:191]
s_xor_b32 s48, s95, 330752
v_add_f32 v142, v142, v192
v_add_f32 v143, v143, v192
v_exp_f32 v52, v128
v_mfma_f32_32x32x16_bf16 v[176:191], a[40:43], v[104:107], v[176:191]
buffer_store_dwordx4 a[172:175], v7, s[20:23], s48 offen sc1
v_exp_f32 v53, v129
v_mov_b32 v60, v52
v_exp_f32 v54, v130
v_mfma_f32_32x32x16_bf16 v[176:191], a[44:47], v[108:111], v[176:191]
s_cmp_lt_u32 s51, 3
s_cbranch_scc1 .Lj0c
.Lj0c_ret:
v_add_f32 v60, v60, v53
v_cvt_pk_bf16_f32 v224, v52, v53
v_exp_f32 v55, v131
v_mfma_f32_32x32x16_bf16 v[176:191], a[48:51], v[112:115], v[176:191]
s_xor_b32 s48, s95, 393216
v_add_f32 v60, v60, v54
v_exp_f32 v52, v132
v_add_f32 v60, v60, v55
v_cvt_pk_bf16_f32 v225, v54, v55
v_mfma_f32_32x32x16_bf16 v[176:191], a[52:55], v[116:119], v[176:191]
buffer_store_dwordx4 a[192:195], v7, s[20:23], s48 offen sc1
v_exp_f32 v53, v133
v_add_f32 v60, v60, v52
v_exp_f32 v54, v134
v_mfma_f32_32x32x16_bf16 v[176:191], a[56:59], v[120:123], v[176:191]
v_add_f32 v60, v60, v53
v_cvt_pk_bf16_f32 v226, v52, v53
v_exp_f32 v55, v135
v_add_f32 v60, v60, v54
v_mfma_f32_32x32x16_bf16 v[176:191], a[60:63], v[124:127], v[176:191]
v_exp_f32 v52, v136
v_add_f32 v60, v60, v55
v_cvt_pk_bf16_f32 v227, v54, v55
v_exp_f32 v53, v137
v_add_f32 v60, v60, v52
v_exp_f32 v54, v138
v_add_f32 v60, v60, v53
v_cvt_pk_bf16_f32 v228, v52, v53
v_exp_f32 v55, v139
v_add_f32 v60, v60, v54
v_exp_f32 v52, v140
v_add_f32 v60, v60, v55
v_cvt_pk_bf16_f32 v229, v54, v55
v_exp_f32 v53, v141
v_add_f32 v60, v60, v52
v_exp_f32 v54, v142
v_add_f32 v60, v60, v53
v_cvt_pk_bf16_f32 v230, v52, v53
v_exp_f32 v55, v143
v_add_f32 v60, v60, v54
v_add_f32 v60, v60, v55
v_cvt_pk_bf16_f32 v231, v54, v55
v_add_f32 v160, v160, v14
v_add_f32 v161, v161, v14
v_add_f32 v162, v162, v14
v_add_f32 v163, v163, v14
v_add_f32 v164, v164, v14
v_add_f32 v165, v165, v14
v_add_f32 v166, v166, v14
v_add_f32 v167, v167, v14
v_add_f32 v168, v168, v14
v_add_f32 v169, v169, v14
v_add_f32 v170, v170, v14
v_add_f32 v171, v171, v14
v_add_f32 v172, v172, v14
v_add_f32 v173, v173, v14
v_add_f32 v174, v174, v14
v_add_f32 v175, v175, v14
v_exp_f32 v56, v160
v_exp_f32 v57, v161
v_mov_b32 v61, v56
v_exp_f32 v58, v162
v_add_f32 v61, v61, v57
v_cvt_pk_bf16_f32 v240, v56, v57
v_exp_f32 v59, v163
v_add_f32 v61, v61, v58
v_exp_f32 v56, v164
v_add_f32 v61, v61, v59
v_cvt_pk_bf16_f32 v241, v58, v59
v_exp_f32 v57, v165
v_add_f32 v61, v61, v56
v_exp_f32 v58, v166
v_add_f32 v61, v61, v57
v_cvt_pk_bf16_f32 v242, v56, v57
v_exp_f32 v59, v167
v_add_f32 v61, v61, v58
v_exp_f32 v56, v168
v_add_f32 v61, v61, v59
v_cvt_pk_bf16_f32 v243, v58, v59
v_exp_f32 v57, v169
v_add_f32 v61, v61, v56
v_exp_f32 v58, v170
v_add_f32 v61, v61, v57
v_cvt_pk_bf16_f32 v244, v56, v57
v_exp_f32 v59, v171
v_add_f32 v61, v61, v58
v_exp_f32 v56, v172
v_add_f32 v61, v61, v59
v_cvt_pk_bf16_f32 v245, v58, v59
v_exp_f32 v57, v173
v_add_f32 v61, v61, v56
v_exp_f32 v58, v174
v_add_f32 v61, v61, v57
v_cvt_pk_bf16_f32 v246, v56, v57
v_exp_f32 v59, v175
v_add_f32 v61, v61, v58
v_add_f32 v61, v61, v59
v_cvt_pk_bf16_f32 v247, v58, v59
s_waitcnt vmcnt(14) lgkmcnt(0)
s_barrier
v_mfma_f32_32x32x16_bf16 v[128:143], a[0:3], v[64:67], v[192:207]
ds_read_b128 a[32:35], v63 offset:0
v_exp_f32 v52, v144
v_exp_f32 v56, v176
v_add_f32 v60, v60, v52
v_mfma_f32_32x32x16_bf16 v[160:175], a[0:3], v[96:99], v[208:223]
s_xor_b32 s48, s95, 394240
ds_read_b128 a[0:3], v62 offset:8192
v_exp_f32 v53, v145
v_add_f32 v61, v61, v56
v_cmp_gt_i32 s[32:33], 0, v13
v_mfma_f32_32x32x16_bf16 v[128:143], a[4:7], v[68:71], v[128:143]
buffer_store_dwordx4 a[196:199], v7, s[20:23], s48 offen sc1
ds_read_b128 a[36:39], v63 offset:1024
v_exp_f32 v57, v177
v_add_f32 v60, v60, v53
v_cvt_pk_bf16_f32 v232, v52, v53
v_mfma_f32_32x32x16_bf16 v[160:175], a[4:7], v[100:103], v[160:175]
s_sub_u32 s52, s52, 1
s_cselect_b32 s52, 0, s52
s_cselect_b32 s53, 0, 0x8000
ds_read_b128 a[4:7], v62 offset:8704
v_exp_f32 v54, v146
v_mfma_f32_32x32x16_bf16 v[128:143], a[8:11], v[72:75], v[128:143]
ds_read_b128 a[40:43], v63 offset:2048
v_add_f32 v61, v61, v57
v_cvt_pk_bf16_f32 v248, v56, v57
v_exp_f32 v58, v178
v_add_f32 v60, v60, v54
v_mfma_f32_32x32x16_bf16 v[160:175], a[8:11], v[104:107], v[160:175]
s_xor_b32 s48, s95, 395264
ds_read_b128 a[8:11], v62 offset:9216
v_exp_f32 v55, v147
v_add_f32 v61, v61, v58
v_nop
v_mfma_f32_32x32x16_bf16 v[128:143], a[12:15], v[76:79], v[128:143]
buffer_store_dwordx4 a[200:203], v7, s[20:23], s48 offen sc1
ds_read_b128 a[44:47], v63 offset:3072
v_exp_f32 v59, v179
v_add_f32 v60, v60, v55
v_cvt_pk_bf16_f32 v233, v54, v55
v_mfma_f32_32x32x16_bf16 v[160:175], a[12:15], v[108:111], v[160:175]
ds_read_b128 a[12:15], v62 offset:9728
v_exp_f32 v52, v148
v_add_f32 v61, v61, v59
v_cvt_pk_bf16_f32 v249, v58, v59
v_cmp_gt_i32 s[34:35], 1, v13
v_mfma_f32_32x32x16_bf16 v[128:143], a[16:19], v[80:83], v[128:143]
ds_read_b128 a[48:51], v63 offset:4096
v_exp_f32 v56, v180
v_add_f32 v60, v60, v52
v_cndmask_b32 v36, v14, v34, s[32:33]
v_cmp_gt_i32 s[32:33], 2, v13
v_mfma_f32_32x32x16_bf16 v[160:175], a[16:19], v[112:115], v[160:175]
s_xor_b32 s48, s95, 396288
ds_read_b128 a[16:19], v62 offset:10240
ds_read_b128 a[56:59], v63 offset:6144
v_exp_f32 v53, v149
v_add_f32 v61, v61, v56
v_mfma_f32_32x32x16_bf16 v[128:143], a[20:23], v[84:87], v[128:143]
buffer_store_dwordx4 a[204:207], v7, s[20:23], s48 offen sc1
ds_read_b128 a[52:55], v63 offset:5120
v_exp_f32 v57, v181
v_add_f32 v60, v60, v53
v_cvt_pk_bf16_f32 v234, v52, v53
v_mfma_f32_32x32x16_bf16 v[160:175], a[20:23], v[116:119], v[160:175]
ds_read_b128 a[20:23], v62 offset:10752
ds_read_b128 a[60:63], v63 offset:7168
v_exp_f32 v54, v150
v_add_f32 v61, v61, v57
v_cvt_pk_bf16_f32 v250, v56, v57
v_mfma_f32_32x32x16_bf16 v[128:143], a[24:27], v[88:91], v[128:143]
v_exp_f32 v58, v182
v_add_f32 v60, v60, v54
v_cndmask_b32 v37, v14, v34, s[34:35]
v_cmp_gt_i32 s[34:35], 3, v13
v_cndmask_b32 v38, v14, v34, s[32:33]
v_mfma_f32_32x32x16_bf16 v[160:175], a[24:27], v[120:123], v[160:175]
s_xor_b32 s48, s95, 458752
ds_read_b128 a[24:27], v62 offset:11264
.Lpad10:
s_nop 0
v_exp_f32 v55, v151
v_add_f32 v61, v61, v58
v_cmp_gt_i32 s[32:33], 8, v13
v_mfma_f32_32x32x16_bf16 v[128:143], a[28:31], v[92:95], v[128:143]
buffer_store_dwordx4 a[224:227], v7, s[20:23], s48 offen sc1
v_exp_f32 v59, v183
v_add_f32 v60, v60, v55
v_cvt_pk_bf16_f32 v235, v54, v55
v_cndmask_b32 v39, v14, v34, s[34:35]
v_mfma_f32_32x32x16_bf16 v[160:175], a[28:31], v[124:127], v[160:175]
s_waitcnt lgkmcnt(1)
ds_read_b128 a[28:31], v62 offset:11776
v_exp_f32 v52, v152
v_add_f32 v61, v61, v59
v_cvt_pk_bf16_f32 v251, v58, v59
v_mfma_f32_32x32x16_bf16 a[64:79], a[32:35], v[224:227], 0
v_exp_f32 v56, v184
v_add_f32 v60, v60, v52
v_exp_f32 v53, v153
v_add_f32 v61, v61, v56
v_mfma_f32_32x32x16_bf16 a[176:191], a[32:35], v[240:243], 0
s_xor_b32 s48, s95, 459776
ds_read_b128 a[32:35], v63 offset:8192
v_add_u32 v62, s46, v32
v_exp_f32 v57, v185
v_add_f32 v60, v60, v53
v_cvt_pk_bf16_f32 v236, v52, v53
v_mfma_f32_32x32x16_bf16 a[80:95], a[36:39], v[224:227], 0
buffer_store_dwordx4 a[228:231], v7, s[20:23], s48 offen sc1
v_exp_f32 v54, v154
v_add_f32 v61, v61, v57
v_cvt_pk_bf16_f32 v252, v56, v57
v_cmp_gt_i32 s[34:35], 9, v13
v_mfma_f32_32x32x16_bf16 a[208:223], a[36:39], v[240:243], 0
ds_read_b128 a[36:39], v63 offset:9216
v_exp_f32 v58, v186
v_add_f32 v60, v60, v54
v_cndmask_b32 v40, v14, v34, s[32:33]
v_cmp_gt_i32 s[32:33], 10, v13
v_mfma_f32_32x32x16_bf16 a[112:127], a[40:43], v[224:227], 0
v_exp_f32 v55, v155
v_add_f32 v61, v61, v58
v_cndmask_b32 v41, v14, v34, s[34:35]
v_cmp_gt_i32 s[34:35], 11, v13
v_cndmask_b32 v42, v14, v34, s[32:33]
v_mfma_f32_32x32x16_bf16 a[240:255], a[40:43], v[240:243], 0
s_xor_b32 s48, s95, 460800
ds_read_b128 a[40:43], v63 offset:10240
v_exp_f32 v59, v187
v_add_f32 v60, v60, v55
v_cvt_pk_bf16_f32 v237, v54, v55
v_mfma_f32_32x32x16_bf16 a[144:159], a[44:47], v[224:227], 0
buffer_store_dwordx4 a[232:235], v7, s[20:23], s48 offen sc1
v_exp_f32 v52, v156
v_add_f32 v61, v61, v59
v_cvt_pk_bf16_f32 v253, v58, v59
v_cmp_gt_i32 s[32:33], 16, v13
v_mfma_f32_32x32x16_bf16 a[96:111], a[44:47], v[240:243], 0
ds_read_b128 a[44:47], v63 offset:11264
v_exp_f32 v56, v188
v_add_f32 v60, v60, v52
v_cndmask_b32 v43, v14, v34, s[34:35]
v_cmp_gt_i32 s[34:35], 17, v13
v_mfma_f32_32x32x16_bf16 a[64:79], a[48:51], v[228:231], a[64:79]
v_exp_f32 v53, v157
v_add_f32 v61, v61, v56
v_cndmask_b32 v44, v14, v34, s[32:33]
v_cmp_gt_i32 s[32:33], 18, v13
v_cndmask_b32 v45, v14, v34, s[34:35]
v_mfma_f32_32x32x16_bf16 a[176:191], a[48:51], v[244:247], a[176:191]
s_xor_b32 s48, s95, 461824
ds_read_b128 a[48:51], v63 offset:12288
v_exp_f32 v57, v189
v_add_f32 v60, v60, v53
v_cvt_pk_bf16_f32 v238, v52, v53
v_mfma_f32_32x32x16_bf16 a[80:95], a[52:55], v[228:231], a[80:95]
global_load_dword v35, v[2:3], off
buffer_store_dwordx4 a[236:239], v7, s[20:23], s48 offen sc1
v_exp_f32 v54, v158
v_add_f32 v61, v61, v57
v_cvt_pk_bf16_f32 v254, v56, v57
v_mfma_f32_32x32x16_bf16 a[208:223], a[52:55], v[244:247], a[208:223]
ds_read_b128 a[52:55], v63 offset:13312
v_add_co_u32 v2, vcc, s53, v2
v_exp_f32 v58, v190
v_add_f32 v60, v60, v54
v_cmp_gt_i32 s[34:35], 19, v13
v_mfma_f32_32x32x16_bf16 a[112:127], a[56:59], v[228:231], a[112:127]
v_exp_f32 v55, v159
v_add_f32 v61, v61, v58
v_cndmask_b32 v46, v14, v34, s[32:33]
v_cmp_gt_i32 s[32:33], 24, v13
v_cndmask_b32 v47, v14, v34, s[34:35]
v_mfma_f32_32x32x16_bf16 a[240:255], a[56:59], v[244:247], a[240:255]
ds_read_b128 a[56:59], v63 offset:14336
v_addc_co_u32 v3, vcc, 0, v3, vcc
v_exp_f32 v59, v191
v_add_f32 v60, v60, v55
v_cvt_pk_bf16_f32 v239, v54, v55
v_mfma_f32_32x32x16_bf16 a[144:159], a[60:63], v[228:231], a[144:159]
v_add_f32 v61, v61, v59
v_cvt_pk_bf16_f32 v255, v58, v59
v_cmp_gt_i32 s[34:35], 25, v13
v_cndmask_b32 v48, v14, v34, s[32:33]
v_cmp_gt_i32 s[32:33], 26, v13
v_cndmask_b32 v49, v14, v34, s[34:35]
v_mfma_f32_32x32x16_bf16 a[96:111], a[60:63], v[244:247], a[96:111]
s_waitcnt vmcnt(18) lgkmcnt(7)
s_barrier
ds_read_b128 a[60:63], v63 offset:15360
v_cmp_gt_i32 s[34:35], 27, v13
v_cndmask_b32 v50, v14, v34, s[32:33]
v_nop
v_cndmask_b32 v51, v14, v34, s[34:35]
s_cmp_eq_u32 s29, 3
s_cbranch_scc1 .LT1
s_nop 0
s_nop 0
s_nop 0
s_nop 0
s_nop 0
s_nop 0
s_nop 0
s_nop 0
s_nop 0
s_nop 0
s_nop 0
s_nop 0
s_nop 0
s_nop 0
s_nop 0
.LD1:
v_mfma_f32_32x32x16_bf16 v[144:159], a[0:3], v[64:67], v[192:207]
s_cmp_lt_u32 s44, s54
s_cselect_b32 m0, s50, s27
v_add_u32 v63, s49, v33
v_exp_f32 v52, v128
v_exp_f32 v56, v160
v_mfma_f32_32x32x16_bf16 v[176:191], a[0:3], v[96:99], v[208:223]
ds_read_b128 a[0:3], v62 offset:0
v_add_f32 v60, v60, v52
v_exp_f32 v53, v129
v_add_f32 v61, v61, v56
v_mfma_f32_32x32x16_bf16 v[144:159], a[4:7], v[68:71], v[144:159]
v_exp_f32 v57, v161
v_add_f32 v60, v60, v53
v_cvt_pk_bf16_f32 v224, v52, v53
v_mfma_f32_32x32x16_bf16 v[176:191], a[4:7], v[100:103], v[176:191]
ds_read_b128 a[4:7], v62 offset:512
v_exp_f32 v54, v130
v_add_f32 v61, v61, v57
v_cvt_pk_bf16_f32 v240, v56, v57
v_mfma_f32_32x32x16_bf16 v[144:159], a[8:11], v[72:75], v[144:159]
s_mov_b32 s49, s46
buffer_load_dwordx4 v7, s[4:7], s44 offen offset:0 sc1 lds
v_exp_f32 v58, v162
v_add_f32 v60, v60, v54
v_mfma_f32_32x32x16_bf16 v[176:191], a[8:11], v[104:107], v[176:191]
ds_read_b128 a[8:11], v62 offset:1024
v_exp_f32 v55, v131
v_add_f32 v61, v61, v58
v_mfma_f32_32x32x16_bf16 v[144:159], a[12:15], v[76:79], v[144:159]
v_exp_f32 v59, v163
v_add_f32 v60, v60, v55
v_cvt_pk_bf16_f32 v225, v54, v55
v_mfma_f32_32x32x16_bf16 v[176:191], a[12:15], v[108:111], v[176:191]
ds_read_b128 a[12:15], v62 offset:1536
v_exp_f32 v52, v132
v_add_f32 v61, v61, v59
v_cvt_pk_bf16_f32 v241, v58, v59
v_mfma_f32_32x32x16_bf16 v[144:159], a[16:19], v[80:83], v[144:159]
s_add_u32 s46, s46, 16384
v_exp_f32 v56, v164
v_add_f32 v60, v60, v52
v_mfma_f32_32x32x16_bf16 v[176:191], a[16:19], v[112:115], v[176:191]
ds_read_b128 a[16:19], v62 offset:2048
v_exp_f32 v53, v133
v_add_f32 v61, v61, v56
v_mfma_f32_32x32x16_bf16 v[144:159], a[20:23], v[84:87], v[144:159]
buffer_load_dwordx4 v7, s[4:7], s44 offen offset:1024 sc1 lds
v_exp_f32 v57, v165
v_add_f32 v60, v60, v53
v_cvt_pk_bf16_f32 v226, v52, v53
v_mfma_f32_32x32x16_bf16 v[176:191], a[20:23], v[116:119], v[176:191]
ds_read_b128 a[20:23], v62 offset:2560
v_exp_f32 v54, v134
v_add_f32 v61, v61, v57
v_cvt_pk_bf16_f32 v242, v56, v57
v_mfma_f32_32x32x16_bf16 v[144:159], a[24:27], v[88:91], v[144:159]
v_exp_f32 v58, v166
v_add_f32 v60, v60, v54
v_mfma_f32_32x32x16_bf16 v[176:191], a[24:27], v[120:123], v[176:191]
ds_read_b128 a[24:27], v62 offset:3072
v_exp_f32 v55, v135
v_add_f32 v61, v61, v58
v_mfma_f32_32x32x16_bf16 v[144:159], a[28:31], v[92:95], v[144:159]
v_exp_f32 v59, v167
v_add_f32 v60, v60, v55
v_cvt_pk_bf16_f32 v227, v54, v55
v_mfma_f32_32x32x16_bf16 v[176:191], a[28:31], v[124:127], v[176:191]
s_waitcnt lgkmcnt(7)
ds_read_b128 a[28:31], v62 offset:3584
v_exp_f32 v52, v136
v_add_f32 v61, v61, v59
v_cvt_pk_bf16_f32 v243, v58, v59
v_mfma_f32_32x32x16_bf16 a[64:79], a[32:35], v[232:235], a[64:79]
buffer_load_dwordx4 v7, s[4:7], s44 offen offset:2048 sc1 lds
v_exp_f32 v56, v168
v_add_f32 v60, v60, v52
v_exp_f32 v53, v137
v_mfma_f32_32x32x16_bf16 a[176:191], a[32:35], v[248:251], a[176:191]
ds_read_b128 a[32:35], v63 offset:0
v_add_f32 v61, v61, v56
v_exp_f32 v57, v169
v_add_f32 v60, v60, v53
v_cvt_pk_bf16_f32 v228, v52, v53
v_mfma_f32_32x32x16_bf16 a[80:95], a[36:39], v[232:235], a[80:95]
s_and_b32 s46, s46, 0xffff
v_exp_f32 v54, v138
v_add_f32 v61, v61, v57
v_cvt_pk_bf16_f32 v244, v56, v57
v_mfma_f32_32x32x16_bf16 a[208:223], a[36:39], v[248:251], a[208:223]
ds_read_b128 a[36:39], v63 offset:1024
v_exp_f32 v58, v170
v_add_f32 v60, v60, v54
v_mfma_f32_32x32x16_bf16 a[112:127], a[40:43], v[232:235], a[112:127]
v_exp_f32 v55, v139
v_add_f32 v61, v61, v58
v_mfma_f32_32x32x16_bf16 a[240:255], a[40:43], v[248:251], a[240:255]
ds_read_b128 a[40:43], v63 offset:2048
v_exp_f32 v59, v171
v_add_f32 v60, v60, v55
v_cvt_pk_bf16_f32 v229, v54, v55
v_mfma_f32_32x32x16_bf16 a[144:159], a[44:47], v[232:235], a[144:159]
buffer_load_dwordx4 v7, s[4:7], s44 offen offset:3072 sc1 lds
v_exp_f32 v52, v140
v_add_f32 v61, v61, v59
v_cvt_pk_bf16_f32 v245, v58, v59
v_mfma_f32_32x32x16_bf16 a[96:111], a[44:47], v[248:251], a[96:111]
ds_read_b128 a[44:47], v63 offset:3072
v_exp_f32 v56, v172
v_add_f32 v60, v60, v52
v_mfma_f32_32x32x16_bf16 a[64:79], a[48:51], v[236:239], a[64:79]
v_exp_f32 v53, v141
v_add_f32 v61, v61, v56
v_mfma_f32_32x32x16_bf16 a[176:191], a[48:51], v[252:255], a[176:191]
ds_read_b128 a[48:51], v63 offset:4096
v_exp_f32 v57, v173
v_add_f32 v60, v60, v53
v_cvt_pk_bf16_f32 v230, v52, v53
v_mfma_f32_32x32x16_bf16 a[80:95], a[52:55], v[236:239], a[80:95]
s_sub_u32 s55, s55, 1
v_exp_f32 v54, v142
v_add_f32 v61, v61, v57
v_cvt_pk_bf16_f32 v246, v56, v57
v_mfma_f32_32x32x16_bf16 a[208:223], a[52:55], v[252:255], a[208:223]
ds_read_b128 a[52:55], v63 offset:5120
v_exp_f32 v58, v174
v_add_f32 v60, v60, v54
v_mfma_f32_32x32x16_bf16 a[112:127], a[56:59], v[236:239], a[112:127]
v_exp_f32 v55, v143
v_add_f32 v61, v61, v58
v_mfma_f32_32x32x16_bf16 a[240:255], a[56:59], v[252:255], a[240:255]
ds_read_b128 a[56:59], v63 offset:6144
v_exp_f32 v59, v175
v_add_f32 v60, v60, v55
v_cvt_pk_bf16_f32 v231, v54, v55
v_mfma_f32_32x32x16_bf16 a[144:159], a[60:63], v[236:239], a[144:159]
v_add_f32 v61, v61, v59
v_cvt_pk_bf16_f32 v247, v58, v59
v_mfma_f32_32x32x16_bf16 a[96:111], a[60:63], v[252:255], a[96:111]
s_waitcnt lgkmcnt(7)
ds_read_b128 a[60:63], v63 offset:7168
s_cbranch_scc1 .Lev_d1
.LD0:
v_mfma_f32_32x32x16_bf16 v[128:143], a[0:3], v[64:67], v[192:207]
s_cmp_lt_u32 s44, s45
s_cselect_b32 m0, s46, s27
v_exp_f32 v52, v144
v_exp_f32 v56, v176
v_add_f32 v60, v60, v52
v_mfma_f32_32x32x16_bf16 v[160:175], a[0:3], v[96:99], v[208:223]
ds_read_b128 a[0:3], v62 offset:8192
v_exp_f32 v53, v145
v_add_f32 v61, v61, v56
v_mfma_f32_32x32x16_bf16 v[128:143], a[4:7], v[68:71], v[128:143]
v_exp_f32 v57, v177
v_add_f32 v60, v60, v53
v_cvt_pk_bf16_f32 v232, v52, v53
v_mfma_f32_32x32x16_bf16 v[160:175], a[4:7], v[100:103], v[160:175]
s_sub_u32 s52, s52, 1
s_cselect_b32 s52, 0, s52
s_cselect_b32 s53, 0, 0x8000
ds_read_b128 a[4:7], v62 offset:8704
v_exp_f32 v54, v146
v_mfma_f32_32x32x16_bf16 v[128:143], a[8:11], v[72:75], v[128:143]
buffer_load_dwordx4 v7, s[8:11], s44 offen offset:0 sc1 lds
v_add_f32 v61, v61, v57
v_cvt_pk_bf16_f32 v248, v56, v57
v_exp_f32 v58, v178
v_add_f32 v60, v60, v54
v_mfma_f32_32x32x16_bf16 v[160:175], a[8:11], v[104:107], v[160:175]
ds_read_b128 a[8:11], v62 offset:9216
v_exp_f32 v55, v147
v_add_f32 v61, v61, v58
v_mfma_f32_32x32x16_bf16 v[128:143], a[12:15], v[76:79], v[128:143]
v_exp_f32 v59, v179
v_add_f32 v60, v60, v55
v_cvt_pk_bf16_f32 v233, v54, v55
v_mfma_f32_32x32x16_bf16 v[160:175], a[12:15], v[108:111], v[160:175]
ds_read_b128 a[12:15], v62 offset:9728
v_exp_f32 v52, v148
v_add_f32 v61, v61, v59
v_cvt_pk_bf16_f32 v249, v58, v59
v_mfma_f32_32x32x16_bf16 v[128:143], a[16:19], v[80:83], v[128:143]
s_xor_b32 s50, s46, 0x18000
v_exp_f32 v56, v180
v_add_f32 v60, v60, v52
v_mfma_f32_32x32x16_bf16 v[160:175], a[16:19], v[112:115], v[160:175]
ds_read_b128 a[16:19], v62 offset:10240
v_exp_f32 v53, v149
v_add_f32 v61, v61, v56
v_mfma_f32_32x32x16_bf16 v[128:143], a[20:23], v[84:87], v[128:143]
buffer_load_dwordx4 v7, s[8:11], s44 offen offset:1024 sc1 lds
v_exp_f32 v57, v181
v_add_f32 v60, v60, v53
v_cvt_pk_bf16_f32 v234, v52, v53
v_mfma_f32_32x32x16_bf16 v[160:175], a[20:23], v[116:119], v[160:175]
ds_read_b128 a[20:23], v62 offset:10752
v_exp_f32 v54, v150
v_add_f32 v61, v61, v57
v_cvt_pk_bf16_f32 v250, v56, v57
v_mfma_f32_32x32x16_bf16 v[128:143], a[24:27], v[88:91], v[128:143]
v_exp_f32 v58, v182
v_add_f32 v60, v60, v54
v_mfma_f32_32x32x16_bf16 v[160:175], a[24:27], v[120:123], v[160:175]
ds_read_b128 a[24:27], v62 offset:11264
v_exp_f32 v55, v151
v_add_f32 v61, v61, v58
v_mfma_f32_32x32x16_bf16 v[128:143], a[28:31], v[92:95], v[128:143]
v_exp_f32 v59, v183
v_add_f32 v60, v60, v55
v_cvt_pk_bf16_f32 v235, v54, v55
v_mfma_f32_32x32x16_bf16 v[160:175], a[28:31], v[124:127], v[160:175]
s_waitcnt lgkmcnt(7)
ds_read_b128 a[28:31], v62 offset:11776
v_exp_f32 v52, v152
v_add_f32 v61, v61, v59
v_cvt_pk_bf16_f32 v251, v58, v59
v_mfma_f32_32x32x16_bf16 a[64:79], a[32:35], v[224:227], a[64:79]
buffer_load_dwordx4 v7, s[8:11], s44 offen offset:2048 sc1 lds
v_exp_f32 v56, v184
v_add_f32 v60, v60, v52
v_exp_f32 v53, v153
v_mfma_f32_32x32x16_bf16 a[176:191], a[32:35], v[240:243], a[176:191]
ds_read_b128 a[32:35], v63 offset:8192
v_add_u32 v62, s46, v32
v_add_f32 v61, v61, v56
v_exp_f32 v57, v185
v_add_f32 v60, v60, v53
v_mfma_f32_32x32x16_bf16 a[80:95], a[36:39], v[224:227], a[80:95]
v_cvt_pk_bf16_f32 v236, v52, v53
v_exp_f32 v54, v154
v_add_f32 v61, v61, v57
v_cvt_pk_bf16_f32 v252, v56, v57
v_mfma_f32_32x32x16_bf16 a[208:223], a[36:39], v[240:243], a[208:223]
ds_read_b128 a[36:39], v63 offset:9216
v_exp_f32 v58, v186
v_add_f32 v60, v60, v54
v_mfma_f32_32x32x16_bf16 a[112:127], a[40:43], v[224:227], a[112:127]
v_exp_f32 v55, v155
v_add_f32 v61, v61, v58
v_mfma_f32_32x32x16_bf16 a[240:255], a[40:43], v[240:243], a[240:255]
ds_read_b128 a[40:43], v63 offset:10240
v_exp_f32 v59, v187
v_add_f32 v60, v60, v55
v_cvt_pk_bf16_f32 v237, v54, v55
v_mfma_f32_32x32x16_bf16 a[144:159], a[44:47], v[224:227], a[144:159]
buffer_load_dwordx4 v7, s[8:11], s44 offen offset:3072 sc1 lds
v_exp_f32 v52, v156
v_add_f32 v61, v61, v59
v_cvt_pk_bf16_f32 v253, v58, v59
v_mfma_f32_32x32x16_bf16 a[96:111], a[44:47], v[240:243], a[96:111]
ds_read_b128 a[44:47], v63 offset:11264
v_exp_f32 v56, v188
v_add_f32 v60, v60, v52
v_mfma_f32_32x32x16_bf16 a[64:79], a[48:51], v[228:231], a[64:79]
s_add_u32 s44, s44, 16384
global_load_dword v35, v[2:3], off
v_exp_f32 v53, v157
v_add_f32 v61, v61, v56
v_mfma_f32_32x32x16_bf16 a[176:191], a[48:51], v[244:247], a[176:191]
ds_read_b128 a[48:51], v63 offset:12288
v_add_co_u32 v2, vcc, s53, v2
v_exp_f32 v57, v189
v_add_f32 v60, v60, v53
v_cvt_pk_bf16_f32 v238, v52, v53
v_mfma_f32_32x32x16_bf16 a[80:95], a[52:55], v[228:231], a[80:95]
s_sub_u32 s55, s55, 1
v_exp_f32 v54, v158
v_add_f32 v61, v61, v57
v_cvt_pk_bf16_f32 v254, v56, v57
v_mfma_f32_32x32x16_bf16 a[208:223], a[52:55], v[244:247], a[208:223]
ds_read_b128 a[52:55], v63 offset:13312
v_addc_co_u32 v3, vcc, 0, v3, vcc
v_exp_f32 v58, v190
v_add_f32 v60, v60, v54
v_mfma_f32_32x32x16_bf16 a[112:127], a[56:59], v[228:231], a[112:127]
v_exp_f32 v55, v159
v_add_f32 v61, v61, v58
v_mfma_f32_32x32x16_bf16 a[240:255], a[56:59], v[244:247], a[240:255]
ds_read_b128 a[56:59], v63 offset:14336
v_exp_f32 v59, v191
v_add_f32 v60, v60, v55
v_cvt_pk_bf16_f32 v239, v54, v55
v_mfma_f32_32x32x16_bf16 a[144:159], a[60:63], v[228:231], a[144:159]
v_add_f32 v61, v61, v59
v_cvt_pk_bf16_f32 v255, v58, v59
v_mfma_f32_32x32x16_bf16 a[96:111], a[60:63], v[244:247], a[96:111]
s_waitcnt vmcnt(10) lgkmcnt(7)
s_barrier
ds_read_b128 a[60:63], v63 offset:15360
s_cbranch_scc0 .LD1
s_cmp_eq_u32 s43, s42
s_cbranch_scc1 .LT1
s_swappc_b64 s[30:31], s[58:59]
.Lev_d0_r:
s_branch .LD1
.LT0:
v_mfma_f32_32x32x16_bf16 v[128:143], a[0:3], v[64:67], v[192:207]
s_cmp_lt_u32 s44, s45
s_cselect_b32 m0, s46, s27
v_exp_f32 v52, v144
v_exp_f32 v56, v176
v_add_f32 v60, v60, v52
v_mfma_f32_32x32x16_bf16 v[160:175], a[0:3], v[96:99], v[36:51]
s_add_u32 s43, s42, 4
s_mov_b32 s42, s56
ds_read_b128 a[0:3], v62 offset:8192
v_exp_f32 v53, v145
v_add_f32 v61, v61, v56
v_mfma_f32_32x32x16_bf16 v[128:143], a[4:7], v[68:71], v[128:143]
s_max_u32 s55, s57, s43
s_min_u32 s55, s55, s42
s_sub_u32 s93, s55, s43
v_exp_f32 v57, v177
v_add_f32 v60, v60, v53
v_cvt_pk_bf16_f32 v232, v52, v53
v_mfma_f32_32x32x16_bf16 v[160:175], a[4:7], v[100:103], v[160:175]
s_mov_b32 s43, s55
ds_read_b128 a[4:7], v62 offset:8704
v_exp_f32 v54, v146
v_add_f32 v61, v61, v57
v_cvt_pk_bf16_f32 v248, v56, v57
v_mfma_f32_32x32x16_bf16 v[128:143], a[8:11], v[72:75], v[128:143]
s_mov_b32 s55, s93
buffer_load_dwordx4 v7, s[8:11], s44 offen offset:0 sc1 lds
v_exp_f32 v58, v178
v_add_f32 v60, v60, v54
v_cmp_gt_i32 s[32:33], 0, v8
v_mfma_f32_32x32x16_bf16 v[160:175], a[8:11], v[104:107], v[160:175]
ds_read_b128 a[8:11], v62 offset:9216
v_exp_f32 v55, v147
v_add_f32 v61, v61, v58
v_nop
v_cmp_gt_i32 s[34:35], 1, v8
v_mfma_f32_32x32x16_bf16 v[128:143], a[12:15], v[76:79], v[128:143]
v_exp_f32 v59, v179
v_add_f32 v60, v60, v55
v_cvt_pk_bf16_f32 v233, v54, v55
v_cndmask_b32 v16, v192, v34, s[32:33]
v_cmp_gt_i32 s[32:33], 2, v8
v_mfma_f32_32x32x16_bf16 v[160:175], a[12:15], v[108:111], v[160:175]
ds_read_b128 a[12:15], v62 offset:9728
v_exp_f32 v52, v148
v_add_f32 v61, v61, v59
v_cvt_pk_bf16_f32 v249, v58, v59
v_cndmask_b32 v17, v193, v34, s[34:35]
v_mfma_f32_32x32x16_bf16 v[128:143], a[16:19], v[80:83], v[128:143]
s_xor_b32 s50, s46, 0x18000
v_exp_f32 v56, v180
v_add_f32 v60, v60, v52
v_cmp_gt_i32 s[34:35], 3, v8
v_cndmask_b32 v18, v194, v34, s[32:33]
v_cmp_gt_i32 s[32:33], 8, v8
v_mfma_f32_32x32x16_bf16 v[160:175], a[16:19], v[112:115], v[160:175]
ds_read_b128 a[16:19], v62 offset:10240
v_exp_f32 v53, v149
v_add_f32 v61, v61, v56
v_cndmask_b32 v19, v195, v34, s[34:35]
v_cmp_gt_i32 s[34:35], 9, v8
v_mfma_f32_32x32x16_bf16 v[128:143], a[20:23], v[84:87], v[128:143]
buffer_load_dwordx4 v7, s[8:11], s44 offen offset:1024 sc1 lds
v_exp_f32 v57, v181
v_add_f32 v60, v60, v53
v_cvt_pk_bf16_f32 v234, v52, v53
v_cndmask_b32 v20, v196, v34, s[32:33]
v_mfma_f32_32x32x16_bf16 v[160:175], a[20:23], v[116:119], v[160:175]
ds_read_b128 a[20:23], v62 offset:10752
v_exp_f32 v54, v150
v_add_f32 v61, v61, v57
v_cvt_pk_bf16_f32 v250, v56, v57
v_cmp_gt_i32 s[32:33], 10, v8
v_mfma_f32_32x32x16_bf16 v[128:143], a[24:27], v[88:91], v[128:143]
v_exp_f32 v58, v182
v_add_f32 v60, v60, v54
v_cndmask_b32 v21, v197, v34, s[34:35]
v_cmp_gt_i32 s[34:35], 11, v8
v_cndmask_b32 v22, v198, v34, s[32:33]
v_mfma_f32_32x32x16_bf16 v[160:175], a[24:27], v[120:123], v[160:175]
ds_read_b128 a[24:27], v62 offset:11264
v_exp_f32 v55, v151
v_add_f32 v61, v61, v58
v_cmp_gt_i32 s[32:33], 16, v8
v_cndmask_b32 v23, v199, v34, s[34:35]
v_mfma_f32_32x32x16_bf16 v[128:143], a[28:31], v[92:95], v[128:143]
v_exp_f32 v59, v183
v_add_f32 v60, v60, v55
v_cvt_pk_bf16_f32 v235, v54, v55
v_cmp_gt_i32 s[34:35], 17, v8
v_cndmask_b32 v24, v200, v34, s[32:33]
v_mfma_f32_32x32x16_bf16 v[160:175], a[28:31], v[124:127], v[160:175]
s_waitcnt lgkmcnt(7)
ds_read_b128 a[28:31], v62 offset:11776
v_exp_f32 v52, v152
v_add_f32 v61, v61, v59
v_cvt_pk_bf16_f32 v251, v58, v59
v_mfma_f32_32x32x16_bf16 a[64:79], a[32:35], v[224:227], a[64:79]
buffer_load_dwordx4 v7, s[8:11], s44 offen offset:2048 sc1 lds
v_exp_f32 v56, v184
v_add_f32 v60, v60, v52
v_exp_f32 v53, v153
v_mfma_f32_32x32x16_bf16 a[176:191], a[32:35], v[240:243], a[176:191]
ds_read_b128 a[32:35], v63 offset:8192
v_add_u32 v62, s46, v32
v_add_f32 v61, v61, v56
v_exp_f32 v57, v185
v_add_f32 v60, v60, v53
v_mfma_f32_32x32x16_bf16 a[80:95], a[36:39], v[224:227], a[80:95]
v_cvt_pk_bf16_f32 v236, v52, v53
v_exp_f32 v54, v154
v_add_f32 v61, v61, v57
v_cvt_pk_bf16_f32 v252, v56, v57
v_cmp_gt_i32 s[32:33], 18, v8
v_mfma_f32_32x32x16_bf16 a[208:223], a[36:39], v[240:243], a[208:223]
ds_read_b128 a[36:39], v63 offset:9216
v_exp_f32 v58, v186
v_add_f32 v60, v60, v54
v_cndmask_b32 v25, v201, v34, s[34:35]
v_cmp_gt_i32 s[34:35], 19, v8
v_mfma_f32_32x32x16_bf16 a[112:127], a[40:43], v[224:227], a[112:127]
v_exp_f32 v55, v155
v_add_f32 v61, v61, v58
v_cndmask_b32 v26, v202, v34, s[32:33]
v_cmp_gt_i32 s[32:33], 24, v8
v_cndmask_b32 v27, v203, v34, s[34:35]
v_mfma_f32_32x32x16_bf16 a[240:255], a[40:43], v[240:243], a[240:255]
ds_read_b128 a[40:43], v63 offset:10240
v_exp_f32 v59, v187
v_add_f32 v60, v60, v55
v_cvt_pk_bf16_f32 v237, v54, v55
v_cmp_gt_i32 s[34:35], 25, v8
v_mfma_f32_32x32x16_bf16 a[144:159], a[44:47], v[224:227], a[144:159]
buffer_load_dwordx4 v7, s[8:11], s44 offen offset:3072 sc1 lds
v_exp_f32 v52, v156
v_add_f32 v61, v61, v59
v_cvt_pk_bf16_f32 v253, v58, v59
v_cndmask_b32 v28, v204, v34, s[32:33]
v_mfma_f32_32x32x16_bf16 a[96:111], a[44:47], v[240:243], a[96:111]
ds_read_b128 a[44:47], v63 offset:11264
v_exp_f32 v56, v188
v_add_f32 v60, v60, v52
v_cmp_gt_i32 s[32:33], 26, v8
v_cndmask_b32 v29, v205, v34, s[34:35]
v_mfma_f32_32x32x16_bf16 a[64:79], a[48:51], v[228:231], a[64:79]
s_add_u32 s44, s44, 16384
v_exp_f32 v53, v157
v_add_f32 v61, v61, v56
v_cmp_gt_i32 s[34:35], 27, v8
v_cndmask_b32 v30, v206, v34, s[32:33]
v_nop
v_mfma_f32_32x32x16_bf16 a[176:191], a[48:51], v[244:247], a[176:191]
ds_read_b128 a[48:51], v63 offset:12288
v_exp_f32 v57, v189
v_add_f32 v60, v60, v53
v_cvt_pk_bf16_f32 v238, v52, v53
v_cndmask_b32 v31, v207, v34, s[34:35]
v_mfma_f32_32x32x16_bf16 a[80:95], a[52:55], v[228:231], a[80:95]
v_exp_f32 v54, v158
v_add_f32 v61, v61, v57
v_cvt_pk_bf16_f32 v254, v56, v57
v_mfma_f32_32x32x16_bf16 a[208:223], a[52:55], v[244:247], a[208:223]
ds_read_b128 a[52:55], v63 offset:13312
v_exp_f32 v58, v190
v_add_f32 v60, v60, v54
v_mfma_f32_32x32x16_bf16 a[112:127], a[56:59], v[228:231], a[112:127]
v_exp_f32 v55, v159
v_add_f32 v61, v61, v58
v_mfma_f32_32x32x16_bf16 a[240:255], a[56:59], v[244:247], a[240:255]
ds_read_b128 a[56:59], v63 offset:14336
v_exp_f32 v59, v191
v_add_f32 v60, v60, v55
v_cvt_pk_bf16_f32 v239, v54, v55
v_mfma_f32_32x32x16_bf16 a[144:159], a[60:63], v[228:231], a[144:159]
v_add_f32 v61, v61, v59
v_cvt_pk_bf16_f32 v255, v58, v59
v_mfma_f32_32x32x16_bf16 a[96:111], a[60:63], v[244:247], a[96:111]
s_waitcnt vmcnt(9) lgkmcnt(7)
s_barrier
ds_read_b128 a[60:63], v63 offset:15360
v_mfma_f32_32x32x16_bf16 v[144:159], a[0:3], v[64:67], v[192:207]
s_cmp_lt_u32 s44, s54
s_cselect_b32 m0, s50, s27
ds_read_b128 a[0:3], v62 offset:0
v_add_u32 v63, s49, v33
v_exp_f32 v52, v128
v_mfma_f32_32x32x16_bf16 v[144:159], a[4:7], v[68:71], v[144:159]
ds_read_b128 a[4:7], v62 offset:512
v_exp_f32 v56, v160
v_add_f32 v60, v60, v52
v_exp_f32 v53, v129
v_mfma_f32_32x32x16_bf16 v[144:159], a[8:11], v[72:75], v[144:159]
ds_read_b128 a[8:11], v62 offset:1024
v_add_f32 v61, v61, v56
v_exp_f32 v57, v161
v_add_f32 v60, v60, v53
v_cvt_pk_bf16_f32 v224, v52, v53
v_mfma_f32_32x32x16_bf16 v[144:159], a[12:15], v[76:79], v[144:159]
s_mov_b32 s49, s46
buffer_load_dwordx4 v7, s[4:7], s44 offen offset:0 sc1 lds
ds_read_b128 a[12:15], v62 offset:1536
v_exp_f32 v54, v130
v_mfma_f32_32x32x16_bf16 v[144:159], a[16:19], v[80:83], v[144:159]
ds_read_b128 a[16:19], v62 offset:2048
v_add_f32 v61, v61, v57
v_cvt_pk_bf16_f32 v240, v56, v57
v_exp_f32 v58, v162
v_add_f32 v60, v60, v54
v_mfma_f32_32x32x16_bf16 v[144:159], a[20:23], v[84:87], v[144:159]
ds_read_b128 a[20:23], v62 offset:2560
v_exp_f32 v55, v131
v_add_f32 v61, v61, v58
v_exp_f32 v59, v163
v_mfma_f32_32x32x16_bf16 v[144:159], a[24:27], v[88:91], v[144:159]
s_add_u32 s46, s46, 16384
ds_read_b128 a[24:27], v62 offset:3072
v_add_f32 v60, v60, v55
v_cvt_pk_bf16_f32 v225, v54, v55
v_exp_f32 v52, v132
v_mfma_f32_32x32x16_bf16 v[144:159], a[28:31], v[92:95], v[144:159]
s_waitcnt lgkmcnt(7)
buffer_load_dwordx4 v7, s[4:7], s44 offen offset:1024 sc1 lds
ds_read_b128 a[28:31], v62 offset:3584
v_add_f32 v61, v61, v59
v_cvt_pk_bf16_f32 v241, v58, v59
v_mfma_f32_32x32x16_bf16 a[64:79], a[32:35], v[232:235], a[64:79]
v_exp_f32 v56, v164
v_add_f32 v60, v60, v52
v_exp_f32 v53, v133
v_add_f32 v61, v61, v56
v_mfma_f32_32x32x16_bf16 a[176:191], a[32:35], v[248:251], a[176:191]
s_and_b32 s36, s86, 1
ds_read_b128 a[32:35], v63 offset:0
v_exp_f32 v57, v165
v_add_f32 v60, v60, v53
v_cvt_pk_bf16_f32 v226, v52, v53
v_mfma_f32_32x32x16_bf16 a[80:95], a[36:39], v[232:235], a[80:95]
v_exp_f32 v54, v134
v_add_f32 v61, v61, v57
v_cvt_pk_bf16_f32 v242, v56, v57
v_exp_f32 v58, v166
v_mfma_f32_32x32x16_bf16 a[208:223], a[36:39], v[248:251], a[208:223]
s_lshl_b32 s36, s36, 3
ds_read_b128 a[36:39], v63 offset:1024
v_add_f32 v60, v60, v54
v_exp_f32 v55, v135
v_add_f32 v61, v61, v58
v_mfma_f32_32x32x16_bf16 a[112:127], a[40:43], v[232:235], a[112:127]
s_and_b32 s28, s87, 7
buffer_load_dwordx4 v7, s[4:7], s44 offen offset:2048 sc1 lds
v_exp_f32 v59, v167
v_add_f32 v60, v60, v55
v_cvt_pk_bf16_f32 v227, v54, v55
.Lpad11:
s_nop 0
v_mfma_f32_32x32x16_bf16 a[240:255], a[40:43], v[248:251], a[240:255]
s_and_b32 s46, s46, 0xffff
ds_read_b128 a[40:43], v63 offset:2048
v_exp_f32 v52, v136
v_add_f32 v61, v61, v59
v_cvt_pk_bf16_f32 v243, v58, v59
v_mfma_f32_32x32x16_bf16 a[144:159], a[44:47], v[232:235], a[144:159]
v_exp_f32 v56, v168
v_add_f32 v60, v60, v52
v_exp_f32 v53, v137
v_add_f32 v61, v61, v56
v_mfma_f32_32x32x16_bf16 a[96:111], a[44:47], v[248:251], a[96:111]
s_or_b32 s36, s36, s28
ds_read_b128 a[44:47], v63 offset:3072
v_exp_f32 v57, v169
v_add_f32 v60, v60, v53
v_cvt_pk_bf16_f32 v228, v52, v53
v_mfma_f32_32x32x16_bf16 a[64:79], a[48:51], v[236:239], a[64:79]
s_lshr_b32 s37, s86, 1
buffer_load_dwordx4 v7, s[4:7], s44 offen offset:3072 sc1 lds
v_exp_f32 v54, v138
v_add_f32 v61, v61, v57
v_cvt_pk_bf16_f32 v244, v56, v57
v_mfma_f32_32x32x16_bf16 a[176:191], a[48:51], v[252:255], a[176:191]
ds_read_b128 a[48:51], v63 offset:4096
v_exp_f32 v58, v170
v_add_f32 v60, v60, v54
v_exp_f32 v55, v139
v_mfma_f32_32x32x16_bf16 a[80:95], a[52:55], v[236:239], a[80:95]
s_lshl_b32 s37, s37, 2
s_lshr_b32 s28, s87, 3
v_add_f32 v61, v61, v58
v_exp_f32 v59, v171
v_add_f32 v60, v60, v55
v_cvt_pk_bf16_f32 v229, v54, v55
v_mfma_f32_32x32x16_bf16 a[208:223], a[52:55], v[252:255], a[208:223]
s_or_b32 s37, s37, s28
ds_read_b128 a[52:55], v63 offset:5120
v_exp_f32 v52, v140
v_add_f32 v61, v61, v59
v_cvt_pk_bf16_f32 v245, v58, v59
v_mfma_f32_32x32x16_bf16 a[112:127], a[56:59], v[236:239], a[112:127]
v_exp_f32 v56, v172
v_add_f32 v60, v60, v52
v_exp_f32 v53, v141
v_add_f32 v61, v61, v56
v_mfma_f32_32x32x16_bf16 a[240:255], a[56:59], v[252:255], a[240:255]
s_lshl_b32 s24, s36, 18
ds_read_b128 a[56:59], v63 offset:6144
v_exp_f32 v57, v173
v_add_f32 v60, v60, v53
v_cvt_pk_bf16_f32 v230, v52, v53
v_mfma_f32_32x32x16_bf16 a[144:159], a[60:63], v[236:239], a[144:159]
s_lshl_b32 s28, s3, 16
s_add_u32 s24, s24, s28
s_mov_b32 s25, 0
s_mul_i32 s26, s3, 98304
s_lshl_b32 s28, s37, 7
s_add_u32 s26, s26, s28
s_add_u32 s47, s26, 4096
s_mul_i32 s48, s3, 7168
s_mov_b32 s12, s64
s_and_b32 s13, s65, 0xffff
s_mov_b32 s14, 0x400000
s_mov_b32 s16, s66
s_and_b32 s17, s67, 0xffff
s_mov_b32 s18, 0x600000
s_lshl_b32 s29, s36, 18
s_lshl_b32 s28, s3, 13
s_add_u32 s29, s29, s28
s_sub_u32 s29, s29, 34816
s_cmp_eq_u32 s36, 0
s_cselect_b32 s29, 0x7f000000, s29
s_lshl_b32 s32, s3, 2
s_and_b32 s33, s3, 1
s_lshl_b32 s33, s33, 1
s_lshr_b32 s34, s3, 1
s_lshl_b32 s35, s34, 13
s_add_u32 s35, s35, 65536
s_mov_b32 s38, 131072
s_lshl_b32 s28, s88, 13
s_add_u32 s28, s28, 33554432
s_add_u32 s20, s84, s28
s_addc_u32 s21, s85, 0
s_and_b32 s21, s21, 0xffff
s_mov_b32 s22, 16777216
s_sub_u32 s40, 15, s89
s_lshl_b32 s40, s40, 2
s_add_u32 s40, s40, s3
s_lshl_b32 s40, s40, 18
s_lshl_b32 s41, s89, 2
s_add_u32 s41, s41, s94
s_lshl_b32 s41, s41, 18
s_mov_b32 s28, 1602224128
v_exp_f32 v54, v142
v_add_f32 v61, v61, v57
v_cvt_pk_bf16_f32 v246, v56, v57
v_exp_f32 v58, v174
v_add_f32 v60, v60, v54
v_exp_f32 v55, v143
v_add_f32 v61, v61, v58
v_exp_f32 v59, v175
v_add_f32 v60, v60, v55
v_cvt_pk_bf16_f32 v231, v54, v55
v_add_f32 v61, v61, v59
v_cvt_pk_bf16_f32 v247, v58, v59
v_mfma_f32_32x32x16_bf16 a[96:111], a[60:63], v[252:255], a[96:111]
s_waitcnt lgkmcnt(7)
ds_read_b128 a[60:63], v63 offset:7168
v_mfma_f32_32x32x16_bf16 v[128:143], a[0:3], v[64:67], v[192:207]
s_cmp_lt_u32 s44, s45
s_cselect_b32 m0, s46, s27
ds_read_b128 a[0:3], v62 offset:8192
v_exp_f32 v52, v144
v_lshrrev_b32 v56, 3, v1
v_mfma_f32_32x32x16_bf16 v[128:143], a[4:7], v[68:71], v[128:143]
ds_read_b128 a[4:7], v62 offset:8704
v_exp_f32 v53, v145
v_add_f32 v60, v60, v52
v_and_b32 v57, 7, v1
v_xor_b32 v58, v57, v56
v_mfma_f32_32x32x16_bf16 v[128:143], a[8:11], v[72:75], v[128:143]
ds_read_b128 a[8:11], v62 offset:9216
v_exp_f32 v54, v146
v_add_f32 v60, v60, v53
v_cvt_pk_bf16_f32 v232, v52, v53
v_lshlrev_b32 v58, 4, v58
v_mfma_f32_32x32x16_bf16 v[128:143], a[12:15], v[76:79], v[128:143]
buffer_load_dwordx4 v7, s[8:11], s44 offen offset:0 sc1 lds
ds_read_b128 a[12:15], v62 offset:9728
v_lshl_add_u32 v36, v56, 11, v58
v_add_u32 v37, 15360, v36
v_add_u32 v38, 30720, v36
v_mfma_f32_32x32x16_bf16 v[128:143], a[16:19], v[80:83], v[128:143]
ds_read_b128 a[16:19], v62 offset:10240
v_exp_f32 v55, v147
v_add_f32 v60, v60, v54
v_add_u32 v39, 46080, v36
v_add_u32 v58, s32, v56
v_mfma_f32_32x32x16_bf16 v[128:143], a[20:23], v[84:87], v[128:143]
ds_read_b128 a[20:23], v62 offset:10752
v_exp_f32 v52, v148
v_add_f32 v60, v60, v55
v_cvt_pk_bf16_f32 v233, v54, v55
v_xor_b32 v58, v58, v57
v_mfma_f32_32x32x16_bf16 v[128:143], a[24:27], v[88:91], v[128:143]
s_xor_b32 s50, s46, 0x18000
ds_read_b128 a[24:27], v62 offset:11264
v_exp_f32 v53, v149
v_add_f32 v60, v60, v52
v_and_b32 v58, 7, v58
v_mfma_f32_32x32x16_bf16 v[128:143], a[28:31], v[92:95], v[128:143]
s_waitcnt lgkmcnt(7)
buffer_load_dwordx4 v7, s[8:11], s44 offen offset:1024 sc1 lds
ds_read_b128 a[28:31], v62 offset:11776
v_lshlrev_b32 v58, 4, v58
v_lshl_add_u32 v58, v56, 11, v58
v_mfma_f32_32x32x16_bf16 a[64:79], a[32:35], v[224:227], a[64:79]
v_exp_f32 v54, v150
v_add_f32 v60, v60, v53
v_cvt_pk_bf16_f32 v234, v52, v53
v_add_u32 v58, s29, v58
v_and_b32 v59, 32, v1
v_mfma_f32_32x32x16_bf16 a[176:191], a[32:35], v[240:243], a[176:191]
ds_read_b128 a[32:35], v63 offset:8192
v_add_u32 v62, s46, v32
v_exp_f32 v55, v151
v_add_f32 v60, v60, v54
v_lshlrev_b32 v59, 25, v59
v_mfma_f32_32x32x16_bf16 a[80:95], a[36:39], v[224:227], a[80:95]
v_exp_f32 v52, v152
v_add_f32 v60, v60, v55
v_cvt_pk_bf16_f32 v235, v54, v55
v_or_b32 v42, v59, v58
v_bfe_u32 v58, v1, 4, 1
v_mfma_f32_32x32x16_bf16 a[208:223], a[36:39], v[240:243], a[208:223]
ds_read_b128 a[36:39], v63 offset:9216
v_lshlrev_b32 v58, 1, v58
v_xor_b32 v58, v58, v57
v_lshlrev_b32 v58, 4, v58
v_mul_u32_u24 v59, 6144, v56
v_add_u32 v40, v59, v58
v_mfma_f32_32x32x16_bf16 a[112:127], a[40:43], v[224:227], a[112:127]
buffer_load_dwordx4 v7, s[8:11], s44 offen offset:2048 sc1 lds
v_exp_f32 v53, v153
v_add_f32 v60, v60, v52
v_xor_b32 v41, 64, v40
v_add_u32 v41, 48128, v41
v_mfma_f32_32x32x16_bf16 a[240:255], a[40:43], v[240:243], a[240:255]
ds_read_b128 a[40:43], v63 offset:10240
v_exp_f32 v54, v154
v_add_f32 v60, v60, v53
v_cvt_pk_bf16_f32 v236, v52, v53
v_and_b32 v56, 15, v1
v_mfma_f32_32x32x16_bf16 a[144:159], a[44:47], v[224:227], a[144:159]
v_exp_f32 v55, v155
v_add_f32 v60, v60, v54
v_lshrrev_b32 v57, 4, v1
v_and_b32 v58, 7, v56
v_xor_b32 v58, v58, v57
v_mfma_f32_32x32x16_bf16 a[96:111], a[44:47], v[240:243], a[96:111]
ds_read_b128 a[44:47], v63 offset:11264
v_lshlrev_b32 v58, 4, v58
v_lshl_add_u32 v43, v56, 7, v58
v_add_u32 v43, s35, v43
v_xor_b32 v44, 64, v43
v_and_b32 v56, 15, v1
v_mfma_f32_32x32x16_bf16 a[64:79], a[48:51], v[228:231], a[64:79]
buffer_load_dwordx4 v7, s[8:11], s44 offen offset:3072 sc1 lds
v_exp_f32 v52, v156
v_add_f32 v60, v60, v55
v_cvt_pk_bf16_f32 v237, v54, v55
v_lshrrev_b32 v57, 2, v56
v_mfma_f32_32x32x16_bf16 a[176:191], a[48:51], v[244:247], a[176:191]
ds_read_b128 a[48:51], v63 offset:12288
v_exp_f32 v53, v157
v_add_f32 v60, v60, v52
v_mul_u32_u24 v57, 6656, v57
v_add_u32 v57, 71680, v57
v_mfma_f32_32x32x16_bf16 a[80:95], a[52:55], v[228:231], a[80:95]
s_add_u32 s44, s44, 16384
v_exp_f32 v54, v158
v_add_f32 v60, v60, v53
v_cvt_pk_bf16_f32 v238, v52, v53
v_mov_b32 v58, -2048
v_mov_b32 v59, s34
v_mfma_f32_32x32x16_bf16 a[208:223], a[52:55], v[244:247], a[208:223]
ds_read_b128 a[52:55], v63 offset:13312
v_cmp_eq_u32 vcc, 0, v59
v_nop
v_nop
v_cndmask_b32 v57, v58, v57, vcc
v_add_u32 v45, v57, v43
v_mfma_f32_32x32x16_bf16 a[112:127], a[56:59], v[228:231], a[112:127]
v_exp_f32 v55, v159
v_add_f32 v60, v60, v54
v_add_u32 v46, v57, v44
v_bfe_u32 v56, v1, 2, 2
v_bfe_u32 v57, v1, 4, 1
v_mfma_f32_32x32x16_bf16 a[240:255], a[56:59], v[244:247], a[240:255]
ds_read_b128 a[56:59], v63 offset:14336
v_add_f32 v60, v60, v55
v_cvt_pk_bf16_f32 v239, v54, v55
v_lshl_add_u32 v58, v57, 3, v56
v_lshlrev_b32 v58, 7, v58
v_lshrrev_b32 v59, 5, v1
v_mfma_f32_32x32x16_bf16 a[144:159], a[60:63], v[228:231], a[144:159]
v_mul_u32_u24 v59, 7168, v59
v_add3_u32 v58, v58, v59, s38
v_lshrrev_b32 v56, 1, v56
v_lshl_or_b32 v56, v57, 1, v56
v_xor_b32 v56, s33, v56
v_lshl_add_u32 v58, v56, 5, v58
v_and_b32 v56, 3, v1
v_lshl_add_u32 v47, v56, 3, v58
v_xor_b32 v48, 32, v47
v_lshrrev_b32 v57, 1, v56
v_and_b32 v56, 1, v56
v_lshl_or_b32 v56, v56, 1, v57
v_lshl_add_u32 v49, v56, 3, v58
v_xor_b32 v50, 32, v49
v_mov_b32 v8, v61
v_add_u32 v51, 4096, v7
v_nop
v_permlane32_swap_b32 v61, v8
v_add_f32 v61, v61, v8
v_cmp_nge_f32 s[52:53], s28, v61
v_mfma_f32_32x32x16_bf16 a[96:111], a[60:63], v[244:247], a[96:111]
s_waitcnt vmcnt(8) lgkmcnt(7)
s_barrier
ds_read_b128 a[60:63], v63 offset:15360
s_cmp_le_u32 s94, 3
s_cbranch_scc1 .LTf0
.LTc0:
s_cmp_lt_i32 s55, 0
s_cbranch_scc1 .LE1
s_branch .LS1
.LTf0:
s_cmp_lg_u64 s[52:53], 0
s_cbranch_scc1 .LTc0
s_getpc_b64 s[30:31]
.LTp0:
s_sub_u32 s30, s30, .LTp0-.LTc0
s_subb_u32 s31, s31, 0
s_branch .Lfb
.LT1:
v_mfma_f32_32x32x16_bf16 v[144:159], a[0:3], v[64:67], v[192:207]
s_cmp_lt_u32 s44, s54
s_cselect_b32 m0, s50, s27
v_add_u32 v63, s49, v33
v_exp_f32 v52, v128
v_exp_f32 v56, v160
v_mfma_f32_32x32x16_bf16 v[176:191], a[0:3], v[96:99], v[36:51]
s_add_u32 s43, s42, 4
ds_read_b128 a[0:3], v62 offset:0
v_add_f32 v60, v60, v52
v_exp_f32 v53, v129
v_add_f32 v61, v61, v56
v_mfma_f32_32x32x16_bf16 v[144:159], a[4:7], v[68:71], v[144:159]
s_mov_b32 s42, s56
s_max_u32 s55, s57, s43
s_min_u32 s55, s55, s42
v_exp_f32 v57, v161
v_add_f32 v60, v60, v53
v_cvt_pk_bf16_f32 v224, v52, v53
v_mfma_f32_32x32x16_bf16 v[176:191], a[4:7], v[100:103], v[176:191]
s_sub_u32 s93, s55, s43
ds_read_b128 a[4:7], v62 offset:512
v_exp_f32 v54, v130
v_add_f32 v61, v61, v57
v_cvt_pk_bf16_f32 v240, v56, v57
v_mfma_f32_32x32x16_bf16 v[144:159], a[8:11], v[72:75], v[144:159]
s_mov_b32 s49, s46
s_mov_b32 s43, s55
buffer_load_dwordx4 v7, s[4:7], s44 offen offset:0 sc1 lds
v_exp_f32 v58, v162
v_add_f32 v60, v60, v54
v_mfma_f32_32x32x16_bf16 v[176:191], a[8:11], v[104:107], v[176:191]
s_mov_b32 s55, s93
ds_read_b128 a[8:11], v62 offset:1024
v_exp_f32 v55, v131
v_add_f32 v61, v61, v58
v_cmp_gt_i32 s[32:33], 0, v8
v_mfma_f32_32x32x16_bf16 v[144:159], a[12:15], v[76:79], v[144:159]
v_exp_f32 v59, v163
v_add_f32 v60, v60, v55
v_cvt_pk_bf16_f32 v225, v54, v55
v_nop
v_cmp_gt_i32 s[34:35], 1, v8
v_mfma_f32_32x32x16_bf16 v[176:191], a[12:15], v[108:111], v[176:191]
ds_read_b128 a[12:15], v62 offset:1536
v_exp_f32 v52, v132
v_add_f32 v61, v61, v59
v_cvt_pk_bf16_f32 v241, v58, v59
v_cndmask_b32 v16, v192, v34, s[32:33]
v_mfma_f32_32x32x16_bf16 v[144:159], a[16:19], v[80:83], v[144:159]
s_add_u32 s46, s46, 16384
v_exp_f32 v56, v164
v_add_f32 v60, v60, v52
v_cmp_gt_i32 s[32:33], 2, v8
v_cndmask_b32 v17, v193, v34, s[34:35]
v_cmp_gt_i32 s[34:35], 3, v8
v_mfma_f32_32x32x16_bf16 v[176:191], a[16:19], v[112:115], v[176:191]
ds_read_b128 a[16:19], v62 offset:2048
v_exp_f32 v53, v133
v_add_f32 v61, v61, v56
v_cndmask_b32 v18, v194, v34, s[32:33]
v_cmp_gt_i32 s[32:33], 8, v8
v_mfma_f32_32x32x16_bf16 v[144:159], a[20:23], v[84:87], v[144:159]
buffer_load_dwordx4 v7, s[4:7], s44 offen offset:1024 sc1 lds
v_exp_f32 v57, v165
v_add_f32 v60, v60, v53
v_cvt_pk_bf16_f32 v226, v52, v53
v_cndmask_b32 v19, v195, v34, s[34:35]
v_mfma_f32_32x32x16_bf16 v[176:191], a[20:23], v[116:119], v[176:191]
ds_read_b128 a[20:23], v62 offset:2560
v_exp_f32 v54, v134
v_add_f32 v61, v61, v57
v_cvt_pk_bf16_f32 v242, v56, v57
v_cmp_gt_i32 s[34:35], 9, v8
v_mfma_f32_32x32x16_bf16 v[144:159], a[24:27], v[88:91], v[144:159]
v_exp_f32 v58, v166
v_add_f32 v60, v60, v54
v_cndmask_b32 v20, v196, v34, s[32:33]
v_cmp_gt_i32 s[32:33], 10, v8
v_cndmask_b32 v21, v197, v34, s[34:35]
v_mfma_f32_32x32x16_bf16 v[176:191], a[24:27], v[120:123], v[176:191]
ds_read_b128 a[24:27], v62 offset:3072
v_exp_f32 v55, v135
v_add_f32 v61, v61, v58
v_cmp_gt_i32 s[34:35], 11, v8
v_cndmask_b32 v22, v198, v34, s[32:33]
v_mfma_f32_32x32x16_bf16 v[144:159], a[28:31], v[92:95], v[144:159]
v_exp_f32 v59, v167
v_add_f32 v60, v60, v55
v_cvt_pk_bf16_f32 v227, v54, v55
v_cmp_gt_i32 s[32:33], 16, v8
v_cndmask_b32 v23, v199, v34, s[34:35]
v_mfma_f32_32x32x16_bf16 v[176:191], a[28:31], v[124:127], v[176:191]
s_waitcnt lgkmcnt(7)
ds_read_b128 a[28:31], v62 offset:3584
v_exp_f32 v52, v136
v_add_f32 v61, v61, v59
v_cvt_pk_bf16_f32 v243, v58, v59
v_mfma_f32_32x32x16_bf16 a[64:79], a[32:35], v[232:235], a[64:79]
buffer_load_dwordx4 v7, s[4:7], s44 offen offset:2048 sc1 lds
v_exp_f32 v56, v168
v_add_f32 v60, v60, v52
v_exp_f32 v53, v137
v_mfma_f32_32x32x16_bf16 a[176:191], a[32:35], v[248:251], a[176:191]
ds_read_b128 a[32:35], v63 offset:0
v_add_f32 v61, v61, v56
v_exp_f32 v57, v169
v_add_f32 v60, v60, v53
v_cvt_pk_bf16_f32 v228, v52, v53
v_mfma_f32_32x32x16_bf16 a[80:95], a[36:39], v[232:235], a[80:95]
s_and_b32 s46, s46, 0xffff
v_exp_f32 v54, v138
v_add_f32 v61, v61, v57
v_cvt_pk_bf16_f32 v244, v56, v57
v_cmp_gt_i32 s[34:35], 17, v8
v_cndmask_b32 v24, v200, v34, s[32:33]
v_mfma_f32_32x32x16_bf16 a[208:223], a[36:39], v[248:251], a[208:223]
ds_read_b128 a[36:39], v63 offset:1024
v_exp_f32 v58, v170
v_add_f32 v60, v60, v54
v_cmp_gt_i32 s[32:33], 18, v8
v_cndmask_b32 v25, v201, v34, s[34:35]
v_mfma_f32_32x32x16_bf16 a[112:127], a[40:43], v[232:235], a[112:127]
v_exp_f32 v55, v139
v_add_f32 v61, v61, v58
v_cmp_gt_i32 s[34:35], 19, v8
v_cndmask_b32 v26, v202, v34, s[32:33]
v_cmp_gt_i32 s[32:33], 24, v8
v_mfma_f32_32x32x16_bf16 a[240:255], a[40:43], v[248:251], a[240:255]
ds_read_b128 a[40:43], v63 offset:2048
v_exp_f32 v59, v171
v_add_f32 v60, v60, v55
v_cvt_pk_bf16_f32 v229, v54, v55
v_cndmask_b32 v27, v203, v34, s[34:35]
v_mfma_f32_32x32x16_bf16 a[144:159], a[44:47], v[232:235], a[144:159]
buffer_load_dwordx4 v7, s[4:7], s44 offen offset:3072 sc1 lds
v_exp_f32 v52, v140
v_add_f32 v61, v61, v59
v_cvt_pk_bf16_f32 v245, v58, v59
v_cmp_gt_i32 s[34:35], 25, v8
v_mfma_f32_32x32x16_bf16 a[96:111], a[44:47], v[248:251], a[96:111]
ds_read_b128 a[44:47], v63 offset:3072
v_exp_f32 v56, v172
v_add_f32 v60, v60, v52
v_cndmask_b32 v28, v204, v34, s[32:33]
v_cmp_gt_i32 s[32:33], 26, v8
v_mfma_f32_32x32x16_bf16 a[64:79], a[48:51], v[236:239], a[64:79]
v_exp_f32 v53, v141
v_add_f32 v61, v61, v56
v_cndmask_b32 v29, v205, v34, s[34:35]
v_cmp_gt_i32 s[34:35], 27, v8
v_cndmask_b32 v30, v206, v34, s[32:33]
v_mfma_f32_32x32x16_bf16 a[176:191], a[48:51], v[252:255], a[176:191]
ds_read_b128 a[48:51], v63 offset:4096
v_exp_f32 v57, v173
v_add_f32 v60, v60, v53
v_cvt_pk_bf16_f32 v230, v52, v53
v_nop
v_mfma_f32_32x32x16_bf16 a[80:95], a[52:55], v[236:239], a[80:95]
v_exp_f32 v54, v142
v_add_f32 v61, v61, v57
v_cvt_pk_bf16_f32 v246, v56, v57
v_cndmask_b32 v31, v207, v34, s[34:35]
v_mfma_f32_32x32x16_bf16 a[208:223], a[52:55], v[252:255], a[208:223]
ds_read_b128 a[52:55], v63 offset:5120
v_exp_f32 v58, v174
v_add_f32 v60, v60, v54
v_mfma_f32_32x32x16_bf16 a[112:127], a[56:59], v[236:239], a[112:127]
v_exp_f32 v55, v143
v_add_f32 v61, v61, v58
v_mfma_f32_32x32x16_bf16 a[240:255], a[56:59], v[252:255], a[240:255]
ds_read_b128 a[56:59], v63 offset:6144
v_exp_f32 v59, v175
v_add_f32 v60, v60, v55
v_cvt_pk_bf16_f32 v231, v54, v55
v_mfma_f32_32x32x16_bf16 a[144:159], a[60:63], v[236:239], a[144:159]
v_add_f32 v61, v61, v59
v_cvt_pk_bf16_f32 v247, v58, v59
v_mfma_f32_32x32x16_bf16 a[96:111], a[60:63], v[252:255], a[96:111]
s_waitcnt lgkmcnt(7)
ds_read_b128 a[60:63], v63 offset:7168
v_mfma_f32_32x32x16_bf16 v[128:143], a[0:3], v[64:67], v[192:207]
s_cmp_lt_u32 s44, s45
s_cselect_b32 m0, s46, s27
ds_read_b128 a[0:3], v62 offset:8192
v_exp_f32 v52, v144
v_mfma_f32_32x32x16_bf16 v[128:143], a[4:7], v[68:71], v[128:143]
ds_read_b128 a[4:7], v62 offset:8704
v_exp_f32 v56, v176
v_add_f32 v60, v60, v52
v_exp_f32 v53, v145
v_mfma_f32_32x32x16_bf16 v[128:143], a[8:11], v[72:75], v[128:143]
ds_read_b128 a[8:11], v62 offset:9216
v_add_f32 v61, v61, v56
v_exp_f32 v57, v177
v_add_f32 v60, v60, v53
v_cvt_pk_bf16_f32 v232, v52, v53
v_mfma_f32_32x32x16_bf16 v[128:143], a[12:15], v[76:79], v[128:143]
buffer_load_dwordx4 v7, s[8:11], s44 offen offset:0 sc1 lds
ds_read_b128 a[12:15], v62 offset:9728
v_exp_f32 v54, v146
v_add_f32 v61, v61, v57
v_mfma_f32_32x32x16_bf16 v[128:143], a[16:19], v[80:83], v[128:143]
s_and_b32 s36, s86, 1
ds_read_b128 a[16:19], v62 offset:10240
v_cvt_pk_bf16_f32 v248, v56, v57
v_exp_f32 v58, v178
v_add_f32 v60, v60, v54
v_mfma_f32_32x32x16_bf16 v[128:143], a[20:23], v[84:87], v[128:143]
ds_read_b128 a[20:23], v62 offset:10752
v_exp_f32 v55, v147
v_add_f32 v61, v61, v58
v_exp_f32 v59, v179
v_mfma_f32_32x32x16_bf16 v[128:143], a[24:27], v[88:91], v[128:143]
s_xor_b32 s50, s46, 0x18000
ds_read_b128 a[24:27], v62 offset:11264
v_add_f32 v60, v60, v55
v_cvt_pk_bf16_f32 v233, v54, v55
v_exp_f32 v52, v148
v_mfma_f32_32x32x16_bf16 v[128:143], a[28:31], v[92:95], v[128:143]
s_waitcnt lgkmcnt(7)
buffer_load_dwordx4 v7, s[8:11], s44 offen offset:1024 sc1 lds
ds_read_b128 a[28:31], v62 offset:11776
v_add_f32 v61, v61, v59
v_cvt_pk_bf16_f32 v249, v58, v59
v_mfma_f32_32x32x16_bf16 a[64:79], a[32:35], v[224:227], a[64:79]
v_exp_f32 v56, v180
v_add_f32 v60, v60, v52
v_exp_f32 v53, v149
v_add_f32 v61, v61, v56
v_mfma_f32_32x32x16_bf16 a[176:191], a[32:35], v[240:243], a[176:191]
ds_read_b128 a[32:35], v63 offset:8192
v_add_u32 v62, s46, v32
v_exp_f32 v57, v181
v_add_f32 v60, v60, v53
v_cvt_pk_bf16_f32 v234, v52, v53
v_mfma_f32_32x32x16_bf16 a[80:95], a[36:39], v[224:227], a[80:95]
v_exp_f32 v54, v150
v_add_f32 v61, v61, v57
v_cvt_pk_bf16_f32 v250, v56, v57
v_exp_f32 v58, v182
v_mfma_f32_32x32x16_bf16 a[208:223], a[36:39], v[240:243], a[208:223]
s_lshl_b32 s36, s36, 3
ds_read_b128 a[36:39], v63 offset:9216
v_add_f32 v60, v60, v54
v_exp_f32 v55, v151
v_add_f32 v61, v61, v58
v_mfma_f32_32x32x16_bf16 a[112:127], a[40:43], v[224:227], a[112:127]
s_and_b32 s28, s87, 7
buffer_load_dwordx4 v7, s[8:11], s44 offen offset:2048 sc1 lds
v_exp_f32 v59, v183
v_add_f32 v60, v60, v55
v_cvt_pk_bf16_f32 v235, v54, v55
v_mfma_f32_32x32x16_bf16 a[240:255], a[40:43], v[240:243], a[240:255]
s_or_b32 s36, s36, s28
ds_read_b128 a[40:43], v63 offset:10240
v_exp_f32 v52, v152
v_add_f32 v61, v61, v59
v_cvt_pk_bf16_f32 v251, v58, v59
v_mfma_f32_32x32x16_bf16 a[144:159], a[44:47], v[224:227], a[144:159]
v_exp_f32 v56, v184
v_add_f32 v60, v60, v52
v_exp_f32 v53, v153
v_add_f32 v61, v61, v56
v_mfma_f32_32x32x16_bf16 a[96:111], a[44:47], v[240:243], a[96:111]
s_lshr_b32 s37, s86, 1
ds_read_b128 a[44:47], v63 offset:11264
v_exp_f32 v57, v185
v_add_f32 v60, v60, v53
v_cvt_pk_bf16_f32 v236, v52, v53
v_mfma_f32_32x32x16_bf16 a[64:79], a[48:51], v[228:231], a[64:79]
s_lshl_b32 s37, s37, 2
buffer_load_dwordx4 v7, s[8:11], s44 offen offset:3072 sc1 lds
v_exp_f32 v54, v154
v_add_f32 v61, v61, v57
v_cvt_pk_bf16_f32 v252, v56, v57
v_mfma_f32_32x32x16_bf16 a[176:191], a[48:51], v[244:247], a[176:191]
ds_read_b128 a[48:51], v63 offset:12288
v_exp_f32 v58, v186
v_add_f32 v60, v60, v54
v_exp_f32 v55, v155
v_mfma_f32_32x32x16_bf16 a[80:95], a[52:55], v[228:231], a[80:95]
s_add_u32 s44, s44, 16384
s_lshr_b32 s28, s87, 3
v_add_f32 v61, v61, v58
v_exp_f32 v59, v187
v_add_f32 v60, v60, v55
v_cvt_pk_bf16_f32 v237, v54, v55
v_mfma_f32_32x32x16_bf16 a[208:223], a[52:55], v[244:247], a[208:223]
s_or_b32 s37, s37, s28
ds_read_b128 a[52:55], v63 offset:13312
v_exp_f32 v52, v156
v_add_f32 v61, v61, v59
v_cvt_pk_bf16_f32 v253, v58, v59
v_mfma_f32_32x32x16_bf16 a[112:127], a[56:59], v[228:231], a[112:127]
v_exp_f32 v56, v188
v_add_f32 v60, v60, v52
v_exp_f32 v53, v157
v_add_f32 v61, v61, v56
v_mfma_f32_32x32x16_bf16 a[240:255], a[56:59], v[244:247], a[240:255]
s_lshl_b32 s24, s36, 18
ds_read_b128 a[56:59], v63 offset:14336
v_exp_f32 v57, v189
v_add_f32 v60, v60, v53
v_cvt_pk_bf16_f32 v238, v52, v53
v_mfma_f32_32x32x16_bf16 a[144:159], a[60:63], v[228:231], a[144:159]
s_lshl_b32 s28, s3, 16
s_add_u32 s24, s24, s28
s_mov_b32 s25, 0
s_mul_i32 s26, s3, 98304
s_lshl_b32 s28, s37, 7
s_add_u32 s26, s26, s28
s_add_u32 s47, s26, 4096
s_mul_i32 s48, s3, 7168
s_mov_b32 s12, s64
s_and_b32 s13, s65, 0xffff
s_mov_b32 s14, 0x400000
s_mov_b32 s16, s66
s_and_b32 s17, s67, 0xffff
s_mov_b32 s18, 0x600000
s_lshl_b32 s29, s36, 18
s_lshl_b32 s28, s3, 13
s_add_u32 s29, s29, s28
s_sub_u32 s29, s29, 34816
s_cmp_eq_u32 s36, 0
s_cselect_b32 s29, 0x7f000000, s29
s_lshl_b32 s32, s3, 2
s_and_b32 s33, s3, 1
s_lshl_b32 s33, s33, 1
s_lshr_b32 s34, s3, 1
s_lshl_b32 s35, s34, 13
s_add_u32 s35, s35, 65536
s_mov_b32 s38, 131072
s_lshl_b32 s28, s88, 13
s_add_u32 s28, s28, 33554432
s_add_u32 s20, s84, s28
s_addc_u32 s21, s85, 0
s_and_b32 s21, s21, 0xffff
s_mov_b32 s22, 16777216
s_sub_u32 s40, 15, s89
s_lshl_b32 s40, s40, 2
s_add_u32 s40, s40, s3
s_lshl_b32 s40, s40, 18
s_lshl_b32 s41, s89, 2
s_add_u32 s41, s41, s94
s_lshl_b32 s41, s41, 18
s_mov_b32 s28, 1602224128
v_exp_f32 v54, v158
v_add_f32 v61, v61, v57
v_cvt_pk_bf16_f32 v254, v56, v57
v_exp_f32 v58, v190
v_add_f32 v60, v60, v54
v_exp_f32 v55, v159
v_add_f32 v61, v61, v58
v_exp_f32 v59, v191
v_add_f32 v60, v60, v55
v_cvt_pk_bf16_f32 v239, v54, v55
v_add_f32 v61, v61, v59
v_cvt_pk_bf16_f32 v255, v58, v59
v_mfma_f32_32x32x16_bf16 a[96:111], a[60:63], v[244:247], a[96:111]
s_waitcnt vmcnt(9) lgkmcnt(7)
s_barrier
ds_read_b128 a[60:63], v63 offset:15360
v_mfma_f32_32x32x16_bf16 v[144:159], a[0:3], v[64:67], v[192:207]
s_cmp_lt_u32 s44, s54
s_cselect_b32 m0, s50, s27
ds_read_b128 a[0:3], v62 offset:0
v_add_u32 v63, s49, v33
v_exp_f32 v52, v128
v_mfma_f32_32x32x16_bf16 v[144:159], a[4:7], v[68:71], v[144:159]
ds_read_b128 a[4:7], v62 offset:512
v_exp_f32 v53, v129
v_add_f32 v60, v60, v52
v_lshrrev_b32 v56, 3, v1
v_and_b32 v57, 7, v1
v_mfma_f32_32x32x16_bf16 v[144:159], a[8:11], v[72:75], v[144:159]
ds_read_b128 a[8:11], v62 offset:1024
v_exp_f32 v54, v130
v_add_f32 v60, v60, v53
v_cvt_pk_bf16_f32 v224, v52, v53
v_xor_b32 v58, v57, v56
v_mfma_f32_32x32x16_bf16 v[144:159], a[12:15], v[76:79], v[144:159]
s_mov_b32 s49, s46
buffer_load_dwordx4 v7, s[4:7], s44 offen offset:0 sc1 lds
ds_read_b128 a[12:15], v62 offset:1536
v_lshlrev_b32 v58, 4, v58
v_lshl_add_u32 v36, v56, 11, v58
v_mfma_f32_32x32x16_bf16 v[144:159], a[16:19], v[80:83], v[144:159]
ds_read_b128 a[16:19], v62 offset:2048
v_exp_f32 v55, v131
v_add_f32 v60, v60, v54
v_add_u32 v37, 15360, v36
v_add_u32 v38, 30720, v36
v_mfma_f32_32x32x16_bf16 v[144:159], a[20:23], v[84:87], v[144:159]
ds_read_b128 a[20:23], v62 offset:2560
v_exp_f32 v52, v132
v_add_f32 v60, v60, v55
v_cvt_pk_bf16_f32 v225, v54, v55
v_add_u32 v39, 46080, v36
v_mfma_f32_32x32x16_bf16 v[144:159], a[24:27], v[88:91], v[144:159]
s_add_u32 s46, s46, 16384
ds_read_b128 a[24:27], v62 offset:3072
v_exp_f32 v53, v133
v_add_f32 v60, v60, v52
v_add_u32 v58, s32, v56
v_mfma_f32_32x32x16_bf16 v[144:159], a[28:31], v[92:95], v[144:159]
s_waitcnt lgkmcnt(7)
buffer_load_dwordx4 v7, s[4:7], s44 offen offset:1024 sc1 lds
ds_read_b128 a[28:31], v62 offset:3584
.Lpad12:
s_nop 0
v_xor_b32 v58, v58, v57
v_and_b32 v58, 7, v58
v_mfma_f32_32x32x16_bf16 a[64:79], a[32:35], v[232:235], a[64:79]
v_exp_f32 v54, v134
v_add_f32 v60, v60, v53
v_cvt_pk_bf16_f32 v226, v52, v53
v_lshlrev_b32 v58, 4, v58
v_lshl_add_u32 v58, v56, 11, v58
v_mfma_f32_32x32x16_bf16 a[176:191], a[32:35], v[248:251], a[176:191]
ds_read_b128 a[32:35], v63 offset:0
v_exp_f32 v55, v135
v_add_f32 v60, v60, v54
v_add_u32 v58, s29, v58
v_and_b32 v59, 32, v1
v_mfma_f32_32x32x16_bf16 a[80:95], a[36:39], v[232:235], a[80:95]
v_exp_f32 v52, v136
v_add_f32 v60, v60, v55
v_cvt_pk_bf16_f32 v227, v54, v55
v_lshlrev_b32 v59, 25, v59
v_or_b32 v42, v59, v58
v_mfma_f32_32x32x16_bf16 a[208:223], a[36:39], v[248:251], a[208:223]
ds_read_b128 a[36:39], v63 offset:1024
v_bfe_u32 v58, v1, 4, 1
v_lshlrev_b32 v58, 1, v58
v_xor_b32 v58, v58, v57
v_lshlrev_b32 v58, 4, v58
v_mul_u32_u24 v59, 6144, v56
v_mfma_f32_32x32x16_bf16 a[112:127], a[40:43], v[232:235], a[112:127]
buffer_load_dwordx4 v7, s[4:7], s44 offen offset:2048 sc1 lds
v_exp_f32 v53, v137
v_add_f32 v60, v60, v52
v_add_u32 v40, v59, v58
v_xor_b32 v41, 64, v40
v_mfma_f32_32x32x16_bf16 a[240:255], a[40:43], v[248:251], a[240:255]
s_and_b32 s46, s46, 0xffff
ds_read_b128 a[40:43], v63 offset:2048
v_exp_f32 v54, v138
v_add_f32 v60, v60, v53
v_cvt_pk_bf16_f32 v228, v52, v53
v_mfma_f32_32x32x16_bf16 a[144:159], a[44:47], v[232:235], a[144:159]
v_exp_f32 v55, v139
v_add_f32 v60, v60, v54
v_add_u32 v41, 48128, v41
v_and_b32 v56, 15, v1
v_lshrrev_b32 v57, 4, v1
v_mfma_f32_32x32x16_bf16 a[96:111], a[44:47], v[248:251], a[96:111]
ds_read_b128 a[44:47], v63 offset:3072
v_and_b32 v58, 7, v56
v_xor_b32 v58, v58, v57
v_lshlrev_b32 v58, 4, v58
v_lshl_add_u32 v43, v56, 7, v58
v_add_u32 v43, s35, v43
v_mfma_f32_32x32x16_bf16 a[64:79], a[48:51], v[236:239], a[64:79]
buffer_load_dwordx4 v7, s[4:7], s44 offen offset:3072 sc1 lds
v_exp_f32 v52, v140
v_add_f32 v60, v60, v55
v_cvt_pk_bf16_f32 v229, v54, v55
v_xor_b32 v44, 64, v43
v_mfma_f32_32x32x16_bf16 a[176:191], a[48:51], v[252:255], a[176:191]
ds_read_b128 a[48:51], v63 offset:4096
v_exp_f32 v53, v141
v_add_f32 v60, v60, v52
v_and_b32 v56, 15, v1
v_lshrrev_b32 v57, 2, v56
v_mfma_f32_32x32x16_bf16 a[80:95], a[52:55], v[236:239], a[80:95]
v_exp_f32 v54, v142
v_add_f32 v60, v60, v53
v_cvt_pk_bf16_f32 v230, v52, v53
v_mul_u32_u24 v57, 6656, v57
v_add_u32 v57, 71680, v57
v_mfma_f32_32x32x16_bf16 a[208:223], a[52:55], v[252:255], a[208:223]
ds_read_b128 a[52:55], v63 offset:5120
v_mov_b32 v58, -2048
v_mov_b32 v59, s34
v_cmp_eq_u32 vcc, 0, v59
v_nop
v_nop
v_mfma_f32_32x32x16_bf16 a[112:127], a[56:59], v[236:239], a[112:127]
v_exp_f32 v55, v143
v_add_f32 v60, v60, v54
v_cndmask_b32 v57, v58, v57, vcc
v_add_u32 v45, v57, v43
v_add_u32 v46, v57, v44
v_mfma_f32_32x32x16_bf16 a[240:255], a[56:59], v[252:255], a[240:255]
ds_read_b128 a[56:59], v63 offset:6144
v_add_f32 v60, v60, v55
v_cvt_pk_bf16_f32 v231, v54, v55
v_bfe_u32 v56, v1, 2, 2
v_bfe_u32 v57, v1, 4, 1
v_lshl_add_u32 v58, v57, 3, v56
v_mfma_f32_32x32x16_bf16 a[144:159], a[60:63], v[236:239], a[144:159]
v_lshlrev_b32 v58, 7, v58
v_lshrrev_b32 v59, 5, v1
v_mul_u32_u24 v59, 7168, v59
v_add3_u32 v58, v58, v59, s38
v_lshrrev_b32 v56, 1, v56
v_lshl_or_b32 v56, v57, 1, v56
v_xor_b32 v56, s33, v56
v_lshl_add_u32 v58, v56, 5, v58
v_and_b32 v56, 3, v1
v_lshl_add_u32 v47, v56, 3, v58
v_xor_b32 v48, 32, v47
v_lshrrev_b32 v57, 1, v56
v_and_b32 v56, 1, v56
v_lshl_or_b32 v56, v56, 1, v57
v_lshl_add_u32 v49, v56, 3, v58
v_xor_b32 v50, 32, v49
v_mov_b32 v8, v61
v_add_u32 v51, 4096, v7
v_nop
v_permlane32_swap_b32 v61, v8
v_add_f32 v61, v61, v8
v_cmp_nge_f32 s[52:53], s28, v61
v_mfma_f32_32x32x16_bf16 a[96:111], a[60:63], v[252:255], a[96:111]
s_waitcnt lgkmcnt(7)
ds_read_b128 a[60:63], v63 offset:7168
s_cmp_le_u32 s94, 3
s_cbranch_scc1 .LTf1
.LTc1:
s_cmp_lt_i32 s55, 0
s_cbranch_scc1 .LE0
s_branch .LS0
.LTf1:
s_cmp_lg_u64 s[52:53], 0
s_cbranch_scc1 .LTc1
s_getpc_b64 s[30:31]
.LTp1:
s_sub_u32 s30, s30, .LTp1-.LTc1
s_subb_u32 s31, s31, 0
s_branch .Lfb
s_nop 0
s_nop 0
s_nop 0
s_nop 0
s_nop 0
s_nop 0
.LS1:
v_mfma_f32_32x32x16_bf16 v[144:159], a[0:3], v[64:67], v[192:207]
s_cmp_lt_u32 s44, s54
s_cselect_b32 m0, s50, s27
ds_read_b128 a[0:3], v62 offset:0
v_add_u32 v63, s49, v33
v_exp_f32 v52, v128
v_mfma_f32_32x32x16_bf16 v[144:159], a[4:7], v[68:71], v[144:159]
ds_read_b128 a[4:7], v62 offset:512
v_exp_f32 v53, v129
v_add_f32 v60, v60, v52
v_exp_f32 v54, v130
v_mfma_f32_32x32x16_bf16 v[144:159], a[8:11], v[72:75], v[144:159]
s_mov_b32 s49, s46
buffer_load_dwordx4 v7, s[4:7], s44 offen offset:0 sc1 lds
ds_read_b128 a[8:11], v62 offset:1024
v_add_f32 v60, v60, v53
v_cvt_pk_bf16_f32 v224, v52, v53
v_mfma_f32_32x32x16_bf16 v[144:159], a[12:15], v[76:79], v[144:159]
ds_read_b128 a[12:15], v62 offset:1536
v_exp_f32 v55, v131
v_add_f32 v60, v60, v54
v_exp_f32 v52, v132
v_mfma_f32_32x32x16_bf16 v[144:159], a[16:19], v[80:83], v[144:159]
s_add_u32 s46, s46, 16384
ds_read_b128 a[16:19], v62 offset:2048
v_add_f32 v60, v60, v55
v_cvt_pk_bf16_f32 v225, v54, v55
v_exp_f32 v53, v133
v_mfma_f32_32x32x16_bf16 v[144:159], a[20:23], v[84:87], v[144:159]
buffer_load_dwordx4 v7, s[4:7], s44 offen offset:1024 sc1 lds
ds_read_b128 a[20:23], v62 offset:2560
v_add_f32 v60, v60, v52
v_exp_f32 v54, v134
v_mfma_f32_32x32x16_bf16 v[144:159], a[24:27], v[88:91], v[144:159]
ds_read_b128 a[24:27], v62 offset:3072
v_add_f32 v60, v60, v53
v_cvt_pk_bf16_f32 v226, v52, v53
v_exp_f32 v55, v135
v_add_f32 v60, v60, v54
v_mfma_f32_32x32x16_bf16 v[144:159], a[28:31], v[92:95], v[144:159]
s_waitcnt lgkmcnt(7)
ds_read_b128 a[28:31], v62 offset:3584
v_exp_f32 v52, v136
v_add_f32 v60, v60, v55
v_cvt_pk_bf16_f32 v227, v54, v55
v_mfma_f32_32x32x16_bf16 a[64:79], a[32:35], v[232:235], a[64:79]
buffer_load_dwordx4 v7, s[4:7], s44 offen offset:2048 sc1 lds
ds_read_b128 a[32:35], v63 offset:0
v_exp_f32 v53, v137
v_add_f32 v60, v60, v52
v_mfma_f32_32x32x16_bf16 a[80:95], a[36:39], v[232:235], a[80:95]
s_and_b32 s46, s46, 0xffff
ds_read_b128 a[36:39], v63 offset:1024
v_exp_f32 v54, v138
v_add_f32 v60, v60, v53
v_cvt_pk_bf16_f32 v228, v52, v53
v_mfma_f32_32x32x16_bf16 a[112:127], a[40:43], v[232:235], a[112:127]
ds_read_b128 a[40:43], v63 offset:2048
v_exp_f32 v55, v139
v_add_f32 v60, v60, v54
v_exp_f32 v52, v140
v_mfma_f32_32x32x16_bf16 a[144:159], a[44:47], v[232:235], a[144:159]
buffer_load_dwordx4 v7, s[4:7], s44 offen offset:3072 sc1 lds
ds_read_b128 a[44:47], v63 offset:3072
v_add_f32 v60, v60, v55
v_cvt_pk_bf16_f32 v229, v54, v55
v_mfma_f32_32x32x16_bf16 a[64:79], a[48:51], v[236:239], a[64:79]
ds_read_b128 a[48:51], v63 offset:4096
v_exp_f32 v53, v141
v_add_f32 v60, v60, v52
v_exp_f32 v54, v142
v_mfma_f32_32x32x16_bf16 a[80:95], a[52:55], v[236:239], a[80:95]
s_sub_u32 s55, s55, 1
ds_read_b128 a[52:55], v63 offset:5120
v_add_f32 v60, v60, v53
v_cvt_pk_bf16_f32 v230, v52, v53
v_exp_f32 v55, v143
v_mfma_f32_32x32x16_bf16 a[112:127], a[56:59], v[236:239], a[112:127]
ds_read_b128 a[56:59], v63 offset:6144
v_add_f32 v60, v60, v54
v_add_f32 v60, v60, v55
v_cvt_pk_bf16_f32 v231, v54, v55
v_mfma_f32_32x32x16_bf16 a[144:159], a[60:63], v[236:239], a[144:159]
s_waitcnt lgkmcnt(7)
ds_read_b128 a[60:63], v63 offset:7168
s_cbranch_scc1 .Lev_s1
.LS0:
v_mfma_f32_32x32x16_bf16 v[128:143], a[0:3], v[64:67], v[192:207]
s_cmp_lt_u32 s44, s45
s_cselect_b32 m0, s46, s27
ds_read_b128 a[0:3], v62 offset:8192
v_exp_f32 v52, v144
v_mfma_f32_32x32x16_bf16 v[128:143], a[4:7], v[68:71], v[128:143]
ds_read_b128 a[4:7], v62 offset:8704
v_exp_f32 v53, v145
v_add_f32 v60, v60, v52
v_exp_f32 v54, v146
v_mfma_f32_32x32x16_bf16 v[128:143], a[8:11], v[72:75], v[128:143]
buffer_load_dwordx4 v7, s[8:11], s44 offen offset:0 sc1 lds
ds_read_b128 a[8:11], v62 offset:9216
v_add_f32 v60, v60, v53
v_cvt_pk_bf16_f32 v232, v52, v53
v_mfma_f32_32x32x16_bf16 v[128:143], a[12:15], v[76:79], v[128:143]
ds_read_b128 a[12:15], v62 offset:9728
v_exp_f32 v55, v147
v_add_f32 v60, v60, v54
v_exp_f32 v52, v148
v_mfma_f32_32x32x16_bf16 v[128:143], a[16:19], v[80:83], v[128:143]
s_xor_b32 s50, s46, 0x18000
ds_read_b128 a[16:19], v62 offset:10240
v_add_f32 v60, v60, v55
v_cvt_pk_bf16_f32 v233, v54, v55
v_exp_f32 v53, v149
v_mfma_f32_32x32x16_bf16 v[128:143], a[20:23], v[84:87], v[128:143]
buffer_load_dwordx4 v7, s[8:11], s44 offen offset:1024 sc1 lds
ds_read_b128 a[20:23], v62 offset:10752
v_add_f32 v60, v60, v52
v_exp_f32 v54, v150
v_mfma_f32_32x32x16_bf16 v[128:143], a[24:27], v[88:91], v[128:143]
ds_read_b128 a[24:27], v62 offset:11264
v_add_f32 v60, v60, v53
v_cvt_pk_bf16_f32 v234, v52, v53
v_exp_f32 v55, v151
v_add_f32 v60, v60, v54
v_mfma_f32_32x32x16_bf16 v[128:143], a[28:31], v[92:95], v[128:143]
s_waitcnt lgkmcnt(7)
ds_read_b128 a[28:31], v62 offset:11776
v_exp_f32 v52, v152
v_add_f32 v60, v60, v55
v_cvt_pk_bf16_f32 v235, v54, v55
v_mfma_f32_32x32x16_bf16 a[64:79], a[32:35], v[224:227], a[64:79]
buffer_load_dwordx4 v7, s[8:11], s44 offen offset:2048 sc1 lds
ds_read_b128 a[32:35], v63 offset:8192
v_exp_f32 v53, v153
v_add_f32 v60, v60, v52
v_mfma_f32_32x32x16_bf16 a[80:95], a[36:39], v[224:227], a[80:95]
ds_read_b128 a[36:39], v63 offset:9216
v_add_u32 v62, s46, v32
v_exp_f32 v54, v154
v_add_f32 v60, v60, v53
v_cvt_pk_bf16_f32 v236, v52, v53
v_mfma_f32_32x32x16_bf16 a[112:127], a[40:43], v[224:227], a[112:127]
ds_read_b128 a[40:43], v63 offset:10240
v_exp_f32 v55, v155
v_add_f32 v60, v60, v54
v_exp_f32 v52, v156
v_mfma_f32_32x32x16_bf16 a[144:159], a[44:47], v[224:227], a[144:159]
buffer_load_dwordx4 v7, s[8:11], s44 offen offset:3072 sc1 lds
ds_read_b128 a[44:47], v63 offset:11264
v_add_f32 v60, v60, v55
v_cvt_pk_bf16_f32 v237, v54, v55
v_mfma_f32_32x32x16_bf16 a[64:79], a[48:51], v[228:231], a[64:79]
s_add_u32 s44, s44, 16384
ds_read_b128 a[48:51], v63 offset:12288
v_exp_f32 v53, v157
v_add_f32 v60, v60, v52
v_mfma_f32_32x32x16_bf16 a[80:95], a[52:55], v[228:231], a[80:95]
s_sub_u32 s55, s55, 1
ds_read_b128 a[52:55], v63 offset:13312
v_exp_f32 v54, v158
v_add_f32 v60, v60, v53
v_cvt_pk_bf16_f32 v238, v52, v53
v_mfma_f32_32x32x16_bf16 a[112:127], a[56:59], v[228:231], a[112:127]
ds_read_b128 a[56:59], v63 offset:14336
v_exp_f32 v55, v159
v_add_f32 v60, v60, v54
v_add_f32 v60, v60, v55
v_cvt_pk_bf16_f32 v239, v54, v55
v_mfma_f32_32x32x16_bf16 a[144:159], a[60:63], v[228:231], a[144:159]
s_waitcnt vmcnt(8) lgkmcnt(7)
s_barrier
ds_read_b128 a[60:63], v63 offset:15360
s_cbranch_scc0 .LS1
s_cmp_eq_u32 s43, s42
s_cbranch_scc1 .LE1
s_swappc_b64 s[30:31], s[58:59]
.Lev_s0_r:
s_branch .LS1
.LE0:
v_mfma_f32_32x32x16_bf16 v[128:143], a[0:3], v[64:67], v[16:31]
s_cmp_lt_u32 s44, s45
s_cselect_b32 m0, s46, s27
v_exp_f32 v52, v144
v_exp_f32 v53, v145
v_add_f32 v60, v60, v52
v_mfma_f32_32x32x16_bf16 v[128:143], a[4:7], v[68:71], v[128:143]
buffer_load_dwordx4 v7, s[8:11], s44 offen offset:0 sc1 lds
buffer_load_dwordx4 v7, s[8:11], s44 offen offset:1024 sc1 lds
v_exp_f32 v54, v146
v_add_f32 v60, v60, v53
v_cvt_pk_bf16_f32 v232, v52, v53
v_mfma_f32_32x32x16_bf16 v[128:143], a[8:11], v[72:75], v[128:143]
buffer_load_dwordx4 v7, s[8:11], s44 offen offset:2048 sc1 lds
buffer_load_dwordx4 v7, s[8:11], s44 offen offset:3072 sc1 lds
v_exp_f32 v55, v147
v_add_f32 v60, v60, v54
v_mfma_f32_32x32x16_bf16 v[128:143], a[12:15], v[76:79], v[128:143]
s_add_u32 m0, s48, 131072
buffer_load_dword v35, v4, s[96:99], 0 offen
v_exp_f32 v52, v148
v_add_f32 v60, v60, v55
v_cvt_pk_bf16_f32 v233, v54, v55
v_mfma_f32_32x32x16_bf16 v[128:143], a[16:19], v[80:83], v[128:143]
s_xor_b32 s50, s46, 0x18000
buffer_load_dwordx4 v40, s[16:19], s26 offen offset:0 lds
v_exp_f32 v53, v149
v_add_f32 v60, v60, v52
v_mfma_f32_32x32x16_bf16 v[128:143], a[20:23], v[84:87], v[128:143]
buffer_load_dwordx4 v41, s[16:19], s26 offen offset:1024 lds
v_exp_f32 v54, v150
v_add_f32 v60, v60, v53
v_cvt_pk_bf16_f32 v234, v52, v53
v_mfma_f32_32x32x16_bf16 v[128:143], a[24:27], v[88:91], v[128:143]
buffer_load_dwordx4 v40, s[16:19], s26 offen offset:2048 lds
v_exp_f32 v55, v151
v_add_f32 v60, v60, v54
v_mfma_f32_32x32x16_bf16 v[128:143], a[28:31], v[92:95], v[128:143]
s_waitcnt lgkmcnt(0)
buffer_load_dwordx4 v41, s[16:19], s26 offen offset:3072 lds
v_exp_f32 v52, v152
v_add_f32 v60, v60, v55
v_cvt_pk_bf16_f32 v235, v54, v55
v_exp_f32 v53, v153
v_mfma_f32_32x32x16_bf16 a[64:79], a[32:35], v[224:227], a[64:79]
s_add_u32 m0, s48, 135168
ds_read_b128 a[32:35], v63 offset:8192
v_add_f32 v60, v60, v52
v_exp_f32 v54, v154
v_mfma_f32_32x32x16_bf16 a[80:95], a[36:39], v[224:227], a[80:95]
buffer_load_dwordx4 v40, s[16:19], s47 offen offset:0 lds
ds_read_b128 a[36:39], v63 offset:9216
v_add_f32 v60, v60, v53
v_cvt_pk_bf16_f32 v236, v52, v53
v_exp_f32 v55, v155
v_add_f32 v60, v60, v54
v_mfma_f32_32x32x16_bf16 a[112:127], a[40:43], v[224:227], a[112:127]
buffer_load_dwordx4 v41, s[16:19], s47 offen offset:1024 lds
ds_read_b128 a[40:43], v63 offset:10240
v_exp_f32 v52, v156
v_add_f32 v60, v60, v55
v_cvt_pk_bf16_f32 v237, v54, v55
v_mfma_f32_32x32x16_bf16 a[144:159], a[44:47], v[224:227], a[144:159]
buffer_load_dwordx4 v42, s[12:15], s25 offen offset:2048 lds
ds_read_b128 a[44:47], v63 offset:11264
v_exp_f32 v53, v157
v_add_f32 v60, v60, v52
v_mfma_f32_32x32x16_bf16 a[64:79], a[48:51], v[228:231], a[64:79]
s_add_u32 s44, s44, 16384
s_add_u32 m0, s91, 65536
ds_read_b128 a[48:51], v63 offset:12288
v_exp_f32 v54, v158
v_add_f32 v60, v60, v53
v_cvt_pk_bf16_f32 v238, v52, v53
v_mfma_f32_32x32x16_bf16 a[80:95], a[52:55], v[228:231], a[80:95]
buffer_load_dwordx4 v36, s[12:15], s24 offen offset:0 lds
buffer_load_dwordx4 v37, s[12:15], s24 offen offset:1024 lds
ds_read_b128 a[52:55], v63 offset:13312
v_exp_f32 v55, v159
v_add_f32 v60, v60, v54
v_mfma_f32_32x32x16_bf16 a[112:127], a[56:59], v[228:231], a[112:127]
buffer_load_dwordx4 v38, s[12:15], s24 offen offset:2048 lds
buffer_load_dwordx4 v39, s[12:15], s24 offen offset:3072 lds
ds_read_b128 a[56:59], v63 offset:14336
v_add_f32 v60, v60, v55
v_cvt_pk_bf16_f32 v239, v54, v55
v_mfma_f32_32x32x16_bf16 a[144:159], a[60:63], v[228:231], a[144:159]
s_waitcnt vmcnt(20)
s_barrier
s_add_u32 s26, s26, 393216
s_add_u32 s47, s47, 393216
s_add_u32 s25, s25, 128
s_add_u32 s24, s24, 128
ds_read_b128 a[60:63], v63 offset:15360
v_add_u32 v63, s49, v33
s_waitcnt lgkmcnt(4)
v_mfma_f32_32x32x16_bf16 a[64:79], a[32:35], v[232:235], a[64:79]
s_add_u32 m0, s91, 81920
ds_read_b128 a[32:35], v63 offset:0
v_exp_f32 v52, v128
v_exp_f32 v53, v129
v_add_f32 v60, v60, v52
v_mfma_f32_32x32x16_bf16 a[80:95], a[36:39], v[232:235], a[80:95]
buffer_load_dwordx4 v36, s[12:15], s24 offen offset:0 lds
ds_read_b128 a[36:39], v63 offset:1024
v_exp_f32 v54, v130
v_add_f32 v60, v60, v53
v_cvt_pk_bf16_f32 v224, v52, v53
v_mfma_f32_32x32x16_bf16 a[112:127], a[40:43], v[232:235], a[112:127]
buffer_load_dwordx4 v37, s[12:15], s24 offen offset:1024 lds
ds_read_b128 a[40:43], v63 offset:2048
v_exp_f32 v55, v131
v_add_f32 v60, v60, v54
v_exp_f32 v52, v132
v_mfma_f32_32x32x16_bf16 a[144:159], a[44:47], v[232:235], a[144:159]
s_waitcnt lgkmcnt(3)
ds_read_b128 a[44:47], v63 offset:3072
v_add_f32 v60, v60, v55
v_cvt_pk_bf16_f32 v225, v54, v55
v_exp_f32 v53, v133
v_add_f32 v60, v60, v52
v_mfma_f32_32x32x16_bf16 a[64:79], a[48:51], v[236:239], a[64:79]
buffer_load_dwordx4 v38, s[12:15], s24 offen offset:2048 lds
ds_read_b128 a[48:51], v63 offset:4096
v_exp_f32 v54, v134
v_add_f32 v60, v60, v53
v_cvt_pk_bf16_f32 v226, v52, v53
v_mfma_f32_32x32x16_bf16 a[80:95], a[52:55], v[236:239], a[80:95]
buffer_load_dwordx4 v39, s[12:15], s24 offen offset:3072 lds
ds_read_b128 a[52:55], v63 offset:5120
v_exp_f32 v55, v135
v_add_f32 v60, v60, v54
v_exp_f32 v52, v136
v_mfma_f32_32x32x16_bf16 a[112:127], a[56:59], v[236:239], a[112:127]
s_add_u32 s24, s24, 128
ds_read_b128 a[56:59], v63 offset:6144
v_add_f32 v60, v60, v55
v_cvt_pk_bf16_f32 v227, v54, v55
v_exp_f32 v53, v137
v_add_f32 v60, v60, v52
v_mfma_f32_32x32x16_bf16 a[144:159], a[60:63], v[236:239], a[144:159]
ds_read_b128 a[60:63], v63 offset:7168
v_exp_f32 v54, v138
v_add_f32 v60, v60, v53
v_cvt_pk_bf16_f32 v228, v52, v53
v_exp_f32 v55, v139
v_add_f32 v60, v60, v54
v_exp_f32 v52, v140
v_add_f32 v60, v60, v55
v_cvt_pk_bf16_f32 v229, v54, v55
v_exp_f32 v53, v141
v_add_f32 v60, v60, v52
v_exp_f32 v54, v142
v_add_f32 v60, v60, v53
v_cvt_pk_bf16_f32 v230, v52, v53
v_exp_f32 v55, v143
v_add_f32 v60, v60, v54
v_add_f32 v60, v60, v55
v_cvt_pk_bf16_f32 v231, v54, v55
s_waitcnt lgkmcnt(4)
v_mfma_f32_32x32x16_bf16 a[64:79], a[32:35], v[224:227], a[64:79]
s_mov_b32 s28, 1602224128
v_mov_b32 v8, v60
v_mfma_f32_32x32x16_bf16 a[80:95], a[36:39], v[224:227], a[80:95]
s_nop 0
v_permlane32_swap_b32 v60, v8
v_mfma_f32_32x32x16_bf16 a[112:127], a[40:43], v[224:227], a[112:127]
v_add_f32 v60, v60, v8
v_mfma_f32_32x32x16_bf16 a[144:159], a[44:47], v[224:227], a[144:159]
s_waitcnt lgkmcnt(0)
v_cmp_nge_f32 s[30:31], s28, v60
v_mfma_f32_32x32x16_bf16 a[64:79], a[48:51], v[228:231], a[64:79]
v_mfma_f32_32x32x16_bf16 a[80:95], a[52:55], v[228:231], a[80:95]
v_mfma_f32_32x32x16_bf16 a[112:127], a[56:59], v[228:231], a[112:127]
v_mfma_f32_32x32x16_bf16 a[144:159], a[60:63], v[228:231], a[144:159]
s_cmp_lt_u32 s3, 2
s_cbranch_scc0 .LEb0
s_waitcnt vmcnt(16)
s_barrier
.LEb0:
s_branch .LEend
.LE1:
v_mfma_f32_32x32x16_bf16 v[144:159], a[0:3], v[64:67], v[16:31]
s_add_u32 m0, s48, 131072
buffer_load_dword v35, v4, s[96:99], 0 offen
v_add_u32 v63, s49, v33
v_exp_f32 v52, v128
v_exp_f32 v53, v129
v_add_f32 v60, v60, v52
v_mfma_f32_32x32x16_bf16 v[144:159], a[4:7], v[68:71], v[144:159]
buffer_load_dwordx4 v40, s[16:19], s26 offen offset:0 lds
v_exp_f32 v54, v130
v_add_f32 v60, v60, v53
v_cvt_pk_bf16_f32 v224, v52, v53
v_mfma_f32_32x32x16_bf16 v[144:159], a[8:11], v[72:75], v[144:159]
buffer_load_dwordx4 v41, s[16:19], s26 offen offset:1024 lds
v_exp_f32 v55, v131
v_add_f32 v60, v60, v54
v_mfma_f32_32x32x16_bf16 v[144:159], a[12:15], v[76:79], v[144:159]
buffer_load_dwordx4 v40, s[16:19], s26 offen offset:2048 lds
v_exp_f32 v52, v132
v_add_f32 v60, v60, v55
v_cvt_pk_bf16_f32 v225, v54, v55
v_mfma_f32_32x32x16_bf16 v[144:159], a[16:19], v[80:83], v[144:159]
buffer_load_dwordx4 v41, s[16:19], s26 offen offset:3072 lds
v_exp_f32 v53, v133
v_add_f32 v60, v60, v52
v_mfma_f32_32x32x16_bf16 v[144:159], a[20:23], v[84:87], v[144:159]
s_add_u32 m0, s48, 135168
v_exp_f32 v54, v134
v_add_f32 v60, v60, v53
v_cvt_pk_bf16_f32 v226, v52, v53
v_mfma_f32_32x32x16_bf16 v[144:159], a[24:27], v[88:91], v[144:159]
buffer_load_dwordx4 v40, s[16:19], s47 offen offset:0 lds
v_exp_f32 v55, v135
v_add_f32 v60, v60, v54
v_mfma_f32_32x32x16_bf16 v[144:159], a[28:31], v[92:95], v[144:159]
s_waitcnt lgkmcnt(0)
buffer_load_dwordx4 v41, s[16:19], s47 offen offset:1024 lds
v_exp_f32 v52, v136
v_add_f32 v60, v60, v55
v_cvt_pk_bf16_f32 v227, v54, v55
v_exp_f32 v53, v137
v_mfma_f32_32x32x16_bf16 a[64:79], a[32:35], v[232:235], a[64:79]
buffer_load_dwordx4 v42, s[12:15], s25 offen offset:2048 lds
ds_read_b128 a[32:35], v63 offset:0
v_add_f32 v60, v60, v52
v_exp_f32 v54, v138
v_add_f32 v60, v60, v53
v_cvt_pk_bf16_f32 v228, v52, v53
v_mfma_f32_32x32x16_bf16 a[80:95], a[36:39], v[232:235], a[80:95]
s_add_u32 m0, s91, 65536
ds_read_b128 a[36:39], v63 offset:1024
v_exp_f32 v55, v139
v_add_f32 v60, v60, v54
v_mfma_f32_32x32x16_bf16 a[112:127], a[40:43], v[232:235], a[112:127]
buffer_load_dwordx4 v36, s[12:15], s24 offen offset:0 lds
ds_read_b128 a[40:43], v63 offset:2048
v_exp_f32 v52, v140
v_add_f32 v60, v60, v55
v_cvt_pk_bf16_f32 v229, v54, v55
v_mfma_f32_32x32x16_bf16 a[144:159], a[44:47], v[232:235], a[144:159]
buffer_load_dwordx4 v37, s[12:15], s24 offen offset:1024 lds
ds_read_b128 a[44:47], v63 offset:3072
v_exp_f32 v53, v141
v_add_f32 v60, v60, v52
v_mfma_f32_32x32x16_bf16 a[64:79], a[48:51], v[236:239], a[64:79]
buffer_load_dwordx4 v38, s[12:15], s24 offen offset:2048 lds
ds_read_b128 a[48:51], v63 offset:4096
v_exp_f32 v54, v142
v_add_f32 v60, v60, v53
v_cvt_pk_bf16_f32 v230, v52, v53
v_mfma_f32_32x32x16_bf16 a[80:95], a[52:55], v[236:239], a[80:95]
buffer_load_dwordx4 v39, s[12:15], s24 offen offset:3072 lds
ds_read_b128 a[52:55], v63 offset:5120
v_exp_f32 v55, v143
v_add_f32 v60, v60, v54
v_mfma_f32_32x32x16_bf16 a[112:127], a[56:59], v[236:239], a[112:127]
ds_read_b128 a[56:59], v63 offset:6144
v_add_f32 v60, v60, v55
v_cvt_pk_bf16_f32 v231, v54, v55
v_mfma_f32_32x32x16_bf16 a[144:159], a[60:63], v[236:239], a[144:159]
s_add_u32 s26, s26, 393216
s_add_u32 s47, s47, 393216
s_add_u32 s25, s25, 128
s_add_u32 s24, s24, 128
ds_read_b128 a[60:63], v63 offset:7168
s_waitcnt lgkmcnt(4)
v_mfma_f32_32x32x16_bf16 a[64:79], a[32:35], v[224:227], a[64:79]
s_add_u32 m0, s91, 81920
ds_read_b128 a[32:35], v63 offset:8192
v_exp_f32 v52, v144
v_exp_f32 v53, v145
v_add_f32 v60, v60, v52
v_mfma_f32_32x32x16_bf16 a[80:95], a[36:39], v[224:227], a[80:95]
buffer_load_dwordx4 v36, s[12:15], s24 offen offset:0 lds
ds_read_b128 a[36:39], v63 offset:9216
v_exp_f32 v54, v146
v_add_f32 v60, v60, v53
v_cvt_pk_bf16_f32 v232, v52, v53
v_mfma_f32_32x32x16_bf16 a[112:127], a[40:43], v[224:227], a[112:127]
buffer_load_dwordx4 v37, s[12:15], s24 offen offset:1024 lds
ds_read_b128 a[40:43], v63 offset:10240
v_exp_f32 v55, v147
v_add_f32 v60, v60, v54
v_exp_f32 v52, v148
v_mfma_f32_32x32x16_bf16 a[144:159], a[44:47], v[224:227], a[144:159]
s_waitcnt lgkmcnt(3)
ds_read_b128 a[44:47], v63 offset:11264
v_add_f32 v60, v60, v55
v_cvt_pk_bf16_f32 v233, v54, v55
v_exp_f32 v53, v149
v_add_f32 v60, v60, v52
v_mfma_f32_32x32x16_bf16 a[64:79], a[48:51], v[228:231], a[64:79]
buffer_load_dwordx4 v38, s[12:15], s24 offen offset:2048 lds
ds_read_b128 a[48:51], v63 offset:12288
v_exp_f32 v54, v150
v_add_f32 v60, v60, v53
v_cvt_pk_bf16_f32 v234, v52, v53
v_mfma_f32_32x32x16_bf16 a[80:95], a[52:55], v[228:231], a[80:95]
buffer_load_dwordx4 v39, s[12:15], s24 offen offset:3072 lds
ds_read_b128 a[52:55], v63 offset:13312
v_exp_f32 v55, v151
v_add_f32 v60, v60, v54
v_exp_f32 v52, v152
v_mfma_f32_32x32x16_bf16 a[112:127], a[56:59], v[228:231], a[112:127]
s_add_u32 s24, s24, 128
ds_read_b128 a[56:59], v63 offset:14336
v_add_f32 v60, v60, v55
v_cvt_pk_bf16_f32 v235, v54, v55
v_exp_f32 v53, v153
v_add_f32 v60, v60, v52
v_mfma_f32_32x32x16_bf16 a[144:159], a[60:63], v[228:231], a[144:159]
ds_read_b128 a[60:63], v63 offset:15360
v_add_u32 v63, s49, v33
v_exp_f32 v54, v154
v_add_f32 v60, v60, v53
v_cvt_pk_bf16_f32 v236, v52, v53
v_exp_f32 v55, v155
v_add_f32 v60, v60, v54
v_exp_f32 v52, v156
v_add_f32 v60, v60, v55
v_cvt_pk_bf16_f32 v237, v54, v55
v_exp_f32 v53, v157
v_add_f32 v60, v60, v52
v_exp_f32 v54, v158
v_add_f32 v60, v60, v53
v_cvt_pk_bf16_f32 v238, v52, v53
v_exp_f32 v55, v159
v_add_f32 v60, v60, v54
v_add_f32 v60, v60, v55
v_cvt_pk_bf16_f32 v239, v54, v55
s_cmp_lt_u32 s3, 2
s_cbranch_scc0 .LEb1
s_waitcnt vmcnt(16)
s_barrier
.LEb1:
s_waitcnt lgkmcnt(4)
v_mfma_f32_32x32x16_bf16 a[64:79], a[32:35], v[232:235], a[64:79]
s_mov_b32 s28, 1602224128
v_mov_b32 v8, v60
v_mfma_f32_32x32x16_bf16 a[80:95], a[36:39], v[232:235], a[80:95]
s_nop 0
v_permlane32_swap_b32 v60, v8
v_mfma_f32_32x32x16_bf16 a[112:127], a[40:43], v[232:235], a[112:127]
v_add_f32 v60, v60, v8
v_mfma_f32_32x32x16_bf16 a[144:159], a[44:47], v[232:235], a[144:159]
s_waitcnt lgkmcnt(0)
v_cmp_nge_f32 s[30:31], s28, v60
v_mfma_f32_32x32x16_bf16 a[64:79], a[48:51], v[236:239], a[64:79]
v_mfma_f32_32x32x16_bf16 a[80:95], a[52:55], v[236:239], a[80:95]
v_mfma_f32_32x32x16_bf16 a[112:127], a[56:59], v[236:239], a[112:127]
v_mfma_f32_32x32x16_bf16 a[144:159], a[60:63], v[236:239], a[144:159]
.LEend:
s_or_b64 s[34:35], s[30:31], s[52:53]
s_cbranch_scc1 .Lx
.Lx_ret:
v_rcp_f32 v160, v60
s_nop 0
v_mov_b32 v161, v160
s_nop 3
v_accvgpr_read_b32 v164, a64
v_accvgpr_read_b32 v165, a65
v_accvgpr_read_b32 v166, a66
v_accvgpr_read_b32 v167, a67
v_accvgpr_read_b32 v168, a68
v_accvgpr_read_b32 v169, a69
v_accvgpr_read_b32 v170, a70
v_accvgpr_read_b32 v171, a71
v_pk_mul_f32 v[164:165], v[164:165], v[160:161]
v_pk_mul_f32 v[166:167], v[166:167], v[160:161]
v_pk_mul_f32 v[168:169], v[168:169], v[160:161]
v_pk_mul_f32 v[170:171], v[170:171], v[160:161]
v_cvt_pk_bf16_f32 v172, v164, v165
v_cvt_pk_bf16_f32 v173, v166, v167
v_cvt_pk_bf16_f32 v174, v168, v169
.Lpad13:
s_cbranch_execz .Ltch3
v_cvt_pk_bf16_f32 v175, v170, v171
s_add_u32 m0, s48, 98304
s_nop 0
buffer_load_dwordx4 v40, s[16:19], s26 offen offset:0 lds
buffer_load_dwordx4 v41, s[16:19], s26 offen offset:1024 lds
buffer_store_dwordx4 v[172:175], v7, s[20:23], s40 offen offset:0 sc1
v_accvgpr_read_b32 v176, a72
v_accvgpr_read_b32 v177, a73
v_accvgpr_read_b32 v178, a74
v_accvgpr_read_b32 v179, a75
v_accvgpr_read_b32 v180, a76
v_accvgpr_read_b32 v181, a77
v_accvgpr_read_b32 v182, a78
v_accvgpr_read_b32 v183, a79
v_pk_mul_f32 v[176:177], v[176:177], v[160:161]
v_pk_mul_f32 v[178:179], v[178:179], v[160:161]
v_pk_mul_f32 v[180:181], v[180:181], v[160:161]
v_pk_mul_f32 v[182:183], v[182:183], v[160:161]
v_cvt_pk_bf16_f32 v184, v176, v177
v_cvt_pk_bf16_f32 v185, v178, v179
v_cvt_pk_bf16_f32 v186, v180, v181
v_cvt_pk_bf16_f32 v187, v182, v183
buffer_load_dwordx4 v40, s[16:19], s26 offen offset:2048 lds
buffer_load_dwordx4 v41, s[16:19], s26 offen offset:3072 lds
buffer_store_dwordx4 v[184:187], v7, s[20:23], s40 offen offset:1024 sc1
v_accvgpr_read_b32 v164, a80
v_accvgpr_read_b32 v165, a81
v_accvgpr_read_b32 v166, a82
v_accvgpr_read_b32 v167, a83
v_accvgpr_read_b32 v168, a84
v_accvgpr_read_b32 v169, a85
v_accvgpr_read_b32 v170, a86
v_accvgpr_read_b32 v171, a87
v_pk_mul_f32 v[164:165], v[164:165], v[160:161]
v_pk_mul_f32 v[166:167], v[166:167], v[160:161]
v_pk_mul_f32 v[168:169], v[168:169], v[160:161]
v_pk_mul_f32 v[170:171], v[170:171], v[160:161]
v_cvt_pk_bf16_f32 v172, v164, v165
v_cvt_pk_bf16_f32 v173, v166, v167
v_cvt_pk_bf16_f32 v174, v168, v169
v_cvt_pk_bf16_f32 v175, v170, v171
s_add_u32 m0, s48, 102400
s_nop 0
buffer_load_dwordx4 v40, s[16:19], s47 offen offset:0 lds
buffer_load_dwordx4 v41, s[16:19], s47 offen offset:1024 lds
buffer_store_dwordx4 v[172:175], v7, s[20:23], s40 offen offset:2048 sc1
v_accvgpr_read_b32 v176, a88
v_accvgpr_read_b32 v177, a89
v_accvgpr_read_b32 v178, a90
v_accvgpr_read_b32 v179, a91
v_accvgpr_read_b32 v180, a92
v_accvgpr_read_b32 v181, a93
v_accvgpr_read_b32 v182, a94
v_accvgpr_read_b32 v183, a95
v_pk_mul_f32 v[176:177], v[176:177], v[160:161]
v_pk_mul_f32 v[178:179], v[178:179], v[160:161]
v_pk_mul_f32 v[180:181], v[180:181], v[160:161]
v_pk_mul_f32 v[182:183], v[182:183], v[160:161]
v_cvt_pk_bf16_f32 v184, v176, v177
v_cvt_pk_bf16_f32 v185, v178, v179
v_cvt_pk_bf16_f32 v186, v180, v181
v_cvt_pk_bf16_f32 v187, v182, v183
buffer_load_dwordx4 v42, s[12:15], s25 offen offset:2048 lds
s_add_u32 m0, s91, 0
s_nop 0
buffer_load_dwordx4 v36, s[12:15], s24 offen offset:0 lds
buffer_store_dwordx4 v[184:187], v7, s[20:23], s40 offen offset:3072 sc1
v_accvgpr_read_b32 v164, a112
v_accvgpr_read_b32 v165, a113
v_accvgpr_read_b32 v166, a114
v_accvgpr_read_b32 v167, a115
v_accvgpr_read_b32 v168, a116
v_accvgpr_read_b32 v169, a117
v_accvgpr_read_b32 v170, a118
v_accvgpr_read_b32 v171, a119
v_pk_mul_f32 v[164:165], v[164:165], v[160:161]
v_pk_mul_f32 v[166:167], v[166:167], v[160:161]
v_pk_mul_f32 v[168:169], v[168:169], v[160:161]
v_pk_mul_f32 v[170:171], v[170:171], v[160:161]
v_cvt_pk_bf16_f32 v172, v164, v165
v_cvt_pk_bf16_f32 v173, v166, v167
v_cvt_pk_bf16_f32 v174, v168, v169
v_cvt_pk_bf16_f32 v175, v170, v171
buffer_load_dwordx4 v37, s[12:15], s24 offen offset:1024 lds
buffer_load_dwordx4 v38, s[12:15], s24 offen offset:2048 lds
buffer_store_dwordx4 v[172:175], v51, s[20:23], s40 offen offset:0 sc1
v_accvgpr_read_b32 v176, a120
v_accvgpr_read_b32 v177, a121
v_accvgpr_read_b32 v178, a122
v_accvgpr_read_b32 v179, a123
v_accvgpr_read_b32 v180, a124
v_accvgpr_read_b32 v181, a125
v_accvgpr_read_b32 v182, a126
v_accvgpr_read_b32 v183, a127
v_pk_mul_f32 v[176:177], v[176:177], v[160:161]
v_pk_mul_f32 v[178:179], v[178:179], v[160:161]
v_pk_mul_f32 v[180:181], v[180:181], v[160:161]
v_pk_mul_f32 v[182:183], v[182:183], v[160:161]
v_cvt_pk_bf16_f32 v184, v176, v177
v_cvt_pk_bf16_f32 v185, v178, v179
v_cvt_pk_bf16_f32 v186, v180, v181
v_cvt_pk_bf16_f32 v187, v182, v183
buffer_load_dwordx4 v39, s[12:15], s24 offen offset:3072 lds
s_add_u32 s26, s26, 393216
s_add_u32 s47, s47, 393216
s_add_u32 s25, s25, 128
s_add_u32 s24, s24, 128
buffer_store_dwordx4 v[184:187], v51, s[20:23], s40 offen offset:1024 sc1
v_accvgpr_read_b32 v164, a144
v_accvgpr_read_b32 v165, a145
v_accvgpr_read_b32 v166, a146
v_accvgpr_read_b32 v167, a147
v_accvgpr_read_b32 v168, a148
v_accvgpr_read_b32 v169, a149
v_accvgpr_read_b32 v170, a150
v_accvgpr_read_b32 v171, a151
v_pk_mul_f32 v[164:165], v[164:165], v[160:161]
v_pk_mul_f32 v[166:167], v[166:167], v[160:161]
v_pk_mul_f32 v[168:169], v[168:169], v[160:161]
v_pk_mul_f32 v[170:171], v[170:171], v[160:161]
v_cvt_pk_bf16_f32 v172, v164, v165
v_cvt_pk_bf16_f32 v173, v166, v167
v_cvt_pk_bf16_f32 v174, v168, v169
v_cvt_pk_bf16_f32 v175, v170, v171
buffer_store_dwordx4 v[172:175], v51, s[20:23], s40 offen offset:2048 sc1
v_accvgpr_read_b32 v176, a152
v_accvgpr_read_b32 v177, a153
v_accvgpr_read_b32 v178, a154
v_accvgpr_read_b32 v179, a155
v_accvgpr_read_b32 v180, a156
v_accvgpr_read_b32 v181, a157
v_accvgpr_read_b32 v182, a158
v_accvgpr_read_b32 v183, a159
v_pk_mul_f32 v[176:177], v[176:177], v[160:161]
v_pk_mul_f32 v[178:179], v[178:179], v[160:161]
v_pk_mul_f32 v[180:181], v[180:181], v[160:161]
v_pk_mul_f32 v[182:183], v[182:183], v[160:161]
v_cvt_pk_bf16_f32 v184, v176, v177
v_cvt_pk_bf16_f32 v185, v178, v179
v_cvt_pk_bf16_f32 v186, v180, v181
v_cvt_pk_bf16_f32 v187, v182, v183
buffer_store_dwordx4 v[184:187], v51, s[20:23], s40 offen offset:3072 sc1
s_cmp_eq_u32 s41, -1
s_cbranch_scc1 .Lfb_ret2
s_getpc_b64 s[30:31]
.Lfbp:
s_add_u32 s30, s30, .Lfb_ret2-.Lfbp
s_addc_u32 s31, s31, 0
.Lfb:
v_rcp_f32 v160, v61
s_nop 0
v_mov_b32 v161, v160
v_accvgpr_read_b32 v164, a176
v_accvgpr_read_b32 v165, a177
v_accvgpr_read_b32 v166, a178
v_accvgpr_read_b32 v167, a179
v_accvgpr_read_b32 v168, a180
v_accvgpr_read_b32 v169, a181
v_accvgpr_read_b32 v170, a182
v_accvgpr_read_b32 v171, a183
v_pk_mul_f32 v[164:165], v[164:165], v[160:161]
v_pk_mul_f32 v[166:167], v[166:167], v[160:161]
v_pk_mul_f32 v[168:169], v[168:169], v[160:161]
v_pk_mul_f32 v[170:171], v[170:171], v[160:161]
v_cvt_pk_bf16_f32 v172, v164, v165
v_cvt_pk_bf16_f32 v173, v166, v167
v_cvt_pk_bf16_f32 v174, v168, v169
v_cvt_pk_bf16_f32 v175, v170, v171
buffer_store_dwordx4 v[172:175], v7, s[20:23], s41 offen offset:0 sc1
v_accvgpr_read_b32 v176, a184
v_accvgpr_read_b32 v177, a185
v_accvgpr_read_b32 v178, a186
v_accvgpr_read_b32 v179, a187
v_accvgpr_read_b32 v180, a188
v_accvgpr_read_b32 v181, a189
v_accvgpr_read_b32 v182, a190
v_accvgpr_read_b32 v183, a191
v_pk_mul_f32 v[176:177], v[176:177], v[160:161]
v_pk_mul_f32 v[178:179], v[178:179], v[160:161]
v_pk_mul_f32 v[180:181], v[180:181], v[160:161]
v_pk_mul_f32 v[182:183], v[182:183], v[160:161]
v_cvt_pk_bf16_f32 v184, v176, v177
v_cvt_pk_bf16_f32 v185, v178, v179
v_cvt_pk_bf16_f32 v186, v180, v181
v_cvt_pk_bf16_f32 v187, v182, v183
buffer_store_dwordx4 v[184:187], v7, s[20:23], s41 offen offset:1024 sc1
v_accvgpr_read_b32 v164, a208
v_accvgpr_read_b32 v165, a209
v_accvgpr_read_b32 v166, a210
v_accvgpr_read_b32 v167, a211
v_accvgpr_read_b32 v168, a212
v_accvgpr_read_b32 v169, a213
v_accvgpr_read_b32 v170, a214
v_accvgpr_read_b32 v171, a215
v_pk_mul_f32 v[164:165], v[164:165], v[160:161]
v_pk_mul_f32 v[166:167], v[166:167], v[160:161]
v_pk_mul_f32 v[168:169], v[168:169], v[160:161]
v_pk_mul_f32 v[170:171], v[170:171], v[160:161]
v_cvt_pk_bf16_f32 v172, v164, v165
v_cvt_pk_bf16_f32 v173, v166, v167
v_cvt_pk_bf16_f32 v174, v168, v169
v_cvt_pk_bf16_f32 v175, v170, v171
buffer_store_dwordx4 v[172:175], v7, s[20:23], s41 offen offset:2048 sc1
v_accvgpr_read_b32 v176, a216
v_accvgpr_read_b32 v177, a217
v_accvgpr_read_b32 v178, a218
v_accvgpr_read_b32 v179, a219
v_accvgpr_read_b32 v180, a220
v_accvgpr_read_b32 v181, a221
v_accvgpr_read_b32 v182, a222
v_accvgpr_read_b32 v183, a223
v_pk_mul_f32 v[176:177], v[176:177], v[160:161]
v_pk_mul_f32 v[178:179], v[178:179], v[160:161]
v_pk_mul_f32 v[180:181], v[180:181], v[160:161]
v_pk_mul_f32 v[182:183], v[182:183], v[160:161]
v_cvt_pk_bf16_f32 v184, v176, v177
v_cvt_pk_bf16_f32 v185, v178, v179
v_cvt_pk_bf16_f32 v186, v180, v181
v_cvt_pk_bf16_f32 v187, v182, v183
buffer_store_dwordx4 v[184:187], v7, s[20:23], s41 offen offset:3072 sc1
v_accvgpr_read_b32 v164, a240
v_accvgpr_read_b32 v165, a241
v_accvgpr_read_b32 v166, a242
v_accvgpr_read_b32 v167, a243
v_accvgpr_read_b32 v168, a244
v_accvgpr_read_b32 v169, a245
v_accvgpr_read_b32 v170, a246
v_accvgpr_read_b32 v171, a247
v_pk_mul_f32 v[164:165], v[164:165], v[160:161]
v_pk_mul_f32 v[166:167], v[166:167], v[160:161]
v_pk_mul_f32 v[168:169], v[168:169], v[160:161]
v_pk_mul_f32 v[170:171], v[170:171], v[160:161]
v_cvt_pk_bf16_f32 v172, v164, v165
v_cvt_pk_bf16_f32 v173, v166, v167
v_cvt_pk_bf16_f32 v174, v168, v169
v_cvt_pk_bf16_f32 v175, v170, v171
buffer_store_dwordx4 v[172:175], v51, s[20:23], s41 offen offset:0 sc1
v_accvgpr_read_b32 v176, a248
v_accvgpr_read_b32 v177, a249
v_accvgpr_read_b32 v178, a250
v_accvgpr_read_b32 v179, a251
v_accvgpr_read_b32 v180, a252
v_accvgpr_read_b32 v181, a253
v_accvgpr_read_b32 v182, a254
v_accvgpr_read_b32 v183, a255
v_pk_mul_f32 v[176:177], v[176:177], v[160:161]
v_pk_mul_f32 v[178:179], v[178:179], v[160:161]
v_pk_mul_f32 v[180:181], v[180:181], v[160:161]
v_pk_mul_f32 v[182:183], v[182:183], v[160:161]
v_cvt_pk_bf16_f32 v184, v176, v177
v_cvt_pk_bf16_f32 v185, v178, v179
v_cvt_pk_bf16_f32 v186, v180, v181
v_cvt_pk_bf16_f32 v187, v182, v183
buffer_store_dwordx4 v[184:187], v51, s[20:23], s41 offen offset:1024 sc1
v_accvgpr_read_b32 v164, a96
v_accvgpr_read_b32 v165, a97
v_accvgpr_read_b32 v166, a98
v_accvgpr_read_b32 v167, a99
v_accvgpr_read_b32 v168, a100
v_accvgpr_read_b32 v169, a101
v_accvgpr_read_b32 v170, a102
v_accvgpr_read_b32 v171, a103
v_pk_mul_f32 v[164:165], v[164:165], v[160:161]
v_pk_mul_f32 v[166:167], v[166:167], v[160:161]
v_pk_mul_f32 v[168:169], v[168:169], v[160:161]
v_pk_mul_f32 v[170:171], v[170:171], v[160:161]
v_cvt_pk_bf16_f32 v172, v164, v165
v_cvt_pk_bf16_f32 v173, v166, v167
v_cvt_pk_bf16_f32 v174, v168, v169
v_cvt_pk_bf16_f32 v175, v170, v171
buffer_store_dwordx4 v[172:175], v51, s[20:23], s41 offen offset:2048 sc1
v_accvgpr_read_b32 v176, a104
v_accvgpr_read_b32 v177, a105
v_accvgpr_read_b32 v178, a106
v_accvgpr_read_b32 v179, a107
v_accvgpr_read_b32 v180, a108
v_accvgpr_read_b32 v181, a109
v_accvgpr_read_b32 v182, a110
v_accvgpr_read_b32 v183, a111
v_pk_mul_f32 v[176:177], v[176:177], v[160:161]
v_pk_mul_f32 v[178:179], v[178:179], v[160:161]
v_pk_mul_f32 v[180:181], v[180:181], v[160:161]
v_pk_mul_f32 v[182:183], v[182:183], v[160:161]
v_cvt_pk_bf16_f32 v184, v176, v177
v_cvt_pk_bf16_f32 v185, v178, v179
v_cvt_pk_bf16_f32 v186, v180, v181
v_cvt_pk_bf16_f32 v187, v182, v183
buffer_store_dwordx4 v[184:187], v51, s[20:23], s41 offen offset:3072 sc1
s_mov_b32 s41, -1
s_setpc_b64 s[30:31]
.Lfb_ret2:
s_cmp_eq_u32 s3, 0
s_cbranch_scc0 .LEend1
s_mov_b64 exec, 0
s_branch .Lpad13
.Ltch3:
s_branch .Lpad14
.Ltch4:
s_branch .Lpad15
.Ltch5:
s_branch .Lpad16
.Ltch6:
s_mov_b64 exec, -1
s_branch .Lfb_ret
.LEend1:
s_cmp_eq_u32 s3, 1
s_cbranch_scc0 .Lfb_ret
s_mov_b64 exec, 0
s_branch .Lpad17
.Ltch7:
s_branch .Lpad18
.Ltch8:
s_mov_b64 exec, -1
.Lfb_ret:
s_cmp_lt_u32 s3, 2
s_waitcnt vmcnt(23)
s_barrier
ds_read_b128 a[128:131], v45
ds_read_b128 a[132:135], v43 offset:0
ds_read_b64_tr_b16 a[164:165], v47 offset:0
ds_read_b64_tr_b16 a[166:167], v47 offset:512
ds_read_b64_tr_b16 a[168:169], v48 offset:0
ds_read_b64_tr_b16 a[170:171], v48 offset:512
ds_read_b128 a[136:139], v43 offset:2048
ds_read_b128 a[140:143], v43 offset:4096
ds_read_b128 a[160:163], v43 offset:6144
ds_read_b64_tr_b16 a[172:173], v47 offset:2048
ds_read_b64_tr_b16 a[174:175], v47 offset:2560
ds_read_b64_tr_b16 a[192:193], v48 offset:2048
ds_read_b64_tr_b16 a[194:195], v48 offset:2560
s_cselect_b32 s39, 1, 0
s_waitcnt lgkmcnt(8)
ds_read_b64_tr_b16 a[196:197], v49 offset:4096
ds_read_b64_tr_b16 a[198:199], v49 offset:4608
ds_read_b64_tr_b16 a[200:201], v50 offset:4096
ds_read_b64_tr_b16 a[202:203], v50 offset:4608
s_waitcnt lgkmcnt(0)
v_mfma_f32_16x16x32_bf16 v[160:163], a[172:175], a[128:131], 0
s_add_u32 m0, s48, 32768
v_mfma_f32_16x16x32_bf16 v[164:167], a[192:195], a[128:131], 0
buffer_load_dwordx4 v40, s[16:19], s26 offen offset:0 lds
v_mfma_f32_16x16x32_bf16 v[200:203], a[128:131], a[196:199], 0
ds_read_b64_tr_b16 a[20:21], v47 offset:14336
ds_read_b64_tr_b16 a[22:23], v47 offset:14848
v_mfma_f32_16x16x32_bf16 v[204:207], a[128:131], a[200:203], 0
ds_read_b128 a[0:3], v46
v_mfma_f32_16x16x32_bf16 v[128:131], a[164:167], a[132:135], 0
buffer_load_dwordx4 v41, s[16:19], s26 offen offset:1024 lds
v_mfma_f32_16x16x32_bf16 v[132:135], a[168:171], a[132:135], 0
ds_read_b64_tr_b16 a[24:25], v48 offset:14336
ds_read_b64_tr_b16 a[26:27], v48 offset:14848
v_mfma_f32_16x16x32_bf16 v[168:171], a[172:175], a[132:135], 0
ds_read_b128 a[4:7], v44 offset:0
v_mfma_f32_16x16x32_bf16 v[172:175], a[192:195], a[132:135], 0
buffer_load_dwordx4 v40, s[16:19], s26 offen offset:2048 lds
v_mfma_f32_16x16x32_bf16 v[208:211], a[132:135], a[196:199], 0
ds_read_b64_tr_b16 a[28:29], v47 offset:16384
ds_read_b64_tr_b16 a[30:31], v47 offset:16896
v_mfma_f32_16x16x32_bf16 v[212:215], a[132:135], a[200:203], 0
ds_read_b128 a[8:11], v44 offset:2048
v_mfma_f32_16x16x32_bf16 v[136:139], a[164:167], a[136:139], 0
buffer_load_dwordx4 v41, s[16:19], s26 offen offset:3072 lds
v_mfma_f32_16x16x32_bf16 v[140:143], a[168:171], a[136:139], 0
ds_read_b64_tr_b16 a[32:33], v48 offset:16384
ds_read_b64_tr_b16 a[34:35], v48 offset:16896
v_mfma_f32_16x16x32_bf16 v[176:179], a[172:175], a[136:139], 0
s_add_u32 m0, s48, 36864
ds_read_b128 a[12:15], v44 offset:4096
v_mfma_f32_16x16x32_bf16 v[180:183], a[192:195], a[136:139], 0
buffer_load_dwordx4 v40, s[16:19], s47 offen offset:0 lds
v_mfma_f32_16x16x32_bf16 v[216:219], a[136:139], a[196:199], 0
ds_read_b64_tr_b16 a[36:37], v49 offset:18432
ds_read_b64_tr_b16 a[38:39], v49 offset:18944
v_mfma_f32_16x16x32_bf16 v[220:223], a[136:139], a[200:203], 0
ds_read_b128 a[16:19], v44 offset:6144
v_mfma_f32_16x16x32_bf16 v[144:147], a[164:167], a[140:143], 0
buffer_load_dwordx4 v41, s[16:19], s47 offen offset:1024 lds
v_mfma_f32_16x16x32_bf16 v[148:151], a[168:171], a[140:143], 0
ds_read_b64_tr_b16 a[40:41], v50 offset:18432
ds_read_b64_tr_b16 a[42:43], v50 offset:18944
v_mfma_f32_16x16x32_bf16 v[184:187], a[172:175], a[140:143], 0
s_cmp_eq_u32 s39, 1
s_mov_b32 s28, 16384
s_cselect_b32 s28, 4294934528, s28
v_mfma_f32_16x16x32_bf16 v[188:191], a[192:195], a[140:143], 0
buffer_load_dwordx4 v42, s[12:15], s25 offen offset:2048 lds
v_mfma_f32_16x16x32_bf16 v[224:227], a[140:143], a[196:199], 0
s_add_u32 s26, s26, 393216
s_add_u32 s47, s47, 393216
s_add_u32 s25, s25, 128
v_mfma_f32_16x16x32_bf16 v[228:231], a[140:143], a[200:203], 0
s_add_u32 m0, s91, 16384
v_add_u32 v43, 16384, v43
v_add_u32 v44, 16384, v44
v_mfma_f32_16x16x32_bf16 v[152:155], a[164:167], a[160:163], 0
buffer_load_dwordx4 v36, s[12:15], s24 offen offset:0 lds
v_mfma_f32_16x16x32_bf16 v[156:159], a[168:171], a[160:163], 0
v_add_u32 v47, 4294934528, v47
v_add_u32 v48, 4294934528, v48
v_mfma_f32_16x16x32_bf16 v[192:195], a[172:175], a[160:163], 0
buffer_load_dwordx4 v37, s[12:15], s24 offen offset:1024 lds
v_mfma_f32_16x16x32_bf16 v[196:199], a[192:195], a[160:163], 0
v_add_u32 v49, 4294934528, v49
v_add_u32 v50, 4294934528, v50
v_mfma_f32_16x16x32_bf16 v[232:235], a[160:163], a[196:199], 0
buffer_load_dwordx4 v38, s[12:15], s24 offen offset:2048 lds
v_mfma_f32_16x16x32_bf16 v[236:239], a[160:163], a[200:203], 0
s_waitcnt vmcnt(19) lgkmcnt(0)
s_barrier
v_add_u32 v45, s28, v45
v_add_u32 v46, s28, v46
v_mfma_f32_16x16x32_bf16 v[160:163], a[28:31], a[0:3], v[160:163]
buffer_load_dwordx4 v39, s[12:15], s24 offen offset:3072 lds
v_mfma_f32_16x16x32_bf16 v[164:167], a[32:35], a[0:3], v[164:167]
s_add_u32 s24, s24, 128
v_mfma_f32_16x16x32_bf16 v[200:203], a[0:3], a[36:39], v[200:203]
s_add_u32 m0, s48, 131072
v_mfma_f32_16x16x32_bf16 v[204:207], a[0:3], a[40:43], v[204:207]
buffer_load_dwordx4 v40, s[16:19], s26 offen offset:0 lds
v_mfma_f32_16x16x32_bf16 v[128:131], a[20:23], a[4:7], v[128:131]
ds_read_b64_tr_b16 a[164:165], v47 offset:0
ds_read_b64_tr_b16 a[166:167], v47 offset:512
v_mfma_f32_16x16x32_bf16 v[132:135], a[24:27], a[4:7], v[132:135]
ds_read_b128 a[128:131], v45
v_mfma_f32_16x16x32_bf16 v[168:171], a[28:31], a[4:7], v[168:171]
ds_read_b64_tr_b16 a[168:169], v48 offset:0
ds_read_b64_tr_b16 a[170:171], v48 offset:512
v_mfma_f32_16x16x32_bf16 v[172:175], a[32:35], a[4:7], v[172:175]
buffer_load_dwordx4 v41, s[16:19], s26 offen offset:1024 lds
v_mfma_f32_16x16x32_bf16 v[208:211], a[4:7], a[36:39], v[208:211]
ds_read_b128 a[132:135], v43 offset:0
v_mfma_f32_16x16x32_bf16 v[212:215], a[4:7], a[40:43], v[212:215]
ds_read_b64_tr_b16 a[172:173], v47 offset:2048
ds_read_b64_tr_b16 a[174:175], v47 offset:2560
v_mfma_f32_16x16x32_bf16 v[136:139], a[20:23], a[8:11], v[136:139]
ds_read_b128 a[136:139], v43 offset:2048
v_mfma_f32_16x16x32_bf16 v[140:143], a[24:27], a[8:11], v[140:143]
ds_read_b64_tr_b16 a[192:193], v48 offset:2048
ds_read_b64_tr_b16 a[194:195], v48 offset:2560
v_mfma_f32_16x16x32_bf16 v[176:179], a[28:31], a[8:11], v[176:179]
buffer_load_dwordx4 v40, s[16:19], s26 offen offset:2048 lds
v_mfma_f32_16x16x32_bf16 v[180:183], a[32:35], a[8:11], v[180:183]
ds_read_b128 a[140:143], v43 offset:4096
v_mfma_f32_16x16x32_bf16 v[216:219], a[8:11], a[36:39], v[216:219]
ds_read_b64_tr_b16 a[196:197], v49 offset:4096
ds_read_b64_tr_b16 a[198:199], v49 offset:4608
v_mfma_f32_16x16x32_bf16 v[220:223], a[8:11], a[40:43], v[220:223]
ds_read_b128 a[160:163], v43 offset:6144
v_mfma_f32_16x16x32_bf16 v[144:147], a[20:23], a[12:15], v[144:147]
ds_read_b64_tr_b16 a[200:201], v50 offset:4096
ds_read_b64_tr_b16 a[202:203], v50 offset:4608
v_mfma_f32_16x16x32_bf16 v[148:151], a[24:27], a[12:15], v[148:151]
buffer_load_dwordx4 v41, s[16:19], s26 offen offset:3072 lds
v_mfma_f32_16x16x32_bf16 v[184:187], a[28:31], a[12:15], v[184:187]
v_mfma_f32_16x16x32_bf16 v[188:191], a[32:35], a[12:15], v[188:191]
v_mfma_f32_16x16x32_bf16 v[224:227], a[12:15], a[36:39], v[224:227]
v_mfma_f32_16x16x32_bf16 v[228:231], a[12:15], a[40:43], v[228:231]
s_add_u32 m0, s48, 135168
v_mfma_f32_16x16x32_bf16 v[152:155], a[20:23], a[16:19], v[152:155]
buffer_load_dwordx4 v40, s[16:19], s47 offen offset:0 lds
v_mfma_f32_16x16x32_bf16 v[156:159], a[24:27], a[16:19], v[156:159]
v_mfma_f32_16x16x32_bf16 v[192:195], a[28:31], a[16:19], v[192:195]
v_mfma_f32_16x16x32_bf16 v[196:199], a[32:35], a[16:19], v[196:199]
v_mfma_f32_16x16x32_bf16 v[232:235], a[16:19], a[36:39], v[232:235]
buffer_load_dwordx4 v41, s[16:19], s47 offen offset:1024 lds
v_mfma_f32_16x16x32_bf16 v[236:239], a[16:19], a[40:43], v[236:239]
s_waitcnt lgkmcnt(0)
v_mfma_f32_16x16x32_bf16 v[160:163], a[172:175], a[128:131], v[160:163]
ds_read_b64_tr_b16 a[20:21], v47 offset:14336
ds_read_b64_tr_b16 a[22:23], v47 offset:14848
v_mfma_f32_16x16x32_bf16 v[164:167], a[192:195], a[128:131], v[164:167]
ds_read_b128 a[0:3], v46
v_mfma_f32_16x16x32_bf16 v[200:203], a[128:131], a[196:199], v[200:203]
ds_read_b64_tr_b16 a[24:25], v48 offset:14336
ds_read_b64_tr_b16 a[26:27], v48 offset:14848
v_mfma_f32_16x16x32_bf16 v[204:207], a[128:131], a[200:203], v[204:207]
buffer_load_dwordx4 v42, s[12:15], s25 offen offset:2048 lds
v_mfma_f32_16x16x32_bf16 v[128:131], a[164:167], a[132:135], v[128:131]
s_add_u32 s26, s26, 393216
s_add_u32 s47, s47, 393216
v_mfma_f32_16x16x32_bf16 v[132:135], a[168:171], a[132:135], v[132:135]
s_add_u32 s25, s25, 128
ds_read_b128 a[4:7], v44 offset:0
v_mfma_f32_16x16x32_bf16 v[168:171], a[172:175], a[132:135], v[168:171]
ds_read_b64_tr_b16 a[28:29], v47 offset:16384
ds_read_b64_tr_b16 a[30:31], v47 offset:16896
v_mfma_f32_16x16x32_bf16 v[172:175], a[192:195], a[132:135], v[172:175]
ds_read_b128 a[8:11], v44 offset:2048
v_mfma_f32_16x16x32_bf16 v[208:211], a[132:135], a[196:199], v[208:211]
s_add_u32 m0, s91, 65536
v_mfma_f32_16x16x32_bf16 v[212:215], a[132:135], a[200:203], v[212:215]
buffer_load_dwordx4 v36, s[12:15], s24 offen offset:0 lds
v_mfma_f32_16x16x32_bf16 v[136:139], a[164:167], a[136:139], v[136:139]
ds_read_b64_tr_b16 a[32:33], v48 offset:16384
ds_read_b64_tr_b16 a[34:35], v48 offset:16896
v_mfma_f32_16x16x32_bf16 v[140:143], a[168:171], a[136:139], v[140:143]
ds_read_b128 a[12:15], v44 offset:4096
v_mfma_f32_16x16x32_bf16 v[176:179], a[172:175], a[136:139], v[176:179]
ds_read_b64_tr_b16 a[36:37], v49 offset:18432
ds_read_b64_tr_b16 a[38:39], v49 offset:18944
v_mfma_f32_16x16x32_bf16 v[180:183], a[192:195], a[136:139], v[180:183]
ds_read_b128 a[16:19], v44 offset:6144
v_mfma_f32_16x16x32_bf16 v[216:219], a[136:139], a[196:199], v[216:219]
buffer_load_dwordx4 v37, s[12:15], s24 offen offset:1024 lds
v_mfma_f32_16x16x32_bf16 v[220:223], a[136:139], a[200:203], v[220:223]
ds_read_b64_tr_b16 a[40:41], v50 offset:18432
ds_read_b64_tr_b16 a[42:43], v50 offset:18944
v_mfma_f32_16x16x32_bf16 v[144:147], a[164:167], a[140:143], v[144:147]
s_cmp_eq_u32 s39, 1
s_mov_b32 s28, 4294885376
s_cselect_b32 s28, 4294901760, s28
v_mfma_f32_16x16x32_bf16 v[148:151], a[168:171], a[140:143], v[148:151]
v_add_u32 v43, 4294885376, v43
v_add_u32 v44, 4294885376, v44
v_mfma_f32_16x16x32_bf16 v[184:187], a[172:175], a[140:143], v[184:187]
v_add_u32 v47, 4294901760, v47
v_add_u32 v48, 4294901760, v48
v_mfma_f32_16x16x32_bf16 v[188:191], a[192:195], a[140:143], v[188:191]
buffer_load_dwordx4 v38, s[12:15], s24 offen offset:2048 lds
v_mfma_f32_16x16x32_bf16 v[224:227], a[140:143], a[196:199], v[224:227]
v_add_u32 v49, 4294901760, v49
v_add_u32 v50, 4294901760, v50
v_mfma_f32_16x16x32_bf16 v[228:231], a[140:143], a[200:203], v[228:231]
v_add_u32 v45, s28, v45
v_add_u32 v46, s28, v46
v_mfma_f32_16x16x32_bf16 v[152:155], a[164:167], a[160:163], v[152:155]
v_mfma_f32_16x16x32_bf16 v[156:159], a[168:171], a[160:163], v[156:159]
v_mfma_f32_16x16x32_bf16 v[192:195], a[172:175], a[160:163], v[192:195]
buffer_load_dwordx4 v39, s[12:15], s24 offen offset:3072 lds
v_mfma_f32_16x16x32_bf16 v[196:199], a[192:195], a[160:163], v[196:199]
s_add_u32 s24, s24, 128
v_mfma_f32_16x16x32_bf16 v[232:235], a[160:163], a[196:199], v[232:235]
v_mfma_f32_16x16x32_bf16 v[236:239], a[160:163], a[200:203], v[236:239]
s_waitcnt vmcnt(15) lgkmcnt(0)
s_barrier
v_mfma_f32_16x16x32_bf16 v[160:163], a[28:31], a[0:3], v[160:163]
ds_read_b64_tr_b16 a[164:165], v47 offset:0
ds_read_b64_tr_b16 a[166:167], v47 offset:512
v_mfma_f32_16x16x32_bf16 v[164:167], a[32:35], a[0:3], v[164:167]
s_add_u32 m0, s48, 98304
ds_read_b128 a[128:131], v45
.Lpad14:
s_cbranch_execz .Ltch4
v_mfma_f32_16x16x32_bf16 v[200:203], a[0:3], a[36:39], v[200:203]
buffer_load_dwordx4 v40, s[16:19], s26 offen offset:0 lds
v_mfma_f32_16x16x32_bf16 v[204:207], a[0:3], a[40:43], v[204:207]
ds_read_b64_tr_b16 a[168:169], v48 offset:0
ds_read_b64_tr_b16 a[170:171], v48 offset:512
v_mfma_f32_16x16x32_bf16 v[128:131], a[20:23], a[4:7], v[128:131]
ds_read_b128 a[132:135], v43 offset:0
v_mfma_f32_16x16x32_bf16 v[132:135], a[24:27], a[4:7], v[132:135]
ds_read_b64_tr_b16 a[172:173], v47 offset:2048
ds_read_b64_tr_b16 a[174:175], v47 offset:2560
v_mfma_f32_16x16x32_bf16 v[168:171], a[28:31], a[4:7], v[168:171]
ds_read_b128 a[136:139], v43 offset:2048
v_mfma_f32_16x16x32_bf16 v[172:175], a[32:35], a[4:7], v[172:175]
buffer_load_dwordx4 v41, s[16:19], s26 offen offset:1024 lds
v_mfma_f32_16x16x32_bf16 v[208:211], a[4:7], a[36:39], v[208:211]
ds_read_b64_tr_b16 a[192:193], v48 offset:2048
ds_read_b64_tr_b16 a[194:195], v48 offset:2560
v_mfma_f32_16x16x32_bf16 v[212:215], a[4:7], a[40:43], v[212:215]
ds_read_b128 a[140:143], v43 offset:4096
v_mfma_f32_16x16x32_bf16 v[136:139], a[20:23], a[8:11], v[136:139]
ds_read_b64_tr_b16 a[196:197], v49 offset:4096
ds_read_b64_tr_b16 a[198:199], v49 offset:4608
v_mfma_f32_16x16x32_bf16 v[140:143], a[24:27], a[8:11], v[140:143]
ds_read_b128 a[160:163], v43 offset:6144
v_mfma_f32_16x16x32_bf16 v[176:179], a[28:31], a[8:11], v[176:179]
buffer_load_dwordx4 v40, s[16:19], s26 offen offset:2048 lds
v_mfma_f32_16x16x32_bf16 v[180:183], a[32:35], a[8:11], v[180:183]
ds_read_b64_tr_b16 a[200:201], v50 offset:4096
ds_read_b64_tr_b16 a[202:203], v50 offset:4608
v_mfma_f32_16x16x32_bf16 v[216:219], a[8:11], a[36:39], v[216:219]
v_mfma_f32_16x16x32_bf16 v[220:223], a[8:11], a[40:43], v[220:223]
v_mfma_f32_16x16x32_bf16 v[144:147], a[20:23], a[12:15], v[144:147]
v_mfma_f32_16x16x32_bf16 v[148:151], a[24:27], a[12:15], v[148:151]
buffer_load_dwordx4 v41, s[16:19], s26 offen offset:3072 lds
v_mfma_f32_16x16x32_bf16 v[184:187], a[28:31], a[12:15], v[184:187]
v_mfma_f32_16x16x32_bf16 v[188:191], a[32:35], a[12:15], v[188:191]
v_mfma_f32_16x16x32_bf16 v[224:227], a[12:15], a[36:39], v[224:227]
v_mfma_f32_16x16x32_bf16 v[228:231], a[12:15], a[40:43], v[228:231]
s_add_u32 m0, s48, 102400
v_mfma_f32_16x16x32_bf16 v[152:155], a[20:23], a[16:19], v[152:155]
buffer_load_dwordx4 v40, s[16:19], s47 offen offset:0 lds
v_mfma_f32_16x16x32_bf16 v[156:159], a[24:27], a[16:19], v[156:159]
v_mfma_f32_16x16x32_bf16 v[192:195], a[28:31], a[16:19], v[192:195]
v_mfma_f32_16x16x32_bf16 v[196:199], a[32:35], a[16:19], v[196:199]
v_mfma_f32_16x16x32_bf16 v[232:235], a[16:19], a[36:39], v[232:235]
buffer_load_dwordx4 v41, s[16:19], s47 offen offset:1024 lds
v_mfma_f32_16x16x32_bf16 v[236:239], a[16:19], a[40:43], v[236:239]
s_waitcnt lgkmcnt(0)
v_mfma_f32_16x16x32_bf16 v[160:163], a[172:175], a[128:131], v[160:163]
ds_read_b64_tr_b16 a[20:21], v47 offset:14336
ds_read_b64_tr_b16 a[22:23], v47 offset:14848
v_mfma_f32_16x16x32_bf16 v[164:167], a[192:195], a[128:131], v[164:167]
ds_read_b128 a[0:3], v46
v_mfma_f32_16x16x32_bf16 v[200:203], a[128:131], a[196:199], v[200:203]
ds_read_b64_tr_b16 a[24:25], v48 offset:14336
ds_read_b64_tr_b16 a[26:27], v48 offset:14848
v_mfma_f32_16x16x32_bf16 v[204:207], a[128:131], a[200:203], v[204:207]
buffer_load_dwordx4 v42, s[12:15], s25 offen offset:2048 lds
v_mfma_f32_16x16x32_bf16 v[128:131], a[164:167], a[132:135], v[128:131]
s_add_u32 s26, s26, 393216
s_add_u32 s47, s47, 393216
v_mfma_f32_16x16x32_bf16 v[132:135], a[168:171], a[132:135], v[132:135]
s_add_u32 s25, s25, 128
ds_read_b128 a[4:7], v44 offset:0
v_mfma_f32_16x16x32_bf16 v[168:171], a[172:175], a[132:135], v[168:171]
ds_read_b64_tr_b16 a[28:29], v47 offset:16384
ds_read_b64_tr_b16 a[30:31], v47 offset:16896
v_mfma_f32_16x16x32_bf16 v[172:175], a[192:195], a[132:135], v[172:175]
ds_read_b128 a[8:11], v44 offset:2048
v_mfma_f32_16x16x32_bf16 v[208:211], a[132:135], a[196:199], v[208:211]
s_add_u32 m0, s91, 81920
v_mfma_f32_16x16x32_bf16 v[212:215], a[132:135], a[200:203], v[212:215]
buffer_load_dwordx4 v36, s[12:15], s24 offen offset:0 lds
v_mfma_f32_16x16x32_bf16 v[136:139], a[164:167], a[136:139], v[136:139]
ds_read_b64_tr_b16 a[32:33], v48 offset:16384
ds_read_b64_tr_b16 a[34:35], v48 offset:16896
v_mfma_f32_16x16x32_bf16 v[140:143], a[168:171], a[136:139], v[140:143]
ds_read_b128 a[12:15], v44 offset:4096
v_mfma_f32_16x16x32_bf16 v[176:179], a[172:175], a[136:139], v[176:179]
ds_read_b64_tr_b16 a[36:37], v49 offset:18432
ds_read_b64_tr_b16 a[38:39], v49 offset:18944
v_mfma_f32_16x16x32_bf16 v[180:183], a[192:195], a[136:139], v[180:183]
ds_read_b128 a[16:19], v44 offset:6144
v_mfma_f32_16x16x32_bf16 v[216:219], a[136:139], a[196:199], v[216:219]
buffer_load_dwordx4 v37, s[12:15], s24 offen offset:1024 lds
v_mfma_f32_16x16x32_bf16 v[220:223], a[136:139], a[200:203], v[220:223]
ds_read_b64_tr_b16 a[40:41], v50 offset:18432
ds_read_b64_tr_b16 a[42:43], v50 offset:18944
v_mfma_f32_16x16x32_bf16 v[144:147], a[164:167], a[140:143], v[144:147]
s_cmp_eq_u32 s39, 1
s_mov_b32 s28, 16384
s_cselect_b32 s28, 98304, s28
v_mfma_f32_16x16x32_bf16 v[148:151], a[168:171], a[140:143], v[148:151]
v_add_u32 v43, 16384, v43
v_add_u32 v44, 16384, v44
v_mfma_f32_16x16x32_bf16 v[184:187], a[172:175], a[140:143], v[184:187]
v_add_u32 v47, 98304, v47
v_add_u32 v48, 98304, v48
v_mfma_f32_16x16x32_bf16 v[188:191], a[192:195], a[140:143], v[188:191]
buffer_load_dwordx4 v38, s[12:15], s24 offen offset:2048 lds
v_mfma_f32_16x16x32_bf16 v[224:227], a[140:143], a[196:199], v[224:227]
v_add_u32 v49, 98304, v49
v_add_u32 v50, 98304, v50
v_mfma_f32_16x16x32_bf16 v[228:231], a[140:143], a[200:203], v[228:231]
v_add_u32 v45, s28, v45
v_add_u32 v46, s28, v46
v_mfma_f32_16x16x32_bf16 v[152:155], a[164:167], a[160:163], v[152:155]
v_mfma_f32_16x16x32_bf16 v[156:159], a[168:171], a[160:163], v[156:159]
v_mfma_f32_16x16x32_bf16 v[192:195], a[172:175], a[160:163], v[192:195]
buffer_load_dwordx4 v39, s[12:15], s24 offen offset:3072 lds
v_mfma_f32_16x16x32_bf16 v[196:199], a[192:195], a[160:163], v[196:199]
s_add_u32 s24, s24, 128
v_mfma_f32_16x16x32_bf16 v[232:235], a[160:163], a[196:199], v[232:235]
v_or_b32 v0, 3, v11
v_mfma_f32_16x16x32_bf16 v[236:239], a[160:163], a[200:203], v[236:239]
s_waitcnt vmcnt(33)
s_waitcnt vmcnt(15) lgkmcnt(0)
s_barrier
v_mfma_f32_16x16x32_bf16 v[160:163], a[28:31], a[0:3], v[160:163]
buffer_store_dword v0, v6, s[60:63], 0 offen sc1
ds_read_b64_tr_b16 a[164:165], v47 offset:0
ds_read_b64_tr_b16 a[166:167], v47 offset:512
v_mfma_f32_16x16x32_bf16 v[164:167], a[32:35], a[0:3], v[164:167]
s_add_u32 m0, s48, 32768
ds_read_b128 a[128:131], v45
v_mfma_f32_16x16x32_bf16 v[200:203], a[0:3], a[36:39], v[200:203]
buffer_load_dwordx4 v40, s[16:19], s26 offen offset:0 lds
v_mfma_f32_16x16x32_bf16 v[204:207], a[0:3], a[40:43], v[204:207]
ds_read_b64_tr_b16 a[168:169], v48 offset:0
ds_read_b64_tr_b16 a[170:171], v48 offset:512
v_mfma_f32_16x16x32_bf16 v[128:131], a[20:23], a[4:7], v[128:131]
ds_read_b128 a[132:135], v43 offset:0
v_mfma_f32_16x16x32_bf16 v[132:135], a[24:27], a[4:7], v[132:135]
ds_read_b64_tr_b16 a[172:173], v47 offset:2048
ds_read_b64_tr_b16 a[174:175], v47 offset:2560
v_mfma_f32_16x16x32_bf16 v[168:171], a[28:31], a[4:7], v[168:171]
ds_read_b128 a[136:139], v43 offset:2048
v_mfma_f32_16x16x32_bf16 v[172:175], a[32:35], a[4:7], v[172:175]
buffer_load_dwordx4 v41, s[16:19], s26 offen offset:1024 lds
v_mfma_f32_16x16x32_bf16 v[208:211], a[4:7], a[36:39], v[208:211]
ds_read_b64_tr_b16 a[192:193], v48 offset:2048
ds_read_b64_tr_b16 a[194:195], v48 offset:2560
v_mfma_f32_16x16x32_bf16 v[212:215], a[4:7], a[40:43], v[212:215]
ds_read_b128 a[140:143], v43 offset:4096
v_mfma_f32_16x16x32_bf16 v[136:139], a[20:23], a[8:11], v[136:139]
ds_read_b64_tr_b16 a[196:197], v49 offset:4096
ds_read_b64_tr_b16 a[198:199], v49 offset:4608
v_mfma_f32_16x16x32_bf16 v[140:143], a[24:27], a[8:11], v[140:143]
ds_read_b128 a[160:163], v43 offset:6144
v_mfma_f32_16x16x32_bf16 v[176:179], a[28:31], a[8:11], v[176:179]
buffer_load_dwordx4 v40, s[16:19], s26 offen offset:2048 lds
v_mfma_f32_16x16x32_bf16 v[180:183], a[32:35], a[8:11], v[180:183]
ds_read_b64_tr_b16 a[200:201], v50 offset:4096
ds_read_b64_tr_b16 a[202:203], v50 offset:4608
v_mfma_f32_16x16x32_bf16 v[216:219], a[8:11], a[36:39], v[216:219]
v_mfma_f32_16x16x32_bf16 v[220:223], a[8:11], a[40:43], v[220:223]
v_mfma_f32_16x16x32_bf16 v[144:147], a[20:23], a[12:15], v[144:147]
v_mfma_f32_16x16x32_bf16 v[148:151], a[24:27], a[12:15], v[148:151]
buffer_load_dwordx4 v41, s[16:19], s26 offen offset:3072 lds
v_mfma_f32_16x16x32_bf16 v[184:187], a[28:31], a[12:15], v[184:187]
v_mfma_f32_16x16x32_bf16 v[188:191], a[32:35], a[12:15], v[188:191]
v_mfma_f32_16x16x32_bf16 v[224:227], a[12:15], a[36:39], v[224:227]
v_mfma_f32_16x16x32_bf16 v[228:231], a[12:15], a[40:43], v[228:231]
s_add_u32 m0, s48, 36864
v_mfma_f32_16x16x32_bf16 v[152:155], a[20:23], a[16:19], v[152:155]
buffer_load_dwordx4 v40, s[16:19], s47 offen offset:0 lds
v_mfma_f32_16x16x32_bf16 v[156:159], a[24:27], a[16:19], v[156:159]
v_mfma_f32_16x16x32_bf16 v[192:195], a[28:31], a[16:19], v[192:195]
v_mfma_f32_16x16x32_bf16 v[196:199], a[32:35], a[16:19], v[196:199]
v_mfma_f32_16x16x32_bf16 v[232:235], a[16:19], a[36:39], v[232:235]
buffer_load_dwordx4 v41, s[16:19], s47 offen offset:1024 lds
v_mfma_f32_16x16x32_bf16 v[236:239], a[16:19], a[40:43], v[236:239]
s_waitcnt lgkmcnt(0)
v_mfma_f32_16x16x32_bf16 v[160:163], a[172:175], a[128:131], v[160:163]
ds_read_b64_tr_b16 a[20:21], v47 offset:14336
ds_read_b64_tr_b16 a[22:23], v47 offset:14848
v_mfma_f32_16x16x32_bf16 v[164:167], a[192:195], a[128:131], v[164:167]
ds_read_b128 a[0:3], v46
v_mfma_f32_16x16x32_bf16 v[200:203], a[128:131], a[196:199], v[200:203]
ds_read_b64_tr_b16 a[24:25], v48 offset:14336
ds_read_b64_tr_b16 a[26:27], v48 offset:14848
v_mfma_f32_16x16x32_bf16 v[204:207], a[128:131], a[200:203], v[204:207]
buffer_load_dwordx4 v42, s[12:15], s25 offen offset:2048 lds
v_mfma_f32_16x16x32_bf16 v[128:131], a[164:167], a[132:135], v[128:131]
s_add_u32 s26, s26, 393216
s_add_u32 s47, s47, 393216
v_mfma_f32_16x16x32_bf16 v[132:135], a[168:171], a[132:135], v[132:135]
s_add_u32 s25, s25, 128
ds_read_b128 a[4:7], v44 offset:0
v_mfma_f32_16x16x32_bf16 v[168:171], a[172:175], a[132:135], v[168:171]
ds_read_b64_tr_b16 a[28:29], v47 offset:16384
ds_read_b64_tr_b16 a[30:31], v47 offset:16896
v_mfma_f32_16x16x32_bf16 v[172:175], a[192:195], a[132:135], v[172:175]
ds_read_b128 a[8:11], v44 offset:2048
v_mfma_f32_16x16x32_bf16 v[208:211], a[132:135], a[196:199], v[208:211]
s_add_u32 m0, s91, 0
v_mfma_f32_16x16x32_bf16 v[212:215], a[132:135], a[200:203], v[212:215]
buffer_load_dwordx4 v36, s[12:15], s24 offen offset:0 lds
v_mfma_f32_16x16x32_bf16 v[136:139], a[164:167], a[136:139], v[136:139]
ds_read_b64_tr_b16 a[32:33], v48 offset:16384
ds_read_b64_tr_b16 a[34:35], v48 offset:16896
v_mfma_f32_16x16x32_bf16 v[140:143], a[168:171], a[136:139], v[140:143]
ds_read_b128 a[12:15], v44 offset:4096
v_mfma_f32_16x16x32_bf16 v[176:179], a[172:175], a[136:139], v[176:179]
ds_read_b64_tr_b16 a[36:37], v49 offset:18432
ds_read_b64_tr_b16 a[38:39], v49 offset:18944
v_mfma_f32_16x16x32_bf16 v[180:183], a[192:195], a[136:139], v[180:183]
ds_read_b128 a[16:19], v44 offset:6144
v_mfma_f32_16x16x32_bf16 v[216:219], a[136:139], a[196:199], v[216:219]
buffer_load_dwordx4 v37, s[12:15], s24 offen offset:1024 lds
v_mfma_f32_16x16x32_bf16 v[220:223], a[136:139], a[200:203], v[220:223]
ds_read_b64_tr_b16 a[40:41], v50 offset:18432
ds_read_b64_tr_b16 a[42:43], v50 offset:18944
v_mfma_f32_16x16x32_bf16 v[144:147], a[164:167], a[140:143], v[144:147]
s_cmp_eq_u32 s39, 1
s_mov_b32 s28, 49152
s_cselect_b32 s28, 4294934528, s28
v_mfma_f32_16x16x32_bf16 v[148:151], a[168:171], a[140:143], v[148:151]
v_add_u32 v43, 49152, v43
v_add_u32 v44, 49152, v44
v_mfma_f32_16x16x32_bf16 v[184:187], a[172:175], a[140:143], v[184:187]
v_add_u32 v47, 4294934528, v47
v_add_u32 v48, 4294934528, v48
v_mfma_f32_16x16x32_bf16 v[188:191], a[192:195], a[140:143], v[188:191]
buffer_load_dwordx4 v38, s[12:15], s24 offen offset:2048 lds
v_mfma_f32_16x16x32_bf16 v[224:227], a[140:143], a[196:199], v[224:227]
v_add_u32 v49, 4294934528, v49
v_add_u32 v50, 4294934528, v50
v_mfma_f32_16x16x32_bf16 v[228:231], a[140:143], a[200:203], v[228:231]
v_add_u32 v45, s28, v45
v_add_u32 v46, s28, v46
v_mfma_f32_16x16x32_bf16 v[152:155], a[164:167], a[160:163], v[152:155]
v_mfma_f32_16x16x32_bf16 v[156:159], a[168:171], a[160:163], v[156:159]
v_mfma_f32_16x16x32_bf16 v[192:195], a[172:175], a[160:163], v[192:195]
buffer_load_dwordx4 v39, s[12:15], s24 offen offset:3072 lds
v_mfma_f32_16x16x32_bf16 v[196:199], a[192:195], a[160:163], v[196:199]
s_add_u32 s24, s24, 128
v_mfma_f32_16x16x32_bf16 v[232:235], a[160:163], a[196:199], v[232:235]
v_mfma_f32_16x16x32_bf16 v[236:239], a[160:163], a[200:203], v[236:239]
s_waitcnt vmcnt(16) lgkmcnt(0)
s_barrier
v_mfma_f32_16x16x32_bf16 v[160:163], a[28:31], a[0:3], v[160:163]
ds_read_b64_tr_b16 a[164:165], v47 offset:0
ds_read_b64_tr_b16 a[166:167], v47 offset:512
v_mfma_f32_16x16x32_bf16 v[164:167], a[32:35], a[0:3], v[164:167]
s_add_u32 m0, s48, 131072
ds_read_b128 a[128:131], v45
v_mfma_f32_16x16x32_bf16 v[200:203], a[0:3], a[36:39], v[200:203]
buffer_load_dwordx4 v40, s[16:19], s26 offen offset:0 lds
v_mfma_f32_16x16x32_bf16 v[204:207], a[0:3], a[40:43], v[204:207]
ds_read_b64_tr_b16 a[168:169], v48 offset:0
ds_read_b64_tr_b16 a[170:171], v48 offset:512
v_mfma_f32_16x16x32_bf16 v[128:131], a[20:23], a[4:7], v[128:131]
ds_read_b128 a[132:135], v43 offset:0
v_mfma_f32_16x16x32_bf16 v[132:135], a[24:27], a[4:7], v[132:135]
ds_read_b64_tr_b16 a[172:173], v47 offset:2048
ds_read_b64_tr_b16 a[174:175], v47 offset:2560
v_mfma_f32_16x16x32_bf16 v[168:171], a[28:31], a[4:7], v[168:171]
ds_read_b128 a[136:139], v43 offset:2048
v_mfma_f32_16x16x32_bf16 v[172:175], a[32:35], a[4:7], v[172:175]
buffer_load_dwordx4 v41, s[16:19], s26 offen offset:1024 lds
v_mfma_f32_16x16x32_bf16 v[208:211], a[4:7], a[36:39], v[208:211]
ds_read_b64_tr_b16 a[192:193], v48 offset:2048
ds_read_b64_tr_b16 a[194:195], v48 offset:2560
v_mfma_f32_16x16x32_bf16 v[212:215], a[4:7], a[40:43], v[212:215]
ds_read_b128 a[140:143], v43 offset:4096
v_mfma_f32_16x16x32_bf16 v[136:139], a[20:23], a[8:11], v[136:139]
ds_read_b64_tr_b16 a[196:197], v49 offset:4096
ds_read_b64_tr_b16 a[198:199], v49 offset:4608
v_mfma_f32_16x16x32_bf16 v[140:143], a[24:27], a[8:11], v[140:143]
ds_read_b128 a[160:163], v43 offset:6144
v_mfma_f32_16x16x32_bf16 v[176:179], a[28:31], a[8:11], v[176:179]
buffer_load_dwordx4 v40, s[16:19], s26 offen offset:2048 lds
v_mfma_f32_16x16x32_bf16 v[180:183], a[32:35], a[8:11], v[180:183]
ds_read_b64_tr_b16 a[200:201], v50 offset:4096
ds_read_b64_tr_b16 a[202:203], v50 offset:4608
v_mfma_f32_16x16x32_bf16 v[216:219], a[8:11], a[36:39], v[216:219]
v_mfma_f32_16x16x32_bf16 v[220:223], a[8:11], a[40:43], v[220:223]
v_mfma_f32_16x16x32_bf16 v[144:147], a[20:23], a[12:15], v[144:147]
v_mfma_f32_16x16x32_bf16 v[148:151], a[24:27], a[12:15], v[148:151]
buffer_load_dwordx4 v41, s[16:19], s26 offen offset:3072 lds
v_mfma_f32_16x16x32_bf16 v[184:187], a[28:31], a[12:15], v[184:187]
v_mfma_f32_16x16x32_bf16 v[188:191], a[32:35], a[12:15], v[188:191]
v_mfma_f32_16x16x32_bf16 v[224:227], a[12:15], a[36:39], v[224:227]
v_mfma_f32_16x16x32_bf16 v[228:231], a[12:15], a[40:43], v[228:231]
s_add_u32 m0, s48, 135168
v_mfma_f32_16x16x32_bf16 v[152:155], a[20:23], a[16:19], v[152:155]
buffer_load_dwordx4 v40, s[16:19], s47 offen offset:0 lds
v_mfma_f32_16x16x32_bf16 v[156:159], a[24:27], a[16:19], v[156:159]
v_mfma_f32_16x16x32_bf16 v[192:195], a[28:31], a[16:19], v[192:195]
v_mfma_f32_16x16x32_bf16 v[196:199], a[32:35], a[16:19], v[196:199]
v_mfma_f32_16x16x32_bf16 v[232:235], a[16:19], a[36:39], v[232:235]
buffer_load_dwordx4 v41, s[16:19], s47 offen offset:1024 lds
v_mfma_f32_16x16x32_bf16 v[236:239], a[16:19], a[40:43], v[236:239]
s_waitcnt lgkmcnt(0)
v_mfma_f32_16x16x32_bf16 v[160:163], a[172:175], a[128:131], v[160:163]
ds_read_b64_tr_b16 a[20:21], v47 offset:14336
ds_read_b64_tr_b16 a[22:23], v47 offset:14848
v_mfma_f32_16x16x32_bf16 v[164:167], a[192:195], a[128:131], v[164:167]
s_lshl_b32 s54, s86, 7
ds_read_b128 a[0:3], v46
v_mfma_f32_16x16x32_bf16 v[200:203], a[128:131], a[196:199], v[200:203]
ds_read_b64_tr_b16 a[24:25], v48 offset:14336
ds_read_b64_tr_b16 a[26:27], v48 offset:14848
v_mfma_f32_16x16x32_bf16 v[204:207], a[128:131], a[200:203], v[204:207]
s_add_u32 s54, s54, 0x14000
buffer_load_dwordx4 v42, s[12:15], s25 offen offset:2048 lds
v_mfma_f32_16x16x32_bf16 v[128:131], a[164:167], a[132:135], v[128:131]
s_add_u32 s26, s26, 393216
s_add_u32 s47, s47, 393216
v_and_b32 v249, 7, v1
v_mfma_f32_16x16x32_bf16 v[132:135], a[168:171], a[132:135], v[132:135]
s_add_u32 s25, s25, 128
ds_read_b128 a[4:7], v44 offset:0
v_mfma_f32_16x16x32_bf16 v[168:171], a[172:175], a[132:135], v[168:171]
ds_read_b64_tr_b16 a[28:29], v47 offset:16384
ds_read_b64_tr_b16 a[30:31], v47 offset:16896
v_mfma_f32_16x16x32_bf16 v[172:175], a[192:195], a[132:135], v[172:175]
ds_read_b128 a[8:11], v44 offset:2048
v_lshl_add_u32 v249, v249, 2, s54
v_mfma_f32_16x16x32_bf16 v[208:211], a[132:135], a[196:199], v[208:211]
s_add_u32 m0, s91, 16384
v_mfma_f32_16x16x32_bf16 v[212:215], a[132:135], a[200:203], v[212:215]
buffer_load_dwordx4 v36, s[12:15], s24 offen offset:0 lds
v_mfma_f32_16x16x32_bf16 v[136:139], a[164:167], a[136:139], v[136:139]
ds_read_b64_tr_b16 a[32:33], v48 offset:16384
ds_read_b64_tr_b16 a[34:35], v48 offset:16896
v_mfma_f32_16x16x32_bf16 v[140:143], a[168:171], a[136:139], v[140:143]
ds_read_b128 a[12:15], v44 offset:4096
v_mfma_f32_16x16x32_bf16 v[176:179], a[172:175], a[136:139], v[176:179]
ds_read_b64_tr_b16 a[36:37], v49 offset:18432
ds_read_b64_tr_b16 a[38:39], v49 offset:18944
v_mfma_f32_16x16x32_bf16 v[180:183], a[192:195], a[136:139], v[180:183]
ds_read_b128 a[16:19], v44 offset:6144
v_mfma_f32_16x16x32_bf16 v[216:219], a[136:139], a[196:199], v[216:219]
buffer_load_dwordx4 v37, s[12:15], s24 offen offset:1024 lds
v_mfma_f32_16x16x32_bf16 v[220:223], a[136:139], a[200:203], v[220:223]
ds_read_b64_tr_b16 a[40:41], v50 offset:18432
ds_read_b64_tr_b16 a[42:43], v50 offset:18944
v_mfma_f32_16x16x32_bf16 v[144:147], a[164:167], a[140:143], v[144:147]
s_cmp_eq_u32 s39, 1
s_mov_b32 s28, 16384
s_cselect_b32 s28, 4294901760, s28
v_mfma_f32_16x16x32_bf16 v[148:151], a[168:171], a[140:143], v[148:151]
v_add_u32 v43, 16384, v43
v_add_u32 v44, 16384, v44
v_mfma_f32_16x16x32_bf16 v[184:187], a[172:175], a[140:143], v[184:187]
v_add_u32 v47, 4294901760, v47
v_add_u32 v48, 4294901760, v48
v_mfma_f32_16x16x32_bf16 v[188:191], a[192:195], a[140:143], v[188:191]
buffer_load_dwordx4 v38, s[12:15], s24 offen offset:2048 lds
v_mfma_f32_16x16x32_bf16 v[224:227], a[140:143], a[196:199], v[224:227]
v_add_u32 v49, 4294901760, v49
v_add_u32 v50, 4294901760, v50
v_mfma_f32_16x16x32_bf16 v[228:231], a[140:143], a[200:203], v[228:231]
v_add_u32 v45, s28, v45
v_add_u32 v46, s28, v46
v_mfma_f32_16x16x32_bf16 v[152:155], a[164:167], a[160:163], v[152:155]
v_mfma_f32_16x16x32_bf16 v[156:159], a[168:171], a[160:163], v[156:159]
v_mfma_f32_16x16x32_bf16 v[192:195], a[172:175], a[160:163], v[192:195]
buffer_load_dwordx4 v39, s[12:15], s24 offen offset:3072 lds
v_mfma_f32_16x16x32_bf16 v[196:199], a[192:195], a[160:163], v[196:199]
s_add_u32 s24, s24, 128
v_mfma_f32_16x16x32_bf16 v[232:235], a[160:163], a[196:199], v[232:235]
v_mfma_f32_16x16x32_bf16 v[236:239], a[160:163], a[200:203], v[236:239]
s_waitcnt vmcnt(15) lgkmcnt(0)
s_barrier
v_mfma_f32_16x16x32_bf16 v[160:163], a[28:31], a[0:3], v[160:163]
ds_read_b64_tr_b16 a[164:165], v47 offset:0
ds_read_b64_tr_b16 a[166:167], v47 offset:512
v_mfma_f32_16x16x32_bf16 v[164:167], a[32:35], a[0:3], v[164:167]
s_add_u32 m0, s48, 98304
ds_read_b128 a[128:131], v45
v_mfma_f32_16x16x32_bf16 v[200:203], a[0:3], a[36:39], v[200:203]
buffer_load_dwordx4 v40, s[16:19], s26 offen offset:0 lds
v_mfma_f32_16x16x32_bf16 v[204:207], a[0:3], a[40:43], v[204:207]
ds_read_b64_tr_b16 a[168:169], v48 offset:0
ds_read_b64_tr_b16 a[170:171], v48 offset:512
v_mfma_f32_16x16x32_bf16 v[128:131], a[20:23], a[4:7], v[128:131]
ds_read_b128 a[132:135], v43 offset:0
v_mfma_f32_16x16x32_bf16 v[132:135], a[24:27], a[4:7], v[132:135]
ds_read_b64_tr_b16 a[172:173], v47 offset:2048
ds_read_b64_tr_b16 a[174:175], v47 offset:2560
v_mfma_f32_16x16x32_bf16 v[168:171], a[28:31], a[4:7], v[168:171]
ds_read_b128 a[136:139], v43 offset:2048
v_mfma_f32_16x16x32_bf16 v[172:175], a[32:35], a[4:7], v[172:175]
buffer_load_dwordx4 v41, s[16:19], s26 offen offset:1024 lds
v_mfma_f32_16x16x32_bf16 v[208:211], a[4:7], a[36:39], v[208:211]
ds_read_b64_tr_b16 a[192:193], v48 offset:2048
ds_read_b64_tr_b16 a[194:195], v48 offset:2560
v_mfma_f32_16x16x32_bf16 v[212:215], a[4:7], a[40:43], v[212:215]
ds_read_b128 a[140:143], v43 offset:4096
v_mfma_f32_16x16x32_bf16 v[136:139], a[20:23], a[8:11], v[136:139]
ds_read_b64_tr_b16 a[196:197], v49 offset:4096
ds_read_b64_tr_b16 a[198:199], v49 offset:4608
v_mfma_f32_16x16x32_bf16 v[140:143], a[24:27], a[8:11], v[140:143]
ds_read_b128 a[160:163], v43 offset:6144
v_mfma_f32_16x16x32_bf16 v[176:179], a[28:31], a[8:11], v[176:179]
buffer_load_dwordx4 v40, s[16:19], s26 offen offset:2048 lds
v_mfma_f32_16x16x32_bf16 v[180:183], a[32:35], a[8:11], v[180:183]
ds_read_b64_tr_b16 a[200:201], v50 offset:4096
ds_read_b64_tr_b16 a[202:203], v50 offset:4608
v_mfma_f32_16x16x32_bf16 v[216:219], a[8:11], a[36:39], v[216:219]
v_mfma_f32_16x16x32_bf16 v[220:223], a[8:11], a[40:43], v[220:223]
v_mfma_f32_16x16x32_bf16 v[144:147], a[20:23], a[12:15], v[144:147]
v_mfma_f32_16x16x32_bf16 v[148:151], a[24:27], a[12:15], v[148:151]
buffer_load_dwordx4 v41, s[16:19], s26 offen offset:3072 lds
v_mfma_f32_16x16x32_bf16 v[184:187], a[28:31], a[12:15], v[184:187]
v_mfma_f32_16x16x32_bf16 v[188:191], a[32:35], a[12:15], v[188:191]
v_mfma_f32_16x16x32_bf16 v[224:227], a[12:15], a[36:39], v[224:227]
v_mfma_f32_16x16x32_bf16 v[228:231], a[12:15], a[40:43], v[228:231]
s_add_u32 m0, s48, 102400
v_mfma_f32_16x16x32_bf16 v[152:155], a[20:23], a[16:19], v[152:155]
buffer_load_dwordx4 v40, s[16:19], s47 offen offset:0 lds
v_mfma_f32_16x16x32_bf16 v[156:159], a[24:27], a[16:19], v[156:159]
v_mfma_f32_16x16x32_bf16 v[192:195], a[28:31], a[16:19], v[192:195]
buffer_load_dword v35, v4, s[96:99], 0 offen
v_mfma_f32_16x16x32_bf16 v[196:199], a[32:35], a[16:19], v[196:199]
v_mfma_f32_16x16x32_bf16 v[232:235], a[16:19], a[36:39], v[232:235]
buffer_load_dwordx4 v41, s[16:19], s47 offen offset:1024 lds
v_mfma_f32_16x16x32_bf16 v[236:239], a[16:19], a[40:43], v[236:239]
s_waitcnt lgkmcnt(0)
v_mfma_f32_16x16x32_bf16 v[160:163], a[172:175], a[128:131], v[160:163]
ds_read_b64_tr_b16 a[20:21], v47 offset:14336
ds_read_b64_tr_b16 a[22:23], v47 offset:14848
v_mfma_f32_16x16x32_bf16 v[164:167], a[192:195], a[128:131], v[164:167]
ds_read_b128 a[0:3], v46
v_mfma_f32_16x16x32_bf16 v[200:203], a[128:131], a[196:199], v[200:203]
ds_read_b64_tr_b16 a[24:25], v48 offset:14336
ds_read_b64_tr_b16 a[26:27], v48 offset:14848
v_mfma_f32_16x16x32_bf16 v[204:207], a[128:131], a[200:203], v[204:207]
buffer_load_dwordx4 v42, s[12:15], s25 offen offset:2048 lds
v_mfma_f32_16x16x32_bf16 v[128:131], a[164:167], a[132:135], v[128:131]
s_add_u32 s26, s26, 393216
s_add_u32 s47, s47, 393216
v_mfma_f32_16x16x32_bf16 v[132:135], a[168:171], a[132:135], v[132:135]
s_add_u32 s25, s25, 128
ds_read_b128 a[4:7], v44 offset:0
v_mfma_f32_16x16x32_bf16 v[168:171], a[172:175], a[132:135], v[168:171]
buffer_load_dword v250, v249, s[60:63], 0 offen sc1
ds_read_b64_tr_b16 a[28:29], v47 offset:16384
ds_read_b64_tr_b16 a[30:31], v47 offset:16896
v_mfma_f32_16x16x32_bf16 v[172:175], a[192:195], a[132:135], v[172:175]
ds_read_b128 a[8:11], v44 offset:2048
v_mfma_f32_16x16x32_bf16 v[208:211], a[132:135], a[196:199], v[208:211]
s_add_u32 m0, s91, 65536
v_mfma_f32_16x16x32_bf16 v[212:215], a[132:135], a[200:203], v[212:215]
buffer_load_dwordx4 v36, s[12:15], s24 offen offset:0 lds
v_mfma_f32_16x16x32_bf16 v[136:139], a[164:167], a[136:139], v[136:139]
ds_read_b64_tr_b16 a[32:33], v48 offset:16384
ds_read_b64_tr_b16 a[34:35], v48 offset:16896
v_mfma_f32_16x16x32_bf16 v[140:143], a[168:171], a[136:139], v[140:143]
ds_read_b128 a[12:15], v44 offset:4096
v_mfma_f32_16x16x32_bf16 v[176:179], a[172:175], a[136:139], v[176:179]
ds_read_b64_tr_b16 a[36:37], v49 offset:18432
ds_read_b64_tr_b16 a[38:39], v49 offset:18944
v_mfma_f32_16x16x32_bf16 v[180:183], a[192:195], a[136:139], v[180:183]
ds_read_b128 a[16:19], v44 offset:6144
v_mfma_f32_16x16x32_bf16 v[216:219], a[136:139], a[196:199], v[216:219]
buffer_load_dwordx4 v37, s[12:15], s24 offen offset:1024 lds
v_mfma_f32_16x16x32_bf16 v[220:223], a[136:139], a[200:203], v[220:223]
ds_read_b64_tr_b16 a[40:41], v50 offset:18432
ds_read_b64_tr_b16 a[42:43], v50 offset:18944
v_mfma_f32_16x16x32_bf16 v[144:147], a[164:167], a[140:143], v[144:147]
s_cmp_eq_u32 s39, 1
s_mov_b32 s28, 4294885376
s_cselect_b32 s28, 98304, s28
v_mfma_f32_16x16x32_bf16 v[148:151], a[168:171], a[140:143], v[148:151]
v_add_u32 v43, 4294885376, v43
v_add_u32 v44, 4294885376, v44
v_mfma_f32_16x16x32_bf16 v[184:187], a[172:175], a[140:143], v[184:187]
v_add_u32 v47, 98304, v47
v_add_u32 v48, 98304, v48
v_mfma_f32_16x16x32_bf16 v[188:191], a[192:195], a[140:143], v[188:191]
buffer_load_dwordx4 v38, s[12:15], s24 offen offset:2048 lds
v_mfma_f32_16x16x32_bf16 v[224:227], a[140:143], a[196:199], v[224:227]
v_add_u32 v49, 98304, v49
v_add_u32 v50, 98304, v50
v_mfma_f32_16x16x32_bf16 v[228:231], a[140:143], a[200:203], v[228:231]
v_add_u32 v45, s28, v45
v_add_u32 v46, s28, v46
v_mfma_f32_16x16x32_bf16 v[152:155], a[164:167], a[160:163], v[152:155]
v_mfma_f32_16x16x32_bf16 v[156:159], a[168:171], a[160:163], v[156:159]
v_mfma_f32_16x16x32_bf16 v[192:195], a[172:175], a[160:163], v[192:195]
buffer_load_dwordx4 v39, s[12:15], s24 offen offset:3072 lds
v_mfma_f32_16x16x32_bf16 v[196:199], a[192:195], a[160:163], v[196:199]
s_add_u32 s24, s24, 128
v_mfma_f32_16x16x32_bf16 v[232:235], a[160:163], a[196:199], v[232:235]
v_mfma_f32_16x16x32_bf16 v[236:239], a[160:163], a[200:203], v[236:239]
s_waitcnt vmcnt(17) lgkmcnt(0)
s_barrier
v_mfma_f32_16x16x32_bf16 v[160:163], a[28:31], a[0:3], v[160:163]
ds_read_b64_tr_b16 a[164:165], v47 offset:0
ds_read_b64_tr_b16 a[166:167], v47 offset:512
v_mfma_f32_16x16x32_bf16 v[164:167], a[32:35], a[0:3], v[164:167]
s_add_u32 m0, s48, 32768
ds_read_b128 a[128:131], v45
v_mfma_f32_16x16x32_bf16 v[200:203], a[0:3], a[36:39], v[200:203]
buffer_load_dwordx4 v40, s[16:19], s26 offen offset:0 lds
v_mfma_f32_16x16x32_bf16 v[204:207], a[0:3], a[40:43], v[204:207]
ds_read_b64_tr_b16 a[168:169], v48 offset:0
ds_read_b64_tr_b16 a[170:171], v48 offset:512
v_mfma_f32_16x16x32_bf16 v[128:131], a[20:23], a[4:7], v[128:131]
ds_read_b128 a[132:135], v43 offset:0
v_mfma_f32_16x16x32_bf16 v[132:135], a[24:27], a[4:7], v[132:135]
ds_read_b64_tr_b16 a[172:173], v47 offset:2048
ds_read_b64_tr_b16 a[174:175], v47 offset:2560
v_mfma_f32_16x16x32_bf16 v[168:171], a[28:31], a[4:7], v[168:171]
ds_read_b128 a[136:139], v43 offset:2048
v_mfma_f32_16x16x32_bf16 v[172:175], a[32:35], a[4:7], v[172:175]
buffer_load_dwordx4 v41, s[16:19], s26 offen offset:1024 lds
v_mfma_f32_16x16x32_bf16 v[208:211], a[4:7], a[36:39], v[208:211]
ds_read_b64_tr_b16 a[192:193], v48 offset:2048
ds_read_b64_tr_b16 a[194:195], v48 offset:2560
v_mfma_f32_16x16x32_bf16 v[212:215], a[4:7], a[40:43], v[212:215]
ds_read_b128 a[140:143], v43 offset:4096
v_mfma_f32_16x16x32_bf16 v[136:139], a[20:23], a[8:11], v[136:139]
ds_read_b64_tr_b16 a[196:197], v49 offset:4096
ds_read_b64_tr_b16 a[198:199], v49 offset:4608
v_mfma_f32_16x16x32_bf16 v[140:143], a[24:27], a[8:11], v[140:143]
ds_read_b128 a[160:163], v43 offset:6144
v_mfma_f32_16x16x32_bf16 v[176:179], a[28:31], a[8:11], v[176:179]
buffer_load_dwordx4 v40, s[16:19], s26 offen offset:2048 lds
v_mfma_f32_16x16x32_bf16 v[180:183], a[32:35], a[8:11], v[180:183]
ds_read_b64_tr_b16 a[200:201], v50 offset:4096
ds_read_b64_tr_b16 a[202:203], v50 offset:4608
.Lpad15:
s_cbranch_execz .Ltch5
v_mfma_f32_16x16x32_bf16 v[216:219], a[8:11], a[36:39], v[216:219]
v_mfma_f32_16x16x32_bf16 v[220:223], a[8:11], a[40:43], v[220:223]
v_mfma_f32_16x16x32_bf16 v[144:147], a[20:23], a[12:15], v[144:147]
v_mfma_f32_16x16x32_bf16 v[148:151], a[24:27], a[12:15], v[148:151]
buffer_load_dwordx4 v41, s[16:19], s26 offen offset:3072 lds
v_mfma_f32_16x16x32_bf16 v[184:187], a[28:31], a[12:15], v[184:187]
v_mfma_f32_16x16x32_bf16 v[188:191], a[32:35], a[12:15], v[188:191]
v_mfma_f32_16x16x32_bf16 v[224:227], a[12:15], a[36:39], v[224:227]
v_mfma_f32_16x16x32_bf16 v[228:231], a[12:15], a[40:43], v[228:231]
s_add_u32 m0, s48, 36864
v_mfma_f32_16x16x32_bf16 v[152:155], a[20:23], a[16:19], v[152:155]
buffer_load_dwordx4 v40, s[16:19], s47 offen offset:0 lds
v_mfma_f32_16x16x32_bf16 v[156:159], a[24:27], a[16:19], v[156:159]
v_mfma_f32_16x16x32_bf16 v[192:195], a[28:31], a[16:19], v[192:195]
v_mfma_f32_16x16x32_bf16 v[196:199], a[32:35], a[16:19], v[196:199]
v_mfma_f32_16x16x32_bf16 v[232:235], a[16:19], a[36:39], v[232:235]
buffer_load_dwordx4 v41, s[16:19], s47 offen offset:1024 lds
v_mfma_f32_16x16x32_bf16 v[236:239], a[16:19], a[40:43], v[236:239]
s_waitcnt lgkmcnt(0)
v_mfma_f32_16x16x32_bf16 v[160:163], a[172:175], a[128:131], v[160:163]
ds_read_b64_tr_b16 a[20:21], v47 offset:14336
ds_read_b64_tr_b16 a[22:23], v47 offset:14848
v_mfma_f32_16x16x32_bf16 v[164:167], a[192:195], a[128:131], v[164:167]
ds_read_b128 a[0:3], v46
v_mfma_f32_16x16x32_bf16 v[200:203], a[128:131], a[196:199], v[200:203]
ds_read_b64_tr_b16 a[24:25], v48 offset:14336
ds_read_b64_tr_b16 a[26:27], v48 offset:14848
v_mfma_f32_16x16x32_bf16 v[204:207], a[128:131], a[200:203], v[204:207]
buffer_load_dwordx4 v42, s[12:15], s25 offen offset:2048 lds
v_mfma_f32_16x16x32_bf16 v[128:131], a[164:167], a[132:135], v[128:131]
s_add_u32 s26, s26, 393216
s_add_u32 s47, s47, 393216
v_mfma_f32_16x16x32_bf16 v[132:135], a[168:171], a[132:135], v[132:135]
s_add_u32 s25, s25, 128
ds_read_b128 a[4:7], v44 offset:0
v_mfma_f32_16x16x32_bf16 v[168:171], a[172:175], a[132:135], v[168:171]
ds_read_b64_tr_b16 a[28:29], v47 offset:16384
ds_read_b64_tr_b16 a[30:31], v47 offset:16896
v_mfma_f32_16x16x32_bf16 v[172:175], a[192:195], a[132:135], v[172:175]
ds_read_b128 a[8:11], v44 offset:2048
v_mfma_f32_16x16x32_bf16 v[208:211], a[132:135], a[196:199], v[208:211]
s_add_u32 m0, s91, 81920
v_mfma_f32_16x16x32_bf16 v[212:215], a[132:135], a[200:203], v[212:215]
buffer_load_dwordx4 v36, s[12:15], s24 offen offset:0 lds
v_mfma_f32_16x16x32_bf16 v[136:139], a[164:167], a[136:139], v[136:139]
ds_read_b64_tr_b16 a[32:33], v48 offset:16384
ds_read_b64_tr_b16 a[34:35], v48 offset:16896
v_mfma_f32_16x16x32_bf16 v[140:143], a[168:171], a[136:139], v[140:143]
ds_read_b128 a[12:15], v44 offset:4096
v_mfma_f32_16x16x32_bf16 v[176:179], a[172:175], a[136:139], v[176:179]
ds_read_b64_tr_b16 a[36:37], v49 offset:18432
ds_read_b64_tr_b16 a[38:39], v49 offset:18944
v_mfma_f32_16x16x32_bf16 v[180:183], a[192:195], a[136:139], v[180:183]
ds_read_b128 a[16:19], v44 offset:6144
v_mfma_f32_16x16x32_bf16 v[216:219], a[136:139], a[196:199], v[216:219]
buffer_load_dwordx4 v37, s[12:15], s24 offen offset:1024 lds
v_mfma_f32_16x16x32_bf16 v[220:223], a[136:139], a[200:203], v[220:223]
ds_read_b64_tr_b16 a[40:41], v50 offset:18432
ds_read_b64_tr_b16 a[42:43], v50 offset:18944
v_mfma_f32_16x16x32_bf16 v[144:147], a[164:167], a[140:143], v[144:147]
s_cmp_eq_u32 s39, 1
s_mov_b32 s28, 16384
s_cselect_b32 s28, 4294934528, s28
v_mfma_f32_16x16x32_bf16 v[148:151], a[168:171], a[140:143], v[148:151]
v_add_u32 v43, 16384, v43
v_add_u32 v44, 16384, v44
v_mfma_f32_16x16x32_bf16 v[184:187], a[172:175], a[140:143], v[184:187]
v_add_u32 v47, 4294934528, v47
v_add_u32 v48, 4294934528, v48
v_mfma_f32_16x16x32_bf16 v[188:191], a[192:195], a[140:143], v[188:191]
buffer_load_dwordx4 v38, s[12:15], s24 offen offset:2048 lds
v_mfma_f32_16x16x32_bf16 v[224:227], a[140:143], a[196:199], v[224:227]
v_add_u32 v49, 4294934528, v49
v_add_u32 v50, 4294934528, v50
v_mfma_f32_16x16x32_bf16 v[228:231], a[140:143], a[200:203], v[228:231]
v_add_u32 v45, s28, v45
v_add_u32 v46, s28, v46
v_mfma_f32_16x16x32_bf16 v[152:155], a[164:167], a[160:163], v[152:155]
v_mfma_f32_16x16x32_bf16 v[156:159], a[168:171], a[160:163], v[156:159]
v_mfma_f32_16x16x32_bf16 v[192:195], a[172:175], a[160:163], v[192:195]
buffer_load_dwordx4 v39, s[12:15], s24 offen offset:3072 lds
v_mfma_f32_16x16x32_bf16 v[196:199], a[192:195], a[160:163], v[196:199]
s_add_u32 s24, s24, 128
v_mfma_f32_16x16x32_bf16 v[232:235], a[160:163], a[196:199], v[232:235]
v_mfma_f32_16x16x32_bf16 v[236:239], a[160:163], a[200:203], v[236:239]
s_waitcnt vmcnt(16) lgkmcnt(0)
s_barrier
v_mfma_f32_16x16x32_bf16 v[160:163], a[28:31], a[0:3], v[160:163]
ds_read_b64_tr_b16 a[164:165], v47 offset:0
ds_read_b64_tr_b16 a[166:167], v47 offset:512
v_mfma_f32_16x16x32_bf16 v[164:167], a[32:35], a[0:3], v[164:167]
s_add_u32 m0, s48, 131072
ds_read_b128 a[128:131], v45
v_mfma_f32_16x16x32_bf16 v[200:203], a[0:3], a[36:39], v[200:203]
buffer_load_dwordx4 v40, s[16:19], s26 offen offset:0 lds
v_mfma_f32_16x16x32_bf16 v[204:207], a[0:3], a[40:43], v[204:207]
ds_read_b64_tr_b16 a[168:169], v48 offset:0
ds_read_b64_tr_b16 a[170:171], v48 offset:512
v_mfma_f32_16x16x32_bf16 v[128:131], a[20:23], a[4:7], v[128:131]
ds_read_b128 a[132:135], v43 offset:0
v_mfma_f32_16x16x32_bf16 v[132:135], a[24:27], a[4:7], v[132:135]
ds_read_b64_tr_b16 a[172:173], v47 offset:2048
ds_read_b64_tr_b16 a[174:175], v47 offset:2560
v_mfma_f32_16x16x32_bf16 v[168:171], a[28:31], a[4:7], v[168:171]
ds_read_b128 a[136:139], v43 offset:2048
v_mfma_f32_16x16x32_bf16 v[172:175], a[32:35], a[4:7], v[172:175]
buffer_load_dwordx4 v41, s[16:19], s26 offen offset:1024 lds
v_mfma_f32_16x16x32_bf16 v[208:211], a[4:7], a[36:39], v[208:211]
ds_read_b64_tr_b16 a[192:193], v48 offset:2048
ds_read_b64_tr_b16 a[194:195], v48 offset:2560
v_mfma_f32_16x16x32_bf16 v[212:215], a[4:7], a[40:43], v[212:215]
ds_read_b128 a[140:143], v43 offset:4096
v_mfma_f32_16x16x32_bf16 v[136:139], a[20:23], a[8:11], v[136:139]
ds_read_b64_tr_b16 a[196:197], v49 offset:4096
ds_read_b64_tr_b16 a[198:199], v49 offset:4608
v_mfma_f32_16x16x32_bf16 v[140:143], a[24:27], a[8:11], v[140:143]
ds_read_b128 a[160:163], v43 offset:6144
v_mfma_f32_16x16x32_bf16 v[176:179], a[28:31], a[8:11], v[176:179]
buffer_load_dwordx4 v40, s[16:19], s26 offen offset:2048 lds
v_mfma_f32_16x16x32_bf16 v[180:183], a[32:35], a[8:11], v[180:183]
ds_read_b64_tr_b16 a[200:201], v50 offset:4096
ds_read_b64_tr_b16 a[202:203], v50 offset:4608
v_mfma_f32_16x16x32_bf16 v[216:219], a[8:11], a[36:39], v[216:219]
v_mfma_f32_16x16x32_bf16 v[220:223], a[8:11], a[40:43], v[220:223]
v_mfma_f32_16x16x32_bf16 v[144:147], a[20:23], a[12:15], v[144:147]
v_mfma_f32_16x16x32_bf16 v[148:151], a[24:27], a[12:15], v[148:151]
buffer_load_dwordx4 v41, s[16:19], s26 offen offset:3072 lds
v_mfma_f32_16x16x32_bf16 v[184:187], a[28:31], a[12:15], v[184:187]
v_mfma_f32_16x16x32_bf16 v[188:191], a[32:35], a[12:15], v[188:191]
v_mfma_f32_16x16x32_bf16 v[224:227], a[12:15], a[36:39], v[224:227]
v_mfma_f32_16x16x32_bf16 v[228:231], a[12:15], a[40:43], v[228:231]
s_add_u32 m0, s48, 135168
v_mfma_f32_16x16x32_bf16 v[152:155], a[20:23], a[16:19], v[152:155]
buffer_load_dwordx4 v40, s[16:19], s47 offen offset:0 lds
v_mfma_f32_16x16x32_bf16 v[156:159], a[24:27], a[16:19], v[156:159]
v_mfma_f32_16x16x32_bf16 v[192:195], a[28:31], a[16:19], v[192:195]
v_mfma_f32_16x16x32_bf16 v[196:199], a[32:35], a[16:19], v[196:199]
v_mfma_f32_16x16x32_bf16 v[232:235], a[16:19], a[36:39], v[232:235]
buffer_load_dwordx4 v41, s[16:19], s47 offen offset:1024 lds
v_mfma_f32_16x16x32_bf16 v[236:239], a[16:19], a[40:43], v[236:239]
s_waitcnt lgkmcnt(0)
v_mfma_f32_16x16x32_bf16 v[160:163], a[172:175], a[128:131], v[160:163]
ds_read_b64_tr_b16 a[20:21], v47 offset:14336
ds_read_b64_tr_b16 a[22:23], v47 offset:14848
v_mfma_f32_16x16x32_bf16 v[164:167], a[192:195], a[128:131], v[164:167]
s_waitcnt vmcnt(21)
ds_read_b128 a[0:3], v46
v_cmp_eq_u32 vcc, 0, v250
v_mfma_f32_16x16x32_bf16 v[200:203], a[128:131], a[196:199], v[200:203]
ds_read_b64_tr_b16 a[24:25], v48 offset:14336
ds_read_b64_tr_b16 a[26:27], v48 offset:14848
v_mfma_f32_16x16x32_bf16 v[204:207], a[128:131], a[200:203], v[204:207]
buffer_load_dwordx4 v42, s[12:15], s25 offen offset:2048 lds
v_mfma_f32_16x16x32_bf16 v[128:131], a[164:167], a[132:135], v[128:131]
s_add_u32 s26, s26, 393216
s_add_u32 s47, s47, 393216
v_mfma_f32_16x16x32_bf16 v[132:135], a[168:171], a[132:135], v[132:135]
s_add_u32 s25, s25, 128
s_cmp_lg_u64 vcc, 0
s_cbranch_scc1 .Lspin6
.Lspin6_ret:
ds_read_b128 a[4:7], v44 offset:0
v_mfma_f32_16x16x32_bf16 v[168:171], a[172:175], a[132:135], v[168:171]
ds_read_b64_tr_b16 a[28:29], v47 offset:16384
ds_read_b64_tr_b16 a[30:31], v47 offset:16896
v_mfma_f32_16x16x32_bf16 v[172:175], a[192:195], a[132:135], v[172:175]
ds_read_b128 a[8:11], v44 offset:2048
v_mov_b32 v251, 0
v_mfma_f32_16x16x32_bf16 v[208:211], a[132:135], a[196:199], v[208:211]
s_add_u32 m0, s91, 0
v_readlane_b32 s54, v250, 0
v_nop
v_mfma_f32_16x16x32_bf16 v[212:215], a[132:135], a[200:203], v[212:215]
buffer_load_dwordx4 v36, s[12:15], s24 offen offset:0 lds
v_nop
v_mfma_f32_16x16x32_bf16 v[136:139], a[164:167], a[136:139], v[136:139]
ds_read_b64_tr_b16 a[32:33], v48 offset:16384
ds_read_b64_tr_b16 a[34:35], v48 offset:16896
v_mfma_f32_16x16x32_bf16 v[140:143], a[168:171], a[136:139], v[140:143]
ds_read_b128 a[12:15], v44 offset:4096
v_subrev_u32 v252, s54, v250
v_mfma_f32_16x16x32_bf16 v[176:179], a[172:175], a[136:139], v[176:179]
ds_read_b64_tr_b16 a[36:37], v49 offset:18432
ds_read_b64_tr_b16 a[38:39], v49 offset:18944
v_mfma_f32_16x16x32_bf16 v[180:183], a[192:195], a[136:139], v[180:183]
ds_read_b128 a[16:19], v44 offset:6144
v_cmp_lt_i32 vcc, 0, v252
v_mfma_f32_16x16x32_bf16 v[216:219], a[136:139], a[196:199], v[216:219]
buffer_load_dwordx4 v37, s[12:15], s24 offen offset:1024 lds
v_nop
v_mfma_f32_16x16x32_bf16 v[220:223], a[136:139], a[200:203], v[220:223]
ds_read_b64_tr_b16 a[40:41], v50 offset:18432
ds_read_b64_tr_b16 a[42:43], v50 offset:18944
v_mfma_f32_16x16x32_bf16 v[144:147], a[164:167], a[140:143], v[144:147]
s_cmp_eq_u32 s39, 1
s_mov_b32 s28, 49152
s_cselect_b32 s28, 4294901760, s28
v_mfma_f32_16x16x32_bf16 v[148:151], a[168:171], a[140:143], v[148:151]
v_add_u32 v43, 49152, v43
v_add_u32 v44, 49152, v44
v_mfma_f32_16x16x32_bf16 v[184:187], a[172:175], a[140:143], v[184:187]
v_add_u32 v47, 4294901760, v47
v_add_u32 v48, 4294901760, v48
v_mfma_f32_16x16x32_bf16 v[188:191], a[192:195], a[140:143], v[188:191]
buffer_load_dwordx4 v38, s[12:15], s24 offen offset:2048 lds
v_nop
v_mfma_f32_16x16x32_bf16 v[224:227], a[140:143], a[196:199], v[224:227]
v_add_u32 v49, 4294901760, v49
v_add_u32 v50, 4294901760, v50
v_mfma_f32_16x16x32_bf16 v[228:231], a[140:143], a[200:203], v[228:231]
v_add_u32 v45, s28, v45
v_add_u32 v46, s28, v46
v_mfma_f32_16x16x32_bf16 v[152:155], a[164:167], a[160:163], v[152:155]
v_addc_co_u32 v251, vcc, 0, v251, vcc
v_readlane_b32 s55, v250, 1
v_mfma_f32_16x16x32_bf16 v[156:159], a[168:171], a[160:163], v[156:159]
v_nop
v_nop
v_mfma_f32_16x16x32_bf16 v[192:195], a[172:175], a[160:163], v[192:195]
buffer_load_dwordx4 v39, s[12:15], s24 offen offset:3072 lds
v_subrev_u32 v252, s55, v250
v_mfma_f32_16x16x32_bf16 v[196:199], a[192:195], a[160:163], v[196:199]
s_add_u32 s24, s24, 128
v_cmp_lt_i32 vcc, 0, v252
v_nop
v_mfma_f32_16x16x32_bf16 v[232:235], a[160:163], a[196:199], v[232:235]
v_nop
v_addc_co_u32 v251, vcc, 0, v251, vcc
v_mfma_f32_16x16x32_bf16 v[236:239], a[160:163], a[200:203], v[236:239]
s_waitcnt vmcnt(15) lgkmcnt(0)
s_barrier
v_readlane_b32 s54, v250, 2
v_mfma_f32_16x16x32_bf16 v[160:163], a[28:31], a[0:3], v[160:163]
ds_read_b64_tr_b16 a[164:165], v47 offset:0
ds_read_b64_tr_b16 a[166:167], v47 offset:512
v_mfma_f32_16x16x32_bf16 v[164:167], a[32:35], a[0:3], v[164:167]
s_add_u32 m0, s48, 98304
ds_read_b128 a[128:131], v45
v_mfma_f32_16x16x32_bf16 v[200:203], a[0:3], a[36:39], v[200:203]
buffer_load_dwordx4 v40, s[16:19], s26 offen offset:0 lds
v_nop
v_mfma_f32_16x16x32_bf16 v[204:207], a[0:3], a[40:43], v[204:207]
ds_read_b64_tr_b16 a[168:169], v48 offset:0
ds_read_b64_tr_b16 a[170:171], v48 offset:512
v_mfma_f32_16x16x32_bf16 v[128:131], a[20:23], a[4:7], v[128:131]
ds_read_b128 a[132:135], v43 offset:0
v_nop
v_mfma_f32_16x16x32_bf16 v[132:135], a[24:27], a[4:7], v[132:135]
ds_read_b64_tr_b16 a[172:173], v47 offset:2048
ds_read_b64_tr_b16 a[174:175], v47 offset:2560
v_mfma_f32_16x16x32_bf16 v[168:171], a[28:31], a[4:7], v[168:171]
ds_read_b128 a[136:139], v43 offset:2048
v_subrev_u32 v252, s54, v250
v_mfma_f32_16x16x32_bf16 v[172:175], a[32:35], a[4:7], v[172:175]
buffer_load_dwordx4 v41, s[16:19], s26 offen offset:1024 lds
v_cmp_lt_i32 vcc, 0, v252
v_mfma_f32_16x16x32_bf16 v[208:211], a[4:7], a[36:39], v[208:211]
ds_read_b64_tr_b16 a[192:193], v48 offset:2048
ds_read_b64_tr_b16 a[194:195], v48 offset:2560
v_mfma_f32_16x16x32_bf16 v[212:215], a[4:7], a[40:43], v[212:215]
ds_read_b128 a[140:143], v43 offset:4096
v_nop
v_mfma_f32_16x16x32_bf16 v[136:139], a[20:23], a[8:11], v[136:139]
ds_read_b64_tr_b16 a[196:197], v49 offset:4096
ds_read_b64_tr_b16 a[198:199], v49 offset:4608
v_mfma_f32_16x16x32_bf16 v[140:143], a[24:27], a[8:11], v[140:143]
ds_read_b128 a[160:163], v43 offset:6144
v_nop
v_mfma_f32_16x16x32_bf16 v[176:179], a[28:31], a[8:11], v[176:179]
buffer_load_dwordx4 v40, s[16:19], s26 offen offset:2048 lds
v_addc_co_u32 v251, vcc, 0, v251, vcc
v_mfma_f32_16x16x32_bf16 v[180:183], a[32:35], a[8:11], v[180:183]
ds_read_b64_tr_b16 a[200:201], v50 offset:4096
ds_read_b64_tr_b16 a[202:203], v50 offset:4608
v_mfma_f32_16x16x32_bf16 v[216:219], a[8:11], a[36:39], v[216:219]
v_readlane_b32 s55, v250, 3
v_nop
v_mfma_f32_16x16x32_bf16 v[220:223], a[8:11], a[40:43], v[220:223]
v_nop
v_subrev_u32 v252, s55, v250
v_mfma_f32_16x16x32_bf16 v[144:147], a[20:23], a[12:15], v[144:147]
v_cmp_lt_i32 vcc, 0, v252
v_nop
v_mfma_f32_16x16x32_bf16 v[148:151], a[24:27], a[12:15], v[148:151]
buffer_load_dwordx4 v41, s[16:19], s26 offen offset:3072 lds
v_nop
v_mfma_f32_16x16x32_bf16 v[184:187], a[28:31], a[12:15], v[184:187]
v_addc_co_u32 v251, vcc, 0, v251, vcc
v_readlane_b32 s54, v250, 4
v_mfma_f32_16x16x32_bf16 v[188:191], a[32:35], a[12:15], v[188:191]
v_nop
v_nop
v_mfma_f32_16x16x32_bf16 v[224:227], a[12:15], a[36:39], v[224:227]
v_subrev_u32 v252, s54, v250
v_cmp_lt_i32 vcc, 0, v252
v_mfma_f32_16x16x32_bf16 v[228:231], a[12:15], a[40:43], v[228:231]
s_add_u32 m0, s48, 102400
v_nop
v_nop
v_mfma_f32_16x16x32_bf16 v[152:155], a[20:23], a[16:19], v[152:155]
buffer_load_dwordx4 v40, s[16:19], s47 offen offset:0 lds
v_addc_co_u32 v251, vcc, 0, v251, vcc
v_mfma_f32_16x16x32_bf16 v[156:159], a[24:27], a[16:19], v[156:159]
v_readlane_b32 s55, v250, 5
v_nop
v_mfma_f32_16x16x32_bf16 v[192:195], a[28:31], a[16:19], v[192:195]
v_nop
v_subrev_u32 v252, s55, v250
v_mfma_f32_16x16x32_bf16 v[196:199], a[32:35], a[16:19], v[196:199]
v_cmp_lt_i32 vcc, 0, v252
v_nop
v_mfma_f32_16x16x32_bf16 v[232:235], a[16:19], a[36:39], v[232:235]
buffer_load_dwordx4 v41, s[16:19], s47 offen offset:1024 lds
v_nop
v_mfma_f32_16x16x32_bf16 v[236:239], a[16:19], a[40:43], v[236:239]
s_waitcnt lgkmcnt(0)
v_addc_co_u32 v251, vcc, 0, v251, vcc
v_readlane_b32 s54, v250, 6
v_mfma_f32_16x16x32_bf16 v[160:163], a[172:175], a[128:131], v[160:163]
ds_read_b64_tr_b16 a[20:21], v47 offset:14336
ds_read_b64_tr_b16 a[22:23], v47 offset:14848
v_mfma_f32_16x16x32_bf16 v[164:167], a[192:195], a[128:131], v[164:167]
ds_read_b128 a[0:3], v46
v_nop
v_mfma_f32_16x16x32_bf16 v[200:203], a[128:131], a[196:199], v[200:203]
ds_read_b64_tr_b16 a[24:25], v48 offset:14336
ds_read_b64_tr_b16 a[26:27], v48 offset:14848
v_mfma_f32_16x16x32_bf16 v[204:207], a[128:131], a[200:203], v[204:207]
buffer_load_dwordx4 v42, s[12:15], s25 offen offset:2048 lds
v_nop
v_mfma_f32_16x16x32_bf16 v[128:131], a[164:167], a[132:135], v[128:131]
s_add_u32 s26, s26, 393216
s_add_u32 s47, s47, 393216
v_subrev_u32 v252, s54, v250
v_mfma_f32_16x16x32_bf16 v[132:135], a[168:171], a[132:135], v[132:135]
s_add_u32 s25, s25, 128
ds_read_b128 a[4:7], v44 offset:0
v_mfma_f32_16x16x32_bf16 v[168:171], a[172:175], a[132:135], v[168:171]
ds_read_b64_tr_b16 a[28:29], v47 offset:16384
ds_read_b64_tr_b16 a[30:31], v47 offset:16896
v_mfma_f32_16x16x32_bf16 v[172:175], a[192:195], a[132:135], v[172:175]
ds_read_b128 a[8:11], v44 offset:2048
v_cmp_lt_i32 vcc, 0, v252
v_mfma_f32_16x16x32_bf16 v[208:211], a[132:135], a[196:199], v[208:211]
s_add_u32 m0, s91, 16384
v_nop
v_nop
v_mfma_f32_16x16x32_bf16 v[212:215], a[132:135], a[200:203], v[212:215]
buffer_load_dwordx4 v36, s[12:15], s24 offen offset:0 lds
v_addc_co_u32 v251, vcc, 0, v251, vcc
v_mfma_f32_16x16x32_bf16 v[136:139], a[164:167], a[136:139], v[136:139]
ds_read_b64_tr_b16 a[32:33], v48 offset:16384
ds_read_b64_tr_b16 a[34:35], v48 offset:16896
v_mfma_f32_16x16x32_bf16 v[140:143], a[168:171], a[136:139], v[140:143]
ds_read_b128 a[12:15], v44 offset:4096
v_readlane_b32 s55, v250, 7
v_mfma_f32_16x16x32_bf16 v[176:179], a[172:175], a[136:139], v[176:179]
ds_read_b64_tr_b16 a[36:37], v49 offset:18432
ds_read_b64_tr_b16 a[38:39], v49 offset:18944
v_mfma_f32_16x16x32_bf16 v[180:183], a[192:195], a[136:139], v[180:183]
ds_read_b128 a[16:19], v44 offset:6144
v_nop
v_mfma_f32_16x16x32_bf16 v[216:219], a[136:139], a[196:199], v[216:219]
buffer_load_dwordx4 v37, s[12:15], s24 offen offset:1024 lds
v_nop
v_mfma_f32_16x16x32_bf16 v[220:223], a[136:139], a[200:203], v[220:223]
ds_read_b64_tr_b16 a[40:41], v50 offset:18432
ds_read_b64_tr_b16 a[42:43], v50 offset:18944
v_mfma_f32_16x16x32_bf16 v[144:147], a[164:167], a[140:143], v[144:147]
s_cmp_eq_u32 s39, 1
s_mov_b32 s28, 16384
s_cselect_b32 s28, 98304, s28
v_mfma_f32_16x16x32_bf16 v[148:151], a[168:171], a[140:143], v[148:151]
v_add_u32 v43, 16384, v43
v_add_u32 v44, 16384, v44
v_mfma_f32_16x16x32_bf16 v[184:187], a[172:175], a[140:143], v[184:187]
v_add_u32 v47, 98304, v47
v_add_u32 v48, 98304, v48
v_mfma_f32_16x16x32_bf16 v[188:191], a[192:195], a[140:143], v[188:191]
buffer_load_dwordx4 v38, s[12:15], s24 offen offset:2048 lds
v_subrev_u32 v252, s55, v250
v_mfma_f32_16x16x32_bf16 v[224:227], a[140:143], a[196:199], v[224:227]
v_add_u32 v49, 98304, v49
v_add_u32 v50, 98304, v50
v_mfma_f32_16x16x32_bf16 v[228:231], a[140:143], a[200:203], v[228:231]
v_add_u32 v45, s28, v45
v_add_u32 v46, s28, v46
v_mfma_f32_16x16x32_bf16 v[152:155], a[164:167], a[160:163], v[152:155]
v_cmp_lt_i32 vcc, 0, v252
v_nop
v_mfma_f32_16x16x32_bf16 v[156:159], a[168:171], a[160:163], v[156:159]
v_nop
v_addc_co_u32 v251, vcc, 0, v251, vcc
v_mfma_f32_16x16x32_bf16 v[192:195], a[172:175], a[160:163], v[192:195]
buffer_load_dwordx4 v39, s[12:15], s24 offen offset:3072 lds
v_nop
v_mfma_f32_16x16x32_bf16 v[196:199], a[192:195], a[160:163], v[196:199]
s_add_u32 s24, s24, 128
v_readlane_b32 s54, v251, s86
v_mfma_f32_16x16x32_bf16 v[232:235], a[160:163], a[196:199], v[232:235]
v_mfma_f32_16x16x32_bf16 v[236:239], a[160:163], a[200:203], v[236:239]
s_waitcnt vmcnt(15) lgkmcnt(0)
s_barrier
s_sub_u32 s55, 7, s54
v_mfma_f32_16x16x32_bf16 v[160:163], a[28:31], a[0:3], v[160:163]
ds_read_b64_tr_b16 a[164:165], v47 offset:0
ds_read_b64_tr_b16 a[166:167], v47 offset:512
v_mfma_f32_16x16x32_bf16 v[164:167], a[32:35], a[0:3], v[164:167]
s_add_u32 m0, s48, 32768
ds_read_b128 a[128:131], v45
v_mfma_f32_16x16x32_bf16 v[200:203], a[0:3], a[36:39], v[200:203]
buffer_load_dwordx4 v40, s[16:19], s26 offen offset:0 lds
v_cmp_eq_u32 vcc, s55, v251
v_mfma_f32_16x16x32_bf16 v[204:207], a[0:3], a[40:43], v[204:207]
ds_read_b64_tr_b16 a[168:169], v48 offset:0
ds_read_b64_tr_b16 a[170:171], v48 offset:512
v_mfma_f32_16x16x32_bf16 v[128:131], a[20:23], a[4:7], v[128:131]
s_ff1_i32_b64 s38, vcc
ds_read_b128 a[132:135], v43 offset:0
v_mfma_f32_16x16x32_bf16 v[132:135], a[24:27], a[4:7], v[132:135]
ds_read_b64_tr_b16 a[172:173], v47 offset:2048
ds_read_b64_tr_b16 a[174:175], v47 offset:2560
v_mfma_f32_16x16x32_bf16 v[168:171], a[28:31], a[4:7], v[168:171]
s_and_b32 s56, s2, -8
ds_read_b128 a[136:139], v43 offset:2048
v_mfma_f32_16x16x32_bf16 v[172:175], a[32:35], a[4:7], v[172:175]
s_or_b32 s38, s38, s56
buffer_load_dwordx4 v41, s[16:19], s26 offen offset:1024 lds
v_mfma_f32_16x16x32_bf16 v[208:211], a[4:7], a[36:39], v[208:211]
ds_read_b64_tr_b16 a[192:193], v48 offset:2048
ds_read_b64_tr_b16 a[194:195], v48 offset:2560
v_mfma_f32_16x16x32_bf16 v[212:215], a[4:7], a[40:43], v[212:215]
s_min_u32 s55, s55, s54
ds_read_b128 a[140:143], v43 offset:4096
v_mfma_f32_16x16x32_bf16 v[136:139], a[20:23], a[8:11], v[136:139]
ds_read_b64_tr_b16 a[196:197], v49 offset:4096
ds_read_b64_tr_b16 a[198:199], v49 offset:4608
v_mfma_f32_16x16x32_bf16 v[140:143], a[24:27], a[8:11], v[140:143]
s_lshr_b32 s54, s54, 2
ds_read_b128 a[160:163], v43 offset:6144
v_mfma_f32_16x16x32_bf16 v[176:179], a[28:31], a[8:11], v[176:179]
s_lshl_b32 s42, s55, 1
buffer_load_dwordx4 v40, s[16:19], s26 offen offset:2048 lds
v_mfma_f32_16x16x32_bf16 v[180:183], a[32:35], a[8:11], v[180:183]
ds_read_b64_tr_b16 a[200:201], v50 offset:4096
ds_read_b64_tr_b16 a[202:203], v50 offset:4608
v_mfma_f32_16x16x32_bf16 v[216:219], a[8:11], a[36:39], v[216:219]
s_or_b32 s42, s42, s54
s_lshl_b32 s55, s55, 3
s_lshr_b32 s43, 707472430, s55
v_mfma_f32_16x16x32_bf16 v[220:223], a[8:11], a[40:43], v[220:223]
s_and_b32 s43, s43, 0xff
v_mfma_f32_16x16x32_bf16 v[144:147], a[20:23], a[12:15], v[144:147]
v_mfma_f32_16x16x32_bf16 v[148:151], a[24:27], a[12:15], v[148:151]
buffer_load_dwordx4 v41, s[16:19], s26 offen offset:3072 lds
v_mfma_f32_16x16x32_bf16 v[184:187], a[28:31], a[12:15], v[184:187]
v_mfma_f32_16x16x32_bf16 v[188:191], a[32:35], a[12:15], v[188:191]
v_mfma_f32_16x16x32_bf16 v[224:227], a[12:15], a[36:39], v[224:227]
v_mfma_f32_16x16x32_bf16 v[228:231], a[12:15], a[40:43], v[228:231]
s_add_u32 m0, s48, 36864
v_mfma_f32_16x16x32_bf16 v[152:155], a[20:23], a[16:19], v[152:155]
buffer_load_dwordx4 v40, s[16:19], s47 offen offset:0 lds
v_mfma_f32_16x16x32_bf16 v[156:159], a[24:27], a[16:19], v[156:159]
v_mfma_f32_16x16x32_bf16 v[192:195], a[28:31], a[16:19], v[192:195]
v_mfma_f32_16x16x32_bf16 v[196:199], a[32:35], a[16:19], v[196:199]
v_mfma_f32_16x16x32_bf16 v[232:235], a[16:19], a[36:39], v[232:235]
buffer_load_dwordx4 v41, s[16:19], s47 offen offset:1024 lds
v_mfma_f32_16x16x32_bf16 v[236:239], a[16:19], a[40:43], v[236:239]
s_waitcnt lgkmcnt(0)
v_mfma_f32_16x16x32_bf16 v[160:163], a[172:175], a[128:131], v[160:163]
ds_read_b64_tr_b16 a[20:21], v47 offset:14336
ds_read_b64_tr_b16 a[22:23], v47 offset:14848
v_mfma_f32_16x16x32_bf16 v[164:167], a[192:195], a[128:131], v[164:167]
ds_read_b128 a[0:3], v46
v_mfma_f32_16x16x32_bf16 v[200:203], a[128:131], a[196:199], v[200:203]
ds_read_b64_tr_b16 a[24:25], v48 offset:14336
ds_read_b64_tr_b16 a[26:27], v48 offset:14848
v_mfma_f32_16x16x32_bf16 v[204:207], a[128:131], a[200:203], v[204:207]
buffer_load_dwordx4 v42, s[12:15], s25 offen offset:2048 lds
v_mfma_f32_16x16x32_bf16 v[128:131], a[164:167], a[132:135], v[128:131]
s_add_u32 s26, s26, 393216
s_add_u32 s47, s47, 393216
v_mfma_f32_16x16x32_bf16 v[132:135], a[168:171], a[132:135], v[132:135]
s_add_u32 s25, s25, 128
ds_read_b128 a[4:7], v44 offset:0
v_mfma_f32_16x16x32_bf16 v[168:171], a[172:175], a[132:135], v[168:171]
ds_read_b64_tr_b16 a[28:29], v47 offset:16384
ds_read_b64_tr_b16 a[30:31], v47 offset:16896
v_mfma_f32_16x16x32_bf16 v[172:175], a[192:195], a[132:135], v[172:175]
ds_read_b128 a[8:11], v44 offset:2048
v_mfma_f32_16x16x32_bf16 v[208:211], a[132:135], a[196:199], v[208:211]
s_add_u32 m0, s91, 65536
v_mfma_f32_16x16x32_bf16 v[212:215], a[132:135], a[200:203], v[212:215]
buffer_load_dwordx4 v36, s[12:15], s24 offen offset:0 lds
v_mfma_f32_16x16x32_bf16 v[136:139], a[164:167], a[136:139], v[136:139]
ds_read_b64_tr_b16 a[32:33], v48 offset:16384
ds_read_b64_tr_b16 a[34:35], v48 offset:16896
v_mfma_f32_16x16x32_bf16 v[140:143], a[168:171], a[136:139], v[140:143]
ds_read_b128 a[12:15], v44 offset:4096
v_mfma_f32_16x16x32_bf16 v[176:179], a[172:175], a[136:139], v[176:179]
ds_read_b64_tr_b16 a[36:37], v49 offset:18432
ds_read_b64_tr_b16 a[38:39], v49 offset:18944
v_mfma_f32_16x16x32_bf16 v[180:183], a[192:195], a[136:139], v[180:183]
ds_read_b128 a[16:19], v44 offset:6144
v_mfma_f32_16x16x32_bf16 v[216:219], a[136:139], a[196:199], v[216:219]
buffer_load_dwordx4 v37, s[12:15], s24 offen offset:1024 lds
v_mfma_f32_16x16x32_bf16 v[220:223], a[136:139], a[200:203], v[220:223]
ds_read_b64_tr_b16 a[40:41], v50 offset:18432
ds_read_b64_tr_b16 a[42:43], v50 offset:18944
v_mfma_f32_16x16x32_bf16 v[144:147], a[164:167], a[140:143], v[144:147]
s_cmp_eq_u32 s39, 1
s_mov_b32 s28, 4294885376
s_cselect_b32 s28, 4294934528, s28
v_mfma_f32_16x16x32_bf16 v[148:151], a[168:171], a[140:143], v[148:151]
v_add_u32 v43, 4294885376, v43
v_add_u32 v44, 4294885376, v44
v_mfma_f32_16x16x32_bf16 v[184:187], a[172:175], a[140:143], v[184:187]
v_add_u32 v47, 4294934528, v47
v_add_u32 v48, 4294934528, v48
v_mfma_f32_16x16x32_bf16 v[188:191], a[192:195], a[140:143], v[188:191]
buffer_load_dwordx4 v38, s[12:15], s24 offen offset:2048 lds
v_mfma_f32_16x16x32_bf16 v[224:227], a[140:143], a[196:199], v[224:227]
v_add_u32 v49, 4294934528, v49
v_add_u32 v50, 4294934528, v50
v_mfma_f32_16x16x32_bf16 v[228:231], a[140:143], a[200:203], v[228:231]
v_add_u32 v45, s28, v45
v_add_u32 v46, s28, v46
v_mfma_f32_16x16x32_bf16 v[152:155], a[164:167], a[160:163], v[152:155]
s_bfe_u32 s51, s42, 0x10001
s_lshl_b32 s51, s51, 3
s_and_b32 s54, s87, 7
v_mfma_f32_16x16x32_bf16 v[156:159], a[168:171], a[160:163], v[156:159]
s_or_b32 s51, s51, s54
s_lshr_b32 s52, s42, 2
s_lshl_b32 s52, s52, 2
v_mfma_f32_16x16x32_bf16 v[192:195], a[172:175], a[160:163], v[192:195]
s_lshr_b32 s54, s87, 3
buffer_load_dwordx4 v39, s[12:15], s24 offen offset:3072 lds
v_mfma_f32_16x16x32_bf16 v[196:199], a[192:195], a[160:163], v[196:199]
s_add_u32 s24, s24, 128
s_or_b32 s52, s52, s54
s_lshl_b32 s52, s52, 8
v_mfma_f32_16x16x32_bf16 v[232:235], a[160:163], a[196:199], v[232:235]
s_and_b32 s55, s42, 1
s_lshl_b32 s54, s51, 2
s_add_u32 s54, s54, s3
v_mfma_f32_16x16x32_bf16 v[236:239], a[160:163], a[200:203], v[236:239]
s_waitcnt vmcnt(15) lgkmcnt(0)
s_barrier
s_lshl_b32 s54, s54, 18
v_mfma_f32_16x16x32_bf16 v[160:163], a[28:31], a[0:3], v[160:163]
ds_read_b64_tr_b16 a[164:165], v47 offset:0
ds_read_b64_tr_b16 a[166:167], v47 offset:512
v_mfma_f32_16x16x32_bf16 v[164:167], a[32:35], a[0:3], v[164:167]
s_add_u32 m0, s48, 131072
ds_read_b128 a[128:131], v45
v_mfma_f32_16x16x32_bf16 v[200:203], a[0:3], a[36:39], v[200:203]
s_add_u32 s54, s54, 33554432
buffer_load_dwordx4 v40, s[16:19], s26 offen offset:0 lds
v_mfma_f32_16x16x32_bf16 v[204:207], a[0:3], a[40:43], v[204:207]
ds_read_b64_tr_b16 a[168:169], v48 offset:0
ds_read_b64_tr_b16 a[170:171], v48 offset:512
v_mfma_f32_16x16x32_bf16 v[128:131], a[20:23], a[4:7], v[128:131]
ds_read_b128 a[132:135], v43 offset:0
v_mfma_f32_16x16x32_bf16 v[132:135], a[24:27], a[4:7], v[132:135]
ds_read_b64_tr_b16 a[172:173], v47 offset:2048
ds_read_b64_tr_b16 a[174:175], v47 offset:2560
.Lpad16:
s_cbranch_execz .Ltch6
v_mfma_f32_16x16x32_bf16 v[168:171], a[28:31], a[4:7], v[168:171]
ds_read_b128 a[136:139], v43 offset:2048
v_mfma_f32_16x16x32_bf16 v[172:175], a[32:35], a[4:7], v[172:175]
buffer_load_dwordx4 v41, s[16:19], s26 offen offset:1024 lds
v_mfma_f32_16x16x32_bf16 v[208:211], a[4:7], a[36:39], v[208:211]
ds_read_b64_tr_b16 a[192:193], v48 offset:2048
ds_read_b64_tr_b16 a[194:195], v48 offset:2560
v_mfma_f32_16x16x32_bf16 v[212:215], a[4:7], a[40:43], v[212:215]
ds_read_b128 a[140:143], v43 offset:4096
v_mfma_f32_16x16x32_bf16 v[136:139], a[20:23], a[8:11], v[136:139]
ds_read_b64_tr_b16 a[196:197], v49 offset:4096
ds_read_b64_tr_b16 a[198:199], v49 offset:4608
v_mfma_f32_16x16x32_bf16 v[140:143], a[24:27], a[8:11], v[140:143]
ds_read_b128 a[160:163], v43 offset:6144
v_mfma_f32_16x16x32_bf16 v[176:179], a[28:31], a[8:11], v[176:179]
buffer_load_dwordx4 v40, s[16:19], s26 offen offset:2048 lds
v_mfma_f32_16x16x32_bf16 v[180:183], a[32:35], a[8:11], v[180:183]
ds_read_b64_tr_b16 a[200:201], v50 offset:4096
ds_read_b64_tr_b16 a[202:203], v50 offset:4608
v_mfma_f32_16x16x32_bf16 v[216:219], a[8:11], a[36:39], v[216:219]
s_add_u32 s4, s84, s54
s_addc_u32 s5, s85, 0
s_and_b32 s5, s5, 0xffff
v_mfma_f32_16x16x32_bf16 v[220:223], a[8:11], a[40:43], v[220:223]
s_lshl_b32 s54, s3, 15
s_add_u32 s54, s54, s52
v_mfma_f32_16x16x32_bf16 v[144:147], a[20:23], a[12:15], v[144:147]
s_add_u32 s8, s80, s54
s_addc_u32 s9, s81, 0
s_and_b32 s9, s9, 0xffff
v_mfma_f32_16x16x32_bf16 v[148:151], a[24:27], a[12:15], v[148:151]
s_lshl_b32 s56, s43, 12
buffer_load_dwordx4 v41, s[16:19], s26 offen offset:3072 lds
v_mfma_f32_16x16x32_bf16 v[184:187], a[28:31], a[12:15], v[184:187]
s_lshl_b32 s57, s43, 17
s_mul_i32 s44, s55, s56
s_mul_i32 s45, s55, s57
v_mfma_f32_16x16x32_bf16 v[188:191], a[32:35], a[12:15], v[188:191]
s_cmp_eq_u32 s55, 0
s_cselect_b32 s6, s56, 0x40000
s_cselect_b32 s57, s57, 0x800000
v_mfma_f32_16x16x32_bf16 v[224:227], a[12:15], a[36:39], v[224:227]
s_sub_u32 s10, s57, s54
s_and_b32 s54, s3, 1
s_lshl_b32 s54, s54, 13
v_mfma_f32_16x16x32_bf16 v[228:231], a[12:15], a[40:43], v[228:231]
s_add_u32 m0, s48, 135168
s_add_u32 s54, s54, 98304
s_lshr_b32 s56, s3, 1
v_mfma_f32_16x16x32_bf16 v[152:155], a[20:23], a[16:19], v[152:155]
s_lshl_b32 s56, s56, 2
buffer_load_dwordx4 v40, s[16:19], s47 offen offset:0 lds
v_mfma_f32_16x16x32_bf16 v[156:159], a[24:27], a[16:19], v[156:159]
s_sub_u32 s57, 15, s51
s_min_u32 s57, s57, s51
s_lshl_b32 s57, s57, 10
v_mfma_f32_16x16x32_bf16 v[192:195], a[28:31], a[16:19], v[192:195]
v_lshrrev_b32 v252, 4, v1
v_and_b32 v253, 15, v1
v_mfma_f32_16x16x32_bf16 v[196:199], a[32:35], a[16:19], v[196:199]
v_lshlrev_b32 v254, 1, v252
v_xor_b32 v254, v254, v253
v_mfma_f32_16x16x32_bf16 v[232:235], a[16:19], a[36:39], v[232:235]
buffer_load_dwordx4 v41, s[16:19], s47 offen offset:1024 lds
v_lshlrev_b32 v254, 4, v254
v_mfma_f32_16x16x32_bf16 v[236:239], a[16:19], a[40:43], v[236:239]
s_waitcnt lgkmcnt(0)
v_lshl_add_u32 v240, v252, 11, v254
v_xor_b32 v241, 0x80, v240
v_mfma_f32_16x16x32_bf16 v[160:163], a[172:175], a[128:131], v[160:163]
ds_read_b64_tr_b16 a[20:21], v47 offset:14336
ds_read_b64_tr_b16 a[22:23], v47 offset:14848
v_mfma_f32_16x16x32_bf16 v[164:167], a[192:195], a[128:131], v[164:167]
ds_read_b128 a[0:3], v46
v_add_u32 v241, 7168, v241
v_mfma_f32_16x16x32_bf16 v[200:203], a[128:131], a[196:199], v[200:203]
ds_read_b64_tr_b16 a[24:25], v48 offset:14336
ds_read_b64_tr_b16 a[26:27], v48 offset:14848
v_mfma_f32_16x16x32_bf16 v[204:207], a[128:131], a[200:203], v[204:207]
buffer_load_dwordx4 v42, s[12:15], s25 offen offset:2048 lds
v_add_u32 v242, 14336, v240
v_mfma_f32_16x16x32_bf16 v[128:131], a[164:167], a[132:135], v[128:131]
s_add_u32 s26, s26, 393216
s_add_u32 s47, s47, 393216
v_add_u32 v243, 14336, v241
v_mfma_f32_16x16x32_bf16 v[132:135], a[168:171], a[132:135], v[132:135]
s_add_u32 s25, s25, 128
ds_read_b128 a[4:7], v44 offset:0
v_mfma_f32_16x16x32_bf16 v[168:171], a[172:175], a[132:135], v[168:171]
ds_read_b64_tr_b16 a[28:29], v47 offset:16384
ds_read_b64_tr_b16 a[30:31], v47 offset:16896
v_mfma_f32_16x16x32_bf16 v[172:175], a[192:195], a[132:135], v[172:175]
ds_read_b128 a[8:11], v44 offset:2048
v_lshlrev_b32 v254, 9, v252
v_mfma_f32_16x16x32_bf16 v[208:211], a[132:135], a[196:199], v[208:211]
s_add_u32 m0, s91, 81920
v_lshl_add_u32 v254, v253, 4, v254
v_add_u32 v244, s54, v254
v_mfma_f32_16x16x32_bf16 v[212:215], a[132:135], a[200:203], v[212:215]
buffer_load_dwordx4 v36, s[12:15], s24 offen offset:0 lds
v_bfe_u32 v254, v1, 2, 3
v_mfma_f32_16x16x32_bf16 v[136:139], a[164:167], a[136:139], v[136:139]
ds_read_b64_tr_b16 a[32:33], v48 offset:16384
ds_read_b64_tr_b16 a[34:35], v48 offset:16896
v_mfma_f32_16x16x32_bf16 v[140:143], a[168:171], a[136:139], v[140:143]
ds_read_b128 a[12:15], v44 offset:4096
v_lshrrev_b32 v255, 5, v1
v_mfma_f32_16x16x32_bf16 v[176:179], a[172:175], a[136:139], v[176:179]
ds_read_b64_tr_b16 a[36:37], v49 offset:18432
ds_read_b64_tr_b16 a[38:39], v49 offset:18944
v_mfma_f32_16x16x32_bf16 v[180:183], a[192:195], a[136:139], v[180:183]
ds_read_b128 a[16:19], v44 offset:6144
v_lshl_add_u32 v255, v255, 4, v254
v_mfma_f32_16x16x32_bf16 v[216:219], a[136:139], a[196:199], v[216:219]
buffer_load_dwordx4 v37, s[12:15], s24 offen offset:1024 lds
v_lshlrev_b32 v255, 8, v255
v_mfma_f32_16x16x32_bf16 v[220:223], a[136:139], a[200:203], v[220:223]
ds_read_b64_tr_b16 a[40:41], v50 offset:18432
ds_read_b64_tr_b16 a[42:43], v50 offset:18944
v_mfma_f32_16x16x32_bf16 v[144:147], a[164:167], a[140:143], v[144:147]
s_cmp_eq_u32 s39, 1
s_mov_b32 s28, 16384
s_cselect_b32 s28, 4294901760, s28
v_mfma_f32_16x16x32_bf16 v[148:151], a[168:171], a[140:143], v[148:151]
v_add_u32 v43, 16384, v43
v_add_u32 v44, 16384, v44
v_mfma_f32_16x16x32_bf16 v[184:187], a[172:175], a[140:143], v[184:187]
v_add_u32 v47, 4294901760, v47
v_add_u32 v48, 4294901760, v48
v_mfma_f32_16x16x32_bf16 v[188:191], a[192:195], a[140:143], v[188:191]
buffer_load_dwordx4 v38, s[12:15], s24 offen offset:2048 lds
v_and_b32 v253, 3, v1
v_mfma_f32_16x16x32_bf16 v[224:227], a[140:143], a[196:199], v[224:227]
v_add_u32 v49, 4294901760, v49
v_add_u32 v50, 4294901760, v50
v_mfma_f32_16x16x32_bf16 v[228:231], a[140:143], a[200:203], v[228:231]
v_add_u32 v45, s28, v45
v_add_u32 v46, s28, v46
v_mfma_f32_16x16x32_bf16 v[152:155], a[164:167], a[160:163], v[152:155]
v_lshl_add_u32 v255, v253, 3, v255
v_add_u32 v255, 114688, v255
v_mfma_f32_16x16x32_bf16 v[156:159], a[168:171], a[160:163], v[156:159]
v_xor_b32 v254, s56, v254
v_xor_b32 v253, 0, v254
v_mfma_f32_16x16x32_bf16 v[192:195], a[172:175], a[160:163], v[192:195]
buffer_load_dwordx4 v39, s[12:15], s24 offen offset:3072 lds
v_lshl_add_u32 v245, v253, 5, v255
v_mfma_f32_16x16x32_bf16 v[196:199], a[192:195], a[160:163], v[196:199]
s_add_u32 s24, s24, 128
v_xor_b32 v253, 1, v254
v_lshl_add_u32 v246, v253, 5, v255
v_mfma_f32_16x16x32_bf16 v[232:235], a[160:163], a[196:199], v[232:235]
v_xor_b32 v253, 2, v254
v_lshl_add_u32 v247, v253, 5, v255
v_mfma_f32_16x16x32_bf16 v[236:239], a[160:163], a[200:203], v[236:239]
s_waitcnt vmcnt(15) lgkmcnt(0)
s_barrier
v_xor_b32 v253, 3, v254
v_mfma_f32_16x16x32_bf16 v[160:163], a[28:31], a[0:3], v[160:163]
ds_read_b64_tr_b16 a[164:165], v47 offset:0
ds_read_b64_tr_b16 a[166:167], v47 offset:512
v_mfma_f32_16x16x32_bf16 v[164:167], a[32:35], a[0:3], v[164:167]
s_add_u32 m0, s48, 98304
ds_read_b128 a[128:131], v45
v_mfma_f32_16x16x32_bf16 v[200:203], a[0:3], a[36:39], v[200:203]
buffer_load_dwordx4 v40, s[16:19], s26 offen offset:0 lds
v_lshl_add_u32 v248, v253, 5, v255
v_mfma_f32_16x16x32_bf16 v[204:207], a[0:3], a[40:43], v[204:207]
ds_read_b64_tr_b16 a[168:169], v48 offset:0
ds_read_b64_tr_b16 a[170:171], v48 offset:512
v_mfma_f32_16x16x32_bf16 v[128:131], a[20:23], a[4:7], v[128:131]
ds_read_b128 a[132:135], v43 offset:0
v_and_b32 v253, 31, v1
v_mfma_f32_16x16x32_bf16 v[132:135], a[24:27], a[4:7], v[132:135]
ds_read_b64_tr_b16 a[172:173], v47 offset:2048
ds_read_b64_tr_b16 a[174:175], v47 offset:2560
v_mfma_f32_16x16x32_bf16 v[168:171], a[28:31], a[4:7], v[168:171]
ds_read_b128 a[136:139], v43 offset:2048
v_lshl_add_u32 v249, v253, 5, s57
v_mfma_f32_16x16x32_bf16 v[172:175], a[32:35], a[4:7], v[172:175]
buffer_load_dwordx4 v41, s[16:19], s26 offen offset:1024 lds
v_mfma_f32_16x16x32_bf16 v[208:211], a[4:7], a[36:39], v[208:211]
ds_read_b64_tr_b16 a[192:193], v48 offset:2048
ds_read_b64_tr_b16 a[194:195], v48 offset:2560
v_mfma_f32_16x16x32_bf16 v[212:215], a[4:7], a[40:43], v[212:215]
ds_read_b128 a[140:143], v43 offset:4096
v_mfma_f32_16x16x32_bf16 v[136:139], a[20:23], a[8:11], v[136:139]
ds_read_b64_tr_b16 a[196:197], v49 offset:4096
ds_read_b64_tr_b16 a[198:199], v49 offset:4608
v_mfma_f32_16x16x32_bf16 v[140:143], a[24:27], a[8:11], v[140:143]
ds_read_b128 a[160:163], v43 offset:6144
v_mfma_f32_16x16x32_bf16 v[176:179], a[28:31], a[8:11], v[176:179]
buffer_load_dwordx4 v40, s[16:19], s26 offen offset:2048 lds
v_mfma_f32_16x16x32_bf16 v[180:183], a[32:35], a[8:11], v[180:183]
ds_read_b64_tr_b16 a[200:201], v50 offset:4096
ds_read_b64_tr_b16 a[202:203], v50 offset:4608
v_mfma_f32_16x16x32_bf16 v[216:219], a[8:11], a[36:39], v[216:219]
v_mfma_f32_16x16x32_bf16 v[220:223], a[8:11], a[40:43], v[220:223]
v_mfma_f32_16x16x32_bf16 v[144:147], a[20:23], a[12:15], v[144:147]
v_mfma_f32_16x16x32_bf16 v[148:151], a[24:27], a[12:15], v[148:151]
buffer_load_dwordx4 v41, s[16:19], s26 offen offset:3072 lds
v_mfma_f32_16x16x32_bf16 v[184:187], a[28:31], a[12:15], v[184:187]
v_mfma_f32_16x16x32_bf16 v[188:191], a[32:35], a[12:15], v[188:191]
v_mfma_f32_16x16x32_bf16 v[224:227], a[12:15], a[36:39], v[224:227]
v_mfma_f32_16x16x32_bf16 v[228:231], a[12:15], a[40:43], v[228:231]
s_add_u32 m0, s48, 102400
v_mfma_f32_16x16x32_bf16 v[152:155], a[20:23], a[16:19], v[152:155]
buffer_load_dwordx4 v40, s[16:19], s47 offen offset:0 lds
v_mfma_f32_16x16x32_bf16 v[156:159], a[24:27], a[16:19], v[156:159]
v_mfma_f32_16x16x32_bf16 v[192:195], a[28:31], a[16:19], v[192:195]
buffer_load_dword v35, v4, s[96:99], 0 offen
v_mfma_f32_16x16x32_bf16 v[196:199], a[32:35], a[16:19], v[196:199]
v_mfma_f32_16x16x32_bf16 v[232:235], a[16:19], a[36:39], v[232:235]
buffer_load_dwordx4 v41, s[16:19], s47 offen offset:1024 lds
v_mfma_f32_16x16x32_bf16 v[236:239], a[16:19], a[40:43], v[236:239]
s_waitcnt lgkmcnt(0)
v_mfma_f32_16x16x32_bf16 v[160:163], a[172:175], a[128:131], v[160:163]
ds_read_b64_tr_b16 a[20:21], v47 offset:14336
ds_read_b64_tr_b16 a[22:23], v47 offset:14848
v_mfma_f32_16x16x32_bf16 v[164:167], a[192:195], a[128:131], v[164:167]
ds_read_b128 a[0:3], v46
v_mfma_f32_16x16x32_bf16 v[200:203], a[128:131], a[196:199], v[200:203]
ds_read_b64_tr_b16 a[24:25], v48 offset:14336
ds_read_b64_tr_b16 a[26:27], v48 offset:14848
v_mfma_f32_16x16x32_bf16 v[204:207], a[128:131], a[200:203], v[204:207]
buffer_load_dwordx4 v42, s[12:15], s25 offen offset:2048 lds
v_mfma_f32_16x16x32_bf16 v[128:131], a[164:167], a[132:135], v[128:131]
s_add_u32 s26, s26, 393216
s_add_u32 s47, s47, 393216
v_mfma_f32_16x16x32_bf16 v[132:135], a[168:171], a[132:135], v[132:135]
s_add_u32 s25, s25, 128
ds_read_b128 a[4:7], v44 offset:0
v_mfma_f32_16x16x32_bf16 v[168:171], a[172:175], a[132:135], v[168:171]
ds_read_b64_tr_b16 a[28:29], v47 offset:16384
ds_read_b64_tr_b16 a[30:31], v47 offset:16896
v_mfma_f32_16x16x32_bf16 v[172:175], a[192:195], a[132:135], v[172:175]
ds_read_b128 a[8:11], v44 offset:2048
v_mfma_f32_16x16x32_bf16 v[208:211], a[132:135], a[196:199], v[208:211]
s_add_u32 m0, s91, 0
v_mfma_f32_16x16x32_bf16 v[212:215], a[132:135], a[200:203], v[212:215]
buffer_load_dwordx4 v36, s[12:15], s24 offen offset:0 lds
v_mfma_f32_16x16x32_bf16 v[136:139], a[164:167], a[136:139], v[136:139]
ds_read_b64_tr_b16 a[32:33], v48 offset:16384
ds_read_b64_tr_b16 a[34:35], v48 offset:16896
v_mfma_f32_16x16x32_bf16 v[140:143], a[168:171], a[136:139], v[140:143]
ds_read_b128 a[12:15], v44 offset:4096
v_mfma_f32_16x16x32_bf16 v[176:179], a[172:175], a[136:139], v[176:179]
ds_read_b64_tr_b16 a[36:37], v49 offset:18432
ds_read_b64_tr_b16 a[38:39], v49 offset:18944
v_mfma_f32_16x16x32_bf16 v[180:183], a[192:195], a[136:139], v[180:183]
ds_read_b128 a[16:19], v44 offset:6144
v_mfma_f32_16x16x32_bf16 v[216:219], a[136:139], a[196:199], v[216:219]
buffer_load_dwordx4 v37, s[12:15], s24 offen offset:1024 lds
v_mfma_f32_16x16x32_bf16 v[220:223], a[136:139], a[200:203], v[220:223]
ds_read_b64_tr_b16 a[40:41], v50 offset:18432
ds_read_b64_tr_b16 a[42:43], v50 offset:18944
v_mfma_f32_16x16x32_bf16 v[144:147], a[164:167], a[140:143], v[144:147]
s_cmp_eq_u32 s39, 1
s_mov_b32 s28, 49152
s_cselect_b32 s28, 98304, s28
v_mfma_f32_16x16x32_bf16 v[148:151], a[168:171], a[140:143], v[148:151]
v_add_u32 v43, 49152, v43
v_add_u32 v44, 49152, v44
v_mfma_f32_16x16x32_bf16 v[184:187], a[172:175], a[140:143], v[184:187]
v_add_u32 v47, 98304, v47
v_add_u32 v48, 98304, v48
v_mfma_f32_16x16x32_bf16 v[188:191], a[192:195], a[140:143], v[188:191]
buffer_load_dwordx4 v38, s[12:15], s24 offen offset:2048 lds
v_mfma_f32_16x16x32_bf16 v[224:227], a[140:143], a[196:199], v[224:227]
v_add_u32 v49, 98304, v49
v_add_u32 v50, 98304, v50
v_mfma_f32_16x16x32_bf16 v[228:231], a[140:143], a[200:203], v[228:231]
v_add_u32 v45, s28, v45
v_add_u32 v46, s28, v46
v_mfma_f32_16x16x32_bf16 v[152:155], a[164:167], a[160:163], v[152:155]
v_mfma_f32_16x16x32_bf16 v[156:159], a[168:171], a[160:163], v[156:159]
v_mfma_f32_16x16x32_bf16 v[192:195], a[172:175], a[160:163], v[192:195]
buffer_load_dwordx4 v39, s[12:15], s24 offen offset:3072 lds
v_mfma_f32_16x16x32_bf16 v[196:199], a[192:195], a[160:163], v[196:199]
s_add_u32 s24, s24, 128
v_mfma_f32_16x16x32_bf16 v[232:235], a[160:163], a[196:199], v[232:235]
v_mfma_f32_16x16x32_bf16 v[236:239], a[160:163], a[200:203], v[236:239]
s_waitcnt vmcnt(16) lgkmcnt(0)
s_barrier
v_mfma_f32_16x16x32_bf16 v[160:163], a[28:31], a[0:3], v[160:163]
ds_read_b64_tr_b16 a[164:165], v47 offset:0
ds_read_b64_tr_b16 a[166:167], v47 offset:512
v_mfma_f32_16x16x32_bf16 v[164:167], a[32:35], a[0:3], v[164:167]
s_add_u32 m0, s48, 32768
ds_read_b128 a[128:131], v45
v_mfma_f32_16x16x32_bf16 v[200:203], a[0:3], a[36:39], v[200:203]
buffer_load_dwordx4 v40, s[16:19], s26 offen offset:0 lds
v_mfma_f32_16x16x32_bf16 v[204:207], a[0:3], a[40:43], v[204:207]
ds_read_b64_tr_b16 a[168:169], v48 offset:0
ds_read_b64_tr_b16 a[170:171], v48 offset:512
v_mfma_f32_16x16x32_bf16 v[128:131], a[20:23], a[4:7], v[128:131]
ds_read_b128 a[132:135], v43 offset:0
v_mfma_f32_16x16x32_bf16 v[132:135], a[24:27], a[4:7], v[132:135]
ds_read_b64_tr_b16 a[172:173], v47 offset:2048
ds_read_b64_tr_b16 a[174:175], v47 offset:2560
v_mfma_f32_16x16x32_bf16 v[168:171], a[28:31], a[4:7], v[168:171]
ds_read_b128 a[136:139], v43 offset:2048
v_mfma_f32_16x16x32_bf16 v[172:175], a[32:35], a[4:7], v[172:175]
buffer_load_dwordx4 v41, s[16:19], s26 offen offset:1024 lds
v_mfma_f32_16x16x32_bf16 v[208:211], a[4:7], a[36:39], v[208:211]
ds_read_b64_tr_b16 a[192:193], v48 offset:2048
ds_read_b64_tr_b16 a[194:195], v48 offset:2560
v_mfma_f32_16x16x32_bf16 v[212:215], a[4:7], a[40:43], v[212:215]
ds_read_b128 a[140:143], v43 offset:4096
v_mfma_f32_16x16x32_bf16 v[136:139], a[20:23], a[8:11], v[136:139]
ds_read_b64_tr_b16 a[196:197], v49 offset:4096
ds_read_b64_tr_b16 a[198:199], v49 offset:4608
v_mfma_f32_16x16x32_bf16 v[140:143], a[24:27], a[8:11], v[140:143]
ds_read_b128 a[160:163], v43 offset:6144
v_mfma_f32_16x16x32_bf16 v[176:179], a[28:31], a[8:11], v[176:179]
buffer_load_dwordx4 v40, s[16:19], s26 offen offset:2048 lds
v_mfma_f32_16x16x32_bf16 v[180:183], a[32:35], a[8:11], v[180:183]
ds_read_b64_tr_b16 a[200:201], v50 offset:4096
ds_read_b64_tr_b16 a[202:203], v50 offset:4608
v_mfma_f32_16x16x32_bf16 v[216:219], a[8:11], a[36:39], v[216:219]
v_mfma_f32_16x16x32_bf16 v[220:223], a[8:11], a[40:43], v[220:223]
v_mfma_f32_16x16x32_bf16 v[144:147], a[20:23], a[12:15], v[144:147]
v_mfma_f32_16x16x32_bf16 v[148:151], a[24:27], a[12:15], v[148:151]
buffer_load_dwordx4 v41, s[16:19], s26 offen offset:3072 lds
v_mfma_f32_16x16x32_bf16 v[184:187], a[28:31], a[12:15], v[184:187]
v_mfma_f32_16x16x32_bf16 v[188:191], a[32:35], a[12:15], v[188:191]
v_mfma_f32_16x16x32_bf16 v[224:227], a[12:15], a[36:39], v[224:227]
v_mfma_f32_16x16x32_bf16 v[228:231], a[12:15], a[40:43], v[228:231]
s_add_u32 m0, s48, 36864
v_mfma_f32_16x16x32_bf16 v[152:155], a[20:23], a[16:19], v[152:155]
buffer_load_dwordx4 v40, s[16:19], s47 offen offset:0 lds
v_mfma_f32_16x16x32_bf16 v[156:159], a[24:27], a[16:19], v[156:159]
v_mfma_f32_16x16x32_bf16 v[192:195], a[28:31], a[16:19], v[192:195]
v_mfma_f32_16x16x32_bf16 v[196:199], a[32:35], a[16:19], v[196:199]
v_mfma_f32_16x16x32_bf16 v[232:235], a[16:19], a[36:39], v[232:235]
buffer_load_dwordx4 v41, s[16:19], s47 offen offset:1024 lds
v_mfma_f32_16x16x32_bf16 v[236:239], a[16:19], a[40:43], v[236:239]
s_waitcnt lgkmcnt(0)
v_mfma_f32_16x16x32_bf16 v[160:163], a[172:175], a[128:131], v[160:163]
ds_read_b64_tr_b16 a[20:21], v47 offset:14336
ds_read_b64_tr_b16 a[22:23], v47 offset:14848
v_mfma_f32_16x16x32_bf16 v[164:167], a[192:195], a[128:131], v[164:167]
ds_read_b128 a[0:3], v46
v_and_b32 v28, 15, v1
v_mfma_f32_16x16x32_bf16 v[200:203], a[128:131], a[196:199], v[200:203]
ds_read_b64_tr_b16 a[24:25], v48 offset:14336
ds_read_b64_tr_b16 a[26:27], v48 offset:14848
v_mfma_f32_16x16x32_bf16 v[204:207], a[128:131], a[200:203], v[204:207]
buffer_load_dwordx4 v42, s[12:15], s25 offen offset:2048 lds
v_lshrrev_b32 v29, 4, v1
v_mfma_f32_16x16x32_bf16 v[128:131], a[164:167], a[132:135], v[128:131]
s_add_u32 s26, s26, 393216
s_add_u32 s47, s47, 393216
v_lshlrev_b32 v29, 2, v29
v_mfma_f32_16x16x32_bf16 v[132:135], a[168:171], a[132:135], v[132:135]
s_add_u32 s25, s25, 128
ds_read_b128 a[4:7], v44 offset:0
v_mfma_f32_16x16x32_bf16 v[168:171], a[172:175], a[132:135], v[168:171]
ds_read_b64_tr_b16 a[28:29], v47 offset:16384
ds_read_b64_tr_b16 a[30:31], v47 offset:16896
v_mfma_f32_16x16x32_bf16 v[172:175], a[192:195], a[132:135], v[172:175]
ds_read_b128 a[8:11], v44 offset:2048
v_sub_u32 v28, v28, v29
v_mfma_f32_16x16x32_bf16 v[208:211], a[132:135], a[196:199], v[208:211]
s_add_u32 m0, s91, 16384
v_mov_b32 v30, 0
v_add_u32 v29, 16, v28
v_mfma_f32_16x16x32_bf16 v[212:215], a[132:135], a[200:203], v[212:215]
buffer_load_dwordx4 v36, s[12:15], s24 offen offset:0 lds
v_and_b32 v29, 0xfffffff1, v29
v_mfma_f32_16x16x32_bf16 v[136:139], a[164:167], a[136:139], v[136:139]
ds_read_b64_tr_b16 a[32:33], v48 offset:16384
ds_read_b64_tr_b16 a[34:35], v48 offset:16896
v_mfma_f32_16x16x32_bf16 v[140:143], a[168:171], a[136:139], v[140:143]
ds_read_b128 a[12:15], v44 offset:4096
v_cmp_eq_u32 vcc, 0, v29
v_mfma_f32_16x16x32_bf16 v[176:179], a[172:175], a[136:139], v[176:179]
ds_read_b64_tr_b16 a[36:37], v49 offset:18432
ds_read_b64_tr_b16 a[38:39], v49 offset:18944
v_mfma_f32_16x16x32_bf16 v[180:183], a[192:195], a[136:139], v[180:183]
ds_read_b128 a[16:19], v44 offset:6144
v_nop
v_mfma_f32_16x16x32_bf16 v[216:219], a[136:139], a[196:199], v[216:219]
buffer_load_dwordx4 v37, s[12:15], s24 offen offset:1024 lds
v_nop
v_mfma_f32_16x16x32_bf16 v[220:223], a[136:139], a[200:203], v[220:223]
ds_read_b64_tr_b16 a[40:41], v50 offset:18432
ds_read_b64_tr_b16 a[42:43], v50 offset:18944
v_mfma_f32_16x16x32_bf16 v[144:147], a[164:167], a[140:143], v[144:147]
s_cmp_eq_u32 s39, 1
s_mov_b32 s28, 16384
s_cselect_b32 s28, 4294934528, s28
v_mfma_f32_16x16x32_bf16 v[148:151], a[168:171], a[140:143], v[148:151]
v_add_u32 v43, 16384, v43
v_add_u32 v44, 16384, v44
v_mfma_f32_16x16x32_bf16 v[184:187], a[172:175], a[140:143], v[184:187]
v_add_u32 v47, 4294934528, v47
v_add_u32 v48, 4294934528, v48
v_mfma_f32_16x16x32_bf16 v[188:191], a[192:195], a[140:143], v[188:191]
buffer_load_dwordx4 v38, s[12:15], s24 offen offset:2048 lds
v_cndmask_b32 v16, v34, v30, vcc
v_mfma_f32_16x16x32_bf16 v[224:227], a[140:143], a[196:199], v[224:227]
v_add_u32 v49, 4294934528, v49
v_add_u32 v50, 4294934528, v50
v_mfma_f32_16x16x32_bf16 v[228:231], a[140:143], a[200:203], v[228:231]
v_add_u32 v45, s28, v45
v_add_u32 v46, s28, v46
v_mfma_f32_16x16x32_bf16 v[152:155], a[164:167], a[160:163], v[152:155]
v_add_u32 v29, 15, v28
v_and_b32 v29, 0xfffffff1, v29
v_mfma_f32_16x16x32_bf16 v[156:159], a[168:171], a[160:163], v[156:159]
v_cmp_eq_u32 vcc, 0, v29
v_nop
v_mfma_f32_16x16x32_bf16 v[192:195], a[172:175], a[160:163], v[192:195]
buffer_load_dwordx4 v39, s[12:15], s24 offen offset:3072 lds
v_nop
v_mfma_f32_16x16x32_bf16 v[196:199], a[192:195], a[160:163], v[196:199]
s_add_u32 s24, s24, 128
v_cndmask_b32 v17, v34, v30, vcc
v_add_u32 v29, 14, v28
v_mfma_f32_16x16x32_bf16 v[232:235], a[160:163], a[196:199], v[232:235]
buffer_load_dword v250, v249, s[60:63], s90 offen sc1
v_and_b32 v29, 0xfffffff1, v29
v_cmp_eq_u32 vcc, 0, v29
v_mfma_f32_16x16x32_bf16 v[236:239], a[160:163], a[200:203], v[236:239]
s_waitcnt vmcnt(16) lgkmcnt(0)
s_barrier
v_nop
v_mfma_f32_16x16x32_bf16 v[160:163], a[28:31], a[0:3], v[160:163]
ds_read_b64_tr_b16 a[164:165], v47 offset:0
ds_read_b64_tr_b16 a[166:167], v47 offset:512
v_mfma_f32_16x16x32_bf16 v[164:167], a[32:35], a[0:3], v[164:167]
s_add_u32 m0, s48, 131072
ds_read_b128 a[128:131], v45
v_mfma_f32_16x16x32_bf16 v[200:203], a[0:3], a[36:39], v[200:203]
buffer_load_dwordx4 v40, s[16:19], s26 offen offset:0 lds
v_nop
v_mfma_f32_16x16x32_bf16 v[204:207], a[0:3], a[40:43], v[204:207]
ds_read_b64_tr_b16 a[168:169], v48 offset:0
ds_read_b64_tr_b16 a[170:171], v48 offset:512
v_mfma_f32_16x16x32_bf16 v[128:131], a[20:23], a[4:7], v[128:131]
ds_read_b128 a[132:135], v43 offset:0
v_cndmask_b32 v18, v34, v30, vcc
v_mfma_f32_16x16x32_bf16 v[132:135], a[24:27], a[4:7], v[132:135]
ds_read_b64_tr_b16 a[172:173], v47 offset:2048
ds_read_b64_tr_b16 a[174:175], v47 offset:2560
v_mfma_f32_16x16x32_bf16 v[168:171], a[28:31], a[4:7], v[168:171]
ds_read_b128 a[136:139], v43 offset:2048
v_add_u32 v29, 13, v28
v_mfma_f32_16x16x32_bf16 v[172:175], a[32:35], a[4:7], v[172:175]
buffer_load_dwordx4 v41, s[16:19], s26 offen offset:1024 lds
v_and_b32 v29, 0xfffffff1, v29
v_mfma_f32_16x16x32_bf16 v[208:211], a[4:7], a[36:39], v[208:211]
ds_read_b64_tr_b16 a[192:193], v48 offset:2048
ds_read_b64_tr_b16 a[194:195], v48 offset:2560
v_mfma_f32_16x16x32_bf16 v[212:215], a[4:7], a[40:43], v[212:215]
ds_read_b128 a[140:143], v43 offset:4096
v_cmp_eq_u32 vcc, 0, v29
v_mfma_f32_16x16x32_bf16 v[136:139], a[20:23], a[8:11], v[136:139]
ds_read_b64_tr_b16 a[196:197], v49 offset:4096
ds_read_b64_tr_b16 a[198:199], v49 offset:4608
v_mfma_f32_16x16x32_bf16 v[140:143], a[24:27], a[8:11], v[140:143]
ds_read_b128 a[160:163], v43 offset:6144
v_nop
v_mfma_f32_16x16x32_bf16 v[176:179], a[28:31], a[8:11], v[176:179]
buffer_load_dwordx4 v40, s[16:19], s26 offen offset:2048 lds
v_nop
v_mfma_f32_16x16x32_bf16 v[180:183], a[32:35], a[8:11], v[180:183]
ds_read_b64_tr_b16 a[200:201], v50 offset:4096
ds_read_b64_tr_b16 a[202:203], v50 offset:4608
v_mfma_f32_16x16x32_bf16 v[216:219], a[8:11], a[36:39], v[216:219]
v_cndmask_b32 v19, v34, v30, vcc
v_add_u32 v29, 0, v28
v_mfma_f32_16x16x32_bf16 v[220:223], a[8:11], a[40:43], v[220:223]
v_and_b32 v29, 0xfffffff1, v29
v_cmp_eq_u32 vcc, 0, v29
v_mfma_f32_16x16x32_bf16 v[144:147], a[20:23], a[12:15], v[144:147]
v_nop
v_nop
v_mfma_f32_16x16x32_bf16 v[148:151], a[24:27], a[12:15], v[148:151]
buffer_load_dwordx4 v41, s[16:19], s26 offen offset:3072 lds
v_cndmask_b32 v20, v34, v30, vcc
v_mfma_f32_16x16x32_bf16 v[184:187], a[28:31], a[12:15], v[184:187]
v_add_u32 v29, -1, v28
v_and_b32 v29, 0xfffffff1, v29
v_mfma_f32_16x16x32_bf16 v[188:191], a[32:35], a[12:15], v[188:191]
v_cmp_eq_u32 vcc, 0, v29
v_nop
v_mfma_f32_16x16x32_bf16 v[224:227], a[12:15], a[36:39], v[224:227]
v_nop
v_cndmask_b32 v21, v34, v30, vcc
v_mfma_f32_16x16x32_bf16 v[228:231], a[12:15], a[40:43], v[228:231]
s_add_u32 m0, s48, 135168
v_add_u32 v29, -2, v28
v_and_b32 v29, 0xfffffff1, v29
v_mfma_f32_16x16x32_bf16 v[152:155], a[20:23], a[16:19], v[152:155]
buffer_load_dwordx4 v40, s[16:19], s47 offen offset:0 lds
v_cmp_eq_u32 vcc, 0, v29
v_mfma_f32_16x16x32_bf16 v[156:159], a[24:27], a[16:19], v[156:159]
v_nop
v_nop
v_mfma_f32_16x16x32_bf16 v[192:195], a[28:31], a[16:19], v[192:195]
v_cndmask_b32 v22, v34, v30, vcc
v_add_u32 v29, -3, v28
v_mfma_f32_16x16x32_bf16 v[196:199], a[32:35], a[16:19], v[196:199]
v_and_b32 v29, 0xfffffff1, v29
v_cmp_eq_u32 vcc, 0, v29
v_mfma_f32_16x16x32_bf16 v[232:235], a[16:19], a[36:39], v[232:235]
buffer_load_dwordx4 v41, s[16:19], s47 offen offset:1024 lds
v_nop
v_mfma_f32_16x16x32_bf16 v[236:239], a[16:19], a[40:43], v[236:239]
s_waitcnt lgkmcnt(0)
v_nop
v_cndmask_b32 v23, v34, v30, vcc
v_mfma_f32_16x16x32_bf16 v[160:163], a[172:175], a[128:131], v[160:163]
ds_read_b64_tr_b16 a[20:21], v47 offset:14336
ds_read_b64_tr_b16 a[22:23], v47 offset:14848
v_mfma_f32_16x16x32_bf16 v[164:167], a[192:195], a[128:131], v[164:167]
s_lshr_b32 s29, s3, 1
ds_read_b128 a[0:3], v46
v_mfma_f32_16x16x32_bf16 v[200:203], a[128:131], a[196:199], v[200:203]
ds_read_b64_tr_b16 a[24:25], v48 offset:14336
ds_read_b64_tr_b16 a[26:27], v48 offset:14848
v_mfma_f32_16x16x32_bf16 v[204:207], a[128:131], a[200:203], v[204:207]
s_or_b32 s29, s29, s36
buffer_load_dwordx4 v42, s[12:15], s25 offen offset:2048 lds
v_mfma_f32_16x16x32_bf16 v[128:131], a[164:167], a[132:135], v[128:131]
s_add_u32 s26, s26, 393216
s_add_u32 s47, s47, 393216
v_mfma_f32_16x16x32_bf16 v[132:135], a[168:171], a[132:135], v[132:135]
s_add_u32 s25, s25, 128
ds_read_b128 a[4:7], v44 offset:0
v_mfma_f32_16x16x32_bf16 v[168:171], a[172:175], a[132:135], v[168:171]
ds_read_b64_tr_b16 a[28:29], v47 offset:16384
ds_read_b64_tr_b16 a[30:31], v47 offset:16896
v_mfma_f32_16x16x32_bf16 v[172:175], a[192:195], a[132:135], v[172:175]
ds_read_b128 a[8:11], v44 offset:2048
v_mfma_f32_16x16x32_bf16 v[208:211], a[132:135], a[196:199], v[208:211]
ds_read_b64_tr_b16 a[32:33], v48 offset:16384
ds_read_b64_tr_b16 a[34:35], v48 offset:16896
v_mfma_f32_16x16x32_bf16 v[212:215], a[132:135], a[200:203], v[212:215]
ds_read_b128 a[12:15], v44 offset:4096
v_mfma_f32_16x16x32_bf16 v[136:139], a[164:167], a[136:139], v[136:139]
ds_read_b64_tr_b16 a[36:37], v49 offset:18432
ds_read_b64_tr_b16 a[38:39], v49 offset:18944
v_mfma_f32_16x16x32_bf16 v[140:143], a[168:171], a[136:139], v[140:143]
ds_read_b128 a[16:19], v44 offset:6144
v_mfma_f32_16x16x32_bf16 v[176:179], a[172:175], a[136:139], v[176:179]
ds_read_b64_tr_b16 a[40:41], v50 offset:18432
ds_read_b64_tr_b16 a[42:43], v50 offset:18944
v_mfma_f32_16x16x32_bf16 v[180:183], a[192:195], a[136:139], v[180:183]
s_cmp_eq_u32 s39, 1
s_mov_b32 s28, 4294885376
s_cselect_b32 s28, 4294901760, s28
v_mfma_f32_16x16x32_bf16 v[216:219], a[136:139], a[196:199], v[216:219]
v_add_u32 v43, 4294885376, v43
v_add_u32 v44, 4294885376, v44
v_mfma_f32_16x16x32_bf16 v[220:223], a[136:139], a[200:203], v[220:223]
v_add_u32 v47, 4294901760, v47
v_add_u32 v48, 4294901760, v48
v_mfma_f32_16x16x32_bf16 v[144:147], a[164:167], a[140:143], v[144:147]
v_add_u32 v49, 4294901760, v49
v_add_u32 v50, 4294901760, v50
v_mfma_f32_16x16x32_bf16 v[148:151], a[168:171], a[140:143], v[148:151]
v_add_u32 v45, s28, v45
v_add_u32 v46, s28, v46
v_mfma_f32_16x16x32_bf16 v[184:187], a[172:175], a[140:143], v[184:187]
s_cmp_eq_u32 s29, 0
s_cselect_b64 s[32:33], -1, 0
v_mfma_f32_16x16x32_bf16 v[188:191], a[192:195], a[140:143], v[188:191]
v_cndmask_b32 v24, v16, v34, s[32:33]
v_cndmask_b32 v25, v17, v34, s[32:33]
v_mfma_f32_16x16x32_bf16 v[224:227], a[140:143], a[196:199], v[224:227]
.Lpad17:
s_cbranch_execz .Ltch7
s_waitcnt vmcnt(7)
v_cndmask_b32 v26, v18, v34, s[32:33]
v_cndmask_b32 v27, v19, v34, s[32:33]
v_and_b32 v250, 7, v250
v_mfma_f32_16x16x32_bf16 v[228:231], a[140:143], a[200:203], v[228:231]
v_mfma_f32_16x16x32_bf16 v[152:155], a[164:167], a[160:163], v[152:155]
v_cmp_gt_u32 vcc, 3, v250
v_mfma_f32_16x16x32_bf16 v[156:159], a[168:171], a[160:163], v[156:159]
v_mfma_f32_16x16x32_bf16 v[192:195], a[172:175], a[160:163], v[192:195]
s_cmp_lg_u64 vcc, 0
s_cbranch_scc1 .Lspin3
.Lspin3_ret:
v_mfma_f32_16x16x32_bf16 v[196:199], a[192:195], a[160:163], v[196:199]
v_mfma_f32_16x16x32_bf16 v[232:235], a[160:163], a[196:199], v[232:235]
s_lshl_b32 s54, s51, 19
s_and_b32 s56, s3, 1
s_lshl_b32 s56, s56, 18
v_mfma_f32_16x16x32_bf16 v[236:239], a[160:163], a[200:203], v[236:239]
s_waitcnt vmcnt(12) lgkmcnt(0)
s_barrier
s_add_u32 s54, s54, s56
v_mfma_f32_16x16x32_bf16 v[160:163], a[28:31], a[0:3], v[160:163]
ds_read_b64_tr_b16 a[164:165], v47 offset:0
ds_read_b64_tr_b16 a[166:167], v47 offset:512
v_mfma_f32_16x16x32_bf16 v[164:167], a[32:35], a[0:3], v[164:167]
s_add_u32 m0, s91, 114688
ds_read_b128 a[128:131], v45
v_mfma_f32_16x16x32_bf16 v[200:203], a[0:3], a[36:39], v[200:203]
buffer_load_dwordx4 v240, s[8:11], s45 offen offset:0 lds
ds_read_b64_tr_b16 a[168:169], v48 offset:0
ds_read_b64_tr_b16 a[170:171], v48 offset:512
v_mfma_f32_16x16x32_bf16 v[204:207], a[0:3], a[40:43], v[204:207]
s_add_u32 s54, s54, s52
ds_read_b128 a[132:135], v43 offset:0
v_mfma_f32_16x16x32_bf16 v[128:131], a[20:23], a[4:7], v[128:131]
ds_read_b64_tr_b16 a[172:173], v47 offset:2048
ds_read_b64_tr_b16 a[174:175], v47 offset:2560
v_mfma_f32_16x16x32_bf16 v[132:135], a[24:27], a[4:7], v[132:135]
buffer_load_dwordx4 v241, s[8:11], s45 offen offset:1024 lds
ds_read_b128 a[136:139], v43 offset:2048
v_mfma_f32_16x16x32_bf16 v[168:171], a[28:31], a[4:7], v[168:171]
ds_read_b64_tr_b16 a[192:193], v48 offset:2048
ds_read_b64_tr_b16 a[194:195], v48 offset:2560
v_mfma_f32_16x16x32_bf16 v[172:175], a[32:35], a[4:7], v[172:175]
s_lshr_b32 s56, s3, 1
ds_read_b128 a[140:143], v43 offset:4096
v_mfma_f32_16x16x32_bf16 v[208:211], a[4:7], a[36:39], v[208:211]
buffer_load_dwordx4 v242, s[8:11], s45 offen offset:2048 lds
ds_read_b64_tr_b16 a[196:197], v49 offset:4096
ds_read_b64_tr_b16 a[198:199], v49 offset:4608
v_mfma_f32_16x16x32_bf16 v[212:215], a[4:7], a[40:43], v[212:215]
s_lshl_b32 s56, s56, 7
ds_read_b128 a[160:163], v43 offset:6144
v_mfma_f32_16x16x32_bf16 v[136:139], a[20:23], a[8:11], v[136:139]
ds_read_b64_tr_b16 a[200:201], v50 offset:4096
ds_read_b64_tr_b16 a[202:203], v50 offset:4608
v_mfma_f32_16x16x32_bf16 v[140:143], a[24:27], a[8:11], v[140:143]
s_add_u32 s54, s54, s56
buffer_load_dwordx4 v243, s[8:11], s45 offen offset:3072 lds
v_mfma_f32_16x16x32_bf16 v[176:179], a[28:31], a[8:11], v[176:179]
s_add_u32 s45, s45, 131072
s_xor_b32 s56, s55, 1
s_lshl_b32 s56, s56, 11
v_mfma_f32_16x16x32_bf16 v[180:183], a[32:35], a[8:11], v[180:183]
s_add_u32 m0, s91, 98304
s_add_u32 s54, s54, s56
s_sub_u32 s18, 0x800000, s54
v_mfma_f32_16x16x32_bf16 v[216:219], a[8:11], a[36:39], v[216:219]
buffer_load_dwordx4 v7, s[4:7], s44 offen offset:0 sc1 lds
v_mfma_f32_16x16x32_bf16 v[220:223], a[8:11], a[40:43], v[220:223]
s_add_u32 s16, s82, s54
s_addc_u32 s17, s83, 0
s_and_b32 s17, s17, 0xffff
v_mfma_f32_16x16x32_bf16 v[144:147], a[20:23], a[12:15], v[144:147]
s_lshr_b32 s54, s42, 1
s_lshl_b32 s54, s54, 5
s_add_u32 s54, s54, s87
v_mfma_f32_16x16x32_bf16 v[148:151], a[24:27], a[12:15], v[148:151]
s_lshl_b32 s54, s54, 16
buffer_load_dwordx4 v7, s[4:7], s44 offen offset:1024 sc1 lds
v_mfma_f32_16x16x32_bf16 v[184:187], a[28:31], a[12:15], v[184:187]
s_lshl_b32 s56, s3, 14
s_add_u32 s54, s54, s56
s_add_u32 s54, s54, 54525952
v_mfma_f32_16x16x32_bf16 v[188:191], a[32:35], a[12:15], v[188:191]
s_add_u32 s12, s84, s54
s_addc_u32 s13, s85, 0
s_and_b32 s13, s13, 0xffff
v_mfma_f32_16x16x32_bf16 v[224:227], a[12:15], a[36:39], v[224:227]
s_mov_b32 s14, 16384
buffer_load_dwordx4 v7, s[4:7], s44 offen offset:2048 sc1 lds
v_mfma_f32_16x16x32_bf16 v[228:231], a[12:15], a[40:43], v[228:231]
s_mov_b32 s56, 131072
s_mov_b32 s57, -32768
s_mov_b32 s46, 98304
v_mfma_f32_16x16x32_bf16 v[152:155], a[20:23], a[16:19], v[152:155]
s_lshr_b32 s30, s51, 3
s_and_b32 s31, s51, 7
s_lshl_b32 s31, s31, 3
v_mfma_f32_16x16x32_bf16 v[156:159], a[24:27], a[16:19], v[156:159]
s_or_b32 s30, s30, s31
buffer_load_dwordx4 v7, s[4:7], s44 offen offset:3072 sc1 lds
v_mfma_f32_16x16x32_bf16 v[192:195], a[28:31], a[16:19], v[192:195]
s_add_u32 s44, s44, 4096
s_mov_b32 s31, s38
s_sub_u32 s50, s43, 14
v_mfma_f32_16x16x32_bf16 v[196:199], a[32:35], a[16:19], v[196:199]
s_sub_u32 s54, 48, s43
v_mfma_f32_16x16x32_bf16 v[232:235], a[16:19], a[36:39], v[232:235]
s_add_u32 m0, s91, 81920
v_mfma_f32_16x16x32_bf16 v[236:239], a[16:19], a[40:43], v[236:239]
s_waitcnt lgkmcnt(0)
buffer_load_dwordx4 v240, s[8:11], s45 offen offset:0 lds
v_mfma_f32_16x16x32_bf16 v[160:163], a[172:175], a[128:131], v[160:163]
ds_read_b64_tr_b16 a[20:21], v47 offset:14336
ds_read_b64_tr_b16 a[22:23], v47 offset:14848
v_mfma_f32_16x16x32_bf16 v[164:167], a[192:195], a[128:131], v[164:167]
s_lshl_b32 s34, s36, 18
ds_read_b128 a[0:3], v46
v_mfma_f32_16x16x32_bf16 v[200:203], a[128:131], a[196:199], v[200:203]
buffer_load_dwordx4 v241, s[8:11], s45 offen offset:1024 lds
ds_read_b64_tr_b16 a[24:25], v48 offset:14336
ds_read_b64_tr_b16 a[26:27], v48 offset:14848
v_mfma_f32_16x16x32_bf16 v[204:207], a[128:131], a[200:203], v[204:207]
s_lshr_b32 s35, s3, 1
ds_read_b128 a[4:7], v44 offset:0
v_mfma_f32_16x16x32_bf16 v[128:131], a[164:167], a[132:135], v[128:131]
ds_read_b64_tr_b16 a[28:29], v47 offset:16384
ds_read_b64_tr_b16 a[30:31], v47 offset:16896
v_mfma_f32_16x16x32_bf16 v[132:135], a[168:171], a[132:135], v[132:135]
s_lshl_b32 s35, s35, 17
buffer_load_dwordx4 v242, s[8:11], s45 offen offset:2048 lds
ds_read_b128 a[8:11], v44 offset:2048
v_mfma_f32_16x16x32_bf16 v[168:171], a[172:175], a[132:135], v[168:171]
ds_read_b64_tr_b16 a[32:33], v48 offset:16384
ds_read_b64_tr_b16 a[34:35], v48 offset:16896
v_mfma_f32_16x16x32_bf16 v[172:175], a[192:195], a[132:135], v[172:175]
s_add_u32 s34, s34, s35
ds_read_b128 a[12:15], v44 offset:4096
v_mfma_f32_16x16x32_bf16 v[208:211], a[132:135], a[196:199], v[208:211]
buffer_load_dwordx4 v243, s[8:11], s45 offen offset:3072 lds
ds_read_b64_tr_b16 a[36:37], v49 offset:18432
ds_read_b64_tr_b16 a[38:39], v49 offset:18944
v_mfma_f32_16x16x32_bf16 v[212:215], a[132:135], a[200:203], v[212:215]
s_lshl_b32 s35, s37, 12
s_add_u32 s45, s45, 131072
ds_read_b128 a[16:19], v44 offset:6144
v_mfma_f32_16x16x32_bf16 v[136:139], a[164:167], a[136:139], v[136:139]
s_add_u32 m0, s91, 65536
ds_read_b64_tr_b16 a[40:41], v50 offset:18432
ds_read_b64_tr_b16 a[42:43], v50 offset:18944
v_mfma_f32_16x16x32_bf16 v[140:143], a[168:171], a[136:139], v[140:143]
s_cmp_eq_u32 s39, 1
s_mov_b32 s28, 16384
s_cselect_b32 s28, 98304, s28
buffer_load_dwordx4 v7, s[4:7], s44 offen offset:0 sc1 lds
v_mfma_f32_16x16x32_bf16 v[176:179], a[172:175], a[136:139], v[176:179]
v_add_u32 v43, 16384, v43
v_add_u32 v44, 16384, v44
v_mfma_f32_16x16x32_bf16 v[180:183], a[192:195], a[136:139], v[180:183]
v_add_u32 v47, 98304, v47
v_add_u32 v48, 98304, v48
v_mfma_f32_16x16x32_bf16 v[216:219], a[136:139], a[196:199], v[216:219]
buffer_load_dwordx4 v7, s[4:7], s44 offen offset:1024 sc1 lds
v_add_u32 v49, 98304, v49
v_add_u32 v50, 98304, v50
v_mfma_f32_16x16x32_bf16 v[220:223], a[136:139], a[200:203], v[220:223]
v_add_u32 v45, s28, v45
v_add_u32 v46, s28, v46
v_mfma_f32_16x16x32_bf16 v[144:147], a[164:167], a[140:143], v[144:147]
s_add_u32 s34, s34, s35
s_and_b32 s35, s3, 1
s_lshl_b32 s35, s35, 11
v_mfma_f32_16x16x32_bf16 v[148:151], a[168:171], a[140:143], v[148:151]
s_add_u32 s34, s34, s35
s_add_u32 s34, s34, 50331648
buffer_load_dwordx4 v7, s[4:7], s44 offen offset:2048 sc1 lds
v_mfma_f32_16x16x32_bf16 v[184:187], a[172:175], a[140:143], v[184:187]
s_add_u32 s20, s84, s34
s_addc_u32 s21, s85, 0
s_and_b32 s21, s21, 0xffff
v_mfma_f32_16x16x32_bf16 v[188:191], a[192:195], a[140:143], v[188:191]
s_mov_b32 s22, 131072
s_mov_b32 s40, 0
s_mov_b32 s41, 65536
v_mfma_f32_16x16x32_bf16 v[224:227], a[140:143], a[196:199], v[224:227]
buffer_load_dwordx4 v7, s[4:7], s44 offen offset:3072 sc1 lds
v_mfma_f32_16x16x32_bf16 v[228:231], a[140:143], a[200:203], v[228:231]
s_add_u32 s44, s44, 4096
v_mfma_f32_16x16x32_bf16 v[152:155], a[164:167], a[160:163], v[152:155]
s_cmp_eq_u32 s55, 0
s_cselect_b64 s[58:59], -1, 0
s_cselect_b32 s50, s50, s54
v_mfma_f32_16x16x32_bf16 v[156:159], a[168:171], a[160:163], v[156:159]
v_and_b32 v252, 7, v1
v_bfe_u32 v253, v1, 3, 1
v_mfma_f32_16x16x32_bf16 v[192:195], a[172:175], a[160:163], v[192:195]
v_bfe_u32 v254, v1, 4, 1
v_lshrrev_b32 v255, 5, v1
v_mfma_f32_16x16x32_bf16 v[196:199], a[192:195], a[160:163], v[196:199]
v_lshlrev_b32 v252, 12, v252
v_lshl_add_u32 v252, v253, 6, v252
v_mfma_f32_16x16x32_bf16 v[232:235], a[160:163], a[196:199], v[232:235]
v_lshl_add_u32 v252, v254, 5, v252
v_lshl_add_u32 v251, v255, 4, v252
v_mfma_f32_16x16x32_bf16 v[236:239], a[160:163], a[200:203], v[236:239]
s_waitcnt vmcnt(16) lgkmcnt(0)
s_barrier
v_bfe_u32 v252, v1, 2, 2
v_mfma_f32_16x16x32_bf16 v[160:163], a[28:31], a[0:3], v[160:163]
ds_read_b64_tr_b16 a[164:165], v47 offset:0
ds_read_b64_tr_b16 a[166:167], v47 offset:512
v_mfma_f32_16x16x32_bf16 v[164:167], a[32:35], a[0:3], v[164:167]
s_add_u32 m0, s91, 49152
ds_read_b128 a[128:131], v45
v_mfma_f32_16x16x32_bf16 v[200:203], a[0:3], a[36:39], v[200:203]
buffer_load_dwordx4 v240, s[8:11], s45 offen offset:0 lds
ds_read_b64_tr_b16 a[168:169], v48 offset:0
ds_read_b64_tr_b16 a[170:171], v48 offset:512
v_mfma_f32_16x16x32_bf16 v[204:207], a[0:3], a[40:43], v[204:207]
ds_read_b128 a[132:135], v43 offset:0
v_and_b32 v253, 3, v1
v_mfma_f32_16x16x32_bf16 v[128:131], a[20:23], a[4:7], v[128:131]
ds_read_b64_tr_b16 a[172:173], v47 offset:2048
ds_read_b64_tr_b16 a[174:175], v47 offset:2560
v_mfma_f32_16x16x32_bf16 v[132:135], a[24:27], a[4:7], v[132:135]
ds_read_b128 a[136:139], v43 offset:2048
v_lshlrev_b32 v252, 1, v252
v_mfma_f32_16x16x32_bf16 v[168:171], a[28:31], a[4:7], v[168:171]
ds_read_b64_tr_b16 a[192:193], v48 offset:2048
ds_read_b64_tr_b16 a[194:195], v48 offset:2560
v_mfma_f32_16x16x32_bf16 v[172:175], a[32:35], a[4:7], v[172:175]
buffer_load_dwordx4 v241, s[8:11], s45 offen offset:1024 lds
ds_read_b128 a[140:143], v43 offset:4096
v_mfma_f32_16x16x32_bf16 v[208:211], a[4:7], a[36:39], v[208:211]
ds_read_b64_tr_b16 a[196:197], v49 offset:4096
ds_read_b64_tr_b16 a[198:199], v49 offset:4608
v_mfma_f32_16x16x32_bf16 v[212:215], a[4:7], a[40:43], v[212:215]
ds_read_b128 a[160:163], v43 offset:6144
v_lshl_or_b32 v252, v253, 6, v252
v_mfma_f32_16x16x32_bf16 v[136:139], a[20:23], a[8:11], v[136:139]
ds_read_b64_tr_b16 a[200:201], v50 offset:4096
ds_read_b64_tr_b16 a[202:203], v50 offset:4608
v_mfma_f32_16x16x32_bf16 v[140:143], a[24:27], a[8:11], v[140:143]
v_or_b32 v252, s30, v252
v_mov_b32 v253, s31
v_mfma_f32_16x16x32_bf16 v[176:179], a[28:31], a[8:11], v[176:179]
buffer_load_dwordx4 v242, s[8:11], s45 offen offset:2048 lds
v_cndmask_b32 v252, v252, v253, s[58:59]
v_mfma_f32_16x16x32_bf16 v[180:183], a[32:35], a[8:11], v[180:183]
v_lshlrev_b32 v249, 5, v252
v_mfma_f32_16x16x32_bf16 v[216:219], a[8:11], a[36:39], v[216:219]
v_mfma_f32_16x16x32_bf16 v[220:223], a[8:11], a[40:43], v[220:223]
v_mfma_f32_16x16x32_bf16 v[144:147], a[20:23], a[12:15], v[144:147]
v_mfma_f32_16x16x32_bf16 v[148:151], a[24:27], a[12:15], v[148:151]
buffer_load_dwordx4 v243, s[8:11], s45 offen offset:3072 lds
v_mfma_f32_16x16x32_bf16 v[184:187], a[28:31], a[12:15], v[184:187]
s_add_u32 s45, s45, 131072
v_mfma_f32_16x16x32_bf16 v[188:191], a[32:35], a[12:15], v[188:191]
v_mfma_f32_16x16x32_bf16 v[224:227], a[12:15], a[36:39], v[224:227]
v_mfma_f32_16x16x32_bf16 v[228:231], a[12:15], a[40:43], v[228:231]
s_add_u32 m0, s91, 32768
v_mfma_f32_16x16x32_bf16 v[152:155], a[20:23], a[16:19], v[152:155]
buffer_load_dwordx4 v7, s[4:7], s44 offen offset:0 sc1 lds
v_mfma_f32_16x16x32_bf16 v[156:159], a[24:27], a[16:19], v[156:159]
v_mfma_f32_16x16x32_bf16 v[192:195], a[28:31], a[16:19], v[192:195]
v_mfma_f32_16x16x32_bf16 v[196:199], a[32:35], a[16:19], v[196:199]
v_mfma_f32_16x16x32_bf16 v[232:235], a[16:19], a[36:39], v[232:235]
v_mfma_f32_16x16x32_bf16 v[236:239], a[16:19], a[40:43], v[236:239]
s_waitcnt lgkmcnt(0)
buffer_load_dwordx4 v7, s[4:7], s44 offen offset:1024 sc1 lds
v_mfma_f32_16x16x32_bf16 v[160:163], a[172:175], a[128:131], v[160:163]
ds_read_b64_tr_b16 a[20:21], v47 offset:14336
ds_read_b64_tr_b16 a[22:23], v47 offset:14848
v_mfma_f32_16x16x32_bf16 v[164:167], a[192:195], a[128:131], v[164:167]
ds_read_b128 a[0:3], v46
v_mfma_f32_16x16x32_bf16 v[200:203], a[128:131], a[196:199], v[200:203]
ds_read_b64_tr_b16 a[24:25], v48 offset:14336
ds_read_b64_tr_b16 a[26:27], v48 offset:14848
v_mfma_f32_16x16x32_bf16 v[204:207], a[128:131], a[200:203], v[204:207]
ds_read_b128 a[4:7], v44 offset:0
v_mfma_f32_16x16x32_bf16 v[128:131], a[164:167], a[132:135], v[128:131]
buffer_load_dwordx4 v7, s[4:7], s44 offen offset:2048 sc1 lds
ds_read_b64_tr_b16 a[28:29], v47 offset:16384
ds_read_b64_tr_b16 a[30:31], v47 offset:16896
v_mfma_f32_16x16x32_bf16 v[132:135], a[168:171], a[132:135], v[132:135]
ds_read_b128 a[8:11], v44 offset:2048
v_mfma_f32_16x16x32_bf16 v[168:171], a[172:175], a[132:135], v[168:171]
ds_read_b64_tr_b16 a[32:33], v48 offset:16384
ds_read_b64_tr_b16 a[34:35], v48 offset:16896
v_mfma_f32_16x16x32_bf16 v[172:175], a[192:195], a[132:135], v[172:175]
ds_read_b128 a[12:15], v44 offset:4096
v_mfma_f32_16x16x32_bf16 v[208:211], a[132:135], a[196:199], v[208:211]
ds_read_b64_tr_b16 a[36:37], v49 offset:18432
ds_read_b64_tr_b16 a[38:39], v49 offset:18944
v_mfma_f32_16x16x32_bf16 v[212:215], a[132:135], a[200:203], v[212:215]
buffer_load_dwordx4 v7, s[4:7], s44 offen offset:3072 sc1 lds
ds_read_b128 a[16:19], v44 offset:6144
v_mfma_f32_16x16x32_bf16 v[136:139], a[164:167], a[136:139], v[136:139]
s_add_u32 s44, s44, 4096
ds_read_b64_tr_b16 a[40:41], v50 offset:18432
ds_read_b64_tr_b16 a[42:43], v50 offset:18944
v_mfma_f32_16x16x32_bf16 v[140:143], a[168:171], a[136:139], v[140:143]
v_mfma_f32_16x16x32_bf16 v[176:179], a[172:175], a[136:139], v[176:179]
v_mfma_f32_16x16x32_bf16 v[180:183], a[192:195], a[136:139], v[180:183]
v_mfma_f32_16x16x32_bf16 v[216:219], a[136:139], a[196:199], v[216:219]
v_mfma_f32_16x16x32_bf16 v[220:223], a[136:139], a[200:203], v[220:223]
v_mfma_f32_16x16x32_bf16 v[144:147], a[164:167], a[140:143], v[144:147]
v_mfma_f32_16x16x32_bf16 v[148:151], a[168:171], a[140:143], v[148:151]
v_mfma_f32_16x16x32_bf16 v[184:187], a[172:175], a[140:143], v[184:187]
v_mfma_f32_16x16x32_bf16 v[188:191], a[192:195], a[140:143], v[188:191]
v_mfma_f32_16x16x32_bf16 v[224:227], a[140:143], a[196:199], v[224:227]
v_mfma_f32_16x16x32_bf16 v[228:231], a[140:143], a[200:203], v[228:231]
v_mfma_f32_16x16x32_bf16 v[152:155], a[164:167], a[160:163], v[152:155]
v_mfma_f32_16x16x32_bf16 v[156:159], a[168:171], a[160:163], v[156:159]
v_mfma_f32_16x16x32_bf16 v[192:195], a[172:175], a[160:163], v[192:195]
v_mfma_f32_16x16x32_bf16 v[196:199], a[192:195], a[160:163], v[196:199]
v_mfma_f32_16x16x32_bf16 v[232:235], a[160:163], a[196:199], v[232:235]
v_mfma_f32_16x16x32_bf16 v[236:239], a[160:163], a[200:203], v[236:239]
s_waitcnt vmcnt(16)
s_waitcnt lgkmcnt(0)
s_barrier
v_mfma_f32_16x16x32_bf16 v[160:163], a[28:31], a[0:3], v[160:163]
v_mfma_f32_16x16x32_bf16 v[164:167], a[32:35], a[0:3], v[164:167]
s_add_u32 m0, s91, 16384
ds_read_b64_tr_b16 a[80:81], v245 offset:0
ds_read_b64_tr_b16 a[82:83], v245 offset:2048
v_mfma_f32_16x16x32_bf16 v[200:203], a[0:3], a[36:39], v[200:203]
buffer_load_dwordx4 v240, s[8:11], s45 offen offset:0 lds
v_mfma_f32_16x16x32_bf16 v[204:207], a[0:3], a[40:43], v[204:207]
v_mfma_f32_16x16x32_bf16 v[128:131], a[20:23], a[4:7], v[128:131]
ds_read_b128 a[64:67], v244 offset:0
ds_read_b128 a[68:71], v244 offset:256
v_mfma_f32_16x16x32_bf16 v[132:135], a[24:27], a[4:7], v[132:135]
buffer_load_dwordx4 v241, s[8:11], s45 offen offset:1024 lds
v_mfma_f32_16x16x32_bf16 v[168:171], a[28:31], a[4:7], v[168:171]
v_mfma_f32_16x16x32_bf16 v[172:175], a[32:35], a[4:7], v[172:175]
ds_read_b64_tr_b16 a[84:85], v246 offset:0
ds_read_b64_tr_b16 a[86:87], v246 offset:2048
v_mfma_f32_16x16x32_bf16 v[208:211], a[4:7], a[36:39], v[208:211]
buffer_load_dwordx4 v242, s[8:11], s45 offen offset:2048 lds
v_mfma_f32_16x16x32_bf16 v[212:215], a[4:7], a[40:43], v[212:215]
v_mfma_f32_16x16x32_bf16 v[136:139], a[20:23], a[8:11], v[136:139]
ds_read_b64_tr_b16 a[88:89], v247 offset:0
ds_read_b64_tr_b16 a[90:91], v247 offset:2048
v_mfma_f32_16x16x32_bf16 v[140:143], a[24:27], a[8:11], v[140:143]
buffer_load_dwordx4 v243, s[8:11], s45 offen offset:3072 lds
v_mfma_f32_16x16x32_bf16 v[176:179], a[28:31], a[8:11], v[176:179]
s_add_u32 s45, s45, 131072
v_mfma_f32_16x16x32_bf16 v[180:183], a[32:35], a[8:11], v[180:183]
s_add_u32 m0, s91, 0
ds_read_b128 a[72:75], v244 offset:4096
ds_read_b128 a[76:79], v244 offset:4352
v_mfma_f32_16x16x32_bf16 v[216:219], a[8:11], a[36:39], v[216:219]
buffer_load_dwordx4 v7, s[4:7], s44 offen offset:0 sc1 lds
v_mfma_f32_16x16x32_bf16 v[220:223], a[8:11], a[40:43], v[220:223]
v_mfma_f32_16x16x32_bf16 v[144:147], a[20:23], a[12:15], v[144:147]
ds_read_b64_tr_b16 a[92:93], v248 offset:0
ds_read_b64_tr_b16 a[94:95], v248 offset:2048
v_mfma_f32_16x16x32_bf16 v[148:151], a[24:27], a[12:15], v[148:151]
buffer_load_dwordx4 v7, s[4:7], s44 offen offset:1024 sc1 lds
v_mfma_f32_16x16x32_bf16 v[184:187], a[28:31], a[12:15], v[184:187]
v_mfma_f32_16x16x32_bf16 v[188:191], a[32:35], a[12:15], v[188:191]
v_mfma_f32_16x16x32_bf16 v[224:227], a[12:15], a[36:39], v[224:227]
buffer_load_dwordx4 v7, s[4:7], s44 offen offset:2048 sc1 lds
v_mfma_f32_16x16x32_bf16 v[228:231], a[12:15], a[40:43], v[228:231]
v_mfma_f32_16x16x32_bf16 v[152:155], a[20:23], a[16:19], v[152:155]
v_mfma_f32_16x16x32_bf16 v[156:159], a[24:27], a[16:19], v[156:159]
buffer_load_dwordx4 v7, s[4:7], s44 offen offset:3072 sc1 lds
v_mfma_f32_16x16x32_bf16 v[192:195], a[28:31], a[16:19], v[192:195]
s_add_u32 s44, s44, 4096
v_mfma_f32_16x16x32_bf16 v[196:199], a[32:35], a[16:19], v[196:199]
v_mfma_f32_16x16x32_bf16 v[232:235], a[16:19], a[36:39], v[232:235]
v_mfma_f32_16x16x32_bf16 v[236:239], a[16:19], a[40:43], v[236:239]
s_mov_b32 s39, 1048744942
s_waitcnt lgkmcnt(0)
v_mfma_f32_16x16x32_bf16 v[64:67], a[80:83], a[64:67], 0
s_add_u32 m0, s91, 147456
ds_read_b64_tr_b16 a[112:113], v245 offset:8192
ds_read_b64_tr_b16 a[114:115], v245 offset:10240
v_mfma_f32_16x16x32_bf16 v[68:71], a[84:87], a[64:67], 0
buffer_load_dwordx4 v240, s[8:11], s45 offen offset:0 lds
v_mfma_f32_16x16x32_bf16 v[72:75], a[88:91], a[64:67], 0
ds_read_b128 a[96:99], v244 offset:2048
ds_read_b128 a[100:103], v244 offset:2304
v_mfma_f32_16x16x32_bf16 v[76:79], a[92:95], a[64:67], 0
buffer_load_dwordx4 v241, s[8:11], s45 offen offset:1024 lds
ds_read_b64_tr_b16 a[116:117], v246 offset:8192
ds_read_b64_tr_b16 a[118:119], v246 offset:10240
v_mfma_f32_16x16x32_bf16 v[80:83], a[80:83], a[68:71], 0
v_cvt_pk_bf16_f32 v160, v160, v161
v_cvt_pk_bf16_f32 v161, v162, v163
v_cvt_pk_bf16_f32 v162, v164, v165
v_cvt_pk_bf16_f32 v163, v166, v167
v_mfma_f32_16x16x32_bf16 v[84:87], a[84:87], a[68:71], 0
buffer_load_dwordx4 v242, s[8:11], s45 offen offset:2048 lds
ds_read_b64_tr_b16 a[120:121], v247 offset:8192
ds_read_b64_tr_b16 a[122:123], v247 offset:10240
v_mfma_f32_16x16x32_bf16 v[88:91], a[88:91], a[68:71], 0
ds_read_b128 a[104:107], v244 offset:6144
ds_read_b128 a[108:111], v244 offset:6400
v_cvt_pk_bf16_f32 v36, v200, v201
v_cvt_pk_bf16_f32 v37, v202, v203
v_mfma_f32_16x16x32_bf16 v[92:95], a[92:95], a[68:71], 0
buffer_load_dwordx4 v243, s[8:11], s45 offen offset:3072 lds
v_cvt_pk_bf16_f32 v46, v204, v205
v_cvt_pk_bf16_f32 v47, v206, v207
v_cvt_pk_bf16_f32 v168, v168, v169
v_mfma_f32_16x16x32_bf16 v[96:99], a[80:83], a[72:75], 0
s_cmp_eq_u32 s46, 0
s_cselect_b32 s53, s56, s57
s_add_u32 s45, s45, 131072
s_add_u32 m0, s91, 131072
ds_read_b64_tr_b16 a[124:125], v248 offset:8192
ds_read_b64_tr_b16 a[126:127], v248 offset:10240
v_mfma_f32_16x16x32_bf16 v[100:103], a[84:87], a[72:75], 0
buffer_load_dwordx4 v7, s[4:7], s44 offen offset:0 sc1 lds
v_cvt_pk_bf16_f32 v169, v170, v171
v_cvt_pk_bf16_f32 v170, v172, v173
v_cvt_pk_bf16_f32 v171, v174, v175
v_mfma_f32_16x16x32_bf16 v[104:107], a[88:91], a[72:75], 0
s_add_u32 s49, s46, s91
s_add_u32 s46, s46, s53
v_add_u32 v244, s53, v244
v_cvt_pk_bf16_f32 v128, v128, v129
v_cvt_pk_bf16_f32 v129, v130, v131
v_mfma_f32_16x16x32_bf16 v[108:111], a[92:95], a[72:75], 0
buffer_load_dwordx4 v7, s[4:7], s44 offen offset:1024 sc1 lds
v_cvt_pk_bf16_f32 v130, v132, v133
v_cvt_pk_bf16_f32 v131, v134, v135
v_cvt_pk_bf16_f32 v38, v208, v209
v_mfma_f32_16x16x32_bf16 v[112:115], a[80:83], a[76:79], 0
v_add_u32 v245, s53, v245
v_add_u32 v246, s53, v246
v_cvt_pk_bf16_f32 v39, v210, v211
v_cvt_pk_bf16_f32 v48, v212, v213
v_mfma_f32_16x16x32_bf16 v[116:119], a[84:87], a[76:79], 0
buffer_load_dwordx4 v7, s[4:7], s44 offen offset:2048 sc1 lds
v_cvt_pk_bf16_f32 v49, v214, v215
v_cvt_pk_bf16_f32 v176, v176, v177
v_cvt_pk_bf16_f32 v177, v178, v179
v_mfma_f32_16x16x32_bf16 v[120:123], a[88:91], a[76:79], 0
buffer_load_dwordx4 v7, s[4:7], s44 offen offset:3072 sc1 lds
v_add_u32 v247, s53, v247
v_add_u32 v248, s53, v248
v_cvt_pk_bf16_f32 v178, v180, v181
v_mfma_f32_16x16x32_bf16 v[124:127], a[92:95], a[76:79], 0
s_waitcnt vmcnt(24) lgkmcnt(0)
s_barrier
v_cvt_pk_bf16_f32 v179, v182, v183
v_cvt_pk_bf16_f32 v136, v136, v137
v_cvt_pk_bf16_f32 v137, v138, v139
v_mfma_f32_16x16x32_bf16 v[64:67], a[112:115], a[96:99], v[64:67]
ds_read_b64_tr_b16 a[80:81], v245 offset:0
ds_read_b64_tr_b16 a[82:83], v245 offset:2048
v_cvt_pk_bf16_f32 v138, v140, v141
v_cvt_pk_bf16_f32 v139, v142, v143
v_mfma_f32_16x16x32_bf16 v[68:71], a[116:119], a[96:99], v[68:71]
s_add_u32 m0, s49, 16384
ds_read_b128 a[64:67], v244 offset:0
v_cvt_pk_bf16_f32 v40, v216, v217
v_cvt_pk_bf16_f32 v41, v218, v219
v_mfma_f32_16x16x32_bf16 v[72:75], a[120:123], a[96:99], v[72:75]
buffer_load_dwordx4 v240, s[8:11], s45 offen offset:0 lds
v_cvt_pk_bf16_f32 v50, v220, v221
v_cvt_pk_bf16_f32 v51, v222, v223
v_cvt_pk_bf16_f32 v184, v184, v185
v_mfma_f32_16x16x32_bf16 v[76:79], a[124:127], a[96:99], v[76:79]
ds_read_b64_tr_b16 a[84:85], v246 offset:0
ds_read_b64_tr_b16 a[86:87], v246 offset:2048
v_cvt_pk_bf16_f32 v185, v186, v187
v_cvt_pk_bf16_f32 v186, v188, v189
v_mfma_f32_16x16x32_bf16 v[80:83], a[112:115], a[100:103], v[80:83]
s_add_u32 s44, s44, 4096
ds_read_b128 a[68:71], v244 offset:256
v_cvt_pk_bf16_f32 v187, v190, v191
v_cvt_pk_bf16_f32 v144, v144, v145
v_mfma_f32_16x16x32_bf16 v[84:87], a[116:119], a[100:103], v[84:87]
ds_read_b64_tr_b16 a[88:89], v247 offset:0
ds_read_b64_tr_b16 a[90:91], v247 offset:2048
v_cvt_pk_bf16_f32 v145, v146, v147
v_cvt_pk_bf16_f32 v146, v148, v149
v_mfma_f32_16x16x32_bf16 v[88:91], a[120:123], a[100:103], v[88:91]
buffer_load_dwordx4 v241, s[8:11], s45 offen offset:1024 lds
v_cvt_pk_bf16_f32 v147, v150, v151
v_cvt_pk_bf16_f32 v42, v224, v225
v_cvt_pk_bf16_f32 v43, v226, v227
v_mfma_f32_16x16x32_bf16 v[92:95], a[124:127], a[100:103], v[92:95]
ds_read_b128 a[72:75], v244 offset:4096
v_cvt_pk_bf16_f32 v52, v228, v229
v_cvt_pk_bf16_f32 v53, v230, v231
v_cvt_pk_bf16_f32 v192, v192, v193
v_mfma_f32_16x16x32_bf16 v[96:99], a[112:115], a[104:107], v[96:99]
ds_read_b64_tr_b16 a[92:93], v248 offset:0
ds_read_b64_tr_b16 a[94:95], v248 offset:2048
v_cvt_pk_bf16_f32 v193, v194, v195
v_cvt_pk_bf16_f32 v194, v196, v197
v_mfma_f32_16x16x32_bf16 v[100:103], a[116:119], a[104:107], v[100:103]
ds_read_b128 a[76:79], v244 offset:4352
v_cvt_pk_bf16_f32 v195, v198, v199
v_cvt_pk_bf16_f32 v152, v152, v153
v_cvt_pk_bf16_f32 v153, v154, v155
v_mfma_f32_16x16x32_bf16 v[104:107], a[120:123], a[104:107], v[104:107]
buffer_load_dwordx4 v242, s[8:11], s45 offen offset:2048 lds
v_cvt_pk_bf16_f32 v154, v156, v157
v_cvt_pk_bf16_f32 v155, v158, v159
v_cvt_pk_bf16_f32 v44, v232, v233
v_mfma_f32_16x16x32_bf16 v[108:111], a[124:127], a[104:107], v[108:111]
s_cmp_eq_u32 s46, 0
s_cselect_b32 s53, s56, s57
v_cvt_pk_bf16_f32 v45, v234, v235
v_cvt_pk_bf16_f32 v54, v236, v237
v_cvt_pk_bf16_f32 v55, v238, v239
v_mfma_f32_16x16x32_bf16 v[200:203], v[160:163], v[128:131], v[24:27]
v_mfma_f32_16x16x32_bf16 v[112:115], a[112:115], a[108:111], v[112:115]
v_mfma_f32_16x16x32_bf16 v[204:207], v[168:171], v[128:131], v[20:23]
v_mfma_f32_16x16x32_bf16 v[116:119], a[116:119], a[108:111], v[116:119]
v_mfma_f32_16x16x32_bf16 v[208:211], v[168:171], v[136:139], v[16:19]
v_mfma_f32_16x16x32_bf16 v[120:123], a[120:123], a[108:111], v[120:123]
buffer_load_dwordx4 v243, s[8:11], s45 offen offset:3072 lds
v_mfma_f32_16x16x32_bf16 v[212:215], v[176:179], v[136:139], v[20:23]
v_mfma_f32_16x16x32_bf16 v[124:127], a[124:127], a[108:111], v[124:127]
s_waitcnt lgkmcnt(0)
s_mov_b32 m0, s49
s_add_u32 s45, s45, 131072
v_mfma_f32_16x16x32_bf16 v[216:219], v[176:179], v[144:147], v[16:19]
v_mfma_f32_16x16x32_bf16 v[64:67], a[80:83], a[64:67], v[64:67]
ds_read_b64_tr_b16 a[112:113], v245 offset:8192
ds_read_b64_tr_b16 a[114:115], v245 offset:10240
v_mfma_f32_16x16x32_bf16 v[220:223], v[184:187], v[144:147], v[20:23]
v_mfma_f32_16x16x32_bf16 v[68:71], a[84:87], a[64:67], v[68:71]
ds_read_b128 a[96:99], v244 offset:2048
v_add_u32 v245, s53, v245
v_mfma_f32_16x16x32_bf16 v[224:227], v[184:187], v[152:155], v[16:19]
v_mfma_f32_16x16x32_bf16 v[72:75], a[88:91], a[64:67], v[72:75]
buffer_load_dwordx4 v7, s[4:7], s44 offen offset:0 sc1 lds
v_mfma_f32_16x16x32_bf16 v[228:231], v[192:195], v[152:155], v[20:23]
v_mfma_f32_16x16x32_bf16 v[76:79], a[92:95], a[64:67], v[76:79]
ds_read_b64_tr_b16 a[116:117], v246 offset:8192
ds_read_b64_tr_b16 a[118:119], v246 offset:10240
v_max3_f32 v56, v200, v201, v202
v_max3_f32 v58, v208, v209, v210
v_mfma_f32_16x16x32_bf16 v[80:83], a[80:83], a[68:71], v[80:83]
ds_read_b128 a[100:103], v244 offset:2304
v_add_u32 v246, s53, v246
v_max3_f32 v60, v216, v217, v218
v_max3_f32 v62, v224, v225, v226
v_mfma_f32_16x16x32_bf16 v[84:87], a[84:87], a[68:71], v[84:87]
ds_read_b64_tr_b16 a[120:121], v247 offset:8192
ds_read_b64_tr_b16 a[122:123], v247 offset:10240
v_max3_f32 v57, v203, v204, v205
v_max3_f32 v59, v211, v212, v213
v_mfma_f32_16x16x32_bf16 v[88:91], a[88:91], a[68:71], v[88:91]
buffer_load_dwordx4 v7, s[4:7], s44 offen offset:1024 sc1 lds
v_max3_f32 v61, v219, v220, v221
v_max3_f32 v63, v227, v228, v229
.Lpad18:
s_cbranch_execz .Ltch8
v_max3_f32 v56, v56, v206, v207
v_mfma_f32_16x16x32_bf16 v[92:95], a[92:95], a[68:71], v[92:95]
ds_read_b128 a[104:107], v244 offset:6144
v_add_u32 v247, s53, v247
v_max3_f32 v58, v58, v214, v215
v_max3_f32 v60, v60, v222, v223
v_mfma_f32_16x16x32_bf16 v[96:99], a[80:83], a[72:75], v[96:99]
ds_read_b64_tr_b16 a[124:125], v248 offset:8192
ds_read_b64_tr_b16 a[126:127], v248 offset:10240
v_max3_f32 v62, v62, v230, v231
v_max_f32 v56, v56, v57
v_mfma_f32_16x16x32_bf16 v[100:103], a[84:87], a[72:75], v[100:103]
ds_read_b128 a[108:111], v244 offset:6400
v_add_u32 v248, s53, v248
v_max_f32 v58, v58, v59
v_max_f32 v60, v60, v61
v_mfma_f32_16x16x32_bf16 v[104:107], a[88:91], a[72:75], v[104:107]
buffer_load_dwordx4 v7, s[4:7], s44 offen offset:2048 sc1 lds
v_max_f32 v62, v62, v63
v_mov_b32 v57, v56
v_mov_b32 v59, v58
v_mfma_f32_16x16x32_bf16 v[108:111], a[92:95], a[72:75], v[108:111]
s_add_u32 s49, s46, s91
v_add_u32 v244, s53, v244
v_mov_b32 v61, v60
v_mov_b32 v63, v62
v_mfma_f32_16x16x32_bf16 v[112:115], a[80:83], a[76:79], v[112:115]
s_add_u32 s46, s46, s53
v_permlane16_swap_b32 v56, v57
v_permlane16_swap_b32 v58, v59
v_mfma_f32_16x16x32_bf16 v[116:119], a[84:87], a[76:79], v[116:119]
v_permlane16_swap_b32 v60, v61
v_permlane16_swap_b32 v62, v63
v_mfma_f32_16x16x32_bf16 v[120:123], a[88:91], a[76:79], v[120:123]
buffer_load_dwordx4 v7, s[4:7], s44 offen offset:3072 sc1 lds
v_max_f32 v56, v56, v57
v_max_f32 v58, v58, v59
v_max_f32 v60, v60, v61
v_mfma_f32_16x16x32_bf16 v[124:127], a[92:95], a[76:79], v[124:127]
s_waitcnt vmcnt(24) lgkmcnt(0)
s_barrier
v_max_f32 v62, v62, v63
v_mov_b32 v57, v56
v_mov_b32 v59, v58
v_mfma_f32_16x16x32_bf16 v[64:67], a[112:115], a[96:99], v[64:67]
ds_read_b64_tr_b16 a[80:81], v245 offset:0
ds_read_b64_tr_b16 a[82:83], v245 offset:2048
v_mov_b32 v61, v60
v_mov_b32 v63, v62
v_mfma_f32_16x16x32_bf16 v[68:71], a[116:119], a[96:99], v[68:71]
s_add_u32 m0, s49, 16384
ds_read_b128 a[64:67], v244 offset:0
v_permlane32_swap_b32 v56, v57
v_mfma_f32_16x16x32_bf16 v[72:75], a[120:123], a[96:99], v[72:75]
buffer_load_dwordx4 v240, s[8:11], s45 offen offset:0 lds
v_permlane32_swap_b32 v58, v59
v_mfma_f32_16x16x32_bf16 v[76:79], a[124:127], a[96:99], v[76:79]
ds_read_b64_tr_b16 a[84:85], v246 offset:0
ds_read_b64_tr_b16 a[86:87], v246 offset:2048
v_permlane32_swap_b32 v60, v61
v_mfma_f32_16x16x32_bf16 v[80:83], a[112:115], a[100:103], v[80:83]
s_add_u32 s44, s44, 4096
ds_read_b128 a[68:71], v244 offset:256
v_permlane32_swap_b32 v62, v63
v_mfma_f32_16x16x32_bf16 v[84:87], a[116:119], a[100:103], v[84:87]
ds_read_b64_tr_b16 a[88:89], v247 offset:0
ds_read_b64_tr_b16 a[90:91], v247 offset:2048
v_max_f32 v56, v56, v57
v_max_f32 v58, v58, v59
v_mfma_f32_16x16x32_bf16 v[88:91], a[120:123], a[100:103], v[88:91]
buffer_load_dwordx4 v241, s[8:11], s45 offen offset:1024 lds
v_max_f32 v60, v60, v61
v_max_f32 v62, v62, v63
v_mul_f32 v56, s39, v56
v_mfma_f32_16x16x32_bf16 v[92:95], a[124:127], a[100:103], v[92:95]
ds_read_b128 a[72:75], v244 offset:4096
v_mul_f32 v58, s39, v58
v_mul_f32 v60, s39, v60
v_mul_f32 v62, s39, v62
v_mfma_f32_16x16x32_bf16 v[96:99], a[112:115], a[104:107], v[96:99]
ds_read_b64_tr_b16 a[92:93], v248 offset:0
ds_read_b64_tr_b16 a[94:95], v248 offset:2048
v_fma_f32 v200, v200, s39, -v56
v_fma_f32 v208, v208, s39, -v58
v_mfma_f32_16x16x32_bf16 v[100:103], a[116:119], a[104:107], v[100:103]
ds_read_b128 a[76:79], v244 offset:4352
v_fma_f32 v216, v216, s39, -v60
v_fma_f32 v224, v224, s39, -v62
v_fma_f32 v201, v201, s39, -v56
v_mfma_f32_16x16x32_bf16 v[104:107], a[120:123], a[104:107], v[104:107]
buffer_load_dwordx4 v242, s[8:11], s45 offen offset:2048 lds
v_fma_f32 v209, v209, s39, -v58
v_fma_f32 v217, v217, s39, -v60
v_fma_f32 v225, v225, s39, -v62
v_mfma_f32_16x16x32_bf16 v[108:111], a[124:127], a[104:107], v[108:111]
s_cmp_eq_u32 s46, 0
s_cselect_b32 s53, s56, s57
v_fma_f32 v202, v202, s39, -v56
v_fma_f32 v210, v210, s39, -v58
v_fma_f32 v218, v218, s39, -v60
v_mfma_f32_16x16x32_bf16 v[112:115], a[112:115], a[108:111], v[112:115]
v_fma_f32 v226, v226, s39, -v62
v_fma_f32 v203, v203, s39, -v56
v_fma_f32 v211, v211, s39, -v58
v_fma_f32 v219, v219, s39, -v60
v_mfma_f32_16x16x32_bf16 v[116:119], a[116:119], a[108:111], v[116:119]
v_fma_f32 v227, v227, s39, -v62
v_fma_f32 v204, v204, s39, -v56
v_fma_f32 v212, v212, s39, -v58
v_fma_f32 v220, v220, s39, -v60
v_mfma_f32_16x16x32_bf16 v[120:123], a[120:123], a[108:111], v[120:123]
buffer_load_dwordx4 v243, s[8:11], s45 offen offset:3072 lds
v_fma_f32 v228, v228, s39, -v62
v_fma_f32 v205, v205, s39, -v56
v_fma_f32 v213, v213, s39, -v58
v_mfma_f32_16x16x32_bf16 v[124:127], a[124:127], a[108:111], v[124:127]
s_waitcnt lgkmcnt(0)
s_mov_b32 m0, s49
s_add_u32 s45, s45, 131072
v_fma_f32 v221, v221, s39, -v60
v_fma_f32 v229, v229, s39, -v62
v_mfma_f32_16x16x32_bf16 v[64:67], a[80:83], a[64:67], v[64:67]
ds_read_b64_tr_b16 a[112:113], v245 offset:8192
ds_read_b64_tr_b16 a[114:115], v245 offset:10240
v_fma_f32 v206, v206, s39, -v56
v_fma_f32 v214, v214, s39, -v58
v_mfma_f32_16x16x32_bf16 v[68:71], a[84:87], a[64:67], v[68:71]
ds_read_b128 a[96:99], v244 offset:2048
v_add_u32 v245, s53, v245
v_fma_f32 v222, v222, s39, -v60
v_fma_f32 v230, v230, s39, -v62
v_mfma_f32_16x16x32_bf16 v[72:75], a[88:91], a[64:67], v[72:75]
buffer_load_dwordx4 v7, s[4:7], s44 offen offset:0 sc1 lds
v_fma_f32 v207, v207, s39, -v56
v_fma_f32 v215, v215, s39, -v58
v_fma_f32 v223, v223, s39, -v60
v_mfma_f32_16x16x32_bf16 v[76:79], a[92:95], a[64:67], v[76:79]
ds_read_b64_tr_b16 a[116:117], v246 offset:8192
ds_read_b64_tr_b16 a[118:119], v246 offset:10240
v_fma_f32 v231, v231, s39, -v62
v_mfma_f32_16x16x32_bf16 v[80:83], a[80:83], a[68:71], v[80:83]
ds_read_b128 a[100:103], v244 offset:2304
v_add_u32 v246, s53, v246
v_exp_f32 v200, v200
v_mfma_f32_16x16x32_bf16 v[84:87], a[84:87], a[68:71], v[84:87]
ds_read_b64_tr_b16 a[120:121], v247 offset:8192
ds_read_b64_tr_b16 a[122:123], v247 offset:10240
v_exp_f32 v208, v208
v_mfma_f32_16x16x32_bf16 v[88:91], a[88:91], a[68:71], v[88:91]
buffer_load_dwordx4 v7, s[4:7], s44 offen offset:1024 sc1 lds
v_exp_f32 v216, v216
v_mfma_f32_16x16x32_bf16 v[92:95], a[92:95], a[68:71], v[92:95]
ds_read_b128 a[104:107], v244 offset:6144
v_add_u32 v247, s53, v247
v_exp_f32 v224, v224
v_mfma_f32_16x16x32_bf16 v[96:99], a[80:83], a[72:75], v[96:99]
ds_read_b64_tr_b16 a[124:125], v248 offset:8192
ds_read_b64_tr_b16 a[126:127], v248 offset:10240
v_exp_f32 v201, v201
v_mfma_f32_16x16x32_bf16 v[100:103], a[84:87], a[72:75], v[100:103]
ds_read_b128 a[108:111], v244 offset:6400
v_add_u32 v248, s53, v248
v_exp_f32 v209, v209
v_mfma_f32_16x16x32_bf16 v[104:107], a[88:91], a[72:75], v[104:107]
buffer_load_dwordx4 v7, s[4:7], s44 offen offset:2048 sc1 lds
v_exp_f32 v217, v217
v_mfma_f32_16x16x32_bf16 v[108:111], a[92:95], a[72:75], v[108:111]
s_add_u32 s49, s46, s91
v_add_u32 v244, s53, v244
v_exp_f32 v225, v225
v_mfma_f32_16x16x32_bf16 v[112:115], a[80:83], a[76:79], v[112:115]
s_add_u32 s46, s46, s53
v_exp_f32 v202, v202
v_exp_f32 v210, v210
v_mfma_f32_16x16x32_bf16 v[116:119], a[84:87], a[76:79], v[116:119]
v_exp_f32 v218, v218
v_exp_f32 v226, v226
v_mfma_f32_16x16x32_bf16 v[120:123], a[88:91], a[76:79], v[120:123]
buffer_load_dwordx4 v7, s[4:7], s44 offen offset:3072 sc1 lds
v_exp_f32 v203, v203
v_mfma_f32_16x16x32_bf16 v[124:127], a[92:95], a[76:79], v[124:127]
s_waitcnt vmcnt(24) lgkmcnt(0)
s_barrier
v_exp_f32 v211, v211
v_mfma_f32_16x16x32_bf16 v[64:67], a[112:115], a[96:99], v[64:67]
ds_read_b64_tr_b16 a[80:81], v245 offset:0
ds_read_b64_tr_b16 a[82:83], v245 offset:2048
v_exp_f32 v219, v219
v_mfma_f32_16x16x32_bf16 v[68:71], a[116:119], a[96:99], v[68:71]
s_add_u32 m0, s49, 16384
ds_read_b128 a[64:67], v244 offset:0
v_exp_f32 v227, v227
v_mfma_f32_16x16x32_bf16 v[72:75], a[120:123], a[96:99], v[72:75]
buffer_load_dwordx4 v240, s[8:11], s45 offen offset:0 lds
v_exp_f32 v204, v204
v_mfma_f32_16x16x32_bf16 v[76:79], a[124:127], a[96:99], v[76:79]
ds_read_b64_tr_b16 a[84:85], v246 offset:0
ds_read_b64_tr_b16 a[86:87], v246 offset:2048
v_exp_f32 v212, v212
v_mfma_f32_16x16x32_bf16 v[80:83], a[112:115], a[100:103], v[80:83]
s_add_u32 s44, s44, 4096
ds_read_b128 a[68:71], v244 offset:256
v_exp_f32 v220, v220
v_mfma_f32_16x16x32_bf16 v[84:87], a[116:119], a[100:103], v[84:87]
ds_read_b64_tr_b16 a[88:89], v247 offset:0
ds_read_b64_tr_b16 a[90:91], v247 offset:2048
v_exp_f32 v228, v228
v_mfma_f32_16x16x32_bf16 v[88:91], a[120:123], a[100:103], v[88:91]
buffer_load_dwordx4 v241, s[8:11], s45 offen offset:1024 lds
v_exp_f32 v205, v205
v_mfma_f32_16x16x32_bf16 v[92:95], a[124:127], a[100:103], v[92:95]
ds_read_b128 a[72:75], v244 offset:4096
v_exp_f32 v213, v213
v_mfma_f32_16x16x32_bf16 v[96:99], a[112:115], a[104:107], v[96:99]
ds_read_b64_tr_b16 a[92:93], v248 offset:0
ds_read_b64_tr_b16 a[94:95], v248 offset:2048
v_exp_f32 v221, v221
v_mfma_f32_16x16x32_bf16 v[100:103], a[116:119], a[104:107], v[100:103]
ds_read_b128 a[76:79], v244 offset:4352
v_exp_f32 v229, v229
v_mfma_f32_16x16x32_bf16 v[104:107], a[120:123], a[104:107], v[104:107]
buffer_load_dwordx4 v242, s[8:11], s45 offen offset:2048 lds
v_exp_f32 v206, v206
v_mfma_f32_16x16x32_bf16 v[108:111], a[124:127], a[104:107], v[108:111]
s_cmp_eq_u32 s46, 0
s_cselect_b32 s53, s56, s57
v_exp_f32 v214, v214
v_mfma_f32_16x16x32_bf16 v[112:115], a[112:115], a[108:111], v[112:115]
v_exp_f32 v222, v222
v_exp_f32 v230, v230
v_mfma_f32_16x16x32_bf16 v[116:119], a[116:119], a[108:111], v[116:119]
v_exp_f32 v207, v207
v_exp_f32 v215, v215
v_mfma_f32_16x16x32_bf16 v[120:123], a[120:123], a[108:111], v[120:123]
buffer_load_dwordx4 v243, s[8:11], s45 offen offset:3072 lds
v_exp_f32 v223, v223
v_mfma_f32_16x16x32_bf16 v[124:127], a[124:127], a[108:111], v[124:127]
s_waitcnt lgkmcnt(0)
s_mov_b32 m0, s49
s_add_u32 s45, s45, 131072
v_exp_f32 v231, v231
v_mfma_f32_16x16x32_bf16 v[64:67], a[80:83], a[64:67], v[64:67]
ds_read_b64_tr_b16 a[112:113], v245 offset:8192
ds_read_b64_tr_b16 a[114:115], v245 offset:10240
v_cvt_pk_bf16_f32 v132, v200, v201
v_cvt_pk_bf16_f32 v140, v208, v209
v_mfma_f32_16x16x32_bf16 v[68:71], a[84:87], a[64:67], v[68:71]
ds_read_b128 a[96:99], v244 offset:2048
v_add_u32 v245, s53, v245
v_cvt_pk_bf16_f32 v148, v216, v217
v_cvt_pk_bf16_f32 v156, v224, v225
v_mfma_f32_16x16x32_bf16 v[72:75], a[88:91], a[64:67], v[72:75]
buffer_load_dwordx4 v7, s[4:7], s44 offen offset:0 sc1 lds
v_cvt_pk_bf16_f32 v133, v202, v203
v_cvt_pk_bf16_f32 v141, v210, v211
v_cvt_pk_bf16_f32 v149, v218, v219
v_mfma_f32_16x16x32_bf16 v[76:79], a[92:95], a[64:67], v[76:79]
ds_read_b64_tr_b16 a[116:117], v246 offset:8192
ds_read_b64_tr_b16 a[118:119], v246 offset:10240
v_cvt_pk_bf16_f32 v157, v226, v227
v_cvt_pk_bf16_f32 v134, v204, v205
v_mfma_f32_16x16x32_bf16 v[80:83], a[80:83], a[68:71], v[80:83]
ds_read_b128 a[100:103], v244 offset:2304
v_add_u32 v246, s53, v246
v_cvt_pk_bf16_f32 v142, v212, v213
v_cvt_pk_bf16_f32 v150, v220, v221
v_mfma_f32_16x16x32_bf16 v[84:87], a[84:87], a[68:71], v[84:87]
ds_read_b64_tr_b16 a[120:121], v247 offset:8192
ds_read_b64_tr_b16 a[122:123], v247 offset:10240
v_cvt_pk_bf16_f32 v158, v228, v229
v_cvt_pk_bf16_f32 v135, v206, v207
v_mfma_f32_16x16x32_bf16 v[88:91], a[88:91], a[68:71], v[88:91]
buffer_load_dwordx4 v7, s[4:7], s44 offen offset:1024 sc1 lds
v_cvt_pk_bf16_f32 v143, v214, v215
v_cvt_pk_bf16_f32 v151, v222, v223
v_cvt_pk_bf16_f32 v159, v230, v231
v_mfma_f32_16x16x32_bf16 v[92:95], a[92:95], a[68:71], v[92:95]
ds_read_b128 a[104:107], v244 offset:6144
v_add_u32 v247, s53, v247
v_add_f32 v56, v200, v201
v_add_f32 v58, v208, v209
v_mfma_f32_16x16x32_bf16 v[96:99], a[80:83], a[72:75], v[96:99]
ds_read_b64_tr_b16 a[124:125], v248 offset:8192
ds_read_b64_tr_b16 a[126:127], v248 offset:10240
v_add_f32 v60, v216, v217
v_add_f32 v62, v224, v225
v_mfma_f32_16x16x32_bf16 v[160:163], v[36:39], v[132:135], 0
v_mfma_f32_16x16x32_bf16 v[100:103], a[84:87], a[72:75], v[100:103]
ds_read_b128 a[108:111], v244 offset:6400
v_add_u32 v248, s53, v248
v_mfma_f32_16x16x32_bf16 v[164:167], v[46:49], v[132:135], 0
v_mfma_f32_16x16x32_bf16 v[104:107], a[88:91], a[72:75], v[104:107]
buffer_load_dwordx4 v7, s[4:7], s44 offen offset:2048 sc1 lds
v_mfma_f32_16x16x32_bf16 v[168:171], v[38:41], v[140:143], 0
v_mfma_f32_16x16x32_bf16 v[108:111], a[92:95], a[72:75], v[108:111]
s_add_u32 s49, s46, s91
v_add_u32 v244, s53, v244
v_mfma_f32_16x16x32_bf16 v[172:175], v[48:51], v[140:143], 0
v_mfma_f32_16x16x32_bf16 v[112:115], a[80:83], a[76:79], v[112:115]
s_add_u32 s46, s46, s53
v_mfma_f32_16x16x32_bf16 v[176:179], v[40:43], v[148:151], 0
v_mfma_f32_16x16x32_bf16 v[116:119], a[84:87], a[76:79], v[116:119]
v_mfma_f32_16x16x32_bf16 v[180:183], v[50:53], v[148:151], 0
v_mfma_f32_16x16x32_bf16 v[120:123], a[88:91], a[76:79], v[120:123]
buffer_load_dwordx4 v7, s[4:7], s44 offen offset:3072 sc1 lds
v_mfma_f32_16x16x32_bf16 v[184:187], v[42:45], v[156:159], 0
v_mfma_f32_16x16x32_bf16 v[124:127], a[92:95], a[76:79], v[124:127]
s_waitcnt vmcnt(24) lgkmcnt(0)
s_barrier
v_mfma_f32_16x16x32_bf16 v[188:191], v[52:55], v[156:159], 0
v_mfma_f32_16x16x32_bf16 v[64:67], a[112:115], a[96:99], v[64:67]
ds_read_b64_tr_b16 a[80:81], v245 offset:0
ds_read_b64_tr_b16 a[82:83], v245 offset:2048
v_add_f32 v57, v202, v203
v_add_f32 v59, v210, v211
v_mfma_f32_16x16x32_bf16 v[68:71], a[116:119], a[96:99], v[68:71]
s_add_u32 m0, s49, 16384
ds_read_b128 a[64:67], v244 offset:0
v_add_f32 v61, v218, v219
v_add_f32 v63, v226, v227
v_mfma_f32_16x16x32_bf16 v[72:75], a[120:123], a[96:99], v[72:75]
buffer_load_dwordx4 v240, s[8:11], s45 offen offset:0 lds
v_add_f32 v56, v56, v204
v_add_f32 v58, v58, v212
v_add_f32 v60, v60, v220
v_mfma_f32_16x16x32_bf16 v[76:79], a[124:127], a[96:99], v[76:79]
ds_read_b64_tr_b16 a[84:85], v246 offset:0
ds_read_b64_tr_b16 a[86:87], v246 offset:2048
v_add_f32 v62, v62, v228
v_add_f32 v57, v57, v205
v_mfma_f32_16x16x32_bf16 v[80:83], a[112:115], a[100:103], v[80:83]
s_add_u32 s44, s44, 4096
ds_read_b128 a[68:71], v244 offset:256
v_add_f32 v59, v59, v213
v_add_f32 v61, v61, v221
v_mfma_f32_16x16x32_bf16 v[84:87], a[116:119], a[100:103], v[84:87]
ds_read_b64_tr_b16 a[88:89], v247 offset:0
ds_read_b64_tr_b16 a[90:91], v247 offset:2048
v_add_f32 v63, v63, v229
v_add_f32 v56, v56, v206
v_mfma_f32_16x16x32_bf16 v[88:91], a[120:123], a[100:103], v[88:91]
buffer_load_dwordx4 v241, s[8:11], s45 offen offset:1024 lds
v_add_f32 v58, v58, v214
v_add_f32 v60, v60, v222
v_add_f32 v62, v62, v230
v_mfma_f32_16x16x32_bf16 v[92:95], a[124:127], a[100:103], v[92:95]
ds_read_b128 a[72:75], v244 offset:4096
v_add_f32 v57, v57, v207
v_add_f32 v59, v59, v215
v_add_f32 v61, v61, v223
v_mfma_f32_16x16x32_bf16 v[96:99], a[112:115], a[104:107], v[96:99]
ds_read_b64_tr_b16 a[92:93], v248 offset:0
ds_read_b64_tr_b16 a[94:95], v248 offset:2048
v_add_f32 v63, v63, v231
v_add_f32 v56, v56, v57
v_mfma_f32_16x16x32_bf16 v[100:103], a[116:119], a[104:107], v[100:103]
ds_read_b128 a[76:79], v244 offset:4352
v_add_f32 v58, v58, v59
v_add_f32 v60, v60, v61
v_add_f32 v62, v62, v63
v_mfma_f32_16x16x32_bf16 v[104:107], a[120:123], a[104:107], v[104:107]
buffer_load_dwordx4 v242, s[8:11], s45 offen offset:2048 lds
v_mov_b32 v57, v56
v_mov_b32 v59, v58
v_mov_b32 v61, v60
v_mfma_f32_16x16x32_bf16 v[108:111], a[124:127], a[104:107], v[108:111]
s_cmp_eq_u32 s46, 0
s_cselect_b32 s53, s56, s57
v_mov_b32 v63, v62
v_permlane16_swap_b32 v56, v57
v_mfma_f32_16x16x32_bf16 v[112:115], a[112:115], a[108:111], v[112:115]
v_permlane16_swap_b32 v58, v59
v_permlane16_swap_b32 v60, v61
v_mfma_f32_16x16x32_bf16 v[116:119], a[116:119], a[108:111], v[116:119]
v_permlane16_swap_b32 v62, v63
v_add_f32 v56, v56, v57
v_add_f32 v58, v58, v59
v_mfma_f32_16x16x32_bf16 v[120:123], a[120:123], a[108:111], v[120:123]
buffer_load_dwordx4 v243, s[8:11], s45 offen offset:3072 lds
v_add_f32 v60, v60, v61
v_add_f32 v62, v62, v63
v_mov_b32 v57, v56
v_mfma_f32_16x16x32_bf16 v[124:127], a[124:127], a[108:111], v[124:127]
s_waitcnt lgkmcnt(0)
s_mov_b32 m0, s49
s_add_u32 s45, s45, 131072
v_mov_b32 v59, v58
v_mov_b32 v61, v60
v_mfma_f32_16x16x32_bf16 v[64:67], a[80:83], a[64:67], v[64:67]
ds_read_b64_tr_b16 a[112:113], v245 offset:8192
ds_read_b64_tr_b16 a[114:115], v245 offset:10240
v_mov_b32 v63, v62
v_mfma_f32_16x16x32_bf16 v[68:71], a[84:87], a[64:67], v[68:71]
ds_read_b128 a[96:99], v244 offset:2048
v_add_u32 v245, s53, v245
v_permlane32_swap_b32 v56, v57
v_mfma_f32_16x16x32_bf16 v[72:75], a[88:91], a[64:67], v[72:75]
buffer_load_dwordx4 v7, s[4:7], s44 offen offset:0 sc1 lds
v_permlane32_swap_b32 v58, v59
v_mfma_f32_16x16x32_bf16 v[76:79], a[92:95], a[64:67], v[76:79]
ds_read_b64_tr_b16 a[116:117], v246 offset:8192
ds_read_b64_tr_b16 a[118:119], v246 offset:10240
v_permlane32_swap_b32 v60, v61
v_mfma_f32_16x16x32_bf16 v[80:83], a[80:83], a[68:71], v[80:83]
ds_read_b128 a[100:103], v244 offset:2304
v_add_u32 v246, s53, v246
v_permlane32_swap_b32 v62, v63
v_mfma_f32_16x16x32_bf16 v[84:87], a[84:87], a[68:71], v[84:87]
ds_read_b64_tr_b16 a[120:121], v247 offset:8192
ds_read_b64_tr_b16 a[122:123], v247 offset:10240
v_add_f32 v56, v56, v57
v_add_f32 v58, v58, v59
v_mfma_f32_16x16x32_bf16 v[88:91], a[88:91], a[68:71], v[88:91]
buffer_load_dwordx4 v7, s[4:7], s44 offen offset:1024 sc1 lds
v_add_f32 v60, v60, v61
v_add_f32 v62, v62, v63
v_mfma_f32_16x16x32_bf16 v[92:95], a[92:95], a[68:71], v[92:95]
ds_read_b128 a[104:107], v244 offset:6144
v_add_u32 v247, s53, v247
v_rcp_f32 v56, v56
v_mfma_f32_16x16x32_bf16 v[96:99], a[80:83], a[72:75], v[96:99]
ds_read_b64_tr_b16 a[124:125], v248 offset:8192
ds_read_b64_tr_b16 a[126:127], v248 offset:10240
v_rcp_f32 v58, v58
v_mfma_f32_16x16x32_bf16 v[100:103], a[84:87], a[72:75], v[100:103]
ds_read_b128 a[108:111], v244 offset:6400
v_add_u32 v248, s53, v248
v_rcp_f32 v60, v60
v_mfma_f32_16x16x32_bf16 v[104:107], a[88:91], a[72:75], v[104:107]
buffer_load_dwordx4 v7, s[4:7], s44 offen offset:2048 sc1 lds
v_rcp_f32 v62, v62
v_mul_f32 v160, v56, v160
v_mfma_f32_16x16x32_bf16 v[108:111], a[92:95], a[72:75], v[108:111]
s_add_u32 s49, s46, s91
v_add_u32 v244, s53, v244
v_mul_f32 v161, v56, v161
v_mul_f32 v162, v56, v162
v_mul_f32 v163, v56, v163
v_mfma_f32_16x16x32_bf16 v[112:115], a[80:83], a[76:79], v[112:115]
s_add_u32 s46, s46, s53
v_mul_f32 v168, v58, v168
v_mul_f32 v169, v58, v169
v_mul_f32 v170, v58, v170
v_mul_f32 v171, v58, v171
v_mfma_f32_16x16x32_bf16 v[116:119], a[84:87], a[76:79], v[116:119]
v_cvt_pk_bf16_f32 v128, v160, v161
v_cvt_pk_bf16_f32 v130, v168, v169
v_cvt_pk_bf16_f32 v129, v162, v163
v_cvt_pk_bf16_f32 v131, v170, v171
v_mfma_f32_16x16x32_bf16 v[120:123], a[88:91], a[76:79], v[120:123]
buffer_load_dwordx4 v7, s[4:7], s44 offen offset:3072 sc1 lds
v_nop
v_permlane16_swap_b32 v128, v130
v_mfma_f32_16x16x32_bf16 v[124:127], a[92:95], a[76:79], v[124:127]
s_waitcnt vmcnt(24) lgkmcnt(0)
s_barrier
v_permlane16_swap_b32 v129, v131
v_mfma_f32_16x16x32_bf16 v[64:67], a[112:115], a[96:99], v[64:67]
buffer_store_dwordx4 v[128:131], v7, s[20:23], s40 offen offset:0 sc1
ds_read_b64_tr_b16 a[80:81], v245 offset:0
ds_read_b64_tr_b16 a[82:83], v245 offset:2048
v_mfma_f32_16x16x32_bf16 v[68:71], a[116:119], a[96:99], v[68:71]
s_add_u32 m0, s49, 16384
ds_read_b128 a[64:67], v244 offset:0
v_mul_f32 v164, v56, v164
v_mul_f32 v165, v56, v165
v_mfma_f32_16x16x32_bf16 v[72:75], a[120:123], a[96:99], v[72:75]
buffer_load_dwordx4 v240, s[8:11], s45 offen offset:0 lds
v_mul_f32 v166, v56, v166
v_mul_f32 v167, v56, v167
v_mul_f32 v172, v58, v172
v_mfma_f32_16x16x32_bf16 v[76:79], a[124:127], a[96:99], v[76:79]
ds_read_b64_tr_b16 a[84:85], v246 offset:0
ds_read_b64_tr_b16 a[86:87], v246 offset:2048
v_mul_f32 v173, v58, v173
v_mul_f32 v174, v58, v174
v_mfma_f32_16x16x32_bf16 v[80:83], a[112:115], a[100:103], v[80:83]
s_add_u32 s44, s44, 4096
ds_read_b128 a[68:71], v244 offset:256
v_mul_f32 v175, v58, v175
v_cvt_pk_bf16_f32 v136, v164, v165
v_mfma_f32_16x16x32_bf16 v[84:87], a[116:119], a[100:103], v[84:87]
ds_read_b64_tr_b16 a[88:89], v247 offset:0
ds_read_b64_tr_b16 a[90:91], v247 offset:2048
v_cvt_pk_bf16_f32 v138, v172, v173
v_cvt_pk_bf16_f32 v137, v166, v167
v_mfma_f32_16x16x32_bf16 v[88:91], a[120:123], a[100:103], v[88:91]
buffer_load_dwordx4 v241, s[8:11], s45 offen offset:1024 lds
v_cvt_pk_bf16_f32 v139, v174, v175
v_nop
v_mfma_f32_16x16x32_bf16 v[92:95], a[124:127], a[100:103], v[92:95]
ds_read_b128 a[72:75], v244 offset:4096
v_permlane16_swap_b32 v136, v138
v_mfma_f32_16x16x32_bf16 v[96:99], a[112:115], a[104:107], v[96:99]
ds_read_b64_tr_b16 a[92:93], v248 offset:0
ds_read_b64_tr_b16 a[94:95], v248 offset:2048
v_permlane16_swap_b32 v137, v139
v_mfma_f32_16x16x32_bf16 v[100:103], a[116:119], a[104:107], v[100:103]
buffer_store_dwordx4 v[136:139], v7, s[20:23], s40 offen offset:1024 sc1
ds_read_b128 a[76:79], v244 offset:4352
v_mul_f32 v176, v60, v176
v_mfma_f32_16x16x32_bf16 v[104:107], a[120:123], a[104:107], v[104:107]
buffer_load_dwordx4 v242, s[8:11], s45 offen offset:2048 lds
v_mul_f32 v177, v60, v177
v_mul_f32 v178, v60, v178
v_mul_f32 v179, v60, v179
v_mfma_f32_16x16x32_bf16 v[108:111], a[124:127], a[104:107], v[108:111]
s_cmp_eq_u32 s46, 0
s_cselect_b32 s53, s56, s57
v_mul_f32 v184, v62, v184
v_mul_f32 v185, v62, v185
v_mul_f32 v186, v62, v186
v_mfma_f32_16x16x32_bf16 v[112:115], a[112:115], a[108:111], v[112:115]
v_mul_f32 v187, v62, v187
v_cvt_pk_bf16_f32 v144, v176, v177
v_cvt_pk_bf16_f32 v146, v184, v185
v_cvt_pk_bf16_f32 v145, v178, v179
v_mfma_f32_16x16x32_bf16 v[116:119], a[116:119], a[108:111], v[116:119]
v_cvt_pk_bf16_f32 v147, v186, v187
v_nop
v_permlane16_swap_b32 v144, v146
v_mfma_f32_16x16x32_bf16 v[120:123], a[120:123], a[108:111], v[120:123]
buffer_load_dwordx4 v243, s[8:11], s45 offen offset:3072 lds
v_permlane16_swap_b32 v145, v147
v_mfma_f32_16x16x32_bf16 v[124:127], a[124:127], a[108:111], v[124:127]
s_waitcnt lgkmcnt(0)
s_mov_b32 m0, s49
s_add_u32 s45, s45, 131072
buffer_store_dwordx4 v[144:147], v7, s[20:23], s41 offen offset:0 sc1
v_mfma_f32_16x16x32_bf16 v[64:67], a[80:83], a[64:67], v[64:67]
ds_read_b64_tr_b16 a[112:113], v245 offset:8192
ds_read_b64_tr_b16 a[114:115], v245 offset:10240
v_mul_f32 v180, v60, v180
v_mul_f32 v181, v60, v181
v_mfma_f32_16x16x32_bf16 v[68:71], a[84:87], a[64:67], v[68:71]
ds_read_b128 a[96:99], v244 offset:2048
v_add_u32 v245, s53, v245
v_mul_f32 v182, v60, v182
v_mul_f32 v183, v60, v183
v_mfma_f32_16x16x32_bf16 v[72:75], a[88:91], a[64:67], v[72:75]
buffer_load_dwordx4 v7, s[4:7], s44 offen offset:0 sc1 lds
v_mul_f32 v188, v62, v188
v_mul_f32 v189, v62, v189
v_mul_f32 v190, v62, v190
v_mfma_f32_16x16x32_bf16 v[76:79], a[92:95], a[64:67], v[76:79]
ds_read_b64_tr_b16 a[116:117], v246 offset:8192
ds_read_b64_tr_b16 a[118:119], v246 offset:10240
v_mul_f32 v191, v62, v191
v_cvt_pk_bf16_f32 v152, v180, v181
v_mfma_f32_16x16x32_bf16 v[80:83], a[80:83], a[68:71], v[80:83]
ds_read_b128 a[100:103], v244 offset:2304
v_add_u32 v246, s53, v246
v_cvt_pk_bf16_f32 v154, v188, v189
v_cvt_pk_bf16_f32 v153, v182, v183
v_mfma_f32_16x16x32_bf16 v[84:87], a[84:87], a[68:71], v[84:87]
ds_read_b64_tr_b16 a[120:121], v247 offset:8192
ds_read_b64_tr_b16 a[122:123], v247 offset:10240
v_cvt_pk_bf16_f32 v155, v190, v191
v_nop
v_mfma_f32_16x16x32_bf16 v[88:91], a[88:91], a[68:71], v[88:91]
buffer_load_dwordx4 v7, s[4:7], s44 offen offset:1024 sc1 lds
v_permlane16_swap_b32 v152, v154
v_mfma_f32_16x16x32_bf16 v[92:95], a[92:95], a[68:71], v[92:95]
ds_read_b128 a[104:107], v244 offset:6144
v_add_u32 v247, s53, v247
v_permlane16_swap_b32 v153, v155
v_mfma_f32_16x16x32_bf16 v[96:99], a[80:83], a[72:75], v[96:99]
buffer_store_dwordx4 v[152:155], v7, s[20:23], s41 offen offset:1024 sc1
ds_read_b64_tr_b16 a[124:125], v248 offset:8192
ds_read_b64_tr_b16 a[126:127], v248 offset:10240
v_mfma_f32_16x16x32_bf16 v[100:103], a[84:87], a[72:75], v[100:103]
ds_read_b128 a[108:111], v244 offset:6400
v_add_u32 v248, s53, v248
v_mfma_f32_16x16x32_bf16 v[104:107], a[88:91], a[72:75], v[104:107]
buffer_load_dwordx4 v7, s[4:7], s44 offen offset:2048 sc1 lds
v_mfma_f32_16x16x32_bf16 v[108:111], a[92:95], a[72:75], v[108:111]
s_add_u32 s49, s46, s91
v_add_u32 v244, s53, v244
v_mfma_f32_16x16x32_bf16 v[112:115], a[80:83], a[76:79], v[112:115]
s_add_u32 s46, s46, s53
v_mfma_f32_16x16x32_bf16 v[116:119], a[84:87], a[76:79], v[116:119]
v_mfma_f32_16x16x32_bf16 v[120:123], a[88:91], a[76:79], v[120:123]
buffer_load_dwordx4 v7, s[4:7], s44 offen offset:3072 sc1 lds
v_mfma_f32_16x16x32_bf16 v[124:127], a[92:95], a[76:79], v[124:127]
s_waitcnt vmcnt(28) lgkmcnt(0)
s_barrier
v_mfma_f32_16x16x32_bf16 v[64:67], a[112:115], a[96:99], v[64:67]
ds_read_b64_tr_b16 a[80:81], v245 offset:0
ds_read_b64_tr_b16 a[82:83], v245 offset:2048
v_mfma_f32_16x16x32_bf16 v[68:71], a[116:119], a[96:99], v[68:71]
s_add_u32 m0, s49, 16384
ds_read_b128 a[64:67], v244 offset:0
v_mfma_f32_16x16x32_bf16 v[72:75], a[120:123], a[96:99], v[72:75]
buffer_load_dwordx4 v240, s[8:11], s45 offen offset:0 lds
v_mfma_f32_16x16x32_bf16 v[76:79], a[124:127], a[96:99], v[76:79]
ds_read_b64_tr_b16 a[84:85], v246 offset:0
ds_read_b64_tr_b16 a[86:87], v246 offset:2048
v_mfma_f32_16x16x32_bf16 v[80:83], a[112:115], a[100:103], v[80:83]
s_add_u32 s44, s44, 4096
ds_read_b128 a[68:71], v244 offset:256
v_mfma_f32_16x16x32_bf16 v[84:87], a[116:119], a[100:103], v[84:87]
ds_read_b64_tr_b16 a[88:89], v247 offset:0
ds_read_b64_tr_b16 a[90:91], v247 offset:2048
v_mfma_f32_16x16x32_bf16 v[88:91], a[120:123], a[100:103], v[88:91]
buffer_load_dwordx4 v241, s[8:11], s45 offen offset:1024 lds
v_mfma_f32_16x16x32_bf16 v[92:95], a[124:127], a[100:103], v[92:95]
ds_read_b128 a[72:75], v244 offset:4096
v_mfma_f32_16x16x32_bf16 v[96:99], a[112:115], a[104:107], v[96:99]
ds_read_b64_tr_b16 a[92:93], v248 offset:0
ds_read_b64_tr_b16 a[94:95], v248 offset:2048
v_mfma_f32_16x16x32_bf16 v[100:103], a[116:119], a[104:107], v[100:103]
ds_read_b128 a[76:79], v244 offset:4352
v_mfma_f32_16x16x32_bf16 v[104:107], a[120:123], a[104:107], v[104:107]
buffer_load_dwordx4 v242, s[8:11], s45 offen offset:2048 lds
v_mfma_f32_16x16x32_bf16 v[108:111], a[124:127], a[104:107], v[108:111]
s_cmp_eq_u32 s46, 0
s_cselect_b32 s53, s56, s57
v_mfma_f32_16x16x32_bf16 v[112:115], a[112:115], a[108:111], v[112:115]
v_mfma_f32_16x16x32_bf16 v[116:119], a[116:119], a[108:111], v[116:119]
.Lpad19:
s_nop 0
v_mfma_f32_16x16x32_bf16 v[120:123], a[120:123], a[108:111], v[120:123]
buffer_load_dwordx4 v243, s[8:11], s45 offen offset:3072 lds
v_mfma_f32_16x16x32_bf16 v[124:127], a[124:127], a[108:111], v[124:127]
s_waitcnt lgkmcnt(0)
s_mov_b32 m0, s49
s_add_u32 s45, s45, 131072
v_mfma_f32_16x16x32_bf16 v[64:67], a[80:83], a[64:67], v[64:67]
ds_read_b64_tr_b16 a[112:113], v245 offset:8192
ds_read_b64_tr_b16 a[114:115], v245 offset:10240
v_mfma_f32_16x16x32_bf16 v[68:71], a[84:87], a[64:67], v[68:71]
ds_read_b128 a[96:99], v244 offset:2048
v_add_u32 v245, s53, v245
v_mfma_f32_16x16x32_bf16 v[72:75], a[88:91], a[64:67], v[72:75]
buffer_load_dwordx4 v7, s[4:7], s44 offen offset:0 sc1 lds
v_mfma_f32_16x16x32_bf16 v[76:79], a[92:95], a[64:67], v[76:79]
ds_read_b64_tr_b16 a[116:117], v246 offset:8192
ds_read_b64_tr_b16 a[118:119], v246 offset:10240
v_mfma_f32_16x16x32_bf16 v[80:83], a[80:83], a[68:71], v[80:83]
ds_read_b128 a[100:103], v244 offset:2304
v_add_u32 v246, s53, v246
v_mfma_f32_16x16x32_bf16 v[84:87], a[84:87], a[68:71], v[84:87]
ds_read_b64_tr_b16 a[120:121], v247 offset:8192
ds_read_b64_tr_b16 a[122:123], v247 offset:10240
v_mfma_f32_16x16x32_bf16 v[88:91], a[88:91], a[68:71], v[88:91]
buffer_load_dwordx4 v7, s[4:7], s44 offen offset:1024 sc1 lds
v_mfma_f32_16x16x32_bf16 v[92:95], a[92:95], a[68:71], v[92:95]
ds_read_b128 a[104:107], v244 offset:6144
v_add_u32 v247, s53, v247
v_mfma_f32_16x16x32_bf16 v[96:99], a[80:83], a[72:75], v[96:99]
ds_read_b64_tr_b16 a[124:125], v248 offset:8192
ds_read_b64_tr_b16 a[126:127], v248 offset:10240
v_mfma_f32_16x16x32_bf16 v[100:103], a[84:87], a[72:75], v[100:103]
ds_read_b128 a[108:111], v244 offset:6400
v_add_u32 v248, s53, v248
v_mfma_f32_16x16x32_bf16 v[104:107], a[88:91], a[72:75], v[104:107]
buffer_load_dwordx4 v7, s[4:7], s44 offen offset:2048 sc1 lds
v_mfma_f32_16x16x32_bf16 v[108:111], a[92:95], a[72:75], v[108:111]
s_add_u32 s49, s46, s91
v_add_u32 v244, s53, v244
v_mfma_f32_16x16x32_bf16 v[112:115], a[80:83], a[76:79], v[112:115]
s_add_u32 s46, s46, s53
v_mfma_f32_16x16x32_bf16 v[116:119], a[84:87], a[76:79], v[116:119]
v_mfma_f32_16x16x32_bf16 v[120:123], a[88:91], a[76:79], v[120:123]
buffer_load_dwordx4 v7, s[4:7], s44 offen offset:3072 sc1 lds
v_mfma_f32_16x16x32_bf16 v[124:127], a[92:95], a[76:79], v[124:127]
s_waitcnt vmcnt(28) lgkmcnt(0)
s_barrier
v_mfma_f32_16x16x32_bf16 v[64:67], a[112:115], a[96:99], v[64:67]
ds_read_b64_tr_b16 a[80:81], v245 offset:0
ds_read_b64_tr_b16 a[82:83], v245 offset:2048
v_mfma_f32_16x16x32_bf16 v[68:71], a[116:119], a[96:99], v[68:71]
s_add_u32 m0, s49, 16384
ds_read_b128 a[64:67], v244 offset:0
v_mfma_f32_16x16x32_bf16 v[72:75], a[120:123], a[96:99], v[72:75]
buffer_load_dwordx4 v240, s[8:11], s45 offen offset:0 lds
v_mfma_f32_16x16x32_bf16 v[76:79], a[124:127], a[96:99], v[76:79]
ds_read_b64_tr_b16 a[84:85], v246 offset:0
ds_read_b64_tr_b16 a[86:87], v246 offset:2048
v_mfma_f32_16x16x32_bf16 v[80:83], a[112:115], a[100:103], v[80:83]
s_add_u32 s44, s44, 4096
ds_read_b128 a[68:71], v244 offset:256
v_mfma_f32_16x16x32_bf16 v[84:87], a[116:119], a[100:103], v[84:87]
ds_read_b64_tr_b16 a[88:89], v247 offset:0
ds_read_b64_tr_b16 a[90:91], v247 offset:2048
v_mfma_f32_16x16x32_bf16 v[88:91], a[120:123], a[100:103], v[88:91]
buffer_load_dwordx4 v241, s[8:11], s45 offen offset:1024 lds
v_mfma_f32_16x16x32_bf16 v[92:95], a[124:127], a[100:103], v[92:95]
ds_read_b128 a[72:75], v244 offset:4096
v_mfma_f32_16x16x32_bf16 v[96:99], a[112:115], a[104:107], v[96:99]
ds_read_b64_tr_b16 a[92:93], v248 offset:0
ds_read_b64_tr_b16 a[94:95], v248 offset:2048
v_mfma_f32_16x16x32_bf16 v[100:103], a[116:119], a[104:107], v[100:103]
ds_read_b128 a[76:79], v244 offset:4352
v_mfma_f32_16x16x32_bf16 v[104:107], a[120:123], a[104:107], v[104:107]
buffer_load_dwordx4 v242, s[8:11], s45 offen offset:2048 lds
v_mfma_f32_16x16x32_bf16 v[108:111], a[124:127], a[104:107], v[108:111]
s_cmp_eq_u32 s46, 0
s_cselect_b32 s53, s56, s57
v_mfma_f32_16x16x32_bf16 v[112:115], a[112:115], a[108:111], v[112:115]
v_mfma_f32_16x16x32_bf16 v[116:119], a[116:119], a[108:111], v[116:119]
v_mfma_f32_16x16x32_bf16 v[120:123], a[120:123], a[108:111], v[120:123]
buffer_load_dwordx4 v243, s[8:11], s45 offen offset:3072 lds
v_mfma_f32_16x16x32_bf16 v[124:127], a[124:127], a[108:111], v[124:127]
s_waitcnt lgkmcnt(0)
s_mov_b32 m0, s49
s_add_u32 s45, s45, 131072
v_mfma_f32_16x16x32_bf16 v[64:67], a[80:83], a[64:67], v[64:67]
ds_read_b64_tr_b16 a[112:113], v245 offset:8192
ds_read_b64_tr_b16 a[114:115], v245 offset:10240
v_mfma_f32_16x16x32_bf16 v[68:71], a[84:87], a[64:67], v[68:71]
ds_read_b128 a[96:99], v244 offset:2048
v_add_u32 v245, s53, v245
v_mfma_f32_16x16x32_bf16 v[72:75], a[88:91], a[64:67], v[72:75]
buffer_load_dwordx4 v7, s[4:7], s44 offen offset:0 sc1 lds
v_mfma_f32_16x16x32_bf16 v[76:79], a[92:95], a[64:67], v[76:79]
ds_read_b64_tr_b16 a[116:117], v246 offset:8192
ds_read_b64_tr_b16 a[118:119], v246 offset:10240
v_mfma_f32_16x16x32_bf16 v[80:83], a[80:83], a[68:71], v[80:83]
ds_read_b128 a[100:103], v244 offset:2304
v_add_u32 v246, s53, v246
v_mfma_f32_16x16x32_bf16 v[84:87], a[84:87], a[68:71], v[84:87]
ds_read_b64_tr_b16 a[120:121], v247 offset:8192
ds_read_b64_tr_b16 a[122:123], v247 offset:10240
v_mfma_f32_16x16x32_bf16 v[88:91], a[88:91], a[68:71], v[88:91]
buffer_load_dwordx4 v7, s[4:7], s44 offen offset:1024 sc1 lds
v_mfma_f32_16x16x32_bf16 v[92:95], a[92:95], a[68:71], v[92:95]
ds_read_b128 a[104:107], v244 offset:6144
v_add_u32 v247, s53, v247
v_mfma_f32_16x16x32_bf16 v[96:99], a[80:83], a[72:75], v[96:99]
ds_read_b64_tr_b16 a[124:125], v248 offset:8192
ds_read_b64_tr_b16 a[126:127], v248 offset:10240
v_mfma_f32_16x16x32_bf16 v[100:103], a[84:87], a[72:75], v[100:103]
ds_read_b128 a[108:111], v244 offset:6400
v_add_u32 v248, s53, v248
v_mfma_f32_16x16x32_bf16 v[104:107], a[88:91], a[72:75], v[104:107]
buffer_load_dwordx4 v7, s[4:7], s44 offen offset:2048 sc1 lds
v_mfma_f32_16x16x32_bf16 v[108:111], a[92:95], a[72:75], v[108:111]
s_add_u32 s49, s46, s91
v_add_u32 v244, s53, v244
v_mfma_f32_16x16x32_bf16 v[112:115], a[80:83], a[76:79], v[112:115]
s_add_u32 s46, s46, s53
v_mfma_f32_16x16x32_bf16 v[116:119], a[84:87], a[76:79], v[116:119]
v_mfma_f32_16x16x32_bf16 v[120:123], a[88:91], a[76:79], v[120:123]
buffer_load_dwordx4 v7, s[4:7], s44 offen offset:3072 sc1 lds
v_mfma_f32_16x16x32_bf16 v[124:127], a[92:95], a[76:79], v[124:127]
s_waitcnt vmcnt(18)
s_waitcnt vmcnt(28) lgkmcnt(0)
s_barrier
v_mfma_f32_16x16x32_bf16 v[64:67], a[112:115], a[96:99], v[64:67]
ds_read_b64_tr_b16 a[80:81], v245 offset:0
ds_read_b64_tr_b16 a[82:83], v245 offset:2048
v_mfma_f32_16x16x32_bf16 v[68:71], a[116:119], a[96:99], v[68:71]
s_add_u32 m0, s49, 16384
ds_read_b128 a[64:67], v244 offset:0
v_mfma_f32_16x16x32_bf16 v[72:75], a[120:123], a[96:99], v[72:75]
buffer_load_dwordx4 v240, s[8:11], s45 offen offset:0 lds
v_mfma_f32_16x16x32_bf16 v[76:79], a[124:127], a[96:99], v[76:79]
ds_read_b64_tr_b16 a[84:85], v246 offset:0
ds_read_b64_tr_b16 a[86:87], v246 offset:2048
v_mfma_f32_16x16x32_bf16 v[80:83], a[112:115], a[100:103], v[80:83]
s_add_u32 s44, s44, 4096
ds_read_b128 a[68:71], v244 offset:256
v_mfma_f32_16x16x32_bf16 v[84:87], a[116:119], a[100:103], v[84:87]
ds_read_b64_tr_b16 a[88:89], v247 offset:0
ds_read_b64_tr_b16 a[90:91], v247 offset:2048
v_mfma_f32_16x16x32_bf16 v[88:91], a[120:123], a[100:103], v[88:91]
buffer_load_dwordx4 v241, s[8:11], s45 offen offset:1024 lds
v_mfma_f32_16x16x32_bf16 v[92:95], a[124:127], a[100:103], v[92:95]
ds_read_b128 a[72:75], v244 offset:4096
v_mfma_f32_16x16x32_bf16 v[96:99], a[112:115], a[104:107], v[96:99]
ds_read_b64_tr_b16 a[92:93], v248 offset:0
ds_read_b64_tr_b16 a[94:95], v248 offset:2048
v_mfma_f32_16x16x32_bf16 v[100:103], a[116:119], a[104:107], v[100:103]
ds_read_b128 a[76:79], v244 offset:4352
v_mfma_f32_16x16x32_bf16 v[104:107], a[120:123], a[104:107], v[104:107]
s_nop 0
buffer_load_dwordx4 v242, s[8:11], s45 offen offset:2048 lds
v_or_b32 v0, 4, v11
v_mfma_f32_16x16x32_bf16 v[108:111], a[124:127], a[104:107], v[108:111]
s_cmp_eq_u32 s46, 0
s_cselect_b32 s53, s56, s57
v_mfma_f32_16x16x32_bf16 v[112:115], a[112:115], a[108:111], v[112:115]
v_mfma_f32_16x16x32_bf16 v[116:119], a[116:119], a[108:111], v[116:119]
v_mfma_f32_16x16x32_bf16 v[120:123], a[120:123], a[108:111], v[120:123]
buffer_load_dwordx4 v243, s[8:11], s45 offen offset:3072 lds
buffer_store_dword v0, v6, s[60:63], 0 offen sc1
v_mfma_f32_16x16x32_bf16 v[124:127], a[124:127], a[108:111], v[124:127]
s_waitcnt lgkmcnt(0)
s_mov_b32 m0, s49
s_add_u32 s45, s45, 131072
v_mfma_f32_16x16x32_bf16 v[64:67], a[80:83], a[64:67], v[64:67]
ds_read_b64_tr_b16 a[112:113], v245 offset:8192
ds_read_b64_tr_b16 a[114:115], v245 offset:10240
v_mfma_f32_16x16x32_bf16 v[68:71], a[84:87], a[64:67], v[68:71]
ds_read_b128 a[96:99], v244 offset:2048
v_add_u32 v245, s53, v245
v_mfma_f32_16x16x32_bf16 v[72:75], a[88:91], a[64:67], v[72:75]
buffer_load_dwordx4 v7, s[4:7], s44 offen offset:0 sc1 lds
v_mfma_f32_16x16x32_bf16 v[76:79], a[92:95], a[64:67], v[76:79]
ds_read_b64_tr_b16 a[116:117], v246 offset:8192
ds_read_b64_tr_b16 a[118:119], v246 offset:10240
v_mfma_f32_16x16x32_bf16 v[80:83], a[80:83], a[68:71], v[80:83]
ds_read_b128 a[100:103], v244 offset:2304
v_add_u32 v246, s53, v246
v_mfma_f32_16x16x32_bf16 v[84:87], a[84:87], a[68:71], v[84:87]
ds_read_b64_tr_b16 a[120:121], v247 offset:8192
ds_read_b64_tr_b16 a[122:123], v247 offset:10240
v_mfma_f32_16x16x32_bf16 v[88:91], a[88:91], a[68:71], v[88:91]
buffer_load_dwordx4 v7, s[4:7], s44 offen offset:1024 sc1 lds
v_mfma_f32_16x16x32_bf16 v[92:95], a[92:95], a[68:71], v[92:95]
ds_read_b128 a[104:107], v244 offset:6144
v_add_u32 v247, s53, v247
v_mfma_f32_16x16x32_bf16 v[96:99], a[80:83], a[72:75], v[96:99]
buffer_load_dword v35, v4, s[96:99], 0 offen
ds_read_b64_tr_b16 a[124:125], v248 offset:8192
ds_read_b64_tr_b16 a[126:127], v248 offset:10240
v_mfma_f32_16x16x32_bf16 v[100:103], a[84:87], a[72:75], v[100:103]
ds_read_b128 a[108:111], v244 offset:6400
v_add_u32 v248, s53, v248
v_mfma_f32_16x16x32_bf16 v[104:107], a[88:91], a[72:75], v[104:107]
buffer_load_dwordx4 v7, s[4:7], s44 offen offset:2048 sc1 lds
v_mfma_f32_16x16x32_bf16 v[108:111], a[92:95], a[72:75], v[108:111]
s_add_u32 s49, s46, s91
v_add_u32 v244, s53, v244
v_mfma_f32_16x16x32_bf16 v[112:115], a[80:83], a[76:79], v[112:115]
s_add_u32 s46, s46, s53
v_mfma_f32_16x16x32_bf16 v[116:119], a[84:87], a[76:79], v[116:119]
v_mfma_f32_16x16x32_bf16 v[120:123], a[88:91], a[76:79], v[120:123]
buffer_load_dwordx4 v7, s[4:7], s44 offen offset:3072 sc1 lds
v_mfma_f32_16x16x32_bf16 v[124:127], a[92:95], a[76:79], v[124:127]
s_waitcnt vmcnt(26) lgkmcnt(0)
s_barrier
s_mov_b64 exec, 0
s_cmp_eq_u32 s3, 0
s_cbranch_scc1 .Lpad20
s_cmp_eq_u32 s3, 1
s_cbranch_scc1 .L5odd_pad
s_cmp_eq_u32 s3, 2
s_cbranch_scc1 .Lpad21
s_branch .Lpad22
.Ltch9:
s_mov_b64 exec, -1
s_nop 0
s_nop 0
s_nop 0
s_nop 0
s_nop 0
s_nop 0
.L5b:
v_mfma_f32_16x16x32_bf16 v[64:67], a[112:115], a[96:99], v[64:67]
ds_read_b64_tr_b16 a[80:81], v245 offset:0
ds_read_b64_tr_b16 a[82:83], v245 offset:2048
v_mfma_f32_16x16x32_bf16 v[68:71], a[116:119], a[96:99], v[68:71]
s_add_u32 m0, s49, 16384
ds_read_b128 a[64:67], v244 offset:0
v_mfma_f32_16x16x32_bf16 v[72:75], a[120:123], a[96:99], v[72:75]
buffer_load_dwordx4 v240, s[8:11], s45 offen offset:0 lds
v_mfma_f32_16x16x32_bf16 v[76:79], a[124:127], a[96:99], v[76:79]
ds_read_b64_tr_b16 a[84:85], v246 offset:0
ds_read_b64_tr_b16 a[86:87], v246 offset:2048
v_mfma_f32_16x16x32_bf16 v[80:83], a[112:115], a[100:103], v[80:83]
s_add_u32 s44, s44, 4096
ds_read_b128 a[68:71], v244 offset:256
v_mfma_f32_16x16x32_bf16 v[84:87], a[116:119], a[100:103], v[84:87]
ds_read_b64_tr_b16 a[88:89], v247 offset:0
ds_read_b64_tr_b16 a[90:91], v247 offset:2048
v_mfma_f32_16x16x32_bf16 v[88:91], a[120:123], a[100:103], v[88:91]
buffer_load_dwordx4 v241, s[8:11], s45 offen offset:1024 lds
v_mfma_f32_16x16x32_bf16 v[92:95], a[124:127], a[100:103], v[92:95]
ds_read_b128 a[72:75], v244 offset:4096
v_mfma_f32_16x16x32_bf16 v[96:99], a[112:115], a[104:107], v[96:99]
ds_read_b64_tr_b16 a[92:93], v248 offset:0
ds_read_b64_tr_b16 a[94:95], v248 offset:2048
v_mfma_f32_16x16x32_bf16 v[100:103], a[116:119], a[104:107], v[100:103]
ds_read_b128 a[76:79], v244 offset:4352
v_mfma_f32_16x16x32_bf16 v[104:107], a[120:123], a[104:107], v[104:107]
buffer_load_dwordx4 v242, s[8:11], s45 offen offset:2048 lds
v_mfma_f32_16x16x32_bf16 v[108:111], a[124:127], a[104:107], v[108:111]
s_cmp_eq_u32 s46, 0
s_cselect_b32 s53, s56, s57
v_mfma_f32_16x16x32_bf16 v[112:115], a[112:115], a[108:111], v[112:115]
v_mfma_f32_16x16x32_bf16 v[116:119], a[116:119], a[108:111], v[116:119]
v_mfma_f32_16x16x32_bf16 v[120:123], a[120:123], a[108:111], v[120:123]
buffer_load_dwordx4 v243, s[8:11], s45 offen offset:3072 lds
v_mfma_f32_16x16x32_bf16 v[124:127], a[124:127], a[108:111], v[124:127]
s_waitcnt lgkmcnt(0)
s_mov_b32 m0, s49
s_add_u32 s45, s45, 131072
v_mfma_f32_16x16x32_bf16 v[64:67], a[80:83], a[64:67], v[64:67]
ds_read_b64_tr_b16 a[112:113], v245 offset:8192
ds_read_b64_tr_b16 a[114:115], v245 offset:10240
v_mfma_f32_16x16x32_bf16 v[68:71], a[84:87], a[64:67], v[68:71]
ds_read_b128 a[96:99], v244 offset:2048
v_add_u32 v245, s53, v245
v_mfma_f32_16x16x32_bf16 v[72:75], a[88:91], a[64:67], v[72:75]
buffer_load_dwordx4 v7, s[4:7], s44 offen offset:0 sc1 lds
v_mfma_f32_16x16x32_bf16 v[76:79], a[92:95], a[64:67], v[76:79]
ds_read_b64_tr_b16 a[116:117], v246 offset:8192
ds_read_b64_tr_b16 a[118:119], v246 offset:10240
v_mfma_f32_16x16x32_bf16 v[80:83], a[80:83], a[68:71], v[80:83]
ds_read_b128 a[100:103], v244 offset:2304
v_add_u32 v246, s53, v246
v_mfma_f32_16x16x32_bf16 v[84:87], a[84:87], a[68:71], v[84:87]
ds_read_b64_tr_b16 a[120:121], v247 offset:8192
ds_read_b64_tr_b16 a[122:123], v247 offset:10240
v_mfma_f32_16x16x32_bf16 v[88:91], a[88:91], a[68:71], v[88:91]
buffer_load_dwordx4 v7, s[4:7], s44 offen offset:1024 sc1 lds
v_mfma_f32_16x16x32_bf16 v[92:95], a[92:95], a[68:71], v[92:95]
ds_read_b128 a[104:107], v244 offset:6144
v_add_u32 v247, s53, v247
v_mfma_f32_16x16x32_bf16 v[96:99], a[80:83], a[72:75], v[96:99]
ds_read_b64_tr_b16 a[124:125], v248 offset:8192
ds_read_b64_tr_b16 a[126:127], v248 offset:10240
v_mfma_f32_16x16x32_bf16 v[100:103], a[84:87], a[72:75], v[100:103]
ds_read_b128 a[108:111], v244 offset:6400
v_add_u32 v248, s53, v248
v_mfma_f32_16x16x32_bf16 v[104:107], a[88:91], a[72:75], v[104:107]
buffer_load_dwordx4 v7, s[4:7], s44 offen offset:2048 sc1 lds
v_mfma_f32_16x16x32_bf16 v[108:111], a[92:95], a[72:75], v[108:111]
s_add_u32 s49, s46, s91
v_add_u32 v244, s53, v244
v_mfma_f32_16x16x32_bf16 v[112:115], a[80:83], a[76:79], v[112:115]
s_add_u32 s46, s46, s53
v_mfma_f32_16x16x32_bf16 v[116:119], a[84:87], a[76:79], v[116:119]
s_sub_u32 s50, s50, 1
v_mfma_f32_16x16x32_bf16 v[120:123], a[88:91], a[76:79], v[120:123]
buffer_load_dwordx4 v7, s[4:7], s44 offen offset:3072 sc1 lds
v_mfma_f32_16x16x32_bf16 v[124:127], a[92:95], a[76:79], v[124:127]
s_waitcnt vmcnt(24) lgkmcnt(0)
s_barrier
s_cbranch_scc0 .L5b
s_cmp_eq_u32 s55, 0
s_cbranch_scc0 .L5odd
v_mfma_f32_16x16x32_bf16 v[64:67], a[112:115], a[96:99], v[64:67]
ds_read_b64_tr_b16 a[80:81], v245 offset:0
ds_read_b64_tr_b16 a[82:83], v245 offset:2048
v_mfma_f32_16x16x32_bf16 v[68:71], a[116:119], a[96:99], v[68:71]
buffer_load_dword v250, v249, s[60:63], s90 offen sc1
ds_read_b128 a[64:67], v244 offset:0
v_mfma_f32_16x16x32_bf16 v[72:75], a[120:123], a[96:99], v[72:75]
v_and_b32 v168, 15, v1
v_lshrrev_b32 v169, 4, v1
v_mfma_f32_16x16x32_bf16 v[76:79], a[124:127], a[96:99], v[76:79]
ds_read_b64_tr_b16 a[84:85], v246 offset:0
ds_read_b64_tr_b16 a[86:87], v246 offset:2048
v_mfma_f32_16x16x32_bf16 v[80:83], a[112:115], a[100:103], v[80:83]
s_mov_b32 s32, 4096
ds_read_b128 a[68:71], v244 offset:256
v_lshlrev_b32 v169, 2, v169
v_mfma_f32_16x16x32_bf16 v[84:87], a[116:119], a[100:103], v[84:87]
ds_read_b64_tr_b16 a[88:89], v247 offset:0
ds_read_b64_tr_b16 a[90:91], v247 offset:2048
v_mfma_f32_16x16x32_bf16 v[88:91], a[120:123], a[100:103], v[88:91]
v_sub_u32 v168, v168, v169
v_mov_b32 v169, 0x3f80
v_mfma_f32_16x16x32_bf16 v[92:95], a[124:127], a[100:103], v[92:95]
ds_read_b128 a[72:75], v244 offset:4096
v_mov_b32 v170, 0x3f800000
v_mfma_f32_16x16x32_bf16 v[96:99], a[112:115], a[104:107], v[96:99]
ds_read_b64_tr_b16 a[92:93], v248 offset:0
ds_read_b64_tr_b16 a[94:95], v248 offset:2048
v_mfma_f32_16x16x32_bf16 v[100:103], a[116:119], a[104:107], v[100:103]
ds_read_b128 a[76:79], v244 offset:4352
v_cmp_eq_u32 s[24:25], 0, v168
v_mfma_f32_16x16x32_bf16 v[104:107], a[120:123], a[104:107], v[104:107]
v_cmp_eq_u32 s[26:27], 1, v168
v_cmp_eq_u32 s[28:29], 2, v168
v_mfma_f32_16x16x32_bf16 v[108:111], a[124:127], a[104:107], v[108:111]
s_cmp_eq_u32 s46, 0
s_cselect_b32 s53, s56, s57
v_cmp_eq_u32 s[30:31], 3, v168
v_mfma_f32_16x16x32_bf16 v[112:115], a[112:115], a[108:111], v[112:115]
v_cndmask_b32 v160, 0, v169, s[24:25]
v_cndmask_b32 v161, 0, v169, s[28:29]
v_mfma_f32_16x16x32_bf16 v[116:119], a[116:119], a[108:111], v[116:119]
v_cndmask_b32 v160, v160, v170, s[26:27]
v_cndmask_b32 v161, v161, v170, s[30:31]
v_mfma_f32_16x16x32_bf16 v[120:123], a[120:123], a[108:111], v[120:123]
v_mov_b32 v162, 0
v_mov_b32 v163, 0
v_mfma_f32_16x16x32_bf16 v[124:127], a[124:127], a[108:111], v[124:127]
s_waitcnt lgkmcnt(0)
v_mov_b32 v164, 0
v_mov_b32 v165, 0
v_mfma_f32_16x16x32_bf16 v[64:67], a[80:83], a[64:67], v[64:67]
ds_read_b64_tr_b16 a[112:113], v245 offset:8192
ds_read_b64_tr_b16 a[114:115], v245 offset:10240
v_mfma_f32_16x16x32_bf16 v[68:71], a[84:87], a[64:67], v[68:71]
ds_read_b128 a[96:99], v244 offset:2048
v_add_u32 v245, s53, v245
v_mfma_f32_16x16x32_bf16 v[72:75], a[88:91], a[64:67], v[72:75]
v_mov_b32 v166, v160
v_mov_b32 v167, v161
v_mfma_f32_16x16x32_bf16 v[76:79], a[92:95], a[64:67], v[76:79]
ds_read_b64_tr_b16 a[116:117], v246 offset:8192
ds_read_b64_tr_b16 a[118:119], v246 offset:10240
v_mfma_f32_16x16x32_bf16 v[80:83], a[80:83], a[68:71], v[80:83]
ds_read_b128 a[100:103], v244 offset:2304
v_add_u32 v246, s53, v246
v_mfma_f32_16x16x32_bf16 v[84:87], a[84:87], a[68:71], v[84:87]
ds_read_b64_tr_b16 a[120:121], v247 offset:8192
ds_read_b64_tr_b16 a[122:123], v247 offset:10240
v_mfma_f32_16x16x32_bf16 v[88:91], a[88:91], a[68:71], v[88:91]
v_mfma_f32_16x16x32_bf16 v[92:95], a[92:95], a[68:71], v[92:95]
ds_read_b128 a[104:107], v244 offset:6144
v_add_u32 v247, s53, v247
v_mfma_f32_16x16x32_bf16 v[96:99], a[80:83], a[72:75], v[96:99]
ds_read_b64_tr_b16 a[124:125], v248 offset:8192
ds_read_b64_tr_b16 a[126:127], v248 offset:10240
v_mfma_f32_16x16x32_bf16 v[100:103], a[84:87], a[72:75], v[100:103]
ds_read_b128 a[108:111], v244 offset:6400
v_add_u32 v248, s53, v248
v_mfma_f32_16x16x32_bf16 v[104:107], a[88:91], a[72:75], v[104:107]
v_mfma_f32_16x16x32_bf16 v[108:111], a[92:95], a[72:75], v[108:111]
s_add_u32 s49, s46, s91
v_add_u32 v244, s53, v244
v_mfma_f32_16x16x32_bf16 v[112:115], a[80:83], a[76:79], v[112:115]
s_add_u32 s46, s46, s53
v_mfma_f32_16x16x32_bf16 v[116:119], a[84:87], a[76:79], v[116:119]
v_mfma_f32_16x16x32_bf16 v[120:123], a[88:91], a[76:79], v[120:123]
v_mfma_f32_16x16x32_bf16 v[124:127], a[92:95], a[76:79], v[124:127]
s_waitcnt vmcnt(17) lgkmcnt(0)
s_barrier
v_mfma_f32_16x16x32_bf16 v[64:67], a[112:115], a[96:99], v[64:67]
ds_read_b64_tr_b16 a[80:81], v245 offset:0
ds_read_b64_tr_b16 a[82:83], v245 offset:2048
v_mfma_f32_16x16x32_bf16 v[68:71], a[116:119], a[96:99], v[68:71]
ds_read_b128 a[64:67], v244 offset:0
v_mfma_f32_16x16x32_bf16 v[72:75], a[120:123], a[96:99], v[72:75]
v_mfma_f32_16x16x32_bf16 v[76:79], a[124:127], a[96:99], v[76:79]
ds_read_b64_tr_b16 a[84:85], v246 offset:0
ds_read_b64_tr_b16 a[86:87], v246 offset:2048
v_mfma_f32_16x16x32_bf16 v[80:83], a[112:115], a[100:103], v[80:83]
ds_read_b128 a[68:71], v244 offset:256
v_mfma_f32_16x16x32_bf16 v[84:87], a[116:119], a[100:103], v[84:87]
ds_read_b64_tr_b16 a[88:89], v247 offset:0
ds_read_b64_tr_b16 a[90:91], v247 offset:2048
v_mfma_f32_16x16x32_bf16 v[88:91], a[120:123], a[100:103], v[88:91]
v_mfma_f32_16x16x32_bf16 v[92:95], a[124:127], a[100:103], v[92:95]
ds_read_b128 a[72:75], v244 offset:4096
v_mfma_f32_16x16x32_bf16 v[96:99], a[112:115], a[104:107], v[96:99]
ds_read_b64_tr_b16 a[92:93], v248 offset:0
ds_read_b64_tr_b16 a[94:95], v248 offset:2048
v_mfma_f32_16x16x32_bf16 v[100:103], a[116:119], a[104:107], v[100:103]
ds_read_b128 a[76:79], v244 offset:4352
v_mfma_f32_16x16x32_bf16 v[104:107], a[120:123], a[104:107], v[104:107]
s_waitcnt vmcnt(0)
v_and_b32 v250, 7, v250
v_mfma_f32_16x16x32_bf16 v[108:111], a[124:127], a[104:107], v[108:111]
s_cmp_eq_u32 s46, 0
s_cselect_b32 s53, s56, s57
v_cmp_gt_u32 vcc, 5, v250
v_mfma_f32_16x16x32_bf16 v[112:115], a[112:115], a[108:111], v[112:115]
v_mfma_f32_16x16x32_bf16 v[116:119], a[116:119], a[108:111], v[116:119]
s_cmp_lg_u64 vcc, 0
s_cbranch_scc1 .Lspin5
.Lspin5_ret:
v_mfma_f32_16x16x32_bf16 v[120:123], a[120:123], a[108:111], v[120:123]
v_mfma_f32_16x16x32_bf16 v[124:127], a[124:127], a[108:111], v[124:127]
s_waitcnt lgkmcnt(0)
v_mfma_f32_16x16x32_bf16 v[64:67], a[80:83], a[64:67], v[64:67]
ds_read_b64_tr_b16 a[112:113], v245 offset:8192
ds_read_b64_tr_b16 a[114:115], v245 offset:10240
v_mfma_f32_16x16x32_bf16 v[68:71], a[84:87], a[64:67], v[68:71]
buffer_load_dwordx4 v[128:131], v7, s[12:15], 0 offen offset:0 sc1
ds_read_b128 a[96:99], v244 offset:2048
v_add_u32 v245, s53, v245
v_mfma_f32_16x16x32_bf16 v[72:75], a[88:91], a[64:67], v[72:75]
v_mfma_f32_16x16x32_bf16 v[76:79], a[92:95], a[64:67], v[76:79]
ds_read_b64_tr_b16 a[116:117], v246 offset:8192
ds_read_b64_tr_b16 a[118:119], v246 offset:10240
v_mfma_f32_16x16x32_bf16 v[80:83], a[80:83], a[68:71], v[80:83]
buffer_load_dwordx4 v[132:135], v7, s[12:15], 0 offen offset:1024 sc1
ds_read_b128 a[100:103], v244 offset:2304
v_add_u32 v246, s53, v246
v_mfma_f32_16x16x32_bf16 v[84:87], a[84:87], a[68:71], v[84:87]
ds_read_b64_tr_b16 a[120:121], v247 offset:8192
ds_read_b64_tr_b16 a[122:123], v247 offset:10240
v_mfma_f32_16x16x32_bf16 v[88:91], a[88:91], a[68:71], v[88:91]
v_mfma_f32_16x16x32_bf16 v[92:95], a[92:95], a[68:71], v[92:95]
buffer_load_dwordx4 v[136:139], v7, s[12:15], 0 offen offset:2048 sc1
ds_read_b128 a[104:107], v244 offset:6144
v_add_u32 v247, s53, v247
v_mfma_f32_16x16x32_bf16 v[96:99], a[80:83], a[72:75], v[96:99]
ds_read_b64_tr_b16 a[124:125], v248 offset:8192
ds_read_b64_tr_b16 a[126:127], v248 offset:10240
v_mfma_f32_16x16x32_bf16 v[100:103], a[84:87], a[72:75], v[100:103]
ds_read_b128 a[108:111], v244 offset:6400
v_add_u32 v248, s53, v248
v_mfma_f32_16x16x32_bf16 v[104:107], a[88:91], a[72:75], v[104:107]
buffer_load_dwordx4 v[140:143], v7, s[12:15], 0 offen offset:3072 sc1
v_mfma_f32_16x16x32_bf16 v[108:111], a[92:95], a[72:75], v[108:111]
s_add_u32 s49, s46, s91
v_add_u32 v244, s53, v244
v_mfma_f32_16x16x32_bf16 v[112:115], a[80:83], a[76:79], v[112:115]
s_add_u32 s46, s46, s53
v_mfma_f32_16x16x32_bf16 v[116:119], a[84:87], a[76:79], v[116:119]
v_mfma_f32_16x16x32_bf16 v[120:123], a[88:91], a[76:79], v[120:123]
v_mfma_f32_16x16x32_bf16 v[124:127], a[92:95], a[76:79], v[124:127]
s_waitcnt vmcnt(13) lgkmcnt(0)
s_barrier
v_mfma_f32_16x16x32_bf16 v[64:67], a[112:115], a[96:99], v[64:67]
ds_read_b64_tr_b16 a[80:81], v245 offset:0
ds_read_b64_tr_b16 a[82:83], v245 offset:2048
v_mfma_f32_16x16x32_bf16 v[68:71], a[116:119], a[96:99], v[68:71]
buffer_load_dwordx4 v[144:147], v7, s[12:15], s32 offen offset:0 sc1
ds_read_b128 a[64:67], v244 offset:0
v_mfma_f32_16x16x32_bf16 v[72:75], a[120:123], a[96:99], v[72:75]
v_mfma_f32_16x16x32_bf16 v[76:79], a[124:127], a[96:99], v[76:79]
ds_read_b64_tr_b16 a[84:85], v246 offset:0
ds_read_b64_tr_b16 a[86:87], v246 offset:2048
v_mfma_f32_16x16x32_bf16 v[80:83], a[112:115], a[100:103], v[80:83]
buffer_load_dwordx4 v[148:151], v7, s[12:15], s32 offen offset:1024 sc1
ds_read_b128 a[68:71], v244 offset:256
v_mfma_f32_16x16x32_bf16 v[84:87], a[116:119], a[100:103], v[84:87]
ds_read_b64_tr_b16 a[88:89], v247 offset:0
ds_read_b64_tr_b16 a[90:91], v247 offset:2048
v_mfma_f32_16x16x32_bf16 v[88:91], a[120:123], a[100:103], v[88:91]
v_mfma_f32_16x16x32_bf16 v[92:95], a[124:127], a[100:103], v[92:95]
ds_read_b128 a[72:75], v244 offset:4096
v_mfma_f32_16x16x32_bf16 v[96:99], a[112:115], a[104:107], v[96:99]
ds_read_b64_tr_b16 a[92:93], v248 offset:0
ds_read_b64_tr_b16 a[94:95], v248 offset:2048
v_mfma_f32_16x16x32_bf16 v[100:103], a[116:119], a[104:107], v[100:103]
buffer_load_dwordx4 v[152:155], v7, s[12:15], s32 offen offset:2048 sc1
ds_read_b128 a[76:79], v244 offset:4352
v_mfma_f32_16x16x32_bf16 v[104:107], a[120:123], a[104:107], v[104:107]
v_mfma_f32_16x16x32_bf16 v[108:111], a[124:127], a[104:107], v[108:111]
s_cmp_eq_u32 s46, 0
s_cselect_b32 s53, s56, s57
v_mfma_f32_16x16x32_bf16 v[112:115], a[112:115], a[108:111], v[112:115]
buffer_load_dwordx4 v[156:159], v7, s[12:15], s32 offen offset:3072 sc1
v_mfma_f32_16x16x32_bf16 v[116:119], a[116:119], a[108:111], v[116:119]
v_mfma_f32_16x16x32_bf16 v[120:123], a[120:123], a[108:111], v[120:123]
v_mfma_f32_16x16x32_bf16 v[124:127], a[124:127], a[108:111], v[124:127]
s_waitcnt lgkmcnt(0)
v_mfma_f32_16x16x32_bf16 v[64:67], a[80:83], a[64:67], v[64:67]
ds_read_b64_tr_b16 a[112:113], v245 offset:8192
ds_read_b64_tr_b16 a[114:115], v245 offset:10240
v_mfma_f32_16x16x32_bf16 v[68:71], a[84:87], a[64:67], v[68:71]
ds_read_b128 a[96:99], v244 offset:2048
v_add_u32 v245, s53, v245
v_mfma_f32_16x16x32_bf16 v[72:75], a[88:91], a[64:67], v[72:75]
v_mfma_f32_16x16x32_bf16 v[76:79], a[92:95], a[64:67], v[76:79]
ds_read_b64_tr_b16 a[116:117], v246 offset:8192
ds_read_b64_tr_b16 a[118:119], v246 offset:10240
v_mfma_f32_16x16x32_bf16 v[80:83], a[80:83], a[68:71], v[80:83]
ds_read_b128 a[100:103], v244 offset:2304
v_add_u32 v246, s53, v246
v_mfma_f32_16x16x32_bf16 v[84:87], a[84:87], a[68:71], v[84:87]
ds_read_b64_tr_b16 a[120:121], v247 offset:8192
ds_read_b64_tr_b16 a[122:123], v247 offset:10240
v_mfma_f32_16x16x32_bf16 v[88:91], a[88:91], a[68:71], v[88:91]
v_mfma_f32_16x16x32_bf16 v[92:95], a[92:95], a[68:71], v[92:95]
ds_read_b128 a[104:107], v244 offset:6144
v_add_u32 v247, s53, v247
v_mfma_f32_16x16x32_bf16 v[96:99], a[80:83], a[72:75], v[96:99]
ds_read_b64_tr_b16 a[124:125], v248 offset:8192
ds_read_b64_tr_b16 a[126:127], v248 offset:10240
v_mfma_f32_16x16x32_bf16 v[100:103], a[84:87], a[72:75], v[100:103]
ds_read_b128 a[108:111], v244 offset:6400
v_add_u32 v248, s53, v248
v_mfma_f32_16x16x32_bf16 v[104:107], a[88:91], a[72:75], v[104:107]
v_mfma_f32_16x16x32_bf16 v[108:111], a[92:95], a[72:75], v[108:111]
s_add_u32 s49, s46, s91
v_add_u32 v244, s53, v244
v_mfma_f32_16x16x32_bf16 v[112:115], a[80:83], a[76:79], v[112:115]
s_add_u32 s46, s46, s53
v_mfma_f32_16x16x32_bf16 v[116:119], a[84:87], a[76:79], v[116:119]
v_mfma_f32_16x16x32_bf16 v[120:123], a[88:91], a[76:79], v[120:123]
v_mfma_f32_16x16x32_bf16 v[124:127], a[92:95], a[76:79], v[124:127]
s_waitcnt vmcnt(9) lgkmcnt(0)
s_barrier
v_mfma_f32_16x16x32_bf16 v[64:67], a[112:115], a[96:99], v[64:67]
ds_read_b64_tr_b16 a[80:81], v245 offset:0
ds_read_b64_tr_b16 a[82:83], v245 offset:2048
v_mfma_f32_16x16x32_bf16 v[68:71], a[116:119], a[96:99], v[68:71]
ds_read_b128 a[64:67], v244 offset:0
ds_read_b128 a[68:71], v244 offset:256
v_mfma_f32_16x16x32_bf16 v[72:75], a[120:123], a[96:99], v[72:75]
ds_read_b64_tr_b16 a[84:85], v246 offset:0
ds_read_b64_tr_b16 a[86:87], v246 offset:2048
v_mfma_f32_16x16x32_bf16 v[76:79], a[124:127], a[96:99], v[76:79]
ds_read_b64_tr_b16 a[88:89], v247 offset:0
ds_read_b64_tr_b16 a[90:91], v247 offset:2048
v_mfma_f32_16x16x32_bf16 v[80:83], a[112:115], a[100:103], v[80:83]
ds_read_b64_tr_b16 a[92:93], v248 offset:0
ds_read_b64_tr_b16 a[94:95], v248 offset:2048
v_mfma_f32_16x16x32_bf16 v[84:87], a[116:119], a[100:103], v[84:87]
ds_read_b64_tr_b16 a[144:145], v245 offset:8192
ds_read_b64_tr_b16 a[146:147], v245 offset:10240
v_mfma_f32_16x16x32_bf16 v[88:91], a[120:123], a[100:103], v[88:91]
ds_read_b128 a[128:131], v244 offset:2048
ds_read_b128 a[132:135], v244 offset:2304
v_mfma_f32_16x16x32_bf16 v[92:95], a[124:127], a[100:103], v[92:95]
ds_read_b64_tr_b16 a[148:149], v246 offset:8192
ds_read_b64_tr_b16 a[150:151], v246 offset:10240
.Lpad20:
s_cbranch_execz .Ltch9
v_mfma_f32_16x16x32_bf16 v[96:99], a[112:115], a[104:107], v[96:99]
ds_read_b64_tr_b16 a[152:153], v247 offset:8192
ds_read_b64_tr_b16 a[154:155], v247 offset:10240
v_mfma_f32_16x16x32_bf16 v[100:103], a[116:119], a[104:107], v[100:103]
ds_read_b64_tr_b16 a[156:157], v248 offset:8192
ds_read_b64_tr_b16 a[158:159], v248 offset:10240
v_mfma_f32_16x16x32_bf16 v[104:107], a[120:123], a[104:107], v[104:107]
ds_read_b128 a[72:75], v244 offset:4096
ds_read_b128 a[76:79], v244 offset:4352
v_mfma_f32_16x16x32_bf16 v[108:111], a[124:127], a[104:107], v[108:111]
ds_read_b128 a[136:139], v244 offset:6144
ds_read_b128 a[140:143], v244 offset:6400
v_mfma_f32_16x16x32_bf16 v[112:115], a[112:115], a[108:111], v[112:115]
s_mov_b32 s24, 0
s_mov_b32 s25, 32768
v_mfma_f32_16x16x32_bf16 v[116:119], a[116:119], a[108:111], v[116:119]
s_mov_b32 s26, 65536
s_mov_b32 s27, 98304
v_mfma_f32_16x16x32_bf16 v[120:123], a[120:123], a[108:111], v[120:123]
s_mov_b32 s28, 131072
s_mov_b32 s29, 163840
v_mfma_f32_16x16x32_bf16 v[124:127], a[124:127], a[108:111], v[124:127]
s_mov_b32 s30, 196608
s_mov_b32 s31, 229376
s_waitcnt vmcnt(0) lgkmcnt(0)
v_mfma_f32_16x16x32_bf16 v[64:67], v[160:163], v[128:131], v[64:67]
v_mfma_f32_16x16x32_bf16 v[68:71], v[164:167], v[128:131], v[68:71]
v_mfma_f32_16x16x32_bf16 v[72:75], v[160:163], v[132:135], v[72:75]
v_mfma_f32_16x16x32_bf16 v[76:79], v[164:167], v[132:135], v[76:79]
v_mfma_f32_16x16x32_bf16 v[64:67], a[80:83], a[64:67], v[64:67]
v_mfma_f32_16x16x32_bf16 v[68:71], a[84:87], a[64:67], v[68:71]
v_mfma_f32_16x16x32_bf16 v[72:75], a[88:91], a[64:67], v[72:75]
v_mfma_f32_16x16x32_bf16 v[76:79], a[92:95], a[64:67], v[76:79]
v_mfma_f32_16x16x32_bf16 v[64:67], a[144:147], a[128:131], v[64:67]
v_mfma_f32_16x16x32_bf16 v[68:71], a[148:151], a[128:131], v[68:71]
v_mfma_f32_16x16x32_bf16 v[72:75], a[152:155], a[128:131], v[72:75]
v_mfma_f32_16x16x32_bf16 v[76:79], a[156:159], a[128:131], v[76:79]
v_mfma_f32_16x16x32_bf16 v[80:83], v[160:163], v[136:139], v[80:83]
v_mfma_f32_16x16x32_bf16 v[84:87], v[164:167], v[136:139], v[84:87]
v_mfma_f32_16x16x32_bf16 v[88:91], v[160:163], v[140:143], v[88:91]
s_nop 1
v_cvt_pk_bf16_f32 v192, v64, v65
v_cvt_pk_bf16_f32 v194, v68, v69
v_cvt_pk_bf16_f32 v193, v66, v67
v_mfma_f32_16x16x32_bf16 v[92:95], v[164:167], v[140:143], v[92:95]
v_cvt_pk_bf16_f32 v195, v70, v71
v_cvt_pk_bf16_f32 v196, v72, v73
v_cvt_pk_bf16_f32 v198, v76, v77
v_mfma_f32_16x16x32_bf16 v[80:83], a[80:83], a[68:71], v[80:83]
v_cvt_pk_bf16_f32 v197, v74, v75
v_cvt_pk_bf16_f32 v199, v78, v79
v_mfma_f32_16x16x32_bf16 v[84:87], a[84:87], a[68:71], v[84:87]
v_permlane16_swap_b32 v192, v194
v_mfma_f32_16x16x32_bf16 v[88:91], a[88:91], a[68:71], v[88:91]
v_permlane16_swap_b32 v193, v195
v_mfma_f32_16x16x32_bf16 v[92:95], a[92:95], a[68:71], v[92:95]
v_permlane16_swap_b32 v196, v198
v_mfma_f32_16x16x32_bf16 v[80:83], a[144:147], a[132:135], v[80:83]
v_permlane16_swap_b32 v197, v199
v_mov_b32 v200, v192
v_mfma_f32_16x16x32_bf16 v[84:87], a[148:151], a[132:135], v[84:87]
v_mov_b32 v201, v193
v_mov_b32 v202, v194
v_mov_b32 v203, v195
v_mfma_f32_16x16x32_bf16 v[88:91], a[152:155], a[132:135], v[88:91]
v_mov_b32_dpp v192, v196 row_ror:8 row_mask:0xf bank_mask:0xc
v_mov_b32_dpp v193, v197 row_ror:8 row_mask:0xf bank_mask:0xc
v_mov_b32_dpp v194, v198 row_ror:8 row_mask:0xf bank_mask:0xc
v_mfma_f32_16x16x32_bf16 v[92:95], a[156:159], a[132:135], v[92:95]
v_mov_b32_dpp v195, v199 row_ror:8 row_mask:0xf bank_mask:0xc
v_mfma_f32_16x16x32_bf16 v[96:99], v[160:163], v[144:147], v[96:99]
buffer_store_dwordx4 v[192:195], v251, s[16:19], s24 offen nt
v_mov_b32_dpp v196, v200 row_ror:8 row_mask:0xf bank_mask:0x3
v_mov_b32_dpp v197, v201 row_ror:8 row_mask:0xf bank_mask:0x3
v_mfma_f32_16x16x32_bf16 v[100:103], v[164:167], v[144:147], v[100:103]
v_mov_b32_dpp v198, v202 row_ror:8 row_mask:0xf bank_mask:0x3
v_mov_b32_dpp v199, v203 row_ror:8 row_mask:0xf bank_mask:0x3
v_mfma_f32_16x16x32_bf16 v[104:107], v[160:163], v[148:151], v[104:107]
buffer_store_dwordx4 v[196:199], v251, s[16:19], s25 offen nt
v_cvt_pk_bf16_f32 v204, v80, v81
v_cvt_pk_bf16_f32 v206, v84, v85
v_mfma_f32_16x16x32_bf16 v[108:111], v[164:167], v[148:151], v[108:111]
v_cvt_pk_bf16_f32 v205, v82, v83
v_cvt_pk_bf16_f32 v207, v86, v87
v_cvt_pk_bf16_f32 v208, v88, v89
v_mfma_f32_16x16x32_bf16 v[96:99], a[80:83], a[72:75], v[96:99]
v_cvt_pk_bf16_f32 v210, v92, v93
v_cvt_pk_bf16_f32 v209, v90, v91
v_cvt_pk_bf16_f32 v211, v94, v95
v_mfma_f32_16x16x32_bf16 v[100:103], a[84:87], a[72:75], v[100:103]
v_permlane16_swap_b32 v204, v206
v_mfma_f32_16x16x32_bf16 v[104:107], a[88:91], a[72:75], v[104:107]
v_permlane16_swap_b32 v205, v207
v_mfma_f32_16x16x32_bf16 v[108:111], a[92:95], a[72:75], v[108:111]
v_permlane16_swap_b32 v208, v210
v_mfma_f32_16x16x32_bf16 v[96:99], a[144:147], a[136:139], v[96:99]
v_permlane16_swap_b32 v209, v211
v_mov_b32 v212, v204
v_mfma_f32_16x16x32_bf16 v[100:103], a[148:151], a[136:139], v[100:103]
v_mov_b32 v213, v205
v_mov_b32 v214, v206
v_mov_b32 v215, v207
v_mfma_f32_16x16x32_bf16 v[104:107], a[152:155], a[136:139], v[104:107]
v_mov_b32_dpp v204, v208 row_ror:8 row_mask:0xf bank_mask:0xc
v_mov_b32_dpp v205, v209 row_ror:8 row_mask:0xf bank_mask:0xc
v_mov_b32_dpp v206, v210 row_ror:8 row_mask:0xf bank_mask:0xc
v_mfma_f32_16x16x32_bf16 v[108:111], a[156:159], a[136:139], v[108:111]
v_mov_b32_dpp v207, v211 row_ror:8 row_mask:0xf bank_mask:0xc
v_mfma_f32_16x16x32_bf16 v[112:115], v[160:163], v[152:155], v[112:115]
buffer_store_dwordx4 v[204:207], v251, s[16:19], s26 offen nt
v_mov_b32_dpp v208, v212 row_ror:8 row_mask:0xf bank_mask:0x3
v_mov_b32_dpp v209, v213 row_ror:8 row_mask:0xf bank_mask:0x3
v_mfma_f32_16x16x32_bf16 v[116:119], v[164:167], v[152:155], v[116:119]
v_mov_b32_dpp v210, v214 row_ror:8 row_mask:0xf bank_mask:0x3
v_mov_b32_dpp v211, v215 row_ror:8 row_mask:0xf bank_mask:0x3
v_mfma_f32_16x16x32_bf16 v[120:123], v[160:163], v[156:159], v[120:123]
buffer_store_dwordx4 v[208:211], v251, s[16:19], s27 offen nt
v_cvt_pk_bf16_f32 v192, v96, v97
v_cvt_pk_bf16_f32 v194, v100, v101
v_mfma_f32_16x16x32_bf16 v[124:127], v[164:167], v[156:159], v[124:127]
v_cvt_pk_bf16_f32 v193, v98, v99
v_cvt_pk_bf16_f32 v195, v102, v103
v_cvt_pk_bf16_f32 v196, v104, v105
v_mfma_f32_16x16x32_bf16 v[112:115], a[80:83], a[76:79], v[112:115]
v_cvt_pk_bf16_f32 v198, v108, v109
v_cvt_pk_bf16_f32 v197, v106, v107
v_cvt_pk_bf16_f32 v199, v110, v111
v_mfma_f32_16x16x32_bf16 v[116:119], a[84:87], a[76:79], v[116:119]
v_permlane16_swap_b32 v192, v194
v_mfma_f32_16x16x32_bf16 v[120:123], a[88:91], a[76:79], v[120:123]
v_permlane16_swap_b32 v193, v195
v_mfma_f32_16x16x32_bf16 v[124:127], a[92:95], a[76:79], v[124:127]
v_permlane16_swap_b32 v196, v198
v_mfma_f32_16x16x32_bf16 v[112:115], a[144:147], a[140:143], v[112:115]
v_permlane16_swap_b32 v197, v199
v_mov_b32 v200, v192
v_mfma_f32_16x16x32_bf16 v[116:119], a[148:151], a[140:143], v[116:119]
v_mov_b32 v201, v193
v_mov_b32 v202, v194
v_mov_b32 v203, v195
v_mfma_f32_16x16x32_bf16 v[120:123], a[152:155], a[140:143], v[120:123]
v_mov_b32_dpp v192, v196 row_ror:8 row_mask:0xf bank_mask:0xc
v_mov_b32_dpp v193, v197 row_ror:8 row_mask:0xf bank_mask:0xc
v_mov_b32_dpp v194, v198 row_ror:8 row_mask:0xf bank_mask:0xc
v_mfma_f32_16x16x32_bf16 v[124:127], a[156:159], a[140:143], v[124:127]
v_mov_b32_dpp v195, v199 row_ror:8 row_mask:0xf bank_mask:0xc
buffer_store_dwordx4 v[192:195], v251, s[16:19], s28 offen nt
v_mov_b32_dpp v196, v200 row_ror:8 row_mask:0xf bank_mask:0x3
v_mov_b32_dpp v197, v201 row_ror:8 row_mask:0xf bank_mask:0x3
v_mov_b32_dpp v198, v202 row_ror:8 row_mask:0xf bank_mask:0x3
v_mov_b32_dpp v199, v203 row_ror:8 row_mask:0xf bank_mask:0x3
buffer_store_dwordx4 v[196:199], v251, s[16:19], s29 offen nt
v_cvt_pk_bf16_f32 v204, v112, v113
v_cvt_pk_bf16_f32 v206, v116, v117
v_cvt_pk_bf16_f32 v205, v114, v115
v_cvt_pk_bf16_f32 v207, v118, v119
v_cvt_pk_bf16_f32 v208, v120, v121
v_cvt_pk_bf16_f32 v210, v124, v125
v_cvt_pk_bf16_f32 v209, v122, v123
v_cvt_pk_bf16_f32 v211, v126, v127
v_permlane16_swap_b32 v204, v206
v_permlane16_swap_b32 v205, v207
v_permlane16_swap_b32 v208, v210
v_permlane16_swap_b32 v209, v211
v_mov_b32 v212, v204
v_mov_b32 v213, v205
v_mov_b32 v214, v206
v_mov_b32 v215, v207
v_mov_b32_dpp v204, v208 row_ror:8 row_mask:0xf bank_mask:0xc
v_mov_b32_dpp v205, v209 row_ror:8 row_mask:0xf bank_mask:0xc
v_mov_b32_dpp v206, v210 row_ror:8 row_mask:0xf bank_mask:0xc
v_mov_b32_dpp v207, v211 row_ror:8 row_mask:0xf bank_mask:0xc
buffer_store_dwordx4 v[204:207], v251, s[16:19], s30 offen nt
v_mov_b32_dpp v208, v212 row_ror:8 row_mask:0xf bank_mask:0x3
v_mov_b32_dpp v209, v213 row_ror:8 row_mask:0xf bank_mask:0x3
v_mov_b32_dpp v210, v214 row_ror:8 row_mask:0xf bank_mask:0x3
v_mov_b32_dpp v211, v215 row_ror:8 row_mask:0xf bank_mask:0x3
buffer_store_dwordx4 v[208:211], v251, s[16:19], s31 offen nt
s_branch .Lend
.L5odd_pad:
s_cbranch_execz .Ltch9
.L5odd:
v_mfma_f32_16x16x32_bf16 v[64:67], a[112:115], a[96:99], v[64:67]
ds_read_b64_tr_b16 a[80:81], v245 offset:0
ds_read_b64_tr_b16 a[82:83], v245 offset:2048
v_mfma_f32_16x16x32_bf16 v[68:71], a[116:119], a[96:99], v[68:71]
s_add_u32 m0, s49, 16384
ds_read_b128 a[64:67], v244 offset:0
v_mfma_f32_16x16x32_bf16 v[72:75], a[120:123], a[96:99], v[72:75]
buffer_load_dwordx4 v240, s[8:11], s45 offen offset:0 lds
v_mfma_f32_16x16x32_bf16 v[76:79], a[124:127], a[96:99], v[76:79]
ds_read_b64_tr_b16 a[84:85], v246 offset:0
ds_read_b64_tr_b16 a[86:87], v246 offset:2048
v_mfma_f32_16x16x32_bf16 v[80:83], a[112:115], a[100:103], v[80:83]
s_add_u32 s44, s44, 4096
ds_read_b128 a[68:71], v244 offset:256
v_mfma_f32_16x16x32_bf16 v[84:87], a[116:119], a[100:103], v[84:87]
ds_read_b64_tr_b16 a[88:89], v247 offset:0
ds_read_b64_tr_b16 a[90:91], v247 offset:2048
v_mfma_f32_16x16x32_bf16 v[88:91], a[120:123], a[100:103], v[88:91]
buffer_load_dwordx4 v241, s[8:11], s45 offen offset:1024 lds
v_mfma_f32_16x16x32_bf16 v[92:95], a[124:127], a[100:103], v[92:95]
buffer_load_dword v250, v249, s[60:63], s90 offen sc1
ds_read_b128 a[72:75], v244 offset:4096
v_mfma_f32_16x16x32_bf16 v[96:99], a[112:115], a[104:107], v[96:99]
ds_read_b64_tr_b16 a[92:93], v248 offset:0
ds_read_b64_tr_b16 a[94:95], v248 offset:2048
v_mfma_f32_16x16x32_bf16 v[100:103], a[116:119], a[104:107], v[100:103]
ds_read_b128 a[76:79], v244 offset:4352
v_mfma_f32_16x16x32_bf16 v[104:107], a[120:123], a[104:107], v[104:107]
buffer_load_dwordx4 v242, s[8:11], s45 offen offset:2048 lds
v_mfma_f32_16x16x32_bf16 v[108:111], a[124:127], a[104:107], v[108:111]
s_cmp_eq_u32 s46, 0
s_cselect_b32 s53, s56, s57
v_mfma_f32_16x16x32_bf16 v[112:115], a[112:115], a[108:111], v[112:115]
v_mfma_f32_16x16x32_bf16 v[116:119], a[116:119], a[108:111], v[116:119]
v_mfma_f32_16x16x32_bf16 v[120:123], a[120:123], a[108:111], v[120:123]
buffer_load_dwordx4 v243, s[8:11], s45 offen offset:3072 lds
v_mfma_f32_16x16x32_bf16 v[124:127], a[124:127], a[108:111], v[124:127]
s_waitcnt lgkmcnt(0)
s_mov_b32 m0, s49
s_add_u32 s45, s45, 131072
v_mfma_f32_16x16x32_bf16 v[64:67], a[80:83], a[64:67], v[64:67]
ds_read_b64_tr_b16 a[112:113], v245 offset:8192
ds_read_b64_tr_b16 a[114:115], v245 offset:10240
v_mfma_f32_16x16x32_bf16 v[68:71], a[84:87], a[64:67], v[68:71]
ds_read_b128 a[96:99], v244 offset:2048
v_add_u32 v245, s53, v245
v_mfma_f32_16x16x32_bf16 v[72:75], a[88:91], a[64:67], v[72:75]
buffer_load_dwordx4 v7, s[4:7], s44 offen offset:0 sc1 lds
v_mfma_f32_16x16x32_bf16 v[76:79], a[92:95], a[64:67], v[76:79]
ds_read_b64_tr_b16 a[116:117], v246 offset:8192
ds_read_b64_tr_b16 a[118:119], v246 offset:10240
v_mfma_f32_16x16x32_bf16 v[80:83], a[80:83], a[68:71], v[80:83]
ds_read_b128 a[100:103], v244 offset:2304
v_add_u32 v246, s53, v246
v_mfma_f32_16x16x32_bf16 v[84:87], a[84:87], a[68:71], v[84:87]
ds_read_b64_tr_b16 a[120:121], v247 offset:8192
ds_read_b64_tr_b16 a[122:123], v247 offset:10240
v_mfma_f32_16x16x32_bf16 v[88:91], a[88:91], a[68:71], v[88:91]
buffer_load_dwordx4 v7, s[4:7], s44 offen offset:1024 sc1 lds
v_mfma_f32_16x16x32_bf16 v[92:95], a[92:95], a[68:71], v[92:95]
ds_read_b128 a[104:107], v244 offset:6144
v_add_u32 v247, s53, v247
v_mfma_f32_16x16x32_bf16 v[96:99], a[80:83], a[72:75], v[96:99]
ds_read_b64_tr_b16 a[124:125], v248 offset:8192
ds_read_b64_tr_b16 a[126:127], v248 offset:10240
v_mfma_f32_16x16x32_bf16 v[100:103], a[84:87], a[72:75], v[100:103]
ds_read_b128 a[108:111], v244 offset:6400
v_add_u32 v248, s53, v248
v_mfma_f32_16x16x32_bf16 v[104:107], a[88:91], a[72:75], v[104:107]
buffer_load_dwordx4 v7, s[4:7], s44 offen offset:2048 sc1 lds
v_mfma_f32_16x16x32_bf16 v[108:111], a[92:95], a[72:75], v[108:111]
s_add_u32 s49, s46, s91
v_add_u32 v244, s53, v244
v_mfma_f32_16x16x32_bf16 v[112:115], a[80:83], a[76:79], v[112:115]
s_add_u32 s46, s46, s53
v_mfma_f32_16x16x32_bf16 v[116:119], a[84:87], a[76:79], v[116:119]
v_mfma_f32_16x16x32_bf16 v[120:123], a[88:91], a[76:79], v[120:123]
buffer_load_dwordx4 v7, s[4:7], s44 offen offset:3072 sc1 lds
v_mfma_f32_16x16x32_bf16 v[124:127], a[92:95], a[76:79], v[124:127]
s_waitcnt vmcnt(25) lgkmcnt(0)
s_barrier
v_mfma_f32_16x16x32_bf16 v[64:67], a[112:115], a[96:99], v[64:67]
ds_read_b64_tr_b16 a[80:81], v245 offset:0
ds_read_b64_tr_b16 a[82:83], v245 offset:2048
v_mfma_f32_16x16x32_bf16 v[68:71], a[116:119], a[96:99], v[68:71]
s_add_u32 m0, s49, 16384
ds_read_b128 a[64:67], v244 offset:0
v_mfma_f32_16x16x32_bf16 v[72:75], a[120:123], a[96:99], v[72:75]
buffer_load_dwordx4 v240, s[8:11], s45 offen offset:0 lds
v_mfma_f32_16x16x32_bf16 v[76:79], a[124:127], a[96:99], v[76:79]
ds_read_b64_tr_b16 a[84:85], v246 offset:0
ds_read_b64_tr_b16 a[86:87], v246 offset:2048
v_mfma_f32_16x16x32_bf16 v[80:83], a[112:115], a[100:103], v[80:83]
s_add_u32 s44, s44, 4096
ds_read_b128 a[68:71], v244 offset:256
v_mfma_f32_16x16x32_bf16 v[84:87], a[116:119], a[100:103], v[84:87]
ds_read_b64_tr_b16 a[88:89], v247 offset:0
ds_read_b64_tr_b16 a[90:91], v247 offset:2048
v_mfma_f32_16x16x32_bf16 v[88:91], a[120:123], a[100:103], v[88:91]
buffer_load_dwordx4 v241, s[8:11], s45 offen offset:1024 lds
v_mfma_f32_16x16x32_bf16 v[92:95], a[124:127], a[100:103], v[92:95]
ds_read_b128 a[72:75], v244 offset:4096
v_mfma_f32_16x16x32_bf16 v[96:99], a[112:115], a[104:107], v[96:99]
ds_read_b64_tr_b16 a[92:93], v248 offset:0
ds_read_b64_tr_b16 a[94:95], v248 offset:2048
v_mfma_f32_16x16x32_bf16 v[100:103], a[116:119], a[104:107], v[100:103]
ds_read_b128 a[76:79], v244 offset:4352
v_mfma_f32_16x16x32_bf16 v[104:107], a[120:123], a[104:107], v[104:107]
buffer_load_dwordx4 v242, s[8:11], s45 offen offset:2048 lds
v_mfma_f32_16x16x32_bf16 v[108:111], a[124:127], a[104:107], v[108:111]
s_cmp_eq_u32 s46, 0
s_cselect_b32 s53, s56, s57
v_mfma_f32_16x16x32_bf16 v[112:115], a[112:115], a[108:111], v[112:115]
v_mfma_f32_16x16x32_bf16 v[116:119], a[116:119], a[108:111], v[116:119]
v_mfma_f32_16x16x32_bf16 v[120:123], a[120:123], a[108:111], v[120:123]
buffer_load_dwordx4 v243, s[8:11], s45 offen offset:3072 lds
v_mfma_f32_16x16x32_bf16 v[124:127], a[124:127], a[108:111], v[124:127]
s_waitcnt lgkmcnt(0)
s_mov_b32 m0, s49
s_add_u32 s45, s45, 131072
v_mfma_f32_16x16x32_bf16 v[64:67], a[80:83], a[64:67], v[64:67]
ds_read_b64_tr_b16 a[112:113], v245 offset:8192
ds_read_b64_tr_b16 a[114:115], v245 offset:10240
v_mfma_f32_16x16x32_bf16 v[68:71], a[84:87], a[64:67], v[68:71]
ds_read_b128 a[96:99], v244 offset:2048
v_add_u32 v245, s53, v245
v_mfma_f32_16x16x32_bf16 v[72:75], a[88:91], a[64:67], v[72:75]
buffer_load_dwordx4 v7, s[4:7], s44 offen offset:0 sc1 lds
v_mfma_f32_16x16x32_bf16 v[76:79], a[92:95], a[64:67], v[76:79]
ds_read_b64_tr_b16 a[116:117], v246 offset:8192
ds_read_b64_tr_b16 a[118:119], v246 offset:10240
v_mfma_f32_16x16x32_bf16 v[80:83], a[80:83], a[68:71], v[80:83]
ds_read_b128 a[100:103], v244 offset:2304
v_add_u32 v246, s53, v246
v_mfma_f32_16x16x32_bf16 v[84:87], a[84:87], a[68:71], v[84:87]
ds_read_b64_tr_b16 a[120:121], v247 offset:8192
ds_read_b64_tr_b16 a[122:123], v247 offset:10240
v_mfma_f32_16x16x32_bf16 v[88:91], a[88:91], a[68:71], v[88:91]
buffer_load_dwordx4 v7, s[4:7], s44 offen offset:1024 sc1 lds
v_mfma_f32_16x16x32_bf16 v[92:95], a[92:95], a[68:71], v[92:95]
ds_read_b128 a[104:107], v244 offset:6144
v_add_u32 v247, s53, v247
v_mfma_f32_16x16x32_bf16 v[96:99], a[80:83], a[72:75], v[96:99]
ds_read_b64_tr_b16 a[124:125], v248 offset:8192
ds_read_b64_tr_b16 a[126:127], v248 offset:10240
v_mfma_f32_16x16x32_bf16 v[100:103], a[84:87], a[72:75], v[100:103]
ds_read_b128 a[108:111], v244 offset:6400
v_add_u32 v248, s53, v248
v_mfma_f32_16x16x32_bf16 v[104:107], a[88:91], a[72:75], v[104:107]
buffer_load_dwordx4 v7, s[4:7], s44 offen offset:2048 sc1 lds
v_mfma_f32_16x16x32_bf16 v[108:111], a[92:95], a[72:75], v[108:111]
s_add_u32 s49, s46, s91
v_add_u32 v244, s53, v244
v_mfma_f32_16x16x32_bf16 v[112:115], a[80:83], a[76:79], v[112:115]
s_add_u32 s46, s46, s53
v_mfma_f32_16x16x32_bf16 v[116:119], a[84:87], a[76:79], v[116:119]
v_mfma_f32_16x16x32_bf16 v[120:123], a[88:91], a[76:79], v[120:123]
buffer_load_dwordx4 v7, s[4:7], s44 offen offset:3072 sc1 lds
v_mfma_f32_16x16x32_bf16 v[124:127], a[92:95], a[76:79], v[124:127]
s_waitcnt vmcnt(25) lgkmcnt(0)
s_barrier
s_lshl_b32 s54, s51, 2
s_add_u32 s54, s54, s3
s_lshl_b32 s54, s54, 16
s_add_u32 s54, s54, 50331648
s_add_u32 s4, s84, s54
s_addc_u32 s5, s85, 0
s_and_b32 s5, s5, 0xffff
s_mov_b32 s6, 65536
s_lshl_b32 s54, s3, 15
s_add_u32 s54, s54, s52
s_sub_u32 s28, 0x200000, s54
s_add_u32 s8, s68, s54
s_addc_u32 s9, s69, 0
s_and_b32 s9, s9, 0xffff
s_mov_b32 s10, s28
s_mov_b32 s44, -4096
s_mov_b32 s45, 0
v_mfma_f32_16x16x32_bf16 v[64:67], a[112:115], a[96:99], v[64:67]
ds_read_b64_tr_b16 a[80:81], v245 offset:0
ds_read_b64_tr_b16 a[82:83], v245 offset:2048
v_mfma_f32_16x16x32_bf16 v[68:71], a[116:119], a[96:99], v[68:71]
s_add_u32 m0, s49, 16384
ds_read_b128 a[64:67], v244 offset:0
v_mfma_f32_16x16x32_bf16 v[72:75], a[120:123], a[96:99], v[72:75]
buffer_load_dwordx4 v240, s[8:11], s45 offen offset:0 lds
v_mfma_f32_16x16x32_bf16 v[76:79], a[124:127], a[96:99], v[76:79]
ds_read_b64_tr_b16 a[84:85], v246 offset:0
ds_read_b64_tr_b16 a[86:87], v246 offset:2048
v_mfma_f32_16x16x32_bf16 v[80:83], a[112:115], a[100:103], v[80:83]
s_add_u32 s44, s44, 4096
ds_read_b128 a[68:71], v244 offset:256
v_mfma_f32_16x16x32_bf16 v[84:87], a[116:119], a[100:103], v[84:87]
ds_read_b64_tr_b16 a[88:89], v247 offset:0
ds_read_b64_tr_b16 a[90:91], v247 offset:2048
v_mfma_f32_16x16x32_bf16 v[88:91], a[120:123], a[100:103], v[88:91]
buffer_load_dwordx4 v241, s[8:11], s45 offen offset:1024 lds
v_mfma_f32_16x16x32_bf16 v[92:95], a[124:127], a[100:103], v[92:95]
ds_read_b128 a[72:75], v244 offset:4096
v_mfma_f32_16x16x32_bf16 v[96:99], a[112:115], a[104:107], v[96:99]
ds_read_b64_tr_b16 a[92:93], v248 offset:0
ds_read_b64_tr_b16 a[94:95], v248 offset:2048
v_mfma_f32_16x16x32_bf16 v[100:103], a[116:119], a[104:107], v[100:103]
ds_read_b128 a[76:79], v244 offset:4352
v_mfma_f32_16x16x32_bf16 v[104:107], a[120:123], a[104:107], v[104:107]
s_waitcnt vmcnt(16)
buffer_load_dwordx4 v242, s[8:11], s45 offen offset:2048 lds
v_and_b32 v250, 7, v250
v_mfma_f32_16x16x32_bf16 v[108:111], a[124:127], a[104:107], v[108:111]
s_cmp_eq_u32 s46, 0
s_cselect_b32 s53, s56, s57
v_cmp_gt_u32 vcc, 4, v250
v_mfma_f32_16x16x32_bf16 v[112:115], a[112:115], a[108:111], v[112:115]
v_mfma_f32_16x16x32_bf16 v[116:119], a[116:119], a[108:111], v[116:119]
s_cmp_lg_u64 vcc, 0
s_cbranch_scc1 .Lspin4
.Lspin4_ret:
v_mfma_f32_16x16x32_bf16 v[120:123], a[120:123], a[108:111], v[120:123]
buffer_load_dwordx4 v243, s[8:11], s45 offen offset:3072 lds
v_mfma_f32_16x16x32_bf16 v[124:127], a[124:127], a[108:111], v[124:127]
s_waitcnt lgkmcnt(0)
s_mov_b32 m0, s49
s_add_u32 s45, s45, 131072
v_mfma_f32_16x16x32_bf16 v[64:67], a[80:83], a[64:67], v[64:67]
ds_read_b64_tr_b16 a[112:113], v245 offset:8192
ds_read_b64_tr_b16 a[114:115], v245 offset:10240
v_mfma_f32_16x16x32_bf16 v[68:71], a[84:87], a[64:67], v[68:71]
ds_read_b128 a[96:99], v244 offset:2048
v_add_u32 v245, s53, v245
v_mfma_f32_16x16x32_bf16 v[72:75], a[88:91], a[64:67], v[72:75]
buffer_load_dwordx4 v7, s[4:7], s44 offen offset:0 sc1 lds
v_mfma_f32_16x16x32_bf16 v[76:79], a[92:95], a[64:67], v[76:79]
ds_read_b64_tr_b16 a[116:117], v246 offset:8192
ds_read_b64_tr_b16 a[118:119], v246 offset:10240
v_mfma_f32_16x16x32_bf16 v[80:83], a[80:83], a[68:71], v[80:83]
ds_read_b128 a[100:103], v244 offset:2304
v_add_u32 v246, s53, v246
v_mfma_f32_16x16x32_bf16 v[84:87], a[84:87], a[68:71], v[84:87]
ds_read_b64_tr_b16 a[120:121], v247 offset:8192
ds_read_b64_tr_b16 a[122:123], v247 offset:10240
v_mfma_f32_16x16x32_bf16 v[88:91], a[88:91], a[68:71], v[88:91]
buffer_load_dwordx4 v7, s[4:7], s44 offen offset:1024 sc1 lds
v_mfma_f32_16x16x32_bf16 v[92:95], a[92:95], a[68:71], v[92:95]
ds_read_b128 a[104:107], v244 offset:6144
v_add_u32 v247, s53, v247
v_mfma_f32_16x16x32_bf16 v[96:99], a[80:83], a[72:75], v[96:99]
ds_read_b64_tr_b16 a[124:125], v248 offset:8192
ds_read_b64_tr_b16 a[126:127], v248 offset:10240
v_mfma_f32_16x16x32_bf16 v[100:103], a[84:87], a[72:75], v[100:103]
ds_read_b128 a[108:111], v244 offset:6400
v_add_u32 v248, s53, v248
v_mfma_f32_16x16x32_bf16 v[104:107], a[88:91], a[72:75], v[104:107]
buffer_load_dwordx4 v7, s[4:7], s44 offen offset:2048 sc1 lds
v_mfma_f32_16x16x32_bf16 v[108:111], a[92:95], a[72:75], v[108:111]
s_add_u32 s49, s46, s91
v_add_u32 v244, s53, v244
v_mfma_f32_16x16x32_bf16 v[112:115], a[80:83], a[76:79], v[112:115]
s_add_u32 s46, s46, s53
v_mfma_f32_16x16x32_bf16 v[116:119], a[84:87], a[76:79], v[116:119]
v_mfma_f32_16x16x32_bf16 v[120:123], a[88:91], a[76:79], v[120:123]
buffer_load_dwordx4 v7, s[4:7], s44 offen offset:3072 sc1 lds
v_mfma_f32_16x16x32_bf16 v[124:127], a[92:95], a[76:79], v[124:127]
s_waitcnt vmcnt(25) lgkmcnt(0)
s_barrier
v_mfma_f32_16x16x32_bf16 v[64:67], a[112:115], a[96:99], v[64:67]
ds_read_b64_tr_b16 a[80:81], v245 offset:0
ds_read_b64_tr_b16 a[82:83], v245 offset:2048
v_mfma_f32_16x16x32_bf16 v[68:71], a[116:119], a[96:99], v[68:71]
s_add_u32 m0, s49, 16384
ds_read_b128 a[64:67], v244 offset:0
v_mfma_f32_16x16x32_bf16 v[72:75], a[120:123], a[96:99], v[72:75]
buffer_load_dwordx4 v240, s[8:11], s45 offen offset:0 lds
v_mfma_f32_16x16x32_bf16 v[76:79], a[124:127], a[96:99], v[76:79]
ds_read_b64_tr_b16 a[84:85], v246 offset:0
ds_read_b64_tr_b16 a[86:87], v246 offset:2048
v_mfma_f32_16x16x32_bf16 v[80:83], a[112:115], a[100:103], v[80:83]
s_add_u32 s44, s44, 4096
ds_read_b128 a[68:71], v244 offset:256
v_mfma_f32_16x16x32_bf16 v[84:87], a[116:119], a[100:103], v[84:87]
ds_read_b64_tr_b16 a[88:89], v247 offset:0
ds_read_b64_tr_b16 a[90:91], v247 offset:2048
v_mfma_f32_16x16x32_bf16 v[88:91], a[120:123], a[100:103], v[88:91]
buffer_load_dwordx4 v241, s[8:11], s45 offen offset:1024 lds
v_mfma_f32_16x16x32_bf16 v[92:95], a[124:127], a[100:103], v[92:95]
ds_read_b128 a[72:75], v244 offset:4096
v_mfma_f32_16x16x32_bf16 v[96:99], a[112:115], a[104:107], v[96:99]
ds_read_b64_tr_b16 a[92:93], v248 offset:0
ds_read_b64_tr_b16 a[94:95], v248 offset:2048
v_mfma_f32_16x16x32_bf16 v[100:103], a[116:119], a[104:107], v[100:103]
ds_read_b128 a[76:79], v244 offset:4352
v_mfma_f32_16x16x32_bf16 v[104:107], a[120:123], a[104:107], v[104:107]
buffer_load_dwordx4 v242, s[8:11], s45 offen offset:2048 lds
v_mfma_f32_16x16x32_bf16 v[108:111], a[124:127], a[104:107], v[108:111]
s_cmp_eq_u32 s46, 0
s_cselect_b32 s53, s56, s57
v_mfma_f32_16x16x32_bf16 v[112:115], a[112:115], a[108:111], v[112:115]
v_mfma_f32_16x16x32_bf16 v[116:119], a[116:119], a[108:111], v[116:119]
v_mfma_f32_16x16x32_bf16 v[120:123], a[120:123], a[108:111], v[120:123]
buffer_load_dwordx4 v243, s[8:11], s45 offen offset:3072 lds
v_mfma_f32_16x16x32_bf16 v[124:127], a[124:127], a[108:111], v[124:127]
s_waitcnt lgkmcnt(0)
s_mov_b32 m0, s49
s_add_u32 s45, s45, 131072
v_mfma_f32_16x16x32_bf16 v[64:67], a[80:83], a[64:67], v[64:67]
ds_read_b64_tr_b16 a[112:113], v245 offset:8192
ds_read_b64_tr_b16 a[114:115], v245 offset:10240
v_mfma_f32_16x16x32_bf16 v[68:71], a[84:87], a[64:67], v[68:71]
ds_read_b128 a[96:99], v244 offset:2048
v_add_u32 v245, s53, v245
v_mfma_f32_16x16x32_bf16 v[72:75], a[88:91], a[64:67], v[72:75]
buffer_load_dwordx4 v7, s[4:7], s44 offen offset:0 sc1 lds
v_mfma_f32_16x16x32_bf16 v[76:79], a[92:95], a[64:67], v[76:79]
ds_read_b64_tr_b16 a[116:117], v246 offset:8192
ds_read_b64_tr_b16 a[118:119], v246 offset:10240
v_mfma_f32_16x16x32_bf16 v[80:83], a[80:83], a[68:71], v[80:83]
ds_read_b128 a[100:103], v244 offset:2304
v_add_u32 v246, s53, v246
v_mfma_f32_16x16x32_bf16 v[84:87], a[84:87], a[68:71], v[84:87]
ds_read_b64_tr_b16 a[120:121], v247 offset:8192
ds_read_b64_tr_b16 a[122:123], v247 offset:10240
v_mfma_f32_16x16x32_bf16 v[88:91], a[88:91], a[68:71], v[88:91]
buffer_load_dwordx4 v7, s[4:7], s44 offen offset:1024 sc1 lds
v_mfma_f32_16x16x32_bf16 v[92:95], a[92:95], a[68:71], v[92:95]
ds_read_b128 a[104:107], v244 offset:6144
v_add_u32 v247, s53, v247
v_mfma_f32_16x16x32_bf16 v[96:99], a[80:83], a[72:75], v[96:99]
ds_read_b64_tr_b16 a[124:125], v248 offset:8192
ds_read_b64_tr_b16 a[126:127], v248 offset:10240
v_mfma_f32_16x16x32_bf16 v[100:103], a[84:87], a[72:75], v[100:103]
ds_read_b128 a[108:111], v244 offset:6400
v_add_u32 v248, s53, v248
v_mfma_f32_16x16x32_bf16 v[104:107], a[88:91], a[72:75], v[104:107]
buffer_load_dwordx4 v7, s[4:7], s44 offen offset:2048 sc1 lds
v_mfma_f32_16x16x32_bf16 v[108:111], a[92:95], a[72:75], v[108:111]
s_add_u32 s49, s46, s91
v_add_u32 v244, s53, v244
v_mfma_f32_16x16x32_bf16 v[112:115], a[80:83], a[76:79], v[112:115]
s_add_u32 s46, s46, s53
v_mfma_f32_16x16x32_bf16 v[116:119], a[84:87], a[76:79], v[116:119]
v_mfma_f32_16x16x32_bf16 v[120:123], a[88:91], a[76:79], v[120:123]
buffer_load_dwordx4 v7, s[4:7], s44 offen offset:3072 sc1 lds
v_mfma_f32_16x16x32_bf16 v[124:127], a[92:95], a[76:79], v[124:127]
s_waitcnt vmcnt(24) lgkmcnt(0)
s_barrier
v_mfma_f32_16x16x32_bf16 v[64:67], a[112:115], a[96:99], v[64:67]
ds_read_b64_tr_b16 a[80:81], v245 offset:0
ds_read_b64_tr_b16 a[82:83], v245 offset:2048
v_mfma_f32_16x16x32_bf16 v[68:71], a[116:119], a[96:99], v[68:71]
s_add_u32 m0, s49, 16384
ds_read_b128 a[64:67], v244 offset:0
v_mfma_f32_16x16x32_bf16 v[72:75], a[120:123], a[96:99], v[72:75]
buffer_load_dwordx4 v240, s[8:11], s45 offen offset:0 lds
v_mfma_f32_16x16x32_bf16 v[76:79], a[124:127], a[96:99], v[76:79]
ds_read_b64_tr_b16 a[84:85], v246 offset:0
ds_read_b64_tr_b16 a[86:87], v246 offset:2048
v_mfma_f32_16x16x32_bf16 v[80:83], a[112:115], a[100:103], v[80:83]
s_add_u32 s44, s44, 4096
ds_read_b128 a[68:71], v244 offset:256
v_mfma_f32_16x16x32_bf16 v[84:87], a[116:119], a[100:103], v[84:87]
ds_read_b64_tr_b16 a[88:89], v247 offset:0
ds_read_b64_tr_b16 a[90:91], v247 offset:2048
v_mfma_f32_16x16x32_bf16 v[88:91], a[120:123], a[100:103], v[88:91]
buffer_load_dwordx4 v241, s[8:11], s45 offen offset:1024 lds
v_mfma_f32_16x16x32_bf16 v[92:95], a[124:127], a[100:103], v[92:95]
ds_read_b128 a[72:75], v244 offset:4096
v_mfma_f32_16x16x32_bf16 v[96:99], a[112:115], a[104:107], v[96:99]
ds_read_b64_tr_b16 a[92:93], v248 offset:0
ds_read_b64_tr_b16 a[94:95], v248 offset:2048
v_mfma_f32_16x16x32_bf16 v[100:103], a[116:119], a[104:107], v[100:103]
ds_read_b128 a[76:79], v244 offset:4352
v_mfma_f32_16x16x32_bf16 v[104:107], a[120:123], a[104:107], v[104:107]
buffer_load_dwordx4 v242, s[8:11], s45 offen offset:2048 lds
v_mfma_f32_16x16x32_bf16 v[108:111], a[124:127], a[104:107], v[108:111]
s_cmp_eq_u32 s46, 0
s_cselect_b32 s53, s56, s57
v_mfma_f32_16x16x32_bf16 v[112:115], a[112:115], a[108:111], v[112:115]
v_mfma_f32_16x16x32_bf16 v[116:119], a[116:119], a[108:111], v[116:119]
v_mfma_f32_16x16x32_bf16 v[120:123], a[120:123], a[108:111], v[120:123]
buffer_load_dwordx4 v243, s[8:11], s45 offen offset:3072 lds
v_mfma_f32_16x16x32_bf16 v[124:127], a[124:127], a[108:111], v[124:127]
s_waitcnt lgkmcnt(0)
s_mov_b32 m0, s49
s_add_u32 s45, s45, 131072
v_mfma_f32_16x16x32_bf16 v[64:67], a[80:83], a[64:67], v[64:67]
ds_read_b64_tr_b16 a[112:113], v245 offset:8192
ds_read_b64_tr_b16 a[114:115], v245 offset:10240
v_mfma_f32_16x16x32_bf16 v[68:71], a[84:87], a[64:67], v[68:71]
ds_read_b128 a[96:99], v244 offset:2048
.Lpad21:
s_cbranch_execz .Ltch9
v_add_u32 v245, s53, v245
v_mfma_f32_16x16x32_bf16 v[72:75], a[88:91], a[64:67], v[72:75]
buffer_load_dwordx4 v7, s[4:7], s44 offen offset:0 sc1 lds
v_mfma_f32_16x16x32_bf16 v[76:79], a[92:95], a[64:67], v[76:79]
ds_read_b64_tr_b16 a[116:117], v246 offset:8192
ds_read_b64_tr_b16 a[118:119], v246 offset:10240
v_mfma_f32_16x16x32_bf16 v[80:83], a[80:83], a[68:71], v[80:83]
ds_read_b128 a[100:103], v244 offset:2304
v_add_u32 v246, s53, v246
v_mfma_f32_16x16x32_bf16 v[84:87], a[84:87], a[68:71], v[84:87]
ds_read_b64_tr_b16 a[120:121], v247 offset:8192
ds_read_b64_tr_b16 a[122:123], v247 offset:10240
v_mfma_f32_16x16x32_bf16 v[88:91], a[88:91], a[68:71], v[88:91]
buffer_load_dwordx4 v7, s[4:7], s44 offen offset:1024 sc1 lds
v_mfma_f32_16x16x32_bf16 v[92:95], a[92:95], a[68:71], v[92:95]
ds_read_b128 a[104:107], v244 offset:6144
v_add_u32 v247, s53, v247
v_mfma_f32_16x16x32_bf16 v[96:99], a[80:83], a[72:75], v[96:99]
ds_read_b64_tr_b16 a[124:125], v248 offset:8192
ds_read_b64_tr_b16 a[126:127], v248 offset:10240
v_mfma_f32_16x16x32_bf16 v[100:103], a[84:87], a[72:75], v[100:103]
ds_read_b128 a[108:111], v244 offset:6400
v_add_u32 v248, s53, v248
v_mfma_f32_16x16x32_bf16 v[104:107], a[88:91], a[72:75], v[104:107]
buffer_load_dwordx4 v7, s[4:7], s44 offen offset:2048 sc1 lds
v_mfma_f32_16x16x32_bf16 v[108:111], a[92:95], a[72:75], v[108:111]
s_add_u32 s49, s46, s91
v_add_u32 v244, s53, v244
v_mfma_f32_16x16x32_bf16 v[112:115], a[80:83], a[76:79], v[112:115]
s_add_u32 s46, s46, s53
v_mfma_f32_16x16x32_bf16 v[116:119], a[84:87], a[76:79], v[116:119]
v_mfma_f32_16x16x32_bf16 v[120:123], a[88:91], a[76:79], v[120:123]
buffer_load_dwordx4 v7, s[4:7], s44 offen offset:3072 sc1 lds
v_mfma_f32_16x16x32_bf16 v[124:127], a[92:95], a[76:79], v[124:127]
s_waitcnt vmcnt(24) lgkmcnt(0)
s_barrier
v_mfma_f32_16x16x32_bf16 v[64:67], a[112:115], a[96:99], v[64:67]
ds_read_b64_tr_b16 a[80:81], v245 offset:0
ds_read_b64_tr_b16 a[82:83], v245 offset:2048
v_mfma_f32_16x16x32_bf16 v[68:71], a[116:119], a[96:99], v[68:71]
s_add_u32 m0, s49, 16384
ds_read_b128 a[64:67], v244 offset:0
v_mfma_f32_16x16x32_bf16 v[72:75], a[120:123], a[96:99], v[72:75]
buffer_load_dwordx4 v240, s[8:11], s45 offen offset:0 lds
v_mfma_f32_16x16x32_bf16 v[76:79], a[124:127], a[96:99], v[76:79]
ds_read_b64_tr_b16 a[84:85], v246 offset:0
ds_read_b64_tr_b16 a[86:87], v246 offset:2048
v_mfma_f32_16x16x32_bf16 v[80:83], a[112:115], a[100:103], v[80:83]
s_add_u32 s44, s44, 4096
ds_read_b128 a[68:71], v244 offset:256
v_mfma_f32_16x16x32_bf16 v[84:87], a[116:119], a[100:103], v[84:87]
ds_read_b64_tr_b16 a[88:89], v247 offset:0
ds_read_b64_tr_b16 a[90:91], v247 offset:2048
v_mfma_f32_16x16x32_bf16 v[88:91], a[120:123], a[100:103], v[88:91]
buffer_load_dwordx4 v241, s[8:11], s45 offen offset:1024 lds
v_mfma_f32_16x16x32_bf16 v[92:95], a[124:127], a[100:103], v[92:95]
ds_read_b128 a[72:75], v244 offset:4096
v_mfma_f32_16x16x32_bf16 v[96:99], a[112:115], a[104:107], v[96:99]
ds_read_b64_tr_b16 a[92:93], v248 offset:0
ds_read_b64_tr_b16 a[94:95], v248 offset:2048
v_mfma_f32_16x16x32_bf16 v[100:103], a[116:119], a[104:107], v[100:103]
ds_read_b128 a[76:79], v244 offset:4352
v_mfma_f32_16x16x32_bf16 v[104:107], a[120:123], a[104:107], v[104:107]
buffer_load_dwordx4 v242, s[8:11], s45 offen offset:2048 lds
v_mfma_f32_16x16x32_bf16 v[108:111], a[124:127], a[104:107], v[108:111]
s_cmp_eq_u32 s46, 0
s_cselect_b32 s53, s56, s57
v_mfma_f32_16x16x32_bf16 v[112:115], a[112:115], a[108:111], v[112:115]
v_mfma_f32_16x16x32_bf16 v[116:119], a[116:119], a[108:111], v[116:119]
v_mfma_f32_16x16x32_bf16 v[120:123], a[120:123], a[108:111], v[120:123]
buffer_load_dwordx4 v243, s[8:11], s45 offen offset:3072 lds
v_mfma_f32_16x16x32_bf16 v[124:127], a[124:127], a[108:111], v[124:127]
s_waitcnt lgkmcnt(0)
s_mov_b32 m0, s49
s_add_u32 s45, s45, 131072
v_mfma_f32_16x16x32_bf16 v[64:67], a[80:83], a[64:67], v[64:67]
ds_read_b64_tr_b16 a[112:113], v245 offset:8192
ds_read_b64_tr_b16 a[114:115], v245 offset:10240
v_mfma_f32_16x16x32_bf16 v[68:71], a[84:87], a[64:67], v[68:71]
ds_read_b128 a[96:99], v244 offset:2048
v_add_u32 v245, s53, v245
v_mfma_f32_16x16x32_bf16 v[72:75], a[88:91], a[64:67], v[72:75]
buffer_load_dwordx4 v7, s[4:7], s44 offen offset:0 sc1 lds
v_mfma_f32_16x16x32_bf16 v[76:79], a[92:95], a[64:67], v[76:79]
ds_read_b64_tr_b16 a[116:117], v246 offset:8192
ds_read_b64_tr_b16 a[118:119], v246 offset:10240
v_mfma_f32_16x16x32_bf16 v[80:83], a[80:83], a[68:71], v[80:83]
ds_read_b128 a[100:103], v244 offset:2304
v_add_u32 v246, s53, v246
v_mfma_f32_16x16x32_bf16 v[84:87], a[84:87], a[68:71], v[84:87]
ds_read_b64_tr_b16 a[120:121], v247 offset:8192
ds_read_b64_tr_b16 a[122:123], v247 offset:10240
v_mfma_f32_16x16x32_bf16 v[88:91], a[88:91], a[68:71], v[88:91]
buffer_load_dwordx4 v7, s[4:7], s44 offen offset:1024 sc1 lds
v_mfma_f32_16x16x32_bf16 v[92:95], a[92:95], a[68:71], v[92:95]
ds_read_b128 a[104:107], v244 offset:6144
v_add_u32 v247, s53, v247
v_mfma_f32_16x16x32_bf16 v[96:99], a[80:83], a[72:75], v[96:99]
ds_read_b64_tr_b16 a[124:125], v248 offset:8192
ds_read_b64_tr_b16 a[126:127], v248 offset:10240
v_mfma_f32_16x16x32_bf16 v[100:103], a[84:87], a[72:75], v[100:103]
ds_read_b128 a[108:111], v244 offset:6400
v_add_u32 v248, s53, v248
v_mfma_f32_16x16x32_bf16 v[104:107], a[88:91], a[72:75], v[104:107]
buffer_load_dwordx4 v7, s[4:7], s44 offen offset:2048 sc1 lds
v_mfma_f32_16x16x32_bf16 v[108:111], a[92:95], a[72:75], v[108:111]
s_add_u32 s49, s46, s91
v_add_u32 v244, s53, v244
v_mfma_f32_16x16x32_bf16 v[112:115], a[80:83], a[76:79], v[112:115]
s_add_u32 s46, s46, s53
v_mfma_f32_16x16x32_bf16 v[116:119], a[84:87], a[76:79], v[116:119]
v_mfma_f32_16x16x32_bf16 v[120:123], a[88:91], a[76:79], v[120:123]
buffer_load_dwordx4 v7, s[4:7], s44 offen offset:3072 sc1 lds
v_mfma_f32_16x16x32_bf16 v[124:127], a[92:95], a[76:79], v[124:127]
s_waitcnt vmcnt(24) lgkmcnt(0)
s_barrier
v_mfma_f32_16x16x32_bf16 v[64:67], a[112:115], a[96:99], v[64:67]
ds_read_b64_tr_b16 a[80:81], v245 offset:0
ds_read_b64_tr_b16 a[82:83], v245 offset:2048
v_mfma_f32_16x16x32_bf16 v[68:71], a[116:119], a[96:99], v[68:71]
s_add_u32 m0, s49, 16384
ds_read_b128 a[64:67], v244 offset:0
v_mfma_f32_16x16x32_bf16 v[72:75], a[120:123], a[96:99], v[72:75]
buffer_load_dwordx4 v240, s[8:11], s45 offen offset:0 lds
v_mfma_f32_16x16x32_bf16 v[76:79], a[124:127], a[96:99], v[76:79]
ds_read_b64_tr_b16 a[84:85], v246 offset:0
ds_read_b64_tr_b16 a[86:87], v246 offset:2048
v_mfma_f32_16x16x32_bf16 v[80:83], a[112:115], a[100:103], v[80:83]
s_add_u32 s44, s44, 4096
ds_read_b128 a[68:71], v244 offset:256
v_mfma_f32_16x16x32_bf16 v[84:87], a[116:119], a[100:103], v[84:87]
ds_read_b64_tr_b16 a[88:89], v247 offset:0
ds_read_b64_tr_b16 a[90:91], v247 offset:2048
v_mfma_f32_16x16x32_bf16 v[88:91], a[120:123], a[100:103], v[88:91]
buffer_load_dwordx4 v241, s[8:11], s45 offen offset:1024 lds
v_mfma_f32_16x16x32_bf16 v[92:95], a[124:127], a[100:103], v[92:95]
ds_read_b128 a[72:75], v244 offset:4096
v_mfma_f32_16x16x32_bf16 v[96:99], a[112:115], a[104:107], v[96:99]
ds_read_b64_tr_b16 a[92:93], v248 offset:0
ds_read_b64_tr_b16 a[94:95], v248 offset:2048
v_mfma_f32_16x16x32_bf16 v[100:103], a[116:119], a[104:107], v[100:103]
ds_read_b128 a[76:79], v244 offset:4352
v_mfma_f32_16x16x32_bf16 v[104:107], a[120:123], a[104:107], v[104:107]
buffer_load_dwordx4 v242, s[8:11], s45 offen offset:2048 lds
v_mfma_f32_16x16x32_bf16 v[108:111], a[124:127], a[104:107], v[108:111]
s_cmp_eq_u32 s46, 0
s_cselect_b32 s53, s56, s57
v_mfma_f32_16x16x32_bf16 v[112:115], a[112:115], a[108:111], v[112:115]
v_mfma_f32_16x16x32_bf16 v[116:119], a[116:119], a[108:111], v[116:119]
v_mfma_f32_16x16x32_bf16 v[120:123], a[120:123], a[108:111], v[120:123]
buffer_load_dwordx4 v243, s[8:11], s45 offen offset:3072 lds
v_mfma_f32_16x16x32_bf16 v[124:127], a[124:127], a[108:111], v[124:127]
s_waitcnt lgkmcnt(0)
s_mov_b32 m0, s49
s_add_u32 s45, s45, 131072
v_mfma_f32_16x16x32_bf16 v[128:131], a[80:83], a[64:67], 0
ds_read_b64_tr_b16 a[112:113], v245 offset:8192
ds_read_b64_tr_b16 a[114:115], v245 offset:10240
v_mfma_f32_16x16x32_bf16 v[132:135], a[84:87], a[64:67], 0
ds_read_b128 a[96:99], v244 offset:2048
v_add_u32 v245, s53, v245
v_mfma_f32_16x16x32_bf16 v[136:139], a[88:91], a[64:67], 0
buffer_load_dwordx4 v7, s[4:7], s44 offen offset:0 sc1 lds
v_mfma_f32_16x16x32_bf16 v[140:143], a[92:95], a[64:67], 0
ds_read_b64_tr_b16 a[116:117], v246 offset:8192
ds_read_b64_tr_b16 a[118:119], v246 offset:10240
v_mfma_f32_16x16x32_bf16 v[144:147], a[80:83], a[68:71], 0
ds_read_b128 a[100:103], v244 offset:2304
v_add_u32 v246, s53, v246
v_mfma_f32_16x16x32_bf16 v[148:151], a[84:87], a[68:71], 0
ds_read_b64_tr_b16 a[120:121], v247 offset:8192
ds_read_b64_tr_b16 a[122:123], v247 offset:10240
v_mfma_f32_16x16x32_bf16 v[152:155], a[88:91], a[68:71], 0
buffer_load_dwordx4 v7, s[4:7], s44 offen offset:1024 sc1 lds
v_mfma_f32_16x16x32_bf16 v[156:159], a[92:95], a[68:71], 0
ds_read_b128 a[104:107], v244 offset:6144
v_add_u32 v247, s53, v247
v_mfma_f32_16x16x32_bf16 v[160:163], a[80:83], a[72:75], 0
ds_read_b64_tr_b16 a[124:125], v248 offset:8192
ds_read_b64_tr_b16 a[126:127], v248 offset:10240
v_mfma_f32_16x16x32_bf16 v[164:167], a[84:87], a[72:75], 0
ds_read_b128 a[108:111], v244 offset:6400
v_add_u32 v248, s53, v248
v_mfma_f32_16x16x32_bf16 v[168:171], a[88:91], a[72:75], 0
buffer_load_dwordx4 v7, s[4:7], s44 offen offset:2048 sc1 lds
v_mfma_f32_16x16x32_bf16 v[172:175], a[92:95], a[72:75], 0
s_add_u32 s49, s46, s91
v_add_u32 v244, s53, v244
v_mfma_f32_16x16x32_bf16 v[176:179], a[80:83], a[76:79], 0
s_add_u32 s46, s46, s53
v_mfma_f32_16x16x32_bf16 v[180:183], a[84:87], a[76:79], 0
v_mfma_f32_16x16x32_bf16 v[184:187], a[88:91], a[76:79], 0
buffer_load_dwordx4 v7, s[4:7], s44 offen offset:3072 sc1 lds
v_mfma_f32_16x16x32_bf16 v[188:191], a[92:95], a[76:79], 0
s_waitcnt vmcnt(24) lgkmcnt(0)
s_barrier
s_mov_b32 s32, 4096
v_mfma_f32_16x16x32_bf16 v[128:131], a[112:115], a[96:99], v[128:131]
ds_read_b64_tr_b16 a[80:81], v245 offset:0
ds_read_b64_tr_b16 a[82:83], v245 offset:2048
v_mfma_f32_16x16x32_bf16 v[132:135], a[116:119], a[96:99], v[132:135]
s_add_u32 m0, s49, 16384
ds_read_b128 a[64:67], v244 offset:0
v_cvt_pk_bf16_f32 v64, v64, v65
v_mfma_f32_16x16x32_bf16 v[136:139], a[120:123], a[96:99], v[136:139]
buffer_load_dwordx4 v240, s[8:11], s45 offen offset:0 lds
v_mfma_f32_16x16x32_bf16 v[140:143], a[124:127], a[96:99], v[140:143]
ds_read_b64_tr_b16 a[84:85], v246 offset:0
ds_read_b64_tr_b16 a[86:87], v246 offset:2048
v_mfma_f32_16x16x32_bf16 v[144:147], a[112:115], a[100:103], v[144:147]
s_add_u32 s44, s44, 4096
ds_read_b128 a[68:71], v244 offset:256
v_cvt_pk_bf16_f32 v65, v66, v67
v_mfma_f32_16x16x32_bf16 v[148:151], a[116:119], a[100:103], v[148:151]
ds_read_b64_tr_b16 a[88:89], v247 offset:0
ds_read_b64_tr_b16 a[90:91], v247 offset:2048
v_cvt_pk_bf16_f32 v66, v68, v69
v_mfma_f32_16x16x32_bf16 v[152:155], a[120:123], a[100:103], v[152:155]
buffer_load_dwordx4 v241, s[8:11], s45 offen offset:1024 lds
v_cvt_pk_bf16_f32 v67, v70, v71
v_mfma_f32_16x16x32_bf16 v[156:159], a[124:127], a[100:103], v[156:159]
buffer_store_dwordx4 v[64:67], v7, s[12:15], 0 offen offset:0 sc1
ds_read_b128 a[72:75], v244 offset:4096
v_mfma_f32_16x16x32_bf16 v[160:163], a[112:115], a[104:107], v[160:163]
ds_read_b64_tr_b16 a[92:93], v248 offset:0
ds_read_b64_tr_b16 a[94:95], v248 offset:2048
v_mfma_f32_16x16x32_bf16 v[164:167], a[116:119], a[104:107], v[164:167]
ds_read_b128 a[76:79], v244 offset:4352
v_mfma_f32_16x16x32_bf16 v[168:171], a[120:123], a[104:107], v[168:171]
buffer_load_dwordx4 v242, s[8:11], s45 offen offset:2048 lds
v_mfma_f32_16x16x32_bf16 v[172:175], a[124:127], a[104:107], v[172:175]
s_cmp_eq_u32 s46, 0
s_cselect_b32 s53, s56, s57
v_mfma_f32_16x16x32_bf16 v[176:179], a[112:115], a[108:111], v[176:179]
v_mfma_f32_16x16x32_bf16 v[180:183], a[116:119], a[108:111], v[180:183]
v_mfma_f32_16x16x32_bf16 v[184:187], a[120:123], a[108:111], v[184:187]
buffer_load_dwordx4 v243, s[8:11], s45 offen offset:3072 lds
v_mfma_f32_16x16x32_bf16 v[188:191], a[124:127], a[108:111], v[188:191]
s_waitcnt lgkmcnt(0)
s_mov_b32 m0, s49
s_add_u32 s45, s45, 131072
v_mfma_f32_16x16x32_bf16 v[128:131], a[80:83], a[64:67], v[128:131]
ds_read_b64_tr_b16 a[112:113], v245 offset:8192
ds_read_b64_tr_b16 a[114:115], v245 offset:10240
v_mfma_f32_16x16x32_bf16 v[132:135], a[84:87], a[64:67], v[132:135]
ds_read_b128 a[96:99], v244 offset:2048
v_add_u32 v245, s53, v245
v_cvt_pk_bf16_f32 v72, v72, v73
v_mfma_f32_16x16x32_bf16 v[136:139], a[88:91], a[64:67], v[136:139]
buffer_load_dwordx4 v7, s[4:7], s44 offen offset:0 sc1 lds
v_mfma_f32_16x16x32_bf16 v[140:143], a[92:95], a[64:67], v[140:143]
ds_read_b64_tr_b16 a[116:117], v246 offset:8192
ds_read_b64_tr_b16 a[118:119], v246 offset:10240
v_mfma_f32_16x16x32_bf16 v[144:147], a[80:83], a[68:71], v[144:147]
ds_read_b128 a[100:103], v244 offset:2304
v_add_u32 v246, s53, v246
v_cvt_pk_bf16_f32 v73, v74, v75
v_mfma_f32_16x16x32_bf16 v[148:151], a[84:87], a[68:71], v[148:151]
ds_read_b64_tr_b16 a[120:121], v247 offset:8192
ds_read_b64_tr_b16 a[122:123], v247 offset:10240
v_cvt_pk_bf16_f32 v74, v76, v77
v_mfma_f32_16x16x32_bf16 v[152:155], a[88:91], a[68:71], v[152:155]
buffer_load_dwordx4 v7, s[4:7], s44 offen offset:1024 sc1 lds
v_cvt_pk_bf16_f32 v75, v78, v79
v_mfma_f32_16x16x32_bf16 v[156:159], a[92:95], a[68:71], v[156:159]
buffer_store_dwordx4 v[72:75], v7, s[12:15], 0 offen offset:1024 sc1
ds_read_b128 a[104:107], v244 offset:6144
v_add_u32 v247, s53, v247
v_mfma_f32_16x16x32_bf16 v[160:163], a[80:83], a[72:75], v[160:163]
ds_read_b64_tr_b16 a[124:125], v248 offset:8192
ds_read_b64_tr_b16 a[126:127], v248 offset:10240
v_mfma_f32_16x16x32_bf16 v[164:167], a[84:87], a[72:75], v[164:167]
ds_read_b128 a[108:111], v244 offset:6400
v_add_u32 v248, s53, v248
v_mfma_f32_16x16x32_bf16 v[168:171], a[88:91], a[72:75], v[168:171]
buffer_load_dwordx4 v7, s[4:7], s44 offen offset:2048 sc1 lds
v_mfma_f32_16x16x32_bf16 v[172:175], a[92:95], a[72:75], v[172:175]
s_add_u32 s49, s46, s91
v_add_u32 v244, s53, v244
v_mfma_f32_16x16x32_bf16 v[176:179], a[80:83], a[76:79], v[176:179]
s_add_u32 s46, s46, s53
v_mfma_f32_16x16x32_bf16 v[180:183], a[84:87], a[76:79], v[180:183]
v_mfma_f32_16x16x32_bf16 v[184:187], a[88:91], a[76:79], v[184:187]
buffer_load_dwordx4 v7, s[4:7], s44 offen offset:3072 sc1 lds
v_mfma_f32_16x16x32_bf16 v[188:191], a[92:95], a[76:79], v[188:191]
s_waitcnt vmcnt(26) lgkmcnt(0)
s_barrier
v_mfma_f32_16x16x32_bf16 v[128:131], a[112:115], a[96:99], v[128:131]
ds_read_b64_tr_b16 a[80:81], v245 offset:0
ds_read_b64_tr_b16 a[82:83], v245 offset:2048
v_mfma_f32_16x16x32_bf16 v[132:135], a[116:119], a[96:99], v[132:135]
s_add_u32 m0, s49, 16384
ds_read_b128 a[64:67], v244 offset:0
v_cvt_pk_bf16_f32 v80, v80, v81
v_mfma_f32_16x16x32_bf16 v[136:139], a[120:123], a[96:99], v[136:139]
buffer_load_dwordx4 v240, s[8:11], s45 offen offset:0 lds
v_mfma_f32_16x16x32_bf16 v[140:143], a[124:127], a[96:99], v[140:143]
ds_read_b64_tr_b16 a[84:85], v246 offset:0
ds_read_b64_tr_b16 a[86:87], v246 offset:2048
v_mfma_f32_16x16x32_bf16 v[144:147], a[112:115], a[100:103], v[144:147]
s_add_u32 s44, s44, 4096
ds_read_b128 a[68:71], v244 offset:256
v_cvt_pk_bf16_f32 v81, v82, v83
v_mfma_f32_16x16x32_bf16 v[148:151], a[116:119], a[100:103], v[148:151]
ds_read_b64_tr_b16 a[88:89], v247 offset:0
ds_read_b64_tr_b16 a[90:91], v247 offset:2048
v_cvt_pk_bf16_f32 v82, v84, v85
v_mfma_f32_16x16x32_bf16 v[152:155], a[120:123], a[100:103], v[152:155]
buffer_load_dwordx4 v241, s[8:11], s45 offen offset:1024 lds
v_cvt_pk_bf16_f32 v83, v86, v87
v_mfma_f32_16x16x32_bf16 v[156:159], a[124:127], a[100:103], v[156:159]
buffer_store_dwordx4 v[80:83], v7, s[12:15], 0 offen offset:2048 sc1
ds_read_b128 a[72:75], v244 offset:4096
v_mfma_f32_16x16x32_bf16 v[160:163], a[112:115], a[104:107], v[160:163]
ds_read_b64_tr_b16 a[92:93], v248 offset:0
ds_read_b64_tr_b16 a[94:95], v248 offset:2048
v_mfma_f32_16x16x32_bf16 v[164:167], a[116:119], a[104:107], v[164:167]
ds_read_b128 a[76:79], v244 offset:4352
v_mfma_f32_16x16x32_bf16 v[168:171], a[120:123], a[104:107], v[168:171]
buffer_load_dwordx4 v242, s[8:11], s45 offen offset:2048 lds
v_mfma_f32_16x16x32_bf16 v[172:175], a[124:127], a[104:107], v[172:175]
s_cmp_eq_u32 s46, 0
s_cselect_b32 s53, s56, s57
v_mfma_f32_16x16x32_bf16 v[176:179], a[112:115], a[108:111], v[176:179]
v_mfma_f32_16x16x32_bf16 v[180:183], a[116:119], a[108:111], v[180:183]
v_mfma_f32_16x16x32_bf16 v[184:187], a[120:123], a[108:111], v[184:187]
buffer_load_dwordx4 v243, s[8:11], s45 offen offset:3072 lds
v_mfma_f32_16x16x32_bf16 v[188:191], a[124:127], a[108:111], v[188:191]
s_waitcnt lgkmcnt(0)
s_mov_b32 m0, s49
s_add_u32 s45, s45, 131072
v_mfma_f32_16x16x32_bf16 v[128:131], a[80:83], a[64:67], v[128:131]
ds_read_b64_tr_b16 a[112:113], v245 offset:8192
ds_read_b64_tr_b16 a[114:115], v245 offset:10240
v_mfma_f32_16x16x32_bf16 v[132:135], a[84:87], a[64:67], v[132:135]
ds_read_b128 a[96:99], v244 offset:2048
v_add_u32 v245, s53, v245
v_cvt_pk_bf16_f32 v88, v88, v89
v_mfma_f32_16x16x32_bf16 v[136:139], a[88:91], a[64:67], v[136:139]
buffer_load_dwordx4 v7, s[4:7], s44 offen offset:0 sc1 lds
v_mfma_f32_16x16x32_bf16 v[140:143], a[92:95], a[64:67], v[140:143]
ds_read_b64_tr_b16 a[116:117], v246 offset:8192
ds_read_b64_tr_b16 a[118:119], v246 offset:10240
v_mfma_f32_16x16x32_bf16 v[144:147], a[80:83], a[68:71], v[144:147]
ds_read_b128 a[100:103], v244 offset:2304
v_add_u32 v246, s53, v246
v_cvt_pk_bf16_f32 v89, v90, v91
v_mfma_f32_16x16x32_bf16 v[148:151], a[84:87], a[68:71], v[148:151]
ds_read_b64_tr_b16 a[120:121], v247 offset:8192
ds_read_b64_tr_b16 a[122:123], v247 offset:10240
v_cvt_pk_bf16_f32 v90, v92, v93
v_mfma_f32_16x16x32_bf16 v[152:155], a[88:91], a[68:71], v[152:155]
buffer_load_dwordx4 v7, s[4:7], s44 offen offset:1024 sc1 lds
v_cvt_pk_bf16_f32 v91, v94, v95
v_mfma_f32_16x16x32_bf16 v[156:159], a[92:95], a[68:71], v[156:159]
buffer_store_dwordx4 v[88:91], v7, s[12:15], 0 offen offset:3072 sc1
ds_read_b128 a[104:107], v244 offset:6144
v_add_u32 v247, s53, v247
v_mfma_f32_16x16x32_bf16 v[160:163], a[80:83], a[72:75], v[160:163]
ds_read_b64_tr_b16 a[124:125], v248 offset:8192
ds_read_b64_tr_b16 a[126:127], v248 offset:10240
v_mfma_f32_16x16x32_bf16 v[164:167], a[84:87], a[72:75], v[164:167]
ds_read_b128 a[108:111], v244 offset:6400
v_add_u32 v248, s53, v248
v_mfma_f32_16x16x32_bf16 v[168:171], a[88:91], a[72:75], v[168:171]
buffer_load_dwordx4 v7, s[4:7], s44 offen offset:2048 sc1 lds
v_mfma_f32_16x16x32_bf16 v[172:175], a[92:95], a[72:75], v[172:175]
s_add_u32 s49, s46, s91
v_add_u32 v244, s53, v244
v_mfma_f32_16x16x32_bf16 v[176:179], a[80:83], a[76:79], v[176:179]
s_add_u32 s46, s46, s53
v_mfma_f32_16x16x32_bf16 v[180:183], a[84:87], a[76:79], v[180:183]
v_mfma_f32_16x16x32_bf16 v[184:187], a[88:91], a[76:79], v[184:187]
buffer_load_dwordx4 v7, s[4:7], s44 offen offset:3072 sc1 lds
v_mfma_f32_16x16x32_bf16 v[188:191], a[92:95], a[76:79], v[188:191]
s_waitcnt vmcnt(28) lgkmcnt(0)
s_barrier
v_mfma_f32_16x16x32_bf16 v[128:131], a[112:115], a[96:99], v[128:131]
ds_read_b64_tr_b16 a[80:81], v245 offset:0
ds_read_b64_tr_b16 a[82:83], v245 offset:2048
v_mfma_f32_16x16x32_bf16 v[132:135], a[116:119], a[96:99], v[132:135]
s_add_u32 m0, s49, 16384
ds_read_b128 a[64:67], v244 offset:0
v_cvt_pk_bf16_f32 v96, v96, v97
v_mfma_f32_16x16x32_bf16 v[136:139], a[120:123], a[96:99], v[136:139]
buffer_load_dwordx4 v240, s[8:11], s45 offen offset:0 lds
v_mfma_f32_16x16x32_bf16 v[140:143], a[124:127], a[96:99], v[140:143]
ds_read_b64_tr_b16 a[84:85], v246 offset:0
ds_read_b64_tr_b16 a[86:87], v246 offset:2048
v_mfma_f32_16x16x32_bf16 v[144:147], a[112:115], a[100:103], v[144:147]
s_add_u32 s44, s44, 4096
ds_read_b128 a[68:71], v244 offset:256
v_cvt_pk_bf16_f32 v97, v98, v99
v_mfma_f32_16x16x32_bf16 v[148:151], a[116:119], a[100:103], v[148:151]
ds_read_b64_tr_b16 a[88:89], v247 offset:0
ds_read_b64_tr_b16 a[90:91], v247 offset:2048
v_cvt_pk_bf16_f32 v98, v100, v101
v_mfma_f32_16x16x32_bf16 v[152:155], a[120:123], a[100:103], v[152:155]
buffer_load_dwordx4 v241, s[8:11], s45 offen offset:1024 lds
v_cvt_pk_bf16_f32 v99, v102, v103
v_mfma_f32_16x16x32_bf16 v[156:159], a[124:127], a[100:103], v[156:159]
buffer_store_dwordx4 v[96:99], v7, s[12:15], s32 offen offset:0 sc1
ds_read_b128 a[72:75], v244 offset:4096
v_mfma_f32_16x16x32_bf16 v[160:163], a[112:115], a[104:107], v[160:163]
ds_read_b64_tr_b16 a[92:93], v248 offset:0
ds_read_b64_tr_b16 a[94:95], v248 offset:2048
v_mfma_f32_16x16x32_bf16 v[164:167], a[116:119], a[104:107], v[164:167]
ds_read_b128 a[76:79], v244 offset:4352
v_mfma_f32_16x16x32_bf16 v[168:171], a[120:123], a[104:107], v[168:171]
buffer_load_dwordx4 v242, s[8:11], s45 offen offset:2048 lds
v_mfma_f32_16x16x32_bf16 v[172:175], a[124:127], a[104:107], v[172:175]
s_cmp_eq_u32 s46, 0
s_cselect_b32 s53, s56, s57
v_mfma_f32_16x16x32_bf16 v[176:179], a[112:115], a[108:111], v[176:179]
v_mfma_f32_16x16x32_bf16 v[180:183], a[116:119], a[108:111], v[180:183]
v_mfma_f32_16x16x32_bf16 v[184:187], a[120:123], a[108:111], v[184:187]
buffer_load_dwordx4 v243, s[8:11], s45 offen offset:3072 lds
v_mfma_f32_16x16x32_bf16 v[188:191], a[124:127], a[108:111], v[188:191]
s_waitcnt lgkmcnt(0)
s_mov_b32 m0, s49
s_add_u32 s45, s45, 131072
v_mfma_f32_16x16x32_bf16 v[128:131], a[80:83], a[64:67], v[128:131]
ds_read_b64_tr_b16 a[112:113], v245 offset:8192
ds_read_b64_tr_b16 a[114:115], v245 offset:10240
v_mfma_f32_16x16x32_bf16 v[132:135], a[84:87], a[64:67], v[132:135]
ds_read_b128 a[96:99], v244 offset:2048
v_add_u32 v245, s53, v245
v_cvt_pk_bf16_f32 v104, v104, v105
v_mfma_f32_16x16x32_bf16 v[136:139], a[88:91], a[64:67], v[136:139]
buffer_load_dwordx4 v7, s[4:7], s44 offen offset:0 sc1 lds
v_mfma_f32_16x16x32_bf16 v[140:143], a[92:95], a[64:67], v[140:143]
ds_read_b64_tr_b16 a[116:117], v246 offset:8192
ds_read_b64_tr_b16 a[118:119], v246 offset:10240
v_mfma_f32_16x16x32_bf16 v[144:147], a[80:83], a[68:71], v[144:147]
ds_read_b128 a[100:103], v244 offset:2304
v_add_u32 v246, s53, v246
v_cvt_pk_bf16_f32 v105, v106, v107
v_mfma_f32_16x16x32_bf16 v[148:151], a[84:87], a[68:71], v[148:151]
ds_read_b64_tr_b16 a[120:121], v247 offset:8192
ds_read_b64_tr_b16 a[122:123], v247 offset:10240
v_cvt_pk_bf16_f32 v106, v108, v109
v_mfma_f32_16x16x32_bf16 v[152:155], a[88:91], a[68:71], v[152:155]
buffer_load_dwordx4 v7, s[4:7], s44 offen offset:1024 sc1 lds
v_cvt_pk_bf16_f32 v107, v110, v111
v_mfma_f32_16x16x32_bf16 v[156:159], a[92:95], a[68:71], v[156:159]
buffer_store_dwordx4 v[104:107], v7, s[12:15], s32 offen offset:1024 sc1
ds_read_b128 a[104:107], v244 offset:6144
v_add_u32 v247, s53, v247
v_mfma_f32_16x16x32_bf16 v[160:163], a[80:83], a[72:75], v[160:163]
ds_read_b64_tr_b16 a[124:125], v248 offset:8192
ds_read_b64_tr_b16 a[126:127], v248 offset:10240
v_mfma_f32_16x16x32_bf16 v[164:167], a[84:87], a[72:75], v[164:167]
ds_read_b128 a[108:111], v244 offset:6400
v_add_u32 v248, s53, v248
v_mfma_f32_16x16x32_bf16 v[168:171], a[88:91], a[72:75], v[168:171]
buffer_load_dwordx4 v7, s[4:7], s44 offen offset:2048 sc1 lds
v_mfma_f32_16x16x32_bf16 v[172:175], a[92:95], a[72:75], v[172:175]
s_add_u32 s49, s46, s91
v_add_u32 v244, s53, v244
v_mfma_f32_16x16x32_bf16 v[176:179], a[80:83], a[76:79], v[176:179]
s_add_u32 s46, s46, s53
v_mfma_f32_16x16x32_bf16 v[180:183], a[84:87], a[76:79], v[180:183]
v_mfma_f32_16x16x32_bf16 v[184:187], a[88:91], a[76:79], v[184:187]
buffer_load_dwordx4 v7, s[4:7], s44 offen offset:3072 sc1 lds
v_mfma_f32_16x16x32_bf16 v[188:191], a[92:95], a[76:79], v[188:191]
s_waitcnt vmcnt(30) lgkmcnt(0)
s_barrier
v_mfma_f32_16x16x32_bf16 v[128:131], a[112:115], a[96:99], v[128:131]
ds_read_b64_tr_b16 a[80:81], v245 offset:0
ds_read_b64_tr_b16 a[82:83], v245 offset:2048
v_mfma_f32_16x16x32_bf16 v[132:135], a[116:119], a[96:99], v[132:135]
s_add_u32 m0, s49, 16384
ds_read_b128 a[64:67], v244 offset:0
v_cvt_pk_bf16_f32 v112, v112, v113
v_mfma_f32_16x16x32_bf16 v[136:139], a[120:123], a[96:99], v[136:139]
buffer_load_dwordx4 v240, s[8:11], s45 offen offset:0 lds
v_mfma_f32_16x16x32_bf16 v[140:143], a[124:127], a[96:99], v[140:143]
ds_read_b64_tr_b16 a[84:85], v246 offset:0
ds_read_b64_tr_b16 a[86:87], v246 offset:2048
v_mfma_f32_16x16x32_bf16 v[144:147], a[112:115], a[100:103], v[144:147]
s_add_u32 s44, s44, 4096
ds_read_b128 a[68:71], v244 offset:256
v_cvt_pk_bf16_f32 v113, v114, v115
v_mfma_f32_16x16x32_bf16 v[148:151], a[116:119], a[100:103], v[148:151]
ds_read_b64_tr_b16 a[88:89], v247 offset:0
ds_read_b64_tr_b16 a[90:91], v247 offset:2048
v_cvt_pk_bf16_f32 v114, v116, v117
v_mfma_f32_16x16x32_bf16 v[152:155], a[120:123], a[100:103], v[152:155]
buffer_load_dwordx4 v241, s[8:11], s45 offen offset:1024 lds
v_cvt_pk_bf16_f32 v115, v118, v119
v_mfma_f32_16x16x32_bf16 v[156:159], a[124:127], a[100:103], v[156:159]
buffer_store_dwordx4 v[112:115], v7, s[12:15], s32 offen offset:2048 sc1
ds_read_b128 a[72:75], v244 offset:4096
v_mfma_f32_16x16x32_bf16 v[160:163], a[112:115], a[104:107], v[160:163]
ds_read_b64_tr_b16 a[92:93], v248 offset:0
ds_read_b64_tr_b16 a[94:95], v248 offset:2048
v_mfma_f32_16x16x32_bf16 v[164:167], a[116:119], a[104:107], v[164:167]
ds_read_b128 a[76:79], v244 offset:4352
v_mfma_f32_16x16x32_bf16 v[168:171], a[120:123], a[104:107], v[168:171]
buffer_load_dwordx4 v242, s[8:11], s45 offen offset:2048 lds
v_mfma_f32_16x16x32_bf16 v[172:175], a[124:127], a[104:107], v[172:175]
s_cmp_eq_u32 s46, 0
s_cselect_b32 s53, s56, s57
v_mfma_f32_16x16x32_bf16 v[176:179], a[112:115], a[108:111], v[176:179]
v_mfma_f32_16x16x32_bf16 v[180:183], a[116:119], a[108:111], v[180:183]
v_mfma_f32_16x16x32_bf16 v[184:187], a[120:123], a[108:111], v[184:187]
buffer_load_dwordx4 v243, s[8:11], s45 offen offset:3072 lds
v_mfma_f32_16x16x32_bf16 v[188:191], a[124:127], a[108:111], v[188:191]
s_waitcnt lgkmcnt(0)
s_mov_b32 m0, s49
s_add_u32 s45, s45, 131072
v_mfma_f32_16x16x32_bf16 v[128:131], a[80:83], a[64:67], v[128:131]
ds_read_b64_tr_b16 a[112:113], v245 offset:8192
ds_read_b64_tr_b16 a[114:115], v245 offset:10240
v_mfma_f32_16x16x32_bf16 v[132:135], a[84:87], a[64:67], v[132:135]
ds_read_b128 a[96:99], v244 offset:2048
v_add_u32 v245, s53, v245
v_cvt_pk_bf16_f32 v120, v120, v121
v_mfma_f32_16x16x32_bf16 v[136:139], a[88:91], a[64:67], v[136:139]
buffer_load_dwordx4 v7, s[4:7], s44 offen offset:0 sc1 lds
v_mfma_f32_16x16x32_bf16 v[140:143], a[92:95], a[64:67], v[140:143]
ds_read_b64_tr_b16 a[116:117], v246 offset:8192
ds_read_b64_tr_b16 a[118:119], v246 offset:10240
v_mfma_f32_16x16x32_bf16 v[144:147], a[80:83], a[68:71], v[144:147]
ds_read_b128 a[100:103], v244 offset:2304
v_add_u32 v246, s53, v246
v_cvt_pk_bf16_f32 v121, v122, v123
v_mfma_f32_16x16x32_bf16 v[148:151], a[84:87], a[68:71], v[148:151]
ds_read_b64_tr_b16 a[120:121], v247 offset:8192
ds_read_b64_tr_b16 a[122:123], v247 offset:10240
v_cvt_pk_bf16_f32 v122, v124, v125
v_mfma_f32_16x16x32_bf16 v[152:155], a[88:91], a[68:71], v[152:155]
buffer_load_dwordx4 v7, s[4:7], s44 offen offset:1024 sc1 lds
v_cvt_pk_bf16_f32 v123, v126, v127
v_mfma_f32_16x16x32_bf16 v[156:159], a[92:95], a[68:71], v[156:159]
buffer_store_dwordx4 v[120:123], v7, s[12:15], s32 offen offset:3072 sc1
ds_read_b128 a[104:107], v244 offset:6144
v_add_u32 v247, s53, v247
v_mfma_f32_16x16x32_bf16 v[160:163], a[80:83], a[72:75], v[160:163]
ds_read_b64_tr_b16 a[124:125], v248 offset:8192
ds_read_b64_tr_b16 a[126:127], v248 offset:10240
v_mfma_f32_16x16x32_bf16 v[164:167], a[84:87], a[72:75], v[164:167]
ds_read_b128 a[108:111], v244 offset:6400
v_add_u32 v248, s53, v248
v_mfma_f32_16x16x32_bf16 v[168:171], a[88:91], a[72:75], v[168:171]
buffer_load_dwordx4 v7, s[4:7], s44 offen offset:2048 sc1 lds
v_mfma_f32_16x16x32_bf16 v[172:175], a[92:95], a[72:75], v[172:175]
s_add_u32 s49, s46, s91
v_add_u32 v244, s53, v244
v_mfma_f32_16x16x32_bf16 v[176:179], a[80:83], a[76:79], v[176:179]
s_add_u32 s46, s46, s53
v_mfma_f32_16x16x32_bf16 v[180:183], a[84:87], a[76:79], v[180:183]
v_mfma_f32_16x16x32_bf16 v[184:187], a[88:91], a[76:79], v[184:187]
buffer_load_dwordx4 v7, s[4:7], s44 offen offset:3072 sc1 lds
v_mfma_f32_16x16x32_bf16 v[188:191], a[92:95], a[76:79], v[188:191]
s_waitcnt vmcnt(30) lgkmcnt(0)
s_barrier
v_mfma_f32_16x16x32_bf16 v[128:131], a[112:115], a[96:99], v[128:131]
ds_read_b64_tr_b16 a[80:81], v245 offset:0
.Lpad22:
s_cbranch_execz .Ltch9
ds_read_b64_tr_b16 a[82:83], v245 offset:2048
v_mfma_f32_16x16x32_bf16 v[132:135], a[116:119], a[96:99], v[132:135]
s_add_u32 m0, s49, 16384
ds_read_b128 a[64:67], v244 offset:0
v_mfma_f32_16x16x32_bf16 v[136:139], a[120:123], a[96:99], v[136:139]
buffer_load_dwordx4 v240, s[8:11], s45 offen offset:0 lds
v_mfma_f32_16x16x32_bf16 v[140:143], a[124:127], a[96:99], v[140:143]
ds_read_b64_tr_b16 a[84:85], v246 offset:0
ds_read_b64_tr_b16 a[86:87], v246 offset:2048
v_mfma_f32_16x16x32_bf16 v[144:147], a[112:115], a[100:103], v[144:147]
s_add_u32 s44, s44, 4096
ds_read_b128 a[68:71], v244 offset:256
v_mfma_f32_16x16x32_bf16 v[148:151], a[116:119], a[100:103], v[148:151]
ds_read_b64_tr_b16 a[88:89], v247 offset:0
ds_read_b64_tr_b16 a[90:91], v247 offset:2048
v_mfma_f32_16x16x32_bf16 v[152:155], a[120:123], a[100:103], v[152:155]
buffer_load_dwordx4 v241, s[8:11], s45 offen offset:1024 lds
v_mfma_f32_16x16x32_bf16 v[156:159], a[124:127], a[100:103], v[156:159]
ds_read_b128 a[72:75], v244 offset:4096
v_mfma_f32_16x16x32_bf16 v[160:163], a[112:115], a[104:107], v[160:163]
ds_read_b64_tr_b16 a[92:93], v248 offset:0
ds_read_b64_tr_b16 a[94:95], v248 offset:2048
v_mfma_f32_16x16x32_bf16 v[164:167], a[116:119], a[104:107], v[164:167]
ds_read_b128 a[76:79], v244 offset:4352
v_mfma_f32_16x16x32_bf16 v[168:171], a[120:123], a[104:107], v[168:171]
buffer_load_dwordx4 v242, s[8:11], s45 offen offset:2048 lds
v_mfma_f32_16x16x32_bf16 v[172:175], a[124:127], a[104:107], v[172:175]
s_cmp_eq_u32 s46, 0
s_cselect_b32 s53, s56, s57
v_mfma_f32_16x16x32_bf16 v[176:179], a[112:115], a[108:111], v[176:179]
v_mfma_f32_16x16x32_bf16 v[180:183], a[116:119], a[108:111], v[180:183]
v_mfma_f32_16x16x32_bf16 v[184:187], a[120:123], a[108:111], v[184:187]
buffer_load_dwordx4 v243, s[8:11], s45 offen offset:3072 lds
v_mfma_f32_16x16x32_bf16 v[188:191], a[124:127], a[108:111], v[188:191]
s_waitcnt lgkmcnt(0)
s_mov_b32 m0, s49
s_add_u32 s45, s45, 131072
v_mfma_f32_16x16x32_bf16 v[128:131], a[80:83], a[64:67], v[128:131]
ds_read_b64_tr_b16 a[112:113], v245 offset:8192
ds_read_b64_tr_b16 a[114:115], v245 offset:10240
v_mfma_f32_16x16x32_bf16 v[132:135], a[84:87], a[64:67], v[132:135]
ds_read_b128 a[96:99], v244 offset:2048
v_add_u32 v245, s53, v245
v_mfma_f32_16x16x32_bf16 v[136:139], a[88:91], a[64:67], v[136:139]
buffer_load_dwordx4 v7, s[4:7], s44 offen offset:0 sc1 lds
v_mfma_f32_16x16x32_bf16 v[140:143], a[92:95], a[64:67], v[140:143]
ds_read_b64_tr_b16 a[116:117], v246 offset:8192
ds_read_b64_tr_b16 a[118:119], v246 offset:10240
v_mfma_f32_16x16x32_bf16 v[144:147], a[80:83], a[68:71], v[144:147]
ds_read_b128 a[100:103], v244 offset:2304
v_add_u32 v246, s53, v246
v_mfma_f32_16x16x32_bf16 v[148:151], a[84:87], a[68:71], v[148:151]
ds_read_b64_tr_b16 a[120:121], v247 offset:8192
ds_read_b64_tr_b16 a[122:123], v247 offset:10240
v_mfma_f32_16x16x32_bf16 v[152:155], a[88:91], a[68:71], v[152:155]
buffer_load_dwordx4 v7, s[4:7], s44 offen offset:1024 sc1 lds
v_mfma_f32_16x16x32_bf16 v[156:159], a[92:95], a[68:71], v[156:159]
ds_read_b128 a[104:107], v244 offset:6144
v_add_u32 v247, s53, v247
v_mfma_f32_16x16x32_bf16 v[160:163], a[80:83], a[72:75], v[160:163]
ds_read_b64_tr_b16 a[124:125], v248 offset:8192
ds_read_b64_tr_b16 a[126:127], v248 offset:10240
v_mfma_f32_16x16x32_bf16 v[164:167], a[84:87], a[72:75], v[164:167]
ds_read_b128 a[108:111], v244 offset:6400
v_add_u32 v248, s53, v248
v_mfma_f32_16x16x32_bf16 v[168:171], a[88:91], a[72:75], v[168:171]
buffer_load_dwordx4 v7, s[4:7], s44 offen offset:2048 sc1 lds
v_mfma_f32_16x16x32_bf16 v[172:175], a[92:95], a[72:75], v[172:175]
s_add_u32 s49, s46, s91
v_add_u32 v244, s53, v244
v_mfma_f32_16x16x32_bf16 v[176:179], a[80:83], a[76:79], v[176:179]
s_add_u32 s46, s46, s53
v_mfma_f32_16x16x32_bf16 v[180:183], a[84:87], a[76:79], v[180:183]
v_mfma_f32_16x16x32_bf16 v[184:187], a[88:91], a[76:79], v[184:187]
buffer_load_dwordx4 v7, s[4:7], s44 offen offset:3072 sc1 lds
v_mfma_f32_16x16x32_bf16 v[188:191], a[92:95], a[76:79], v[188:191]
s_waitcnt vmcnt(28) lgkmcnt(0)
s_barrier
v_mfma_f32_16x16x32_bf16 v[128:131], a[112:115], a[96:99], v[128:131]
ds_read_b64_tr_b16 a[80:81], v245 offset:0
ds_read_b64_tr_b16 a[82:83], v245 offset:2048
v_mfma_f32_16x16x32_bf16 v[132:135], a[116:119], a[96:99], v[132:135]
s_add_u32 m0, s49, 16384
ds_read_b128 a[64:67], v244 offset:0
v_mfma_f32_16x16x32_bf16 v[136:139], a[120:123], a[96:99], v[136:139]
buffer_load_dwordx4 v240, s[8:11], s45 offen offset:0 lds
v_mfma_f32_16x16x32_bf16 v[140:143], a[124:127], a[96:99], v[140:143]
ds_read_b64_tr_b16 a[84:85], v246 offset:0
ds_read_b64_tr_b16 a[86:87], v246 offset:2048
v_mfma_f32_16x16x32_bf16 v[144:147], a[112:115], a[100:103], v[144:147]
s_add_u32 s44, s44, 4096
ds_read_b128 a[68:71], v244 offset:256
v_mfma_f32_16x16x32_bf16 v[148:151], a[116:119], a[100:103], v[148:151]
ds_read_b64_tr_b16 a[88:89], v247 offset:0
ds_read_b64_tr_b16 a[90:91], v247 offset:2048
v_mfma_f32_16x16x32_bf16 v[152:155], a[120:123], a[100:103], v[152:155]
buffer_load_dwordx4 v241, s[8:11], s45 offen offset:1024 lds
v_mfma_f32_16x16x32_bf16 v[156:159], a[124:127], a[100:103], v[156:159]
ds_read_b128 a[72:75], v244 offset:4096
v_mfma_f32_16x16x32_bf16 v[160:163], a[112:115], a[104:107], v[160:163]
ds_read_b64_tr_b16 a[92:93], v248 offset:0
ds_read_b64_tr_b16 a[94:95], v248 offset:2048
v_mfma_f32_16x16x32_bf16 v[164:167], a[116:119], a[104:107], v[164:167]
ds_read_b128 a[76:79], v244 offset:4352
v_mfma_f32_16x16x32_bf16 v[168:171], a[120:123], a[104:107], v[168:171]
buffer_load_dwordx4 v242, s[8:11], s45 offen offset:2048 lds
v_mfma_f32_16x16x32_bf16 v[172:175], a[124:127], a[104:107], v[172:175]
s_cmp_eq_u32 s46, 0
s_cselect_b32 s53, s56, s57
v_mfma_f32_16x16x32_bf16 v[176:179], a[112:115], a[108:111], v[176:179]
v_mfma_f32_16x16x32_bf16 v[180:183], a[116:119], a[108:111], v[180:183]
v_mfma_f32_16x16x32_bf16 v[184:187], a[120:123], a[108:111], v[184:187]
buffer_load_dwordx4 v243, s[8:11], s45 offen offset:3072 lds
v_mfma_f32_16x16x32_bf16 v[188:191], a[124:127], a[108:111], v[188:191]
s_waitcnt lgkmcnt(0)
s_mov_b32 m0, s49
s_add_u32 s45, s45, 131072
v_mfma_f32_16x16x32_bf16 v[128:131], a[80:83], a[64:67], v[128:131]
ds_read_b64_tr_b16 a[112:113], v245 offset:8192
ds_read_b64_tr_b16 a[114:115], v245 offset:10240
v_mfma_f32_16x16x32_bf16 v[132:135], a[84:87], a[64:67], v[132:135]
ds_read_b128 a[96:99], v244 offset:2048
v_add_u32 v245, s53, v245
v_mfma_f32_16x16x32_bf16 v[136:139], a[88:91], a[64:67], v[136:139]
buffer_load_dwordx4 v7, s[4:7], s44 offen offset:0 sc1 lds
v_mfma_f32_16x16x32_bf16 v[140:143], a[92:95], a[64:67], v[140:143]
ds_read_b64_tr_b16 a[116:117], v246 offset:8192
ds_read_b64_tr_b16 a[118:119], v246 offset:10240
v_mfma_f32_16x16x32_bf16 v[144:147], a[80:83], a[68:71], v[144:147]
ds_read_b128 a[100:103], v244 offset:2304
v_add_u32 v246, s53, v246
v_mfma_f32_16x16x32_bf16 v[148:151], a[84:87], a[68:71], v[148:151]
ds_read_b64_tr_b16 a[120:121], v247 offset:8192
ds_read_b64_tr_b16 a[122:123], v247 offset:10240
v_mfma_f32_16x16x32_bf16 v[152:155], a[88:91], a[68:71], v[152:155]
buffer_load_dwordx4 v7, s[4:7], s44 offen offset:1024 sc1 lds
v_mfma_f32_16x16x32_bf16 v[156:159], a[92:95], a[68:71], v[156:159]
ds_read_b128 a[104:107], v244 offset:6144
v_add_u32 v247, s53, v247
v_mfma_f32_16x16x32_bf16 v[160:163], a[80:83], a[72:75], v[160:163]
ds_read_b64_tr_b16 a[124:125], v248 offset:8192
ds_read_b64_tr_b16 a[126:127], v248 offset:10240
v_mfma_f32_16x16x32_bf16 v[164:167], a[84:87], a[72:75], v[164:167]
ds_read_b128 a[108:111], v244 offset:6400
v_add_u32 v248, s53, v248
v_mfma_f32_16x16x32_bf16 v[168:171], a[88:91], a[72:75], v[168:171]
buffer_load_dwordx4 v7, s[4:7], s44 offen offset:2048 sc1 lds
v_mfma_f32_16x16x32_bf16 v[172:175], a[92:95], a[72:75], v[172:175]
s_add_u32 s49, s46, s91
v_add_u32 v244, s53, v244
v_mfma_f32_16x16x32_bf16 v[176:179], a[80:83], a[76:79], v[176:179]
s_add_u32 s46, s46, s53
v_mfma_f32_16x16x32_bf16 v[180:183], a[84:87], a[76:79], v[180:183]
v_mfma_f32_16x16x32_bf16 v[184:187], a[88:91], a[76:79], v[184:187]
buffer_load_dwordx4 v7, s[4:7], s44 offen offset:3072 sc1 lds
v_mfma_f32_16x16x32_bf16 v[188:191], a[92:95], a[76:79], v[188:191]
s_waitcnt vmcnt(18)
s_waitcnt vmcnt(26) lgkmcnt(0)
s_barrier
v_mfma_f32_16x16x32_bf16 v[128:131], a[112:115], a[96:99], v[128:131]
ds_read_b64_tr_b16 a[80:81], v245 offset:0
ds_read_b64_tr_b16 a[82:83], v245 offset:2048
v_mfma_f32_16x16x32_bf16 v[132:135], a[116:119], a[96:99], v[132:135]
s_add_u32 m0, s49, 16384
ds_read_b128 a[64:67], v244 offset:0
v_mfma_f32_16x16x32_bf16 v[136:139], a[120:123], a[96:99], v[136:139]
buffer_load_dwordx4 v240, s[8:11], s45 offen offset:0 lds
v_mfma_f32_16x16x32_bf16 v[140:143], a[124:127], a[96:99], v[140:143]
ds_read_b64_tr_b16 a[84:85], v246 offset:0
ds_read_b64_tr_b16 a[86:87], v246 offset:2048
v_mfma_f32_16x16x32_bf16 v[144:147], a[112:115], a[100:103], v[144:147]
s_add_u32 s44, s44, 4096
ds_read_b128 a[68:71], v244 offset:256
v_mfma_f32_16x16x32_bf16 v[148:151], a[116:119], a[100:103], v[148:151]
ds_read_b64_tr_b16 a[88:89], v247 offset:0
ds_read_b64_tr_b16 a[90:91], v247 offset:2048
v_mfma_f32_16x16x32_bf16 v[152:155], a[120:123], a[100:103], v[152:155]
buffer_load_dwordx4 v241, s[8:11], s45 offen offset:1024 lds
v_mfma_f32_16x16x32_bf16 v[156:159], a[124:127], a[100:103], v[156:159]
ds_read_b128 a[72:75], v244 offset:4096
v_mfma_f32_16x16x32_bf16 v[160:163], a[112:115], a[104:107], v[160:163]
ds_read_b64_tr_b16 a[92:93], v248 offset:0
ds_read_b64_tr_b16 a[94:95], v248 offset:2048
v_mfma_f32_16x16x32_bf16 v[164:167], a[116:119], a[104:107], v[164:167]
ds_read_b128 a[76:79], v244 offset:4352
v_mfma_f32_16x16x32_bf16 v[168:171], a[120:123], a[104:107], v[168:171]
s_nop 0
buffer_load_dwordx4 v242, s[8:11], s45 offen offset:2048 lds
v_or_b32 v0, 5, v11
v_mfma_f32_16x16x32_bf16 v[172:175], a[124:127], a[104:107], v[172:175]
s_cmp_eq_u32 s46, 0
s_cselect_b32 s53, s56, s57
v_mfma_f32_16x16x32_bf16 v[176:179], a[112:115], a[108:111], v[176:179]
v_mfma_f32_16x16x32_bf16 v[180:183], a[116:119], a[108:111], v[180:183]
v_mfma_f32_16x16x32_bf16 v[184:187], a[120:123], a[108:111], v[184:187]
buffer_load_dwordx4 v243, s[8:11], s45 offen offset:3072 lds
buffer_store_dword v0, v6, s[60:63], 0 offen sc1
v_mfma_f32_16x16x32_bf16 v[188:191], a[124:127], a[108:111], v[188:191]
s_waitcnt lgkmcnt(0)
s_mov_b32 m0, s49
s_add_u32 s45, s45, 131072
v_mfma_f32_16x16x32_bf16 v[128:131], a[80:83], a[64:67], v[128:131]
ds_read_b64_tr_b16 a[112:113], v245 offset:8192
ds_read_b64_tr_b16 a[114:115], v245 offset:10240
v_mfma_f32_16x16x32_bf16 v[132:135], a[84:87], a[64:67], v[132:135]
ds_read_b128 a[96:99], v244 offset:2048
v_add_u32 v245, s53, v245
v_mfma_f32_16x16x32_bf16 v[136:139], a[88:91], a[64:67], v[136:139]
buffer_load_dwordx4 v7, s[4:7], s44 offen offset:0 sc1 lds
v_mfma_f32_16x16x32_bf16 v[140:143], a[92:95], a[64:67], v[140:143]
ds_read_b64_tr_b16 a[116:117], v246 offset:8192
ds_read_b64_tr_b16 a[118:119], v246 offset:10240
v_mfma_f32_16x16x32_bf16 v[144:147], a[80:83], a[68:71], v[144:147]
ds_read_b128 a[100:103], v244 offset:2304
v_add_u32 v246, s53, v246
v_mfma_f32_16x16x32_bf16 v[148:151], a[84:87], a[68:71], v[148:151]
ds_read_b64_tr_b16 a[120:121], v247 offset:8192
ds_read_b64_tr_b16 a[122:123], v247 offset:10240
v_mfma_f32_16x16x32_bf16 v[152:155], a[88:91], a[68:71], v[152:155]
buffer_load_dwordx4 v7, s[4:7], s44 offen offset:1024 sc1 lds
v_mfma_f32_16x16x32_bf16 v[156:159], a[92:95], a[68:71], v[156:159]
ds_read_b128 a[104:107], v244 offset:6144
v_add_u32 v247, s53, v247
v_mfma_f32_16x16x32_bf16 v[160:163], a[80:83], a[72:75], v[160:163]
ds_read_b64_tr_b16 a[124:125], v248 offset:8192
ds_read_b64_tr_b16 a[126:127], v248 offset:10240
v_mfma_f32_16x16x32_bf16 v[164:167], a[84:87], a[72:75], v[164:167]
ds_read_b128 a[108:111], v244 offset:6400
v_add_u32 v248, s53, v248
v_mfma_f32_16x16x32_bf16 v[168:171], a[88:91], a[72:75], v[168:171]
buffer_load_dwordx4 v7, s[4:7], s44 offen offset:2048 sc1 lds
v_mfma_f32_16x16x32_bf16 v[172:175], a[92:95], a[72:75], v[172:175]
s_add_u32 s49, s46, s91
v_add_u32 v244, s53, v244
v_mfma_f32_16x16x32_bf16 v[176:179], a[80:83], a[76:79], v[176:179]
s_add_u32 s46, s46, s53
v_mfma_f32_16x16x32_bf16 v[180:183], a[84:87], a[76:79], v[180:183]
v_mfma_f32_16x16x32_bf16 v[184:187], a[88:91], a[76:79], v[184:187]
buffer_load_dwordx4 v7, s[4:7], s44 offen offset:3072 sc1 lds
v_mfma_f32_16x16x32_bf16 v[188:191], a[92:95], a[76:79], v[188:191]
s_waitcnt vmcnt(25) lgkmcnt(0)
s_barrier
v_mfma_f32_16x16x32_bf16 v[128:131], a[112:115], a[96:99], v[128:131]
ds_read_b64_tr_b16 a[80:81], v245 offset:0
ds_read_b64_tr_b16 a[82:83], v245 offset:2048
v_mfma_f32_16x16x32_bf16 v[132:135], a[116:119], a[96:99], v[132:135]
s_add_u32 m0, s49, 16384
ds_read_b128 a[64:67], v244 offset:0
v_mfma_f32_16x16x32_bf16 v[136:139], a[120:123], a[96:99], v[136:139]
buffer_load_dwordx4 v240, s[8:11], s45 offen offset:0 lds
v_mfma_f32_16x16x32_bf16 v[140:143], a[124:127], a[96:99], v[140:143]
ds_read_b64_tr_b16 a[84:85], v246 offset:0
ds_read_b64_tr_b16 a[86:87], v246 offset:2048
v_mfma_f32_16x16x32_bf16 v[144:147], a[112:115], a[100:103], v[144:147]
s_add_u32 s44, s44, 4096
ds_read_b128 a[68:71], v244 offset:256
v_mfma_f32_16x16x32_bf16 v[148:151], a[116:119], a[100:103], v[148:151]
ds_read_b64_tr_b16 a[88:89], v247 offset:0
ds_read_b64_tr_b16 a[90:91], v247 offset:2048
v_mfma_f32_16x16x32_bf16 v[152:155], a[120:123], a[100:103], v[152:155]
buffer_load_dwordx4 v241, s[8:11], s45 offen offset:1024 lds
v_mfma_f32_16x16x32_bf16 v[156:159], a[124:127], a[100:103], v[156:159]
ds_read_b128 a[72:75], v244 offset:4096
v_mfma_f32_16x16x32_bf16 v[160:163], a[112:115], a[104:107], v[160:163]
ds_read_b64_tr_b16 a[92:93], v248 offset:0
ds_read_b64_tr_b16 a[94:95], v248 offset:2048
v_mfma_f32_16x16x32_bf16 v[164:167], a[116:119], a[104:107], v[164:167]
ds_read_b128 a[76:79], v244 offset:4352
v_mfma_f32_16x16x32_bf16 v[168:171], a[120:123], a[104:107], v[168:171]
buffer_load_dwordx4 v242, s[8:11], s45 offen offset:2048 lds
v_mfma_f32_16x16x32_bf16 v[172:175], a[124:127], a[104:107], v[172:175]
s_cmp_eq_u32 s46, 0
s_cselect_b32 s53, s56, s57
v_mfma_f32_16x16x32_bf16 v[176:179], a[112:115], a[108:111], v[176:179]
v_mfma_f32_16x16x32_bf16 v[180:183], a[116:119], a[108:111], v[180:183]
v_mfma_f32_16x16x32_bf16 v[184:187], a[120:123], a[108:111], v[184:187]
buffer_load_dwordx4 v243, s[8:11], s45 offen offset:3072 lds
v_mfma_f32_16x16x32_bf16 v[188:191], a[124:127], a[108:111], v[188:191]
s_waitcnt lgkmcnt(0)
s_mov_b32 m0, s49
s_add_u32 s45, s45, 131072
v_mfma_f32_16x16x32_bf16 v[128:131], a[80:83], a[64:67], v[128:131]
ds_read_b64_tr_b16 a[112:113], v245 offset:8192
ds_read_b64_tr_b16 a[114:115], v245 offset:10240
v_mfma_f32_16x16x32_bf16 v[132:135], a[84:87], a[64:67], v[132:135]
ds_read_b128 a[96:99], v244 offset:2048
v_add_u32 v245, s53, v245
v_mfma_f32_16x16x32_bf16 v[136:139], a[88:91], a[64:67], v[136:139]
buffer_load_dwordx4 v7, s[4:7], s44 offen offset:0 sc1 lds
v_mfma_f32_16x16x32_bf16 v[140:143], a[92:95], a[64:67], v[140:143]
ds_read_b64_tr_b16 a[116:117], v246 offset:8192
ds_read_b64_tr_b16 a[118:119], v246 offset:10240
v_mfma_f32_16x16x32_bf16 v[144:147], a[80:83], a[68:71], v[144:147]
ds_read_b128 a[100:103], v244 offset:2304
v_add_u32 v246, s53, v246
v_mfma_f32_16x16x32_bf16 v[148:151], a[84:87], a[68:71], v[148:151]
ds_read_b64_tr_b16 a[120:121], v247 offset:8192
ds_read_b64_tr_b16 a[122:123], v247 offset:10240
v_mfma_f32_16x16x32_bf16 v[152:155], a[88:91], a[68:71], v[152:155]
buffer_load_dwordx4 v7, s[4:7], s44 offen offset:1024 sc1 lds
v_mfma_f32_16x16x32_bf16 v[156:159], a[92:95], a[68:71], v[156:159]
ds_read_b128 a[104:107], v244 offset:6144
v_add_u32 v247, s53, v247
v_mfma_f32_16x16x32_bf16 v[160:163], a[80:83], a[72:75], v[160:163]
ds_read_b64_tr_b16 a[124:125], v248 offset:8192
ds_read_b64_tr_b16 a[126:127], v248 offset:10240
v_mfma_f32_16x16x32_bf16 v[164:167], a[84:87], a[72:75], v[164:167]
ds_read_b128 a[108:111], v244 offset:6400
v_add_u32 v248, s53, v248
v_mfma_f32_16x16x32_bf16 v[168:171], a[88:91], a[72:75], v[168:171]
buffer_load_dwordx4 v7, s[4:7], s44 offen offset:2048 sc1 lds
v_mfma_f32_16x16x32_bf16 v[172:175], a[92:95], a[72:75], v[172:175]
s_add_u32 s49, s46, s91
v_add_u32 v244, s53, v244
v_mfma_f32_16x16x32_bf16 v[176:179], a[80:83], a[76:79], v[176:179]
s_add_u32 s46, s46, s53
v_mfma_f32_16x16x32_bf16 v[180:183], a[84:87], a[76:79], v[180:183]
v_mfma_f32_16x16x32_bf16 v[184:187], a[88:91], a[76:79], v[184:187]
buffer_load_dwordx4 v7, s[4:7], s44 offen offset:3072 sc1 lds
v_mfma_f32_16x16x32_bf16 v[188:191], a[92:95], a[76:79], v[188:191]
s_waitcnt vmcnt(25) lgkmcnt(0)
s_barrier
v_mfma_f32_16x16x32_bf16 v[128:131], a[112:115], a[96:99], v[128:131]
ds_read_b64_tr_b16 a[80:81], v245 offset:0
ds_read_b64_tr_b16 a[82:83], v245 offset:2048
v_mfma_f32_16x16x32_bf16 v[132:135], a[116:119], a[96:99], v[132:135]
s_add_u32 m0, s49, 16384
ds_read_b128 a[64:67], v244 offset:0
v_mfma_f32_16x16x32_bf16 v[136:139], a[120:123], a[96:99], v[136:139]
buffer_load_dwordx4 v240, s[8:11], s45 offen offset:0 lds
v_mfma_f32_16x16x32_bf16 v[140:143], a[124:127], a[96:99], v[140:143]
ds_read_b64_tr_b16 a[84:85], v246 offset:0
ds_read_b64_tr_b16 a[86:87], v246 offset:2048
v_mfma_f32_16x16x32_bf16 v[144:147], a[112:115], a[100:103], v[144:147]
s_add_u32 s44, s44, 4096
ds_read_b128 a[68:71], v244 offset:256
v_mfma_f32_16x16x32_bf16 v[148:151], a[116:119], a[100:103], v[148:151]
ds_read_b64_tr_b16 a[88:89], v247 offset:0
ds_read_b64_tr_b16 a[90:91], v247 offset:2048
v_mfma_f32_16x16x32_bf16 v[152:155], a[120:123], a[100:103], v[152:155]
buffer_load_dwordx4 v241, s[8:11], s45 offen offset:1024 lds
v_mfma_f32_16x16x32_bf16 v[156:159], a[124:127], a[100:103], v[156:159]
ds_read_b128 a[72:75], v244 offset:4096
v_mfma_f32_16x16x32_bf16 v[160:163], a[112:115], a[104:107], v[160:163]
ds_read_b64_tr_b16 a[92:93], v248 offset:0
ds_read_b64_tr_b16 a[94:95], v248 offset:2048
v_mfma_f32_16x16x32_bf16 v[164:167], a[116:119], a[104:107], v[164:167]
ds_read_b128 a[76:79], v244 offset:4352
v_mfma_f32_16x16x32_bf16 v[168:171], a[120:123], a[104:107], v[168:171]
buffer_load_dwordx4 v242, s[8:11], s45 offen offset:2048 lds
v_mfma_f32_16x16x32_bf16 v[172:175], a[124:127], a[104:107], v[172:175]
s_cmp_eq_u32 s46, 0
s_cselect_b32 s53, s56, s57
v_mfma_f32_16x16x32_bf16 v[176:179], a[112:115], a[108:111], v[176:179]
v_mfma_f32_16x16x32_bf16 v[180:183], a[116:119], a[108:111], v[180:183]
v_mfma_f32_16x16x32_bf16 v[184:187], a[120:123], a[108:111], v[184:187]
buffer_load_dwordx4 v243, s[8:11], s45 offen offset:3072 lds
v_mfma_f32_16x16x32_bf16 v[188:191], a[124:127], a[108:111], v[188:191]
s_waitcnt lgkmcnt(0)
s_mov_b32 m0, s49
s_add_u32 s45, s45, 131072
v_mfma_f32_16x16x32_bf16 v[128:131], a[80:83], a[64:67], v[128:131]
ds_read_b64_tr_b16 a[112:113], v245 offset:8192
ds_read_b64_tr_b16 a[114:115], v245 offset:10240
v_mfma_f32_16x16x32_bf16 v[132:135], a[84:87], a[64:67], v[132:135]
ds_read_b128 a[96:99], v244 offset:2048
v_add_u32 v245, s53, v245
v_mfma_f32_16x16x32_bf16 v[136:139], a[88:91], a[64:67], v[136:139]
buffer_load_dwordx4 v7, s[4:7], s44 offen offset:0 sc1 lds
v_mfma_f32_16x16x32_bf16 v[140:143], a[92:95], a[64:67], v[140:143]
ds_read_b64_tr_b16 a[116:117], v246 offset:8192
ds_read_b64_tr_b16 a[118:119], v246 offset:10240
v_mfma_f32_16x16x32_bf16 v[144:147], a[80:83], a[68:71], v[144:147]
ds_read_b128 a[100:103], v244 offset:2304
v_add_u32 v246, s53, v246
v_mfma_f32_16x16x32_bf16 v[148:151], a[84:87], a[68:71], v[148:151]
ds_read_b64_tr_b16 a[120:121], v247 offset:8192
ds_read_b64_tr_b16 a[122:123], v247 offset:10240
v_mfma_f32_16x16x32_bf16 v[152:155], a[88:91], a[68:71], v[152:155]
buffer_load_dwordx4 v7, s[4:7], s44 offen offset:1024 sc1 lds
v_mfma_f32_16x16x32_bf16 v[156:159], a[92:95], a[68:71], v[156:159]
ds_read_b128 a[104:107], v244 offset:6144
v_add_u32 v247, s53, v247
v_mfma_f32_16x16x32_bf16 v[160:163], a[80:83], a[72:75], v[160:163]
ds_read_b64_tr_b16 a[124:125], v248 offset:8192
ds_read_b64_tr_b16 a[126:127], v248 offset:10240
v_mfma_f32_16x16x32_bf16 v[164:167], a[84:87], a[72:75], v[164:167]
ds_read_b128 a[108:111], v244 offset:6400
v_add_u32 v248, s53, v248
v_mfma_f32_16x16x32_bf16 v[168:171], a[88:91], a[72:75], v[168:171]
buffer_load_dwordx4 v7, s[4:7], s44 offen offset:2048 sc1 lds
v_mfma_f32_16x16x32_bf16 v[172:175], a[92:95], a[72:75], v[172:175]
s_add_u32 s49, s46, s91
v_add_u32 v244, s53, v244
v_mfma_f32_16x16x32_bf16 v[176:179], a[80:83], a[76:79], v[176:179]
s_add_u32 s46, s46, s53
v_mfma_f32_16x16x32_bf16 v[180:183], a[84:87], a[76:79], v[180:183]
v_mfma_f32_16x16x32_bf16 v[184:187], a[88:91], a[76:79], v[184:187]
buffer_load_dwordx4 v7, s[4:7], s44 offen offset:3072 sc1 lds
v_mfma_f32_16x16x32_bf16 v[188:191], a[92:95], a[76:79], v[188:191]
s_waitcnt vmcnt(25) lgkmcnt(0)
s_barrier
v_mfma_f32_16x16x32_bf16 v[128:131], a[112:115], a[96:99], v[128:131]
ds_read_b64_tr_b16 a[80:81], v245 offset:0
ds_read_b64_tr_b16 a[82:83], v245 offset:2048
v_mfma_f32_16x16x32_bf16 v[132:135], a[116:119], a[96:99], v[132:135]
s_add_u32 m0, s49, 16384
ds_read_b128 a[64:67], v244 offset:0
v_mfma_f32_16x16x32_bf16 v[136:139], a[120:123], a[96:99], v[136:139]
buffer_load_dwordx4 v240, s[8:11], s45 offen offset:0 lds
v_mfma_f32_16x16x32_bf16 v[140:143], a[124:127], a[96:99], v[140:143]
ds_read_b64_tr_b16 a[84:85], v246 offset:0
ds_read_b64_tr_b16 a[86:87], v246 offset:2048
v_mfma_f32_16x16x32_bf16 v[144:147], a[112:115], a[100:103], v[144:147]
s_add_u32 s44, s44, 4096
ds_read_b128 a[68:71], v244 offset:256
v_mfma_f32_16x16x32_bf16 v[148:151], a[116:119], a[100:103], v[148:151]
ds_read_b64_tr_b16 a[88:89], v247 offset:0
ds_read_b64_tr_b16 a[90:91], v247 offset:2048
v_mfma_f32_16x16x32_bf16 v[152:155], a[120:123], a[100:103], v[152:155]
buffer_load_dwordx4 v241, s[8:11], s45 offen offset:1024 lds
v_mfma_f32_16x16x32_bf16 v[156:159], a[124:127], a[100:103], v[156:159]
ds_read_b128 a[72:75], v244 offset:4096
v_mfma_f32_16x16x32_bf16 v[160:163], a[112:115], a[104:107], v[160:163]
ds_read_b64_tr_b16 a[92:93], v248 offset:0
ds_read_b64_tr_b16 a[94:95], v248 offset:2048
v_mfma_f32_16x16x32_bf16 v[164:167], a[116:119], a[104:107], v[164:167]
ds_read_b128 a[76:79], v244 offset:4352
v_mfma_f32_16x16x32_bf16 v[168:171], a[120:123], a[104:107], v[168:171]
buffer_load_dwordx4 v242, s[8:11], s45 offen offset:2048 lds
v_mfma_f32_16x16x32_bf16 v[172:175], a[124:127], a[104:107], v[172:175]
s_cmp_eq_u32 s46, 0
s_cselect_b32 s53, s56, s57
v_mfma_f32_16x16x32_bf16 v[176:179], a[112:115], a[108:111], v[176:179]
v_mfma_f32_16x16x32_bf16 v[180:183], a[116:119], a[108:111], v[180:183]
v_mfma_f32_16x16x32_bf16 v[184:187], a[120:123], a[108:111], v[184:187]
buffer_load_dwordx4 v243, s[8:11], s45 offen offset:3072 lds
v_mfma_f32_16x16x32_bf16 v[188:191], a[124:127], a[108:111], v[188:191]
s_waitcnt lgkmcnt(0)
s_mov_b32 m0, s49
s_add_u32 s45, s45, 131072
v_mfma_f32_16x16x32_bf16 v[128:131], a[80:83], a[64:67], v[128:131]
ds_read_b64_tr_b16 a[112:113], v245 offset:8192
ds_read_b64_tr_b16 a[114:115], v245 offset:10240
v_mfma_f32_16x16x32_bf16 v[132:135], a[84:87], a[64:67], v[132:135]
ds_read_b128 a[96:99], v244 offset:2048
v_add_u32 v245, s53, v245
v_mfma_f32_16x16x32_bf16 v[136:139], a[88:91], a[64:67], v[136:139]
buffer_load_dwordx4 v7, s[4:7], s44 offen offset:0 sc1 lds
v_mfma_f32_16x16x32_bf16 v[140:143], a[92:95], a[64:67], v[140:143]
ds_read_b64_tr_b16 a[116:117], v246 offset:8192
ds_read_b64_tr_b16 a[118:119], v246 offset:10240
v_mfma_f32_16x16x32_bf16 v[144:147], a[80:83], a[68:71], v[144:147]
ds_read_b128 a[100:103], v244 offset:2304
v_add_u32 v246, s53, v246
v_mfma_f32_16x16x32_bf16 v[148:151], a[84:87], a[68:71], v[148:151]
ds_read_b64_tr_b16 a[120:121], v247 offset:8192
ds_read_b64_tr_b16 a[122:123], v247 offset:10240
v_mfma_f32_16x16x32_bf16 v[152:155], a[88:91], a[68:71], v[152:155]
buffer_load_dwordx4 v7, s[4:7], s44 offen offset:1024 sc1 lds
v_mfma_f32_16x16x32_bf16 v[156:159], a[92:95], a[68:71], v[156:159]
ds_read_b128 a[104:107], v244 offset:6144
v_add_u32 v247, s53, v247
v_mfma_f32_16x16x32_bf16 v[160:163], a[80:83], a[72:75], v[160:163]
ds_read_b64_tr_b16 a[124:125], v248 offset:8192
ds_read_b64_tr_b16 a[126:127], v248 offset:10240
v_mfma_f32_16x16x32_bf16 v[164:167], a[84:87], a[72:75], v[164:167]
ds_read_b128 a[108:111], v244 offset:6400
v_add_u32 v248, s53, v248
v_mfma_f32_16x16x32_bf16 v[168:171], a[88:91], a[72:75], v[168:171]
buffer_load_dwordx4 v7, s[4:7], s44 offen offset:2048 sc1 lds
v_mfma_f32_16x16x32_bf16 v[172:175], a[92:95], a[72:75], v[172:175]
s_add_u32 s49, s46, s91
v_add_u32 v244, s53, v244
v_mfma_f32_16x16x32_bf16 v[176:179], a[80:83], a[76:79], v[176:179]
s_add_u32 s46, s46, s53
v_mfma_f32_16x16x32_bf16 v[180:183], a[84:87], a[76:79], v[180:183]
v_mfma_f32_16x16x32_bf16 v[184:187], a[88:91], a[76:79], v[184:187]
buffer_load_dwordx4 v7, s[4:7], s44 offen offset:3072 sc1 lds
v_mfma_f32_16x16x32_bf16 v[188:191], a[92:95], a[76:79], v[188:191]
s_waitcnt vmcnt(24) lgkmcnt(0)
s_barrier
v_mfma_f32_16x16x32_bf16 v[128:131], a[112:115], a[96:99], v[128:131]
ds_read_b64_tr_b16 a[80:81], v245 offset:0
ds_read_b64_tr_b16 a[82:83], v245 offset:2048
v_mfma_f32_16x16x32_bf16 v[132:135], a[116:119], a[96:99], v[132:135]
s_add_u32 m0, s49, 16384
ds_read_b128 a[64:67], v244 offset:0
v_mfma_f32_16x16x32_bf16 v[136:139], a[120:123], a[96:99], v[136:139]
buffer_load_dwordx4 v240, s[8:11], s45 offen offset:0 lds
v_mfma_f32_16x16x32_bf16 v[140:143], a[124:127], a[96:99], v[140:143]
ds_read_b64_tr_b16 a[84:85], v246 offset:0
ds_read_b64_tr_b16 a[86:87], v246 offset:2048
v_mfma_f32_16x16x32_bf16 v[144:147], a[112:115], a[100:103], v[144:147]
s_add_u32 s44, s44, 4096
ds_read_b128 a[68:71], v244 offset:256
v_mfma_f32_16x16x32_bf16 v[148:151], a[116:119], a[100:103], v[148:151]
ds_read_b64_tr_b16 a[88:89], v247 offset:0
ds_read_b64_tr_b16 a[90:91], v247 offset:2048
v_mfma_f32_16x16x32_bf16 v[152:155], a[120:123], a[100:103], v[152:155]
buffer_load_dwordx4 v241, s[8:11], s45 offen offset:1024 lds
v_mfma_f32_16x16x32_bf16 v[156:159], a[124:127], a[100:103], v[156:159]
ds_read_b128 a[72:75], v244 offset:4096
v_mfma_f32_16x16x32_bf16 v[160:163], a[112:115], a[104:107], v[160:163]
ds_read_b64_tr_b16 a[92:93], v248 offset:0
ds_read_b64_tr_b16 a[94:95], v248 offset:2048
v_mfma_f32_16x16x32_bf16 v[164:167], a[116:119], a[104:107], v[164:167]
ds_read_b128 a[76:79], v244 offset:4352
v_mfma_f32_16x16x32_bf16 v[168:171], a[120:123], a[104:107], v[168:171]
buffer_load_dwordx4 v242, s[8:11], s45 offen offset:2048 lds
v_mfma_f32_16x16x32_bf16 v[172:175], a[124:127], a[104:107], v[172:175]
s_cmp_eq_u32 s46, 0
s_cselect_b32 s53, s56, s57
v_mfma_f32_16x16x32_bf16 v[176:179], a[112:115], a[108:111], v[176:179]
v_mfma_f32_16x16x32_bf16 v[180:183], a[116:119], a[108:111], v[180:183]
v_mfma_f32_16x16x32_bf16 v[184:187], a[120:123], a[108:111], v[184:187]
buffer_load_dwordx4 v243, s[8:11], s45 offen offset:3072 lds
v_mfma_f32_16x16x32_bf16 v[188:191], a[124:127], a[108:111], v[188:191]
s_waitcnt lgkmcnt(0)
s_mov_b32 m0, s49
s_add_u32 s45, s45, 131072
v_mfma_f32_16x16x32_bf16 v[128:131], a[80:83], a[64:67], v[128:131]
ds_read_b64_tr_b16 a[112:113], v245 offset:8192
ds_read_b64_tr_b16 a[114:115], v245 offset:10240
v_mfma_f32_16x16x32_bf16 v[132:135], a[84:87], a[64:67], v[132:135]
ds_read_b128 a[96:99], v244 offset:2048
v_add_u32 v245, s53, v245
v_mfma_f32_16x16x32_bf16 v[136:139], a[88:91], a[64:67], v[136:139]
buffer_load_dwordx4 v7, s[4:7], s44 offen offset:0 sc1 lds
v_mfma_f32_16x16x32_bf16 v[140:143], a[92:95], a[64:67], v[140:143]
ds_read_b64_tr_b16 a[116:117], v246 offset:8192
ds_read_b64_tr_b16 a[118:119], v246 offset:10240
v_mfma_f32_16x16x32_bf16 v[144:147], a[80:83], a[68:71], v[144:147]
ds_read_b128 a[100:103], v244 offset:2304
v_add_u32 v246, s53, v246
v_mfma_f32_16x16x32_bf16 v[148:151], a[84:87], a[68:71], v[148:151]
ds_read_b64_tr_b16 a[120:121], v247 offset:8192
ds_read_b64_tr_b16 a[122:123], v247 offset:10240
v_mfma_f32_16x16x32_bf16 v[152:155], a[88:91], a[68:71], v[152:155]
buffer_load_dwordx4 v7, s[4:7], s44 offen offset:1024 sc1 lds
v_mfma_f32_16x16x32_bf16 v[156:159], a[92:95], a[68:71], v[156:159]
ds_read_b128 a[104:107], v244 offset:6144
v_add_u32 v247, s53, v247
v_mfma_f32_16x16x32_bf16 v[160:163], a[80:83], a[72:75], v[160:163]
ds_read_b64_tr_b16 a[124:125], v248 offset:8192
ds_read_b64_tr_b16 a[126:127], v248 offset:10240
v_mfma_f32_16x16x32_bf16 v[164:167], a[84:87], a[72:75], v[164:167]
ds_read_b128 a[108:111], v244 offset:6400
v_add_u32 v248, s53, v248
v_mfma_f32_16x16x32_bf16 v[168:171], a[88:91], a[72:75], v[168:171]
buffer_load_dwordx4 v7, s[4:7], s44 offen offset:2048 sc1 lds
v_mfma_f32_16x16x32_bf16 v[172:175], a[92:95], a[72:75], v[172:175]
s_add_u32 s49, s46, s91
v_add_u32 v244, s53, v244
v_mfma_f32_16x16x32_bf16 v[176:179], a[80:83], a[76:79], v[176:179]
s_add_u32 s46, s46, s53
v_mfma_f32_16x16x32_bf16 v[180:183], a[84:87], a[76:79], v[180:183]
.Lpad23:
s_nop 0
v_mfma_f32_16x16x32_bf16 v[184:187], a[88:91], a[76:79], v[184:187]
buffer_load_dwordx4 v7, s[4:7], s44 offen offset:3072 sc1 lds
v_mfma_f32_16x16x32_bf16 v[188:191], a[92:95], a[76:79], v[188:191]
s_waitcnt vmcnt(24) lgkmcnt(0)
s_barrier
v_mfma_f32_16x16x32_bf16 v[128:131], a[112:115], a[96:99], v[128:131]
ds_read_b64_tr_b16 a[80:81], v245 offset:0
ds_read_b64_tr_b16 a[82:83], v245 offset:2048
v_mfma_f32_16x16x32_bf16 v[132:135], a[116:119], a[96:99], v[132:135]
ds_read_b128 a[64:67], v244 offset:0
v_mfma_f32_16x16x32_bf16 v[136:139], a[120:123], a[96:99], v[136:139]
v_mfma_f32_16x16x32_bf16 v[140:143], a[124:127], a[96:99], v[140:143]
ds_read_b64_tr_b16 a[84:85], v246 offset:0
ds_read_b64_tr_b16 a[86:87], v246 offset:2048
v_mfma_f32_16x16x32_bf16 v[144:147], a[112:115], a[100:103], v[144:147]
ds_read_b128 a[68:71], v244 offset:256
v_mfma_f32_16x16x32_bf16 v[148:151], a[116:119], a[100:103], v[148:151]
ds_read_b64_tr_b16 a[88:89], v247 offset:0
ds_read_b64_tr_b16 a[90:91], v247 offset:2048
v_mfma_f32_16x16x32_bf16 v[152:155], a[120:123], a[100:103], v[152:155]
v_mfma_f32_16x16x32_bf16 v[156:159], a[124:127], a[100:103], v[156:159]
ds_read_b128 a[72:75], v244 offset:4096
v_mfma_f32_16x16x32_bf16 v[160:163], a[112:115], a[104:107], v[160:163]
ds_read_b64_tr_b16 a[92:93], v248 offset:0
ds_read_b64_tr_b16 a[94:95], v248 offset:2048
v_mfma_f32_16x16x32_bf16 v[164:167], a[116:119], a[104:107], v[164:167]
ds_read_b128 a[76:79], v244 offset:4352
v_mfma_f32_16x16x32_bf16 v[168:171], a[120:123], a[104:107], v[168:171]
v_mfma_f32_16x16x32_bf16 v[172:175], a[124:127], a[104:107], v[172:175]
s_cmp_eq_u32 s46, 0
s_cselect_b32 s53, s56, s57
v_mfma_f32_16x16x32_bf16 v[176:179], a[112:115], a[108:111], v[176:179]
v_mfma_f32_16x16x32_bf16 v[180:183], a[116:119], a[108:111], v[180:183]
v_mfma_f32_16x16x32_bf16 v[184:187], a[120:123], a[108:111], v[184:187]
v_mfma_f32_16x16x32_bf16 v[188:191], a[124:127], a[108:111], v[188:191]
s_waitcnt lgkmcnt(0)
v_mfma_f32_16x16x32_bf16 v[128:131], a[80:83], a[64:67], v[128:131]
ds_read_b64_tr_b16 a[112:113], v245 offset:8192
ds_read_b64_tr_b16 a[114:115], v245 offset:10240
v_mfma_f32_16x16x32_bf16 v[132:135], a[84:87], a[64:67], v[132:135]
ds_read_b128 a[96:99], v244 offset:2048
v_add_u32 v245, s53, v245
v_mfma_f32_16x16x32_bf16 v[136:139], a[88:91], a[64:67], v[136:139]
v_mfma_f32_16x16x32_bf16 v[140:143], a[92:95], a[64:67], v[140:143]
ds_read_b64_tr_b16 a[116:117], v246 offset:8192
ds_read_b64_tr_b16 a[118:119], v246 offset:10240
v_mfma_f32_16x16x32_bf16 v[144:147], a[80:83], a[68:71], v[144:147]
ds_read_b128 a[100:103], v244 offset:2304
v_add_u32 v246, s53, v246
v_mfma_f32_16x16x32_bf16 v[148:151], a[84:87], a[68:71], v[148:151]
ds_read_b64_tr_b16 a[120:121], v247 offset:8192
ds_read_b64_tr_b16 a[122:123], v247 offset:10240
v_mfma_f32_16x16x32_bf16 v[152:155], a[88:91], a[68:71], v[152:155]
v_mfma_f32_16x16x32_bf16 v[156:159], a[92:95], a[68:71], v[156:159]
ds_read_b128 a[104:107], v244 offset:6144
v_add_u32 v247, s53, v247
v_mfma_f32_16x16x32_bf16 v[160:163], a[80:83], a[72:75], v[160:163]
ds_read_b64_tr_b16 a[124:125], v248 offset:8192
ds_read_b64_tr_b16 a[126:127], v248 offset:10240
v_mfma_f32_16x16x32_bf16 v[164:167], a[84:87], a[72:75], v[164:167]
ds_read_b128 a[108:111], v244 offset:6400
v_add_u32 v248, s53, v248
v_mfma_f32_16x16x32_bf16 v[168:171], a[88:91], a[72:75], v[168:171]
v_mfma_f32_16x16x32_bf16 v[172:175], a[92:95], a[72:75], v[172:175]
s_add_u32 s49, s46, s91
v_add_u32 v244, s53, v244
v_mfma_f32_16x16x32_bf16 v[176:179], a[80:83], a[76:79], v[176:179]
s_add_u32 s46, s46, s53
v_mfma_f32_16x16x32_bf16 v[180:183], a[84:87], a[76:79], v[180:183]
v_mfma_f32_16x16x32_bf16 v[184:187], a[88:91], a[76:79], v[184:187]
v_mfma_f32_16x16x32_bf16 v[188:191], a[92:95], a[76:79], v[188:191]
s_waitcnt vmcnt(16) lgkmcnt(0)
s_barrier
v_mfma_f32_16x16x32_bf16 v[128:131], a[112:115], a[96:99], v[128:131]
ds_read_b64_tr_b16 a[80:81], v245 offset:0
ds_read_b64_tr_b16 a[82:83], v245 offset:2048
v_mfma_f32_16x16x32_bf16 v[132:135], a[116:119], a[96:99], v[132:135]
ds_read_b128 a[64:67], v244 offset:0
v_mfma_f32_16x16x32_bf16 v[136:139], a[120:123], a[96:99], v[136:139]
v_mfma_f32_16x16x32_bf16 v[140:143], a[124:127], a[96:99], v[140:143]
ds_read_b64_tr_b16 a[84:85], v246 offset:0
ds_read_b64_tr_b16 a[86:87], v246 offset:2048
v_mfma_f32_16x16x32_bf16 v[144:147], a[112:115], a[100:103], v[144:147]
ds_read_b128 a[68:71], v244 offset:256
v_mfma_f32_16x16x32_bf16 v[148:151], a[116:119], a[100:103], v[148:151]
ds_read_b64_tr_b16 a[88:89], v247 offset:0
ds_read_b64_tr_b16 a[90:91], v247 offset:2048
v_mfma_f32_16x16x32_bf16 v[152:155], a[120:123], a[100:103], v[152:155]
v_mfma_f32_16x16x32_bf16 v[156:159], a[124:127], a[100:103], v[156:159]
ds_read_b128 a[72:75], v244 offset:4096
v_mfma_f32_16x16x32_bf16 v[160:163], a[112:115], a[104:107], v[160:163]
ds_read_b64_tr_b16 a[92:93], v248 offset:0
ds_read_b64_tr_b16 a[94:95], v248 offset:2048
v_mfma_f32_16x16x32_bf16 v[164:167], a[116:119], a[104:107], v[164:167]
ds_read_b128 a[76:79], v244 offset:4352
v_mfma_f32_16x16x32_bf16 v[168:171], a[120:123], a[104:107], v[168:171]
v_mfma_f32_16x16x32_bf16 v[172:175], a[124:127], a[104:107], v[172:175]
s_cmp_eq_u32 s46, 0
s_cselect_b32 s53, s56, s57
v_mfma_f32_16x16x32_bf16 v[176:179], a[112:115], a[108:111], v[176:179]
v_mfma_f32_16x16x32_bf16 v[180:183], a[116:119], a[108:111], v[180:183]
v_mfma_f32_16x16x32_bf16 v[184:187], a[120:123], a[108:111], v[184:187]
v_mfma_f32_16x16x32_bf16 v[188:191], a[124:127], a[108:111], v[188:191]
s_waitcnt lgkmcnt(0)
v_mfma_f32_16x16x32_bf16 v[128:131], a[80:83], a[64:67], v[128:131]
ds_read_b64_tr_b16 a[112:113], v245 offset:8192
ds_read_b64_tr_b16 a[114:115], v245 offset:10240
v_mfma_f32_16x16x32_bf16 v[132:135], a[84:87], a[64:67], v[132:135]
ds_read_b128 a[96:99], v244 offset:2048
v_add_u32 v245, s53, v245
v_mfma_f32_16x16x32_bf16 v[136:139], a[88:91], a[64:67], v[136:139]
v_mfma_f32_16x16x32_bf16 v[140:143], a[92:95], a[64:67], v[140:143]
ds_read_b64_tr_b16 a[116:117], v246 offset:8192
ds_read_b64_tr_b16 a[118:119], v246 offset:10240
v_mfma_f32_16x16x32_bf16 v[144:147], a[80:83], a[68:71], v[144:147]
ds_read_b128 a[100:103], v244 offset:2304
v_add_u32 v246, s53, v246
v_mfma_f32_16x16x32_bf16 v[148:151], a[84:87], a[68:71], v[148:151]
ds_read_b64_tr_b16 a[120:121], v247 offset:8192
ds_read_b64_tr_b16 a[122:123], v247 offset:10240
v_mfma_f32_16x16x32_bf16 v[152:155], a[88:91], a[68:71], v[152:155]
v_mfma_f32_16x16x32_bf16 v[156:159], a[92:95], a[68:71], v[156:159]
ds_read_b128 a[104:107], v244 offset:6144
v_add_u32 v247, s53, v247
v_mfma_f32_16x16x32_bf16 v[160:163], a[80:83], a[72:75], v[160:163]
ds_read_b64_tr_b16 a[124:125], v248 offset:8192
ds_read_b64_tr_b16 a[126:127], v248 offset:10240
v_mfma_f32_16x16x32_bf16 v[164:167], a[84:87], a[72:75], v[164:167]
ds_read_b128 a[108:111], v244 offset:6400
v_add_u32 v248, s53, v248
v_mfma_f32_16x16x32_bf16 v[168:171], a[88:91], a[72:75], v[168:171]
v_mfma_f32_16x16x32_bf16 v[172:175], a[92:95], a[72:75], v[172:175]
s_add_u32 s49, s46, s91
v_add_u32 v244, s53, v244
v_mfma_f32_16x16x32_bf16 v[176:179], a[80:83], a[76:79], v[176:179]
s_add_u32 s46, s46, s53
v_mfma_f32_16x16x32_bf16 v[180:183], a[84:87], a[76:79], v[180:183]
v_mfma_f32_16x16x32_bf16 v[184:187], a[88:91], a[76:79], v[184:187]
v_mfma_f32_16x16x32_bf16 v[188:191], a[92:95], a[76:79], v[188:191]
s_waitcnt vmcnt(8) lgkmcnt(0)
s_barrier
v_mfma_f32_16x16x32_bf16 v[128:131], a[112:115], a[96:99], v[128:131]
ds_read_b64_tr_b16 a[80:81], v245 offset:0
ds_read_b64_tr_b16 a[82:83], v245 offset:2048
v_mfma_f32_16x16x32_bf16 v[132:135], a[116:119], a[96:99], v[132:135]
ds_read_b128 a[64:67], v244 offset:0
v_mfma_f32_16x16x32_bf16 v[136:139], a[120:123], a[96:99], v[136:139]
v_mfma_f32_16x16x32_bf16 v[140:143], a[124:127], a[96:99], v[140:143]
ds_read_b64_tr_b16 a[84:85], v246 offset:0
ds_read_b64_tr_b16 a[86:87], v246 offset:2048
v_mfma_f32_16x16x32_bf16 v[144:147], a[112:115], a[100:103], v[144:147]
ds_read_b128 a[68:71], v244 offset:256
v_mfma_f32_16x16x32_bf16 v[148:151], a[116:119], a[100:103], v[148:151]
ds_read_b64_tr_b16 a[88:89], v247 offset:0
ds_read_b64_tr_b16 a[90:91], v247 offset:2048
v_mfma_f32_16x16x32_bf16 v[152:155], a[120:123], a[100:103], v[152:155]
v_mfma_f32_16x16x32_bf16 v[156:159], a[124:127], a[100:103], v[156:159]
ds_read_b128 a[72:75], v244 offset:4096
v_mfma_f32_16x16x32_bf16 v[160:163], a[112:115], a[104:107], v[160:163]
ds_read_b64_tr_b16 a[92:93], v248 offset:0
ds_read_b64_tr_b16 a[94:95], v248 offset:2048
v_mfma_f32_16x16x32_bf16 v[164:167], a[116:119], a[104:107], v[164:167]
ds_read_b128 a[76:79], v244 offset:4352
v_mfma_f32_16x16x32_bf16 v[168:171], a[120:123], a[104:107], v[168:171]
v_mfma_f32_16x16x32_bf16 v[172:175], a[124:127], a[104:107], v[172:175]
s_cmp_eq_u32 s46, 0
s_cselect_b32 s53, s56, s57
v_mfma_f32_16x16x32_bf16 v[176:179], a[112:115], a[108:111], v[176:179]
v_mfma_f32_16x16x32_bf16 v[180:183], a[116:119], a[108:111], v[180:183]
v_mfma_f32_16x16x32_bf16 v[184:187], a[120:123], a[108:111], v[184:187]
v_mfma_f32_16x16x32_bf16 v[188:191], a[124:127], a[108:111], v[188:191]
s_waitcnt lgkmcnt(0)
v_mfma_f32_16x16x32_bf16 v[128:131], a[80:83], a[64:67], v[128:131]
ds_read_b64_tr_b16 a[112:113], v245 offset:8192
ds_read_b64_tr_b16 a[114:115], v245 offset:10240
v_mfma_f32_16x16x32_bf16 v[132:135], a[84:87], a[64:67], v[132:135]
ds_read_b128 a[96:99], v244 offset:2048
v_add_u32 v245, s53, v245
v_mfma_f32_16x16x32_bf16 v[136:139], a[88:91], a[64:67], v[136:139]
v_mfma_f32_16x16x32_bf16 v[140:143], a[92:95], a[64:67], v[140:143]
ds_read_b64_tr_b16 a[116:117], v246 offset:8192
ds_read_b64_tr_b16 a[118:119], v246 offset:10240
v_mfma_f32_16x16x32_bf16 v[144:147], a[80:83], a[68:71], v[144:147]
ds_read_b128 a[100:103], v244 offset:2304
v_add_u32 v246, s53, v246
v_mfma_f32_16x16x32_bf16 v[148:151], a[84:87], a[68:71], v[148:151]
ds_read_b64_tr_b16 a[120:121], v247 offset:8192
ds_read_b64_tr_b16 a[122:123], v247 offset:10240
v_mfma_f32_16x16x32_bf16 v[152:155], a[88:91], a[68:71], v[152:155]
v_mfma_f32_16x16x32_bf16 v[156:159], a[92:95], a[68:71], v[156:159]
ds_read_b128 a[104:107], v244 offset:6144
v_add_u32 v247, s53, v247
v_mfma_f32_16x16x32_bf16 v[160:163], a[80:83], a[72:75], v[160:163]
ds_read_b64_tr_b16 a[124:125], v248 offset:8192
ds_read_b64_tr_b16 a[126:127], v248 offset:10240
v_mfma_f32_16x16x32_bf16 v[164:167], a[84:87], a[72:75], v[164:167]
ds_read_b128 a[108:111], v244 offset:6400
v_add_u32 v248, s53, v248
v_mfma_f32_16x16x32_bf16 v[168:171], a[88:91], a[72:75], v[168:171]
v_mfma_f32_16x16x32_bf16 v[172:175], a[92:95], a[72:75], v[172:175]
s_add_u32 s49, s46, s91
v_add_u32 v244, s53, v244
v_mfma_f32_16x16x32_bf16 v[176:179], a[80:83], a[76:79], v[176:179]
s_add_u32 s46, s46, s53
v_mfma_f32_16x16x32_bf16 v[180:183], a[84:87], a[76:79], v[180:183]
v_mfma_f32_16x16x32_bf16 v[184:187], a[88:91], a[76:79], v[184:187]
v_mfma_f32_16x16x32_bf16 v[188:191], a[92:95], a[76:79], v[188:191]
s_waitcnt vmcnt(0) lgkmcnt(0)
s_barrier
v_mfma_f32_16x16x32_bf16 v[128:131], a[112:115], a[96:99], v[128:131]
ds_read_b64_tr_b16 a[80:81], v245 offset:0
ds_read_b64_tr_b16 a[82:83], v245 offset:2048
v_mfma_f32_16x16x32_bf16 v[132:135], a[116:119], a[96:99], v[132:135]
ds_read_b128 a[64:67], v244 offset:0
ds_read_b128 a[68:71], v244 offset:256
v_mfma_f32_16x16x32_bf16 v[136:139], a[120:123], a[96:99], v[136:139]
ds_read_b64_tr_b16 a[84:85], v246 offset:0
ds_read_b64_tr_b16 a[86:87], v246 offset:2048
v_mfma_f32_16x16x32_bf16 v[140:143], a[124:127], a[96:99], v[140:143]
ds_read_b64_tr_b16 a[88:89], v247 offset:0
ds_read_b64_tr_b16 a[90:91], v247 offset:2048
v_mfma_f32_16x16x32_bf16 v[144:147], a[112:115], a[100:103], v[144:147]
ds_read_b64_tr_b16 a[92:93], v248 offset:0
ds_read_b64_tr_b16 a[94:95], v248 offset:2048
v_mfma_f32_16x16x32_bf16 v[148:151], a[116:119], a[100:103], v[148:151]
ds_read_b64_tr_b16 a[144:145], v245 offset:8192
ds_read_b64_tr_b16 a[146:147], v245 offset:10240
v_mfma_f32_16x16x32_bf16 v[152:155], a[120:123], a[100:103], v[152:155]
ds_read_b128 a[128:131], v244 offset:2048
ds_read_b128 a[132:135], v244 offset:2304
v_mfma_f32_16x16x32_bf16 v[156:159], a[124:127], a[100:103], v[156:159]
ds_read_b64_tr_b16 a[148:149], v246 offset:8192
ds_read_b64_tr_b16 a[150:151], v246 offset:10240
v_mfma_f32_16x16x32_bf16 v[160:163], a[112:115], a[104:107], v[160:163]
ds_read_b64_tr_b16 a[152:153], v247 offset:8192
ds_read_b64_tr_b16 a[154:155], v247 offset:10240
v_mfma_f32_16x16x32_bf16 v[164:167], a[116:119], a[104:107], v[164:167]
ds_read_b64_tr_b16 a[156:157], v248 offset:8192
ds_read_b64_tr_b16 a[158:159], v248 offset:10240
v_mfma_f32_16x16x32_bf16 v[168:171], a[120:123], a[104:107], v[168:171]
ds_read_b128 a[72:75], v244 offset:4096
ds_read_b128 a[76:79], v244 offset:4352
v_mfma_f32_16x16x32_bf16 v[172:175], a[124:127], a[104:107], v[172:175]
ds_read_b128 a[136:139], v244 offset:6144
ds_read_b128 a[140:143], v244 offset:6400
v_mfma_f32_16x16x32_bf16 v[176:179], a[112:115], a[108:111], v[176:179]
s_mov_b32 s24, 0
s_mov_b32 s25, 32768
v_mfma_f32_16x16x32_bf16 v[180:183], a[116:119], a[108:111], v[180:183]
s_mov_b32 s26, 65536
s_mov_b32 s27, 98304
v_mfma_f32_16x16x32_bf16 v[184:187], a[120:123], a[108:111], v[184:187]
s_mov_b32 s28, 131072
s_mov_b32 s29, 163840
v_mfma_f32_16x16x32_bf16 v[188:191], a[124:127], a[108:111], v[188:191]
s_mov_b32 s30, 196608
s_mov_b32 s31, 229376
s_waitcnt lgkmcnt(0)
v_mfma_f32_16x16x32_bf16 v[128:131], a[80:83], a[64:67], v[128:131]
v_mfma_f32_16x16x32_bf16 v[132:135], a[84:87], a[64:67], v[132:135]
v_mfma_f32_16x16x32_bf16 v[136:139], a[88:91], a[64:67], v[136:139]
v_mfma_f32_16x16x32_bf16 v[140:143], a[92:95], a[64:67], v[140:143]
v_mfma_f32_16x16x32_bf16 v[128:131], a[144:147], a[128:131], v[128:131]
v_mfma_f32_16x16x32_bf16 v[132:135], a[148:151], a[128:131], v[132:135]
v_mfma_f32_16x16x32_bf16 v[136:139], a[152:155], a[128:131], v[136:139]
v_mfma_f32_16x16x32_bf16 v[140:143], a[156:159], a[128:131], v[140:143]
v_mfma_f32_16x16x32_bf16 v[144:147], a[80:83], a[68:71], v[144:147]
v_mfma_f32_16x16x32_bf16 v[148:151], a[84:87], a[68:71], v[148:151]
v_mfma_f32_16x16x32_bf16 v[152:155], a[88:91], a[68:71], v[152:155]
s_nop 1
v_cvt_pk_bf16_f32 v192, v128, v129
v_cvt_pk_bf16_f32 v194, v132, v133
v_cvt_pk_bf16_f32 v193, v130, v131
v_mfma_f32_16x16x32_bf16 v[156:159], a[92:95], a[68:71], v[156:159]
v_cvt_pk_bf16_f32 v195, v134, v135
v_cvt_pk_bf16_f32 v196, v136, v137
v_cvt_pk_bf16_f32 v198, v140, v141
v_mfma_f32_16x16x32_bf16 v[144:147], a[144:147], a[132:135], v[144:147]
v_cvt_pk_bf16_f32 v197, v138, v139
v_cvt_pk_bf16_f32 v199, v142, v143
v_mfma_f32_16x16x32_bf16 v[148:151], a[148:151], a[132:135], v[148:151]
v_permlane16_swap_b32 v192, v194
v_mfma_f32_16x16x32_bf16 v[152:155], a[152:155], a[132:135], v[152:155]
v_permlane16_swap_b32 v193, v195
v_mfma_f32_16x16x32_bf16 v[156:159], a[156:159], a[132:135], v[156:159]
v_permlane16_swap_b32 v196, v198
v_mfma_f32_16x16x32_bf16 v[160:163], a[80:83], a[72:75], v[160:163]
v_permlane16_swap_b32 v197, v199
v_mov_b32 v200, v192
v_mfma_f32_16x16x32_bf16 v[164:167], a[84:87], a[72:75], v[164:167]
v_mov_b32 v201, v193
v_mov_b32 v202, v194
v_mov_b32 v203, v195
v_mfma_f32_16x16x32_bf16 v[168:171], a[88:91], a[72:75], v[168:171]
v_mov_b32_dpp v192, v196 row_ror:8 row_mask:0xf bank_mask:0xc
v_mov_b32_dpp v193, v197 row_ror:8 row_mask:0xf bank_mask:0xc
v_mov_b32_dpp v194, v198 row_ror:8 row_mask:0xf bank_mask:0xc
v_mfma_f32_16x16x32_bf16 v[172:175], a[92:95], a[72:75], v[172:175]
v_mov_b32_dpp v195, v199 row_ror:8 row_mask:0xf bank_mask:0xc
v_mfma_f32_16x16x32_bf16 v[160:163], a[144:147], a[136:139], v[160:163]
buffer_store_dwordx4 v[192:195], v251, s[16:19], s24 offen nt
v_mov_b32_dpp v196, v200 row_ror:8 row_mask:0xf bank_mask:0x3
v_mov_b32_dpp v197, v201 row_ror:8 row_mask:0xf bank_mask:0x3
v_mfma_f32_16x16x32_bf16 v[164:167], a[148:151], a[136:139], v[164:167]
v_mov_b32_dpp v198, v202 row_ror:8 row_mask:0xf bank_mask:0x3
v_mov_b32_dpp v199, v203 row_ror:8 row_mask:0xf bank_mask:0x3
v_mfma_f32_16x16x32_bf16 v[168:171], a[152:155], a[136:139], v[168:171]
buffer_store_dwordx4 v[196:199], v251, s[16:19], s25 offen nt
v_cvt_pk_bf16_f32 v204, v144, v145
v_cvt_pk_bf16_f32 v206, v148, v149
v_mfma_f32_16x16x32_bf16 v[172:175], a[156:159], a[136:139], v[172:175]
v_cvt_pk_bf16_f32 v205, v146, v147
v_cvt_pk_bf16_f32 v207, v150, v151
v_cvt_pk_bf16_f32 v208, v152, v153
v_mfma_f32_16x16x32_bf16 v[176:179], a[80:83], a[76:79], v[176:179]
v_cvt_pk_bf16_f32 v210, v156, v157
v_cvt_pk_bf16_f32 v209, v154, v155
v_cvt_pk_bf16_f32 v211, v158, v159
v_mfma_f32_16x16x32_bf16 v[180:183], a[84:87], a[76:79], v[180:183]
v_permlane16_swap_b32 v204, v206
v_mfma_f32_16x16x32_bf16 v[184:187], a[88:91], a[76:79], v[184:187]
v_permlane16_swap_b32 v205, v207
v_mfma_f32_16x16x32_bf16 v[188:191], a[92:95], a[76:79], v[188:191]
v_permlane16_swap_b32 v208, v210
v_mfma_f32_16x16x32_bf16 v[176:179], a[144:147], a[140:143], v[176:179]
v_permlane16_swap_b32 v209, v211
v_mov_b32 v212, v204
v_mfma_f32_16x16x32_bf16 v[180:183], a[148:151], a[140:143], v[180:183]
v_mov_b32 v213, v205
v_mov_b32 v214, v206
v_mov_b32 v215, v207
v_mfma_f32_16x16x32_bf16 v[184:187], a[152:155], a[140:143], v[184:187]
v_mov_b32_dpp v204, v208 row_ror:8 row_mask:0xf bank_mask:0xc
v_mov_b32_dpp v205, v209 row_ror:8 row_mask:0xf bank_mask:0xc
v_mov_b32_dpp v206, v210 row_ror:8 row_mask:0xf bank_mask:0xc
v_mfma_f32_16x16x32_bf16 v[188:191], a[156:159], a[140:143], v[188:191]
v_mov_b32_dpp v207, v211 row_ror:8 row_mask:0xf bank_mask:0xc
buffer_store_dwordx4 v[204:207], v251, s[16:19], s26 offen nt
v_mov_b32_dpp v208, v212 row_ror:8 row_mask:0xf bank_mask:0x3
v_mov_b32_dpp v209, v213 row_ror:8 row_mask:0xf bank_mask:0x3
v_mov_b32_dpp v210, v214 row_ror:8 row_mask:0xf bank_mask:0x3
v_mov_b32_dpp v211, v215 row_ror:8 row_mask:0xf bank_mask:0x3
buffer_store_dwordx4 v[208:211], v251, s[16:19], s27 offen nt
v_cvt_pk_bf16_f32 v192, v160, v161
v_cvt_pk_bf16_f32 v194, v164, v165
v_cvt_pk_bf16_f32 v193, v162, v163
v_cvt_pk_bf16_f32 v195, v166, v167
v_cvt_pk_bf16_f32 v196, v168, v169
v_cvt_pk_bf16_f32 v198, v172, v173
v_cvt_pk_bf16_f32 v197, v170, v171
v_cvt_pk_bf16_f32 v199, v174, v175
v_permlane16_swap_b32 v192, v194
v_permlane16_swap_b32 v193, v195
v_permlane16_swap_b32 v196, v198
v_permlane16_swap_b32 v197, v199
v_mov_b32 v200, v192
v_mov_b32 v201, v193
v_mov_b32 v202, v194
v_mov_b32 v203, v195
v_mov_b32_dpp v192, v196 row_ror:8 row_mask:0xf bank_mask:0xc
v_mov_b32_dpp v193, v197 row_ror:8 row_mask:0xf bank_mask:0xc
v_mov_b32_dpp v194, v198 row_ror:8 row_mask:0xf bank_mask:0xc
v_mov_b32_dpp v195, v199 row_ror:8 row_mask:0xf bank_mask:0xc
buffer_store_dwordx4 v[192:195], v251, s[16:19], s28 offen nt
v_mov_b32_dpp v196, v200 row_ror:8 row_mask:0xf bank_mask:0x3
v_mov_b32_dpp v197, v201 row_ror:8 row_mask:0xf bank_mask:0x3
v_mov_b32_dpp v198, v202 row_ror:8 row_mask:0xf bank_mask:0x3
v_mov_b32_dpp v199, v203 row_ror:8 row_mask:0xf bank_mask:0x3
buffer_store_dwordx4 v[196:199], v251, s[16:19], s29 offen nt
v_cvt_pk_bf16_f32 v204, v176, v177
v_cvt_pk_bf16_f32 v206, v180, v181
v_cvt_pk_bf16_f32 v205, v178, v179
v_cvt_pk_bf16_f32 v207, v182, v183
v_cvt_pk_bf16_f32 v208, v184, v185
v_cvt_pk_bf16_f32 v210, v188, v189
v_cvt_pk_bf16_f32 v209, v186, v187
v_cvt_pk_bf16_f32 v211, v190, v191
v_permlane16_swap_b32 v204, v206
v_permlane16_swap_b32 v205, v207
v_permlane16_swap_b32 v208, v210
v_permlane16_swap_b32 v209, v211
v_mov_b32 v212, v204
v_mov_b32 v213, v205
v_mov_b32 v214, v206
v_mov_b32 v215, v207
v_mov_b32_dpp v204, v208 row_ror:8 row_mask:0xf bank_mask:0xc
v_mov_b32_dpp v205, v209 row_ror:8 row_mask:0xf bank_mask:0xc
v_mov_b32_dpp v206, v210 row_ror:8 row_mask:0xf bank_mask:0xc
v_mov_b32_dpp v207, v211 row_ror:8 row_mask:0xf bank_mask:0xc
buffer_store_dwordx4 v[204:207], v251, s[16:19], s30 offen nt
v_mov_b32_dpp v208, v212 row_ror:8 row_mask:0xf bank_mask:0x3
v_mov_b32_dpp v209, v213 row_ror:8 row_mask:0xf bank_mask:0x3
v_mov_b32_dpp v210, v214 row_ror:8 row_mask:0xf bank_mask:0x3
v_mov_b32_dpp v211, v215 row_ror:8 row_mask:0xf bank_mask:0x3
buffer_store_dwordx4 v[208:211], v251, s[16:19], s31 offen nt
.Lend:
s_endpgm
.Lspin1:
s_mov_b32 s93, 0
.Lspin1_l:
s_sleep 4
buffer_load_dword v8, v3, s[60:63], s90 offen sc1
s_waitcnt vmcnt(0)
v_and_b32 v0, 7, v8
v_cmp_lt_u32 s[28:29], v0, v14
v_cmp_eq_u32 s[30:31], v8, v2
s_nop 0
s_or_b64 s[28:29], s[28:29], s[30:31]
s_cbranch_scc0 .Lspin1_ret
s_add_u32 s93, s93, 1
s_cmp_lt_u32 s93, 0x100000
s_cbranch_scc1 .Lspin1_l
s_branch .Lspin1_ret
.Lj0_pad:
s_cbranch_execz .Ltch2
.Lj0a:
v_cmp_gt_i32 s[32:33], 0, v8
v_nop
v_cmp_gt_i32 s[34:35], 1, v8
v_cndmask_b32 v160, v160, v34, s[32:33]
v_cmp_gt_i32 s[32:33], 2, v8
v_cndmask_b32 v161, v161, v34, s[34:35]
v_cmp_gt_i32 s[34:35], 3, v8
v_cndmask_b32 v162, v162, v34, s[32:33]
v_cmp_gt_i32 s[32:33], 8, v8
v_cndmask_b32 v163, v163, v34, s[34:35]
v_cmp_gt_i32 s[34:35], 9, v8
v_cndmask_b32 v164, v164, v34, s[32:33]
v_cmp_gt_i32 s[32:33], 10, v8
v_cndmask_b32 v165, v165, v34, s[34:35]
v_cmp_gt_i32 s[34:35], 11, v8
v_cndmask_b32 v166, v166, v34, s[32:33]
v_cmp_gt_i32 s[32:33], 16, v8
v_cndmask_b32 v167, v167, v34, s[34:35]
v_cmp_gt_i32 s[34:35], 17, v8
v_cndmask_b32 v168, v168, v34, s[32:33]
v_cmp_gt_i32 s[32:33], 18, v8
v_cndmask_b32 v169, v169, v34, s[34:35]
v_cmp_gt_i32 s[34:35], 19, v8
v_cndmask_b32 v170, v170, v34, s[32:33]
v_cmp_gt_i32 s[32:33], 24, v8
v_cndmask_b32 v171, v171, v34, s[34:35]
v_cmp_gt_i32 s[34:35], 25, v8
v_cndmask_b32 v172, v172, v34, s[32:33]
v_cmp_gt_i32 s[32:33], 26, v8
v_cndmask_b32 v173, v173, v34, s[34:35]
v_cmp_gt_i32 s[34:35], 27, v8
v_cndmask_b32 v174, v174, v34, s[32:33]
v_nop
v_cndmask_b32 v175, v175, v34, s[34:35]
s_branch .Lj0a_ret
.Lj0b:
s_cmp_eq_u32 s51, 1
s_cbranch_scc1 .Lj0b1
v_mov_b32 v208, v34
v_mov_b32 v209, v34
v_mov_b32 v210, v34
v_mov_b32 v211, v34
v_mov_b32 v212, v34
v_mov_b32 v213, v34
v_mov_b32 v214, v34
v_mov_b32 v215, v34
v_mov_b32 v216, v34
v_mov_b32 v217, v34
v_mov_b32 v218, v34
v_mov_b32 v219, v34
v_mov_b32 v220, v34
v_mov_b32 v221, v34
v_mov_b32 v222, v34
v_mov_b32 v223, v34
s_branch .Lj0b_ret
.Lj0b1:
v_cmp_gt_i32 s[32:33], 0, v8
v_nop
v_cmp_gt_i32 s[34:35], 1, v8
v_cndmask_b32 v208, v208, v34, s[32:33]
v_cmp_gt_i32 s[32:33], 2, v8
v_cndmask_b32 v209, v209, v34, s[34:35]
v_cmp_gt_i32 s[34:35], 3, v8
v_cndmask_b32 v210, v210, v34, s[32:33]
v_cmp_gt_i32 s[32:33], 8, v8
v_cndmask_b32 v211, v211, v34, s[34:35]
v_cmp_gt_i32 s[34:35], 9, v8
v_cndmask_b32 v212, v212, v34, s[32:33]
v_cmp_gt_i32 s[32:33], 10, v8
v_cndmask_b32 v213, v213, v34, s[34:35]
v_cmp_gt_i32 s[34:35], 11, v8
v_cndmask_b32 v214, v214, v34, s[32:33]
v_cmp_gt_i32 s[32:33], 16, v8
v_cndmask_b32 v215, v215, v34, s[34:35]
v_cmp_gt_i32 s[34:35], 17, v8
v_cndmask_b32 v216, v216, v34, s[32:33]
v_cmp_gt_i32 s[32:33], 18, v8
v_cndmask_b32 v217, v217, v34, s[34:35]
v_cmp_gt_i32 s[34:35], 19, v8
v_cndmask_b32 v218, v218, v34, s[32:33]
v_cmp_gt_i32 s[32:33], 24, v8
v_cndmask_b32 v219, v219, v34, s[34:35]
v_cmp_gt_i32 s[34:35], 25, v8
v_cndmask_b32 v220, v220, v34, s[32:33]
v_cmp_gt_i32 s[32:33], 26, v8
v_cndmask_b32 v221, v221, v34, s[34:35]
v_cmp_gt_i32 s[34:35], 27, v8
v_cndmask_b32 v222, v222, v34, s[32:33]
v_nop
v_cndmask_b32 v223, v223, v34, s[34:35]
s_branch .Lj0b_ret
.Lj0c:
s_cmp_eq_u32 s51, 0
s_cbranch_scc1 .Lj0c_ret
s_cmp_eq_u32 s51, 2
s_cbranch_scc1 .Lj0c1
v_mov_b32 v208, v34
v_mov_b32 v209, v34
v_mov_b32 v210, v34
v_mov_b32 v211, v34
v_mov_b32 v212, v34
v_mov_b32 v213, v34
v_mov_b32 v214, v34
v_mov_b32 v215, v34
v_mov_b32 v216, v34
v_mov_b32 v217, v34
v_mov_b32 v218, v34
v_mov_b32 v219, v34
v_mov_b32 v220, v34
v_mov_b32 v221, v34
v_mov_b32 v222, v34
v_mov_b32 v223, v34
s_branch .Lj0c_ret
.Lj0c1:
v_cmp_gt_i32 s[32:33], 0, v8
v_nop
v_cmp_gt_i32 s[34:35], 1, v8
v_cndmask_b32 v208, v208, v34, s[32:33]
v_cmp_gt_i32 s[32:33], 2, v8
v_cndmask_b32 v209, v209, v34, s[34:35]
v_cmp_gt_i32 s[34:35], 3, v8
v_cndmask_b32 v210, v210, v34, s[32:33]
v_cmp_gt_i32 s[32:33], 8, v8
v_cndmask_b32 v211, v211, v34, s[34:35]
v_cmp_gt_i32 s[34:35], 9, v8
v_cndmask_b32 v212, v212, v34, s[32:33]
v_cmp_gt_i32 s[32:33], 10, v8
v_cndmask_b32 v213, v213, v34, s[34:35]
v_cmp_gt_i32 s[34:35], 11, v8
v_cndmask_b32 v214, v214, v34, s[32:33]
v_cmp_gt_i32 s[32:33], 16, v8
v_cndmask_b32 v215, v215, v34, s[34:35]
v_cmp_gt_i32 s[34:35], 17, v8
v_cndmask_b32 v216, v216, v34, s[32:33]
v_cmp_gt_i32 s[32:33], 18, v8
v_cndmask_b32 v217, v217, v34, s[34:35]
v_cmp_gt_i32 s[34:35], 19, v8
v_cndmask_b32 v218, v218, v34, s[32:33]
v_cmp_gt_i32 s[32:33], 24, v8
v_cndmask_b32 v219, v219, v34, s[34:35]
v_cmp_gt_i32 s[34:35], 25, v8
v_cndmask_b32 v220, v220, v34, s[32:33]
v_cmp_gt_i32 s[32:33], 26, v8
v_cndmask_b32 v221, v221, v34, s[34:35]
v_cmp_gt_i32 s[34:35], 27, v8
v_cndmask_b32 v222, v222, v34, s[32:33]
v_nop
v_cndmask_b32 v223, v223, v34, s[34:35]
s_branch .Lj0c_ret
.Lool_pad:
s_nop 0
.Lev_d1:
s_cmp_eq_u32 s43, s42
s_cbranch_scc1 .LT0
s_swappc_b64 s[30:31], s[58:59]
.Lev_d1_r:
s_branch .LD0
.Lev_s1:
s_cmp_eq_u32 s43, s42
s_cbranch_scc1 .LE0
s_swappc_b64 s[30:31], s[58:59]
.Lev_s1_r:
s_branch .LS0
.Lspin21:
s_mov_b32 s93, 0
.Lspin21_l:
s_sleep 4
buffer_load_dword v9, v10, s[60:63], 0 offen sc1
s_waitcnt vmcnt(0)
v_cmp_gt_u32 vcc, 1, v9
s_cmp_lg_u64 vcc, 0
s_cbranch_scc0 .Lspin21_ret
s_add_u32 s93, s93, 1
s_cmp_lt_u32 s93, 0x100000
s_cbranch_scc1 .Lspin21_l
s_branch .Lspin21_ret
.Ljob0:
s_waitcnt vmcnt(8)
v_mov_b32 v0, 1
buffer_store_dword v0, v5, s[60:63], 0 offen sc1
s_mov_b32 s57, 10
s_add_u32 s93, s43, 1
s_max_u32 s43, s57, s93
s_min_u32 s43, s43, s42
s_sub_u32 s55, s43, s93
s_swappc_b64 s[58:59], s[30:31]
.Ljob1:
v_and_b32 v10, 7, v1
v_and_b32 v9, 8, v1
v_lshl_or_b32 v10, v9, 1, v10
s_and_b32 s93, s87, 2
s_lshl_b32 s93, s93, 2
v_or_b32 v10, s93, v10
v_lshlrev_b32 v10, 9, v10
v_bfe_u32 v9, v1, 4, 1
v_lshl_or_b32 v10, v9, 2, v10
s_lshl_b32 s93, s86, 6
.Lpad24:
s_nop 0
v_add_u32 v10, s93, v10
s_and_b32 s93, s87, 1
s_lshl_b32 s93, s93, 3
s_add_u32 s93, s93, 0x10000
v_add_u32 v10, s93, v10
buffer_load_dword v9, v10, s[60:63], 0 offen sc1
s_mov_b32 s57, 12
s_add_u32 s93, s43, 1
s_max_u32 s43, s57, s93
s_min_u32 s43, s43, s42
s_sub_u32 s55, s43, s93
s_swappc_b64 s[58:59], s[30:31]
.Ljob2:
s_waitcnt vmcnt(4)
v_cmp_gt_u32 vcc, 1, v9
s_cmp_lg_u64 vcc, 0
s_cbranch_scc1 .Lspin21
.Lspin21_ret:
s_cmp_eq_u32 s87, 8
s_cselect_b32 s0, 44, 0x7fffffff
s_mov_b32 s57, s0
s_add_u32 s93, s43, 1
s_max_u32 s43, s57, s93
s_min_u32 s43, s43, s42
s_sub_u32 s55, s43, s93
s_swappc_b64 s[58:59], s[30:31]
.Ljob3:
s_memrealtime s[100:101]
s_lshl_b32 s93, s87, 2
s_add_u32 s93, s93, s3
s_xor_b32 s93, s93, 32
s_lshl_b32 s93, s93, 20
s_lshl_b32 s0, s86, 2
s_add_u32 s93, s93, s0
s_add_u32 s93, s93, 0x14000
v_lshlrev_b32 v9, 7, v1
v_add_u32 v10, s93, v9
s_waitcnt lgkmcnt(0)
s_and_b32 s100, s100, -16
s_or_b32 s100, s100, s86
s_or_b32 s100, s100, 8
v_mov_b32 v0, s100
buffer_store_dword v0, v10, s[60:63], 0 offen sc1
s_mov_b32 s57, 2147483647
s_add_u32 s93, s43, 1
s_max_u32 s43, s57, s93
s_min_u32 s43, s43, s42
s_sub_u32 s55, s43, s93
s_swappc_b64 s[58:59], s[30:31]
.Lx:
s_sub_u32 s4, s4, s91
s_subb_u32 s5, s5, 0
s_mov_b32 s6, 0x80000
s_sub_u32 s8, s8, s91
s_subb_u32 s9, s9, 0
s_mov_b32 s10, 0x84000
s_mov_b64 s[38:39], s[52:53]
v_add_u32 v222, 0xffff0000, v32
v_add_u32 v222, s91, v222
v_add_u32 v223, 0x4000, v33
v_add_u32 v223, s91, v223
s_cmp_eq_u64 s[30:31], 0
s_cbranch_scc1 .Lx_s0
v_mov_b32 v224, v64
v_mov_b32 v225, v65
v_mov_b32 v226, v66
v_mov_b32 v227, v67
v_mov_b32 v228, v68
v_mov_b32 v229, v69
v_mov_b32 v230, v70
v_mov_b32 v231, v71
v_mov_b32 v232, v72
v_mov_b32 v233, v73
v_mov_b32 v234, v74
v_mov_b32 v235, v75
v_mov_b32 v236, v76
v_mov_b32 v237, v77
v_mov_b32 v238, v78
v_mov_b32 v239, v79
v_mov_b32 v240, v80
v_mov_b32 v241, v81
v_mov_b32 v242, v82
v_mov_b32 v243, v83
v_mov_b32 v244, v84
v_mov_b32 v245, v85
v_mov_b32 v246, v86
v_mov_b32 v247, v87
v_mov_b32 v248, v88
v_mov_b32 v249, v89
v_mov_b32 v250, v90
v_mov_b32 v251, v91
v_mov_b32 v252, v92
v_mov_b32 v253, v93
v_mov_b32 v254, v94
v_mov_b32 v255, v95
s_sub_u32 s50, 15, s89
s_lshl_b32 s50, s50, 2
s_add_u32 s50, s50, s3
s_getpc_b64 s[42:43]
.Lx_p0:
s_add_u32 s42, s42, .Lx_r0-.Lx_p0
s_addc_u32 s43, s43, 0
s_branch .Lx_core
.Lx_r0:
v_accvgpr_write_b32 a64, v128
v_accvgpr_write_b32 a65, v129
v_accvgpr_write_b32 a66, v130
v_accvgpr_write_b32 a67, v131
v_accvgpr_write_b32 a68, v132
v_accvgpr_write_b32 a69, v133
v_accvgpr_write_b32 a70, v134
v_accvgpr_write_b32 a71, v135
v_accvgpr_write_b32 a72, v136
v_accvgpr_write_b32 a73, v137
v_accvgpr_write_b32 a74, v138
v_accvgpr_write_b32 a75, v139
v_accvgpr_write_b32 a76, v140
v_accvgpr_write_b32 a77, v141
v_accvgpr_write_b32 a78, v142
v_accvgpr_write_b32 a79, v143
v_accvgpr_write_b32 a80, v144
v_accvgpr_write_b32 a81, v145
v_accvgpr_write_b32 a82, v146
v_accvgpr_write_b32 a83, v147
v_accvgpr_write_b32 a84, v148
v_accvgpr_write_b32 a85, v149
v_accvgpr_write_b32 a86, v150
v_accvgpr_write_b32 a87, v151
v_accvgpr_write_b32 a88, v152
v_accvgpr_write_b32 a89, v153
v_accvgpr_write_b32 a90, v154
v_accvgpr_write_b32 a91, v155
v_accvgpr_write_b32 a92, v156
v_accvgpr_write_b32 a93, v157
v_accvgpr_write_b32 a94, v158
v_accvgpr_write_b32 a95, v159
v_accvgpr_write_b32 a112, v160
v_accvgpr_write_b32 a113, v161
v_accvgpr_write_b32 a114, v162
v_accvgpr_write_b32 a115, v163
v_accvgpr_write_b32 a116, v164
v_accvgpr_write_b32 a117, v165
v_accvgpr_write_b32 a118, v166
v_accvgpr_write_b32 a119, v167
v_accvgpr_write_b32 a120, v168
v_accvgpr_write_b32 a121, v169
v_accvgpr_write_b32 a122, v170
v_accvgpr_write_b32 a123, v171
v_accvgpr_write_b32 a124, v172
v_accvgpr_write_b32 a125, v173
v_accvgpr_write_b32 a126, v174
v_accvgpr_write_b32 a127, v175
v_accvgpr_write_b32 a144, v176
v_accvgpr_write_b32 a145, v177
v_accvgpr_write_b32 a146, v178
v_accvgpr_write_b32 a147, v179
v_accvgpr_write_b32 a148, v180
v_accvgpr_write_b32 a149, v181
v_accvgpr_write_b32 a150, v182
v_accvgpr_write_b32 a151, v183
v_accvgpr_write_b32 a152, v184
v_accvgpr_write_b32 a153, v185
v_accvgpr_write_b32 a154, v186
v_accvgpr_write_b32 a155, v187
v_accvgpr_write_b32 a156, v188
v_accvgpr_write_b32 a157, v189
v_accvgpr_write_b32 a158, v190
v_accvgpr_write_b32 a159, v191
v_mov_b32 v60, v220
.Lx_s0:
s_cmp_eq_u64 s[38:39], 0
s_cbranch_scc1 .Lx_s1
v_mov_b32 v224, v96
v_mov_b32 v225, v97
v_mov_b32 v226, v98
v_mov_b32 v227, v99
v_mov_b32 v228, v100
v_mov_b32 v229, v101
v_mov_b32 v230, v102
v_mov_b32 v231, v103
v_mov_b32 v232, v104
v_mov_b32 v233, v105
v_mov_b32 v234, v106
v_mov_b32 v235, v107
v_mov_b32 v236, v108
v_mov_b32 v237, v109
v_mov_b32 v238, v110
v_mov_b32 v239, v111
v_mov_b32 v240, v112
v_mov_b32 v241, v113
v_mov_b32 v242, v114
v_mov_b32 v243, v115
v_mov_b32 v244, v116
v_mov_b32 v245, v117
v_mov_b32 v246, v118
v_mov_b32 v247, v119
v_mov_b32 v248, v120
v_mov_b32 v249, v121
v_mov_b32 v250, v122
v_mov_b32 v251, v123
v_mov_b32 v252, v124
v_mov_b32 v253, v125
v_mov_b32 v254, v126
v_mov_b32 v255, v127
s_mov_b32 s50, s89
s_lshl_b32 s50, s50, 2
s_add_u32 s50, s50, s94
s_getpc_b64 s[42:43]
.Lx_p1:
s_add_u32 s42, s42, .Lx_r1-.Lx_p1
s_addc_u32 s43, s43, 0
s_branch .Lx_core
.Lx_r1:
v_accvgpr_write_b32 a176, v128
v_accvgpr_write_b32 a177, v129
v_accvgpr_write_b32 a178, v130
v_accvgpr_write_b32 a179, v131
v_accvgpr_write_b32 a180, v132
v_accvgpr_write_b32 a181, v133
v_accvgpr_write_b32 a182, v134
v_accvgpr_write_b32 a183, v135
v_accvgpr_write_b32 a184, v136
v_accvgpr_write_b32 a185, v137
v_accvgpr_write_b32 a186, v138
v_accvgpr_write_b32 a187, v139
v_accvgpr_write_b32 a188, v140
v_accvgpr_write_b32 a189, v141
v_accvgpr_write_b32 a190, v142
v_accvgpr_write_b32 a191, v143
v_accvgpr_write_b32 a208, v144
v_accvgpr_write_b32 a209, v145
v_accvgpr_write_b32 a210, v146
v_accvgpr_write_b32 a211, v147
v_accvgpr_write_b32 a212, v148
v_accvgpr_write_b32 a213, v149
v_accvgpr_write_b32 a214, v150
v_accvgpr_write_b32 a215, v151
v_accvgpr_write_b32 a216, v152
v_accvgpr_write_b32 a217, v153
v_accvgpr_write_b32 a218, v154
v_accvgpr_write_b32 a219, v155
v_accvgpr_write_b32 a220, v156
v_accvgpr_write_b32 a221, v157
v_accvgpr_write_b32 a222, v158
v_accvgpr_write_b32 a223, v159
v_accvgpr_write_b32 a240, v160
v_accvgpr_write_b32 a241, v161
v_accvgpr_write_b32 a242, v162
v_accvgpr_write_b32 a243, v163
v_accvgpr_write_b32 a244, v164
v_accvgpr_write_b32 a245, v165
v_accvgpr_write_b32 a246, v166
v_accvgpr_write_b32 a247, v167
v_accvgpr_write_b32 a248, v168
v_accvgpr_write_b32 a249, v169
v_accvgpr_write_b32 a250, v170
v_accvgpr_write_b32 a251, v171
v_accvgpr_write_b32 a252, v172
v_accvgpr_write_b32 a253, v173
v_accvgpr_write_b32 a254, v174
v_accvgpr_write_b32 a255, v175
v_accvgpr_write_b32 a96, v176
v_accvgpr_write_b32 a97, v177
v_accvgpr_write_b32 a98, v178
v_accvgpr_write_b32 a99, v179
v_accvgpr_write_b32 a100, v180
v_accvgpr_write_b32 a101, v181
v_accvgpr_write_b32 a102, v182
v_accvgpr_write_b32 a103, v183
v_accvgpr_write_b32 a104, v184
v_accvgpr_write_b32 a105, v185
v_accvgpr_write_b32 a106, v186
v_accvgpr_write_b32 a107, v187
v_accvgpr_write_b32 a108, v188
v_accvgpr_write_b32 a109, v189
v_accvgpr_write_b32 a110, v190
v_accvgpr_write_b32 a111, v191
v_mov_b32 v61, v220
.Lx_s1:
s_branch .Lx_ret
.Lx_core:
s_lshl_b32 s51, s50, 5
v_and_b32 v221, 31, v1
v_lshrrev_b32 v218, 5, v1
v_lshlrev_b32 v218, 2, v218
v_sub_u32 v221, v221, v218
v_add_u32 v221, s51, v221
v_mov_b32 v216, v34
v_mov_b32 v220, 0
s_mov_b32 s44, 0
s_mov_b32 s45, 4096
s_mov_b32 s46, 0
v_mov_b32 v128, 0
v_mov_b32 v129, 0
v_mov_b32 v130, 0
v_mov_b32 v131, 0
v_mov_b32 v132, 0
v_mov_b32 v133, 0
v_mov_b32 v134, 0
v_mov_b32 v135, 0
v_mov_b32 v136, 0
v_mov_b32 v137, 0
v_mov_b32 v138, 0
v_mov_b32 v139, 0
v_mov_b32 v140, 0
v_mov_b32 v141, 0
v_mov_b32 v142, 0
v_mov_b32 v143, 0
v_mov_b32 v144, 0
v_mov_b32 v145, 0
v_mov_b32 v146, 0
v_mov_b32 v147, 0
v_mov_b32 v148, 0
v_mov_b32 v149, 0
v_mov_b32 v150, 0
v_mov_b32 v151, 0
v_mov_b32 v152, 0
v_mov_b32 v153, 0
v_mov_b32 v154, 0
v_mov_b32 v155, 0
v_mov_b32 v156, 0
v_mov_b32 v157, 0
v_mov_b32 v158, 0
v_mov_b32 v159, 0
v_mov_b32 v160, 0
v_mov_b32 v161, 0
v_mov_b32 v162, 0
v_mov_b32 v163, 0
v_mov_b32 v164, 0
v_mov_b32 v165, 0
v_mov_b32 v166, 0
v_mov_b32 v167, 0
v_mov_b32 v168, 0
v_mov_b32 v169, 0
v_mov_b32 v170, 0
v_mov_b32 v171, 0
v_mov_b32 v172, 0
v_mov_b32 v173, 0
v_mov_b32 v174, 0
v_mov_b32 v175, 0
v_mov_b32 v176, 0
v_mov_b32 v177, 0
v_mov_b32 v178, 0
v_mov_b32 v179, 0
v_mov_b32 v180, 0
v_mov_b32 v181, 0
v_mov_b32 v182, 0
v_mov_b32 v183, 0
v_mov_b32 v184, 0
v_mov_b32 v185, 0
v_mov_b32 v186, 0
v_mov_b32 v187, 0
v_mov_b32 v188, 0
v_mov_b32 v189, 0
v_mov_b32 v190, 0
v_mov_b32 v191, 0
.Lx_loop:
buffer_load_dwordx4 a[0:3], v222, s[4:7], s44 offen offset:0 sc1
buffer_load_dwordx4 a[4:7], v222, s[4:7], s44 offen offset:512 sc1
buffer_load_dwordx4 a[8:11], v222, s[4:7], s44 offen offset:1024 sc1
buffer_load_dwordx4 a[12:15], v222, s[4:7], s44 offen offset:1536 sc1
buffer_load_dwordx4 a[16:19], v222, s[4:7], s44 offen offset:2048 sc1
buffer_load_dwordx4 a[20:23], v222, s[4:7], s44 offen offset:2560 sc1
buffer_load_dwordx4 a[24:27], v222, s[4:7], s44 offen offset:3072 sc1
buffer_load_dwordx4 a[28:31], v222, s[4:7], s44 offen offset:3584 sc1
buffer_load_dwordx4 a[32:35], v223, s[8:11], s44 offen offset:0 sc1
buffer_load_dwordx4 a[36:39], v223, s[8:11], s44 offen offset:1024 sc1
buffer_load_dwordx4 a[40:43], v223, s[8:11], s44 offen offset:2048 sc1
buffer_load_dwordx4 a[44:47], v223, s[8:11], s44 offen offset:3072 sc1
buffer_load_dwordx4 a[48:51], v223, s[8:11], s45 offen offset:0 sc1
buffer_load_dwordx4 a[52:55], v223, s[8:11], s45 offen offset:1024 sc1
buffer_load_dwordx4 a[56:59], v223, s[8:11], s45 offen offset:2048 sc1
buffer_load_dwordx4 a[60:63], v223, s[8:11], s45 offen offset:3072 sc1
s_waitcnt vmcnt(8)
v_mfma_f32_32x32x16_bf16 v[192:207], a[0:3], v[224:227], 0
v_mfma_f32_32x32x16_bf16 v[192:207], a[4:7], v[228:231], v[192:207]
v_mfma_f32_32x32x16_bf16 v[192:207], a[8:11], v[232:235], v[192:207]
v_mfma_f32_32x32x16_bf16 v[192:207], a[12:15], v[236:239], v[192:207]
v_mfma_f32_32x32x16_bf16 v[192:207], a[16:19], v[240:243], v[192:207]
v_mfma_f32_32x32x16_bf16 v[192:207], a[20:23], v[244:247], v[192:207]
v_mfma_f32_32x32x16_bf16 v[192:207], a[24:27], v[248:251], v[192:207]
v_mfma_f32_32x32x16_bf16 v[192:207], a[28:31], v[252:255], v[192:207]
v_cmp_gt_i32 s[32:33], 0, v221
v_nop
v_cmp_gt_i32 s[34:35], 1, v221
s_nop 8
v_cndmask_b32 v192, v192, v34, s[32:33]
v_cmp_gt_i32 s[32:33], 2, v221
v_cndmask_b32 v193, v193, v34, s[34:35]
v_cmp_gt_i32 s[34:35], 3, v221
v_cndmask_b32 v194, v194, v34, s[32:33]
v_cmp_gt_i32 s[32:33], 8, v221
v_cndmask_b32 v195, v195, v34, s[34:35]
v_cmp_gt_i32 s[34:35], 9, v221
v_cndmask_b32 v196, v196, v34, s[32:33]
v_cmp_gt_i32 s[32:33], 10, v221
v_cndmask_b32 v197, v197, v34, s[34:35]
v_cmp_gt_i32 s[34:35], 11, v221
v_cndmask_b32 v198, v198, v34, s[32:33]
v_cmp_gt_i32 s[32:33], 16, v221
v_cndmask_b32 v199, v199, v34, s[34:35]
v_cmp_gt_i32 s[34:35], 17, v221
v_cndmask_b32 v200, v200, v34, s[32:33]
v_cmp_gt_i32 s[32:33], 18, v221
v_cndmask_b32 v201, v201, v34, s[34:35]
v_cmp_gt_i32 s[34:35], 19, v221
v_cndmask_b32 v202, v202, v34, s[32:33]
v_cmp_gt_i32 s[32:33], 24, v221
v_cndmask_b32 v203, v203, v34, s[34:35]
v_cmp_gt_i32 s[34:35], 25, v221
v_cndmask_b32 v204, v204, v34, s[32:33]
v_cmp_gt_i32 s[32:33], 26, v221
v_cndmask_b32 v205, v205, v34, s[34:35]
v_cmp_gt_i32 s[34:35], 27, v221
v_cndmask_b32 v206, v206, v34, s[32:33]
v_nop
v_cndmask_b32 v207, v207, v34, s[34:35]
v_max3_f32 v217, v192, v193, v216
v_max3_f32 v217, v217, v194, v195
v_max3_f32 v217, v217, v196, v197
v_max3_f32 v217, v217, v198, v199
v_max3_f32 v217, v217, v200, v201
v_max3_f32 v217, v217, v202, v203
v_max3_f32 v217, v217, v204, v205
v_max3_f32 v217, v217, v206, v207
v_mov_b32 v218, v217
s_nop 1
v_permlane32_swap_b32 v217, v218
v_max_f32 v217, v217, v218
v_sub_f32 v219, v216, v217
v_exp_f32 v219, v219
v_mov_b32 v216, v217
v_sub_f32 v192, v192, v217
v_sub_f32 v193, v193, v217
v_sub_f32 v194, v194, v217
v_sub_f32 v195, v195, v217
v_sub_f32 v196, v196, v217
v_sub_f32 v197, v197, v217
v_sub_f32 v198, v198, v217
v_sub_f32 v199, v199, v217
v_sub_f32 v200, v200, v217
v_sub_f32 v201, v201, v217
v_sub_f32 v202, v202, v217
v_sub_f32 v203, v203, v217
v_sub_f32 v204, v204, v217
v_sub_f32 v205, v205, v217
v_sub_f32 v206, v206, v217
v_sub_f32 v207, v207, v217
v_exp_f32 v192, v192
v_exp_f32 v193, v193
v_exp_f32 v194, v194
v_exp_f32 v195, v195
v_exp_f32 v196, v196
v_exp_f32 v197, v197
v_exp_f32 v198, v198
v_exp_f32 v199, v199
v_exp_f32 v200, v200
v_exp_f32 v201, v201
v_exp_f32 v202, v202
v_exp_f32 v203, v203
v_exp_f32 v204, v204
v_exp_f32 v205, v205
v_exp_f32 v206, v206
v_exp_f32 v207, v207
v_mul_f32 v220, v220, v219
v_add_f32 v220, v220, v192
v_add_f32 v220, v220, v193
v_add_f32 v220, v220, v194
v_add_f32 v220, v220, v195
v_add_f32 v220, v220, v196
v_add_f32 v220, v220, v197
v_add_f32 v220, v220, v198
v_add_f32 v220, v220, v199
v_add_f32 v220, v220, v200
v_add_f32 v220, v220, v201
v_add_f32 v220, v220, v202
v_add_f32 v220, v220, v203
v_add_f32 v220, v220, v204
v_add_f32 v220, v220, v205
v_add_f32 v220, v220, v206
v_add_f32 v220, v220, v207
v_cvt_pk_bf16_f32 v208, v192, v193
v_cvt_pk_bf16_f32 v209, v194, v195
v_cvt_pk_bf16_f32 v210, v196, v197
v_cvt_pk_bf16_f32 v211, v198, v199
v_cvt_pk_bf16_f32 v212, v200, v201
v_cvt_pk_bf16_f32 v213, v202, v203
v_cvt_pk_bf16_f32 v214, v204, v205
v_cvt_pk_bf16_f32 v215, v206, v207
v_mul_f32 v128, v219, v128
v_mul_f32 v129, v219, v129
v_mul_f32 v130, v219, v130
v_mul_f32 v131, v219, v131
v_mul_f32 v132, v219, v132
v_mul_f32 v133, v219, v133
v_mul_f32 v134, v219, v134
v_mul_f32 v135, v219, v135
v_mul_f32 v136, v219, v136
v_mul_f32 v137, v219, v137
v_mul_f32 v138, v219, v138
v_mul_f32 v139, v219, v139
v_mul_f32 v140, v219, v140
v_mul_f32 v141, v219, v141
v_mul_f32 v142, v219, v142
v_mul_f32 v143, v219, v143
v_mul_f32 v144, v219, v144
v_mul_f32 v145, v219, v145
v_mul_f32 v146, v219, v146
v_mul_f32 v147, v219, v147
v_mul_f32 v148, v219, v148
v_mul_f32 v149, v219, v149
v_mul_f32 v150, v219, v150
v_mul_f32 v151, v219, v151
v_mul_f32 v152, v219, v152
v_mul_f32 v153, v219, v153
v_mul_f32 v154, v219, v154
v_mul_f32 v155, v219, v155
v_mul_f32 v156, v219, v156
v_mul_f32 v157, v219, v157
v_mul_f32 v158, v219, v158
v_mul_f32 v159, v219, v159
v_mul_f32 v160, v219, v160
v_mul_f32 v161, v219, v161
v_mul_f32 v162, v219, v162
v_mul_f32 v163, v219, v163
v_mul_f32 v164, v219, v164
v_mul_f32 v165, v219, v165
v_mul_f32 v166, v219, v166
v_mul_f32 v167, v219, v167
v_mul_f32 v168, v219, v168
v_mul_f32 v169, v219, v169
v_mul_f32 v170, v219, v170
v_mul_f32 v171, v219, v171
v_mul_f32 v172, v219, v172
v_mul_f32 v173, v219, v173
v_mul_f32 v174, v219, v174
v_mul_f32 v175, v219, v175
v_mul_f32 v176, v219, v176
v_mul_f32 v177, v219, v177
v_mul_f32 v178, v219, v178
v_mul_f32 v179, v219, v179
v_mul_f32 v180, v219, v180
v_mul_f32 v181, v219, v181
v_mul_f32 v182, v219, v182
v_mul_f32 v183, v219, v183
v_mul_f32 v184, v219, v184
v_mul_f32 v185, v219, v185
v_mul_f32 v186, v219, v186
v_mul_f32 v187, v219, v187
v_mul_f32 v188, v219, v188
v_mul_f32 v189, v219, v189
v_mul_f32 v190, v219, v190
v_mul_f32 v191, v219, v191
s_waitcnt vmcnt(0)
v_mfma_f32_32x32x16_bf16 v[128:143], a[32:35], v[208:211], v[128:143]
v_mfma_f32_32x32x16_bf16 v[144:159], a[36:39], v[208:211], v[144:159]
v_mfma_f32_32x32x16_bf16 v[160:175], a[40:43], v[208:211], v[160:175]
v_mfma_f32_32x32x16_bf16 v[176:191], a[44:47], v[208:211], v[176:191]
v_mfma_f32_32x32x16_bf16 v[128:143], a[48:51], v[212:215], v[128:143]
v_mfma_f32_32x32x16_bf16 v[144:159], a[52:55], v[212:215], v[144:159]
v_mfma_f32_32x32x16_bf16 v[160:175], a[56:59], v[212:215], v[160:175]
v_mfma_f32_32x32x16_bf16 v[176:191], a[60:63], v[212:215], v[176:191]
v_subrev_u32 v221, 32, v221
s_add_u32 s44, s44, 8192
s_add_u32 s45, s45, 8192
s_add_u32 s46, s46, 1
s_cmp_le_u32 s46, s50
s_cbranch_scc1 .Lx_loop
v_mov_b32 v218, v220
s_nop 1
v_permlane32_swap_b32 v220, v218
v_add_f32 v220, v220, v218
s_setpc_b64 s[42:43]
.Lspin6:
s_mov_b32 s93, 0
.Lspin6_l:
s_sleep 4
buffer_load_dword v250, v249, s[60:63], 0 offen sc1
s_waitcnt vmcnt(0)
v_cmp_eq_u32 vcc, 0, v250
s_cmp_lg_u64 vcc, 0
s_cbranch_scc0 .Lspin6_ret
s_add_u32 s93, s93, 1
s_cmp_lt_u32 s93, 0x100000
s_cbranch_scc1 .Lspin6_l
s_branch .Lspin6_ret
.Lspin3:
s_mov_b32 s93, 0
.Lspin3_l:
s_sleep 4
buffer_load_dword v250, v249, s[60:63], s90 offen sc1
s_waitcnt vmcnt(0)
v_and_b32 v250, 7, v250
v_cmp_gt_u32 vcc, 3, v250
s_cmp_lg_u64 vcc, 0
s_cbranch_scc0 .Lspin3_ret
s_add_u32 s93, s93, 1
s_cmp_lt_u32 s93, 0x100000
s_cbranch_scc1 .Lspin3_l
s_branch .Lspin3_ret
.Lspin5:
s_mov_b32 s93, 0
.Lspin5_l:
s_sleep 4
buffer_load_dword v250, v249, s[60:63], s90 offen sc1
s_waitcnt vmcnt(0)
v_and_b32 v250, 7, v250
v_cmp_gt_u32 vcc, 5, v250
s_cmp_lg_u64 vcc, 0
s_cbranch_scc0 .Lspin5_ret
s_add_u32 s93, s93, 1
s_cmp_lt_u32 s93, 0x100000
s_cbranch_scc1 .Lspin5_l
s_branch .Lspin5_ret
.Lspin4:
s_mov_b32 s93, 0
.Lspin4_l:
s_sleep 4
buffer_load_dword v250, v249, s[60:63], s90 offen sc1
s_waitcnt vmcnt(0)
v_and_b32 v250, 7, v250
v_cmp_gt_u32 vcc, 4, v250
s_cmp_lg_u64 vcc, 0
s_cbranch_scc0 .Lspin4_ret
s_add_u32 s93, s93, 1
s_cmp_lt_u32 s93, 0x100000
s_cbranch_scc1 .Lspin4_l
s_branch .Lspin4_ret
.Lpret:
.Lcode1:
)ASM"
    :: "{s[0:1]}"(__builtin_amdgcn_kernarg_segment_ptr()), "{s2}"(blockIdx.x), "{v0}"(threadIdx.x),
       "{s3}"((unsigned)(size_t)(__attribute__((address_space(3))) char*)smem)
    : "v0", "v1", "v2", "v3", "v4", "v5", "v6", "v7", "v8", "v9", "v10", "v11", "v12", "v13", "v14", "v15", "v16", "v17", "v18", "v19", "v20", "v21", "v22", "v23", "v24", "v25", "v26", "v27", "v28", "v29", "v30", "v31", "v32", "v33", "v34", "v35", "v36", "v37", "v38", "v39", "v40", "v41", "v42", "v43", "v44", "v45", "v46", "v47", "v48", "v49", "v50", "v51", "v52", "v53", "v54", "v55", "v56", "v57", "v58", "v59", "v60", "v61", "v62", "v63", "v64", "v65", "v66", "v67", "v68", "v69", "v70", "v71", "v72", "v73", "v74", "v75", "v76", "v77", "v78", "v79", "v80", "v81", "v82", "v83", "v84", "v85", "v86", "v87", "v88", "v89", "v90", "v91", "v92", "v93", "v94", "v95", "v96", "v97", "v98", "v99", "v100", "v101", "v102", "v103", "v104", "v105", "v106", "v107", "v108", "v109", "v110", "v111", "v112", "v113", "v114", "v115", "v116", "v117", "v118", "v119", "v120", "v121", "v122", "v123", "v124", "v125", "v126", "v127", "v128", "v129", "v130", "v131", "v132", "v133", "v134", "v135", "v136", "v137", "v138", "v139", "v140", "v141", "v142", "v143", "v144", "v145", "v146", "v147", "v148", "v149", "v150", "v151", "v152", "v153", "v154", "v155", "v156", "v157", "v158", "v159", "v160", "v161", "v162", "v163", "v164", "v165", "v166", "v167", "v168", "v169", "v170", "v171", "v172", "v173", "v174", "v175", "v176", "v177", "v178", "v179", "v180", "v181", "v182", "v183", "v184", "v185", "v186", "v187", "v188", "v189", "v190", "v191", "v192", "v193", "v194", "v195", "v196", "v197", "v198", "v199", "v200", "v201", "v202", "v203", "v204", "v205", "v206", "v207", "v208", "v209", "v210", "v211", "v212", "v213", "v214", "v215", "v216", "v217", "v218", "v219", "v220", "v221", "v222", "v223", "v224", "v225", "v226", "v227", "v228", "v229", "v230", "v231", "v232", "v233", "v234", "v235", "v236", "v237", "v238", "v239", "v240", "v241", "v242", "v243", "v244", "v245", "v246", "v247", "v248", "v249", "v250", "v251", "v252", "v253", "v254", "v255", "a0", "a1", "a2", "a3", "a4", "a5", "a6", "a7", "a8", "a9", "a10", "a11", "a12", "a13", "a14", "a15", "a16", "a17", "a18", "a19", "a20", "a21", "a22", "a23", "a24", "a25", "a26", "a27", "a28", "a29", "a30", "a31", "a32", "a33", "a34", "a35", "a36", "a37", "a38", "a39", "a40", "a41", "a42", "a43", "a44", "a45", "a46", "a47", "a48", "a49", "a50", "a51", "a52", "a53", "a54", "a55", "a56", "a57", "a58", "a59", "a60", "a61", "a62", "a63", "a64", "a65", "a66", "a67", "a68", "a69", "a70", "a71", "a72", "a73", "a74", "a75", "a76", "a77", "a78", "a79", "a80", "a81", "a82", "a83", "a84", "a85", "a86", "a87", "a88", "a89", "a90", "a91", "a92", "a93", "a94", "a95", "a96", "a97", "a98", "a99", "a100", "a101", "a102", "a103", "a104", "a105", "a106", "a107", "a108", "a109", "a110", "a111", "a112", "a113", "a114", "a115", "a116", "a117", "a118", "a119", "a120", "a121", "a122", "a123", "a124", "a125", "a126", "a127", "a128", "a129", "a130", "a131", "a132", "a133", "a134", "a135", "a136", "a137", "a138", "a139", "a140", "a141", "a142", "a143", "a144", "a145", "a146", "a147", "a148", "a149", "a150", "a151", "a152", "a153", "a154", "a155", "a156", "a157", "a158", "a159", "a160", "a161", "a162", "a163", "a164", "a165", "a166", "a167", "a168", "a169", "a170", "a171", "a172", "a173", "a174", "a175", "a176", "a177", "a178", "a179", "a180", "a181", "a182", "a183", "a184", "a185", "a186", "a187", "a188", "a189", "a190", "a191", "a192", "a193", "a194", "a195", "a196", "a197", "a198", "a199", "a200", "a201", "a202", "a203", "a204", "a205", "a206", "a207", "a208", "a209", "a210", "a211", "a212", "a213", "a214", "a215", "a216", "a217", "a218", "a219", "a220", "a221", "a222", "a223", "a224", "a225", "a226", "a227", "a228", "a229", "a230", "a231", "a232", "a233", "a234", "a235", "a236", "a237", "a238", "a239", "a240", "a241", "a242", "a243", "a244", "a245", "a246", "a247", "a248", "a249", "a250", "a251", "a252", "a253", "a254", "a255", "s3", "s4", "s5", "s6", "s7", "s8", "s9", "s10", "s11", "s12", "s13", "s14", "s15", "s16", "s17", "s18", "s19", "s20", "s21", "s22", "s23", "s24", "s25", "s26", "s27", "s28", "s29", "s30", "s31", "s32", "s33", "s34", "s35", "s36", "s37", "s38", "s39", "s40", "s41", "s42", "s43", "s44", "s45", "s46", "s47", "s48", "s49", "s50", "s51", "s52", "s53", "s54", "s55", "s56", "s57", "s58", "s59", "s60", "s61", "s62", "s63", "s64", "s65", "s66", "s67", "s68", "s69", "s70", "s71", "s72", "s73", "s74", "s75", "s76", "s77", "s78", "s79", "s80", "s81", "s82", "s83", "s84", "s85", "s86", "s87", "s88", "s89", "s90", "s91", "s92", "s93", "s94", "s95", "s96", "s97", "s98", "s99", "s100", "s101", "vcc", "m0", "scc", "memory");
}
extern "C" void kernel_launch(void* const* d_in, const int* in_sizes, int n_in, void* d_out, int out_size,
                              void* d_ws, size_t ws_size, hipStream_t stream) {
  Args a;
  for (int i = 0; i < 9; ++i) a.p[i] = d_in[i];
  a.out = d_out; a.ws = (char*)d_ws + (0 ? ((ws_size - (72u << 20)) & ~(size_t)0xfffff) : 0);
  mega_kernel<<<256, 256, 0, stream>>>(a);
}
```

```cpp
#include <hip/hip_runtime.h>
#include <stdint.h>
struct Args { const void* p[9]; void* out; void* ws; };
__global__ __launch_bounds__(256, 1) __attribute__((aligned(4096))) void mega_kernel(Args args) {
  __shared__ __attribute__((aligned(16))) char smem[163840];
  asm volatile(R"ASM(
s_getpc_b64 s[96:97]
.Lcode0:
s_load_dwordx16 s[64:79], s[0:1], 0
s_load_dwordx4 s[80:83], s[0:1], 64
s_load_dwordx2 s[84:85], s[0:1], 80
v_and_b32 v1, 63, v0
v_lshrrev_b32 v16, 6, v0
v_lshlrev_b32 v7, 4, v1
v_readfirstlane_b32 s3, v16
s_and_b32 s86, s2, 7
s_lshr_b32 s87, s2, 3
s_and_b32 s4, s87, 3
s_lshl_b32 s88, s86, 2
s_add_u32 s88, s88, s4
s_lshr_b32 s89, s2, 5
s_lshl_b32 s91, s3, 12
s_lshl_b32 s92, s3, 13
s_and_b32 s97, s97, 0xffff
s_mov_b32 s98, .Lcode1-.Lcode0
s_mov_b32 s99, 0x20000
s_lshl_b32 s33, s87, 2
s_add_u32 s33, s33, s3
s_lshl_b32 s33, s33, 10
v_and_b32 v4, 7, v1
v_lshlrev_b32 v4, 7, v4
v_add_u32 v4, s33, v4
buffer_load_dword v35, v4, s[96:99], 0 offen
s_and_b32 s27, s87, 7
s_bfe_u32 s28, s87, 0x10003
s_lshl_b32 s29, s86, 1
s_add_u32 s28, s28, s29
v_lshrrev_b32 v40, 3, v1
v_and_b32 v41, 7, v1
v_xor_b32 v41, v41, v40
v_lshlrev_b32 v41, 4, v41
v_lshl_add_u32 v202, v40, 10, v41
v_add_u32 v203, 7168, v202
v_add_u32 v204, 14336, v202
v_add_u32 v205, 21504, v202
v_lshl_add_u32 v20, v40, 11, v41
v_add_u32 v21, 15360, v20
v_add_u32 v22, 30720, v20
v_add_u32 v23, 46080, v20
v_lshrrev_b32 v40, 4, v1
v_and_b32 v41, 15, v1
v_lshlrev_b32 v42, 1, v40
v_xor_b32 v41, v41, v42
v_lshlrev_b32 v42, 4, v41
v_lshl_add_u32 v16, v40, 13, v42
v_add_u32 v17, 31744, v16
v_xor_b32 v18, 0x80, v16
v_add_u32 v18, 63488, v18
v_add_u32 v19, 31744, v18
s_and_b32 s30, s3, 1
s_lshr_b32 s31, s3, 1
s_lshl_b32 s32, s30, 20
s_lshl_b32 s33, s27, 16
s_add_u32 s32, s32, s33
s_lshl_b32 s33, s31, 15
s_add_u32 s32, s32, s33
s_lshl_b32 s34, s28, 9
s_lshl_b32 s33, s31, 8
s_add_u32 s34, s34, s33
s_lshl_b32 s33, s30, 18
s_add_u32 s34, s34, s33
s_sub_u32 s35, 0x200000, s32
s_sub_u32 s36, 0x400000, s34
s_cmp_lt_u32 s87, 16
s_cselect_b32 s46, 1, 0
s_waitcnt lgkmcnt(0)
s_cselect_b32 s38, s76, s78
s_cselect_b32 s39, s77, s79
s_add_u32 s8, s38, s34
s_addc_u32 s9, s39, 0
s_and_b32 s9, s9, 0xffff
s_mov_b32 s10, s36
s_mov_b32 s11, 0x20000
s_add_u32 s4, s72, s32
s_addc_u32 s5, s73, 0
s_and_b32 s5, s5, 0xffff
s_mov_b32 s6, s35
s_mov_b32 s7, 0x20000
s_add_u32 s37, s92, 0
s_mov_b32 s42, 0
s_mov_b32 s43, 131072
s_mov_b32 m0, s37
s_nop 0
buffer_load_dwordx4 v16, s[8:11], s42 offen offset:0 nt lds
buffer_load_dwordx4 v17, s[8:11], s42 offen offset:1024 nt lds
buffer_load_dwordx4 v18, s[8:11], s42 offen offset:2048 nt lds
buffer_load_dwordx4 v19, s[8:11], s42 offen offset:3072 nt lds
s_add_u32 m0, s37, 4096
s_nop 0
buffer_load_dwordx4 v16, s[8:11], s43 offen offset:0 nt lds
buffer_load_dwordx4 v17, s[8:11], s43 offen offset:1024 nt lds
buffer_load_dwordx4 v18, s[8:11], s43 offen offset:2048 nt lds
buffer_load_dwordx4 v19, s[8:11], s43 offen offset:3072 nt lds
s_add_u32 s60, s84, 62914560
s_addc_u32 s61, s85, 0
s_and_b32 s61, s61, 0xffff
s_mov_b32 s62, 82944
s_mov_b32 s63, 0x20000
s_lshl_b32 s90, s86, 13
v_lshlrev_b32 v3, 5, v0
s_lshl_b32 s93, s2, 4
s_add_u32 s93, s93, s3
s_lshl_b32 s93, s93, 2
s_add_u32 s93, s93, 0x10000
s_add_u32 s37, s92, 32768
s_mov_b32 s40, 0
s_mov_b32 s41, 524288
s_mov_b32 m0, s37
s_nop 0
buffer_load_dwordx4 v202, s[4:7], s40 offen offset:0 lds
v_lshlrev_b32 v45, 20, v1
v_add_u32 v44, s93, v45
s_lshl_b32 s93, s87, 2
s_add_u32 s93, s93, s3
s_xor_b32 s93, s93, 32
s_lshl_b32 s93, s93, 20
s_lshl_b32 s0, s86, 2
s_add_u32 s93, s93, s0
s_add_u32 s93, s93, 0x14000
v_lshlrev_b32 v45, 7, v1
v_add_u32 v46, s93, v45
buffer_load_dwordx4 v203, s[4:7], s40 offen offset:1024 lds
v_mov_b32 v45, 0
s_lshl_b32 s33, s2, 5
v_lshlrev_b32 v41, 13, v0
v_lshrrev_b32 v42, 3, v0
v_lshl_add_u32 v41, v42, 20, v41
v_add_u32 v6, s33, v41
s_and_b32 s33, s87, 2
s_lshl_b32 s33, s33, 5
s_or_b32 s33, s33, s86
v_and_b32 v41, 0x47, v0
v_cmp_eq_u32 vcc, s33, v41
buffer_load_dwordx4 v204, s[4:7], s40 offen offset:2048 lds
s_nop 0
v_cndmask_b32 v14, 0, 1, vcc
s_lshl_b32 s33, s30, 14
s_add_u32 s33, s33, 32768
v_and_b32 v36, 15, v1
v_lshrrev_b32 v37, 4, v1
v_and_b32 v38, 7, v36
v_xor_b32 v38, v38, v37
v_lshlrev_b32 v38, 4, v38
v_lshl_add_u32 v192, v36, 7, v38
v_add_u32 v192, s33, v192
v_xor_b32 v193, 64, v192
buffer_load_dwordx4 v205, s[4:7], s40 offen offset:3072 lds
s_mov_b64 exec, 0
s_cmp_eq_u32 s3, 0
s_cbranch_scc1 .Lpad1
s_cmp_eq_u32 s3, 1
s_cbranch_scc1 .Lp1_v_pad
s_cmp_eq_u32 s3, 2
s_cbranch_scc1 .Lpad2
s_branch .Lpad3
.Ltch0:
s_mov_b64 exec, -1
s_lshl_b32 s33, s31, 14
v_and_b32 v36, 15, v1
v_lshrrev_b32 v37, 4, v1
v_lshrrev_b32 v38, 2, v36
v_and_b32 v39, 3, v36
v_lshl_add_u32 v40, v37, 3, v38
v_and_b32 v41, 1, v37
v_lshl_or_b32 v41, v41, 2, v38
v_lshlrev_b32 v40, 8, v40
v_lshl_add_u32 v40, v39, 3, v40
v_add_u32 v40, s33, v40
s_add_u32 m0, s37, 4096
s_nop 0
buffer_load_dwordx4 v202, s[4:7], s41 offen offset:0 lds
v_xor_b32 v36, 0, v41
v_lshl_add_u32 v194, v36, 5, v40
v_xor_b32 v36, 1, v41
v_lshl_add_u32 v195, v36, 5, v40
v_xor_b32 v36, 2, v41
v_lshl_add_u32 v196, v36, 5, v40
v_xor_b32 v36, 3, v41
v_lshl_add_u32 v197, v36, 5, v40
v_xor_b32 v36, 4, v41
v_lshl_add_u32 v198, v36, 5, v40
v_xor_b32 v36, 5, v41
buffer_load_dwordx4 v203, s[4:7], s41 offen offset:1024 lds
v_lshl_add_u32 v199, v36, 5, v40
v_xor_b32 v36, 6, v41
v_lshl_add_u32 v200, v36, 5, v40
v_xor_b32 v36, 7, v41
v_lshl_add_u32 v201, v36, 5, v40
s_and_b32 s95, s27, 3
s_add_u32 s34, s46, 5
s_lshl_b32 s33, s95, s34
v_xor_b32 v194, s33, v194
v_xor_b32 v195, s33, v195
v_xor_b32 v196, s33, v196
buffer_load_dwordx4 v204, s[4:7], s41 offen offset:2048 lds
v_xor_b32 v197, s33, v197
v_xor_b32 v198, s33, v198
v_xor_b32 v199, s33, v199
v_xor_b32 v200, s33, v200
v_xor_b32 v201, s33, v201
s_lshl_b32 s95, s95, 10
s_add_u32 s33, s92, 32768
v_and_b32 v36, 15, v1
v_lshrrev_b32 v37, 4, v1
v_and_b32 v38, 7, v36
v_xor_b32 v38, v38, v37
buffer_load_dwordx4 v205, s[4:7], s41 offen offset:3072 lds
s_mov_b64 exec, 0
s_cmp_eq_u32 s3, 0
s_cbranch_scc1 .Lp2_pad
s_cmp_eq_u32 s3, 1
s_cbranch_scc1 .Lpad4
s_cmp_eq_u32 s3, 2
s_cbranch_scc1 .Lpad5
s_branch .Lpad6
.Ltch1:
s_mov_b64 exec, -1
v_lshlrev_b32 v38, 4, v38
v_lshl_add_u32 v24, v36, 7, v38
v_add_u32 v24, s33, v24
v_xor_b32 v25, 64, v24
v_and_b32 v36, 15, v1
v_lshrrev_b32 v37, 4, v1
v_lshrrev_b32 v38, 2, v36
v_and_b32 v39, 3, v36
v_lshl_add_u32 v40, v37, 3, v38
v_and_b32 v41, 1, v37
v_lshl_or_b32 v41, v41, 2, v38
s_add_u32 s37, s92, 65536
s_mov_b32 s42, 524288
s_mov_b32 s43, 655360
s_mov_b32 m0, s37
s_nop 0
buffer_load_dwordx4 v16, s[8:11], s42 offen offset:0 nt lds
v_lshlrev_b32 v40, 8, v40
v_lshl_add_u32 v40, v39, 3, v40
v_add_u32 v40, 65536, v40
v_xor_b32 v36, 0, v41
v_lshl_add_u32 v26, v36, 5, v40
v_xor_b32 v36, 1, v41
v_lshl_add_u32 v27, v36, 5, v40
v_xor_b32 v36, 2, v41
v_lshl_add_u32 v28, v36, 5, v40
v_xor_b32 v36, 3, v41
v_lshl_add_u32 v29, v36, 5, v40
buffer_load_dwordx4 v17, s[8:11], s42 offen offset:1024 nt lds
v_xor_b32 v36, 4, v41
v_lshl_add_u32 v30, v36, 5, v40
v_xor_b32 v36, 5, v41
v_lshl_add_u32 v31, v36, 5, v40
v_xor_b32 v36, 6, v41
v_lshl_add_u32 v32, v36, 5, v40
v_xor_b32 v36, 7, v41
v_lshl_add_u32 v33, v36, 5, v40
s_add_u32 s12, s70, 0
s_addc_u32 s13, s71, 0
s_and_b32 s13, s13, 0xffff
buffer_load_dwordx4 v18, s[8:11], s42 offen offset:2048 nt lds
s_mov_b32 s14, 4194304
s_mov_b32 s15, 0x20000
s_lshl_b32 s33, s88, 8
s_lshl_b32 s34, s3, 17
s_add_u32 s33, s33, s34
s_sub_u32 s34, 0x800000, s33
s_add_u32 s16, s74, s33
s_addc_u32 s17, s75, 0
s_and_b32 s17, s17, 0xffff
s_mov_b32 s18, s34
s_mov_b32 s19, 0x20000
buffer_load_dwordx4 v19, s[8:11], s42 offen offset:3072 nt lds
s_mov_b64 exec, 0
s_cmp_eq_u32 s3, 0
s_cbranch_scc1 .Lpad7
s_cmp_eq_u32 s3, 1
s_cbranch_scc1 .Lpad8
s_cmp_eq_u32 s3, 2
s_cbranch_scc1 .Lpad9
s_branch .Lj0_pad
.Ltch2:
s_mov_b64 exec, -1
s_sub_u32 s24, 15, s89
s_lshl_b32 s24, s24, 18
s_lshl_b32 s25, s89, 18
s_lshl_b32 s33, s3, 16
s_add_u32 s94, s3, 1
s_and_b32 s94, s94, 3
s_add_u32 s24, s24, s33
s_lshl_b32 s33, s94, 16
s_add_u32 s25, s25, s33
s_mov_b32 s26, 0
s_lshl_b32 s33, s28, 1
s_add_u32 m0, s37, 4096
s_nop 0
buffer_load_dwordx4 v16, s[8:11], s43 offen offset:0 nt lds
s_add_u32 s33, s33, s31
s_lshl_b32 s33, s33, 19
s_lshl_b32 s34, s27, 1
s_add_u32 s34, s34, s30
s_lshl_b32 s34, s34, 13
s_add_u32 s33, s33, s34
s_xor_b32 s34, s46, 1
s_lshl_b32 s34, s34, 24
s_add_u32 s33, s33, s34
s_add_u32 s20, s84, s33
s_addc_u32 s21, s85, 0
buffer_load_dwordx4 v17, s[8:11], s43 offen offset:1024 nt lds
s_and_b32 s21, s21, 0xffff
s_mov_b32 s22, 524288
s_mov_b32 s23, 0x20000
s_cmp_eq_u32 s46, 1
s_cselect_b32 s47, 1040356334, 1.0
s_lshl_b32 s33, s87, 2
s_add_u32 s33, s33, s3
s_lshl_b32 s33, s33, 13
s_lshl_b32 s34, s86, 10
s_add_u32 s33, s33, s34
v_and_b32 v36, 7, v1
buffer_load_dwordx4 v18, s[8:11], s43 offen offset:2048 nt lds
v_lshlrev_b32 v36, 7, v36
v_add_u32 v206, s33, v36
s_lshl_b32 s33, s2, 2
s_add_u32 s33, s33, s3
s_lshl_b32 s33, s33, 12
v_bfe_u32 v37, v1, 1, 1
v_and_b32 v38, 1, v1
v_lshlrev_b32 v37, 11, v37
v_lshl_add_u32 v37, v38, 7, v37
v_add_u32 v207, s33, v37
s_mov_b32 s56, s74
buffer_load_dwordx4 v19, s[8:11], s43 offen offset:3072 nt lds
s_and_b32 s57, s75, 0xffff
s_mov_b32 s58, 0x800000
s_mov_b32 s59, 0x20000
s_mov_b32 s48, 0x100000
s_mov_b32 s49, 256
s_mov_b32 s35, s92
s_add_u32 s36, s92, 32768
s_add_u32 s39, s92, 163840
s_mov_b32 s54, 65536
s_mov_b32 s55, -98304
s_mov_b32 s38, 1
s_waitcnt vmcnt(8)
s_barrier
ds_read_b64_tr_b16 v[96:97], v194 offset:0
ds_read_b64_tr_b16 v[98:99], v194 offset:1024
ds_read_b128 v[64:67], v192 offset:0
ds_read_b64_tr_b16 v[100:101], v195 offset:0
ds_read_b64_tr_b16 v[102:103], v195 offset:1024
ds_read_b128 v[68:71], v192 offset:2048
ds_read_b64_tr_b16 v[104:105], v196 offset:0
ds_read_b64_tr_b16 v[106:107], v196 offset:1024
ds_read_b128 v[72:75], v192 offset:4096
ds_read_b64_tr_b16 v[108:109], v197 offset:0
ds_read_b64_tr_b16 v[110:111], v197 offset:1024
ds_read_b128 v[76:79], v192 offset:6144
s_waitcnt lgkmcnt(6)
ds_read_b64_tr_b16 v[112:113], v198 offset:0
ds_read_b64_tr_b16 v[114:115], v198 offset:1024
ds_read_b128 v[80:83], v192 offset:8192
ds_read_b64_tr_b16 v[116:117], v199 offset:0
ds_read_b64_tr_b16 v[118:119], v199 offset:1024
ds_read_b128 v[84:87], v192 offset:10240
ds_read_b64_tr_b16 v[120:121], v200 offset:0
ds_read_b64_tr_b16 v[122:123], v200 offset:1024
ds_read_b128 v[88:91], v192 offset:12288
ds_read_b64_tr_b16 v[124:125], v201 offset:0
ds_read_b64_tr_b16 v[126:127], v201 offset:1024
ds_read_b128 v[92:95], v192 offset:14336
s_cmp_eq_u32 s46, 1
s_waitcnt lgkmcnt(0)
s_cbranch_scc0 .Lp1_v
v_mfma_f32_16x16x32_bf16 a[0:3], v[96:99], v[64:67], 0
ds_read_b64_tr_b16 v[160:161], v194 offset:8192
v_mfma_f32_16x16x32_bf16 a[4:7], v[100:103], v[64:67], 0
s_add_u32 s37, s92, 98304
s_mov_b32 s40, 128
s_mov_b32 s41, 524416
v_mfma_f32_16x16x32_bf16 a[8:11], v[104:107], v[64:67], 0
s_mov_b32 m0, s37
ds_read_b64_tr_b16 v[162:163], v194 offset:9216
v_mfma_f32_16x16x32_bf16 a[12:15], v[108:111], v[64:67], 0
buffer_load_dwordx4 v202, s[4:7], s40 offen offset:0 lds
v_mfma_f32_16x16x32_bf16 a[16:19], v[112:115], v[64:67], 0
ds_read_b128 v[128:131], v193 offset:0
v_mfma_f32_16x16x32_bf16 a[20:23], v[116:119], v[64:67], 0
buffer_load_dwordx4 v203, s[4:7], s40 offen offset:1024 lds
v_mfma_f32_16x16x32_bf16 a[24:27], v[120:123], v[64:67], 0
ds_read_b64_tr_b16 v[164:165], v195 offset:8192
v_mfma_f32_16x16x32_bf16 a[28:31], v[124:127], v[64:67], 0
buffer_load_dwordx4 v204, s[4:7], s40 offen offset:2048 lds
v_mfma_f32_16x16x32_bf16 a[32:35], v[96:99], v[68:71], 0
ds_read_b64_tr_b16 v[166:167], v195 offset:9216
v_mfma_f32_16x16x32_bf16 a[36:39], v[100:103], v[68:71], 0
buffer_load_dwordx4 v205, s[4:7], s40 offen offset:3072 lds
v_mfma_f32_16x16x32_bf16 a[40:43], v[104:107], v[68:71], 0
s_add_u32 m0, s37, 4096
ds_read_b128 v[132:135], v193 offset:2048
v_mfma_f32_16x16x32_bf16 a[44:47], v[108:111], v[68:71], 0
buffer_load_dwordx4 v202, s[4:7], s41 offen offset:0 lds
v_mfma_f32_16x16x32_bf16 a[48:51], v[112:115], v[68:71], 0
ds_read_b64_tr_b16 v[168:169], v196 offset:8192
v_mfma_f32_16x16x32_bf16 a[52:55], v[116:119], v[68:71], 0
buffer_load_dwordx4 v203, s[4:7], s41 offen offset:1024 lds
v_mfma_f32_16x16x32_bf16 a[56:59], v[120:123], v[68:71], 0
ds_read_b64_tr_b16 v[170:171], v196 offset:9216
v_mfma_f32_16x16x32_bf16 a[60:63], v[124:127], v[68:71], 0
buffer_load_dwordx4 v204, s[4:7], s41 offen offset:2048 lds
v_mfma_f32_16x16x32_bf16 a[64:67], v[96:99], v[72:75], 0
ds_read_b128 v[136:139], v193 offset:4096
v_mfma_f32_16x16x32_bf16 a[68:71], v[100:103], v[72:75], 0
buffer_load_dwordx4 v205, s[4:7], s41 offen offset:3072 lds
v_mfma_f32_16x16x32_bf16 a[72:75], v[104:107], v[72:75], 0
ds_read_b64_tr_b16 v[172:173], v197 offset:8192
v_mfma_f32_16x16x32_bf16 a[76:79], v[108:111], v[72:75], 0
s_add_u32 s37, s92, 131072
s_mov_b32 s42, 1048576
s_mov_b32 s43, 1179648
v_mfma_f32_16x16x32_bf16 a[80:83], v[112:115], v[72:75], 0
s_mov_b32 m0, s37
ds_read_b64_tr_b16 v[174:175], v197 offset:9216
v_mfma_f32_16x16x32_bf16 a[84:87], v[116:119], v[72:75], 0
buffer_load_dwordx4 v16, s[8:11], s42 offen offset:0 nt lds
v_mfma_f32_16x16x32_bf16 a[88:91], v[120:123], v[72:75], 0
ds_read_b128 v[140:143], v193 offset:6144
v_mfma_f32_16x16x32_bf16 a[92:95], v[124:127], v[72:75], 0
buffer_load_dwordx4 v17, s[8:11], s42 offen offset:1024 nt lds
v_mfma_f32_16x16x32_bf16 a[96:99], v[96:99], v[76:79], 0
ds_read_b64_tr_b16 v[176:177], v198 offset:8192
v_mfma_f32_16x16x32_bf16 a[100:103], v[100:103], v[76:79], 0
buffer_load_dwordx4 v18, s[8:11], s42 offen offset:2048 nt lds
v_mfma_f32_16x16x32_bf16 a[104:107], v[104:107], v[76:79], 0
ds_read_b64_tr_b16 v[178:179], v198 offset:9216
v_mfma_f32_16x16x32_bf16 a[108:111], v[108:111], v[76:79], 0
buffer_load_dwordx4 v19, s[8:11], s42 offen offset:3072 nt lds
v_mfma_f32_16x16x32_bf16 a[112:115], v[112:115], v[76:79], 0
s_add_u32 m0, s37, 4096
ds_read_b128 v[144:147], v193 offset:8192
v_mfma_f32_16x16x32_bf16 a[116:119], v[116:119], v[76:79], 0
buffer_load_dwordx4 v16, s[8:11], s43 offen offset:0 nt lds
v_mfma_f32_16x16x32_bf16 a[120:123], v[120:123], v[76:79], 0
ds_read_b64_tr_b16 v[180:181], v199 offset:8192
v_mfma_f32_16x16x32_bf16 a[124:127], v[124:127], v[76:79], 0
buffer_load_dwordx4 v17, s[8:11], s43 offen offset:1024 nt lds
v_mfma_f32_16x16x32_bf16 a[128:131], v[96:99], v[80:83], 0
ds_read_b64_tr_b16 v[182:183], v199 offset:9216
v_mfma_f32_16x16x32_bf16 a[132:135], v[100:103], v[80:83], 0
buffer_load_dwordx4 v18, s[8:11], s43 offen offset:2048 nt lds
v_mfma_f32_16x16x32_bf16 a[136:139], v[104:107], v[80:83], 0
ds_read_b128 v[148:151], v193 offset:10240
v_mfma_f32_16x16x32_bf16 a[140:143], v[108:111], v[80:83], 0
buffer_load_dwordx4 v19, s[8:11], s43 offen offset:3072 nt lds
v_mfma_f32_16x16x32_bf16 a[144:147], v[112:115], v[80:83], 0
ds_read_b64_tr_b16 v[184:185], v200 offset:8192
v_mfma_f32_16x16x32_bf16 a[148:151], v[116:119], v[80:83], 0
v_mfma_f32_16x16x32_bf16 a[152:155], v[120:123], v[80:83], 0
ds_read_b64_tr_b16 v[186:187], v200 offset:9216
v_mfma_f32_16x16x32_bf16 a[156:159], v[124:127], v[80:83], 0
v_mfma_f32_16x16x32_bf16 a[160:163], v[96:99], v[84:87], 0
ds_read_b128 v[152:155], v193 offset:12288
v_mfma_f32_16x16x32_bf16 a[164:167], v[100:103], v[84:87], 0
v_mfma_f32_16x16x32_bf16 a[168:171], v[104:107], v[84:87], 0
ds_read_b64_tr_b16 v[188:189], v201 offset:8192
v_mfma_f32_16x16x32_bf16 a[172:175], v[108:111], v[84:87], 0
v_mfma_f32_16x16x32_bf16 a[176:179], v[112:115], v[84:87], 0
s_add_u32 s44, s35, 65536
ds_read_b64_tr_b16 v[190:191], v201 offset:9216
v_mfma_f32_16x16x32_bf16 a[180:183], v[116:119], v[84:87], 0
s_cmp_ge_u32 s44, s39
s_cselect_b32 s45, s55, s54
v_mfma_f32_16x16x32_bf16 a[184:187], v[120:123], v[84:87], 0
ds_read_b128 v[156:159], v193 offset:14336
v_mfma_f32_16x16x32_bf16 a[188:191], v[124:127], v[84:87], 0
s_add_u32 s44, s36, 65536
v_mfma_f32_16x16x32_bf16 a[192:195], v[96:99], v[88:91], 0
v_mfma_f32_16x16x32_bf16 a[196:199], v[100:103], v[88:91], 0
s_cmp_ge_u32 s44, s39
s_cselect_b32 s34, s55, s54
v_mfma_f32_16x16x32_bf16 a[200:203], v[104:107], v[88:91], 0
v_add_u32 v194, s45, v194
v_mfma_f32_16x16x32_bf16 a[204:207], v[108:111], v[88:91], 0
v_add_u32 v195, s45, v195
v_mfma_f32_16x16x32_bf16 a[208:211], v[112:115], v[88:91], 0
v_add_u32 v196, s45, v196
v_mfma_f32_16x16x32_bf16 a[212:215], v[116:119], v[88:91], 0
v_add_u32 v197, s45, v197
v_mfma_f32_16x16x32_bf16 a[216:219], v[120:123], v[88:91], 0
v_add_u32 v198, s45, v198
v_mfma_f32_16x16x32_bf16 a[220:223], v[124:127], v[88:91], 0
v_add_u32 v199, s45, v199
v_mfma_f32_16x16x32_bf16 a[224:227], v[96:99], v[92:95], 0
v_add_u32 v200, s45, v200
v_mfma_f32_16x16x32_bf16 a[228:231], v[100:103], v[92:95], 0
v_add_u32 v201, s45, v201
v_mfma_f32_16x16x32_bf16 a[232:235], v[104:107], v[92:95], 0
v_add_u32 v192, s34, v192
v_mfma_f32_16x16x32_bf16 a[236:239], v[108:111], v[92:95], 0
v_add_u32 v193, s34, v193
v_mfma_f32_16x16x32_bf16 a[240:243], v[112:115], v[92:95], 0
v_mfma_f32_16x16x32_bf16 a[244:247], v[116:119], v[92:95], 0
s_mov_b32 s40, 256
v_mfma_f32_16x16x32_bf16 a[248:251], v[120:123], v[92:95], 0
s_mov_b32 s41, 524544
v_mfma_f32_16x16x32_bf16 a[252:255], v[124:127], v[92:95], 0
s_waitcnt vmcnt(8) lgkmcnt(0)
s_barrier
v_mfma_f32_16x16x32_bf16 a[0:3], v[160:163], v[128:131], a[0:3]
s_mov_b32 m0, s35
ds_read_b64_tr_b16 v[96:97], v194 offset:0
v_mfma_f32_16x16x32_bf16 a[4:7], v[164:167], v[128:131], a[4:7]
buffer_load_dwordx4 v202, s[4:7], s40 offen offset:0 lds
v_mfma_f32_16x16x32_bf16 a[8:11], v[168:171], v[128:131], a[8:11]
ds_read_b64_tr_b16 v[98:99], v194 offset:1024
v_mfma_f32_16x16x32_bf16 a[12:15], v[172:175], v[128:131], a[12:15]
v_mfma_f32_16x16x32_bf16 a[16:19], v[176:179], v[128:131], a[16:19]
ds_read_b128 v[64:67], v192 offset:0
v_mfma_f32_16x16x32_bf16 a[20:23], v[180:183], v[128:131], a[20:23]
buffer_load_dwordx4 v203, s[4:7], s40 offen offset:1024 lds
v_mfma_f32_16x16x32_bf16 a[24:27], v[184:187], v[128:131], a[24:27]
ds_read_b64_tr_b16 v[100:101], v195 offset:0
v_mfma_f32_16x16x32_bf16 a[28:31], v[188:191], v[128:131], a[28:31]
v_mfma_f32_16x16x32_bf16 a[32:35], v[160:163], v[132:135], a[32:35]
ds_read_b64_tr_b16 v[102:103], v195 offset:1024
v_mfma_f32_16x16x32_bf16 a[36:39], v[164:167], v[132:135], a[36:39]
buffer_load_dwordx4 v204, s[4:7], s40 offen offset:2048 lds
v_mfma_f32_16x16x32_bf16 a[40:43], v[168:171], v[132:135], a[40:43]
ds_read_b128 v[68:71], v192 offset:2048
v_mfma_f32_16x16x32_bf16 a[44:47], v[172:175], v[132:135], a[44:47]
v_mfma_f32_16x16x32_bf16 a[48:51], v[176:179], v[132:135], a[48:51]
ds_read_b64_tr_b16 v[104:105], v196 offset:0
v_mfma_f32_16x16x32_bf16 a[52:55], v[180:183], v[132:135], a[52:55]
buffer_load_dwordx4 v205, s[4:7], s40 offen offset:3072 lds
v_mfma_f32_16x16x32_bf16 a[56:59], v[184:187], v[132:135], a[56:59]
ds_read_b64_tr_b16 v[106:107], v196 offset:1024
v_mfma_f32_16x16x32_bf16 a[60:63], v[188:191], v[132:135], a[60:63]
v_mfma_f32_16x16x32_bf16 a[64:67], v[160:163], v[136:139], a[64:67]
s_add_u32 m0, s35, 4096
ds_read_b128 v[72:75], v192 offset:4096
v_mfma_f32_16x16x32_bf16 a[68:71], v[164:167], v[136:139], a[68:71]
buffer_load_dwordx4 v202, s[4:7], s41 offen offset:0 lds
v_mfma_f32_16x16x32_bf16 a[72:75], v[168:171], v[136:139], a[72:75]
ds_read_b64_tr_b16 v[108:109], v197 offset:0
v_mfma_f32_16x16x32_bf16 a[76:79], v[172:175], v[136:139], a[76:79]
v_mfma_f32_16x16x32_bf16 a[80:83], v[176:179], v[136:139], a[80:83]
ds_read_b64_tr_b16 v[110:111], v197 offset:1024
v_mfma_f32_16x16x32_bf16 a[84:87], v[180:183], v[136:139], a[84:87]
buffer_load_dwordx4 v203, s[4:7], s41 offen offset:1024 lds
v_mfma_f32_16x16x32_bf16 a[88:91], v[184:187], v[136:139], a[88:91]
ds_read_b128 v[76:79], v192 offset:6144
v_mfma_f32_16x16x32_bf16 a[92:95], v[188:191], v[136:139], a[92:95]
v_mfma_f32_16x16x32_bf16 a[96:99], v[160:163], v[140:143], a[96:99]
ds_read_b64_tr_b16 v[112:113], v198 offset:0
v_mfma_f32_16x16x32_bf16 a[100:103], v[164:167], v[140:143], a[100:103]
buffer_load_dwordx4 v204, s[4:7], s41 offen offset:2048 lds
v_mfma_f32_16x16x32_bf16 a[104:107], v[168:171], v[140:143], a[104:107]
ds_read_b64_tr_b16 v[114:115], v198 offset:1024
v_mfma_f32_16x16x32_bf16 a[108:111], v[172:175], v[140:143], a[108:111]
v_mfma_f32_16x16x32_bf16 a[112:115], v[176:179], v[140:143], a[112:115]
ds_read_b128 v[80:83], v192 offset:8192
v_mfma_f32_16x16x32_bf16 a[116:119], v[180:183], v[140:143], a[116:119]
buffer_load_dwordx4 v205, s[4:7], s41 offen offset:3072 lds
v_mfma_f32_16x16x32_bf16 a[120:123], v[184:187], v[140:143], a[120:123]
ds_read_b64_tr_b16 v[116:117], v199 offset:0
v_mfma_f32_16x16x32_bf16 a[124:127], v[188:191], v[140:143], a[124:127]
v_mfma_f32_16x16x32_bf16 a[128:131], v[160:163], v[144:147], a[128:131]
ds_read_b64_tr_b16 v[118:119], v199 offset:1024
v_mfma_f32_16x16x32_bf16 a[132:135], v[164:167], v[144:147], a[132:135]
buffer_load_dword v35, v206, s[56:59], s48 offen
v_mfma_f32_16x16x32_bf16 a[136:139], v[168:171], v[144:147], a[136:139]
ds_read_b128 v[84:87], v192 offset:10240
v_mfma_f32_16x16x32_bf16 a[140:143], v[172:175], v[144:147], a[140:143]
buffer_load_dword v2, v3, s[60:63], s90 offen sc1
v_mfma_f32_16x16x32_bf16 a[144:147], v[176:179], v[144:147], a[144:147]
s_mov_b32 s42, 1572864
ds_read_b64_tr_b16 v[120:121], v200 offset:0
v_mfma_f32_16x16x32_bf16 a[148:151], v[180:183], v[144:147], a[148:151]
s_mov_b32 s43, 1703936
buffer_load_dword v35, v207, s[12:15], s49 offen
v_mfma_f32_16x16x32_bf16 a[152:155], v[184:187], v[144:147], a[152:155]
ds_read_b64_tr_b16 v[122:123], v200 offset:1024
v_mfma_f32_16x16x32_bf16 a[156:159], v[188:191], v[144:147], a[156:159]
buffer_load_dword v11, v6, s[60:63], 0 offen sc1
v_mfma_f32_16x16x32_bf16 a[160:163], v[160:163], v[148:151], a[160:163]
s_add_u32 s40, s40, 128
ds_read_b128 v[88:91], v192 offset:12288
v_mfma_f32_16x16x32_bf16 a[164:167], v[164:167], v[148:151], a[164:167]
s_add_u32 s41, s41, 128
v_mfma_f32_16x16x32_bf16 a[168:171], v[168:171], v[148:151], a[168:171]
ds_read_b64_tr_b16 v[124:125], v201 offset:0
v_mfma_f32_16x16x32_bf16 a[172:175], v[172:175], v[148:151], a[172:175]
s_add_u32 s48, s48, 0x100000
v_mfma_f32_16x16x32_bf16 a[176:179], v[176:179], v[148:151], a[176:179]
ds_read_b64_tr_b16 v[126:127], v201 offset:1024
v_mfma_f32_16x16x32_bf16 a[180:183], v[180:183], v[148:151], a[180:183]
s_add_u32 s49, s49, 256
v_mfma_f32_16x16x32_bf16 a[184:187], v[184:187], v[148:151], a[184:187]
ds_read_b128 v[92:95], v192 offset:14336
v_mfma_f32_16x16x32_bf16 a[188:191], v[188:191], v[148:151], a[188:191]
buffer_store_dword v45, v44, s[60:63], 0 offen sc1
v_mfma_f32_16x16x32_bf16 a[192:195], v[160:163], v[152:155], a[192:195]
v_mfma_f32_16x16x32_bf16 a[196:199], v[164:167], v[152:155], a[196:199]
v_mfma_f32_16x16x32_bf16 a[200:203], v[168:171], v[152:155], a[200:203]
v_mfma_f32_16x16x32_bf16 a[204:207], v[172:175], v[152:155], a[204:207]
buffer_store_dword v45, v46, s[60:63], 0 offen sc1
v_mfma_f32_16x16x32_bf16 a[208:211], v[176:179], v[152:155], a[208:211]
v_mfma_f32_16x16x32_bf16 a[212:215], v[180:183], v[152:155], a[212:215]
v_mfma_f32_16x16x32_bf16 a[216:219], v[184:187], v[152:155], a[216:219]
v_mfma_f32_16x16x32_bf16 a[220:223], v[188:191], v[152:155], a[220:223]
v_mfma_f32_16x16x32_bf16 a[224:227], v[160:163], v[156:159], a[224:227]
v_mfma_f32_16x16x32_bf16 a[228:231], v[164:167], v[156:159], a[228:231]
s_mov_b32 s37, s36
v_mfma_f32_16x16x32_bf16 a[232:235], v[168:171], v[156:159], a[232:235]
s_add_u32 s35, s35, s45
v_mfma_f32_16x16x32_bf16 a[236:239], v[172:175], v[156:159], a[236:239]
s_add_u32 s36, s36, s34
v_mfma_f32_16x16x32_bf16 a[240:243], v[176:179], v[156:159], a[240:243]
v_mfma_f32_16x16x32_bf16 a[244:247], v[180:183], v[156:159], a[244:247]
v_mfma_f32_16x16x32_bf16 a[248:251], v[184:187], v[156:159], a[248:251]
v_mfma_f32_16x16x32_bf16 a[252:255], v[188:191], v[156:159], a[252:255]
s_waitcnt lgkmcnt(0)
s_nop 0
s_nop 0
s_nop 0
s_nop 0
s_nop 0
s_nop 0
.Lp1_kloop:
v_mfma_f32_16x16x32_bf16 a[0:3], v[96:99], v[64:67], a[0:3]
s_mov_b32 m0, s37
ds_read_b64_tr_b16 v[160:161], v194 offset:8192
v_mfma_f32_16x16x32_bf16 a[4:7], v[100:103], v[64:67], a[4:7]
buffer_load_dwordx4 v16, s[8:11], s42 offen offset:0 nt lds
v_mfma_f32_16x16x32_bf16 a[8:11], v[104:107], v[64:67], a[8:11]
ds_read_b64_tr_b16 v[162:163], v194 offset:9216
v_mfma_f32_16x16x32_bf16 a[12:15], v[108:111], v[64:67], a[12:15]
v_mfma_f32_16x16x32_bf16 a[16:19], v[112:115], v[64:67], a[16:19]
ds_read_b128 v[128:131], v193 offset:0
v_mfma_f32_16x16x32_bf16 a[20:23], v[116:119], v[64:67], a[20:23]
buffer_load_dwordx4 v17, s[8:11], s42 offen offset:1024 nt lds
v_mfma_f32_16x16x32_bf16 a[24:27], v[120:123], v[64:67], a[24:27]
ds_read_b64_tr_b16 v[164:165], v195 offset:8192
v_mfma_f32_16x16x32_bf16 a[28:31], v[124:127], v[64:67], a[28:31]
v_mfma_f32_16x16x32_bf16 a[32:35], v[96:99], v[68:71], a[32:35]
ds_read_b64_tr_b16 v[166:167], v195 offset:9216
.Lpad1:
s_cbranch_execz .Ltch0
v_mfma_f32_16x16x32_bf16 a[36:39], v[100:103], v[68:71], a[36:39]
buffer_load_dwordx4 v18, s[8:11], s42 offen offset:2048 nt lds
v_mfma_f32_16x16x32_bf16 a[40:43], v[104:107], v[68:71], a[40:43]
ds_read_b128 v[132:135], v193 offset:2048
v_mfma_f32_16x16x32_bf16 a[44:47], v[108:111], v[68:71], a[44:47]
v_mfma_f32_16x16x32_bf16 a[48:51], v[112:115], v[68:71], a[48:51]
ds_read_b64_tr_b16 v[168:169], v196 offset:8192
v_mfma_f32_16x16x32_bf16 a[52:55], v[116:119], v[68:71], a[52:55]
buffer_load_dwordx4 v19, s[8:11], s42 offen offset:3072 nt lds
v_mfma_f32_16x16x32_bf16 a[56:59], v[120:123], v[68:71], a[56:59]
ds_read_b64_tr_b16 v[170:171], v196 offset:9216
v_mfma_f32_16x16x32_bf16 a[60:63], v[124:127], v[68:71], a[60:63]
v_mfma_f32_16x16x32_bf16 a[64:67], v[96:99], v[72:75], a[64:67]
s_add_u32 m0, s37, 4096
ds_read_b128 v[136:139], v193 offset:4096
v_mfma_f32_16x16x32_bf16 a[68:71], v[100:103], v[72:75], a[68:71]
buffer_load_dwordx4 v16, s[8:11], s43 offen offset:0 nt lds
v_mfma_f32_16x16x32_bf16 a[72:75], v[104:107], v[72:75], a[72:75]
ds_read_b64_tr_b16 v[172:173], v197 offset:8192
v_mfma_f32_16x16x32_bf16 a[76:79], v[108:111], v[72:75], a[76:79]
v_mfma_f32_16x16x32_bf16 a[80:83], v[112:115], v[72:75], a[80:83]
ds_read_b64_tr_b16 v[174:175], v197 offset:9216
v_mfma_f32_16x16x32_bf16 a[84:87], v[116:119], v[72:75], a[84:87]
buffer_load_dwordx4 v17, s[8:11], s43 offen offset:1024 nt lds
v_mfma_f32_16x16x32_bf16 a[88:91], v[120:123], v[72:75], a[88:91]
ds_read_b128 v[140:143], v193 offset:6144
v_mfma_f32_16x16x32_bf16 a[92:95], v[124:127], v[72:75], a[92:95]
v_mfma_f32_16x16x32_bf16 a[96:99], v[96:99], v[76:79], a[96:99]
ds_read_b64_tr_b16 v[176:177], v198 offset:8192
v_mfma_f32_16x16x32_bf16 a[100:103], v[100:103], v[76:79], a[100:103]
buffer_load_dwordx4 v18, s[8:11], s43 offen offset:2048 nt lds
v_mfma_f32_16x16x32_bf16 a[104:107], v[104:107], v[76:79], a[104:107]
ds_read_b64_tr_b16 v[178:179], v198 offset:9216
v_mfma_f32_16x16x32_bf16 a[108:111], v[108:111], v[76:79], a[108:111]
v_mfma_f32_16x16x32_bf16 a[112:115], v[112:115], v[76:79], a[112:115]
ds_read_b128 v[144:147], v193 offset:8192
v_mfma_f32_16x16x32_bf16 a[116:119], v[116:119], v[76:79], a[116:119]
buffer_load_dwordx4 v19, s[8:11], s43 offen offset:3072 nt lds
v_mfma_f32_16x16x32_bf16 a[120:123], v[120:123], v[76:79], a[120:123]
ds_read_b64_tr_b16 v[180:181], v199 offset:8192
v_mfma_f32_16x16x32_bf16 a[124:127], v[124:127], v[76:79], a[124:127]
v_mfma_f32_16x16x32_bf16 a[128:131], v[96:99], v[80:83], a[128:131]
ds_read_b64_tr_b16 v[182:183], v199 offset:9216
v_mfma_f32_16x16x32_bf16 a[132:135], v[100:103], v[80:83], a[132:135]
v_mfma_f32_16x16x32_bf16 a[136:139], v[104:107], v[80:83], a[136:139]
ds_read_b128 v[148:151], v193 offset:10240
v_mfma_f32_16x16x32_bf16 a[140:143], v[108:111], v[80:83], a[140:143]
v_mfma_f32_16x16x32_bf16 a[144:147], v[112:115], v[80:83], a[144:147]
ds_read_b64_tr_b16 v[184:185], v200 offset:8192
v_mfma_f32_16x16x32_bf16 a[148:151], v[116:119], v[80:83], a[148:151]
v_mfma_f32_16x16x32_bf16 a[152:155], v[120:123], v[80:83], a[152:155]
ds_read_b64_tr_b16 v[186:187], v200 offset:9216
v_mfma_f32_16x16x32_bf16 a[156:159], v[124:127], v[80:83], a[156:159]
v_mfma_f32_16x16x32_bf16 a[160:163], v[96:99], v[84:87], a[160:163]
s_add_u32 s42, s42, 524288
ds_read_b128 v[152:155], v193 offset:12288
v_mfma_f32_16x16x32_bf16 a[164:167], v[100:103], v[84:87], a[164:167]
s_add_u32 s43, s43, 524288
v_mfma_f32_16x16x32_bf16 a[168:171], v[104:107], v[84:87], a[168:171]
ds_read_b64_tr_b16 v[188:189], v201 offset:8192
v_mfma_f32_16x16x32_bf16 a[172:175], v[108:111], v[84:87], a[172:175]
v_mfma_f32_16x16x32_bf16 a[176:179], v[112:115], v[84:87], a[176:179]
s_add_u32 s44, s35, 65536
ds_read_b64_tr_b16 v[190:191], v201 offset:9216
v_mfma_f32_16x16x32_bf16 a[180:183], v[116:119], v[84:87], a[180:183]
s_cmp_ge_u32 s44, s39
s_cselect_b32 s45, s55, s54
v_mfma_f32_16x16x32_bf16 a[184:187], v[120:123], v[84:87], a[184:187]
ds_read_b128 v[156:159], v193 offset:14336
v_mfma_f32_16x16x32_bf16 a[188:191], v[124:127], v[84:87], a[188:191]
s_add_u32 s44, s36, 65536
v_mfma_f32_16x16x32_bf16 a[192:195], v[96:99], v[88:91], a[192:195]
v_mfma_f32_16x16x32_bf16 a[196:199], v[100:103], v[88:91], a[196:199]
s_cmp_ge_u32 s44, s39
s_cselect_b32 s34, s55, s54
v_mfma_f32_16x16x32_bf16 a[200:203], v[104:107], v[88:91], a[200:203]
v_add_u32 v194, s45, v194
v_mfma_f32_16x16x32_bf16 a[204:207], v[108:111], v[88:91], a[204:207]
v_add_u32 v195, s45, v195
v_mfma_f32_16x16x32_bf16 a[208:211], v[112:115], v[88:91], a[208:211]
v_add_u32 v196, s45, v196
v_mfma_f32_16x16x32_bf16 a[212:215], v[116:119], v[88:91], a[212:215]
v_add_u32 v197, s45, v197
v_mfma_f32_16x16x32_bf16 a[216:219], v[120:123], v[88:91], a[216:219]
v_add_u32 v198, s45, v198
v_mfma_f32_16x16x32_bf16 a[220:223], v[124:127], v[88:91], a[220:223]
v_add_u32 v199, s45, v199
v_mfma_f32_16x16x32_bf16 a[224:227], v[96:99], v[92:95], a[224:227]
v_add_u32 v200, s45, v200
v_mfma_f32_16x16x32_bf16 a[228:231], v[100:103], v[92:95], a[228:231]
v_add_u32 v201, s45, v201
v_mfma_f32_16x16x32_bf16 a[232:235], v[104:107], v[92:95], a[232:235]
v_add_u32 v192, s34, v192
v_mfma_f32_16x16x32_bf16 a[236:239], v[108:111], v[92:95], a[236:239]
v_add_u32 v193, s34, v193
v_mfma_f32_16x16x32_bf16 a[240:243], v[112:115], v[92:95], a[240:243]
v_mfma_f32_16x16x32_bf16 a[244:247], v[116:119], v[92:95], a[244:247]
v_mfma_f32_16x16x32_bf16 a[248:251], v[120:123], v[92:95], a[248:251]
v_mfma_f32_16x16x32_bf16 a[252:255], v[124:127], v[92:95], a[252:255]
s_waitcnt vmcnt(10) lgkmcnt(0)
s_barrier
v_mfma_f32_16x16x32_bf16 a[0:3], v[160:163], v[128:131], a[0:3]
s_mov_b32 m0, s35
ds_read_b64_tr_b16 v[96:97], v194 offset:0
v_mfma_f32_16x16x32_bf16 a[4:7], v[164:167], v[128:131], a[4:7]
buffer_load_dwordx4 v202, s[4:7], s40 offen offset:0 lds
v_mfma_f32_16x16x32_bf16 a[8:11], v[168:171], v[128:131], a[8:11]
ds_read_b64_tr_b16 v[98:99], v194 offset:1024
v_mfma_f32_16x16x32_bf16 a[12:15], v[172:175], v[128:131], a[12:15]
v_mfma_f32_16x16x32_bf16 a[16:19], v[176:179], v[128:131], a[16:19]
ds_read_b128 v[64:67], v192 offset:0
v_mfma_f32_16x16x32_bf16 a[20:23], v[180:183], v[128:131], a[20:23]
buffer_load_dwordx4 v203, s[4:7], s40 offen offset:1024 lds
v_mfma_f32_16x16x32_bf16 a[24:27], v[184:187], v[128:131], a[24:27]
ds_read_b64_tr_b16 v[100:101], v195 offset:0
v_mfma_f32_16x16x32_bf16 a[28:31], v[188:191], v[128:131], a[28:31]
v_mfma_f32_16x16x32_bf16 a[32:35], v[160:163], v[132:135], a[32:35]
ds_read_b64_tr_b16 v[102:103], v195 offset:1024
v_mfma_f32_16x16x32_bf16 a[36:39], v[164:167], v[132:135], a[36:39]
buffer_load_dwordx4 v204, s[4:7], s40 offen offset:2048 lds
v_mfma_f32_16x16x32_bf16 a[40:43], v[168:171], v[132:135], a[40:43]
ds_read_b128 v[68:71], v192 offset:2048
v_mfma_f32_16x16x32_bf16 a[44:47], v[172:175], v[132:135], a[44:47]
v_mfma_f32_16x16x32_bf16 a[48:51], v[176:179], v[132:135], a[48:51]
ds_read_b64_tr_b16 v[104:105], v196 offset:0
v_mfma_f32_16x16x32_bf16 a[52:55], v[180:183], v[132:135], a[52:55]
buffer_load_dwordx4 v205, s[4:7], s40 offen offset:3072 lds
v_mfma_f32_16x16x32_bf16 a[56:59], v[184:187], v[132:135], a[56:59]
ds_read_b64_tr_b16 v[106:107], v196 offset:1024
v_mfma_f32_16x16x32_bf16 a[60:63], v[188:191], v[132:135], a[60:63]
v_mfma_f32_16x16x32_bf16 a[64:67], v[160:163], v[136:139], a[64:67]
s_add_u32 m0, s35, 4096
ds_read_b128 v[72:75], v192 offset:4096
v_mfma_f32_16x16x32_bf16 a[68:71], v[164:167], v[136:139], a[68:71]
buffer_load_dwordx4 v202, s[4:7], s41 offen offset:0 lds
v_mfma_f32_16x16x32_bf16 a[72:75], v[168:171], v[136:139], a[72:75]
ds_read_b64_tr_b16 v[108:109], v197 offset:0
v_mfma_f32_16x16x32_bf16 a[76:79], v[172:175], v[136:139], a[76:79]
v_mfma_f32_16x16x32_bf16 a[80:83], v[176:179], v[136:139], a[80:83]
ds_read_b64_tr_b16 v[110:111], v197 offset:1024
v_mfma_f32_16x16x32_bf16 a[84:87], v[180:183], v[136:139], a[84:87]
buffer_load_dwordx4 v203, s[4:7], s41 offen offset:1024 lds
v_mfma_f32_16x16x32_bf16 a[88:91], v[184:187], v[136:139], a[88:91]
ds_read_b128 v[76:79], v192 offset:6144
v_mfma_f32_16x16x32_bf16 a[92:95], v[188:191], v[136:139], a[92:95]
v_mfma_f32_16x16x32_bf16 a[96:99], v[160:163], v[140:143], a[96:99]
ds_read_b64_tr_b16 v[112:113], v198 offset:0
v_mfma_f32_16x16x32_bf16 a[100:103], v[164:167], v[140:143], a[100:103]
buffer_load_dwordx4 v204, s[4:7], s41 offen offset:2048 lds
v_mfma_f32_16x16x32_bf16 a[104:107], v[168:171], v[140:143], a[104:107]
ds_read_b64_tr_b16 v[114:115], v198 offset:1024
v_mfma_f32_16x16x32_bf16 a[108:111], v[172:175], v[140:143], a[108:111]
v_mfma_f32_16x16x32_bf16 a[112:115], v[176:179], v[140:143], a[112:115]
ds_read_b128 v[80:83], v192 offset:8192
v_mfma_f32_16x16x32_bf16 a[116:119], v[180:183], v[140:143], a[116:119]
buffer_load_dwordx4 v205, s[4:7], s41 offen offset:3072 lds
v_mfma_f32_16x16x32_bf16 a[120:123], v[184:187], v[140:143], a[120:123]
ds_read_b64_tr_b16 v[116:117], v199 offset:0
v_mfma_f32_16x16x32_bf16 a[124:127], v[188:191], v[140:143], a[124:127]
v_mfma_f32_16x16x32_bf16 a[128:131], v[160:163], v[144:147], a[128:131]
ds_read_b64_tr_b16 v[118:119], v199 offset:1024
v_mfma_f32_16x16x32_bf16 a[132:135], v[164:167], v[144:147], a[132:135]
buffer_load_dword v35, v206, s[56:59], s48 offen
v_mfma_f32_16x16x32_bf16 a[136:139], v[168:171], v[144:147], a[136:139]
ds_read_b128 v[84:87], v192 offset:10240
v_mfma_f32_16x16x32_bf16 a[140:143], v[172:175], v[144:147], a[140:143]
v_mfma_f32_16x16x32_bf16 a[144:147], v[176:179], v[144:147], a[144:147]
ds_read_b64_tr_b16 v[120:121], v200 offset:0
v_mfma_f32_16x16x32_bf16 a[148:151], v[180:183], v[144:147], a[148:151]
buffer_load_dword v35, v207, s[12:15], s49 offen
v_mfma_f32_16x16x32_bf16 a[152:155], v[184:187], v[144:147], a[152:155]
ds_read_b64_tr_b16 v[122:123], v200 offset:1024
v_mfma_f32_16x16x32_bf16 a[156:159], v[188:191], v[144:147], a[156:159]
v_mfma_f32_16x16x32_bf16 a[160:163], v[160:163], v[148:151], a[160:163]
s_add_u32 s40, s40, 128
ds_read_b128 v[88:91], v192 offset:12288
v_mfma_f32_16x16x32_bf16 a[164:167], v[164:167], v[148:151], a[164:167]
s_add_u32 s41, s41, 128
v_mfma_f32_16x16x32_bf16 a[168:171], v[168:171], v[148:151], a[168:171]
ds_read_b64_tr_b16 v[124:125], v201 offset:0
v_mfma_f32_16x16x32_bf16 a[172:175], v[172:175], v[148:151], a[172:175]
s_add_u32 s48, s48, 0x100000
v_mfma_f32_16x16x32_bf16 a[176:179], v[176:179], v[148:151], a[176:179]
ds_read_b64_tr_b16 v[126:127], v201 offset:1024
v_mfma_f32_16x16x32_bf16 a[180:183], v[180:183], v[148:151], a[180:183]
s_add_u32 s49, s49, 256
v_mfma_f32_16x16x32_bf16 a[184:187], v[184:187], v[148:151], a[184:187]
ds_read_b128 v[92:95], v192 offset:14336
v_mfma_f32_16x16x32_bf16 a[188:191], v[188:191], v[148:151], a[188:191]
v_mfma_f32_16x16x32_bf16 a[192:195], v[160:163], v[152:155], a[192:195]
v_mfma_f32_16x16x32_bf16 a[196:199], v[164:167], v[152:155], a[196:199]
v_mfma_f32_16x16x32_bf16 a[200:203], v[168:171], v[152:155], a[200:203]
v_mfma_f32_16x16x32_bf16 a[204:207], v[172:175], v[152:155], a[204:207]
v_mfma_f32_16x16x32_bf16 a[208:211], v[176:179], v[152:155], a[208:211]
v_mfma_f32_16x16x32_bf16 a[212:215], v[180:183], v[152:155], a[212:215]
v_mfma_f32_16x16x32_bf16 a[216:219], v[184:187], v[152:155], a[216:219]
v_mfma_f32_16x16x32_bf16 a[220:223], v[188:191], v[152:155], a[220:223]
v_mfma_f32_16x16x32_bf16 a[224:227], v[160:163], v[156:159], a[224:227]
v_mfma_f32_16x16x32_bf16 a[228:231], v[164:167], v[156:159], a[228:231]
s_mov_b32 s37, s36
v_mfma_f32_16x16x32_bf16 a[232:235], v[168:171], v[156:159], a[232:235]
s_add_u32 s35, s35, s45
v_mfma_f32_16x16x32_bf16 a[236:239], v[172:175], v[156:159], a[236:239]
s_add_u32 s36, s36, s34
v_mfma_f32_16x16x32_bf16 a[240:243], v[176:179], v[156:159], a[240:243]
s_add_u32 s38, s38, 1
v_mfma_f32_16x16x32_bf16 a[244:247], v[180:183], v[156:159], a[244:247]
s_cmp_lt_u32 s38, 6
v_mfma_f32_16x16x32_bf16 a[248:251], v[184:187], v[156:159], a[248:251]
v_mfma_f32_16x16x32_bf16 a[252:255], v[188:191], v[156:159], a[252:255]
s_waitcnt lgkmcnt(0)
s_cbranch_scc1 .Lp1_kloop
v_mfma_f32_16x16x32_bf16 a[0:3], v[96:99], v[64:67], a[0:3]
s_add_u32 m0, s92, 32768
ds_read_b64_tr_b16 v[160:161], v194 offset:8192
v_mfma_f32_16x16x32_bf16 a[4:7], v[100:103], v[64:67], a[4:7]
buffer_load_dwordx4 v20, s[12:15], s24 offen offset:0 lds
v_mfma_f32_16x16x32_bf16 a[8:11], v[104:107], v[64:67], a[8:11]
ds_read_b64_tr_b16 v[162:163], v194 offset:9216
v_mfma_f32_16x16x32_bf16 a[12:15], v[108:111], v[64:67], a[12:15]
v_mfma_f32_16x16x32_bf16 a[16:19], v[112:115], v[64:67], a[16:19]
ds_read_b128 v[128:131], v193 offset:0
v_mfma_f32_16x16x32_bf16 a[20:23], v[116:119], v[64:67], a[20:23]
buffer_load_dwordx4 v21, s[12:15], s24 offen offset:1024 lds
v_mfma_f32_16x16x32_bf16 a[24:27], v[120:123], v[64:67], a[24:27]
ds_read_b64_tr_b16 v[164:165], v195 offset:8192
v_mfma_f32_16x16x32_bf16 a[28:31], v[124:127], v[64:67], a[28:31]
v_mfma_f32_16x16x32_bf16 a[32:35], v[96:99], v[68:71], a[32:35]
ds_read_b64_tr_b16 v[166:167], v195 offset:9216
v_mfma_f32_16x16x32_bf16 a[36:39], v[100:103], v[68:71], a[36:39]
buffer_load_dwordx4 v22, s[12:15], s24 offen offset:2048 lds
v_mfma_f32_16x16x32_bf16 a[40:43], v[104:107], v[68:71], a[40:43]
ds_read_b128 v[132:135], v193 offset:2048
v_mfma_f32_16x16x32_bf16 a[44:47], v[108:111], v[68:71], a[44:47]
v_mfma_f32_16x16x32_bf16 a[48:51], v[112:115], v[68:71], a[48:51]
ds_read_b64_tr_b16 v[168:169], v196 offset:8192
v_mfma_f32_16x16x32_bf16 a[52:55], v[116:119], v[68:71], a[52:55]
buffer_load_dwordx4 v23, s[12:15], s24 offen offset:3072 lds
v_mfma_f32_16x16x32_bf16 a[56:59], v[120:123], v[68:71], a[56:59]
ds_read_b64_tr_b16 v[170:171], v196 offset:9216
v_mfma_f32_16x16x32_bf16 a[60:63], v[124:127], v[68:71], a[60:63]
v_mfma_f32_16x16x32_bf16 a[64:67], v[96:99], v[72:75], a[64:67]
s_add_u32 m0, s92, 36864
ds_read_b128 v[136:139], v193 offset:4096
v_mfma_f32_16x16x32_bf16 a[68:71], v[100:103], v[72:75], a[68:71]
buffer_load_dwordx4 v20, s[12:15], s25 offen offset:0 lds
v_mfma_f32_16x16x32_bf16 a[72:75], v[104:107], v[72:75], a[72:75]
ds_read_b64_tr_b16 v[172:173], v197 offset:8192
v_mfma_f32_16x16x32_bf16 a[76:79], v[108:111], v[72:75], a[76:79]
v_mfma_f32_16x16x32_bf16 a[80:83], v[112:115], v[72:75], a[80:83]
ds_read_b64_tr_b16 v[174:175], v197 offset:9216
v_mfma_f32_16x16x32_bf16 a[84:87], v[116:119], v[72:75], a[84:87]
buffer_load_dwordx4 v21, s[12:15], s25 offen offset:1024 lds
v_mfma_f32_16x16x32_bf16 a[88:91], v[120:123], v[72:75], a[88:91]
ds_read_b128 v[140:143], v193 offset:6144
v_mfma_f32_16x16x32_bf16 a[92:95], v[124:127], v[72:75], a[92:95]
v_mfma_f32_16x16x32_bf16 a[96:99], v[96:99], v[76:79], a[96:99]
ds_read_b64_tr_b16 v[176:177], v198 offset:8192
v_mfma_f32_16x16x32_bf16 a[100:103], v[100:103], v[76:79], a[100:103]
buffer_load_dwordx4 v22, s[12:15], s25 offen offset:2048 lds
v_mfma_f32_16x16x32_bf16 a[104:107], v[104:107], v[76:79], a[104:107]
ds_read_b64_tr_b16 v[178:179], v198 offset:9216
v_mfma_f32_16x16x32_bf16 a[108:111], v[108:111], v[76:79], a[108:111]
v_mfma_f32_16x16x32_bf16 a[112:115], v[112:115], v[76:79], a[112:115]
ds_read_b128 v[144:147], v193 offset:8192
v_mfma_f32_16x16x32_bf16 a[116:119], v[116:119], v[76:79], a[116:119]
buffer_load_dwordx4 v23, s[12:15], s25 offen offset:3072 lds
v_mfma_f32_16x16x32_bf16 a[120:123], v[120:123], v[76:79], a[120:123]
ds_read_b64_tr_b16 v[180:181], v199 offset:8192
v_mfma_f32_16x16x32_bf16 a[124:127], v[124:127], v[76:79], a[124:127]
v_mfma_f32_16x16x32_bf16 a[128:131], v[96:99], v[80:83], a[128:131]
ds_read_b64_tr_b16 v[182:183], v199 offset:9216
v_mfma_f32_16x16x32_bf16 a[132:135], v[100:103], v[80:83], a[132:135]
v_mfma_f32_16x16x32_bf16 a[136:139], v[104:107], v[80:83], a[136:139]
ds_read_b128 v[148:151], v193 offset:10240
v_mfma_f32_16x16x32_bf16 a[140:143], v[108:111], v[80:83], a[140:143]
v_mfma_f32_16x16x32_bf16 a[144:147], v[112:115], v[80:83], a[144:147]
ds_read_b64_tr_b16 v[184:185], v200 offset:8192
v_mfma_f32_16x16x32_bf16 a[148:151], v[116:119], v[80:83], a[148:151]
v_mfma_f32_16x16x32_bf16 a[152:155], v[120:123], v[80:83], a[152:155]
ds_read_b64_tr_b16 v[186:187], v200 offset:9216
v_mfma_f32_16x16x32_bf16 a[156:159], v[124:127], v[80:83], a[156:159]
v_mfma_f32_16x16x32_bf16 a[160:163], v[96:99], v[84:87], a[160:163]
s_add_u32 s24, s24, 128
s_add_u32 s25, s25, 128
ds_read_b128 v[152:155], v193 offset:12288
v_mfma_f32_16x16x32_bf16 a[164:167], v[100:103], v[84:87], a[164:167]
v_mfma_f32_16x16x32_bf16 a[168:171], v[104:107], v[84:87], a[168:171]
ds_read_b64_tr_b16 v[188:189], v201 offset:8192
v_mfma_f32_16x16x32_bf16 a[172:175], v[108:111], v[84:87], a[172:175]
v_mfma_f32_16x16x32_bf16 a[176:179], v[112:115], v[84:87], a[176:179]
s_add_u32 s44, s35, 65536
ds_read_b64_tr_b16 v[190:191], v201 offset:9216
v_mfma_f32_16x16x32_bf16 a[180:183], v[116:119], v[84:87], a[180:183]
s_cmp_ge_u32 s44, s39
s_cselect_b32 s45, s55, s54
v_mfma_f32_16x16x32_bf16 a[184:187], v[120:123], v[84:87], a[184:187]
ds_read_b128 v[156:159], v193 offset:14336
v_mfma_f32_16x16x32_bf16 a[188:191], v[124:127], v[84:87], a[188:191]
s_add_u32 s44, s36, 65536
v_mfma_f32_16x16x32_bf16 a[192:195], v[96:99], v[88:91], a[192:195]
v_mfma_f32_16x16x32_bf16 a[196:199], v[100:103], v[88:91], a[196:199]
s_cmp_ge_u32 s44, s39
s_cselect_b32 s34, s55, s54
v_mfma_f32_16x16x32_bf16 a[200:203], v[104:107], v[88:91], a[200:203]
v_add_u32 v194, s45, v194
v_mfma_f32_16x16x32_bf16 a[204:207], v[108:111], v[88:91], a[204:207]
v_add_u32 v195, s45, v195
v_mfma_f32_16x16x32_bf16 a[208:211], v[112:115], v[88:91], a[208:211]
v_add_u32 v196, s45, v196
v_mfma_f32_16x16x32_bf16 a[212:215], v[116:119], v[88:91], a[212:215]
v_add_u32 v197, s45, v197
v_mfma_f32_16x16x32_bf16 a[216:219], v[120:123], v[88:91], a[216:219]
v_add_u32 v198, s45, v198
v_mfma_f32_16x16x32_bf16 a[220:223], v[124:127], v[88:91], a[220:223]
v_add_u32 v199, s45, v199
v_mfma_f32_16x16x32_bf16 a[224:227], v[96:99], v[92:95], a[224:227]
v_add_u32 v200, s45, v200
v_mfma_f32_16x16x32_bf16 a[228:231], v[100:103], v[92:95], a[228:231]
v_add_u32 v201, s45, v201
v_mfma_f32_16x16x32_bf16 a[232:235], v[104:107], v[92:95], a[232:235]
v_add_u32 v192, s34, v192
v_mfma_f32_16x16x32_bf16 a[236:239], v[108:111], v[92:95], a[236:239]
v_add_u32 v193, s34, v193
v_mfma_f32_16x16x32_bf16 a[240:243], v[112:115], v[92:95], a[240:243]
v_mfma_f32_16x16x32_bf16 a[244:247], v[116:119], v[92:95], a[244:247]
v_mfma_f32_16x16x32_bf16 a[248:251], v[120:123], v[92:95], a[248:251]
v_mfma_f32_16x16x32_bf16 a[252:255], v[124:127], v[92:95], a[252:255]
s_waitcnt vmcnt(10) lgkmcnt(0)
s_barrier
v_mfma_f32_16x16x32_bf16 a[0:3], v[160:163], v[128:131], a[0:3]
s_add_u32 m0, s91, 65536
ds_read_b64_tr_b16 v[96:97], v194 offset:0
v_mfma_f32_16x16x32_bf16 a[4:7], v[164:167], v[128:131], a[4:7]
buffer_load_dwordx4 v16, s[16:19], s26 offen offset:0 lds
v_mfma_f32_16x16x32_bf16 a[8:11], v[168:171], v[128:131], a[8:11]
ds_read_b64_tr_b16 v[98:99], v194 offset:1024
v_mfma_f32_16x16x32_bf16 a[12:15], v[172:175], v[128:131], a[12:15]
v_mfma_f32_16x16x32_bf16 a[16:19], v[176:179], v[128:131], a[16:19]
ds_read_b128 v[64:67], v192 offset:0
v_mfma_f32_16x16x32_bf16 a[20:23], v[180:183], v[128:131], a[20:23]
buffer_load_dwordx4 v17, s[16:19], s26 offen offset:1024 lds
v_mfma_f32_16x16x32_bf16 a[24:27], v[184:187], v[128:131], a[24:27]
ds_read_b64_tr_b16 v[100:101], v195 offset:0
v_mfma_f32_16x16x32_bf16 a[28:31], v[188:191], v[128:131], a[28:31]
v_mfma_f32_16x16x32_bf16 a[32:35], v[160:163], v[132:135], a[32:35]
ds_read_b64_tr_b16 v[102:103], v195 offset:1024
v_mfma_f32_16x16x32_bf16 a[36:39], v[164:167], v[132:135], a[36:39]
buffer_load_dwordx4 v18, s[16:19], s26 offen offset:2048 lds
v_mfma_f32_16x16x32_bf16 a[40:43], v[168:171], v[132:135], a[40:43]
ds_read_b128 v[68:71], v192 offset:2048
v_mfma_f32_16x16x32_bf16 a[44:47], v[172:175], v[132:135], a[44:47]
v_mfma_f32_16x16x32_bf16 a[48:51], v[176:179], v[132:135], a[48:51]
ds_read_b64_tr_b16 v[104:105], v196 offset:0
v_mfma_f32_16x16x32_bf16 a[52:55], v[180:183], v[132:135], a[52:55]
buffer_load_dwordx4 v19, s[16:19], s26 offen offset:3072 lds
v_mfma_f32_16x16x32_bf16 a[56:59], v[184:187], v[132:135], a[56:59]
ds_read_b64_tr_b16 v[106:107], v196 offset:1024
v_mfma_f32_16x16x32_bf16 a[60:63], v[188:191], v[132:135], a[60:63]
s_add_u32 s26, s26, 524288
v_mfma_f32_16x16x32_bf16 a[64:67], v[160:163], v[136:139], a[64:67]
s_add_u32 m0, s91, 81920
ds_read_b128 v[72:75], v192 offset:4096
v_mfma_f32_16x16x32_bf16 a[68:71], v[164:167], v[136:139], a[68:71]
buffer_load_dwordx4 v16, s[16:19], s26 offen offset:0 lds
v_mfma_f32_16x16x32_bf16 a[72:75], v[168:171], v[136:139], a[72:75]
ds_read_b64_tr_b16 v[108:109], v197 offset:0
v_mfma_f32_16x16x32_bf16 a[76:79], v[172:175], v[136:139], a[76:79]
v_mfma_f32_16x16x32_bf16 a[80:83], v[176:179], v[136:139], a[80:83]
ds_read_b64_tr_b16 v[110:111], v197 offset:1024
v_mfma_f32_16x16x32_bf16 a[84:87], v[180:183], v[136:139], a[84:87]
buffer_load_dwordx4 v17, s[16:19], s26 offen offset:1024 lds
v_mfma_f32_16x16x32_bf16 a[88:91], v[184:187], v[136:139], a[88:91]
ds_read_b128 v[76:79], v192 offset:6144
v_mfma_f32_16x16x32_bf16 a[92:95], v[188:191], v[136:139], a[92:95]
v_mfma_f32_16x16x32_bf16 a[96:99], v[160:163], v[140:143], a[96:99]
ds_read_b64_tr_b16 v[112:113], v198 offset:0
v_mfma_f32_16x16x32_bf16 a[100:103], v[164:167], v[140:143], a[100:103]
buffer_load_dwordx4 v18, s[16:19], s26 offen offset:2048 lds
v_mfma_f32_16x16x32_bf16 a[104:107], v[168:171], v[140:143], a[104:107]
ds_read_b64_tr_b16 v[114:115], v198 offset:1024
v_mfma_f32_16x16x32_bf16 a[108:111], v[172:175], v[140:143], a[108:111]
v_mfma_f32_16x16x32_bf16 a[112:115], v[176:179], v[140:143], a[112:115]
ds_read_b128 v[80:83], v192 offset:8192
v_mfma_f32_16x16x32_bf16 a[116:119], v[180:183], v[140:143], a[116:119]
buffer_load_dwordx4 v19, s[16:19], s26 offen offset:3072 lds
v_mfma_f32_16x16x32_bf16 a[120:123], v[184:187], v[140:143], a[120:123]
ds_read_b64_tr_b16 v[116:117], v199 offset:0
v_mfma_f32_16x16x32_bf16 a[124:127], v[188:191], v[140:143], a[124:127]
s_add_u32 s26, s26, 524288
v_mfma_f32_16x16x32_bf16 a[128:131], v[160:163], v[144:147], a[128:131]
ds_read_b64_tr_b16 v[118:119], v199 offset:1024
v_mfma_f32_16x16x32_bf16 a[132:135], v[164:167], v[144:147], a[132:135]
buffer_load_dword v35, v206, s[56:59], s48 offen
v_mfma_f32_16x16x32_bf16 a[136:139], v[168:171], v[144:147], a[136:139]
ds_read_b128 v[84:87], v192 offset:10240
v_mfma_f32_16x16x32_bf16 a[140:143], v[172:175], v[144:147], a[140:143]
v_mfma_f32_16x16x32_bf16 a[144:147], v[176:179], v[144:147], a[144:147]
ds_read_b64_tr_b16 v[120:121], v200 offset:0
v_mfma_f32_16x16x32_bf16 a[148:151], v[180:183], v[144:147], a[148:151]
buffer_load_dword v35, v207, s[12:15], s49 offen
v_add_u32 v11, 8, v11
v_and_b32 v11, -8, v11
v_mfma_f32_16x16x32_bf16 a[152:155], v[184:187], v[144:147], a[152:155]
ds_read_b64_tr_b16 v[122:123], v200 offset:1024
v_mfma_f32_16x16x32_bf16 a[156:159], v[188:191], v[144:147], a[156:159]
v_mfma_f32_16x16x32_bf16 a[160:163], v[160:163], v[148:151], a[160:163]
ds_read_b128 v[88:91], v192 offset:12288
v_mfma_f32_16x16x32_bf16 a[164:167], v[164:167], v[148:151], a[164:167]
s_nop 0
v_or_b32 v0, 0, v11
v_mfma_f32_16x16x32_bf16 a[168:171], v[168:171], v[148:151], a[168:171]
ds_read_b64_tr_b16 v[124:125], v201 offset:0
v_mfma_f32_16x16x32_bf16 a[172:175], v[172:175], v[148:151], a[172:175]
s_add_u32 s48, s48, 0x100000
v_mfma_f32_16x16x32_bf16 a[176:179], v[176:179], v[148:151], a[176:179]
ds_read_b64_tr_b16 v[126:127], v201 offset:1024
v_mfma_f32_16x16x32_bf16 a[180:183], v[180:183], v[148:151], a[180:183]
s_add_u32 s49, s49, 256
v_mfma_f32_16x16x32_bf16 a[184:187], v[184:187], v[148:151], a[184:187]
ds_read_b128 v[92:95], v192 offset:14336
v_mfma_f32_16x16x32_bf16 a[188:191], v[188:191], v[148:151], a[188:191]
buffer_store_dword v0, v6, s[60:63], 0 offen sc1
v_mfma_f32_16x16x32_bf16 a[192:195], v[160:163], v[152:155], a[192:195]
v_mfma_f32_16x16x32_bf16 a[196:199], v[164:167], v[152:155], a[196:199]
v_mfma_f32_16x16x32_bf16 a[200:203], v[168:171], v[152:155], a[200:203]
v_mfma_f32_16x16x32_bf16 a[204:207], v[172:175], v[152:155], a[204:207]
buffer_load_dword v35, v4, s[96:99], 0 offen
v_mfma_f32_16x16x32_bf16 a[208:211], v[176:179], v[152:155], a[208:211]
v_mfma_f32_16x16x32_bf16 a[212:215], v[180:183], v[152:155], a[212:215]
v_mfma_f32_16x16x32_bf16 a[216:219], v[184:187], v[152:155], a[216:219]
v_mfma_f32_16x16x32_bf16 a[220:223], v[188:191], v[152:155], a[220:223]
v_mfma_f32_16x16x32_bf16 a[224:227], v[160:163], v[156:159], a[224:227]
v_mfma_f32_16x16x32_bf16 a[228:231], v[164:167], v[156:159], a[228:231]
s_mov_b32 s37, s36
v_mfma_f32_16x16x32_bf16 a[232:235], v[168:171], v[156:159], a[232:235]
s_add_u32 s35, s35, s45
v_mfma_f32_16x16x32_bf16 a[236:239], v[172:175], v[156:159], a[236:239]
s_add_u32 s36, s36, s34
v_mfma_f32_16x16x32_bf16 a[240:243], v[176:179], v[156:159], a[240:243]
v_mfma_f32_16x16x32_bf16 a[244:247], v[180:183], v[156:159], a[244:247]
v_mfma_f32_16x16x32_bf16 a[248:251], v[184:187], v[156:159], a[248:251]
v_mfma_f32_16x16x32_bf16 a[252:255], v[188:191], v[156:159], a[252:255]
s_waitcnt lgkmcnt(0)
v_mfma_f32_16x16x32_bf16 a[0:3], v[96:99], v[64:67], a[0:3]
s_add_u32 m0, s92, 98304
ds_read_b64_tr_b16 v[160:161], v194 offset:8192
v_mfma_f32_16x16x32_bf16 a[4:7], v[100:103], v[64:67], a[4:7]
buffer_load_dwordx4 v20, s[12:15], s24 offen offset:0 lds
v_mfma_f32_16x16x32_bf16 a[8:11], v[104:107], v[64:67], a[8:11]
ds_read_b64_tr_b16 v[162:163], v194 offset:9216
v_mfma_f32_16x16x32_bf16 a[12:15], v[108:111], v[64:67], a[12:15]
v_mfma_f32_16x16x32_bf16 a[16:19], v[112:115], v[64:67], a[16:19]
ds_read_b128 v[128:131], v193 offset:0
v_mfma_f32_16x16x32_bf16 a[20:23], v[116:119], v[64:67], a[20:23]
buffer_load_dwordx4 v21, s[12:15], s24 offen offset:1024 lds
v_mfma_f32_16x16x32_bf16 a[24:27], v[120:123], v[64:67], a[24:27]
ds_read_b64_tr_b16 v[164:165], v195 offset:8192
v_mfma_f32_16x16x32_bf16 a[28:31], v[124:127], v[64:67], a[28:31]
v_mfma_f32_16x16x32_bf16 a[32:35], v[96:99], v[68:71], a[32:35]
ds_read_b64_tr_b16 v[166:167], v195 offset:9216
v_mfma_f32_16x16x32_bf16 a[36:39], v[100:103], v[68:71], a[36:39]
buffer_load_dwordx4 v22, s[12:15], s24 offen offset:2048 lds
v_mfma_f32_16x16x32_bf16 a[40:43], v[104:107], v[68:71], a[40:43]
ds_read_b128 v[132:135], v193 offset:2048
v_mfma_f32_16x16x32_bf16 a[44:47], v[108:111], v[68:71], a[44:47]
v_mfma_f32_16x16x32_bf16 a[48:51], v[112:115], v[68:71], a[48:51]
ds_read_b64_tr_b16 v[168:169], v196 offset:8192
v_mfma_f32_16x16x32_bf16 a[52:55], v[116:119], v[68:71], a[52:55]
buffer_load_dwordx4 v23, s[12:15], s24 offen offset:3072 lds
v_mfma_f32_16x16x32_bf16 a[56:59], v[120:123], v[68:71], a[56:59]
ds_read_b64_tr_b16 v[170:171], v196 offset:9216
v_mfma_f32_16x16x32_bf16 a[60:63], v[124:127], v[68:71], a[60:63]
v_mfma_f32_16x16x32_bf16 a[64:67], v[96:99], v[72:75], a[64:67]
s_add_u32 m0, s92, 102400
ds_read_b128 v[136:139], v193 offset:4096
v_mfma_f32_16x16x32_bf16 a[68:71], v[100:103], v[72:75], a[68:71]
buffer_load_dwordx4 v20, s[12:15], s25 offen offset:0 lds
v_mfma_f32_16x16x32_bf16 a[72:75], v[104:107], v[72:75], a[72:75]
ds_read_b64_tr_b16 v[172:173], v197 offset:8192
v_mfma_f32_16x16x32_bf16 a[76:79], v[108:111], v[72:75], a[76:79]
v_mfma_f32_16x16x32_bf16 a[80:83], v[112:115], v[72:75], a[80:83]
ds_read_b64_tr_b16 v[174:175], v197 offset:9216
v_mfma_f32_16x16x32_bf16 a[84:87], v[116:119], v[72:75], a[84:87]
buffer_load_dwordx4 v21, s[12:15], s25 offen offset:1024 lds
v_mfma_f32_16x16x32_bf16 a[88:91], v[120:123], v[72:75], a[88:91]
ds_read_b128 v[140:143], v193 offset:6144
v_mfma_f32_16x16x32_bf16 a[92:95], v[124:127], v[72:75], a[92:95]
v_mfma_f32_16x16x32_bf16 a[96:99], v[96:99], v[76:79], a[96:99]
ds_read_b64_tr_b16 v[176:177], v198 offset:8192
v_mfma_f32_16x16x32_bf16 a[100:103], v[100:103], v[76:79], a[100:103]
buffer_load_dwordx4 v22, s[12:15], s25 offen offset:2048 lds
v_mfma_f32_16x16x32_bf16 a[104:107], v[104:107], v[76:79], a[104:107]
ds_read_b64_tr_b16 v[178:179], v198 offset:9216
v_mfma_f32_16x16x32_bf16 a[108:111], v[108:111], v[76:79], a[108:111]
v_mfma_f32_16x16x32_bf16 a[112:115], v[112:115], v[76:79], a[112:115]
ds_read_b128 v[144:147], v193 offset:8192
v_mfma_f32_16x16x32_bf16 a[116:119], v[116:119], v[76:79], a[116:119]
buffer_load_dwordx4 v23, s[12:15], s25 offen offset:3072 lds
v_mfma_f32_16x16x32_bf16 a[120:123], v[120:123], v[76:79], a[120:123]
ds_read_b64_tr_b16 v[180:181], v199 offset:8192
v_mfma_f32_16x16x32_bf16 a[124:127], v[124:127], v[76:79], a[124:127]
v_mfma_f32_16x16x32_bf16 a[128:131], v[96:99], v[80:83], a[128:131]
ds_read_b64_tr_b16 v[182:183], v199 offset:9216
v_mfma_f32_16x16x32_bf16 a[132:135], v[100:103], v[80:83], a[132:135]
v_mfma_f32_16x16x32_bf16 a[136:139], v[104:107], v[80:83], a[136:139]
ds_read_b128 v[148:151], v193 offset:10240
v_mfma_f32_16x16x32_bf16 a[140:143], v[108:111], v[80:83], a[140:143]
v_mfma_f32_16x16x32_bf16 a[144:147], v[112:115], v[80:83], a[144:147]
ds_read_b64_tr_b16 v[184:185], v200 offset:8192
v_mfma_f32_16x16x32_bf16 a[148:151], v[116:119], v[80:83], a[148:151]
v_mfma_f32_16x16x32_bf16 a[152:155], v[120:123], v[80:83], a[152:155]
ds_read_b64_tr_b16 v[186:187], v200 offset:9216
v_mfma_f32_16x16x32_bf16 a[156:159], v[124:127], v[80:83], a[156:159]
v_mfma_f32_16x16x32_bf16 a[160:163], v[96:99], v[84:87], a[160:163]
s_add_u32 s24, s24, 128
s_add_u32 s25, s25, 128
ds_read_b128 v[152:155], v193 offset:12288
v_mfma_f32_16x16x32_bf16 a[164:167], v[100:103], v[84:87], a[164:167]
v_mfma_f32_16x16x32_bf16 a[168:171], v[104:107], v[84:87], a[168:171]
ds_read_b64_tr_b16 v[188:189], v201 offset:8192
v_mfma_f32_16x16x32_bf16 a[172:175], v[108:111], v[84:87], a[172:175]
v_mfma_f32_16x16x32_bf16 a[176:179], v[112:115], v[84:87], a[176:179]
ds_read_b64_tr_b16 v[190:191], v201 offset:9216
v_mfma_f32_16x16x32_bf16 a[180:183], v[116:119], v[84:87], a[180:183]
v_mfma_f32_16x16x32_bf16 a[184:187], v[120:123], v[84:87], a[184:187]
ds_read_b128 v[156:159], v193 offset:14336
v_mfma_f32_16x16x32_bf16 a[188:191], v[124:127], v[84:87], a[188:191]
v_mfma_f32_16x16x32_bf16 a[192:195], v[96:99], v[88:91], a[192:195]
v_mfma_f32_16x16x32_bf16 a[196:199], v[100:103], v[88:91], a[196:199]
v_mfma_f32_16x16x32_bf16 a[200:203], v[104:107], v[88:91], a[200:203]
v_mfma_f32_16x16x32_bf16 a[204:207], v[108:111], v[88:91], a[204:207]
v_mfma_f32_16x16x32_bf16 a[208:211], v[112:115], v[88:91], a[208:211]
v_mfma_f32_16x16x32_bf16 a[212:215], v[116:119], v[88:91], a[212:215]
v_mfma_f32_16x16x32_bf16 a[216:219], v[120:123], v[88:91], a[216:219]
.Lpad2:
s_cbranch_execz .Ltch0
v_mfma_f32_16x16x32_bf16 a[220:223], v[124:127], v[88:91], a[220:223]
v_mfma_f32_16x16x32_bf16 a[224:227], v[96:99], v[92:95], a[224:227]
v_mfma_f32_16x16x32_bf16 a[228:231], v[100:103], v[92:95], a[228:231]
v_mfma_f32_16x16x32_bf16 a[232:235], v[104:107], v[92:95], a[232:235]
v_mfma_f32_16x16x32_bf16 a[236:239], v[108:111], v[92:95], a[236:239]
v_mfma_f32_16x16x32_bf16 a[240:243], v[112:115], v[92:95], a[240:243]
v_mfma_f32_16x16x32_bf16 a[244:247], v[116:119], v[92:95], a[244:247]
v_mfma_f32_16x16x32_bf16 a[248:251], v[120:123], v[92:95], a[248:251]
v_mfma_f32_16x16x32_bf16 a[252:255], v[124:127], v[92:95], a[252:255]
s_waitcnt vmcnt(16) lgkmcnt(0)
s_barrier
v_mfma_f32_16x16x32_bf16 a[0:3], v[160:163], v[128:131], a[0:3]
s_add_u32 m0, s91, 0
ds_read_b64_tr_b16 v[64:65], v26 offset:0
v_mfma_f32_16x16x32_bf16 a[4:7], v[164:167], v[128:131], a[4:7]
buffer_load_dwordx4 v16, s[16:19], s26 offen offset:0 lds
v_mfma_f32_16x16x32_bf16 a[8:11], v[168:171], v[128:131], a[8:11]
ds_read_b64_tr_b16 v[66:67], v26 offset:1024
v_mfma_f32_16x16x32_bf16 a[12:15], v[172:175], v[128:131], a[12:15]
v_mfma_f32_16x16x32_bf16 a[16:19], v[176:179], v[128:131], a[16:19]
ds_read_b64_tr_b16 v[68:69], v27 offset:0
v_mfma_f32_16x16x32_bf16 a[20:23], v[180:183], v[128:131], a[20:23]
buffer_load_dwordx4 v17, s[16:19], s26 offen offset:1024 lds
v_mfma_f32_16x16x32_bf16 a[24:27], v[184:187], v[128:131], a[24:27]
ds_read_b64_tr_b16 v[70:71], v27 offset:1024
v_mfma_f32_16x16x32_bf16 a[28:31], v[188:191], v[128:131], a[28:31]
v_mfma_f32_16x16x32_bf16 a[32:35], v[160:163], v[132:135], a[32:35]
ds_read_b64_tr_b16 v[72:73], v28 offset:0
v_mfma_f32_16x16x32_bf16 a[36:39], v[164:167], v[132:135], a[36:39]
buffer_load_dwordx4 v18, s[16:19], s26 offen offset:2048 lds
v_mfma_f32_16x16x32_bf16 a[40:43], v[168:171], v[132:135], a[40:43]
ds_read_b64_tr_b16 v[74:75], v28 offset:1024
v_mfma_f32_16x16x32_bf16 a[44:47], v[172:175], v[132:135], a[44:47]
v_mfma_f32_16x16x32_bf16 a[48:51], v[176:179], v[132:135], a[48:51]
ds_read_b64_tr_b16 v[76:77], v29 offset:0
v_mfma_f32_16x16x32_bf16 a[52:55], v[180:183], v[132:135], a[52:55]
buffer_load_dwordx4 v19, s[16:19], s26 offen offset:3072 lds
v_mfma_f32_16x16x32_bf16 a[56:59], v[184:187], v[132:135], a[56:59]
ds_read_b64_tr_b16 v[78:79], v29 offset:1024
v_mfma_f32_16x16x32_bf16 a[60:63], v[188:191], v[132:135], a[60:63]
s_add_u32 s26, s26, 524288
v_mfma_f32_16x16x32_bf16 a[64:67], v[160:163], v[136:139], a[64:67]
s_add_u32 m0, s92, 131072
ds_read_b64_tr_b16 v[80:81], v30 offset:0
v_mfma_f32_16x16x32_bf16 a[68:71], v[164:167], v[136:139], a[68:71]
buffer_load_dwordx4 v20, s[12:15], s24 offen offset:0 lds
v_mfma_f32_16x16x32_bf16 a[72:75], v[168:171], v[136:139], a[72:75]
ds_read_b64_tr_b16 v[82:83], v30 offset:1024
v_mfma_f32_16x16x32_bf16 a[76:79], v[172:175], v[136:139], a[76:79]
v_mfma_f32_16x16x32_bf16 a[80:83], v[176:179], v[136:139], a[80:83]
ds_read_b64_tr_b16 v[84:85], v31 offset:0
v_mfma_f32_16x16x32_bf16 a[84:87], v[180:183], v[136:139], a[84:87]
buffer_load_dwordx4 v21, s[12:15], s24 offen offset:1024 lds
v_mfma_f32_16x16x32_bf16 a[88:91], v[184:187], v[136:139], a[88:91]
ds_read_b64_tr_b16 v[86:87], v31 offset:1024
v_mfma_f32_16x16x32_bf16 a[92:95], v[188:191], v[136:139], a[92:95]
v_mfma_f32_16x16x32_bf16 a[96:99], v[160:163], v[140:143], a[96:99]
ds_read_b64_tr_b16 v[88:89], v32 offset:0
v_mfma_f32_16x16x32_bf16 a[100:103], v[164:167], v[140:143], a[100:103]
buffer_load_dwordx4 v22, s[12:15], s24 offen offset:2048 lds
v_mfma_f32_16x16x32_bf16 a[104:107], v[168:171], v[140:143], a[104:107]
ds_read_b64_tr_b16 v[90:91], v32 offset:1024
v_mfma_f32_16x16x32_bf16 a[108:111], v[172:175], v[140:143], a[108:111]
v_mfma_f32_16x16x32_bf16 a[112:115], v[176:179], v[140:143], a[112:115]
ds_read_b64_tr_b16 v[92:93], v33 offset:0
v_mfma_f32_16x16x32_bf16 a[116:119], v[180:183], v[140:143], a[116:119]
buffer_load_dwordx4 v23, s[12:15], s24 offen offset:3072 lds
v_mfma_f32_16x16x32_bf16 a[120:123], v[184:187], v[140:143], a[120:123]
ds_read_b64_tr_b16 v[94:95], v33 offset:1024
v_mfma_f32_16x16x32_bf16 a[124:127], v[188:191], v[140:143], a[124:127]
v_mfma_f32_16x16x32_bf16 a[128:131], v[160:163], v[144:147], a[128:131]
s_add_u32 m0, s92, 135168
v_mfma_f32_16x16x32_bf16 a[132:135], v[164:167], v[144:147], a[132:135]
buffer_load_dwordx4 v20, s[12:15], s25 offen offset:0 lds
v_mfma_f32_16x16x32_bf16 a[136:139], v[168:171], v[144:147], a[136:139]
ds_read_b128 v[48:51], v24 offset:0
v_mfma_f32_16x16x32_bf16 a[140:143], v[172:175], v[144:147], a[140:143]
v_mfma_f32_16x16x32_bf16 a[144:147], v[176:179], v[144:147], a[144:147]
ds_read_b128 v[52:55], v24 offset:2048
v_mfma_f32_16x16x32_bf16 a[148:151], v[180:183], v[144:147], a[148:151]
buffer_load_dwordx4 v21, s[12:15], s25 offen offset:1024 lds
v_mfma_f32_16x16x32_bf16 a[152:155], v[184:187], v[144:147], a[152:155]
ds_read_b128 v[56:59], v24 offset:4096
v_mfma_f32_16x16x32_bf16 a[156:159], v[188:191], v[144:147], a[156:159]
v_mfma_f32_16x16x32_bf16 a[160:163], v[160:163], v[148:151], a[160:163]
v_mfma_f32_16x16x32_bf16 a[164:167], v[164:167], v[148:151], a[164:167]
buffer_load_dwordx4 v22, s[12:15], s25 offen offset:2048 lds
v_mfma_f32_16x16x32_bf16 a[168:171], v[168:171], v[148:151], a[168:171]
v_mfma_f32_16x16x32_bf16 a[172:175], v[172:175], v[148:151], a[172:175]
v_mfma_f32_16x16x32_bf16 a[176:179], v[176:179], v[148:151], a[176:179]
v_mfma_f32_16x16x32_bf16 a[180:183], v[180:183], v[148:151], a[180:183]
buffer_load_dwordx4 v23, s[12:15], s25 offen offset:3072 lds
v_mfma_f32_16x16x32_bf16 a[184:187], v[184:187], v[148:151], a[184:187]
v_mfma_f32_16x16x32_bf16 a[188:191], v[188:191], v[148:151], a[188:191]
v_mfma_f32_16x16x32_bf16 a[192:195], v[160:163], v[152:155], a[192:195]
v_mfma_f32_16x16x32_bf16 a[196:199], v[164:167], v[152:155], a[196:199]
s_add_u32 s24, s24, 128
s_add_u32 s25, s25, 128
v_mfma_f32_16x16x32_bf16 a[200:203], v[168:171], v[152:155], a[200:203]
v_mfma_f32_16x16x32_bf16 a[204:207], v[172:175], v[152:155], a[204:207]
v_mfma_f32_16x16x32_bf16 a[208:211], v[176:179], v[152:155], a[208:211]
v_mfma_f32_16x16x32_bf16 a[212:215], v[180:183], v[152:155], a[212:215]
v_mfma_f32_16x16x32_bf16 a[216:219], v[184:187], v[152:155], a[216:219]
v_mfma_f32_16x16x32_bf16 a[220:223], v[188:191], v[152:155], a[220:223]
v_mfma_f32_16x16x32_bf16 a[224:227], v[160:163], v[156:159], a[224:227]
v_mfma_f32_16x16x32_bf16 a[228:231], v[164:167], v[156:159], a[228:231]
v_mfma_f32_16x16x32_bf16 a[232:235], v[168:171], v[156:159], a[232:235]
v_mfma_f32_16x16x32_bf16 a[236:239], v[172:175], v[156:159], a[236:239]
v_mfma_f32_16x16x32_bf16 a[240:243], v[176:179], v[156:159], a[240:243]
v_mfma_f32_16x16x32_bf16 a[244:247], v[180:183], v[156:159], a[244:247]
v_mfma_f32_16x16x32_bf16 a[248:251], v[184:187], v[156:159], a[248:251]
v_mfma_f32_16x16x32_bf16 a[252:255], v[188:191], v[156:159], a[252:255]
s_waitcnt lgkmcnt(0)
s_branch .Lp2
.Lp1_v_pad:
s_cbranch_execz .Ltch0
.Lp1_v:
v_mfma_f32_16x16x32_bf16 a[0:3], v[64:67], v[96:99], 0
ds_read_b64_tr_b16 v[160:161], v194 offset:8192
v_mfma_f32_16x16x32_bf16 a[8:11], v[64:67], v[100:103], 0
s_add_u32 s37, s92, 98304
s_mov_b32 s40, 128
s_mov_b32 s41, 524416
v_mfma_f32_16x16x32_bf16 a[16:19], v[64:67], v[104:107], 0
s_mov_b32 m0, s37
ds_read_b64_tr_b16 v[162:163], v194 offset:9216
v_mfma_f32_16x16x32_bf16 a[24:27], v[64:67], v[108:111], 0
buffer_load_dwordx4 v202, s[4:7], s40 offen offset:0 lds
v_mfma_f32_16x16x32_bf16 a[32:35], v[64:67], v[112:115], 0
ds_read_b128 v[128:131], v193 offset:0
v_mfma_f32_16x16x32_bf16 a[40:43], v[64:67], v[116:119], 0
buffer_load_dwordx4 v203, s[4:7], s40 offen offset:1024 lds
v_mfma_f32_16x16x32_bf16 a[48:51], v[64:67], v[120:123], 0
ds_read_b64_tr_b16 v[164:165], v195 offset:8192
v_mfma_f32_16x16x32_bf16 a[56:59], v[64:67], v[124:127], 0
buffer_load_dwordx4 v204, s[4:7], s40 offen offset:2048 lds
v_mfma_f32_16x16x32_bf16 a[4:7], v[68:71], v[96:99], 0
ds_read_b64_tr_b16 v[166:167], v195 offset:9216
v_mfma_f32_16x16x32_bf16 a[12:15], v[68:71], v[100:103], 0
buffer_load_dwordx4 v205, s[4:7], s40 offen offset:3072 lds
v_mfma_f32_16x16x32_bf16 a[20:23], v[68:71], v[104:107], 0
s_add_u32 m0, s37, 4096
ds_read_b128 v[132:135], v193 offset:2048
v_mfma_f32_16x16x32_bf16 a[28:31], v[68:71], v[108:111], 0
buffer_load_dwordx4 v202, s[4:7], s41 offen offset:0 lds
v_mfma_f32_16x16x32_bf16 a[36:39], v[68:71], v[112:115], 0
ds_read_b64_tr_b16 v[168:169], v196 offset:8192
v_mfma_f32_16x16x32_bf16 a[44:47], v[68:71], v[116:119], 0
buffer_load_dwordx4 v203, s[4:7], s41 offen offset:1024 lds
v_mfma_f32_16x16x32_bf16 a[52:55], v[68:71], v[120:123], 0
ds_read_b64_tr_b16 v[170:171], v196 offset:9216
v_mfma_f32_16x16x32_bf16 a[60:63], v[68:71], v[124:127], 0
buffer_load_dwordx4 v204, s[4:7], s41 offen offset:2048 lds
v_mfma_f32_16x16x32_bf16 a[64:67], v[72:75], v[96:99], 0
ds_read_b128 v[136:139], v193 offset:4096
v_mfma_f32_16x16x32_bf16 a[72:75], v[72:75], v[100:103], 0
buffer_load_dwordx4 v205, s[4:7], s41 offen offset:3072 lds
v_mfma_f32_16x16x32_bf16 a[80:83], v[72:75], v[104:107], 0
ds_read_b64_tr_b16 v[172:173], v197 offset:8192
v_mfma_f32_16x16x32_bf16 a[88:91], v[72:75], v[108:111], 0
s_add_u32 s37, s92, 131072
s_mov_b32 s42, 1048576
s_mov_b32 s43, 1179648
v_mfma_f32_16x16x32_bf16 a[96:99], v[72:75], v[112:115], 0
s_mov_b32 m0, s37
ds_read_b64_tr_b16 v[174:175], v197 offset:9216
v_mfma_f32_16x16x32_bf16 a[104:107], v[72:75], v[116:119], 0
buffer_load_dwordx4 v16, s[8:11], s42 offen offset:0 nt lds
v_mfma_f32_16x16x32_bf16 a[112:115], v[72:75], v[120:123], 0
ds_read_b128 v[140:143], v193 offset:6144
v_mfma_f32_16x16x32_bf16 a[120:123], v[72:75], v[124:127], 0
buffer_load_dwordx4 v17, s[8:11], s42 offen offset:1024 nt lds
v_mfma_f32_16x16x32_bf16 a[68:71], v[76:79], v[96:99], 0
ds_read_b64_tr_b16 v[176:177], v198 offset:8192
v_mfma_f32_16x16x32_bf16 a[76:79], v[76:79], v[100:103], 0
buffer_load_dwordx4 v18, s[8:11], s42 offen offset:2048 nt lds
v_mfma_f32_16x16x32_bf16 a[84:87], v[76:79], v[104:107], 0
ds_read_b64_tr_b16 v[178:179], v198 offset:9216
v_mfma_f32_16x16x32_bf16 a[92:95], v[76:79], v[108:111], 0
buffer_load_dwordx4 v19, s[8:11], s42 offen offset:3072 nt lds
v_mfma_f32_16x16x32_bf16 a[100:103], v[76:79], v[112:115], 0
s_add_u32 m0, s37, 4096
ds_read_b128 v[144:147], v193 offset:8192
v_mfma_f32_16x16x32_bf16 a[108:111], v[76:79], v[116:119], 0
buffer_load_dwordx4 v16, s[8:11], s43 offen offset:0 nt lds
v_mfma_f32_16x16x32_bf16 a[116:119], v[76:79], v[120:123], 0
ds_read_b64_tr_b16 v[180:181], v199 offset:8192
v_mfma_f32_16x16x32_bf16 a[124:127], v[76:79], v[124:127], 0
buffer_load_dwordx4 v17, s[8:11], s43 offen offset:1024 nt lds
v_mfma_f32_16x16x32_bf16 a[128:131], v[80:83], v[96:99], 0
ds_read_b64_tr_b16 v[182:183], v199 offset:9216
v_mfma_f32_16x16x32_bf16 a[136:139], v[80:83], v[100:103], 0
buffer_load_dwordx4 v18, s[8:11], s43 offen offset:2048 nt lds
v_mfma_f32_16x16x32_bf16 a[144:147], v[80:83], v[104:107], 0
ds_read_b128 v[148:151], v193 offset:10240
v_mfma_f32_16x16x32_bf16 a[152:155], v[80:83], v[108:111], 0
buffer_load_dwordx4 v19, s[8:11], s43 offen offset:3072 nt lds
v_mfma_f32_16x16x32_bf16 a[160:163], v[80:83], v[112:115], 0
ds_read_b64_tr_b16 v[184:185], v200 offset:8192
v_mfma_f32_16x16x32_bf16 a[168:171], v[80:83], v[116:119], 0
v_mfma_f32_16x16x32_bf16 a[176:179], v[80:83], v[120:123], 0
ds_read_b64_tr_b16 v[186:187], v200 offset:9216
v_mfma_f32_16x16x32_bf16 a[184:187], v[80:83], v[124:127], 0
v_mfma_f32_16x16x32_bf16 a[132:135], v[84:87], v[96:99], 0
ds_read_b128 v[152:155], v193 offset:12288
v_mfma_f32_16x16x32_bf16 a[140:143], v[84:87], v[100:103], 0
v_mfma_f32_16x16x32_bf16 a[148:151], v[84:87], v[104:107], 0
ds_read_b64_tr_b16 v[188:189], v201 offset:8192
v_mfma_f32_16x16x32_bf16 a[156:159], v[84:87], v[108:111], 0
v_mfma_f32_16x16x32_bf16 a[164:167], v[84:87], v[112:115], 0
s_add_u32 s44, s35, 65536
ds_read_b64_tr_b16 v[190:191], v201 offset:9216
v_mfma_f32_16x16x32_bf16 a[172:175], v[84:87], v[116:119], 0
s_cmp_ge_u32 s44, s39
s_cselect_b32 s45, s55, s54
v_mfma_f32_16x16x32_bf16 a[180:183], v[84:87], v[120:123], 0
ds_read_b128 v[156:159], v193 offset:14336
v_mfma_f32_16x16x32_bf16 a[188:191], v[84:87], v[124:127], 0
s_add_u32 s44, s36, 65536
v_mfma_f32_16x16x32_bf16 a[192:195], v[88:91], v[96:99], 0
v_mfma_f32_16x16x32_bf16 a[200:203], v[88:91], v[100:103], 0
s_cmp_ge_u32 s44, s39
s_cselect_b32 s34, s55, s54
v_mfma_f32_16x16x32_bf16 a[208:211], v[88:91], v[104:107], 0
v_add_u32 v194, s45, v194
v_mfma_f32_16x16x32_bf16 a[216:219], v[88:91], v[108:111], 0
v_add_u32 v195, s45, v195
v_mfma_f32_16x16x32_bf16 a[224:227], v[88:91], v[112:115], 0
v_add_u32 v196, s45, v196
v_mfma_f32_16x16x32_bf16 a[232:235], v[88:91], v[116:119], 0
v_add_u32 v197, s45, v197
v_mfma_f32_16x16x32_bf16 a[240:243], v[88:91], v[120:123], 0
v_add_u32 v198, s45, v198
v_mfma_f32_16x16x32_bf16 a[248:251], v[88:91], v[124:127], 0
v_add_u32 v199, s45, v199
v_mfma_f32_16x16x32_bf16 a[196:199], v[92:95], v[96:99], 0
v_add_u32 v200, s45, v200
v_mfma_f32_16x16x32_bf16 a[204:207], v[92:95], v[100:103], 0
v_add_u32 v201, s45, v201
v_mfma_f32_16x16x32_bf16 a[212:215], v[92:95], v[104:107], 0
v_add_u32 v192, s34, v192
v_mfma_f32_16x16x32_bf16 a[220:223], v[92:95], v[108:111], 0
v_add_u32 v193, s34, v193
v_mfma_f32_16x16x32_bf16 a[228:231], v[92:95], v[112:115], 0
v_mfma_f32_16x16x32_bf16 a[236:239], v[92:95], v[116:119], 0
s_mov_b32 s40, 256
v_mfma_f32_16x16x32_bf16 a[244:247], v[92:95], v[120:123], 0
s_mov_b32 s41, 524544
v_mfma_f32_16x16x32_bf16 a[252:255], v[92:95], v[124:127], 0
s_waitcnt vmcnt(8) lgkmcnt(0)
s_barrier
v_mfma_f32_16x16x32_bf16 a[0:3], v[128:131], v[160:163], a[0:3]
s_mov_b32 m0, s35
ds_read_b64_tr_b16 v[96:97], v194 offset:0
v_mfma_f32_16x16x32_bf16 a[8:11], v[128:131], v[164:167], a[8:11]
buffer_load_dwordx4 v202, s[4:7], s40 offen offset:0 lds
v_mfma_f32_16x16x32_bf16 a[16:19], v[128:131], v[168:171], a[16:19]
ds_read_b64_tr_b16 v[98:99], v194 offset:1024
v_mfma_f32_16x16x32_bf16 a[24:27], v[128:131], v[172:175], a[24:27]
v_mfma_f32_16x16x32_bf16 a[32:35], v[128:131], v[176:179], a[32:35]
ds_read_b128 v[64:67], v192 offset:0
v_mfma_f32_16x16x32_bf16 a[40:43], v[128:131], v[180:183], a[40:43]
buffer_load_dwordx4 v203, s[4:7], s40 offen offset:1024 lds
v_mfma_f32_16x16x32_bf16 a[48:51], v[128:131], v[184:187], a[48:51]
ds_read_b64_tr_b16 v[100:101], v195 offset:0
v_mfma_f32_16x16x32_bf16 a[56:59], v[128:131], v[188:191], a[56:59]
v_mfma_f32_16x16x32_bf16 a[4:7], v[132:135], v[160:163], a[4:7]
ds_read_b64_tr_b16 v[102:103], v195 offset:1024
v_mfma_f32_16x16x32_bf16 a[12:15], v[132:135], v[164:167], a[12:15]
buffer_load_dwordx4 v204, s[4:7], s40 offen offset:2048 lds
v_mfma_f32_16x16x32_bf16 a[20:23], v[132:135], v[168:171], a[20:23]
ds_read_b128 v[68:71], v192 offset:2048
v_mfma_f32_16x16x32_bf16 a[28:31], v[132:135], v[172:175], a[28:31]
v_mfma_f32_16x16x32_bf16 a[36:39], v[132:135], v[176:179], a[36:39]
ds_read_b64_tr_b16 v[104:105], v196 offset:0
v_mfma_f32_16x16x32_bf16 a[44:47], v[132:135], v[180:183], a[44:47]
buffer_load_dwordx4 v205, s[4:7], s40 offen offset:3072 lds
v_mfma_f32_16x16x32_bf16 a[52:55], v[132:135], v[184:187], a[52:55]
ds_read_b64_tr_b16 v[106:107], v196 offset:1024
v_mfma_f32_16x16x32_bf16 a[60:63], v[132:135], v[188:191], a[60:63]
v_mfma_f32_16x16x32_bf16 a[64:67], v[136:139], v[160:163], a[64:67]
s_add_u32 m0, s35, 4096
ds_read_b128 v[72:75], v192 offset:4096
v_mfma_f32_16x16x32_bf16 a[72:75], v[136:139], v[164:167], a[72:75]
buffer_load_dwordx4 v202, s[4:7], s41 offen offset:0 lds
v_mfma_f32_16x16x32_bf16 a[80:83], v[136:139], v[168:171], a[80:83]
ds_read_b64_tr_b16 v[108:109], v197 offset:0
v_mfma_f32_16x16x32_bf16 a[88:91], v[136:139], v[172:175], a[88:91]
v_mfma_f32_16x16x32_bf16 a[96:99], v[136:139], v[176:179], a[96:99]
ds_read_b64_tr_b16 v[110:111], v197 offset:1024
v_mfma_f32_16x16x32_bf16 a[104:107], v[136:139], v[180:183], a[104:107]
buffer_load_dwordx4 v203, s[4:7], s41 offen offset:1024 lds
v_mfma_f32_16x16x32_bf16 a[112:115], v[136:139], v[184:187], a[112:115]
ds_read_b128 v[76:79], v192 offset:6144
v_mfma_f32_16x16x32_bf16 a[120:123], v[136:139], v[188:191], a[120:123]
v_mfma_f32_16x16x32_bf16 a[68:71], v[140:143], v[160:163], a[68:71]
ds_read_b64_tr_b16 v[112:113], v198 offset:0
v_mfma_f32_16x16x32_bf16 a[76:79], v[140:143], v[164:167], a[76:79]
buffer_load_dwordx4 v204, s[4:7], s41 offen offset:2048 lds
v_mfma_f32_16x16x32_bf16 a[84:87], v[140:143], v[168:171], a[84:87]
ds_read_b64_tr_b16 v[114:115], v198 offset:1024
v_mfma_f32_16x16x32_bf16 a[92:95], v[140:143], v[172:175], a[92:95]
v_mfma_f32_16x16x32_bf16 a[100:103], v[140:143], v[176:179], a[100:103]
ds_read_b128 v[80:83], v192 offset:8192
v_mfma_f32_16x16x32_bf16 a[108:111], v[140:143], v[180:183], a[108:111]
buffer_load_dwordx4 v205, s[4:7], s41 offen offset:3072 lds
v_mfma_f32_16x16x32_bf16 a[116:119], v[140:143], v[184:187], a[116:119]
ds_read_b64_tr_b16 v[116:117], v199 offset:0
v_mfma_f32_16x16x32_bf16 a[124:127], v[140:143], v[188:191], a[124:127]
v_mfma_f32_16x16x32_bf16 a[128:131], v[144:147], v[160:163], a[128:131]
ds_read_b64_tr_b16 v[118:119], v199 offset:1024
v_mfma_f32_16x16x32_bf16 a[136:139], v[144:147], v[164:167], a[136:139]
buffer_load_dword v35, v206, s[56:59], s48 offen
v_mfma_f32_16x16x32_bf16 a[144:147], v[144:147], v[168:171], a[144:147]
ds_read_b128 v[84:87], v192 offset:10240
v_mfma_f32_16x16x32_bf16 a[152:155], v[144:147], v[172:175], a[152:155]
buffer_load_dword v2, v3, s[60:63], s90 offen sc1
v_mfma_f32_16x16x32_bf16 a[160:163], v[144:147], v[176:179], a[160:163]
s_mov_b32 s42, 1572864
ds_read_b64_tr_b16 v[120:121], v200 offset:0
v_mfma_f32_16x16x32_bf16 a[168:171], v[144:147], v[180:183], a[168:171]
s_mov_b32 s43, 1703936
buffer_load_dword v35, v207, s[12:15], s49 offen
v_mfma_f32_16x16x32_bf16 a[176:179], v[144:147], v[184:187], a[176:179]
ds_read_b64_tr_b16 v[122:123], v200 offset:1024
v_mfma_f32_16x16x32_bf16 a[184:187], v[144:147], v[188:191], a[184:187]
buffer_load_dword v11, v6, s[60:63], 0 offen sc1
v_mfma_f32_16x16x32_bf16 a[132:135], v[148:151], v[160:163], a[132:135]
s_add_u32 s40, s40, 128
ds_read_b128 v[88:91], v192 offset:12288
v_mfma_f32_16x16x32_bf16 a[140:143], v[148:151], v[164:167], a[140:143]
s_add_u32 s41, s41, 128
v_mfma_f32_16x16x32_bf16 a[148:151], v[148:151], v[168:171], a[148:151]
ds_read_b64_tr_b16 v[124:125], v201 offset:0
v_mfma_f32_16x16x32_bf16 a[156:159], v[148:151], v[172:175], a[156:159]
s_add_u32 s48, s48, 0x100000
v_mfma_f32_16x16x32_bf16 a[164:167], v[148:151], v[176:179], a[164:167]
ds_read_b64_tr_b16 v[126:127], v201 offset:1024
v_mfma_f32_16x16x32_bf16 a[172:175], v[148:151], v[180:183], a[172:175]
s_add_u32 s49, s49, 256
v_mfma_f32_16x16x32_bf16 a[180:183], v[148:151], v[184:187], a[180:183]
ds_read_b128 v[92:95], v192 offset:14336
v_mfma_f32_16x16x32_bf16 a[188:191], v[148:151], v[188:191], a[188:191]
buffer_store_dword v45, v44, s[60:63], 0 offen sc1
v_mfma_f32_16x16x32_bf16 a[192:195], v[152:155], v[160:163], a[192:195]
v_mfma_f32_16x16x32_bf16 a[200:203], v[152:155], v[164:167], a[200:203]
v_mfma_f32_16x16x32_bf16 a[208:211], v[152:155], v[168:171], a[208:211]
v_mfma_f32_16x16x32_bf16 a[216:219], v[152:155], v[172:175], a[216:219]
buffer_store_dword v45, v46, s[60:63], 0 offen sc1
v_mfma_f32_16x16x32_bf16 a[224:227], v[152:155], v[176:179], a[224:227]
v_mfma_f32_16x16x32_bf16 a[232:235], v[152:155], v[180:183], a[232:235]
v_mfma_f32_16x16x32_bf16 a[240:243], v[152:155], v[184:187], a[240:243]
v_mfma_f32_16x16x32_bf16 a[248:251], v[152:155], v[188:191], a[248:251]
v_mfma_f32_16x16x32_bf16 a[196:199], v[156:159], v[160:163], a[196:199]
v_mfma_f32_16x16x32_bf16 a[204:207], v[156:159], v[164:167], a[204:207]
s_mov_b32 s37, s36
v_mfma_f32_16x16x32_bf16 a[212:215], v[156:159], v[168:171], a[212:215]
s_add_u32 s35, s35, s45
v_mfma_f32_16x16x32_bf16 a[220:223], v[156:159], v[172:175], a[220:223]
s_add_u32 s36, s36, s34
v_mfma_f32_16x16x32_bf16 a[228:231], v[156:159], v[176:179], a[228:231]
v_mfma_f32_16x16x32_bf16 a[236:239], v[156:159], v[180:183], a[236:239]
v_mfma_f32_16x16x32_bf16 a[244:247], v[156:159], v[184:187], a[244:247]
v_mfma_f32_16x16x32_bf16 a[252:255], v[156:159], v[188:191], a[252:255]
s_waitcnt lgkmcnt(0)
s_nop 0
s_nop 0
s_nop 0
s_nop 0
s_nop 0
s_nop 0
.Lp1_vloop:
v_mfma_f32_16x16x32_bf16 a[0:3], v[64:67], v[96:99], a[0:3]
s_mov_b32 m0, s37
ds_read_b64_tr_b16 v[160:161], v194 offset:8192
v_mfma_f32_16x16x32_bf16 a[8:11], v[64:67], v[100:103], a[8:11]
buffer_load_dwordx4 v16, s[8:11], s42 offen offset:0 nt lds
v_mfma_f32_16x16x32_bf16 a[16:19], v[64:67], v[104:107], a[16:19]
ds_read_b64_tr_b16 v[162:163], v194 offset:9216
v_mfma_f32_16x16x32_bf16 a[24:27], v[64:67], v[108:111], a[24:27]
v_mfma_f32_16x16x32_bf16 a[32:35], v[64:67], v[112:115], a[32:35]
ds_read_b128 v[128:131], v193 offset:0
v_mfma_f32_16x16x32_bf16 a[40:43], v[64:67], v[116:119], a[40:43]
buffer_load_dwordx4 v17, s[8:11], s42 offen offset:1024 nt lds
v_mfma_f32_16x16x32_bf16 a[48:51], v[64:67], v[120:123], a[48:51]
ds_read_b64_tr_b16 v[164:165], v195 offset:8192
v_mfma_f32_16x16x32_bf16 a[56:59], v[64:67], v[124:127], a[56:59]
v_mfma_f32_16x16x32_bf16 a[4:7], v[68:71], v[96:99], a[4:7]
ds_read_b64_tr_b16 v[166:167], v195 offset:9216
v_mfma_f32_16x16x32_bf16 a[12:15], v[68:71], v[100:103], a[12:15]
buffer_load_dwordx4 v18, s[8:11], s42 offen offset:2048 nt lds
v_mfma_f32_16x16x32_bf16 a[20:23], v[68:71], v[104:107], a[20:23]
ds_read_b128 v[132:135], v193 offset:2048
v_mfma_f32_16x16x32_bf16 a[28:31], v[68:71], v[108:111], a[28:31]
v_mfma_f32_16x16x32_bf16 a[36:39], v[68:71], v[112:115], a[36:39]
ds_read_b64_tr_b16 v[168:169], v196 offset:8192
v_mfma_f32_16x16x32_bf16 a[44:47], v[68:71], v[116:119], a[44:47]
buffer_load_dwordx4 v19, s[8:11], s42 offen offset:3072 nt lds
v_mfma_f32_16x16x32_bf16 a[52:55], v[68:71], v[120:123], a[52:55]
ds_read_b64_tr_b16 v[170:171], v196 offset:9216
v_mfma_f32_16x16x32_bf16 a[60:63], v[68:71], v[124:127], a[60:63]
v_mfma_f32_16x16x32_bf16 a[64:67], v[72:75], v[96:99], a[64:67]
s_add_u32 m0, s37, 4096
ds_read_b128 v[136:139], v193 offset:4096
v_mfma_f32_16x16x32_bf16 a[72:75], v[72:75], v[100:103], a[72:75]
buffer_load_dwordx4 v16, s[8:11], s43 offen offset:0 nt lds
v_mfma_f32_16x16x32_bf16 a[80:83], v[72:75], v[104:107], a[80:83]
ds_read_b64_tr_b16 v[172:173], v197 offset:8192
v_mfma_f32_16x16x32_bf16 a[88:91], v[72:75], v[108:111], a[88:91]
v_mfma_f32_16x16x32_bf16 a[96:99], v[72:75], v[112:115], a[96:99]
ds_read_b64_tr_b16 v[174:175], v197 offset:9216
v_mfma_f32_16x16x32_bf16 a[104:107], v[72:75], v[116:119], a[104:107]
buffer_load_dwordx4 v17, s[8:11], s43 offen offset:1024 nt lds
v_mfma_f32_16x16x32_bf16 a[112:115], v[72:75], v[120:123], a[112:115]
ds_read_b128 v[140:143], v193 offset:6144
v_mfma_f32_16x16x32_bf16 a[120:123], v[72:75], v[124:127], a[120:123]
v_mfma_f32_16x16x32_bf16 a[68:71], v[76:79], v[96:99], a[68:71]
ds_read_b64_tr_b16 v[176:177], v198 offset:8192
v_mfma_f32_16x16x32_bf16 a[76:79], v[76:79], v[100:103], a[76:79]
buffer_load_dwordx4 v18, s[8:11], s43 offen offset:2048 nt lds
v_mfma_f32_16x16x32_bf16 a[84:87], v[76:79], v[104:107], a[84:87]
ds_read_b64_tr_b16 v[178:179], v198 offset:9216
v_mfma_f32_16x16x32_bf16 a[92:95], v[76:79], v[108:111], a[92:95]
v_mfma_f32_16x16x32_bf16 a[100:103], v[76:79], v[112:115], a[100:103]
ds_read_b128 v[144:147], v193 offset:8192
v_mfma_f32_16x16x32_bf16 a[108:111], v[76:79], v[116:119], a[108:111]
buffer_load_dwordx4 v19, s[8:11], s43 offen offset:3072 nt lds
v_mfma_f32_16x16x32_bf16 a[116:119], v[76:79], v[120:123], a[116:119]
ds_read_b64_tr_b16 v[180:181], v199 offset:8192
v_mfma_f32_16x16x32_bf16 a[124:127], v[76:79], v[124:127], a[124:127]
v_mfma_f32_16x16x32_bf16 a[128:131], v[80:83], v[96:99], a[128:131]
ds_read_b64_tr_b16 v[182:183], v199 offset:9216
v_mfma_f32_16x16x32_bf16 a[136:139], v[80:83], v[100:103], a[136:139]
v_mfma_f32_16x16x32_bf16 a[144:147], v[80:83], v[104:107], a[144:147]
ds_read_b128 v[148:151], v193 offset:10240
v_mfma_f32_16x16x32_bf16 a[152:155], v[80:83], v[108:111], a[152:155]
v_mfma_f32_16x16x32_bf16 a[160:163], v[80:83], v[112:115], a[160:163]
ds_read_b64_tr_b16 v[184:185], v200 offset:8192
v_mfma_f32_16x16x32_bf16 a[168:171], v[80:83], v[116:119], a[168:171]
v_mfma_f32_16x16x32_bf16 a[176:179], v[80:83], v[120:123], a[176:179]
ds_read_b64_tr_b16 v[186:187], v200 offset:9216
v_mfma_f32_16x16x32_bf16 a[184:187], v[80:83], v[124:127], a[184:187]
v_mfma_f32_16x16x32_bf16 a[132:135], v[84:87], v[96:99], a[132:135]
s_add_u32 s42, s42, 524288
ds_read_b128 v[152:155], v193 offset:12288
v_mfma_f32_16x16x32_bf16 a[140:143], v[84:87], v[100:103], a[140:143]
s_add_u32 s43, s43, 524288
v_mfma_f32_16x16x32_bf16 a[148:151], v[84:87], v[104:107], a[148:151]
ds_read_b64_tr_b16 v[188:189], v201 offset:8192
v_mfma_f32_16x16x32_bf16 a[156:159], v[84:87], v[108:111], a[156:159]
v_mfma_f32_16x16x32_bf16 a[164:167], v[84:87], v[112:115], a[164:167]
s_add_u32 s44, s35, 65536
ds_read_b64_tr_b16 v[190:191], v201 offset:9216
v_mfma_f32_16x16x32_bf16 a[172:175], v[84:87], v[116:119], a[172:175]
s_cmp_ge_u32 s44, s39
s_cselect_b32 s45, s55, s54
v_mfma_f32_16x16x32_bf16 a[180:183], v[84:87], v[120:123], a[180:183]
ds_read_b128 v[156:159], v193 offset:14336
v_mfma_f32_16x16x32_bf16 a[188:191], v[84:87], v[124:127], a[188:191]
s_add_u32 s44, s36, 65536
v_mfma_f32_16x16x32_bf16 a[192:195], v[88:91], v[96:99], a[192:195]
v_mfma_f32_16x16x32_bf16 a[200:203], v[88:91], v[100:103], a[200:203]
s_cmp_ge_u32 s44, s39
s_cselect_b32 s34, s55, s54
v_mfma_f32_16x16x32_bf16 a[208:211], v[88:91], v[104:107], a[208:211]
v_add_u32 v194, s45, v194
v_mfma_f32_16x16x32_bf16 a[216:219], v[88:91], v[108:111], a[216:219]
v_add_u32 v195, s45, v195
v_mfma_f32_16x16x32_bf16 a[224:227], v[88:91], v[112:115], a[224:227]
v_add_u32 v196, s45, v196
v_mfma_f32_16x16x32_bf16 a[232:235], v[88:91], v[116:119], a[232:235]
v_add_u32 v197, s45, v197
v_mfma_f32_16x16x32_bf16 a[240:243], v[88:91], v[120:123], a[240:243]
v_add_u32 v198, s45, v198
v_mfma_f32_16x16x32_bf16 a[248:251], v[88:91], v[124:127], a[248:251]
v_add_u32 v199, s45, v199
v_mfma_f32_16x16x32_bf16 a[196:199], v[92:95], v[96:99], a[196:199]
v_add_u32 v200, s45, v200
v_mfma_f32_16x16x32_bf16 a[204:207], v[92:95], v[100:103], a[204:207]
v_add_u32 v201, s45, v201
v_mfma_f32_16x16x32_bf16 a[212:215], v[92:95], v[104:107], a[212:215]
v_add_u32 v192, s34, v192
v_mfma_f32_16x16x32_bf16 a[220:223], v[92:95], v[108:111], a[220:223]
v_add_u32 v193, s34, v193
v_mfma_f32_16x16x32_bf16 a[228:231], v[92:95], v[112:115], a[228:231]
v_mfma_f32_16x16x32_bf16 a[236:239], v[92:95], v[116:119], a[236:239]
v_mfma_f32_16x16x32_bf16 a[244:247], v[92:95], v[120:123], a[244:247]
v_mfma_f32_16x16x32_bf16 a[252:255], v[92:95], v[124:127], a[252:255]
s_waitcnt vmcnt(10) lgkmcnt(0)
s_barrier
v_mfma_f32_16x16x32_bf16 a[0:3], v[128:131], v[160:163], a[0:3]
s_mov_b32 m0, s35
ds_read_b64_tr_b16 v[96:97], v194 offset:0
v_mfma_f32_16x16x32_bf16 a[8:11], v[128:131], v[164:167], a[8:11]
buffer_load_dwordx4 v202, s[4:7], s40 offen offset:0 lds
v_mfma_f32_16x16x32_bf16 a[16:19], v[128:131], v[168:171], a[16:19]
ds_read_b64_tr_b16 v[98:99], v194 offset:1024
v_mfma_f32_16x16x32_bf16 a[24:27], v[128:131], v[172:175], a[24:27]
v_mfma_f32_16x16x32_bf16 a[32:35], v[128:131], v[176:179], a[32:35]
ds_read_b128 v[64:67], v192 offset:0
v_mfma_f32_16x16x32_bf16 a[40:43], v[128:131], v[180:183], a[40:43]
buffer_load_dwordx4 v203, s[4:7], s40 offen offset:1024 lds
v_mfma_f32_16x16x32_bf16 a[48:51], v[128:131], v[184:187], a[48:51]
ds_read_b64_tr_b16 v[100:101], v195 offset:0
v_mfma_f32_16x16x32_bf16 a[56:59], v[128:131], v[188:191], a[56:59]
v_mfma_f32_16x16x32_bf16 a[4:7], v[132:135], v[160:163], a[4:7]
ds_read_b64_tr_b16 v[102:103], v195 offset:1024
v_mfma_f32_16x16x32_bf16 a[12:15], v[132:135], v[164:167], a[12:15]
buffer_load_dwordx4 v204, s[4:7], s40 offen offset:2048 lds
v_mfma_f32_16x16x32_bf16 a[20:23], v[132:135], v[168:171], a[20:23]
ds_read_b128 v[68:71], v192 offset:2048
v_mfma_f32_16x16x32_bf16 a[28:31], v[132:135], v[172:175], a[28:31]
v_mfma_f32_16x16x32_bf16 a[36:39], v[132:135], v[176:179], a[36:39]
ds_read_b64_tr_b16 v[104:105], v196 offset:0
v_mfma_f32_16x16x32_bf16 a[44:47], v[132:135], v[180:183], a[44:47]
buffer_load_dwordx4 v205, s[4:7], s40 offen offset:3072 lds
v_mfma_f32_16x16x32_bf16 a[52:55], v[132:135], v[184:187], a[52:55]
ds_read_b64_tr_b16 v[106:107], v196 offset:1024
v_mfma_f32_16x16x32_bf16 a[60:63], v[132:135], v[188:191], a[60:63]
v_mfma_f32_16x16x32_bf16 a[64:67], v[136:139], v[160:163], a[64:67]
s_add_u32 m0, s35, 4096
ds_read_b128 v[72:75], v192 offset:4096
v_mfma_f32_16x16x32_bf16 a[72:75], v[136:139], v[164:167], a[72:75]
buffer_load_dwordx4 v202, s[4:7], s41 offen offset:0 lds
v_mfma_f32_16x16x32_bf16 a[80:83], v[136:139], v[168:171], a[80:83]
ds_read_b64_tr_b16 v[108:109], v197 offset:0
v_mfma_f32_16x16x32_bf16 a[88:91], v[136:139], v[172:175], a[88:91]
v_mfma_f32_16x16x32_bf16 a[96:99], v[136:139], v[176:179], a[96:99]
ds_read_b64_tr_b16 v[110:111], v197 offset:1024
v_mfma_f32_16x16x32_bf16 a[104:107], v[136:139], v[180:183], a[104:107]
buffer_load_dwordx4 v203, s[4:7], s41 offen offset:1024 lds
v_mfma_f32_16x16x32_bf16 a[112:115], v[136:139], v[184:187], a[112:115]
ds_read_b128 v[76:79], v192 offset:6144
v_mfma_f32_16x16x32_bf16 a[120:123], v[136:139], v[188:191], a[120:123]
v_mfma_f32_16x16x32_bf16 a[68:71], v[140:143], v[160:163], a[68:71]
ds_read_b64_tr_b16 v[112:113], v198 offset:0
v_mfma_f32_16x16x32_bf16 a[76:79], v[140:143], v[164:167], a[76:79]
buffer_load_dwordx4 v204, s[4:7], s41 offen offset:2048 lds
v_mfma_f32_16x16x32_bf16 a[84:87], v[140:143], v[168:171], a[84:87]
ds_read_b64_tr_b16 v[114:115], v198 offset:1024
v_mfma_f32_16x16x32_bf16 a[92:95], v[140:143], v[172:175], a[92:95]
.Lpad3:
s_cbranch_execz .Ltch0
v_mfma_f32_16x16x32_bf16 a[100:103], v[140:143], v[176:179], a[100:103]
ds_read_b128 v[80:83], v192 offset:8192
v_mfma_f32_16x16x32_bf16 a[108:111], v[140:143], v[180:183], a[108:111]
buffer_load_dwordx4 v205, s[4:7], s41 offen offset:3072 lds
v_mfma_f32_16x16x32_bf16 a[116:119], v[140:143], v[184:187], a[116:119]
ds_read_b64_tr_b16 v[116:117], v199 offset:0
v_mfma_f32_16x16x32_bf16 a[124:127], v[140:143], v[188:191], a[124:127]
v_mfma_f32_16x16x32_bf16 a[128:131], v[144:147], v[160:163], a[128:131]
ds_read_b64_tr_b16 v[118:119], v199 offset:1024
v_mfma_f32_16x16x32_bf16 a[136:139], v[144:147], v[164:167], a[136:139]
buffer_load_dword v35, v206, s[56:59], s48 offen
v_mfma_f32_16x16x32_bf16 a[144:147], v[144:147], v[168:171], a[144:147]
ds_read_b128 v[84:87], v192 offset:10240
v_mfma_f32_16x16x32_bf16 a[152:155], v[144:147], v[172:175], a[152:155]
v_mfma_f32_16x16x32_bf16 a[160:163], v[144:147], v[176:179], a[160:163]
ds_read_b64_tr_b16 v[120:121], v200 offset:0
v_mfma_f32_16x16x32_bf16 a[168:171], v[144:147], v[180:183], a[168:171]
buffer_load_dword v35, v207, s[12:15], s49 offen
v_mfma_f32_16x16x32_bf16 a[176:179], v[144:147], v[184:187], a[176:179]
ds_read_b64_tr_b16 v[122:123], v200 offset:1024
v_mfma_f32_16x16x32_bf16 a[184:187], v[144:147], v[188:191], a[184:187]
v_mfma_f32_16x16x32_bf16 a[132:135], v[148:151], v[160:163], a[132:135]
s_add_u32 s40, s40, 128
ds_read_b128 v[88:91], v192 offset:12288
v_mfma_f32_16x16x32_bf16 a[140:143], v[148:151], v[164:167], a[140:143]
s_add_u32 s41, s41, 128
v_mfma_f32_16x16x32_bf16 a[148:151], v[148:151], v[168:171], a[148:151]
ds_read_b64_tr_b16 v[124:125], v201 offset:0
v_mfma_f32_16x16x32_bf16 a[156:159], v[148:151], v[172:175], a[156:159]
s_add_u32 s48, s48, 0x100000
v_mfma_f32_16x16x32_bf16 a[164:167], v[148:151], v[176:179], a[164:167]
ds_read_b64_tr_b16 v[126:127], v201 offset:1024
v_mfma_f32_16x16x32_bf16 a[172:175], v[148:151], v[180:183], a[172:175]
s_add_u32 s49, s49, 256
v_mfma_f32_16x16x32_bf16 a[180:183], v[148:151], v[184:187], a[180:183]
ds_read_b128 v[92:95], v192 offset:14336
v_mfma_f32_16x16x32_bf16 a[188:191], v[148:151], v[188:191], a[188:191]
v_mfma_f32_16x16x32_bf16 a[192:195], v[152:155], v[160:163], a[192:195]
v_mfma_f32_16x16x32_bf16 a[200:203], v[152:155], v[164:167], a[200:203]
v_mfma_f32_16x16x32_bf16 a[208:211], v[152:155], v[168:171], a[208:211]
v_mfma_f32_16x16x32_bf16 a[216:219], v[152:155], v[172:175], a[216:219]
v_mfma_f32_16x16x32_bf16 a[224:227], v[152:155], v[176:179], a[224:227]
v_mfma_f32_16x16x32_bf16 a[232:235], v[152:155], v[180:183], a[232:235]
v_mfma_f32_16x16x32_bf16 a[240:243], v[152:155], v[184:187], a[240:243]
v_mfma_f32_16x16x32_bf16 a[248:251], v[152:155], v[188:191], a[248:251]
v_mfma_f32_16x16x32_bf16 a[196:199], v[156:159], v[160:163], a[196:199]
v_mfma_f32_16x16x32_bf16 a[204:207], v[156:159], v[164:167], a[204:207]
s_mov_b32 s37, s36
v_mfma_f32_16x16x32_bf16 a[212:215], v[156:159], v[168:171], a[212:215]
s_add_u32 s35, s35, s45
v_mfma_f32_16x16x32_bf16 a[220:223], v[156:159], v[172:175], a[220:223]
s_add_u32 s36, s36, s34
v_mfma_f32_16x16x32_bf16 a[228:231], v[156:159], v[176:179], a[228:231]
s_add_u32 s38, s38, 1
v_mfma_f32_16x16x32_bf16 a[236:239], v[156:159], v[180:183], a[236:239]
s_cmp_lt_u32 s38, 6
v_mfma_f32_16x16x32_bf16 a[244:247], v[156:159], v[184:187], a[244:247]
v_mfma_f32_16x16x32_bf16 a[252:255], v[156:159], v[188:191], a[252:255]
s_waitcnt lgkmcnt(0)
s_cbranch_scc1 .Lp1_vloop
v_mfma_f32_16x16x32_bf16 a[0:3], v[64:67], v[96:99], a[0:3]
s_add_u32 m0, s92, 32768
ds_read_b64_tr_b16 v[160:161], v194 offset:8192
v_mfma_f32_16x16x32_bf16 a[8:11], v[64:67], v[100:103], a[8:11]
buffer_load_dwordx4 v20, s[12:15], s24 offen offset:0 lds
v_mfma_f32_16x16x32_bf16 a[16:19], v[64:67], v[104:107], a[16:19]
ds_read_b64_tr_b16 v[162:163], v194 offset:9216
v_mfma_f32_16x16x32_bf16 a[24:27], v[64:67], v[108:111], a[24:27]
v_mfma_f32_16x16x32_bf16 a[32:35], v[64:67], v[112:115], a[32:35]
ds_read_b128 v[128:131], v193 offset:0
v_mfma_f32_16x16x32_bf16 a[40:43], v[64:67], v[116:119], a[40:43]
buffer_load_dwordx4 v21, s[12:15], s24 offen offset:1024 lds
v_mfma_f32_16x16x32_bf16 a[48:51], v[64:67], v[120:123], a[48:51]
ds_read_b64_tr_b16 v[164:165], v195 offset:8192
v_mfma_f32_16x16x32_bf16 a[56:59], v[64:67], v[124:127], a[56:59]
v_mfma_f32_16x16x32_bf16 a[4:7], v[68:71], v[96:99], a[4:7]
ds_read_b64_tr_b16 v[166:167], v195 offset:9216
v_mfma_f32_16x16x32_bf16 a[12:15], v[68:71], v[100:103], a[12:15]
buffer_load_dwordx4 v22, s[12:15], s24 offen offset:2048 lds
v_mfma_f32_16x16x32_bf16 a[20:23], v[68:71], v[104:107], a[20:23]
ds_read_b128 v[132:135], v193 offset:2048
v_mfma_f32_16x16x32_bf16 a[28:31], v[68:71], v[108:111], a[28:31]
v_mfma_f32_16x16x32_bf16 a[36:39], v[68:71], v[112:115], a[36:39]
ds_read_b64_tr_b16 v[168:169], v196 offset:8192
v_mfma_f32_16x16x32_bf16 a[44:47], v[68:71], v[116:119], a[44:47]
buffer_load_dwordx4 v23, s[12:15], s24 offen offset:3072 lds
v_mfma_f32_16x16x32_bf16 a[52:55], v[68:71], v[120:123], a[52:55]
ds_read_b64_tr_b16 v[170:171], v196 offset:9216
v_mfma_f32_16x16x32_bf16 a[60:63], v[68:71], v[124:127], a[60:63]
v_mfma_f32_16x16x32_bf16 a[64:67], v[72:75], v[96:99], a[64:67]
s_add_u32 m0, s92, 36864
ds_read_b128 v[136:139], v193 offset:4096
v_mfma_f32_16x16x32_bf16 a[72:75], v[72:75], v[100:103], a[72:75]
buffer_load_dwordx4 v20, s[12:15], s25 offen offset:0 lds
v_mfma_f32_16x16x32_bf16 a[80:83], v[72:75], v[104:107], a[80:83]
ds_read_b64_tr_b16 v[172:173], v197 offset:8192
v_mfma_f32_16x16x32_bf16 a[88:91], v[72:75], v[108:111], a[88:91]
v_mfma_f32_16x16x32_bf16 a[96:99], v[72:75], v[112:115], a[96:99]
ds_read_b64_tr_b16 v[174:175], v197 offset:9216
v_mfma_f32_16x16x32_bf16 a[104:107], v[72:75], v[116:119], a[104:107]
buffer_load_dwordx4 v21, s[12:15], s25 offen offset:1024 lds
v_mfma_f32_16x16x32_bf16 a[112:115], v[72:75], v[120:123], a[112:115]
ds_read_b128 v[140:143], v193 offset:6144
v_mfma_f32_16x16x32_bf16 a[120:123], v[72:75], v[124:127], a[120:123]
v_mfma_f32_16x16x32_bf16 a[68:71], v[76:79], v[96:99], a[68:71]
ds_read_b64_tr_b16 v[176:177], v198 offset:8192
v_mfma_f32_16x16x32_bf16 a[76:79], v[76:79], v[100:103], a[76:79]
buffer_load_dwordx4 v22, s[12:15], s25 offen offset:2048 lds
v_mfma_f32_16x16x32_bf16 a[84:87], v[76:79], v[104:107], a[84:87]
ds_read_b64_tr_b16 v[178:179], v198 offset:9216
v_mfma_f32_16x16x32_bf16 a[92:95], v[76:79], v[108:111], a[92:95]
v_mfma_f32_16x16x32_bf16 a[100:103], v[76:79], v[112:115], a[100:103]
ds_read_b128 v[144:147], v193 offset:8192
v_mfma_f32_16x16x32_bf16 a[108:111], v[76:79], v[116:119], a[108:111]
buffer_load_dwordx4 v23, s[12:15], s25 offen offset:3072 lds
v_mfma_f32_16x16x32_bf16 a[116:119], v[76:79], v[120:123], a[116:119]
ds_read_b64_tr_b16 v[180:181], v199 offset:8192
v_mfma_f32_16x16x32_bf16 a[124:127], v[76:79], v[124:127], a[124:127]
v_mfma_f32_16x16x32_bf16 a[128:131], v[80:83], v[96:99], a[128:131]
ds_read_b64_tr_b16 v[182:183], v199 offset:9216
v_mfma_f32_16x16x32_bf16 a[136:139], v[80:83], v[100:103], a[136:139]
v_mfma_f32_16x16x32_bf16 a[144:147], v[80:83], v[104:107], a[144:147]
ds_read_b128 v[148:151], v193 offset:10240
v_mfma_f32_16x16x32_bf16 a[152:155], v[80:83], v[108:111], a[152:155]
v_mfma_f32_16x16x32_bf16 a[160:163], v[80:83], v[112:115], a[160:163]
ds_read_b64_tr_b16 v[184:185], v200 offset:8192
v_mfma_f32_16x16x32_bf16 a[168:171], v[80:83], v[116:119], a[168:171]
v_mfma_f32_16x16x32_bf16 a[176:179], v[80:83], v[120:123], a[176:179]
ds_read_b64_tr_b16 v[186:187], v200 offset:9216
v_mfma_f32_16x16x32_bf16 a[184:187], v[80:83], v[124:127], a[184:187]
v_mfma_f32_16x16x32_bf16 a[132:135], v[84:87], v[96:99], a[132:135]
s_add_u32 s24, s24, 128
s_add_u32 s25, s25, 128
ds_read_b128 v[152:155], v193 offset:12288
v_mfma_f32_16x16x32_bf16 a[140:143], v[84:87], v[100:103], a[140:143]
v_mfma_f32_16x16x32_bf16 a[148:151], v[84:87], v[104:107], a[148:151]
ds_read_b64_tr_b16 v[188:189], v201 offset:8192
v_mfma_f32_16x16x32_bf16 a[156:159], v[84:87], v[108:111], a[156:159]
v_mfma_f32_16x16x32_bf16 a[164:167], v[84:87], v[112:115], a[164:167]
s_add_u32 s44, s35, 65536
ds_read_b64_tr_b16 v[190:191], v201 offset:9216
v_mfma_f32_16x16x32_bf16 a[172:175], v[84:87], v[116:119], a[172:175]
s_cmp_ge_u32 s44, s39
s_cselect_b32 s45, s55, s54
v_mfma_f32_16x16x32_bf16 a[180:183], v[84:87], v[120:123], a[180:183]
ds_read_b128 v[156:159], v193 offset:14336
v_mfma_f32_16x16x32_bf16 a[188:191], v[84:87], v[124:127], a[188:191]
s_add_u32 s44, s36, 65536
v_mfma_f32_16x16x32_bf16 a[192:195], v[88:91], v[96:99], a[192:195]
v_mfma_f32_16x16x32_bf16 a[200:203], v[88:91], v[100:103], a[200:203]
s_cmp_ge_u32 s44, s39
s_cselect_b32 s34, s55, s54
v_mfma_f32_16x16x32_bf16 a[208:211], v[88:91], v[104:107], a[208:211]
v_add_u32 v194, s45, v194
v_mfma_f32_16x16x32_bf16 a[216:219], v[88:91], v[108:111], a[216:219]
v_add_u32 v195, s45, v195
v_mfma_f32_16x16x32_bf16 a[224:227], v[88:91], v[112:115], a[224:227]
v_add_u32 v196, s45, v196
v_mfma_f32_16x16x32_bf16 a[232:235], v[88:91], v[116:119], a[232:235]
v_add_u32 v197, s45, v197
v_mfma_f32_16x16x32_bf16 a[240:243], v[88:91], v[120:123], a[240:243]
v_add_u32 v198, s45, v198
v_mfma_f32_16x16x32_bf16 a[248:251], v[88:91], v[124:127], a[248:251]
v_add_u32 v199, s45, v199
v_mfma_f32_16x16x32_bf16 a[196:199], v[92:95], v[96:99], a[196:199]
v_add_u32 v200, s45, v200
v_mfma_f32_16x16x32_bf16 a[204:207], v[92:95], v[100:103], a[204:207]
v_add_u32 v201, s45, v201
v_mfma_f32_16x16x32_bf16 a[212:215], v[92:95], v[104:107], a[212:215]
v_add_u32 v192, s34, v192
v_mfma_f32_16x16x32_bf16 a[220:223], v[92:95], v[108:111], a[220:223]
v_add_u32 v193, s34, v193
v_mfma_f32_16x16x32_bf16 a[228:231], v[92:95], v[112:115], a[228:231]
v_mfma_f32_16x16x32_bf16 a[236:239], v[92:95], v[116:119], a[236:239]
v_mfma_f32_16x16x32_bf16 a[244:247], v[92:95], v[120:123], a[244:247]
v_mfma_f32_16x16x32_bf16 a[252:255], v[92:95], v[124:127], a[252:255]
s_waitcnt vmcnt(10) lgkmcnt(0)
s_barrier
v_mfma_f32_16x16x32_bf16 a[0:3], v[128:131], v[160:163], a[0:3]
s_add_u32 m0, s91, 65536
ds_read_b64_tr_b16 v[96:97], v194 offset:0
v_mfma_f32_16x16x32_bf16 a[8:11], v[128:131], v[164:167], a[8:11]
buffer_load_dwordx4 v16, s[16:19], s26 offen offset:0 lds
v_mfma_f32_16x16x32_bf16 a[16:19], v[128:131], v[168:171], a[16:19]
ds_read_b64_tr_b16 v[98:99], v194 offset:1024
v_mfma_f32_16x16x32_bf16 a[24:27], v[128:131], v[172:175], a[24:27]
v_mfma_f32_16x16x32_bf16 a[32:35], v[128:131], v[176:179], a[32:35]
ds_read_b128 v[64:67], v192 offset:0
v_mfma_f32_16x16x32_bf16 a[40:43], v[128:131], v[180:183], a[40:43]
buffer_load_dwordx4 v17, s[16:19], s26 offen offset:1024 lds
v_mfma_f32_16x16x32_bf16 a[48:51], v[128:131], v[184:187], a[48:51]
ds_read_b64_tr_b16 v[100:101], v195 offset:0
v_mfma_f32_16x16x32_bf16 a[56:59], v[128:131], v[188:191], a[56:59]
v_mfma_f32_16x16x32_bf16 a[4:7], v[132:135], v[160:163], a[4:7]
ds_read_b64_tr_b16 v[102:103], v195 offset:1024
v_mfma_f32_16x16x32_bf16 a[12:15], v[132:135], v[164:167], a[12:15]
buffer_load_dwordx4 v18, s[16:19], s26 offen offset:2048 lds
v_mfma_f32_16x16x32_bf16 a[20:23], v[132:135], v[168:171], a[20:23]
ds_read_b128 v[68:71], v192 offset:2048
v_mfma_f32_16x16x32_bf16 a[28:31], v[132:135], v[172:175], a[28:31]
v_mfma_f32_16x16x32_bf16 a[36:39], v[132:135], v[176:179], a[36:39]
ds_read_b64_tr_b16 v[104:105], v196 offset:0
v_mfma_f32_16x16x32_bf16 a[44:47], v[132:135], v[180:183], a[44:47]
buffer_load_dwordx4 v19, s[16:19], s26 offen offset:3072 lds
v_mfma_f32_16x16x32_bf16 a[52:55], v[132:135], v[184:187], a[52:55]
ds_read_b64_tr_b16 v[106:107], v196 offset:1024
v_mfma_f32_16x16x32_bf16 a[60:63], v[132:135], v[188:191], a[60:63]
s_add_u32 s26, s26, 524288
v_mfma_f32_16x16x32_bf16 a[64:67], v[136:139], v[160:163], a[64:67]
s_add_u32 m0, s91, 81920
ds_read_b128 v[72:75], v192 offset:4096
v_mfma_f32_16x16x32_bf16 a[72:75], v[136:139], v[164:167], a[72:75]
buffer_load_dwordx4 v16, s[16:19], s26 offen offset:0 lds
v_mfma_f32_16x16x32_bf16 a[80:83], v[136:139], v[168:171], a[80:83]
ds_read_b64_tr_b16 v[108:109], v197 offset:0
v_mfma_f32_16x16x32_bf16 a[88:91], v[136:139], v[172:175], a[88:91]
v_mfma_f32_16x16x32_bf16 a[96:99], v[136:139], v[176:179], a[96:99]
ds_read_b64_tr_b16 v[110:111], v197 offset:1024
v_mfma_f32_16x16x32_bf16 a[104:107], v[136:139], v[180:183], a[104:107]
buffer_load_dwordx4 v17, s[16:19], s26 offen offset:1024 lds
v_mfma_f32_16x16x32_bf16 a[112:115], v[136:139], v[184:187], a[112:115]
ds_read_b128 v[76:79], v192 offset:6144
v_mfma_f32_16x16x32_bf16 a[120:123], v[136:139], v[188:191], a[120:123]
v_mfma_f32_16x16x32_bf16 a[68:71], v[140:143], v[160:163], a[68:71]
ds_read_b64_tr_b16 v[112:113], v198 offset:0
v_mfma_f32_16x16x32_bf16 a[76:79], v[140:143], v[164:167], a[76:79]
buffer_load_dwordx4 v18, s[16:19], s26 offen offset:2048 lds
v_mfma_f32_16x16x32_bf16 a[84:87], v[140:143], v[168:171], a[84:87]
ds_read_b64_tr_b16 v[114:115], v198 offset:1024
v_mfma_f32_16x16x32_bf16 a[92:95], v[140:143], v[172:175], a[92:95]
v_mfma_f32_16x16x32_bf16 a[100:103], v[140:143], v[176:179], a[100:103]
ds_read_b128 v[80:83], v192 offset:8192
v_mfma_f32_16x16x32_bf16 a[108:111], v[140:143], v[180:183], a[108:111]
buffer_load_dwordx4 v19, s[16:19], s26 offen offset:3072 lds
v_mfma_f32_16x16x32_bf16 a[116:119], v[140:143], v[184:187], a[116:119]
ds_read_b64_tr_b16 v[116:117], v199 offset:0
v_mfma_f32_16x16x32_bf16 a[124:127], v[140:143], v[188:191], a[124:127]
s_add_u32 s26, s26, 524288
v_mfma_f32_16x16x32_bf16 a[128:131], v[144:147], v[160:163], a[128:131]
ds_read_b64_tr_b16 v[118:119], v199 offset:1024
v_mfma_f32_16x16x32_bf16 a[136:139], v[144:147], v[164:167], a[136:139]
buffer_load_dword v35, v206, s[56:59], s48 offen
v_mfma_f32_16x16x32_bf16 a[144:147], v[144:147], v[168:171], a[144:147]
ds_read_b128 v[84:87], v192 offset:10240
v_mfma_f32_16x16x32_bf16 a[152:155], v[144:147], v[172:175], a[152:155]
v_mfma_f32_16x16x32_bf16 a[160:163], v[144:147], v[176:179], a[160:163]
ds_read_b64_tr_b16 v[120:121], v200 offset:0
v_mfma_f32_16x16x32_bf16 a[168:171], v[144:147], v[180:183], a[168:171]
buffer_load_dword v35, v207, s[12:15], s49 offen
v_add_u32 v11, 8, v11
v_and_b32 v11, -8, v11
v_mfma_f32_16x16x32_bf16 a[176:179], v[144:147], v[184:187], a[176:179]
ds_read_b64_tr_b16 v[122:123], v200 offset:1024
v_mfma_f32_16x16x32_bf16 a[184:187], v[144:147], v[188:191], a[184:187]
v_mfma_f32_16x16x32_bf16 a[132:135], v[148:151], v[160:163], a[132:135]
ds_read_b128 v[88:91], v192 offset:12288
v_mfma_f32_16x16x32_bf16 a[140:143], v[148:151], v[164:167], a[140:143]
s_nop 0
v_or_b32 v0, 0, v11
v_mfma_f32_16x16x32_bf16 a[148:151], v[148:151], v[168:171], a[148:151]
ds_read_b64_tr_b16 v[124:125], v201 offset:0
v_mfma_f32_16x16x32_bf16 a[156:159], v[148:151], v[172:175], a[156:159]
s_add_u32 s48, s48, 0x100000
v_mfma_f32_16x16x32_bf16 a[164:167], v[148:151], v[176:179], a[164:167]
ds_read_b64_tr_b16 v[126:127], v201 offset:1024
v_mfma_f32_16x16x32_bf16 a[172:175], v[148:151], v[180:183], a[172:175]
s_add_u32 s49, s49, 256
v_mfma_f32_16x16x32_bf16 a[180:183], v[148:151], v[184:187], a[180:183]
ds_read_b128 v[92:95], v192 offset:14336
v_mfma_f32_16x16x32_bf16 a[188:191], v[148:151], v[188:191], a[188:191]
buffer_store_dword v0, v6, s[60:63], 0 offen sc1
v_mfma_f32_16x16x32_bf16 a[192:195], v[152:155], v[160:163], a[192:195]
v_mfma_f32_16x16x32_bf16 a[200:203], v[152:155], v[164:167], a[200:203]
v_mfma_f32_16x16x32_bf16 a[208:211], v[152:155], v[168:171], a[208:211]
v_mfma_f32_16x16x32_bf16 a[216:219], v[152:155], v[172:175], a[216:219]
buffer_load_dword v35, v4, s[96:99], 0 offen
v_mfma_f32_16x16x32_bf16 a[224:227], v[152:155], v[176:179], a[224:227]
v_mfma_f32_16x16x32_bf16 a[232:235], v[152:155], v[180:183], a[232:235]
v_mfma_f32_16x16x32_bf16 a[240:243], v[152:155], v[184:187], a[240:243]
v_mfma_f32_16x16x32_bf16 a[248:251], v[152:155], v[188:191], a[248:251]
v_mfma_f32_16x16x32_bf16 a[196:199], v[156:159], v[160:163], a[196:199]
v_mfma_f32_16x16x32_bf16 a[204:207], v[156:159], v[164:167], a[204:207]
s_mov_b32 s37, s36
v_mfma_f32_16x16x32_bf16 a[212:215], v[156:159], v[168:171], a[212:215]
s_add_u32 s35, s35, s45
v_mfma_f32_16x16x32_bf16 a[220:223], v[156:159], v[172:175], a[220:223]
s_add_u32 s36, s36, s34
v_mfma_f32_16x16x32_bf16 a[228:231], v[156:159], v[176:179], a[228:231]
v_mfma_f32_16x16x32_bf16 a[236:239], v[156:159], v[180:183], a[236:239]
v_mfma_f32_16x16x32_bf16 a[244:247], v[156:159], v[184:187], a[244:247]
v_mfma_f32_16x16x32_bf16 a[252:255], v[156:159], v[188:191], a[252:255]
s_waitcnt lgkmcnt(0)
v_mfma_f32_16x16x32_bf16 a[0:3], v[64:67], v[96:99], a[0:3]
s_add_u32 m0, s92, 98304
ds_read_b64_tr_b16 v[160:161], v194 offset:8192
v_mfma_f32_16x16x32_bf16 a[8:11], v[64:67], v[100:103], a[8:11]
buffer_load_dwordx4 v20, s[12:15], s24 offen offset:0 lds
v_mfma_f32_16x16x32_bf16 a[16:19], v[64:67], v[104:107], a[16:19]
ds_read_b64_tr_b16 v[162:163], v194 offset:9216
v_mfma_f32_16x16x32_bf16 a[24:27], v[64:67], v[108:111], a[24:27]
v_mfma_f32_16x16x32_bf16 a[32:35], v[64:67], v[112:115], a[32:35]
ds_read_b128 v[128:131], v193 offset:0
v_mfma_f32_16x16x32_bf16 a[40:43], v[64:67], v[116:119], a[40:43]
buffer_load_dwordx4 v21, s[12:15], s24 offen offset:1024 lds
v_mfma_f32_16x16x32_bf16 a[48:51], v[64:67], v[120:123], a[48:51]
ds_read_b64_tr_b16 v[164:165], v195 offset:8192
v_mfma_f32_16x16x32_bf16 a[56:59], v[64:67], v[124:127], a[56:59]
v_mfma_f32_16x16x32_bf16 a[4:7], v[68:71], v[96:99], a[4:7]
ds_read_b64_tr_b16 v[166:167], v195 offset:9216
v_mfma_f32_16x16x32_bf16 a[12:15], v[68:71], v[100:103], a[12:15]
buffer_load_dwordx4 v22, s[12:15], s24 offen offset:2048 lds
v_mfma_f32_16x16x32_bf16 a[20:23], v[68:71], v[104:107], a[20:23]
ds_read_b128 v[132:135], v193 offset:2048
v_mfma_f32_16x16x32_bf16 a[28:31], v[68:71], v[108:111], a[28:31]
v_mfma_f32_16x16x32_bf16 a[36:39], v[68:71], v[112:115], a[36:39]
ds_read_b64_tr_b16 v[168:169], v196 offset:8192
v_mfma_f32_16x16x32_bf16 a[44:47], v[68:71], v[116:119], a[44:47]
buffer_load_dwordx4 v23, s[12:15], s24 offen offset:3072 lds
v_mfma_f32_16x16x32_bf16 a[52:55], v[68:71], v[120:123], a[52:55]
ds_read_b64_tr_b16 v[170:171], v196 offset:9216
v_mfma_f32_16x16x32_bf16 a[60:63], v[68:71], v[124:127], a[60:63]
v_mfma_f32_16x16x32_bf16 a[64:67], v[72:75], v[96:99], a[64:67]
s_add_u32 m0, s92, 102400
ds_read_b128 v[136:139], v193 offset:4096
v_mfma_f32_16x16x32_bf16 a[72:75], v[72:75], v[100:103], a[72:75]
buffer_load_dwordx4 v20, s[12:15], s25 offen offset:0 lds
v_mfma_f32_16x16x32_bf16 a[80:83], v[72:75], v[104:107], a[80:83]
ds_read_b64_tr_b16 v[172:173], v197 offset:8192
v_mfma_f32_16x16x32_bf16 a[88:91], v[72:75], v[108:111], a[88:91]
v_mfma_f32_16x16x32_bf16 a[96:99], v[72:75], v[112:115], a[96:99]
ds_read_b64_tr_b16 v[174:175], v197 offset:9216
v_mfma_f32_16x16x32_bf16 a[104:107], v[72:75], v[116:119], a[104:107]
buffer_load_dwordx4 v21, s[12:15], s25 offen offset:1024 lds
v_mfma_f32_16x16x32_bf16 a[112:115], v[72:75], v[120:123], a[112:115]
ds_read_b128 v[140:143], v193 offset:6144
v_mfma_f32_16x16x32_bf16 a[120:123], v[72:75], v[124:127], a[120:123]
v_mfma_f32_16x16x32_bf16 a[68:71], v[76:79], v[96:99], a[68:71]
ds_read_b64_tr_b16 v[176:177], v198 offset:8192
v_mfma_f32_16x16x32_bf16 a[76:79], v[76:79], v[100:103], a[76:79]
buffer_load_dwordx4 v22, s[12:15], s25 offen offset:2048 lds
v_mfma_f32_16x16x32_bf16 a[84:87], v[76:79], v[104:107], a[84:87]
ds_read_b64_tr_b16 v[178:179], v198 offset:9216
v_mfma_f32_16x16x32_bf16 a[92:95], v[76:79], v[108:111], a[92:95]
v_mfma_f32_16x16x32_bf16 a[100:103], v[76:79], v[112:115], a[100:103]
ds_read_b128 v[144:147], v193 offset:8192
v_mfma_f32_16x16x32_bf16 a[108:111], v[76:79], v[116:119], a[108:111]
buffer_load_dwordx4 v23, s[12:15], s25 offen offset:3072 lds
v_mfma_f32_16x16x32_bf16 a[116:119], v[76:79], v[120:123], a[116:119]
ds_read_b64_tr_b16 v[180:181], v199 offset:8192
v_mfma_f32_16x16x32_bf16 a[124:127], v[76:79], v[124:127], a[124:127]
v_mfma_f32_16x16x32_bf16 a[128:131], v[80:83], v[96:99], a[128:131]
ds_read_b64_tr_b16 v[182:183], v199 offset:9216
v_mfma_f32_16x16x32_bf16 a[136:139], v[80:83], v[100:103], a[136:139]
v_mfma_f32_16x16x32_bf16 a[144:147], v[80:83], v[104:107], a[144:147]
ds_read_b128 v[148:151], v193 offset:10240
v_mfma_f32_16x16x32_bf16 a[152:155], v[80:83], v[108:111], a[152:155]
v_mfma_f32_16x16x32_bf16 a[160:163], v[80:83], v[112:115], a[160:163]
ds_read_b64_tr_b16 v[184:185], v200 offset:8192
v_mfma_f32_16x16x32_bf16 a[168:171], v[80:83], v[116:119], a[168:171]
v_mfma_f32_16x16x32_bf16 a[176:179], v[80:83], v[120:123], a[176:179]
ds_read_b64_tr_b16 v[186:187], v200 offset:9216
v_mfma_f32_16x16x32_bf16 a[184:187], v[80:83], v[124:127], a[184:187]
v_mfma_f32_16x16x32_bf16 a[132:135], v[84:87], v[96:99], a[132:135]
s_add_u32 s24, s24, 128
s_add_u32 s25, s25, 128
ds_read_b128 v[152:155], v193 offset:12288
v_mfma_f32_16x16x32_bf16 a[140:143], v[84:87], v[100:103], a[140:143]
v_mfma_f32_16x16x32_bf16 a[148:151], v[84:87], v[104:107], a[148:151]
ds_read_b64_tr_b16 v[188:189], v201 offset:8192
v_mfma_f32_16x16x32_bf16 a[156:159], v[84:87], v[108:111], a[156:159]
v_mfma_f32_16x16x32_bf16 a[164:167], v[84:87], v[112:115], a[164:167]
ds_read_b64_tr_b16 v[190:191], v201 offset:9216
v_mfma_f32_16x16x32_bf16 a[172:175], v[84:87], v[116:119], a[172:175]
v_mfma_f32_16x16x32_bf16 a[180:183], v[84:87], v[120:123], a[180:183]
ds_read_b128 v[156:159], v193 offset:14336
v_mfma_f32_16x16x32_bf16 a[188:191], v[84:87], v[124:127], a[188:191]
v_mfma_f32_16x16x32_bf16 a[192:195], v[88:91], v[96:99], a[192:195]
v_mfma_f32_16x16x32_bf16 a[200:203], v[88:91], v[100:103], a[200:203]
v_mfma_f32_16x16x32_bf16 a[208:211], v[88:91], v[104:107], a[208:211]
v_mfma_f32_16x16x32_bf16 a[216:219], v[88:91], v[108:111], a[216:219]
v_mfma_f32_16x16x32_bf16 a[224:227], v[88:91], v[112:115], a[224:227]
v_mfma_f32_16x16x32_bf16 a[232:235], v[88:91], v[116:119], a[232:235]
v_mfma_f32_16x16x32_bf16 a[240:243], v[88:91], v[120:123], a[240:243]
v_mfma_f32_16x16x32_bf16 a[248:251], v[88:91], v[124:127], a[248:251]
v_mfma_f32_16x16x32_bf16 a[196:199], v[92:95], v[96:99], a[196:199]
v_mfma_f32_16x16x32_bf16 a[204:207], v[92:95], v[100:103], a[204:207]
v_mfma_f32_16x16x32_bf16 a[212:215], v[92:95], v[104:107], a[212:215]
v_mfma_f32_16x16x32_bf16 a[220:223], v[92:95], v[108:111], a[220:223]
v_mfma_f32_16x16x32_bf16 a[228:231], v[92:95], v[112:115], a[228:231]
v_mfma_f32_16x16x32_bf16 a[236:239], v[92:95], v[116:119], a[236:239]
v_mfma_f32_16x16x32_bf16 a[244:247], v[92:95], v[120:123], a[244:247]
v_mfma_f32_16x16x32_bf16 a[252:255], v[92:95], v[124:127], a[252:255]
s_waitcnt vmcnt(16) lgkmcnt(0)
s_barrier
v_mfma_f32_16x16x32_bf16 a[0:3], v[128:131], v[160:163], a[0:3]
s_add_u32 m0, s91, 0
ds_read_b64_tr_b16 v[64:65], v26 offset:0
v_mfma_f32_16x16x32_bf16 a[8:11], v[128:131], v[164:167], a[8:11]
buffer_load_dwordx4 v16, s[16:19], s26 offen offset:0 lds
v_mfma_f32_16x16x32_bf16 a[16:19], v[128:131], v[168:171], a[16:19]
ds_read_b64_tr_b16 v[66:67], v26 offset:1024
v_mfma_f32_16x16x32_bf16 a[24:27], v[128:131], v[172:175], a[24:27]
v_mfma_f32_16x16x32_bf16 a[32:35], v[128:131], v[176:179], a[32:35]
ds_read_b64_tr_b16 v[68:69], v27 offset:0
v_mfma_f32_16x16x32_bf16 a[40:43], v[128:131], v[180:183], a[40:43]
buffer_load_dwordx4 v17, s[16:19], s26 offen offset:1024 lds
v_mfma_f32_16x16x32_bf16 a[48:51], v[128:131], v[184:187], a[48:51]
ds_read_b64_tr_b16 v[70:71], v27 offset:1024
v_mfma_f32_16x16x32_bf16 a[56:59], v[128:131], v[188:191], a[56:59]
v_mfma_f32_16x16x32_bf16 a[4:7], v[132:135], v[160:163], a[4:7]
ds_read_b64_tr_b16 v[72:73], v28 offset:0
v_mfma_f32_16x16x32_bf16 a[12:15], v[132:135], v[164:167], a[12:15]
buffer_load_dwordx4 v18, s[16:19], s26 offen offset:2048 lds
v_mfma_f32_16x16x32_bf16 a[20:23], v[132:135], v[168:171], a[20:23]
ds_read_b64_tr_b16 v[74:75], v28 offset:1024
v_mfma_f32_16x16x32_bf16 a[28:31], v[132:135], v[172:175], a[28:31]
v_mfma_f32_16x16x32_bf16 a[36:39], v[132:135], v[176:179], a[36:39]
ds_read_b64_tr_b16 v[76:77], v29 offset:0
v_mfma_f32_16x16x32_bf16 a[44:47], v[132:135], v[180:183], a[44:47]
buffer_load_dwordx4 v19, s[16:19], s26 offen offset:3072 lds
v_mfma_f32_16x16x32_bf16 a[52:55], v[132:135], v[184:187], a[52:55]
ds_read_b64_tr_b16 v[78:79], v29 offset:1024
v_mfma_f32_16x16x32_bf16 a[60:63], v[132:135], v[188:191], a[60:63]
s_add_u32 s26, s26, 524288
v_mfma_f32_16x16x32_bf16 a[64:67], v[136:139], v[160:163], a[64:67]
s_add_u32 m0, s92, 131072
ds_read_b64_tr_b16 v[80:81], v30 offset:0
v_mfma_f32_16x16x32_bf16 a[72:75], v[136:139], v[164:167], a[72:75]
buffer_load_dwordx4 v20, s[12:15], s24 offen offset:0 lds
v_mfma_f32_16x16x32_bf16 a[80:83], v[136:139], v[168:171], a[80:83]
ds_read_b64_tr_b16 v[82:83], v30 offset:1024
v_mfma_f32_16x16x32_bf16 a[88:91], v[136:139], v[172:175], a[88:91]
v_mfma_f32_16x16x32_bf16 a[96:99], v[136:139], v[176:179], a[96:99]
ds_read_b64_tr_b16 v[84:85], v31 offset:0
v_mfma_f32_16x16x32_bf16 a[104:107], v[136:139], v[180:183], a[104:107]
buffer_load_dwordx4 v21, s[12:15], s24 offen offset:1024 lds
v_mfma_f32_16x16x32_bf16 a[112:115], v[136:139], v[184:187], a[112:115]
ds_read_b64_tr_b16 v[86:87], v31 offset:1024
v_mfma_f32_16x16x32_bf16 a[120:123], v[136:139], v[188:191], a[120:123]
v_mfma_f32_16x16x32_bf16 a[68:71], v[140:143], v[160:163], a[68:71]
ds_read_b64_tr_b16 v[88:89], v32 offset:0
v_mfma_f32_16x16x32_bf16 a[76:79], v[140:143], v[164:167], a[76:79]
buffer_load_dwordx4 v22, s[12:15], s24 offen offset:2048 lds
v_mfma_f32_16x16x32_bf16 a[84:87], v[140:143], v[168:171], a[84:87]
ds_read_b64_tr_b16 v[90:91], v32 offset:1024
v_mfma_f32_16x16x32_bf16 a[92:95], v[140:143], v[172:175], a[92:95]
v_mfma_f32_16x16x32_bf16 a[100:103], v[140:143], v[176:179], a[100:103]
ds_read_b64_tr_b16 v[92:93], v33 offset:0
v_mfma_f32_16x16x32_bf16 a[108:111], v[140:143], v[180:183], a[108:111]
buffer_load_dwordx4 v23, s[12:15], s24 offen offset:3072 lds
v_mfma_f32_16x16x32_bf16 a[116:119], v[140:143], v[184:187], a[116:119]
ds_read_b64_tr_b16 v[94:95], v33 offset:1024
v_mfma_f32_16x16x32_bf16 a[124:127], v[140:143], v[188:191], a[124:127]
v_mfma_f32_16x16x32_bf16 a[128:131], v[144:147], v[160:163], a[128:131]
s_add_u32 m0, s92, 135168
v_mfma_f32_16x16x32_bf16 a[136:139], v[144:147], v[164:167], a[136:139]
buffer_load_dwordx4 v20, s[12:15], s25 offen offset:0 lds
v_mfma_f32_16x16x32_bf16 a[144:147], v[144:147], v[168:171], a[144:147]
ds_read_b128 v[48:51], v24 offset:0
v_mfma_f32_16x16x32_bf16 a[152:155], v[144:147], v[172:175], a[152:155]
v_mfma_f32_16x16x32_bf16 a[160:163], v[144:147], v[176:179], a[160:163]
ds_read_b128 v[52:55], v24 offset:2048
v_mfma_f32_16x16x32_bf16 a[168:171], v[144:147], v[180:183], a[168:171]
buffer_load_dwordx4 v21, s[12:15], s25 offen offset:1024 lds
v_mfma_f32_16x16x32_bf16 a[176:179], v[144:147], v[184:187], a[176:179]
ds_read_b128 v[56:59], v24 offset:4096
v_mfma_f32_16x16x32_bf16 a[184:187], v[144:147], v[188:191], a[184:187]
v_mfma_f32_16x16x32_bf16 a[132:135], v[148:151], v[160:163], a[132:135]
v_mfma_f32_16x16x32_bf16 a[140:143], v[148:151], v[164:167], a[140:143]
buffer_load_dwordx4 v22, s[12:15], s25 offen offset:2048 lds
v_mfma_f32_16x16x32_bf16 a[148:151], v[148:151], v[168:171], a[148:151]
v_mfma_f32_16x16x32_bf16 a[156:159], v[148:151], v[172:175], a[156:159]
v_mfma_f32_16x16x32_bf16 a[164:167], v[148:151], v[176:179], a[164:167]
v_mfma_f32_16x16x32_bf16 a[172:175], v[148:151], v[180:183], a[172:175]
buffer_load_dwordx4 v23, s[12:15], s25 offen offset:3072 lds
v_mfma_f32_16x16x32_bf16 a[180:183], v[148:151], v[184:187], a[180:183]
v_mfma_f32_16x16x32_bf16 a[188:191], v[148:151], v[188:191], a[188:191]
v_mfma_f32_16x16x32_bf16 a[192:195], v[152:155], v[160:163], a[192:195]
v_mfma_f32_16x16x32_bf16 a[200:203], v[152:155], v[164:167], a[200:203]
s_add_u32 s24, s24, 128
s_add_u32 s25, s25, 128
v_mfma_f32_16x16x32_bf16 a[208:211], v[152:155], v[168:171], a[208:211]
v_mfma_f32_16x16x32_bf16 a[216:219], v[152:155], v[172:175], a[216:219]
v_mfma_f32_16x16x32_bf16 a[224:227], v[152:155], v[176:179], a[224:227]
v_mfma_f32_16x16x32_bf16 a[232:235], v[152:155], v[180:183], a[232:235]
v_mfma_f32_16x16x32_bf16 a[240:243], v[152:155], v[184:187], a[240:243]
v_mfma_f32_16x16x32_bf16 a[248:251], v[152:155], v[188:191], a[248:251]
v_mfma_f32_16x16x32_bf16 a[196:199], v[156:159], v[160:163], a[196:199]
v_mfma_f32_16x16x32_bf16 a[204:207], v[156:159], v[164:167], a[204:207]
v_mfma_f32_16x16x32_bf16 a[212:215], v[156:159], v[168:171], a[212:215]
v_mfma_f32_16x16x32_bf16 a[220:223], v[156:159], v[172:175], a[220:223]
v_mfma_f32_16x16x32_bf16 a[228:231], v[156:159], v[176:179], a[228:231]
v_mfma_f32_16x16x32_bf16 a[236:239], v[156:159], v[180:183], a[236:239]
v_mfma_f32_16x16x32_bf16 a[244:247], v[156:159], v[184:187], a[244:247]
v_mfma_f32_16x16x32_bf16 a[252:255], v[156:159], v[188:191], a[252:255]
s_waitcnt lgkmcnt(0)
.Lp2_pad:
s_cbranch_execz .Ltch1
.Lp2:
v_mfma_f32_16x16x32_bf16 v[128:131], v[64:67], v[48:51], 0
ds_read_b128 v[60:63], v24 offset:6144
v_mfma_f32_16x16x32_bf16 v[132:135], v[68:71], v[48:51], 0
v_add_u32 v24, 65536, v24
v_accvgpr_read_b32 v36, a0
v_mfma_f32_16x16x32_bf16 v[136:139], v[72:75], v[48:51], 0
ds_read_b64_tr_b16 v[96:97], v26 offset:8192
ds_read_b64_tr_b16 v[98:99], v26 offset:9216
v_mfma_f32_16x16x32_bf16 v[140:143], v[76:79], v[48:51], 0
v_accvgpr_read_b32 v37, a1
v_mul_f32 v36, s47, v36
v_mfma_f32_16x16x32_bf16 v[144:147], v[80:83], v[48:51], 0
ds_read_b64_tr_b16 v[100:101], v27 offset:8192
ds_read_b64_tr_b16 v[102:103], v27 offset:9216
v_mfma_f32_16x16x32_bf16 v[148:151], v[84:87], v[48:51], 0
v_mul_f32 v37, s47, v37
v_cvt_pk_bf16_f32 v40, v36, v37
v_mfma_f32_16x16x32_bf16 v[152:155], v[88:91], v[48:51], 0
ds_read_b64_tr_b16 v[104:105], v28 offset:8192
ds_read_b64_tr_b16 v[106:107], v28 offset:9216
v_mfma_f32_16x16x32_bf16 v[156:159], v[92:95], v[48:51], 0
v_accvgpr_read_b32 v38, a2
v_accvgpr_read_b32 v39, a3
v_mfma_f32_16x16x32_bf16 v[160:163], v[64:67], v[52:55], 0
ds_read_b128 v[48:51], v25 offset:0
v_mul_f32 v38, s47, v38
v_mfma_f32_16x16x32_bf16 v[164:167], v[68:71], v[52:55], 0
v_mul_f32 v39, s47, v39
v_cvt_pk_bf16_f32 v41, v38, v39
v_mfma_f32_16x16x32_bf16 v[168:171], v[72:75], v[52:55], 0
s_add_u32 m0, s91, 16384
ds_read_b64_tr_b16 v[108:109], v29 offset:8192
ds_read_b64_tr_b16 v[110:111], v29 offset:9216
v_mfma_f32_16x16x32_bf16 v[172:175], v[76:79], v[52:55], 0
buffer_load_dwordx4 v16, s[16:19], s26 offen offset:0 lds
v_accvgpr_read_b32 v36, a4
v_mfma_f32_16x16x32_bf16 v[176:179], v[80:83], v[52:55], 0
ds_read_b64_tr_b16 v[112:113], v30 offset:8192
ds_read_b64_tr_b16 v[114:115], v30 offset:9216
.Lpad4:
s_cbranch_execz .Ltch1
v_mfma_f32_16x16x32_bf16 v[180:183], v[84:87], v[52:55], 0
v_accvgpr_read_b32 v37, a5
v_mul_f32 v36, s47, v36
v_mfma_f32_16x16x32_bf16 v[184:187], v[88:91], v[52:55], 0
ds_read_b64_tr_b16 v[116:117], v31 offset:8192
ds_read_b64_tr_b16 v[118:119], v31 offset:9216
v_mfma_f32_16x16x32_bf16 v[188:191], v[92:95], v[52:55], 0
buffer_load_dwordx4 v17, s[16:19], s26 offen offset:1024 lds
v_mul_f32 v37, s47, v37
v_mfma_f32_16x16x32_bf16 v[192:195], v[64:67], v[56:59], 0
ds_read_b128 v[52:55], v25 offset:2048
v_cvt_pk_bf16_f32 v42, v36, v37
v_mfma_f32_16x16x32_bf16 v[196:199], v[68:71], v[56:59], 0
v_accvgpr_read_b32 v38, a6
v_accvgpr_read_b32 v39, a7
v_mfma_f32_16x16x32_bf16 v[200:203], v[72:75], v[56:59], 0
ds_read_b64_tr_b16 v[120:121], v32 offset:8192
ds_read_b64_tr_b16 v[122:123], v32 offset:9216
v_mfma_f32_16x16x32_bf16 v[204:207], v[76:79], v[56:59], 0
buffer_load_dwordx4 v18, s[16:19], s26 offen offset:2048 lds
v_mul_f32 v38, s47, v38
v_mfma_f32_16x16x32_bf16 v[208:211], v[80:83], v[56:59], 0
ds_read_b64_tr_b16 v[124:125], v33 offset:8192
ds_read_b64_tr_b16 v[126:127], v33 offset:9216
v_mfma_f32_16x16x32_bf16 v[212:215], v[84:87], v[56:59], 0
v_mul_f32 v39, s47, v39
v_cvt_pk_bf16_f32 v43, v38, v39
v_mfma_f32_16x16x32_bf16 v[216:219], v[88:91], v[56:59], 0
s_waitcnt lgkmcnt(15)
s_xor_b32 s48, s95, 0
v_accvgpr_read_b32 v36, a128
v_mfma_f32_16x16x32_bf16 v[220:223], v[92:95], v[56:59], 0
buffer_store_dwordx4 v[40:43], v7, s[20:23], s48 offen sc1
v_accvgpr_read_b32 v37, a129
v_mfma_f32_16x16x32_bf16 v[224:227], v[64:67], v[60:63], 0
ds_read_b128 v[56:59], v25 offset:4096
v_mul_f32 v36, s47, v36
v_mfma_f32_16x16x32_bf16 v[228:231], v[68:71], v[60:63], 0
buffer_load_dwordx4 v19, s[16:19], s26 offen offset:3072 lds
v_mul_f32 v37, s47, v37
v_mfma_f32_16x16x32_bf16 v[232:235], v[72:75], v[60:63], 0
v_cvt_pk_bf16_f32 v44, v36, v37
v_accvgpr_read_b32 v38, a130
v_mfma_f32_16x16x32_bf16 v[236:239], v[76:79], v[60:63], 0
s_add_u32 s26, s26, 524288
v_accvgpr_read_b32 v39, a131
v_mul_f32 v38, s47, v38
v_mfma_f32_16x16x32_bf16 v[240:243], v[80:83], v[60:63], 0
v_mul_f32 v39, s47, v39
v_cvt_pk_bf16_f32 v45, v38, v39
v_mfma_f32_16x16x32_bf16 v[244:247], v[84:87], v[60:63], 0
v_accvgpr_read_b32 v36, a132
v_accvgpr_read_b32 v37, a133
v_mfma_f32_16x16x32_bf16 v[248:251], v[88:91], v[60:63], 0
v_mul_f32 v36, s47, v36
v_mul_f32 v37, s47, v37
v_mfma_f32_16x16x32_bf16 v[252:255], v[92:95], v[60:63], 0
s_waitcnt vmcnt(17) lgkmcnt(0)
s_barrier
v_cvt_pk_bf16_f32 v46, v36, v37
v_mfma_f32_16x16x32_bf16 v[128:131], v[96:99], v[48:51], v[128:131]
ds_read_b128 v[60:63], v25 offset:6144
v_accvgpr_read_b32 v38, a134
v_mfma_f32_16x16x32_bf16 v[132:135], v[100:103], v[48:51], v[132:135]
v_add_u32 v25, 65536, v25
v_accvgpr_read_b32 v39, a135
v_mfma_f32_16x16x32_bf16 v[136:139], v[104:107], v[48:51], v[136:139]
s_add_u32 m0, s92, 32768
ds_read_b64_tr_b16 v[64:65], v26 offset:16384
ds_read_b64_tr_b16 v[66:67], v26 offset:17408
v_mfma_f32_16x16x32_bf16 v[140:143], v[108:111], v[48:51], v[140:143]
buffer_load_dwordx4 v20, s[12:15], s24 offen offset:0 lds
v_mul_f32 v38, s47, v38
v_mfma_f32_16x16x32_bf16 v[144:147], v[112:115], v[48:51], v[144:147]
ds_read_b64_tr_b16 v[68:69], v27 offset:16384
ds_read_b64_tr_b16 v[70:71], v27 offset:17408
v_mfma_f32_16x16x32_bf16 v[148:151], v[116:119], v[48:51], v[148:151]
v_mul_f32 v39, s47, v39
v_cvt_pk_bf16_f32 v47, v38, v39
v_mfma_f32_16x16x32_bf16 v[152:155], v[120:123], v[48:51], v[152:155]
ds_read_b64_tr_b16 v[72:73], v28 offset:16384
ds_read_b64_tr_b16 v[74:75], v28 offset:17408
v_mfma_f32_16x16x32_bf16 v[156:159], v[124:127], v[48:51], v[156:159]
buffer_load_dwordx4 v21, s[12:15], s24 offen offset:1024 lds
v_accvgpr_write_b32 a128, v44
v_mfma_f32_16x16x32_bf16 v[160:163], v[96:99], v[52:55], v[160:163]
ds_read_b128 v[48:51], v24 offset:0
v_accvgpr_write_b32 a129, v45
v_mfma_f32_16x16x32_bf16 v[164:167], v[100:103], v[52:55], v[164:167]
v_accvgpr_write_b32 a130, v46
v_accvgpr_write_b32 a131, v47
v_mfma_f32_16x16x32_bf16 v[168:171], v[104:107], v[52:55], v[168:171]
ds_read_b64_tr_b16 v[76:77], v29 offset:16384
ds_read_b64_tr_b16 v[78:79], v29 offset:17408
v_mfma_f32_16x16x32_bf16 v[172:175], v[108:111], v[52:55], v[172:175]
buffer_load_dwordx4 v22, s[12:15], s24 offen offset:2048 lds
v_accvgpr_read_b32 v36, a136
v_mfma_f32_16x16x32_bf16 v[176:179], v[112:115], v[52:55], v[176:179]
ds_read_b64_tr_b16 v[80:81], v30 offset:16384
ds_read_b64_tr_b16 v[82:83], v30 offset:17408
v_mfma_f32_16x16x32_bf16 v[180:183], v[116:119], v[52:55], v[180:183]
v_accvgpr_read_b32 v37, a137
v_mul_f32 v36, s47, v36
v_mfma_f32_16x16x32_bf16 v[184:187], v[120:123], v[52:55], v[184:187]
ds_read_b64_tr_b16 v[84:85], v31 offset:16384
ds_read_b64_tr_b16 v[86:87], v31 offset:17408
v_mfma_f32_16x16x32_bf16 v[188:191], v[124:127], v[52:55], v[188:191]
buffer_load_dwordx4 v23, s[12:15], s24 offen offset:3072 lds
v_mul_f32 v37, s47, v37
v_mfma_f32_16x16x32_bf16 v[192:195], v[96:99], v[56:59], v[192:195]
ds_read_b128 v[52:55], v24 offset:2048
v_cvt_pk_bf16_f32 v40, v36, v37
v_mfma_f32_16x16x32_bf16 v[196:199], v[100:103], v[56:59], v[196:199]
v_accvgpr_read_b32 v38, a138
v_accvgpr_read_b32 v39, a139
v_mfma_f32_16x16x32_bf16 v[200:203], v[104:107], v[56:59], v[200:203]
s_add_u32 m0, s92, 36864
ds_read_b64_tr_b16 v[88:89], v32 offset:16384
ds_read_b64_tr_b16 v[90:91], v32 offset:17408
v_mfma_f32_16x16x32_bf16 v[204:207], v[108:111], v[56:59], v[204:207]
buffer_load_dwordx4 v20, s[12:15], s25 offen offset:0 lds
v_mul_f32 v38, s47, v38
v_mfma_f32_16x16x32_bf16 v[208:211], v[112:115], v[56:59], v[208:211]
ds_read_b64_tr_b16 v[92:93], v33 offset:16384
ds_read_b64_tr_b16 v[94:95], v33 offset:17408
v_mfma_f32_16x16x32_bf16 v[212:215], v[116:119], v[56:59], v[212:215]
v_mul_f32 v39, s47, v39
v_cvt_pk_bf16_f32 v41, v38, v39
v_mfma_f32_16x16x32_bf16 v[216:219], v[120:123], v[56:59], v[216:219]
s_waitcnt lgkmcnt(15)
v_accvgpr_read_b32 v36, a140
v_accvgpr_read_b32 v37, a141
v_mfma_f32_16x16x32_bf16 v[220:223], v[124:127], v[56:59], v[220:223]
v_mul_f32 v36, s47, v36
v_mul_f32 v37, s47, v37
v_mfma_f32_16x16x32_bf16 v[224:227], v[96:99], v[60:63], v[224:227]
ds_read_b128 v[56:59], v24 offset:4096
v_cvt_pk_bf16_f32 v42, v36, v37
v_mfma_f32_16x16x32_bf16 v[228:231], v[100:103], v[60:63], v[228:231]
buffer_load_dwordx4 v21, s[12:15], s25 offen offset:1024 lds
v_accvgpr_read_b32 v38, a142
v_mfma_f32_16x16x32_bf16 v[232:235], v[104:107], v[60:63], v[232:235]
v_accvgpr_read_b32 v39, a143
v_mul_f32 v38, s47, v38
v_mfma_f32_16x16x32_bf16 v[236:239], v[108:111], v[60:63], v[236:239]
v_mul_f32 v39, s47, v39
v_cvt_pk_bf16_f32 v43, v38, v39
v_mfma_f32_16x16x32_bf16 v[240:243], v[112:115], v[60:63], v[240:243]
v_accvgpr_write_b32 a132, v40
v_accvgpr_write_b32 a133, v41
v_mfma_f32_16x16x32_bf16 v[244:247], v[116:119], v[60:63], v[244:247]
v_accvgpr_write_b32 a134, v42
v_accvgpr_write_b32 a135, v43
v_mfma_f32_16x16x32_bf16 v[248:251], v[120:123], v[60:63], v[248:251]
v_accvgpr_read_b32 v36, a8
v_accvgpr_read_b32 v37, a9
v_mfma_f32_16x16x32_bf16 v[252:255], v[124:127], v[60:63], v[252:255]
s_waitcnt lgkmcnt(0)
v_mul_f32 v36, s47, v36
v_mul_f32 v37, s47, v37
v_mfma_f32_16x16x32_bf16 v[128:131], v[64:67], v[48:51], v[128:131]
ds_read_b128 v[60:63], v24 offset:6144
v_cvt_pk_bf16_f32 v44, v36, v37
v_mfma_f32_16x16x32_bf16 v[132:135], v[68:71], v[48:51], v[132:135]
v_add_u32 v24, 32768, v24
v_accvgpr_read_b32 v38, a10
v_mfma_f32_16x16x32_bf16 v[136:139], v[72:75], v[48:51], v[136:139]
ds_read_b64_tr_b16 v[96:97], v26 offset:24576
ds_read_b64_tr_b16 v[98:99], v26 offset:25600
v_mfma_f32_16x16x32_bf16 v[140:143], v[76:79], v[48:51], v[140:143]
buffer_load_dwordx4 v22, s[12:15], s25 offen offset:2048 lds
v_accvgpr_read_b32 v39, a11
v_mfma_f32_16x16x32_bf16 v[144:147], v[80:83], v[48:51], v[144:147]
ds_read_b64_tr_b16 v[100:101], v27 offset:24576
ds_read_b64_tr_b16 v[102:103], v27 offset:25600
v_mfma_f32_16x16x32_bf16 v[148:151], v[84:87], v[48:51], v[148:151]
v_mul_f32 v38, s47, v38
v_mul_f32 v39, s47, v39
v_mfma_f32_16x16x32_bf16 v[152:155], v[88:91], v[48:51], v[152:155]
ds_read_b64_tr_b16 v[104:105], v28 offset:24576
ds_read_b64_tr_b16 v[106:107], v28 offset:25600
v_mfma_f32_16x16x32_bf16 v[156:159], v[92:95], v[48:51], v[156:159]
buffer_load_dwordx4 v23, s[12:15], s25 offen offset:3072 lds
v_cvt_pk_bf16_f32 v45, v38, v39
v_mfma_f32_16x16x32_bf16 v[160:163], v[64:67], v[52:55], v[160:163]
ds_read_b128 v[48:51], v25 offset:0
v_accvgpr_read_b32 v36, a12
v_mfma_f32_16x16x32_bf16 v[164:167], v[68:71], v[52:55], v[164:167]
s_add_u32 s24, s24, 128
s_add_u32 s25, s25, 128
v_accvgpr_read_b32 v37, a13
v_mfma_f32_16x16x32_bf16 v[168:171], v[72:75], v[52:55], v[168:171]
s_add_u32 m0, s91, 65536
ds_read_b64_tr_b16 v[108:109], v29 offset:24576
ds_read_b64_tr_b16 v[110:111], v29 offset:25600
v_mfma_f32_16x16x32_bf16 v[172:175], v[76:79], v[52:55], v[172:175]
buffer_load_dwordx4 v16, s[16:19], s26 offen offset:0 lds
v_mul_f32 v36, s47, v36
v_mfma_f32_16x16x32_bf16 v[176:179], v[80:83], v[52:55], v[176:179]
ds_read_b64_tr_b16 v[112:113], v30 offset:24576
ds_read_b64_tr_b16 v[114:115], v30 offset:25600
v_mfma_f32_16x16x32_bf16 v[180:183], v[84:87], v[52:55], v[180:183]
v_mul_f32 v37, s47, v37
v_cvt_pk_bf16_f32 v46, v36, v37
v_mfma_f32_16x16x32_bf16 v[184:187], v[88:91], v[52:55], v[184:187]
ds_read_b64_tr_b16 v[116:117], v31 offset:24576
ds_read_b64_tr_b16 v[118:119], v31 offset:25600
v_mfma_f32_16x16x32_bf16 v[188:191], v[92:95], v[52:55], v[188:191]
buffer_load_dwordx4 v17, s[16:19], s26 offen offset:1024 lds
v_accvgpr_read_b32 v38, a14
v_mfma_f32_16x16x32_bf16 v[192:195], v[64:67], v[56:59], v[192:195]
ds_read_b128 v[52:55], v25 offset:2048
v_accvgpr_read_b32 v39, a15
v_mfma_f32_16x16x32_bf16 v[196:199], v[68:71], v[56:59], v[196:199]
v_mul_f32 v38, s47, v38
v_mul_f32 v39, s47, v39
v_mfma_f32_16x16x32_bf16 v[200:203], v[72:75], v[56:59], v[200:203]
ds_read_b64_tr_b16 v[120:121], v32 offset:24576
ds_read_b64_tr_b16 v[122:123], v32 offset:25600
v_mfma_f32_16x16x32_bf16 v[204:207], v[76:79], v[56:59], v[204:207]
buffer_load_dwordx4 v18, s[16:19], s26 offen offset:2048 lds
v_cvt_pk_bf16_f32 v47, v38, v39
v_mfma_f32_16x16x32_bf16 v[208:211], v[80:83], v[56:59], v[208:211]
ds_read_b64_tr_b16 v[124:125], v33 offset:24576
ds_read_b64_tr_b16 v[126:127], v33 offset:25600
v_mfma_f32_16x16x32_bf16 v[212:215], v[84:87], v[56:59], v[212:215]
s_xor_b32 s48, s95, 1024
buffer_store_dwordx4 v[44:47], v7, s[20:23], s48 offen sc1
v_mfma_f32_16x16x32_bf16 v[216:219], v[88:91], v[56:59], v[216:219]
s_waitcnt lgkmcnt(15)
v_accvgpr_read_b32 v36, a144
v_accvgpr_read_b32 v37, a145
v_mfma_f32_16x16x32_bf16 v[220:223], v[92:95], v[56:59], v[220:223]
v_xor_b32 v26, 0x10000, v26
v_mul_f32 v36, s47, v36
v_mfma_f32_16x16x32_bf16 v[224:227], v[64:67], v[60:63], v[224:227]
ds_read_b128 v[56:59], v25 offset:4096
v_xor_b32 v27, 0x10000, v27
v_mfma_f32_16x16x32_bf16 v[228:231], v[68:71], v[60:63], v[228:231]
buffer_load_dwordx4 v19, s[16:19], s26 offen offset:3072 lds
v_xor_b32 v28, 0x10000, v28
v_mfma_f32_16x16x32_bf16 v[232:235], v[72:75], v[60:63], v[232:235]
v_xor_b32 v29, 0x10000, v29
v_mul_f32 v37, s47, v37
v_mfma_f32_16x16x32_bf16 v[236:239], v[76:79], v[60:63], v[236:239]
s_add_u32 s26, s26, 524288
v_xor_b32 v30, 0x10000, v30
v_cvt_pk_bf16_f32 v40, v36, v37
v_mfma_f32_16x16x32_bf16 v[240:243], v[80:83], v[60:63], v[240:243]
v_xor_b32 v31, 0x10000, v31
v_accvgpr_read_b32 v38, a146
v_mfma_f32_16x16x32_bf16 v[244:247], v[84:87], v[60:63], v[244:247]
v_xor_b32 v32, 0x10000, v32
v_accvgpr_read_b32 v39, a147
v_mfma_f32_16x16x32_bf16 v[248:251], v[88:91], v[60:63], v[248:251]
v_xor_b32 v33, 0x10000, v33
v_mul_f32 v38, s47, v38
v_mfma_f32_16x16x32_bf16 v[252:255], v[92:95], v[60:63], v[252:255]
s_waitcnt vmcnt(18) lgkmcnt(0)
s_barrier
v_mul_f32 v39, s47, v39
v_mfma_f32_16x16x32_bf16 v[128:131], v[96:99], v[48:51], v[128:131]
ds_read_b128 v[60:63], v25 offset:6144
v_cvt_pk_bf16_f32 v41, v38, v39
v_mfma_f32_16x16x32_bf16 v[132:135], v[100:103], v[48:51], v[132:135]
v_add_u32 v25, 32768, v25
v_accvgpr_read_b32 v36, a148
v_mfma_f32_16x16x32_bf16 v[136:139], v[104:107], v[48:51], v[136:139]
s_add_u32 m0, s92, 98304
ds_read_b64_tr_b16 v[64:65], v26 offset:0
ds_read_b64_tr_b16 v[66:67], v26 offset:1024
v_mfma_f32_16x16x32_bf16 v[140:143], v[108:111], v[48:51], v[140:143]
buffer_load_dwordx4 v20, s[12:15], s24 offen offset:0 lds
v_accvgpr_read_b32 v37, a149
v_mfma_f32_16x16x32_bf16 v[144:147], v[112:115], v[48:51], v[144:147]
ds_read_b64_tr_b16 v[68:69], v27 offset:0
ds_read_b64_tr_b16 v[70:71], v27 offset:1024
v_mfma_f32_16x16x32_bf16 v[148:151], v[116:119], v[48:51], v[148:151]
v_mul_f32 v36, s47, v36
v_mul_f32 v37, s47, v37
v_mfma_f32_16x16x32_bf16 v[152:155], v[120:123], v[48:51], v[152:155]
ds_read_b64_tr_b16 v[72:73], v28 offset:0
ds_read_b64_tr_b16 v[74:75], v28 offset:1024
v_mfma_f32_16x16x32_bf16 v[156:159], v[124:127], v[48:51], v[156:159]
buffer_load_dwordx4 v21, s[12:15], s24 offen offset:1024 lds
v_cvt_pk_bf16_f32 v42, v36, v37
v_mfma_f32_16x16x32_bf16 v[160:163], v[96:99], v[52:55], v[160:163]
ds_read_b128 v[48:51], v24 offset:0
v_accvgpr_read_b32 v38, a150
v_mfma_f32_16x16x32_bf16 v[164:167], v[100:103], v[52:55], v[164:167]
v_accvgpr_read_b32 v39, a151
v_mul_f32 v38, s47, v38
v_mfma_f32_16x16x32_bf16 v[168:171], v[104:107], v[52:55], v[168:171]
ds_read_b64_tr_b16 v[76:77], v29 offset:0
ds_read_b64_tr_b16 v[78:79], v29 offset:1024
v_mfma_f32_16x16x32_bf16 v[172:175], v[108:111], v[52:55], v[172:175]
buffer_load_dwordx4 v22, s[12:15], s24 offen offset:2048 lds
v_mul_f32 v39, s47, v39
v_mfma_f32_16x16x32_bf16 v[176:179], v[112:115], v[52:55], v[176:179]
ds_read_b64_tr_b16 v[80:81], v30 offset:0
ds_read_b64_tr_b16 v[82:83], v30 offset:1024
v_mfma_f32_16x16x32_bf16 v[180:183], v[116:119], v[52:55], v[180:183]
v_cvt_pk_bf16_f32 v43, v38, v39
v_accvgpr_write_b32 a136, v40
v_mfma_f32_16x16x32_bf16 v[184:187], v[120:123], v[52:55], v[184:187]
ds_read_b64_tr_b16 v[84:85], v31 offset:0
ds_read_b64_tr_b16 v[86:87], v31 offset:1024
v_mfma_f32_16x16x32_bf16 v[188:191], v[124:127], v[52:55], v[188:191]
buffer_load_dwordx4 v23, s[12:15], s24 offen offset:3072 lds
v_accvgpr_write_b32 a137, v41
v_mfma_f32_16x16x32_bf16 v[192:195], v[96:99], v[56:59], v[192:195]
ds_read_b128 v[52:55], v24 offset:2048
v_accvgpr_write_b32 a138, v42
v_mfma_f32_16x16x32_bf16 v[196:199], v[100:103], v[56:59], v[196:199]
v_accvgpr_write_b32 a139, v43
v_accvgpr_read_b32 v36, a16
v_mfma_f32_16x16x32_bf16 v[200:203], v[104:107], v[56:59], v[200:203]
s_add_u32 m0, s92, 102400
ds_read_b64_tr_b16 v[88:89], v32 offset:0
ds_read_b64_tr_b16 v[90:91], v32 offset:1024
v_mfma_f32_16x16x32_bf16 v[204:207], v[108:111], v[56:59], v[204:207]
buffer_load_dwordx4 v20, s[12:15], s25 offen offset:0 lds
v_accvgpr_read_b32 v37, a17
v_mfma_f32_16x16x32_bf16 v[208:211], v[112:115], v[56:59], v[208:211]
ds_read_b64_tr_b16 v[92:93], v33 offset:0
ds_read_b64_tr_b16 v[94:95], v33 offset:1024
v_mfma_f32_16x16x32_bf16 v[212:215], v[116:119], v[56:59], v[212:215]
v_mul_f32 v36, s47, v36
v_mul_f32 v37, s47, v37
v_mfma_f32_16x16x32_bf16 v[216:219], v[120:123], v[56:59], v[216:219]
s_waitcnt lgkmcnt(15)
v_cvt_pk_bf16_f32 v44, v36, v37
v_accvgpr_read_b32 v38, a18
v_mfma_f32_16x16x32_bf16 v[220:223], v[124:127], v[56:59], v[220:223]
v_accvgpr_read_b32 v39, a19
v_mul_f32 v38, s47, v38
v_mfma_f32_16x16x32_bf16 v[224:227], v[96:99], v[60:63], v[224:227]
ds_read_b128 v[56:59], v24 offset:4096
v_mul_f32 v39, s47, v39
v_mfma_f32_16x16x32_bf16 v[228:231], v[100:103], v[60:63], v[228:231]
buffer_load_dwordx4 v21, s[12:15], s25 offen offset:1024 lds
v_cvt_pk_bf16_f32 v45, v38, v39
v_mfma_f32_16x16x32_bf16 v[232:235], v[104:107], v[60:63], v[232:235]
v_accvgpr_read_b32 v36, a20
v_accvgpr_read_b32 v37, a21
v_mfma_f32_16x16x32_bf16 v[236:239], v[108:111], v[60:63], v[236:239]
v_mul_f32 v36, s47, v36
v_mul_f32 v37, s47, v37
v_mfma_f32_16x16x32_bf16 v[240:243], v[112:115], v[60:63], v[240:243]
v_cvt_pk_bf16_f32 v46, v36, v37
v_accvgpr_read_b32 v38, a22
v_mfma_f32_16x16x32_bf16 v[244:247], v[116:119], v[60:63], v[244:247]
v_accvgpr_read_b32 v39, a23
v_mul_f32 v38, s47, v38
v_mfma_f32_16x16x32_bf16 v[248:251], v[120:123], v[60:63], v[248:251]
v_mul_f32 v39, s47, v39
v_cvt_pk_bf16_f32 v47, v38, v39
v_mfma_f32_16x16x32_bf16 v[252:255], v[124:127], v[60:63], v[252:255]
s_waitcnt lgkmcnt(0)
s_xor_b32 s48, s95, 2048
v_accvgpr_read_b32 v36, a152
v_mfma_f32_16x16x32_bf16 v[128:131], v[64:67], v[48:51], v[128:131]
ds_read_b128 v[60:63], v24 offset:6144
v_accvgpr_read_b32 v37, a153
v_mfma_f32_16x16x32_bf16 v[132:135], v[68:71], v[48:51], v[132:135]
buffer_store_dwordx4 v[44:47], v7, s[20:23], s48 offen sc1
v_add_u32 v24, 4294868992, v24
v_mfma_f32_16x16x32_bf16 v[136:139], v[72:75], v[48:51], v[136:139]
ds_read_b64_tr_b16 v[96:97], v26 offset:8192
ds_read_b64_tr_b16 v[98:99], v26 offset:9216
v_mfma_f32_16x16x32_bf16 v[140:143], v[76:79], v[48:51], v[140:143]
buffer_load_dwordx4 v22, s[12:15], s25 offen offset:2048 lds
v_mul_f32 v36, s47, v36
v_mfma_f32_16x16x32_bf16 v[144:147], v[80:83], v[48:51], v[144:147]
ds_read_b64_tr_b16 v[100:101], v27 offset:8192
ds_read_b64_tr_b16 v[102:103], v27 offset:9216
v_mfma_f32_16x16x32_bf16 v[148:151], v[84:87], v[48:51], v[148:151]
v_mul_f32 v37, s47, v37
v_cvt_pk_bf16_f32 v40, v36, v37
v_mfma_f32_16x16x32_bf16 v[152:155], v[88:91], v[48:51], v[152:155]
ds_read_b64_tr_b16 v[104:105], v28 offset:8192
ds_read_b64_tr_b16 v[106:107], v28 offset:9216
v_mfma_f32_16x16x32_bf16 v[156:159], v[92:95], v[48:51], v[156:159]
buffer_load_dwordx4 v23, s[12:15], s25 offen offset:3072 lds
v_accvgpr_read_b32 v38, a154
v_mfma_f32_16x16x32_bf16 v[160:163], v[64:67], v[52:55], v[160:163]
ds_read_b128 v[48:51], v25 offset:0
v_accvgpr_read_b32 v39, a155
v_mfma_f32_16x16x32_bf16 v[164:167], v[68:71], v[52:55], v[164:167]
s_add_u32 s24, s24, 128
s_add_u32 s25, s25, 128
v_mul_f32 v38, s47, v38
v_mfma_f32_16x16x32_bf16 v[168:171], v[72:75], v[52:55], v[168:171]
s_add_u32 m0, s91, 81920
ds_read_b64_tr_b16 v[108:109], v29 offset:8192
ds_read_b64_tr_b16 v[110:111], v29 offset:9216
v_mfma_f32_16x16x32_bf16 v[172:175], v[76:79], v[52:55], v[172:175]
buffer_load_dwordx4 v16, s[16:19], s26 offen offset:0 lds
v_mul_f32 v39, s47, v39
v_mfma_f32_16x16x32_bf16 v[176:179], v[80:83], v[52:55], v[176:179]
ds_read_b64_tr_b16 v[112:113], v30 offset:8192
ds_read_b64_tr_b16 v[114:115], v30 offset:9216
v_mfma_f32_16x16x32_bf16 v[180:183], v[84:87], v[52:55], v[180:183]
v_cvt_pk_bf16_f32 v41, v38, v39
v_accvgpr_read_b32 v36, a156
v_mfma_f32_16x16x32_bf16 v[184:187], v[88:91], v[52:55], v[184:187]
ds_read_b64_tr_b16 v[116:117], v31 offset:8192
ds_read_b64_tr_b16 v[118:119], v31 offset:9216
v_mfma_f32_16x16x32_bf16 v[188:191], v[92:95], v[52:55], v[188:191]
buffer_load_dwordx4 v17, s[16:19], s26 offen offset:1024 lds
v_accvgpr_read_b32 v37, a157
v_mfma_f32_16x16x32_bf16 v[192:195], v[64:67], v[56:59], v[192:195]
ds_read_b128 v[52:55], v25 offset:2048
v_mul_f32 v36, s47, v36
v_mfma_f32_16x16x32_bf16 v[196:199], v[68:71], v[56:59], v[196:199]
v_mul_f32 v37, s47, v37
v_cvt_pk_bf16_f32 v42, v36, v37
v_mfma_f32_16x16x32_bf16 v[200:203], v[72:75], v[56:59], v[200:203]
ds_read_b64_tr_b16 v[120:121], v32 offset:8192
ds_read_b64_tr_b16 v[122:123], v32 offset:9216
v_mfma_f32_16x16x32_bf16 v[204:207], v[76:79], v[56:59], v[204:207]
buffer_load_dwordx4 v18, s[16:19], s26 offen offset:2048 lds
v_accvgpr_read_b32 v38, a158
v_mfma_f32_16x16x32_bf16 v[208:211], v[80:83], v[56:59], v[208:211]
ds_read_b64_tr_b16 v[124:125], v33 offset:8192
ds_read_b64_tr_b16 v[126:127], v33 offset:9216
v_mfma_f32_16x16x32_bf16 v[212:215], v[84:87], v[56:59], v[212:215]
v_accvgpr_read_b32 v39, a159
v_mul_f32 v38, s47, v38
v_mfma_f32_16x16x32_bf16 v[216:219], v[88:91], v[56:59], v[216:219]
s_waitcnt lgkmcnt(15)
v_mul_f32 v39, s47, v39
v_cvt_pk_bf16_f32 v43, v38, v39
v_mfma_f32_16x16x32_bf16 v[220:223], v[92:95], v[56:59], v[220:223]
v_accvgpr_write_b32 a140, v40
v_accvgpr_write_b32 a141, v41
v_mfma_f32_16x16x32_bf16 v[224:227], v[64:67], v[60:63], v[224:227]
ds_read_b128 v[56:59], v25 offset:4096
v_accvgpr_write_b32 a142, v42
v_mfma_f32_16x16x32_bf16 v[228:231], v[68:71], v[60:63], v[228:231]
buffer_load_dwordx4 v19, s[16:19], s26 offen offset:3072 lds
v_accvgpr_write_b32 a143, v43
v_mfma_f32_16x16x32_bf16 v[232:235], v[72:75], v[60:63], v[232:235]
v_accvgpr_read_b32 v36, a24
v_accvgpr_read_b32 v37, a25
v_mfma_f32_16x16x32_bf16 v[236:239], v[76:79], v[60:63], v[236:239]
s_add_u32 s26, s26, 524288
v_mul_f32 v36, s47, v36
v_mul_f32 v37, s47, v37
v_mfma_f32_16x16x32_bf16 v[240:243], v[80:83], v[60:63], v[240:243]
v_cvt_pk_bf16_f32 v44, v36, v37
v_accvgpr_read_b32 v38, a26
v_mfma_f32_16x16x32_bf16 v[244:247], v[84:87], v[60:63], v[244:247]
v_accvgpr_read_b32 v39, a27
v_mul_f32 v38, s47, v38
v_mfma_f32_16x16x32_bf16 v[248:251], v[88:91], v[60:63], v[248:251]
v_mul_f32 v39, s47, v39
v_cvt_pk_bf16_f32 v45, v38, v39
v_mfma_f32_16x16x32_bf16 v[252:255], v[92:95], v[60:63], v[252:255]
s_waitcnt vmcnt(18) lgkmcnt(0)
s_barrier
v_accvgpr_read_b32 v36, a28
v_mfma_f32_16x16x32_bf16 v[128:131], v[96:99], v[48:51], v[128:131]
ds_read_b128 v[60:63], v25 offset:6144
v_accvgpr_read_b32 v37, a29
v_mfma_f32_16x16x32_bf16 v[132:135], v[100:103], v[48:51], v[132:135]
v_add_u32 v25, 4294868992, v25
v_mul_f32 v36, s47, v36
v_mfma_f32_16x16x32_bf16 v[136:139], v[104:107], v[48:51], v[136:139]
s_add_u32 m0, s92, 131072
ds_read_b64_tr_b16 v[64:65], v26 offset:16384
ds_read_b64_tr_b16 v[66:67], v26 offset:17408
v_mfma_f32_16x16x32_bf16 v[140:143], v[108:111], v[48:51], v[140:143]
buffer_load_dwordx4 v20, s[12:15], s24 offen offset:0 lds
v_mul_f32 v37, s47, v37
v_mfma_f32_16x16x32_bf16 v[144:147], v[112:115], v[48:51], v[144:147]
ds_read_b64_tr_b16 v[68:69], v27 offset:16384
ds_read_b64_tr_b16 v[70:71], v27 offset:17408
v_mfma_f32_16x16x32_bf16 v[148:151], v[116:119], v[48:51], v[148:151]
v_cvt_pk_bf16_f32 v46, v36, v37
v_accvgpr_read_b32 v38, a30
v_mfma_f32_16x16x32_bf16 v[152:155], v[120:123], v[48:51], v[152:155]
ds_read_b64_tr_b16 v[72:73], v28 offset:16384
ds_read_b64_tr_b16 v[74:75], v28 offset:17408
v_mfma_f32_16x16x32_bf16 v[156:159], v[124:127], v[48:51], v[156:159]
buffer_load_dwordx4 v21, s[12:15], s24 offen offset:1024 lds
v_accvgpr_read_b32 v39, a31
v_mfma_f32_16x16x32_bf16 v[160:163], v[96:99], v[52:55], v[160:163]
ds_read_b128 v[48:51], v24 offset:0
v_mul_f32 v38, s47, v38
v_mfma_f32_16x16x32_bf16 v[164:167], v[100:103], v[52:55], v[164:167]
v_mul_f32 v39, s47, v39
v_cvt_pk_bf16_f32 v47, v38, v39
v_mfma_f32_16x16x32_bf16 v[168:171], v[104:107], v[52:55], v[168:171]
ds_read_b64_tr_b16 v[76:77], v29 offset:16384
ds_read_b64_tr_b16 v[78:79], v29 offset:17408
v_mfma_f32_16x16x32_bf16 v[172:175], v[108:111], v[52:55], v[172:175]
s_xor_b32 s48, s95, 3072
buffer_load_dwordx4 v22, s[12:15], s24 offen offset:2048 lds
v_mfma_f32_16x16x32_bf16 v[176:179], v[112:115], v[52:55], v[176:179]
ds_read_b64_tr_b16 v[80:81], v30 offset:16384
ds_read_b64_tr_b16 v[82:83], v30 offset:17408
v_mfma_f32_16x16x32_bf16 v[180:183], v[116:119], v[52:55], v[180:183]
buffer_store_dwordx4 v[44:47], v7, s[20:23], s48 offen sc1
v_accvgpr_read_b32 v36, a160
v_mfma_f32_16x16x32_bf16 v[184:187], v[120:123], v[52:55], v[184:187]
ds_read_b64_tr_b16 v[84:85], v31 offset:16384
ds_read_b64_tr_b16 v[86:87], v31 offset:17408
v_mfma_f32_16x16x32_bf16 v[188:191], v[124:127], v[52:55], v[188:191]
buffer_load_dwordx4 v23, s[12:15], s24 offen offset:3072 lds
v_accvgpr_read_b32 v37, a161
v_mfma_f32_16x16x32_bf16 v[192:195], v[96:99], v[56:59], v[192:195]
ds_read_b128 v[52:55], v24 offset:2048
v_mul_f32 v36, s47, v36
v_mfma_f32_16x16x32_bf16 v[196:199], v[100:103], v[56:59], v[196:199]
v_mul_f32 v37, s47, v37
v_cvt_pk_bf16_f32 v40, v36, v37
v_mfma_f32_16x16x32_bf16 v[200:203], v[104:107], v[56:59], v[200:203]
s_add_u32 m0, s92, 135168
ds_read_b64_tr_b16 v[88:89], v32 offset:16384
ds_read_b64_tr_b16 v[90:91], v32 offset:17408
v_mfma_f32_16x16x32_bf16 v[204:207], v[108:111], v[56:59], v[204:207]
buffer_load_dwordx4 v20, s[12:15], s25 offen offset:0 lds
v_accvgpr_read_b32 v38, a162
v_mfma_f32_16x16x32_bf16 v[208:211], v[112:115], v[56:59], v[208:211]
ds_read_b64_tr_b16 v[92:93], v33 offset:16384
ds_read_b64_tr_b16 v[94:95], v33 offset:17408
v_mfma_f32_16x16x32_bf16 v[212:215], v[116:119], v[56:59], v[212:215]
v_accvgpr_read_b32 v39, a163
v_mul_f32 v38, s47, v38
v_mfma_f32_16x16x32_bf16 v[216:219], v[120:123], v[56:59], v[216:219]
s_waitcnt lgkmcnt(15)
v_mul_f32 v39, s47, v39
v_cvt_pk_bf16_f32 v41, v38, v39
v_mfma_f32_16x16x32_bf16 v[220:223], v[124:127], v[56:59], v[220:223]
v_accvgpr_read_b32 v36, a164
v_accvgpr_read_b32 v37, a165
v_mfma_f32_16x16x32_bf16 v[224:227], v[96:99], v[60:63], v[224:227]
ds_read_b128 v[56:59], v24 offset:4096
v_mul_f32 v36, s47, v36
v_mfma_f32_16x16x32_bf16 v[228:231], v[100:103], v[60:63], v[228:231]
buffer_load_dwordx4 v21, s[12:15], s25 offen offset:1024 lds
v_mul_f32 v37, s47, v37
v_mfma_f32_16x16x32_bf16 v[232:235], v[104:107], v[60:63], v[232:235]
v_cvt_pk_bf16_f32 v42, v36, v37
v_accvgpr_read_b32 v38, a166
v_mfma_f32_16x16x32_bf16 v[236:239], v[108:111], v[60:63], v[236:239]
v_accvgpr_read_b32 v39, a167
v_mul_f32 v38, s47, v38
v_mfma_f32_16x16x32_bf16 v[240:243], v[112:115], v[60:63], v[240:243]
v_mul_f32 v39, s47, v39
v_cvt_pk_bf16_f32 v43, v38, v39
v_mfma_f32_16x16x32_bf16 v[244:247], v[116:119], v[60:63], v[244:247]
v_accvgpr_write_b32 a160, v40
.Lpad5:
s_cbranch_execz .Ltch1
v_accvgpr_write_b32 a161, v41
v_mfma_f32_16x16x32_bf16 v[248:251], v[120:123], v[60:63], v[248:251]
v_accvgpr_write_b32 a162, v42
v_accvgpr_write_b32 a163, v43
v_mfma_f32_16x16x32_bf16 v[252:255], v[124:127], v[60:63], v[252:255]
s_waitcnt lgkmcnt(0)
v_accvgpr_read_b32 v36, a168
v_accvgpr_read_b32 v37, a169
v_mfma_f32_16x16x32_bf16 v[128:131], v[64:67], v[48:51], v[128:131]
ds_read_b128 v[60:63], v24 offset:6144
v_mul_f32 v36, s47, v36
v_mfma_f32_16x16x32_bf16 v[132:135], v[68:71], v[48:51], v[132:135]
v_add_u32 v24, 65536, v24
v_mul_f32 v37, s47, v37
v_mfma_f32_16x16x32_bf16 v[136:139], v[72:75], v[48:51], v[136:139]
ds_read_b64_tr_b16 v[96:97], v26 offset:24576
ds_read_b64_tr_b16 v[98:99], v26 offset:25600
v_mfma_f32_16x16x32_bf16 v[140:143], v[76:79], v[48:51], v[140:143]
buffer_load_dwordx4 v22, s[12:15], s25 offen offset:2048 lds
v_cvt_pk_bf16_f32 v44, v36, v37
v_mfma_f32_16x16x32_bf16 v[144:147], v[80:83], v[48:51], v[144:147]
ds_read_b64_tr_b16 v[100:101], v27 offset:24576
ds_read_b64_tr_b16 v[102:103], v27 offset:25600
v_mfma_f32_16x16x32_bf16 v[148:151], v[84:87], v[48:51], v[148:151]
v_accvgpr_read_b32 v38, a170
v_accvgpr_read_b32 v39, a171
v_mfma_f32_16x16x32_bf16 v[152:155], v[88:91], v[48:51], v[152:155]
ds_read_b64_tr_b16 v[104:105], v28 offset:24576
ds_read_b64_tr_b16 v[106:107], v28 offset:25600
v_mfma_f32_16x16x32_bf16 v[156:159], v[92:95], v[48:51], v[156:159]
buffer_load_dwordx4 v23, s[12:15], s25 offen offset:3072 lds
v_mul_f32 v38, s47, v38
v_mfma_f32_16x16x32_bf16 v[160:163], v[64:67], v[52:55], v[160:163]
ds_read_b128 v[48:51], v25 offset:0
v_mul_f32 v39, s47, v39
v_mfma_f32_16x16x32_bf16 v[164:167], v[68:71], v[52:55], v[164:167]
s_add_u32 s24, s24, 128
s_add_u32 s25, s25, 128
v_cvt_pk_bf16_f32 v45, v38, v39
v_mfma_f32_16x16x32_bf16 v[168:171], v[72:75], v[52:55], v[168:171]
s_add_u32 m0, s91, 0
ds_read_b64_tr_b16 v[108:109], v29 offset:24576
ds_read_b64_tr_b16 v[110:111], v29 offset:25600
v_mfma_f32_16x16x32_bf16 v[172:175], v[76:79], v[52:55], v[172:175]
buffer_load_dwordx4 v16, s[16:19], s26 offen offset:0 lds
v_accvgpr_read_b32 v36, a172
v_mfma_f32_16x16x32_bf16 v[176:179], v[80:83], v[52:55], v[176:179]
ds_read_b64_tr_b16 v[112:113], v30 offset:24576
ds_read_b64_tr_b16 v[114:115], v30 offset:25600
v_mfma_f32_16x16x32_bf16 v[180:183], v[84:87], v[52:55], v[180:183]
v_accvgpr_read_b32 v37, a173
v_mul_f32 v36, s47, v36
v_mfma_f32_16x16x32_bf16 v[184:187], v[88:91], v[52:55], v[184:187]
ds_read_b64_tr_b16 v[116:117], v31 offset:24576
ds_read_b64_tr_b16 v[118:119], v31 offset:25600
v_mfma_f32_16x16x32_bf16 v[188:191], v[92:95], v[52:55], v[188:191]
buffer_load_dwordx4 v17, s[16:19], s26 offen offset:1024 lds
v_mul_f32 v37, s47, v37
v_mfma_f32_16x16x32_bf16 v[192:195], v[64:67], v[56:59], v[192:195]
ds_read_b128 v[52:55], v25 offset:2048
v_cvt_pk_bf16_f32 v46, v36, v37
v_mfma_f32_16x16x32_bf16 v[196:199], v[68:71], v[56:59], v[196:199]
v_accvgpr_read_b32 v38, a174
v_accvgpr_read_b32 v39, a175
v_mfma_f32_16x16x32_bf16 v[200:203], v[72:75], v[56:59], v[200:203]
ds_read_b64_tr_b16 v[120:121], v32 offset:24576
ds_read_b64_tr_b16 v[122:123], v32 offset:25600
v_mfma_f32_16x16x32_bf16 v[204:207], v[76:79], v[56:59], v[204:207]
buffer_load_dwordx4 v18, s[16:19], s26 offen offset:2048 lds
v_mul_f32 v38, s47, v38
v_mfma_f32_16x16x32_bf16 v[208:211], v[80:83], v[56:59], v[208:211]
ds_read_b64_tr_b16 v[124:125], v33 offset:24576
ds_read_b64_tr_b16 v[126:127], v33 offset:25600
v_mfma_f32_16x16x32_bf16 v[212:215], v[84:87], v[56:59], v[212:215]
v_mul_f32 v39, s47, v39
v_cvt_pk_bf16_f32 v47, v38, v39
v_mfma_f32_16x16x32_bf16 v[216:219], v[88:91], v[56:59], v[216:219]
s_waitcnt lgkmcnt(15)
v_accvgpr_write_b32 a164, v44
v_accvgpr_write_b32 a165, v45
v_mfma_f32_16x16x32_bf16 v[220:223], v[92:95], v[56:59], v[220:223]
v_xor_b32 v26, 0x10000, v26
v_accvgpr_write_b32 a166, v46
v_mfma_f32_16x16x32_bf16 v[224:227], v[64:67], v[60:63], v[224:227]
ds_read_b128 v[56:59], v25 offset:4096
v_xor_b32 v27, 0x10000, v27
v_mfma_f32_16x16x32_bf16 v[228:231], v[68:71], v[60:63], v[228:231]
buffer_load_dwordx4 v19, s[16:19], s26 offen offset:3072 lds
v_xor_b32 v28, 0x10000, v28
v_mfma_f32_16x16x32_bf16 v[232:235], v[72:75], v[60:63], v[232:235]
v_xor_b32 v29, 0x10000, v29
v_accvgpr_write_b32 a167, v47
v_mfma_f32_16x16x32_bf16 v[236:239], v[76:79], v[60:63], v[236:239]
s_add_u32 s26, s26, 524288
v_xor_b32 v30, 0x10000, v30
v_accvgpr_read_b32 v36, a32
v_mfma_f32_16x16x32_bf16 v[240:243], v[80:83], v[60:63], v[240:243]
v_xor_b32 v31, 0x10000, v31
v_accvgpr_read_b32 v37, a33
v_mfma_f32_16x16x32_bf16 v[244:247], v[84:87], v[60:63], v[244:247]
v_xor_b32 v32, 0x10000, v32
v_mul_f32 v36, s47, v36
v_mfma_f32_16x16x32_bf16 v[248:251], v[88:91], v[60:63], v[248:251]
v_xor_b32 v33, 0x10000, v33
v_mul_f32 v37, s47, v37
v_mfma_f32_16x16x32_bf16 v[252:255], v[92:95], v[60:63], v[252:255]
s_waitcnt vmcnt(17) lgkmcnt(0)
s_barrier
v_cvt_pk_bf16_f32 v40, v36, v37
v_mfma_f32_16x16x32_bf16 v[128:131], v[96:99], v[48:51], v[128:131]
ds_read_b128 v[60:63], v25 offset:6144
v_accvgpr_read_b32 v38, a34
v_mfma_f32_16x16x32_bf16 v[132:135], v[100:103], v[48:51], v[132:135]
v_add_u32 v25, 65536, v25
v_accvgpr_read_b32 v39, a35
v_mfma_f32_16x16x32_bf16 v[136:139], v[104:107], v[48:51], v[136:139]
s_add_u32 m0, s92, 32768
ds_read_b64_tr_b16 v[64:65], v26 offset:0
ds_read_b64_tr_b16 v[66:67], v26 offset:1024
v_mfma_f32_16x16x32_bf16 v[140:143], v[108:111], v[48:51], v[140:143]
buffer_load_dwordx4 v20, s[12:15], s24 offen offset:0 lds
v_mul_f32 v38, s47, v38
v_mfma_f32_16x16x32_bf16 v[144:147], v[112:115], v[48:51], v[144:147]
ds_read_b64_tr_b16 v[68:69], v27 offset:0
ds_read_b64_tr_b16 v[70:71], v27 offset:1024
v_mfma_f32_16x16x32_bf16 v[148:151], v[116:119], v[48:51], v[148:151]
v_mul_f32 v39, s47, v39
v_cvt_pk_bf16_f32 v41, v38, v39
v_mfma_f32_16x16x32_bf16 v[152:155], v[120:123], v[48:51], v[152:155]
ds_read_b64_tr_b16 v[72:73], v28 offset:0
ds_read_b64_tr_b16 v[74:75], v28 offset:1024
v_mfma_f32_16x16x32_bf16 v[156:159], v[124:127], v[48:51], v[156:159]
buffer_load_dwordx4 v21, s[12:15], s24 offen offset:1024 lds
v_accvgpr_read_b32 v36, a36
v_mfma_f32_16x16x32_bf16 v[160:163], v[96:99], v[52:55], v[160:163]
ds_read_b128 v[48:51], v24 offset:0
v_accvgpr_read_b32 v37, a37
v_mfma_f32_16x16x32_bf16 v[164:167], v[100:103], v[52:55], v[164:167]
v_mul_f32 v36, s47, v36
v_mul_f32 v37, s47, v37
v_mfma_f32_16x16x32_bf16 v[168:171], v[104:107], v[52:55], v[168:171]
ds_read_b64_tr_b16 v[76:77], v29 offset:0
ds_read_b64_tr_b16 v[78:79], v29 offset:1024
v_mfma_f32_16x16x32_bf16 v[172:175], v[108:111], v[52:55], v[172:175]
buffer_load_dwordx4 v22, s[12:15], s24 offen offset:2048 lds
v_cvt_pk_bf16_f32 v42, v36, v37
v_mfma_f32_16x16x32_bf16 v[176:179], v[112:115], v[52:55], v[176:179]
ds_read_b64_tr_b16 v[80:81], v30 offset:0
ds_read_b64_tr_b16 v[82:83], v30 offset:1024
v_mfma_f32_16x16x32_bf16 v[180:183], v[116:119], v[52:55], v[180:183]
v_accvgpr_read_b32 v38, a38
v_accvgpr_read_b32 v39, a39
v_mfma_f32_16x16x32_bf16 v[184:187], v[120:123], v[52:55], v[184:187]
ds_read_b64_tr_b16 v[84:85], v31 offset:0
ds_read_b64_tr_b16 v[86:87], v31 offset:1024
v_mfma_f32_16x16x32_bf16 v[188:191], v[124:127], v[52:55], v[188:191]
buffer_load_dwordx4 v23, s[12:15], s24 offen offset:3072 lds
v_mul_f32 v38, s47, v38
v_mfma_f32_16x16x32_bf16 v[192:195], v[96:99], v[56:59], v[192:195]
ds_read_b128 v[52:55], v24 offset:2048
v_mul_f32 v39, s47, v39
v_mfma_f32_16x16x32_bf16 v[196:199], v[100:103], v[56:59], v[196:199]
v_cvt_pk_bf16_f32 v43, v38, v39
v_accvgpr_read_b32 v36, a176
v_mfma_f32_16x16x32_bf16 v[200:203], v[104:107], v[56:59], v[200:203]
s_add_u32 m0, s92, 36864
ds_read_b64_tr_b16 v[88:89], v32 offset:0
ds_read_b64_tr_b16 v[90:91], v32 offset:1024
v_mfma_f32_16x16x32_bf16 v[204:207], v[108:111], v[56:59], v[204:207]
s_xor_b32 s48, s95, 4096
buffer_load_dwordx4 v20, s[12:15], s25 offen offset:0 lds
v_mfma_f32_16x16x32_bf16 v[208:211], v[112:115], v[56:59], v[208:211]
ds_read_b64_tr_b16 v[92:93], v33 offset:0
ds_read_b64_tr_b16 v[94:95], v33 offset:1024
v_mfma_f32_16x16x32_bf16 v[212:215], v[116:119], v[56:59], v[212:215]
buffer_store_dwordx4 v[40:43], v7, s[20:23], s48 offen sc1
v_accvgpr_read_b32 v37, a177
v_mfma_f32_16x16x32_bf16 v[216:219], v[120:123], v[56:59], v[216:219]
s_waitcnt lgkmcnt(15)
v_mul_f32 v36, s47, v36
v_mul_f32 v37, s47, v37
v_mfma_f32_16x16x32_bf16 v[220:223], v[124:127], v[56:59], v[220:223]
v_cvt_pk_bf16_f32 v44, v36, v37
v_accvgpr_read_b32 v38, a178
v_mfma_f32_16x16x32_bf16 v[224:227], v[96:99], v[60:63], v[224:227]
ds_read_b128 v[56:59], v24 offset:4096
v_accvgpr_read_b32 v39, a179
v_mfma_f32_16x16x32_bf16 v[228:231], v[100:103], v[60:63], v[228:231]
buffer_load_dwordx4 v21, s[12:15], s25 offen offset:1024 lds
v_mul_f32 v38, s47, v38
v_mfma_f32_16x16x32_bf16 v[232:235], v[104:107], v[60:63], v[232:235]
v_mul_f32 v39, s47, v39
v_cvt_pk_bf16_f32 v45, v38, v39
v_mfma_f32_16x16x32_bf16 v[236:239], v[108:111], v[60:63], v[236:239]
v_accvgpr_read_b32 v36, a180
v_accvgpr_read_b32 v37, a181
v_mfma_f32_16x16x32_bf16 v[240:243], v[112:115], v[60:63], v[240:243]
v_mul_f32 v36, s47, v36
v_mul_f32 v37, s47, v37
v_mfma_f32_16x16x32_bf16 v[244:247], v[116:119], v[60:63], v[244:247]
v_cvt_pk_bf16_f32 v46, v36, v37
v_accvgpr_read_b32 v38, a182
v_mfma_f32_16x16x32_bf16 v[248:251], v[120:123], v[60:63], v[248:251]
v_accvgpr_read_b32 v39, a183
v_mul_f32 v38, s47, v38
v_mfma_f32_16x16x32_bf16 v[252:255], v[124:127], v[60:63], v[252:255]
s_waitcnt lgkmcnt(0)
v_mul_f32 v39, s47, v39
v_cvt_pk_bf16_f32 v47, v38, v39
v_mfma_f32_16x16x32_bf16 v[128:131], v[64:67], v[48:51], v[128:131]
ds_read_b128 v[60:63], v24 offset:6144
v_accvgpr_write_b32 a168, v44
v_mfma_f32_16x16x32_bf16 v[132:135], v[68:71], v[48:51], v[132:135]
v_add_u32 v24, 32768, v24
v_accvgpr_write_b32 a169, v45
v_mfma_f32_16x16x32_bf16 v[136:139], v[72:75], v[48:51], v[136:139]
ds_read_b64_tr_b16 v[96:97], v26 offset:8192
ds_read_b64_tr_b16 v[98:99], v26 offset:9216
v_mfma_f32_16x16x32_bf16 v[140:143], v[76:79], v[48:51], v[140:143]
buffer_load_dwordx4 v22, s[12:15], s25 offen offset:2048 lds
v_accvgpr_write_b32 a170, v46
v_mfma_f32_16x16x32_bf16 v[144:147], v[80:83], v[48:51], v[144:147]
ds_read_b64_tr_b16 v[100:101], v27 offset:8192
ds_read_b64_tr_b16 v[102:103], v27 offset:9216
v_mfma_f32_16x16x32_bf16 v[148:151], v[84:87], v[48:51], v[148:151]
v_accvgpr_write_b32 a171, v47
v_accvgpr_read_b32 v36, a40
v_mfma_f32_16x16x32_bf16 v[152:155], v[88:91], v[48:51], v[152:155]
ds_read_b64_tr_b16 v[104:105], v28 offset:8192
ds_read_b64_tr_b16 v[106:107], v28 offset:9216
v_mfma_f32_16x16x32_bf16 v[156:159], v[92:95], v[48:51], v[156:159]
buffer_load_dwordx4 v23, s[12:15], s25 offen offset:3072 lds
v_accvgpr_read_b32 v37, a41
v_mfma_f32_16x16x32_bf16 v[160:163], v[64:67], v[52:55], v[160:163]
ds_read_b128 v[48:51], v25 offset:0
v_mul_f32 v36, s47, v36
v_mfma_f32_16x16x32_bf16 v[164:167], v[68:71], v[52:55], v[164:167]
s_add_u32 s24, s24, 128
s_add_u32 s25, s25, 128
buffer_load_dword v35, v4, s[96:99], 0 offen
v_mfma_f32_16x16x32_bf16 v[168:171], v[72:75], v[52:55], v[168:171]
s_add_u32 m0, s91, 16384
ds_read_b64_tr_b16 v[108:109], v29 offset:8192
ds_read_b64_tr_b16 v[110:111], v29 offset:9216
v_mfma_f32_16x16x32_bf16 v[172:175], v[76:79], v[52:55], v[172:175]
buffer_load_dwordx4 v16, s[16:19], s26 offen offset:0 lds
v_mul_f32 v37, s47, v37
v_mfma_f32_16x16x32_bf16 v[176:179], v[80:83], v[52:55], v[176:179]
ds_read_b64_tr_b16 v[112:113], v30 offset:8192
ds_read_b64_tr_b16 v[114:115], v30 offset:9216
v_mfma_f32_16x16x32_bf16 v[180:183], v[84:87], v[52:55], v[180:183]
v_cvt_pk_bf16_f32 v40, v36, v37
v_accvgpr_read_b32 v38, a42
v_mfma_f32_16x16x32_bf16 v[184:187], v[88:91], v[52:55], v[184:187]
ds_read_b64_tr_b16 v[116:117], v31 offset:8192
ds_read_b64_tr_b16 v[118:119], v31 offset:9216
v_mfma_f32_16x16x32_bf16 v[188:191], v[92:95], v[52:55], v[188:191]
buffer_load_dwordx4 v17, s[16:19], s26 offen offset:1024 lds
v_accvgpr_read_b32 v39, a43
v_mfma_f32_16x16x32_bf16 v[192:195], v[64:67], v[56:59], v[192:195]
ds_read_b128 v[52:55], v25 offset:2048
v_mul_f32 v38, s47, v38
v_mfma_f32_16x16x32_bf16 v[196:199], v[68:71], v[56:59], v[196:199]
v_mul_f32 v39, s47, v39
v_cvt_pk_bf16_f32 v41, v38, v39
v_mfma_f32_16x16x32_bf16 v[200:203], v[72:75], v[56:59], v[200:203]
ds_read_b64_tr_b16 v[120:121], v32 offset:8192
ds_read_b64_tr_b16 v[122:123], v32 offset:9216
v_mfma_f32_16x16x32_bf16 v[204:207], v[76:79], v[56:59], v[204:207]
buffer_load_dwordx4 v18, s[16:19], s26 offen offset:2048 lds
v_accvgpr_read_b32 v36, a44
v_mfma_f32_16x16x32_bf16 v[208:211], v[80:83], v[56:59], v[208:211]
ds_read_b64_tr_b16 v[124:125], v33 offset:8192
ds_read_b64_tr_b16 v[126:127], v33 offset:9216
v_mfma_f32_16x16x32_bf16 v[212:215], v[84:87], v[56:59], v[212:215]
v_accvgpr_read_b32 v37, a45
v_mul_f32 v36, s47, v36
v_mfma_f32_16x16x32_bf16 v[216:219], v[88:91], v[56:59], v[216:219]
s_waitcnt lgkmcnt(15)
v_mul_f32 v37, s47, v37
v_cvt_pk_bf16_f32 v42, v36, v37
v_mfma_f32_16x16x32_bf16 v[220:223], v[92:95], v[56:59], v[220:223]
v_accvgpr_read_b32 v38, a46
v_accvgpr_read_b32 v39, a47
v_mfma_f32_16x16x32_bf16 v[224:227], v[64:67], v[60:63], v[224:227]
ds_read_b128 v[56:59], v25 offset:4096
v_mul_f32 v38, s47, v38
v_mfma_f32_16x16x32_bf16 v[228:231], v[68:71], v[60:63], v[228:231]
buffer_load_dwordx4 v19, s[16:19], s26 offen offset:3072 lds
v_mul_f32 v39, s47, v39
v_mfma_f32_16x16x32_bf16 v[232:235], v[72:75], v[60:63], v[232:235]
v_cvt_pk_bf16_f32 v43, v38, v39
v_accvgpr_read_b32 v36, a184
v_mfma_f32_16x16x32_bf16 v[236:239], v[76:79], v[60:63], v[236:239]
s_add_u32 s26, s26, 524288
s_xor_b32 s48, s95, 5120
v_accvgpr_read_b32 v37, a185
v_mfma_f32_16x16x32_bf16 v[240:243], v[80:83], v[60:63], v[240:243]
buffer_store_dwordx4 v[40:43], v7, s[20:23], s48 offen sc1
v_mul_f32 v36, s47, v36
v_mfma_f32_16x16x32_bf16 v[244:247], v[84:87], v[60:63], v[244:247]
v_mul_f32 v37, s47, v37
v_cvt_pk_bf16_f32 v44, v36, v37
v_mfma_f32_16x16x32_bf16 v[248:251], v[88:91], v[60:63], v[248:251]
v_accvgpr_read_b32 v38, a186
v_accvgpr_read_b32 v39, a187
v_mfma_f32_16x16x32_bf16 v[252:255], v[92:95], v[60:63], v[252:255]
s_waitcnt vmcnt(19) lgkmcnt(0)
s_barrier
v_mul_f32 v38, s47, v38
v_mfma_f32_16x16x32_bf16 v[128:131], v[96:99], v[48:51], v[128:131]
ds_read_b128 v[60:63], v25 offset:6144
v_mul_f32 v39, s47, v39
v_mfma_f32_16x16x32_bf16 v[132:135], v[100:103], v[48:51], v[132:135]
v_add_u32 v25, 32768, v25
v_cvt_pk_bf16_f32 v45, v38, v39
v_mfma_f32_16x16x32_bf16 v[136:139], v[104:107], v[48:51], v[136:139]
s_add_u32 m0, s92, 98304
ds_read_b64_tr_b16 v[64:65], v26 offset:16384
ds_read_b64_tr_b16 v[66:67], v26 offset:17408
v_mfma_f32_16x16x32_bf16 v[140:143], v[108:111], v[48:51], v[140:143]
buffer_load_dwordx4 v20, s[12:15], s24 offen offset:0 lds
v_accvgpr_read_b32 v36, a188
v_mfma_f32_16x16x32_bf16 v[144:147], v[112:115], v[48:51], v[144:147]
ds_read_b64_tr_b16 v[68:69], v27 offset:16384
ds_read_b64_tr_b16 v[70:71], v27 offset:17408
v_mfma_f32_16x16x32_bf16 v[148:151], v[116:119], v[48:51], v[148:151]
v_accvgpr_read_b32 v37, a189
v_mul_f32 v36, s47, v36
v_mfma_f32_16x16x32_bf16 v[152:155], v[120:123], v[48:51], v[152:155]
ds_read_b64_tr_b16 v[72:73], v28 offset:16384
ds_read_b64_tr_b16 v[74:75], v28 offset:17408
v_mfma_f32_16x16x32_bf16 v[156:159], v[124:127], v[48:51], v[156:159]
buffer_load_dwordx4 v21, s[12:15], s24 offen offset:1024 lds
v_mul_f32 v37, s47, v37
v_mfma_f32_16x16x32_bf16 v[160:163], v[96:99], v[52:55], v[160:163]
ds_read_b128 v[48:51], v24 offset:0
v_cvt_pk_bf16_f32 v46, v36, v37
v_mfma_f32_16x16x32_bf16 v[164:167], v[100:103], v[52:55], v[164:167]
v_accvgpr_read_b32 v38, a190
v_accvgpr_read_b32 v39, a191
v_mfma_f32_16x16x32_bf16 v[168:171], v[104:107], v[52:55], v[168:171]
ds_read_b64_tr_b16 v[76:77], v29 offset:16384
ds_read_b64_tr_b16 v[78:79], v29 offset:17408
v_mfma_f32_16x16x32_bf16 v[172:175], v[108:111], v[52:55], v[172:175]
buffer_load_dwordx4 v22, s[12:15], s24 offen offset:2048 lds
v_mul_f32 v38, s47, v38
v_mfma_f32_16x16x32_bf16 v[176:179], v[112:115], v[52:55], v[176:179]
ds_read_b64_tr_b16 v[80:81], v30 offset:16384
ds_read_b64_tr_b16 v[82:83], v30 offset:17408
v_mfma_f32_16x16x32_bf16 v[180:183], v[116:119], v[52:55], v[180:183]
v_mul_f32 v39, s47, v39
v_cvt_pk_bf16_f32 v47, v38, v39
v_mfma_f32_16x16x32_bf16 v[184:187], v[120:123], v[52:55], v[184:187]
ds_read_b64_tr_b16 v[84:85], v31 offset:16384
ds_read_b64_tr_b16 v[86:87], v31 offset:17408
v_mfma_f32_16x16x32_bf16 v[188:191], v[124:127], v[52:55], v[188:191]
buffer_load_dwordx4 v23, s[12:15], s24 offen offset:3072 lds
v_accvgpr_write_b32 a172, v44
v_mfma_f32_16x16x32_bf16 v[192:195], v[96:99], v[56:59], v[192:195]
ds_read_b128 v[52:55], v24 offset:2048
v_accvgpr_write_b32 a173, v45
v_mfma_f32_16x16x32_bf16 v[196:199], v[100:103], v[56:59], v[196:199]
v_accvgpr_write_b32 a174, v46
v_accvgpr_write_b32 a175, v47
v_mfma_f32_16x16x32_bf16 v[200:203], v[104:107], v[56:59], v[200:203]
s_add_u32 m0, s92, 102400
ds_read_b64_tr_b16 v[88:89], v32 offset:16384
ds_read_b64_tr_b16 v[90:91], v32 offset:17408
v_mfma_f32_16x16x32_bf16 v[204:207], v[108:111], v[56:59], v[204:207]
buffer_load_dwordx4 v20, s[12:15], s25 offen offset:0 lds
v_accvgpr_read_b32 v36, a48
v_mfma_f32_16x16x32_bf16 v[208:211], v[112:115], v[56:59], v[208:211]
ds_read_b64_tr_b16 v[92:93], v33 offset:16384
ds_read_b64_tr_b16 v[94:95], v33 offset:17408
v_mfma_f32_16x16x32_bf16 v[212:215], v[116:119], v[56:59], v[212:215]
v_accvgpr_read_b32 v37, a49
v_mul_f32 v36, s47, v36
v_mfma_f32_16x16x32_bf16 v[216:219], v[120:123], v[56:59], v[216:219]
s_waitcnt lgkmcnt(15)
v_mul_f32 v37, s47, v37
v_cvt_pk_bf16_f32 v40, v36, v37
v_mfma_f32_16x16x32_bf16 v[220:223], v[124:127], v[56:59], v[220:223]
v_accvgpr_read_b32 v38, a50
v_accvgpr_read_b32 v39, a51
v_mfma_f32_16x16x32_bf16 v[224:227], v[96:99], v[60:63], v[224:227]
ds_read_b128 v[56:59], v24 offset:4096
v_mul_f32 v38, s47, v38
v_mfma_f32_16x16x32_bf16 v[228:231], v[100:103], v[60:63], v[228:231]
buffer_load_dwordx4 v21, s[12:15], s25 offen offset:1024 lds
v_mul_f32 v39, s47, v39
v_mfma_f32_16x16x32_bf16 v[232:235], v[104:107], v[60:63], v[232:235]
v_cvt_pk_bf16_f32 v41, v38, v39
v_accvgpr_read_b32 v36, a52
v_mfma_f32_16x16x32_bf16 v[236:239], v[108:111], v[60:63], v[236:239]
v_accvgpr_read_b32 v37, a53
v_mul_f32 v36, s47, v36
v_mfma_f32_16x16x32_bf16 v[240:243], v[112:115], v[60:63], v[240:243]
v_mul_f32 v37, s47, v37
v_cvt_pk_bf16_f32 v42, v36, v37
v_mfma_f32_16x16x32_bf16 v[244:247], v[116:119], v[60:63], v[244:247]
v_accvgpr_read_b32 v38, a54
v_accvgpr_read_b32 v39, a55
v_mfma_f32_16x16x32_bf16 v[248:251], v[120:123], v[60:63], v[248:251]
v_mul_f32 v38, s47, v38
v_mul_f32 v39, s47, v39
v_mfma_f32_16x16x32_bf16 v[252:255], v[124:127], v[60:63], v[252:255]
s_waitcnt lgkmcnt(0)
v_cvt_pk_bf16_f32 v43, v38, v39
v_accvgpr_read_b32 v36, a192
v_mfma_f32_16x16x32_bf16 v[128:131], v[64:67], v[48:51], v[128:131]
s_xor_b32 s48, s95, 6144
ds_read_b128 v[60:63], v24 offset:6144
v_mfma_f32_16x16x32_bf16 v[132:135], v[68:71], v[48:51], v[132:135]
buffer_store_dwordx4 v[40:43], v7, s[20:23], s48 offen sc1
v_add_u32 v24, 4294868992, v24
v_mfma_f32_16x16x32_bf16 v[136:139], v[72:75], v[48:51], v[136:139]
ds_read_b64_tr_b16 v[96:97], v26 offset:24576
ds_read_b64_tr_b16 v[98:99], v26 offset:25600
v_mfma_f32_16x16x32_bf16 v[140:143], v[76:79], v[48:51], v[140:143]
buffer_load_dwordx4 v22, s[12:15], s25 offen offset:2048 lds
v_accvgpr_read_b32 v37, a193
v_mfma_f32_16x16x32_bf16 v[144:147], v[80:83], v[48:51], v[144:147]
ds_read_b64_tr_b16 v[100:101], v27 offset:24576
ds_read_b64_tr_b16 v[102:103], v27 offset:25600
v_mfma_f32_16x16x32_bf16 v[148:151], v[84:87], v[48:51], v[148:151]
v_mul_f32 v36, s47, v36
v_mul_f32 v37, s47, v37
v_mfma_f32_16x16x32_bf16 v[152:155], v[88:91], v[48:51], v[152:155]
ds_read_b64_tr_b16 v[104:105], v28 offset:24576
ds_read_b64_tr_b16 v[106:107], v28 offset:25600
v_mfma_f32_16x16x32_bf16 v[156:159], v[92:95], v[48:51], v[156:159]
buffer_load_dwordx4 v23, s[12:15], s25 offen offset:3072 lds
v_cvt_pk_bf16_f32 v44, v36, v37
v_mfma_f32_16x16x32_bf16 v[160:163], v[64:67], v[52:55], v[160:163]
ds_read_b128 v[48:51], v25 offset:0
v_accvgpr_read_b32 v38, a194
v_mfma_f32_16x16x32_bf16 v[164:167], v[68:71], v[52:55], v[164:167]
s_add_u32 s24, s24, 128
s_add_u32 s25, s25, 128
v_accvgpr_read_b32 v39, a195
v_mfma_f32_16x16x32_bf16 v[168:171], v[72:75], v[52:55], v[168:171]
s_add_u32 m0, s91, 65536
ds_read_b64_tr_b16 v[108:109], v29 offset:24576
ds_read_b64_tr_b16 v[110:111], v29 offset:25600
v_mfma_f32_16x16x32_bf16 v[172:175], v[76:79], v[52:55], v[172:175]
buffer_load_dwordx4 v16, s[16:19], s26 offen offset:0 lds
v_mul_f32 v38, s47, v38
v_mfma_f32_16x16x32_bf16 v[176:179], v[80:83], v[52:55], v[176:179]
ds_read_b64_tr_b16 v[112:113], v30 offset:24576
ds_read_b64_tr_b16 v[114:115], v30 offset:25600
v_mfma_f32_16x16x32_bf16 v[180:183], v[84:87], v[52:55], v[180:183]
v_mul_f32 v39, s47, v39
v_cvt_pk_bf16_f32 v45, v38, v39
v_mfma_f32_16x16x32_bf16 v[184:187], v[88:91], v[52:55], v[184:187]
ds_read_b64_tr_b16 v[116:117], v31 offset:24576
ds_read_b64_tr_b16 v[118:119], v31 offset:25600
v_mfma_f32_16x16x32_bf16 v[188:191], v[92:95], v[52:55], v[188:191]
buffer_load_dwordx4 v17, s[16:19], s26 offen offset:1024 lds
v_accvgpr_read_b32 v36, a196
v_mfma_f32_16x16x32_bf16 v[192:195], v[64:67], v[56:59], v[192:195]
ds_read_b128 v[52:55], v25 offset:2048
v_accvgpr_read_b32 v37, a197
v_mfma_f32_16x16x32_bf16 v[196:199], v[68:71], v[56:59], v[196:199]
v_mul_f32 v36, s47, v36
v_mul_f32 v37, s47, v37
v_mfma_f32_16x16x32_bf16 v[200:203], v[72:75], v[56:59], v[200:203]
ds_read_b64_tr_b16 v[120:121], v32 offset:24576
ds_read_b64_tr_b16 v[122:123], v32 offset:25600
v_mfma_f32_16x16x32_bf16 v[204:207], v[76:79], v[56:59], v[204:207]
buffer_load_dwordx4 v18, s[16:19], s26 offen offset:2048 lds
v_cvt_pk_bf16_f32 v46, v36, v37
v_mfma_f32_16x16x32_bf16 v[208:211], v[80:83], v[56:59], v[208:211]
ds_read_b64_tr_b16 v[124:125], v33 offset:24576
ds_read_b64_tr_b16 v[126:127], v33 offset:25600
v_mfma_f32_16x16x32_bf16 v[212:215], v[84:87], v[56:59], v[212:215]
v_accvgpr_read_b32 v38, a198
v_accvgpr_read_b32 v39, a199
v_mfma_f32_16x16x32_bf16 v[216:219], v[88:91], v[56:59], v[216:219]
s_waitcnt lgkmcnt(15)
v_mul_f32 v38, s47, v38
v_mul_f32 v39, s47, v39
v_mfma_f32_16x16x32_bf16 v[220:223], v[92:95], v[56:59], v[220:223]
v_xor_b32 v26, 0x10000, v26
v_cvt_pk_bf16_f32 v47, v38, v39
v_mfma_f32_16x16x32_bf16 v[224:227], v[64:67], v[60:63], v[224:227]
ds_read_b128 v[56:59], v25 offset:4096
v_xor_b32 v27, 0x10000, v27
v_mfma_f32_16x16x32_bf16 v[228:231], v[68:71], v[60:63], v[228:231]
buffer_load_dwordx4 v19, s[16:19], s26 offen offset:3072 lds
v_xor_b32 v28, 0x10000, v28
v_mfma_f32_16x16x32_bf16 v[232:235], v[72:75], v[60:63], v[232:235]
v_xor_b32 v29, 0x10000, v29
v_accvgpr_write_b32 a192, v44
v_mfma_f32_16x16x32_bf16 v[236:239], v[76:79], v[60:63], v[236:239]
s_add_u32 s26, s26, 524288
v_xor_b32 v30, 0x10000, v30
v_accvgpr_write_b32 a193, v45
v_mfma_f32_16x16x32_bf16 v[240:243], v[80:83], v[60:63], v[240:243]
v_xor_b32 v31, 0x10000, v31
v_accvgpr_write_b32 a194, v46
v_mfma_f32_16x16x32_bf16 v[244:247], v[84:87], v[60:63], v[244:247]
v_xor_b32 v32, 0x10000, v32
v_accvgpr_write_b32 a195, v47
v_mfma_f32_16x16x32_bf16 v[248:251], v[88:91], v[60:63], v[248:251]
v_xor_b32 v33, 0x10000, v33
v_mfma_f32_16x16x32_bf16 v[252:255], v[92:95], v[60:63], v[252:255]
s_waitcnt vmcnt(19) lgkmcnt(0)
s_barrier
v_accvgpr_read_b32 v36, a56
v_mfma_f32_16x16x32_bf16 v[128:131], v[96:99], v[48:51], v[128:131]
ds_read_b128 v[60:63], v25 offset:6144
v_accvgpr_read_b32 v37, a57
v_mfma_f32_16x16x32_bf16 v[132:135], v[100:103], v[48:51], v[132:135]
v_add_u32 v25, 4294868992, v25
v_mul_f32 v36, s47, v36
v_mfma_f32_16x16x32_bf16 v[136:139], v[104:107], v[48:51], v[136:139]
s_add_u32 m0, s92, 131072
ds_read_b64_tr_b16 v[64:65], v26 offset:0
ds_read_b64_tr_b16 v[66:67], v26 offset:1024
v_mfma_f32_16x16x32_bf16 v[140:143], v[108:111], v[48:51], v[140:143]
buffer_load_dwordx4 v20, s[12:15], s24 offen offset:0 lds
v_mul_f32 v37, s47, v37
v_mfma_f32_16x16x32_bf16 v[144:147], v[112:115], v[48:51], v[144:147]
ds_read_b64_tr_b16 v[68:69], v27 offset:0
ds_read_b64_tr_b16 v[70:71], v27 offset:1024
v_mfma_f32_16x16x32_bf16 v[148:151], v[116:119], v[48:51], v[148:151]
v_cvt_pk_bf16_f32 v40, v36, v37
v_accvgpr_read_b32 v38, a58
v_mfma_f32_16x16x32_bf16 v[152:155], v[120:123], v[48:51], v[152:155]
ds_read_b64_tr_b16 v[72:73], v28 offset:0
ds_read_b64_tr_b16 v[74:75], v28 offset:1024
v_mfma_f32_16x16x32_bf16 v[156:159], v[124:127], v[48:51], v[156:159]
buffer_load_dwordx4 v21, s[12:15], s24 offen offset:1024 lds
v_accvgpr_read_b32 v39, a59
v_mfma_f32_16x16x32_bf16 v[160:163], v[96:99], v[52:55], v[160:163]
ds_read_b128 v[48:51], v24 offset:0
v_mul_f32 v38, s47, v38
v_mfma_f32_16x16x32_bf16 v[164:167], v[100:103], v[52:55], v[164:167]
v_mul_f32 v39, s47, v39
v_cvt_pk_bf16_f32 v41, v38, v39
v_mfma_f32_16x16x32_bf16 v[168:171], v[104:107], v[52:55], v[168:171]
ds_read_b64_tr_b16 v[76:77], v29 offset:0
ds_read_b64_tr_b16 v[78:79], v29 offset:1024
.Lpad6:
s_cbranch_execz .Ltch1
v_mfma_f32_16x16x32_bf16 v[172:175], v[108:111], v[52:55], v[172:175]
buffer_load_dwordx4 v22, s[12:15], s24 offen offset:2048 lds
v_accvgpr_read_b32 v36, a60
v_mfma_f32_16x16x32_bf16 v[176:179], v[112:115], v[52:55], v[176:179]
ds_read_b64_tr_b16 v[80:81], v30 offset:0
ds_read_b64_tr_b16 v[82:83], v30 offset:1024
v_mfma_f32_16x16x32_bf16 v[180:183], v[116:119], v[52:55], v[180:183]
v_accvgpr_read_b32 v37, a61
v_mul_f32 v36, s47, v36
v_mfma_f32_16x16x32_bf16 v[184:187], v[120:123], v[52:55], v[184:187]
ds_read_b64_tr_b16 v[84:85], v31 offset:0
ds_read_b64_tr_b16 v[86:87], v31 offset:1024
v_mfma_f32_16x16x32_bf16 v[188:191], v[124:127], v[52:55], v[188:191]
buffer_load_dwordx4 v23, s[12:15], s24 offen offset:3072 lds
v_mul_f32 v37, s47, v37
v_mfma_f32_16x16x32_bf16 v[192:195], v[96:99], v[56:59], v[192:195]
ds_read_b128 v[52:55], v24 offset:2048
v_cvt_pk_bf16_f32 v42, v36, v37
v_mfma_f32_16x16x32_bf16 v[196:199], v[100:103], v[56:59], v[196:199]
v_accvgpr_read_b32 v38, a62
v_accvgpr_read_b32 v39, a63
v_mfma_f32_16x16x32_bf16 v[200:203], v[104:107], v[56:59], v[200:203]
s_add_u32 m0, s92, 135168
ds_read_b64_tr_b16 v[88:89], v32 offset:0
ds_read_b64_tr_b16 v[90:91], v32 offset:1024
v_mfma_f32_16x16x32_bf16 v[204:207], v[108:111], v[56:59], v[204:207]
buffer_load_dwordx4 v20, s[12:15], s25 offen offset:0 lds
v_mul_f32 v38, s47, v38
v_mfma_f32_16x16x32_bf16 v[208:211], v[112:115], v[56:59], v[208:211]
ds_read_b64_tr_b16 v[92:93], v33 offset:0
ds_read_b64_tr_b16 v[94:95], v33 offset:1024
v_mfma_f32_16x16x32_bf16 v[212:215], v[116:119], v[56:59], v[212:215]
v_mul_f32 v39, s47, v39
v_cvt_pk_bf16_f32 v43, v38, v39
v_mfma_f32_16x16x32_bf16 v[216:219], v[120:123], v[56:59], v[216:219]
s_waitcnt lgkmcnt(15)
s_xor_b32 s48, s95, 7168
v_accvgpr_read_b32 v36, a200
v_mfma_f32_16x16x32_bf16 v[220:223], v[124:127], v[56:59], v[220:223]
buffer_store_dwordx4 v[40:43], v7, s[20:23], s48 offen sc1
v_accvgpr_read_b32 v37, a201
v_mfma_f32_16x16x32_bf16 v[224:227], v[96:99], v[60:63], v[224:227]
ds_read_b128 v[56:59], v24 offset:4096
v_mul_f32 v36, s47, v36
v_mfma_f32_16x16x32_bf16 v[228:231], v[100:103], v[60:63], v[228:231]
buffer_load_dwordx4 v21, s[12:15], s25 offen offset:1024 lds
v_mul_f32 v37, s47, v37
v_mfma_f32_16x16x32_bf16 v[232:235], v[104:107], v[60:63], v[232:235]
v_cvt_pk_bf16_f32 v44, v36, v37
v_accvgpr_read_b32 v38, a202
v_mfma_f32_16x16x32_bf16 v[236:239], v[108:111], v[60:63], v[236:239]
v_accvgpr_read_b32 v39, a203
v_mul_f32 v38, s47, v38
v_mfma_f32_16x16x32_bf16 v[240:243], v[112:115], v[60:63], v[240:243]
v_mul_f32 v39, s47, v39
v_cvt_pk_bf16_f32 v45, v38, v39
v_mfma_f32_16x16x32_bf16 v[244:247], v[116:119], v[60:63], v[244:247]
v_accvgpr_read_b32 v36, a204
v_accvgpr_read_b32 v37, a205
v_mfma_f32_16x16x32_bf16 v[248:251], v[120:123], v[60:63], v[248:251]
v_mul_f32 v36, s47, v36
v_mul_f32 v37, s47, v37
v_mfma_f32_16x16x32_bf16 v[252:255], v[124:127], v[60:63], v[252:255]
s_waitcnt lgkmcnt(0)
v_cvt_pk_bf16_f32 v46, v36, v37
v_accvgpr_read_b32 v38, a206
v_mfma_f32_16x16x32_bf16 v[128:131], v[64:67], v[48:51], v[128:131]
ds_read_b128 v[60:63], v24 offset:6144
v_accvgpr_read_b32 v39, a207
v_mfma_f32_16x16x32_bf16 v[132:135], v[68:71], v[48:51], v[132:135]
v_add_u32 v24, 65536, v24
v_mul_f32 v38, s47, v38
v_mfma_f32_16x16x32_bf16 v[136:139], v[72:75], v[48:51], v[136:139]
ds_read_b64_tr_b16 v[96:97], v26 offset:8192
ds_read_b64_tr_b16 v[98:99], v26 offset:9216
v_mfma_f32_16x16x32_bf16 v[140:143], v[76:79], v[48:51], v[140:143]
buffer_load_dwordx4 v22, s[12:15], s25 offen offset:2048 lds
v_mul_f32 v39, s47, v39
v_mfma_f32_16x16x32_bf16 v[144:147], v[80:83], v[48:51], v[144:147]
ds_read_b64_tr_b16 v[100:101], v27 offset:8192
ds_read_b64_tr_b16 v[102:103], v27 offset:9216
v_mfma_f32_16x16x32_bf16 v[148:151], v[84:87], v[48:51], v[148:151]
v_cvt_pk_bf16_f32 v47, v38, v39
v_accvgpr_write_b32 a196, v44
v_mfma_f32_16x16x32_bf16 v[152:155], v[88:91], v[48:51], v[152:155]
ds_read_b64_tr_b16 v[104:105], v28 offset:8192
ds_read_b64_tr_b16 v[106:107], v28 offset:9216
v_mfma_f32_16x16x32_bf16 v[156:159], v[92:95], v[48:51], v[156:159]
buffer_load_dwordx4 v23, s[12:15], s25 offen offset:3072 lds
v_accvgpr_write_b32 a197, v45
v_mfma_f32_16x16x32_bf16 v[160:163], v[64:67], v[52:55], v[160:163]
ds_read_b128 v[48:51], v25 offset:0
v_accvgpr_write_b32 a198, v46
v_mfma_f32_16x16x32_bf16 v[164:167], v[68:71], v[52:55], v[164:167]
s_add_u32 s24, s24, 128
s_add_u32 s25, s25, 128
v_accvgpr_write_b32 a199, v47
v_mfma_f32_16x16x32_bf16 v[168:171], v[72:75], v[52:55], v[168:171]
s_add_u32 m0, s91, 81920
ds_read_b64_tr_b16 v[108:109], v29 offset:8192
ds_read_b64_tr_b16 v[110:111], v29 offset:9216
v_mfma_f32_16x16x32_bf16 v[172:175], v[76:79], v[52:55], v[172:175]
buffer_load_dwordx4 v16, s[16:19], s26 offen offset:0 lds
v_mfma_f32_16x16x32_bf16 v[176:179], v[80:83], v[52:55], v[176:179]
ds_read_b64_tr_b16 v[112:113], v30 offset:8192
ds_read_b64_tr_b16 v[114:115], v30 offset:9216
v_mfma_f32_16x16x32_bf16 v[180:183], v[84:87], v[52:55], v[180:183]
v_mfma_f32_16x16x32_bf16 v[184:187], v[88:91], v[52:55], v[184:187]
ds_read_b64_tr_b16 v[116:117], v31 offset:8192
ds_read_b64_tr_b16 v[118:119], v31 offset:9216
v_mfma_f32_16x16x32_bf16 v[188:191], v[92:95], v[52:55], v[188:191]
buffer_load_dwordx4 v17, s[16:19], s26 offen offset:1024 lds
v_mfma_f32_16x16x32_bf16 v[192:195], v[64:67], v[56:59], v[192:195]
ds_read_b128 v[52:55], v25 offset:2048
v_mfma_f32_16x16x32_bf16 v[196:199], v[68:71], v[56:59], v[196:199]
v_accvgpr_read_b32 v36, a64
v_accvgpr_read_b32 v37, a65
v_mfma_f32_16x16x32_bf16 v[200:203], v[72:75], v[56:59], v[200:203]
ds_read_b64_tr_b16 v[120:121], v32 offset:8192
ds_read_b64_tr_b16 v[122:123], v32 offset:9216
v_mfma_f32_16x16x32_bf16 v[204:207], v[76:79], v[56:59], v[204:207]
buffer_load_dwordx4 v18, s[16:19], s26 offen offset:2048 lds
v_mul_f32 v36, s47, v36
v_mfma_f32_16x16x32_bf16 v[208:211], v[80:83], v[56:59], v[208:211]
ds_read_b64_tr_b16 v[124:125], v33 offset:8192
ds_read_b64_tr_b16 v[126:127], v33 offset:9216
v_mfma_f32_16x16x32_bf16 v[212:215], v[84:87], v[56:59], v[212:215]
v_mul_f32 v37, s47, v37
v_cvt_pk_bf16_f32 v40, v36, v37
v_mfma_f32_16x16x32_bf16 v[216:219], v[88:91], v[56:59], v[216:219]
s_waitcnt lgkmcnt(15)
v_accvgpr_read_b32 v38, a66
v_accvgpr_read_b32 v39, a67
v_mfma_f32_16x16x32_bf16 v[220:223], v[92:95], v[56:59], v[220:223]
v_mul_f32 v38, s47, v38
v_mul_f32 v39, s47, v39
v_mfma_f32_16x16x32_bf16 v[224:227], v[64:67], v[60:63], v[224:227]
ds_read_b128 v[56:59], v25 offset:4096
v_cvt_pk_bf16_f32 v41, v38, v39
v_mfma_f32_16x16x32_bf16 v[228:231], v[68:71], v[60:63], v[228:231]
buffer_load_dwordx4 v19, s[16:19], s26 offen offset:3072 lds
v_accvgpr_read_b32 v36, a68
v_mfma_f32_16x16x32_bf16 v[232:235], v[72:75], v[60:63], v[232:235]
v_accvgpr_read_b32 v37, a69
v_mul_f32 v36, s47, v36
v_mfma_f32_16x16x32_bf16 v[236:239], v[76:79], v[60:63], v[236:239]
s_add_u32 s26, s26, 524288
v_mul_f32 v37, s47, v37
v_cvt_pk_bf16_f32 v42, v36, v37
v_mfma_f32_16x16x32_bf16 v[240:243], v[80:83], v[60:63], v[240:243]
v_accvgpr_read_b32 v38, a70
v_accvgpr_read_b32 v39, a71
v_mfma_f32_16x16x32_bf16 v[244:247], v[84:87], v[60:63], v[244:247]
v_mul_f32 v38, s47, v38
v_mul_f32 v39, s47, v39
v_mfma_f32_16x16x32_bf16 v[248:251], v[88:91], v[60:63], v[248:251]
v_cvt_pk_bf16_f32 v43, v38, v39
v_accvgpr_read_b32 v36, a208
v_mfma_f32_16x16x32_bf16 v[252:255], v[92:95], v[60:63], v[252:255]
s_waitcnt vmcnt(17) lgkmcnt(0)
s_barrier
s_xor_b32 s48, s95, 131072
v_mfma_f32_16x16x32_bf16 v[128:131], v[96:99], v[48:51], v[128:131]
ds_read_b128 v[60:63], v25 offset:6144
v_accvgpr_read_b32 v37, a209
v_mfma_f32_16x16x32_bf16 v[132:135], v[100:103], v[48:51], v[132:135]
buffer_store_dwordx4 v[40:43], v7, s[20:23], s48 offen sc1
v_add_u32 v25, 65536, v25
v_mfma_f32_16x16x32_bf16 v[136:139], v[104:107], v[48:51], v[136:139]
s_add_u32 m0, s92, 32768
ds_read_b64_tr_b16 v[64:65], v26 offset:16384
ds_read_b64_tr_b16 v[66:67], v26 offset:17408
v_mfma_f32_16x16x32_bf16 v[140:143], v[108:111], v[48:51], v[140:143]
buffer_load_dwordx4 v20, s[12:15], s24 offen offset:0 lds
v_mul_f32 v36, s47, v36
v_mfma_f32_16x16x32_bf16 v[144:147], v[112:115], v[48:51], v[144:147]
ds_read_b64_tr_b16 v[68:69], v27 offset:16384
ds_read_b64_tr_b16 v[70:71], v27 offset:17408
v_mfma_f32_16x16x32_bf16 v[148:151], v[116:119], v[48:51], v[148:151]
v_mul_f32 v37, s47, v37
v_cvt_pk_bf16_f32 v44, v36, v37
v_mfma_f32_16x16x32_bf16 v[152:155], v[120:123], v[48:51], v[152:155]
ds_read_b64_tr_b16 v[72:73], v28 offset:16384
ds_read_b64_tr_b16 v[74:75], v28 offset:17408
v_mfma_f32_16x16x32_bf16 v[156:159], v[124:127], v[48:51], v[156:159]
buffer_load_dwordx4 v21, s[12:15], s24 offen offset:1024 lds
v_accvgpr_read_b32 v38, a210
v_mfma_f32_16x16x32_bf16 v[160:163], v[96:99], v[52:55], v[160:163]
ds_read_b128 v[48:51], v24 offset:0
v_accvgpr_read_b32 v39, a211
v_mfma_f32_16x16x32_bf16 v[164:167], v[100:103], v[52:55], v[164:167]
v_mul_f32 v38, s47, v38
v_mul_f32 v39, s47, v39
v_mfma_f32_16x16x32_bf16 v[168:171], v[104:107], v[52:55], v[168:171]
ds_read_b64_tr_b16 v[76:77], v29 offset:16384
ds_read_b64_tr_b16 v[78:79], v29 offset:17408
v_mfma_f32_16x16x32_bf16 v[172:175], v[108:111], v[52:55], v[172:175]
buffer_load_dwordx4 v22, s[12:15], s24 offen offset:2048 lds
v_cvt_pk_bf16_f32 v45, v38, v39
v_mfma_f32_16x16x32_bf16 v[176:179], v[112:115], v[52:55], v[176:179]
ds_read_b64_tr_b16 v[80:81], v30 offset:16384
ds_read_b64_tr_b16 v[82:83], v30 offset:17408
v_mfma_f32_16x16x32_bf16 v[180:183], v[116:119], v[52:55], v[180:183]
v_accvgpr_read_b32 v36, a212
v_accvgpr_read_b32 v37, a213
v_mfma_f32_16x16x32_bf16 v[184:187], v[120:123], v[52:55], v[184:187]
ds_read_b64_tr_b16 v[84:85], v31 offset:16384
ds_read_b64_tr_b16 v[86:87], v31 offset:17408
v_mfma_f32_16x16x32_bf16 v[188:191], v[124:127], v[52:55], v[188:191]
buffer_load_dwordx4 v23, s[12:15], s24 offen offset:3072 lds
v_mul_f32 v36, s47, v36
v_mfma_f32_16x16x32_bf16 v[192:195], v[96:99], v[56:59], v[192:195]
ds_read_b128 v[52:55], v24 offset:2048
v_mul_f32 v37, s47, v37
v_mfma_f32_16x16x32_bf16 v[196:199], v[100:103], v[56:59], v[196:199]
v_cvt_pk_bf16_f32 v46, v36, v37
v_accvgpr_read_b32 v38, a214
v_mfma_f32_16x16x32_bf16 v[200:203], v[104:107], v[56:59], v[200:203]
s_add_u32 m0, s92, 36864
ds_read_b64_tr_b16 v[88:89], v32 offset:16384
ds_read_b64_tr_b16 v[90:91], v32 offset:17408
v_mfma_f32_16x16x32_bf16 v[204:207], v[108:111], v[56:59], v[204:207]
buffer_load_dwordx4 v20, s[12:15], s25 offen offset:0 lds
v_accvgpr_read_b32 v39, a215
v_mfma_f32_16x16x32_bf16 v[208:211], v[112:115], v[56:59], v[208:211]
ds_read_b64_tr_b16 v[92:93], v33 offset:16384
ds_read_b64_tr_b16 v[94:95], v33 offset:17408
v_mfma_f32_16x16x32_bf16 v[212:215], v[116:119], v[56:59], v[212:215]
v_mul_f32 v38, s47, v38
v_mul_f32 v39, s47, v39
v_mfma_f32_16x16x32_bf16 v[216:219], v[120:123], v[56:59], v[216:219]
s_waitcnt lgkmcnt(15)
v_cvt_pk_bf16_f32 v47, v38, v39
v_accvgpr_write_b32 a200, v44
v_mfma_f32_16x16x32_bf16 v[220:223], v[124:127], v[56:59], v[220:223]
v_accvgpr_write_b32 a201, v45
v_accvgpr_write_b32 a202, v46
v_mfma_f32_16x16x32_bf16 v[224:227], v[96:99], v[60:63], v[224:227]
ds_read_b128 v[56:59], v24 offset:4096
v_accvgpr_write_b32 a203, v47
v_mfma_f32_16x16x32_bf16 v[228:231], v[100:103], v[60:63], v[228:231]
buffer_load_dwordx4 v21, s[12:15], s25 offen offset:1024 lds
v_accvgpr_read_b32 v36, a216
v_mfma_f32_16x16x32_bf16 v[232:235], v[104:107], v[60:63], v[232:235]
v_accvgpr_read_b32 v37, a217
v_mul_f32 v36, s47, v36
v_mfma_f32_16x16x32_bf16 v[236:239], v[108:111], v[60:63], v[236:239]
v_mul_f32 v37, s47, v37
v_cvt_pk_bf16_f32 v40, v36, v37
v_mfma_f32_16x16x32_bf16 v[240:243], v[112:115], v[60:63], v[240:243]
v_accvgpr_read_b32 v38, a218
v_accvgpr_read_b32 v39, a219
v_mfma_f32_16x16x32_bf16 v[244:247], v[116:119], v[60:63], v[244:247]
v_mul_f32 v38, s47, v38
v_mul_f32 v39, s47, v39
v_mfma_f32_16x16x32_bf16 v[248:251], v[120:123], v[60:63], v[248:251]
v_cvt_pk_bf16_f32 v41, v38, v39
v_accvgpr_read_b32 v36, a220
v_mfma_f32_16x16x32_bf16 v[252:255], v[124:127], v[60:63], v[252:255]
s_waitcnt lgkmcnt(0)
v_accvgpr_read_b32 v37, a221
v_mul_f32 v36, s47, v36
v_mfma_f32_16x16x32_bf16 v[128:131], v[64:67], v[48:51], v[128:131]
ds_read_b128 v[60:63], v24 offset:6144
v_mul_f32 v37, s47, v37
v_mfma_f32_16x16x32_bf16 v[132:135], v[68:71], v[48:51], v[132:135]
v_add_u32 v24, 32768, v24
v_cvt_pk_bf16_f32 v42, v36, v37
v_mfma_f32_16x16x32_bf16 v[136:139], v[72:75], v[48:51], v[136:139]
ds_read_b64_tr_b16 v[96:97], v26 offset:24576
ds_read_b64_tr_b16 v[98:99], v26 offset:25600
v_mfma_f32_16x16x32_bf16 v[140:143], v[76:79], v[48:51], v[140:143]
buffer_load_dwordx4 v22, s[12:15], s25 offen offset:2048 lds
v_accvgpr_read_b32 v38, a222
v_mfma_f32_16x16x32_bf16 v[144:147], v[80:83], v[48:51], v[144:147]
ds_read_b64_tr_b16 v[100:101], v27 offset:24576
ds_read_b64_tr_b16 v[102:103], v27 offset:25600
v_mfma_f32_16x16x32_bf16 v[148:151], v[84:87], v[48:51], v[148:151]
v_accvgpr_read_b32 v39, a223
v_mul_f32 v38, s47, v38
v_mfma_f32_16x16x32_bf16 v[152:155], v[88:91], v[48:51], v[152:155]
ds_read_b64_tr_b16 v[104:105], v28 offset:24576
ds_read_b64_tr_b16 v[106:107], v28 offset:25600
v_mfma_f32_16x16x32_bf16 v[156:159], v[92:95], v[48:51], v[156:159]
buffer_load_dwordx4 v23, s[12:15], s25 offen offset:3072 lds
v_mul_f32 v39, s47, v39
v_mfma_f32_16x16x32_bf16 v[160:163], v[64:67], v[52:55], v[160:163]
ds_read_b128 v[48:51], v25 offset:0
v_cvt_pk_bf16_f32 v43, v38, v39
v_mfma_f32_16x16x32_bf16 v[164:167], v[68:71], v[52:55], v[164:167]
s_add_u32 s24, s24, 128
s_add_u32 s25, s25, 128
v_accvgpr_write_b32 a204, v40
v_mfma_f32_16x16x32_bf16 v[168:171], v[72:75], v[52:55], v[168:171]
s_add_u32 m0, s91, 0
ds_read_b64_tr_b16 v[108:109], v29 offset:24576
ds_read_b64_tr_b16 v[110:111], v29 offset:25600
v_mfma_f32_16x16x32_bf16 v[172:175], v[76:79], v[52:55], v[172:175]
buffer_load_dwordx4 v16, s[16:19], s26 offen offset:0 lds
v_accvgpr_write_b32 a205, v41
v_mfma_f32_16x16x32_bf16 v[176:179], v[80:83], v[52:55], v[176:179]
ds_read_b64_tr_b16 v[112:113], v30 offset:24576
ds_read_b64_tr_b16 v[114:115], v30 offset:25600
v_mfma_f32_16x16x32_bf16 v[180:183], v[84:87], v[52:55], v[180:183]
v_accvgpr_write_b32 a206, v42
v_accvgpr_write_b32 a207, v43
v_mfma_f32_16x16x32_bf16 v[184:187], v[88:91], v[52:55], v[184:187]
ds_read_b64_tr_b16 v[116:117], v31 offset:24576
ds_read_b64_tr_b16 v[118:119], v31 offset:25600
v_mfma_f32_16x16x32_bf16 v[188:191], v[92:95], v[52:55], v[188:191]
buffer_load_dwordx4 v17, s[16:19], s26 offen offset:1024 lds
v_accvgpr_read_b32 v36, a72
v_mfma_f32_16x16x32_bf16 v[192:195], v[64:67], v[56:59], v[192:195]
ds_read_b128 v[52:55], v25 offset:2048
v_accvgpr_read_b32 v37, a73
v_mfma_f32_16x16x32_bf16 v[196:199], v[68:71], v[56:59], v[196:199]
v_mul_f32 v36, s47, v36
v_mul_f32 v37, s47, v37
v_mfma_f32_16x16x32_bf16 v[200:203], v[72:75], v[56:59], v[200:203]
ds_read_b64_tr_b16 v[120:121], v32 offset:24576
ds_read_b64_tr_b16 v[122:123], v32 offset:25600
v_mfma_f32_16x16x32_bf16 v[204:207], v[76:79], v[56:59], v[204:207]
buffer_load_dwordx4 v18, s[16:19], s26 offen offset:2048 lds
v_cvt_pk_bf16_f32 v44, v36, v37
v_mfma_f32_16x16x32_bf16 v[208:211], v[80:83], v[56:59], v[208:211]
ds_read_b64_tr_b16 v[124:125], v33 offset:24576
ds_read_b64_tr_b16 v[126:127], v33 offset:25600
v_mfma_f32_16x16x32_bf16 v[212:215], v[84:87], v[56:59], v[212:215]
v_accvgpr_read_b32 v38, a74
v_accvgpr_read_b32 v39, a75
v_mfma_f32_16x16x32_bf16 v[216:219], v[88:91], v[56:59], v[216:219]
s_waitcnt lgkmcnt(15)
v_mul_f32 v38, s47, v38
v_mul_f32 v39, s47, v39
v_mfma_f32_16x16x32_bf16 v[220:223], v[92:95], v[56:59], v[220:223]
v_xor_b32 v26, 0x10000, v26
v_cvt_pk_bf16_f32 v45, v38, v39
v_mfma_f32_16x16x32_bf16 v[224:227], v[64:67], v[60:63], v[224:227]
ds_read_b128 v[56:59], v25 offset:4096
v_xor_b32 v27, 0x10000, v27
v_mfma_f32_16x16x32_bf16 v[228:231], v[68:71], v[60:63], v[228:231]
buffer_load_dwordx4 v19, s[16:19], s26 offen offset:3072 lds
v_xor_b32 v28, 0x10000, v28
v_mfma_f32_16x16x32_bf16 v[232:235], v[72:75], v[60:63], v[232:235]
v_xor_b32 v29, 0x10000, v29
v_accvgpr_read_b32 v36, a76
v_mfma_f32_16x16x32_bf16 v[236:239], v[76:79], v[60:63], v[236:239]
s_add_u32 s26, s26, 524288
v_xor_b32 v30, 0x10000, v30
v_accvgpr_read_b32 v37, a77
v_mfma_f32_16x16x32_bf16 v[240:243], v[80:83], v[60:63], v[240:243]
v_xor_b32 v31, 0x10000, v31
v_mul_f32 v36, s47, v36
v_mfma_f32_16x16x32_bf16 v[244:247], v[84:87], v[60:63], v[244:247]
v_xor_b32 v32, 0x10000, v32
v_mul_f32 v37, s47, v37
v_mfma_f32_16x16x32_bf16 v[248:251], v[88:91], v[60:63], v[248:251]
v_xor_b32 v33, 0x10000, v33
v_cvt_pk_bf16_f32 v46, v36, v37
v_mfma_f32_16x16x32_bf16 v[252:255], v[92:95], v[60:63], v[252:255]
s_waitcnt vmcnt(17) lgkmcnt(0)
s_barrier
v_accvgpr_read_b32 v38, a78
v_mfma_f32_16x16x32_bf16 v[128:131], v[96:99], v[48:51], v[128:131]
ds_read_b128 v[60:63], v25 offset:6144
v_accvgpr_read_b32 v39, a79
v_mfma_f32_16x16x32_bf16 v[132:135], v[100:103], v[48:51], v[132:135]
v_add_u32 v25, 32768, v25
v_mul_f32 v38, s47, v38
v_mfma_f32_16x16x32_bf16 v[136:139], v[104:107], v[48:51], v[136:139]
s_add_u32 m0, s92, 98304
ds_read_b64_tr_b16 v[64:65], v26 offset:0
ds_read_b64_tr_b16 v[66:67], v26 offset:1024
v_mfma_f32_16x16x32_bf16 v[140:143], v[108:111], v[48:51], v[140:143]
buffer_load_dwordx4 v20, s[12:15], s24 offen offset:0 lds
v_mul_f32 v39, s47, v39
v_mfma_f32_16x16x32_bf16 v[144:147], v[112:115], v[48:51], v[144:147]
ds_read_b64_tr_b16 v[68:69], v27 offset:0
ds_read_b64_tr_b16 v[70:71], v27 offset:1024
v_mfma_f32_16x16x32_bf16 v[148:151], v[116:119], v[48:51], v[148:151]
v_cvt_pk_bf16_f32 v47, v38, v39
v_accvgpr_read_b32 v36, a224
v_mfma_f32_16x16x32_bf16 v[152:155], v[120:123], v[48:51], v[152:155]
ds_read_b64_tr_b16 v[72:73], v28 offset:0
ds_read_b64_tr_b16 v[74:75], v28 offset:1024
v_mfma_f32_16x16x32_bf16 v[156:159], v[124:127], v[48:51], v[156:159]
s_xor_b32 s48, s95, 132096
buffer_load_dwordx4 v21, s[12:15], s24 offen offset:1024 lds
v_mfma_f32_16x16x32_bf16 v[160:163], v[96:99], v[52:55], v[160:163]
ds_read_b128 v[48:51], v24 offset:0
v_accvgpr_read_b32 v37, a225
v_mfma_f32_16x16x32_bf16 v[164:167], v[100:103], v[52:55], v[164:167]
buffer_store_dwordx4 v[44:47], v7, s[20:23], s48 offen sc1
v_mul_f32 v36, s47, v36
v_mfma_f32_16x16x32_bf16 v[168:171], v[104:107], v[52:55], v[168:171]
ds_read_b64_tr_b16 v[76:77], v29 offset:0
ds_read_b64_tr_b16 v[78:79], v29 offset:1024
v_mfma_f32_16x16x32_bf16 v[172:175], v[108:111], v[52:55], v[172:175]
buffer_load_dwordx4 v22, s[12:15], s24 offen offset:2048 lds
v_mul_f32 v37, s47, v37
v_mfma_f32_16x16x32_bf16 v[176:179], v[112:115], v[52:55], v[176:179]
ds_read_b64_tr_b16 v[80:81], v30 offset:0
ds_read_b64_tr_b16 v[82:83], v30 offset:1024
v_mfma_f32_16x16x32_bf16 v[180:183], v[116:119], v[52:55], v[180:183]
v_cvt_pk_bf16_f32 v40, v36, v37
v_accvgpr_read_b32 v38, a226
v_mfma_f32_16x16x32_bf16 v[184:187], v[120:123], v[52:55], v[184:187]
ds_read_b64_tr_b16 v[84:85], v31 offset:0
ds_read_b64_tr_b16 v[86:87], v31 offset:1024
v_mfma_f32_16x16x32_bf16 v[188:191], v[124:127], v[52:55], v[188:191]
buffer_load_dwordx4 v23, s[12:15], s24 offen offset:3072 lds
v_accvgpr_read_b32 v39, a227
v_mfma_f32_16x16x32_bf16 v[192:195], v[96:99], v[56:59], v[192:195]
ds_read_b128 v[52:55], v24 offset:2048
v_mul_f32 v38, s47, v38
v_mfma_f32_16x16x32_bf16 v[196:199], v[100:103], v[56:59], v[196:199]
v_mul_f32 v39, s47, v39
v_cvt_pk_bf16_f32 v41, v38, v39
v_mfma_f32_16x16x32_bf16 v[200:203], v[104:107], v[56:59], v[200:203]
s_add_u32 m0, s92, 102400
ds_read_b64_tr_b16 v[88:89], v32 offset:0
ds_read_b64_tr_b16 v[90:91], v32 offset:1024
v_mfma_f32_16x16x32_bf16 v[204:207], v[108:111], v[56:59], v[204:207]
buffer_load_dwordx4 v20, s[12:15], s25 offen offset:0 lds
v_accvgpr_read_b32 v36, a228
v_mfma_f32_16x16x32_bf16 v[208:211], v[112:115], v[56:59], v[208:211]
ds_read_b64_tr_b16 v[92:93], v33 offset:0
ds_read_b64_tr_b16 v[94:95], v33 offset:1024
v_mfma_f32_16x16x32_bf16 v[212:215], v[116:119], v[56:59], v[212:215]
v_accvgpr_read_b32 v37, a229
v_mul_f32 v36, s47, v36
v_mfma_f32_16x16x32_bf16 v[216:219], v[120:123], v[56:59], v[216:219]
s_waitcnt lgkmcnt(15)
v_mul_f32 v37, s47, v37
v_cvt_pk_bf16_f32 v42, v36, v37
v_mfma_f32_16x16x32_bf16 v[220:223], v[124:127], v[56:59], v[220:223]
v_accvgpr_read_b32 v38, a230
v_accvgpr_read_b32 v39, a231
v_mfma_f32_16x16x32_bf16 v[224:227], v[96:99], v[60:63], v[224:227]
ds_read_b128 v[56:59], v24 offset:4096
v_mul_f32 v38, s47, v38
v_mfma_f32_16x16x32_bf16 v[228:231], v[100:103], v[60:63], v[228:231]
buffer_load_dwordx4 v21, s[12:15], s25 offen offset:1024 lds
v_mul_f32 v39, s47, v39
v_mfma_f32_16x16x32_bf16 v[232:235], v[104:107], v[60:63], v[232:235]
v_cvt_pk_bf16_f32 v43, v38, v39
v_accvgpr_write_b32 a224, v40
v_mfma_f32_16x16x32_bf16 v[236:239], v[108:111], v[60:63], v[236:239]
v_accvgpr_write_b32 a225, v41
v_accvgpr_write_b32 a226, v42
v_mfma_f32_16x16x32_bf16 v[240:243], v[112:115], v[60:63], v[240:243]
v_accvgpr_write_b32 a227, v43
v_accvgpr_read_b32 v36, a80
v_mfma_f32_16x16x32_bf16 v[244:247], v[116:119], v[60:63], v[244:247]
v_accvgpr_read_b32 v37, a81
v_mul_f32 v36, s47, v36
v_mfma_f32_16x16x32_bf16 v[248:251], v[120:123], v[60:63], v[248:251]
v_mul_f32 v37, s47, v37
v_cvt_pk_bf16_f32 v44, v36, v37
v_mfma_f32_16x16x32_bf16 v[252:255], v[124:127], v[60:63], v[252:255]
s_waitcnt lgkmcnt(0)
v_accvgpr_read_b32 v38, a82
v_accvgpr_read_b32 v39, a83
v_mfma_f32_16x16x32_bf16 v[128:131], v[64:67], v[48:51], v[128:131]
ds_read_b128 v[60:63], v24 offset:6144
v_mul_f32 v38, s47, v38
v_mfma_f32_16x16x32_bf16 v[132:135], v[68:71], v[48:51], v[132:135]
v_add_u32 v24, 4294868992, v24
v_mul_f32 v39, s47, v39
v_mfma_f32_16x16x32_bf16 v[136:139], v[72:75], v[48:51], v[136:139]
ds_read_b64_tr_b16 v[96:97], v26 offset:8192
ds_read_b64_tr_b16 v[98:99], v26 offset:9216
v_mfma_f32_16x16x32_bf16 v[140:143], v[76:79], v[48:51], v[140:143]
s_lshl_b32 s28, s88, 19
buffer_load_dwordx4 v22, s[12:15], s25 offen offset:2048 lds
v_mfma_f32_16x16x32_bf16 v[144:147], v[80:83], v[48:51], v[144:147]
ds_read_b64_tr_b16 v[100:101], v27 offset:8192
ds_read_b64_tr_b16 v[102:103], v27 offset:9216
v_mfma_f32_16x16x32_bf16 v[148:151], v[84:87], v[48:51], v[148:151]
s_add_u32 s28, s28, s91
v_cvt_pk_bf16_f32 v45, v38, v39
v_accvgpr_read_b32 v36, a84
v_mfma_f32_16x16x32_bf16 v[152:155], v[88:91], v[48:51], v[152:155]
ds_read_b64_tr_b16 v[104:105], v28 offset:8192
ds_read_b64_tr_b16 v[106:107], v28 offset:9216
v_mfma_f32_16x16x32_bf16 v[156:159], v[92:95], v[48:51], v[156:159]
s_lshl_b32 s29, s89, 15
buffer_load_dwordx4 v23, s[12:15], s25 offen offset:3072 lds
v_mfma_f32_16x16x32_bf16 v[160:163], v[64:67], v[52:55], v[160:163]
s_sub_u32 s54, 0x80000, s29
ds_read_b128 v[48:51], v25 offset:0
v_mfma_f32_16x16x32_bf16 v[164:167], v[68:71], v[52:55], v[164:167]
s_add_u32 s24, s24, 128
s_add_u32 s25, s25, 128
v_accvgpr_read_b32 v37, a85
v_mfma_f32_16x16x32_bf16 v[168:171], v[72:75], v[52:55], v[168:171]
s_add_u32 m0, s91, 16384
ds_read_b64_tr_b16 v[108:109], v29 offset:8192
ds_read_b64_tr_b16 v[110:111], v29 offset:9216
v_mfma_f32_16x16x32_bf16 v[172:175], v[76:79], v[52:55], v[172:175]
s_sub_u32 s29, s54, s91
buffer_load_dwordx4 v16, s[16:19], s26 offen offset:0 lds
v_mfma_f32_16x16x32_bf16 v[176:179], v[80:83], v[52:55], v[176:179]
ds_read_b64_tr_b16 v[112:113], v30 offset:8192
ds_read_b64_tr_b16 v[114:115], v30 offset:9216
v_mfma_f32_16x16x32_bf16 v[180:183], v[84:87], v[52:55], v[180:183]
s_add_u32 s4, s84, s28
s_addc_u32 s5, s85, 0
v_mul_f32 v36, s47, v36
v_mfma_f32_16x16x32_bf16 v[184:187], v[88:91], v[52:55], v[184:187]
ds_read_b64_tr_b16 v[116:117], v31 offset:8192
ds_read_b64_tr_b16 v[118:119], v31 offset:9216
v_mfma_f32_16x16x32_bf16 v[188:191], v[92:95], v[52:55], v[188:191]
s_and_b32 s5, s5, 0xffff
buffer_load_dwordx4 v17, s[16:19], s26 offen offset:1024 lds
v_mfma_f32_16x16x32_bf16 v[192:195], v[64:67], v[56:59], v[192:195]
s_mov_b32 s6, s29
ds_read_b128 v[52:55], v25 offset:2048
v_mfma_f32_16x16x32_bf16 v[196:199], v[68:71], v[56:59], v[196:199]
s_mov_b32 s7, 0x20000
v_mul_f32 v37, s47, v37
v_cvt_pk_bf16_f32 v46, v36, v37
v_mfma_f32_16x16x32_bf16 v[200:203], v[72:75], v[56:59], v[200:203]
ds_read_b64_tr_b16 v[120:121], v32 offset:8192
ds_read_b64_tr_b16 v[122:123], v32 offset:9216
v_mfma_f32_16x16x32_bf16 v[204:207], v[76:79], v[56:59], v[204:207]
s_add_u32 s28, s28, 16760832
buffer_load_dwordx4 v18, s[16:19], s26 offen offset:2048 lds
v_mfma_f32_16x16x32_bf16 v[208:211], v[80:83], v[56:59], v[208:211]
ds_read_b64_tr_b16 v[124:125], v33 offset:8192
ds_read_b64_tr_b16 v[126:127], v33 offset:9216
v_mfma_f32_16x16x32_bf16 v[212:215], v[84:87], v[56:59], v[212:215]
s_add_u32 s29, s29, 16384
v_accvgpr_read_b32 v38, a86
v_accvgpr_read_b32 v39, a87
v_mfma_f32_16x16x32_bf16 v[216:219], v[88:91], v[56:59], v[216:219]
s_waitcnt lgkmcnt(15)
s_add_u32 s45, s54, 16384
v_mul_f32 v38, s47, v38
v_mfma_f32_16x16x32_bf16 v[220:223], v[92:95], v[56:59], v[220:223]
s_add_u32 s8, s84, s28
s_addc_u32 s9, s85, 0
v_mul_f32 v39, s47, v39
v_mfma_f32_16x16x32_bf16 v[224:227], v[64:67], v[60:63], v[224:227]
s_and_b32 s9, s9, 0xffff
ds_read_b128 v[56:59], v25 offset:4096
v_mfma_f32_16x16x32_bf16 v[228:231], v[68:71], v[60:63], v[228:231]
s_mov_b32 s10, s29
buffer_load_dwordx4 v19, s[16:19], s26 offen offset:3072 lds
v_mfma_f32_16x16x32_bf16 v[232:235], v[72:75], v[60:63], v[232:235]
s_mov_b32 s11, 0x20000
v_cvt_pk_bf16_f32 v47, v38, v39
v_accvgpr_read_b32 v36, a232
v_mfma_f32_16x16x32_bf16 v[236:239], v[76:79], v[60:63], v[236:239]
s_add_u32 s26, s26, 524288
s_xor_b32 s48, s95, 133120
v_accvgpr_read_b32 v37, a233
v_mfma_f32_16x16x32_bf16 v[240:243], v[80:83], v[60:63], v[240:243]
.Lpad7:
s_cbranch_execz .Ltch2
s_and_b32 s46, s89, 1
buffer_store_dwordx4 v[44:47], v7, s[20:23], s48 offen sc1
v_mfma_f32_16x16x32_bf16 v[244:247], v[84:87], v[60:63], v[244:247]
s_lshl_b32 s46, s46, 15
v_mul_f32 v36, s47, v36
v_mul_f32 v37, s47, v37
v_mfma_f32_16x16x32_bf16 v[248:251], v[88:91], v[60:63], v[248:251]
s_add_u32 s46, s46, s91
v_cvt_pk_bf16_f32 v40, v36, v37
v_accvgpr_read_b32 v38, a234
v_mfma_f32_16x16x32_bf16 v[252:255], v[92:95], v[60:63], v[252:255]
s_waitcnt vmcnt(34)
s_waitcnt vmcnt(18) lgkmcnt(0)
s_barrier
s_mov_b32 s39, s46
v_mfma_f32_16x16x32_bf16 v[128:131], v[96:99], v[48:51], v[128:131]
s_mov_b32 s27, 0x30000
ds_read_b128 v[60:63], v25 offset:6144
v_mfma_f32_16x16x32_bf16 v[132:135], v[100:103], v[48:51], v[132:135]
s_nop 0
v_add_u32 v25, 4294868992, v25
v_accvgpr_read_b32 v39, a235
v_or_b32 v0, 1, v11
v_mfma_f32_16x16x32_bf16 v[136:139], v[104:107], v[48:51], v[136:139]
s_add_u32 m0, s92, 131072
ds_read_b64_tr_b16 v[64:65], v26 offset:16384
ds_read_b64_tr_b16 v[66:67], v26 offset:17408
v_mfma_f32_16x16x32_bf16 v[140:143], v[108:111], v[48:51], v[140:143]
buffer_load_dwordx4 v20, s[12:15], s24 offen offset:0 lds
v_mul_f32 v38, s47, v38
v_mfma_f32_16x16x32_bf16 v[144:147], v[112:115], v[48:51], v[144:147]
ds_read_b64_tr_b16 v[68:69], v27 offset:16384
ds_read_b64_tr_b16 v[70:71], v27 offset:17408
v_mfma_f32_16x16x32_bf16 v[148:151], v[116:119], v[48:51], v[148:151]
buffer_store_dword v0, v6, s[60:63], 0 offen sc1
v_mul_f32 v39, s47, v39
v_cvt_pk_bf16_f32 v41, v38, v39
v_mfma_f32_16x16x32_bf16 v[152:155], v[120:123], v[48:51], v[152:155]
ds_read_b64_tr_b16 v[72:73], v28 offset:16384
ds_read_b64_tr_b16 v[74:75], v28 offset:17408
v_mfma_f32_16x16x32_bf16 v[156:159], v[124:127], v[48:51], v[156:159]
buffer_load_dwordx4 v21, s[12:15], s24 offen offset:1024 lds
v_accvgpr_read_b32 v36, a236
v_mfma_f32_16x16x32_bf16 v[160:163], v[96:99], v[52:55], v[160:163]
ds_read_b128 v[48:51], v24 offset:0
v_accvgpr_read_b32 v37, a237
v_mfma_f32_16x16x32_bf16 v[164:167], v[100:103], v[52:55], v[164:167]
v_mul_f32 v36, s47, v36
v_mul_f32 v37, s47, v37
v_mfma_f32_16x16x32_bf16 v[168:171], v[104:107], v[52:55], v[168:171]
ds_read_b64_tr_b16 v[76:77], v29 offset:16384
ds_read_b64_tr_b16 v[78:79], v29 offset:17408
v_mfma_f32_16x16x32_bf16 v[172:175], v[108:111], v[52:55], v[172:175]
buffer_load_dwordx4 v22, s[12:15], s24 offen offset:2048 lds
v_cvt_pk_bf16_f32 v42, v36, v37
v_mfma_f32_16x16x32_bf16 v[176:179], v[112:115], v[52:55], v[176:179]
ds_read_b64_tr_b16 v[80:81], v30 offset:16384
ds_read_b64_tr_b16 v[82:83], v30 offset:17408
v_mfma_f32_16x16x32_bf16 v[180:183], v[116:119], v[52:55], v[180:183]
v_accvgpr_read_b32 v38, a238
v_accvgpr_read_b32 v39, a239
v_mfma_f32_16x16x32_bf16 v[184:187], v[120:123], v[52:55], v[184:187]
ds_read_b64_tr_b16 v[84:85], v31 offset:16384
ds_read_b64_tr_b16 v[86:87], v31 offset:17408
v_mfma_f32_16x16x32_bf16 v[188:191], v[124:127], v[52:55], v[188:191]
buffer_load_dwordx4 v23, s[12:15], s24 offen offset:3072 lds
v_mul_f32 v38, s47, v38
v_mfma_f32_16x16x32_bf16 v[192:195], v[96:99], v[56:59], v[192:195]
ds_read_b128 v[52:55], v24 offset:2048
v_mul_f32 v39, s47, v39
v_mfma_f32_16x16x32_bf16 v[196:199], v[100:103], v[56:59], v[196:199]
v_cvt_pk_bf16_f32 v43, v38, v39
v_accvgpr_write_b32 a228, v40
v_mfma_f32_16x16x32_bf16 v[200:203], v[104:107], v[56:59], v[200:203]
s_add_u32 m0, s92, 135168
ds_read_b64_tr_b16 v[88:89], v32 offset:16384
ds_read_b64_tr_b16 v[90:91], v32 offset:17408
v_mfma_f32_16x16x32_bf16 v[204:207], v[108:111], v[56:59], v[204:207]
buffer_load_dwordx4 v20, s[12:15], s25 offen offset:0 lds
v_accvgpr_write_b32 a229, v41
v_mfma_f32_16x16x32_bf16 v[208:211], v[112:115], v[56:59], v[208:211]
ds_read_b64_tr_b16 v[92:93], v33 offset:16384
ds_read_b64_tr_b16 v[94:95], v33 offset:17408
v_mfma_f32_16x16x32_bf16 v[212:215], v[116:119], v[56:59], v[212:215]
v_accvgpr_write_b32 a230, v42
v_accvgpr_write_b32 a231, v43
v_mfma_f32_16x16x32_bf16 v[216:219], v[120:123], v[56:59], v[216:219]
s_waitcnt lgkmcnt(15)
v_accvgpr_read_b32 v36, a88
v_accvgpr_read_b32 v37, a89
v_mfma_f32_16x16x32_bf16 v[220:223], v[124:127], v[56:59], v[220:223]
v_mul_f32 v36, s47, v36
v_mul_f32 v37, s47, v37
v_mfma_f32_16x16x32_bf16 v[224:227], v[96:99], v[60:63], v[224:227]
ds_read_b128 v[56:59], v24 offset:4096
v_cvt_pk_bf16_f32 v44, v36, v37
v_mfma_f32_16x16x32_bf16 v[228:231], v[100:103], v[60:63], v[228:231]
buffer_load_dwordx4 v21, s[12:15], s25 offen offset:1024 lds
v_accvgpr_read_b32 v38, a90
v_mfma_f32_16x16x32_bf16 v[232:235], v[104:107], v[60:63], v[232:235]
v_accvgpr_read_b32 v39, a91
v_mul_f32 v38, s47, v38
v_mfma_f32_16x16x32_bf16 v[236:239], v[108:111], v[60:63], v[236:239]
v_mul_f32 v39, s47, v39
v_cvt_pk_bf16_f32 v45, v38, v39
v_mfma_f32_16x16x32_bf16 v[240:243], v[112:115], v[60:63], v[240:243]
v_accvgpr_read_b32 v36, a92
v_accvgpr_read_b32 v37, a93
v_mfma_f32_16x16x32_bf16 v[244:247], v[116:119], v[60:63], v[244:247]
v_mul_f32 v36, s47, v36
v_mul_f32 v37, s47, v37
v_mfma_f32_16x16x32_bf16 v[248:251], v[120:123], v[60:63], v[248:251]
v_cvt_pk_bf16_f32 v46, v36, v37
v_accvgpr_read_b32 v38, a94
v_mfma_f32_16x16x32_bf16 v[252:255], v[124:127], v[60:63], v[252:255]
s_waitcnt lgkmcnt(0)
v_accvgpr_read_b32 v39, a95
v_mul_f32 v38, s47, v38
v_mfma_f32_16x16x32_bf16 v[128:131], v[64:67], v[48:51], v[128:131]
ds_read_b128 v[60:63], v24 offset:6144
v_mul_f32 v39, s47, v39
v_mfma_f32_16x16x32_bf16 v[132:135], v[68:71], v[48:51], v[132:135]
v_add_u32 v24, 65536, v24
v_cvt_pk_bf16_f32 v47, v38, v39
v_mfma_f32_16x16x32_bf16 v[136:139], v[72:75], v[48:51], v[136:139]
ds_read_b64_tr_b16 v[96:97], v26 offset:24576
ds_read_b64_tr_b16 v[98:99], v26 offset:25600
v_mfma_f32_16x16x32_bf16 v[140:143], v[76:79], v[48:51], v[140:143]
s_xor_b32 s48, s95, 134144
buffer_load_dwordx4 v22, s[12:15], s25 offen offset:2048 lds
v_mfma_f32_16x16x32_bf16 v[144:147], v[80:83], v[48:51], v[144:147]
ds_read_b64_tr_b16 v[100:101], v27 offset:24576
ds_read_b64_tr_b16 v[102:103], v27 offset:25600
v_mfma_f32_16x16x32_bf16 v[148:151], v[84:87], v[48:51], v[148:151]
buffer_store_dwordx4 v[44:47], v7, s[20:23], s48 offen sc1
v_accvgpr_read_b32 v36, a240
v_mfma_f32_16x16x32_bf16 v[152:155], v[88:91], v[48:51], v[152:155]
ds_read_b64_tr_b16 v[104:105], v28 offset:24576
ds_read_b64_tr_b16 v[106:107], v28 offset:25600
v_mfma_f32_16x16x32_bf16 v[156:159], v[92:95], v[48:51], v[156:159]
buffer_load_dwordx4 v23, s[12:15], s25 offen offset:3072 lds
v_accvgpr_read_b32 v37, a241
v_mfma_f32_16x16x32_bf16 v[160:163], v[64:67], v[52:55], v[160:163]
ds_read_b128 v[48:51], v25 offset:0
v_mul_f32 v36, s47, v36
v_mfma_f32_16x16x32_bf16 v[164:167], v[68:71], v[52:55], v[164:167]
s_add_u32 s24, s24, 128
s_add_u32 s25, s25, 128
buffer_load_dword v35, v4, s[96:99], 0 offen
v_mfma_f32_16x16x32_bf16 v[168:171], v[72:75], v[52:55], v[168:171]
s_add_u32 m0, s91, 65536
ds_read_b64_tr_b16 v[108:109], v29 offset:24576
ds_read_b64_tr_b16 v[110:111], v29 offset:25600
v_mfma_f32_16x16x32_bf16 v[172:175], v[76:79], v[52:55], v[172:175]
buffer_load_dwordx4 v16, s[16:19], s26 offen offset:0 lds
v_mul_f32 v37, s47, v37
v_mfma_f32_16x16x32_bf16 v[176:179], v[80:83], v[52:55], v[176:179]
ds_read_b64_tr_b16 v[112:113], v30 offset:24576
ds_read_b64_tr_b16 v[114:115], v30 offset:25600
v_mfma_f32_16x16x32_bf16 v[180:183], v[84:87], v[52:55], v[180:183]
v_cvt_pk_bf16_f32 v40, v36, v37
v_accvgpr_read_b32 v38, a242
v_mfma_f32_16x16x32_bf16 v[184:187], v[88:91], v[52:55], v[184:187]
ds_read_b64_tr_b16 v[116:117], v31 offset:24576
ds_read_b64_tr_b16 v[118:119], v31 offset:25600
v_mfma_f32_16x16x32_bf16 v[188:191], v[92:95], v[52:55], v[188:191]
buffer_load_dwordx4 v17, s[16:19], s26 offen offset:1024 lds
v_accvgpr_read_b32 v39, a243
v_mfma_f32_16x16x32_bf16 v[192:195], v[64:67], v[56:59], v[192:195]
ds_read_b128 v[52:55], v25 offset:2048
v_mul_f32 v38, s47, v38
v_mfma_f32_16x16x32_bf16 v[196:199], v[68:71], v[56:59], v[196:199]
v_mul_f32 v39, s47, v39
v_cvt_pk_bf16_f32 v41, v38, v39
v_mfma_f32_16x16x32_bf16 v[200:203], v[72:75], v[56:59], v[200:203]
ds_read_b64_tr_b16 v[120:121], v32 offset:24576
ds_read_b64_tr_b16 v[122:123], v32 offset:25600
v_mfma_f32_16x16x32_bf16 v[204:207], v[76:79], v[56:59], v[204:207]
buffer_load_dwordx4 v18, s[16:19], s26 offen offset:2048 lds
v_accvgpr_read_b32 v36, a244
v_mfma_f32_16x16x32_bf16 v[208:211], v[80:83], v[56:59], v[208:211]
ds_read_b64_tr_b16 v[124:125], v33 offset:24576
ds_read_b64_tr_b16 v[126:127], v33 offset:25600
v_mfma_f32_16x16x32_bf16 v[212:215], v[84:87], v[56:59], v[212:215]
v_accvgpr_read_b32 v37, a245
v_mul_f32 v36, s47, v36
v_mfma_f32_16x16x32_bf16 v[216:219], v[88:91], v[56:59], v[216:219]
s_waitcnt lgkmcnt(15)
v_mul_f32 v37, s47, v37
v_cvt_pk_bf16_f32 v42, v36, v37
v_mfma_f32_16x16x32_bf16 v[220:223], v[92:95], v[56:59], v[220:223]
v_xor_b32 v26, 0x10000, v26
v_accvgpr_read_b32 v38, a246
v_mfma_f32_16x16x32_bf16 v[224:227], v[64:67], v[60:63], v[224:227]
ds_read_b128 v[56:59], v25 offset:4096
v_xor_b32 v27, 0x10000, v27
v_mfma_f32_16x16x32_bf16 v[228:231], v[68:71], v[60:63], v[228:231]
buffer_load_dwordx4 v19, s[16:19], s26 offen offset:3072 lds
v_xor_b32 v28, 0x10000, v28
v_mfma_f32_16x16x32_bf16 v[232:235], v[72:75], v[60:63], v[232:235]
v_xor_b32 v29, 0x10000, v29
v_accvgpr_read_b32 v39, a247
v_mfma_f32_16x16x32_bf16 v[236:239], v[76:79], v[60:63], v[236:239]
s_add_u32 s26, s26, 524288
v_xor_b32 v30, 0x10000, v30
v_mul_f32 v38, s47, v38
v_mfma_f32_16x16x32_bf16 v[240:243], v[80:83], v[60:63], v[240:243]
v_xor_b32 v31, 0x10000, v31
v_mul_f32 v39, s47, v39
v_mfma_f32_16x16x32_bf16 v[244:247], v[84:87], v[60:63], v[244:247]
v_xor_b32 v32, 0x10000, v32
v_cvt_pk_bf16_f32 v43, v38, v39
v_mfma_f32_16x16x32_bf16 v[248:251], v[88:91], v[60:63], v[248:251]
v_xor_b32 v33, 0x10000, v33
v_accvgpr_write_b32 a232, v40
v_mfma_f32_16x16x32_bf16 v[252:255], v[92:95], v[60:63], v[252:255]
s_waitcnt vmcnt(20) lgkmcnt(0)
s_barrier
v_accvgpr_write_b32 a233, v41
v_mfma_f32_16x16x32_bf16 v[128:131], v[96:99], v[48:51], v[128:131]
ds_read_b128 v[60:63], v25 offset:6144
v_accvgpr_write_b32 a234, v42
v_mfma_f32_16x16x32_bf16 v[132:135], v[100:103], v[48:51], v[132:135]
v_add_u32 v25, 65536, v25
v_accvgpr_write_b32 a235, v43
v_mfma_f32_16x16x32_bf16 v[136:139], v[104:107], v[48:51], v[136:139]
s_add_u32 m0, s92, 32768
ds_read_b64_tr_b16 v[64:65], v26 offset:0
ds_read_b64_tr_b16 v[66:67], v26 offset:1024
v_mfma_f32_16x16x32_bf16 v[140:143], v[108:111], v[48:51], v[140:143]
buffer_load_dwordx4 v20, s[12:15], s24 offen offset:0 lds
v_accvgpr_read_b32 v36, a96
v_mfma_f32_16x16x32_bf16 v[144:147], v[112:115], v[48:51], v[144:147]
ds_read_b64_tr_b16 v[68:69], v27 offset:0
ds_read_b64_tr_b16 v[70:71], v27 offset:1024
v_mfma_f32_16x16x32_bf16 v[148:151], v[116:119], v[48:51], v[148:151]
v_accvgpr_read_b32 v37, a97
v_mul_f32 v36, s47, v36
v_mfma_f32_16x16x32_bf16 v[152:155], v[120:123], v[48:51], v[152:155]
ds_read_b64_tr_b16 v[72:73], v28 offset:0
ds_read_b64_tr_b16 v[74:75], v28 offset:1024
v_mfma_f32_16x16x32_bf16 v[156:159], v[124:127], v[48:51], v[156:159]
buffer_load_dwordx4 v21, s[12:15], s24 offen offset:1024 lds
v_mul_f32 v37, s47, v37
v_mfma_f32_16x16x32_bf16 v[160:163], v[96:99], v[52:55], v[160:163]
ds_read_b128 v[48:51], v24 offset:0
v_cvt_pk_bf16_f32 v44, v36, v37
v_mfma_f32_16x16x32_bf16 v[164:167], v[100:103], v[52:55], v[164:167]
v_accvgpr_read_b32 v38, a98
v_accvgpr_read_b32 v39, a99
v_mfma_f32_16x16x32_bf16 v[168:171], v[104:107], v[52:55], v[168:171]
ds_read_b64_tr_b16 v[76:77], v29 offset:0
ds_read_b64_tr_b16 v[78:79], v29 offset:1024
v_mfma_f32_16x16x32_bf16 v[172:175], v[108:111], v[52:55], v[172:175]
buffer_load_dwordx4 v22, s[12:15], s24 offen offset:2048 lds
v_mul_f32 v38, s47, v38
v_mfma_f32_16x16x32_bf16 v[176:179], v[112:115], v[52:55], v[176:179]
ds_read_b64_tr_b16 v[80:81], v30 offset:0
ds_read_b64_tr_b16 v[82:83], v30 offset:1024
v_mfma_f32_16x16x32_bf16 v[180:183], v[116:119], v[52:55], v[180:183]
v_mul_f32 v39, s47, v39
v_cvt_pk_bf16_f32 v45, v38, v39
v_mfma_f32_16x16x32_bf16 v[184:187], v[120:123], v[52:55], v[184:187]
ds_read_b64_tr_b16 v[84:85], v31 offset:0
ds_read_b64_tr_b16 v[86:87], v31 offset:1024
v_mfma_f32_16x16x32_bf16 v[188:191], v[124:127], v[52:55], v[188:191]
buffer_load_dwordx4 v23, s[12:15], s24 offen offset:3072 lds
v_accvgpr_read_b32 v36, a100
v_mfma_f32_16x16x32_bf16 v[192:195], v[96:99], v[56:59], v[192:195]
ds_read_b128 v[52:55], v24 offset:2048
v_accvgpr_read_b32 v37, a101
v_mfma_f32_16x16x32_bf16 v[196:199], v[100:103], v[56:59], v[196:199]
v_mul_f32 v36, s47, v36
v_mul_f32 v37, s47, v37
v_mfma_f32_16x16x32_bf16 v[200:203], v[104:107], v[56:59], v[200:203]
s_add_u32 m0, s92, 36864
ds_read_b64_tr_b16 v[88:89], v32 offset:0
ds_read_b64_tr_b16 v[90:91], v32 offset:1024
v_mfma_f32_16x16x32_bf16 v[204:207], v[108:111], v[56:59], v[204:207]
buffer_load_dwordx4 v20, s[12:15], s25 offen offset:0 lds
v_cvt_pk_bf16_f32 v46, v36, v37
v_mfma_f32_16x16x32_bf16 v[208:211], v[112:115], v[56:59], v[208:211]
ds_read_b64_tr_b16 v[92:93], v33 offset:0
ds_read_b64_tr_b16 v[94:95], v33 offset:1024
v_mfma_f32_16x16x32_bf16 v[212:215], v[116:119], v[56:59], v[212:215]
v_accvgpr_read_b32 v38, a102
v_accvgpr_read_b32 v39, a103
v_mfma_f32_16x16x32_bf16 v[216:219], v[120:123], v[56:59], v[216:219]
s_waitcnt lgkmcnt(15)
v_mul_f32 v38, s47, v38
v_mul_f32 v39, s47, v39
v_mfma_f32_16x16x32_bf16 v[220:223], v[124:127], v[56:59], v[220:223]
v_cvt_pk_bf16_f32 v47, v38, v39
v_accvgpr_read_b32 v36, a248
v_mfma_f32_16x16x32_bf16 v[224:227], v[96:99], v[60:63], v[224:227]
s_xor_b32 s48, s95, 135168
ds_read_b128 v[56:59], v24 offset:4096
v_mfma_f32_16x16x32_bf16 v[228:231], v[100:103], v[60:63], v[228:231]
buffer_load_dwordx4 v21, s[12:15], s25 offen offset:1024 lds
v_accvgpr_read_b32 v37, a249
v_mfma_f32_16x16x32_bf16 v[232:235], v[104:107], v[60:63], v[232:235]
buffer_store_dwordx4 v[44:47], v7, s[20:23], s48 offen sc1
v_mul_f32 v36, s47, v36
v_mfma_f32_16x16x32_bf16 v[236:239], v[108:111], v[60:63], v[236:239]
v_mul_f32 v37, s47, v37
v_cvt_pk_bf16_f32 v40, v36, v37
v_mfma_f32_16x16x32_bf16 v[240:243], v[112:115], v[60:63], v[240:243]
v_accvgpr_read_b32 v38, a250
v_accvgpr_read_b32 v39, a251
v_mfma_f32_16x16x32_bf16 v[244:247], v[116:119], v[60:63], v[244:247]
v_mul_f32 v38, s47, v38
v_mul_f32 v39, s47, v39
v_mfma_f32_16x16x32_bf16 v[248:251], v[120:123], v[60:63], v[248:251]
v_cvt_pk_bf16_f32 v41, v38, v39
v_accvgpr_read_b32 v36, a252
v_mfma_f32_16x16x32_bf16 v[252:255], v[124:127], v[60:63], v[252:255]
s_waitcnt lgkmcnt(0)
v_accvgpr_read_b32 v37, a253
v_mul_f32 v36, s47, v36
v_mfma_f32_16x16x32_bf16 v[128:131], v[64:67], v[48:51], v[128:131]
ds_read_b128 v[60:63], v24 offset:6144
v_mul_f32 v37, s47, v37
v_mfma_f32_16x16x32_bf16 v[132:135], v[68:71], v[48:51], v[132:135]
v_add_u32 v24, 32768, v24
v_cvt_pk_bf16_f32 v42, v36, v37
v_mfma_f32_16x16x32_bf16 v[136:139], v[72:75], v[48:51], v[136:139]
ds_read_b64_tr_b16 v[96:97], v26 offset:8192
ds_read_b64_tr_b16 v[98:99], v26 offset:9216
v_mfma_f32_16x16x32_bf16 v[140:143], v[76:79], v[48:51], v[140:143]
buffer_load_dwordx4 v22, s[12:15], s25 offen offset:2048 lds
v_accvgpr_read_b32 v38, a254
v_mfma_f32_16x16x32_bf16 v[144:147], v[80:83], v[48:51], v[144:147]
ds_read_b64_tr_b16 v[100:101], v27 offset:8192
ds_read_b64_tr_b16 v[102:103], v27 offset:9216
v_mfma_f32_16x16x32_bf16 v[148:151], v[84:87], v[48:51], v[148:151]
v_accvgpr_read_b32 v39, a255
v_mul_f32 v38, s47, v38
v_mfma_f32_16x16x32_bf16 v[152:155], v[88:91], v[48:51], v[152:155]
ds_read_b64_tr_b16 v[104:105], v28 offset:8192
ds_read_b64_tr_b16 v[106:107], v28 offset:9216
v_mfma_f32_16x16x32_bf16 v[156:159], v[92:95], v[48:51], v[156:159]
buffer_load_dwordx4 v23, s[12:15], s25 offen offset:3072 lds
v_mul_f32 v39, s47, v39
v_mfma_f32_16x16x32_bf16 v[160:163], v[64:67], v[52:55], v[160:163]
ds_read_b128 v[48:51], v25 offset:0
v_cvt_pk_bf16_f32 v43, v38, v39
v_mfma_f32_16x16x32_bf16 v[164:167], v[68:71], v[52:55], v[164:167]
s_add_u32 s24, s24, 128
s_add_u32 s25, s25, 128
v_accvgpr_write_b32 a236, v40
v_mfma_f32_16x16x32_bf16 v[168:171], v[72:75], v[52:55], v[168:171]
s_add_u32 m0, s91, 81920
ds_read_b64_tr_b16 v[108:109], v29 offset:8192
ds_read_b64_tr_b16 v[110:111], v29 offset:9216
v_mfma_f32_16x16x32_bf16 v[172:175], v[76:79], v[52:55], v[172:175]
buffer_load_dwordx4 v16, s[16:19], s26 offen offset:0 lds
v_accvgpr_write_b32 a237, v41
v_mfma_f32_16x16x32_bf16 v[176:179], v[80:83], v[52:55], v[176:179]
ds_read_b64_tr_b16 v[112:113], v30 offset:8192
ds_read_b64_tr_b16 v[114:115], v30 offset:9216
v_mfma_f32_16x16x32_bf16 v[180:183], v[84:87], v[52:55], v[180:183]
v_accvgpr_write_b32 a238, v42
v_accvgpr_write_b32 a239, v43
v_mfma_f32_16x16x32_bf16 v[184:187], v[88:91], v[52:55], v[184:187]
ds_read_b64_tr_b16 v[116:117], v31 offset:8192
ds_read_b64_tr_b16 v[118:119], v31 offset:9216
v_mfma_f32_16x16x32_bf16 v[188:191], v[92:95], v[52:55], v[188:191]
buffer_load_dwordx4 v17, s[16:19], s26 offen offset:1024 lds
v_accvgpr_read_b32 v36, a104
v_mfma_f32_16x16x32_bf16 v[192:195], v[64:67], v[56:59], v[192:195]
ds_read_b128 v[52:55], v25 offset:2048
v_accvgpr_read_b32 v37, a105
v_mfma_f32_16x16x32_bf16 v[196:199], v[68:71], v[56:59], v[196:199]
v_mul_f32 v36, s47, v36
v_mul_f32 v37, s47, v37
v_mfma_f32_16x16x32_bf16 v[200:203], v[72:75], v[56:59], v[200:203]
ds_read_b64_tr_b16 v[120:121], v32 offset:8192
ds_read_b64_tr_b16 v[122:123], v32 offset:9216
v_mfma_f32_16x16x32_bf16 v[204:207], v[76:79], v[56:59], v[204:207]
buffer_load_dwordx4 v18, s[16:19], s26 offen offset:2048 lds
v_cvt_pk_bf16_f32 v44, v36, v37
v_mfma_f32_16x16x32_bf16 v[208:211], v[80:83], v[56:59], v[208:211]
ds_read_b64_tr_b16 v[124:125], v33 offset:8192
ds_read_b64_tr_b16 v[126:127], v33 offset:9216
v_mfma_f32_16x16x32_bf16 v[212:215], v[84:87], v[56:59], v[212:215]
v_accvgpr_read_b32 v38, a106
v_accvgpr_read_b32 v39, a107
v_mfma_f32_16x16x32_bf16 v[216:219], v[88:91], v[56:59], v[216:219]
s_waitcnt lgkmcnt(15)
v_mul_f32 v38, s47, v38
v_mul_f32 v39, s47, v39
v_mfma_f32_16x16x32_bf16 v[220:223], v[92:95], v[56:59], v[220:223]
v_cvt_pk_bf16_f32 v45, v38, v39
v_accvgpr_read_b32 v36, a108
v_mfma_f32_16x16x32_bf16 v[224:227], v[64:67], v[60:63], v[224:227]
ds_read_b128 v[56:59], v25 offset:4096
v_accvgpr_read_b32 v37, a109
v_mfma_f32_16x16x32_bf16 v[228:231], v[68:71], v[60:63], v[228:231]
buffer_load_dwordx4 v19, s[16:19], s26 offen offset:3072 lds
v_mul_f32 v36, s47, v36
v_mfma_f32_16x16x32_bf16 v[232:235], v[72:75], v[60:63], v[232:235]
v_mul_f32 v37, s47, v37
v_cvt_pk_bf16_f32 v46, v36, v37
v_mfma_f32_16x16x32_bf16 v[236:239], v[76:79], v[60:63], v[236:239]
s_add_u32 s26, s26, 524288
v_accvgpr_read_b32 v38, a110
v_accvgpr_read_b32 v39, a111
v_mfma_f32_16x16x32_bf16 v[240:243], v[80:83], v[60:63], v[240:243]
v_mul_f32 v38, s47, v38
v_mul_f32 v39, s47, v39
v_mfma_f32_16x16x32_bf16 v[244:247], v[84:87], v[60:63], v[244:247]
v_cvt_pk_bf16_f32 v47, v38, v39
v_mfma_f32_16x16x32_bf16 v[248:251], v[88:91], v[60:63], v[248:251]
s_xor_b32 s48, s95, 136192
buffer_store_dwordx4 v[44:47], v7, s[20:23], s48 offen sc1
v_mfma_f32_16x16x32_bf16 v[252:255], v[92:95], v[60:63], v[252:255]
s_waitcnt vmcnt(19) lgkmcnt(0)
s_barrier
v_mfma_f32_16x16x32_bf16 v[128:131], v[96:99], v[48:51], v[128:131]
ds_read_b128 v[60:63], v25 offset:6144
v_mfma_f32_16x16x32_bf16 v[132:135], v[100:103], v[48:51], v[132:135]
v_add_u32 v25, 32768, v25
v_mfma_f32_16x16x32_bf16 v[136:139], v[104:107], v[48:51], v[136:139]
s_add_u32 m0, s92, 98304
ds_read_b64_tr_b16 v[64:65], v26 offset:16384
ds_read_b64_tr_b16 v[66:67], v26 offset:17408
v_mfma_f32_16x16x32_bf16 v[140:143], v[108:111], v[48:51], v[140:143]
buffer_load_dwordx4 v20, s[12:15], s24 offen offset:0 lds
v_mfma_f32_16x16x32_bf16 v[144:147], v[112:115], v[48:51], v[144:147]
ds_read_b64_tr_b16 v[68:69], v27 offset:16384
ds_read_b64_tr_b16 v[70:71], v27 offset:17408
v_mfma_f32_16x16x32_bf16 v[148:151], v[116:119], v[48:51], v[148:151]
v_mfma_f32_16x16x32_bf16 v[152:155], v[120:123], v[48:51], v[152:155]
ds_read_b64_tr_b16 v[72:73], v28 offset:16384
ds_read_b64_tr_b16 v[74:75], v28 offset:17408
v_mfma_f32_16x16x32_bf16 v[156:159], v[124:127], v[48:51], v[156:159]
buffer_load_dwordx4 v21, s[12:15], s24 offen offset:1024 lds
v_mfma_f32_16x16x32_bf16 v[160:163], v[96:99], v[52:55], v[160:163]
ds_read_b128 v[48:51], v24 offset:0
v_mfma_f32_16x16x32_bf16 v[164:167], v[100:103], v[52:55], v[164:167]
v_mfma_f32_16x16x32_bf16 v[168:171], v[104:107], v[52:55], v[168:171]
ds_read_b64_tr_b16 v[76:77], v29 offset:16384
ds_read_b64_tr_b16 v[78:79], v29 offset:17408
v_mfma_f32_16x16x32_bf16 v[172:175], v[108:111], v[52:55], v[172:175]
buffer_load_dwordx4 v22, s[12:15], s24 offen offset:2048 lds
v_mfma_f32_16x16x32_bf16 v[176:179], v[112:115], v[52:55], v[176:179]
ds_read_b64_tr_b16 v[80:81], v30 offset:16384
ds_read_b64_tr_b16 v[82:83], v30 offset:17408
v_mfma_f32_16x16x32_bf16 v[180:183], v[116:119], v[52:55], v[180:183]
v_mfma_f32_16x16x32_bf16 v[184:187], v[120:123], v[52:55], v[184:187]
ds_read_b64_tr_b16 v[84:85], v31 offset:16384
ds_read_b64_tr_b16 v[86:87], v31 offset:17408
v_mfma_f32_16x16x32_bf16 v[188:191], v[124:127], v[52:55], v[188:191]
buffer_load_dwordx4 v23, s[12:15], s24 offen offset:3072 lds
v_mfma_f32_16x16x32_bf16 v[192:195], v[96:99], v[56:59], v[192:195]
ds_read_b128 v[52:55], v24 offset:2048
v_mfma_f32_16x16x32_bf16 v[196:199], v[100:103], v[56:59], v[196:199]
v_mfma_f32_16x16x32_bf16 v[200:203], v[104:107], v[56:59], v[200:203]
s_add_u32 m0, s92, 102400
ds_read_b64_tr_b16 v[88:89], v32 offset:16384
ds_read_b64_tr_b16 v[90:91], v32 offset:17408
v_mfma_f32_16x16x32_bf16 v[204:207], v[108:111], v[56:59], v[204:207]
buffer_load_dwordx4 v20, s[12:15], s25 offen offset:0 lds
v_mfma_f32_16x16x32_bf16 v[208:211], v[112:115], v[56:59], v[208:211]
ds_read_b64_tr_b16 v[92:93], v33 offset:16384
ds_read_b64_tr_b16 v[94:95], v33 offset:17408
v_mfma_f32_16x16x32_bf16 v[212:215], v[116:119], v[56:59], v[212:215]
v_mfma_f32_16x16x32_bf16 v[216:219], v[120:123], v[56:59], v[216:219]
s_waitcnt lgkmcnt(15)
v_mfma_f32_16x16x32_bf16 v[220:223], v[124:127], v[56:59], v[220:223]
v_mfma_f32_16x16x32_bf16 v[224:227], v[96:99], v[60:63], v[224:227]
ds_read_b128 v[56:59], v24 offset:4096
v_mfma_f32_16x16x32_bf16 v[228:231], v[100:103], v[60:63], v[228:231]
buffer_load_dwordx4 v21, s[12:15], s25 offen offset:1024 lds
v_mfma_f32_16x16x32_bf16 v[232:235], v[104:107], v[60:63], v[232:235]
v_mfma_f32_16x16x32_bf16 v[236:239], v[108:111], v[60:63], v[236:239]
v_mfma_f32_16x16x32_bf16 v[240:243], v[112:115], v[60:63], v[240:243]
v_mfma_f32_16x16x32_bf16 v[244:247], v[116:119], v[60:63], v[244:247]
v_accvgpr_read_b32 v36, a112
v_accvgpr_read_b32 v37, a113
v_mfma_f32_16x16x32_bf16 v[248:251], v[120:123], v[60:63], v[248:251]
v_mul_f32 v36, s47, v36
v_mul_f32 v37, s47, v37
v_mfma_f32_16x16x32_bf16 v[252:255], v[124:127], v[60:63], v[252:255]
s_waitcnt lgkmcnt(0)
v_cvt_pk_bf16_f32 v40, v36, v37
v_accvgpr_read_b32 v38, a114
v_mfma_f32_16x16x32_bf16 v[128:131], v[64:67], v[48:51], v[128:131]
ds_read_b128 v[60:63], v24 offset:6144
v_accvgpr_read_b32 v39, a115
v_mfma_f32_16x16x32_bf16 v[132:135], v[68:71], v[48:51], v[132:135]
v_add_u32 v24, 4294868992, v24
v_mul_f32 v38, s47, v38
v_mfma_f32_16x16x32_bf16 v[136:139], v[72:75], v[48:51], v[136:139]
ds_read_b64_tr_b16 v[96:97], v26 offset:24576
ds_read_b64_tr_b16 v[98:99], v26 offset:25600
v_mfma_f32_16x16x32_bf16 v[140:143], v[76:79], v[48:51], v[140:143]
buffer_load_dwordx4 v22, s[12:15], s25 offen offset:2048 lds
v_mul_f32 v39, s47, v39
v_mfma_f32_16x16x32_bf16 v[144:147], v[80:83], v[48:51], v[144:147]
ds_read_b64_tr_b16 v[100:101], v27 offset:24576
ds_read_b64_tr_b16 v[102:103], v27 offset:25600
v_mfma_f32_16x16x32_bf16 v[148:151], v[84:87], v[48:51], v[148:151]
v_cvt_pk_bf16_f32 v41, v38, v39
v_accvgpr_read_b32 v36, a116
v_mfma_f32_16x16x32_bf16 v[152:155], v[88:91], v[48:51], v[152:155]
ds_read_b64_tr_b16 v[104:105], v28 offset:24576
ds_read_b64_tr_b16 v[106:107], v28 offset:25600
v_mfma_f32_16x16x32_bf16 v[156:159], v[92:95], v[48:51], v[156:159]
buffer_load_dwordx4 v23, s[12:15], s25 offen offset:3072 lds
v_accvgpr_read_b32 v37, a117
v_mfma_f32_16x16x32_bf16 v[160:163], v[64:67], v[52:55], v[160:163]
ds_read_b128 v[48:51], v25 offset:0
v_mul_f32 v36, s47, v36
v_mfma_f32_16x16x32_bf16 v[164:167], v[68:71], v[52:55], v[164:167]
s_add_u32 s24, s24, 128
s_add_u32 s25, s25, 128
v_mul_f32 v37, s47, v37
v_mfma_f32_16x16x32_bf16 v[168:171], v[72:75], v[52:55], v[168:171]
s_add_u32 m0, s91, 0
ds_read_b64_tr_b16 v[108:109], v29 offset:24576
ds_read_b64_tr_b16 v[110:111], v29 offset:25600
v_mfma_f32_16x16x32_bf16 v[172:175], v[76:79], v[52:55], v[172:175]
buffer_load_dwordx4 v16, s[16:19], s26 offen offset:0 lds
v_cvt_pk_bf16_f32 v42, v36, v37
v_mfma_f32_16x16x32_bf16 v[176:179], v[80:83], v[52:55], v[176:179]
ds_read_b64_tr_b16 v[112:113], v30 offset:24576
ds_read_b64_tr_b16 v[114:115], v30 offset:25600
v_mfma_f32_16x16x32_bf16 v[180:183], v[84:87], v[52:55], v[180:183]
v_accvgpr_read_b32 v38, a118
v_accvgpr_read_b32 v39, a119
v_mfma_f32_16x16x32_bf16 v[184:187], v[88:91], v[52:55], v[184:187]
ds_read_b64_tr_b16 v[116:117], v31 offset:24576
ds_read_b64_tr_b16 v[118:119], v31 offset:25600
v_mfma_f32_16x16x32_bf16 v[188:191], v[92:95], v[52:55], v[188:191]
buffer_load_dwordx4 v17, s[16:19], s26 offen offset:1024 lds
v_mul_f32 v38, s47, v38
v_mfma_f32_16x16x32_bf16 v[192:195], v[64:67], v[56:59], v[192:195]
ds_read_b128 v[52:55], v25 offset:2048
v_mul_f32 v39, s47, v39
v_mfma_f32_16x16x32_bf16 v[196:199], v[68:71], v[56:59], v[196:199]
v_cvt_pk_bf16_f32 v43, v38, v39
v_mfma_f32_16x16x32_bf16 v[200:203], v[72:75], v[56:59], v[200:203]
ds_read_b64_tr_b16 v[120:121], v32 offset:24576
ds_read_b64_tr_b16 v[122:123], v32 offset:25600
v_mfma_f32_16x16x32_bf16 v[204:207], v[76:79], v[56:59], v[204:207]
s_xor_b32 s48, s95, 137216
buffer_load_dwordx4 v18, s[16:19], s26 offen offset:2048 lds
v_mfma_f32_16x16x32_bf16 v[208:211], v[80:83], v[56:59], v[208:211]
ds_read_b64_tr_b16 v[124:125], v33 offset:24576
ds_read_b64_tr_b16 v[126:127], v33 offset:25600
.Lpad8:
s_cbranch_execz .Ltch2
v_mfma_f32_16x16x32_bf16 v[212:215], v[84:87], v[56:59], v[212:215]
buffer_store_dwordx4 v[40:43], v7, s[20:23], s48 offen sc1
v_mfma_f32_16x16x32_bf16 v[216:219], v[88:91], v[56:59], v[216:219]
s_waitcnt lgkmcnt(15)
v_mfma_f32_16x16x32_bf16 v[220:223], v[92:95], v[56:59], v[220:223]
v_xor_b32 v26, 0x10000, v26
v_mfma_f32_16x16x32_bf16 v[224:227], v[64:67], v[60:63], v[224:227]
ds_read_b128 v[56:59], v25 offset:4096
v_xor_b32 v27, 0x10000, v27
v_mfma_f32_16x16x32_bf16 v[228:231], v[68:71], v[60:63], v[228:231]
buffer_load_dwordx4 v19, s[16:19], s26 offen offset:3072 lds
v_xor_b32 v28, 0x10000, v28
v_mfma_f32_16x16x32_bf16 v[232:235], v[72:75], v[60:63], v[232:235]
v_xor_b32 v29, 0x10000, v29
v_mfma_f32_16x16x32_bf16 v[236:239], v[76:79], v[60:63], v[236:239]
s_add_u32 s26, s26, 524288
v_xor_b32 v30, 0x10000, v30
v_mfma_f32_16x16x32_bf16 v[240:243], v[80:83], v[60:63], v[240:243]
v_xor_b32 v31, 0x10000, v31
v_mfma_f32_16x16x32_bf16 v[244:247], v[84:87], v[60:63], v[244:247]
v_xor_b32 v32, 0x10000, v32
v_mfma_f32_16x16x32_bf16 v[248:251], v[88:91], v[60:63], v[248:251]
v_xor_b32 v33, 0x10000, v33
v_mfma_f32_16x16x32_bf16 v[252:255], v[92:95], v[60:63], v[252:255]
s_waitcnt vmcnt(18) lgkmcnt(0)
s_barrier
v_mfma_f32_16x16x32_bf16 v[128:131], v[96:99], v[48:51], v[128:131]
ds_read_b128 v[60:63], v25 offset:6144
v_mfma_f32_16x16x32_bf16 v[132:135], v[100:103], v[48:51], v[132:135]
v_add_u32 v25, 4294868992, v25
v_mfma_f32_16x16x32_bf16 v[136:139], v[104:107], v[48:51], v[136:139]
s_add_u32 m0, s92, 131072
ds_read_b64_tr_b16 v[64:65], v26 offset:0
ds_read_b64_tr_b16 v[66:67], v26 offset:1024
v_mfma_f32_16x16x32_bf16 v[140:143], v[108:111], v[48:51], v[140:143]
buffer_load_dwordx4 v20, s[12:15], s24 offen offset:0 lds
v_mfma_f32_16x16x32_bf16 v[144:147], v[112:115], v[48:51], v[144:147]
ds_read_b64_tr_b16 v[68:69], v27 offset:0
ds_read_b64_tr_b16 v[70:71], v27 offset:1024
v_mfma_f32_16x16x32_bf16 v[148:151], v[116:119], v[48:51], v[148:151]
v_mfma_f32_16x16x32_bf16 v[152:155], v[120:123], v[48:51], v[152:155]
ds_read_b64_tr_b16 v[72:73], v28 offset:0
ds_read_b64_tr_b16 v[74:75], v28 offset:1024
v_mfma_f32_16x16x32_bf16 v[156:159], v[124:127], v[48:51], v[156:159]
buffer_load_dwordx4 v21, s[12:15], s24 offen offset:1024 lds
v_mfma_f32_16x16x32_bf16 v[160:163], v[96:99], v[52:55], v[160:163]
ds_read_b128 v[48:51], v24 offset:0
v_mfma_f32_16x16x32_bf16 v[164:167], v[100:103], v[52:55], v[164:167]
v_mfma_f32_16x16x32_bf16 v[168:171], v[104:107], v[52:55], v[168:171]
ds_read_b64_tr_b16 v[76:77], v29 offset:0
ds_read_b64_tr_b16 v[78:79], v29 offset:1024
v_mfma_f32_16x16x32_bf16 v[172:175], v[108:111], v[52:55], v[172:175]
buffer_load_dwordx4 v22, s[12:15], s24 offen offset:2048 lds
v_mfma_f32_16x16x32_bf16 v[176:179], v[112:115], v[52:55], v[176:179]
ds_read_b64_tr_b16 v[80:81], v30 offset:0
ds_read_b64_tr_b16 v[82:83], v30 offset:1024
v_mfma_f32_16x16x32_bf16 v[180:183], v[116:119], v[52:55], v[180:183]
v_mfma_f32_16x16x32_bf16 v[184:187], v[120:123], v[52:55], v[184:187]
ds_read_b64_tr_b16 v[84:85], v31 offset:0
ds_read_b64_tr_b16 v[86:87], v31 offset:1024
v_mfma_f32_16x16x32_bf16 v[188:191], v[124:127], v[52:55], v[188:191]
buffer_load_dwordx4 v23, s[12:15], s24 offen offset:3072 lds
v_accvgpr_read_b32 v36, a120
v_mfma_f32_16x16x32_bf16 v[192:195], v[96:99], v[56:59], v[192:195]
ds_read_b128 v[52:55], v24 offset:2048
v_accvgpr_read_b32 v37, a121
v_mfma_f32_16x16x32_bf16 v[196:199], v[100:103], v[56:59], v[196:199]
v_mul_f32 v36, s47, v36
v_mul_f32 v37, s47, v37
v_mfma_f32_16x16x32_bf16 v[200:203], v[104:107], v[56:59], v[200:203]
s_add_u32 m0, s92, 135168
ds_read_b64_tr_b16 v[88:89], v32 offset:0
ds_read_b64_tr_b16 v[90:91], v32 offset:1024
v_mfma_f32_16x16x32_bf16 v[204:207], v[108:111], v[56:59], v[204:207]
buffer_load_dwordx4 v20, s[12:15], s25 offen offset:0 lds
v_cvt_pk_bf16_f32 v44, v36, v37
v_mfma_f32_16x16x32_bf16 v[208:211], v[112:115], v[56:59], v[208:211]
ds_read_b64_tr_b16 v[92:93], v33 offset:0
ds_read_b64_tr_b16 v[94:95], v33 offset:1024
v_mfma_f32_16x16x32_bf16 v[212:215], v[116:119], v[56:59], v[212:215]
v_accvgpr_read_b32 v38, a122
v_accvgpr_read_b32 v39, a123
v_mfma_f32_16x16x32_bf16 v[216:219], v[120:123], v[56:59], v[216:219]
s_waitcnt lgkmcnt(15)
v_mul_f32 v38, s47, v38
v_mul_f32 v39, s47, v39
v_mfma_f32_16x16x32_bf16 v[220:223], v[124:127], v[56:59], v[220:223]
v_cvt_pk_bf16_f32 v45, v38, v39
v_accvgpr_read_b32 v36, a124
v_mfma_f32_16x16x32_bf16 v[224:227], v[96:99], v[60:63], v[224:227]
ds_read_b128 v[56:59], v24 offset:4096
v_accvgpr_read_b32 v37, a125
v_mfma_f32_16x16x32_bf16 v[228:231], v[100:103], v[60:63], v[228:231]
buffer_load_dwordx4 v21, s[12:15], s25 offen offset:1024 lds
v_mul_f32 v36, s47, v36
v_mfma_f32_16x16x32_bf16 v[232:235], v[104:107], v[60:63], v[232:235]
v_mul_f32 v37, s47, v37
v_cvt_pk_bf16_f32 v46, v36, v37
v_mfma_f32_16x16x32_bf16 v[236:239], v[108:111], v[60:63], v[236:239]
v_accvgpr_read_b32 v38, a126
v_accvgpr_read_b32 v39, a127
v_mfma_f32_16x16x32_bf16 v[240:243], v[112:115], v[60:63], v[240:243]
v_mul_f32 v38, s47, v38
v_mul_f32 v39, s47, v39
v_mfma_f32_16x16x32_bf16 v[244:247], v[116:119], v[60:63], v[244:247]
v_cvt_pk_bf16_f32 v47, v38, v39
v_mfma_f32_16x16x32_bf16 v[248:251], v[120:123], v[60:63], v[248:251]
s_xor_b32 s48, s95, 138240
buffer_store_dwordx4 v[44:47], v7, s[20:23], s48 offen sc1
v_mfma_f32_16x16x32_bf16 v[252:255], v[124:127], v[60:63], v[252:255]
s_waitcnt lgkmcnt(0)
v_mfma_f32_16x16x32_bf16 v[128:131], v[64:67], v[48:51], v[128:131]
ds_read_b128 v[60:63], v24 offset:6144
v_mfma_f32_16x16x32_bf16 v[132:135], v[68:71], v[48:51], v[132:135]
v_add_u32 v24, 65536, v24
v_mfma_f32_16x16x32_bf16 v[136:139], v[72:75], v[48:51], v[136:139]
ds_read_b64_tr_b16 v[96:97], v26 offset:8192
ds_read_b64_tr_b16 v[98:99], v26 offset:9216
v_mfma_f32_16x16x32_bf16 v[140:143], v[76:79], v[48:51], v[140:143]
buffer_load_dwordx4 v22, s[12:15], s25 offen offset:2048 lds
v_mfma_f32_16x16x32_bf16 v[144:147], v[80:83], v[48:51], v[144:147]
ds_read_b64_tr_b16 v[100:101], v27 offset:8192
ds_read_b64_tr_b16 v[102:103], v27 offset:9216
v_mfma_f32_16x16x32_bf16 v[148:151], v[84:87], v[48:51], v[148:151]
v_mfma_f32_16x16x32_bf16 v[152:155], v[88:91], v[48:51], v[152:155]
ds_read_b64_tr_b16 v[104:105], v28 offset:8192
ds_read_b64_tr_b16 v[106:107], v28 offset:9216
v_mfma_f32_16x16x32_bf16 v[156:159], v[92:95], v[48:51], v[156:159]
buffer_load_dwordx4 v23, s[12:15], s25 offen offset:3072 lds
v_mfma_f32_16x16x32_bf16 v[160:163], v[64:67], v[52:55], v[160:163]
ds_read_b128 v[48:51], v25 offset:0
v_mfma_f32_16x16x32_bf16 v[164:167], v[68:71], v[52:55], v[164:167]
s_add_u32 s24, s24, 128
s_add_u32 s25, s25, 128
v_mfma_f32_16x16x32_bf16 v[168:171], v[72:75], v[52:55], v[168:171]
s_add_u32 m0, s91, 16384
ds_read_b64_tr_b16 v[108:109], v29 offset:8192
ds_read_b64_tr_b16 v[110:111], v29 offset:9216
v_mfma_f32_16x16x32_bf16 v[172:175], v[76:79], v[52:55], v[172:175]
buffer_load_dwordx4 v16, s[16:19], s26 offen offset:0 lds
v_mfma_f32_16x16x32_bf16 v[176:179], v[80:83], v[52:55], v[176:179]
ds_read_b64_tr_b16 v[112:113], v30 offset:8192
ds_read_b64_tr_b16 v[114:115], v30 offset:9216
v_mfma_f32_16x16x32_bf16 v[180:183], v[84:87], v[52:55], v[180:183]
v_mfma_f32_16x16x32_bf16 v[184:187], v[88:91], v[52:55], v[184:187]
ds_read_b64_tr_b16 v[116:117], v31 offset:8192
ds_read_b64_tr_b16 v[118:119], v31 offset:9216
v_mfma_f32_16x16x32_bf16 v[188:191], v[92:95], v[52:55], v[188:191]
buffer_load_dwordx4 v17, s[16:19], s26 offen offset:1024 lds
v_mfma_f32_16x16x32_bf16 v[192:195], v[64:67], v[56:59], v[192:195]
ds_read_b128 v[52:55], v25 offset:2048
v_mfma_f32_16x16x32_bf16 v[196:199], v[68:71], v[56:59], v[196:199]
v_mfma_f32_16x16x32_bf16 v[200:203], v[72:75], v[56:59], v[200:203]
ds_read_b64_tr_b16 v[120:121], v32 offset:8192
ds_read_b64_tr_b16 v[122:123], v32 offset:9216
v_mfma_f32_16x16x32_bf16 v[204:207], v[76:79], v[56:59], v[204:207]
buffer_load_dwordx4 v18, s[16:19], s26 offen offset:2048 lds
v_mfma_f32_16x16x32_bf16 v[208:211], v[80:83], v[56:59], v[208:211]
ds_read_b64_tr_b16 v[124:125], v33 offset:8192
ds_read_b64_tr_b16 v[126:127], v33 offset:9216
v_mfma_f32_16x16x32_bf16 v[212:215], v[84:87], v[56:59], v[212:215]
v_mfma_f32_16x16x32_bf16 v[216:219], v[88:91], v[56:59], v[216:219]
s_waitcnt lgkmcnt(15)
v_mfma_f32_16x16x32_bf16 v[220:223], v[92:95], v[56:59], v[220:223]
v_mfma_f32_16x16x32_bf16 v[224:227], v[64:67], v[60:63], v[224:227]
ds_read_b128 v[56:59], v25 offset:4096
v_mfma_f32_16x16x32_bf16 v[228:231], v[68:71], v[60:63], v[228:231]
buffer_load_dwordx4 v19, s[16:19], s26 offen offset:3072 lds
v_mfma_f32_16x16x32_bf16 v[232:235], v[72:75], v[60:63], v[232:235]
v_mfma_f32_16x16x32_bf16 v[236:239], v[76:79], v[60:63], v[236:239]
s_add_u32 s26, s26, 524288
v_mfma_f32_16x16x32_bf16 v[240:243], v[80:83], v[60:63], v[240:243]
v_mfma_f32_16x16x32_bf16 v[244:247], v[84:87], v[60:63], v[244:247]
buffer_load_dword v8, v3, s[60:63], s90 offen sc1
v_mfma_f32_16x16x32_bf16 v[248:251], v[88:91], v[60:63], v[248:251]
v_mfma_f32_16x16x32_bf16 v[252:255], v[92:95], v[60:63], v[252:255]
s_waitcnt vmcnt(19) lgkmcnt(0)
s_barrier
v_mfma_f32_16x16x32_bf16 v[128:131], v[96:99], v[48:51], v[128:131]
ds_read_b128 v[60:63], v25 offset:6144
v_mfma_f32_16x16x32_bf16 v[132:135], v[100:103], v[48:51], v[132:135]
v_add_u32 v25, 65536, v25
v_mfma_f32_16x16x32_bf16 v[136:139], v[104:107], v[48:51], v[136:139]
s_add_u32 m0, s92, 32768
ds_read_b64_tr_b16 v[64:65], v26 offset:16384
ds_read_b64_tr_b16 v[66:67], v26 offset:17408
v_mfma_f32_16x16x32_bf16 v[140:143], v[108:111], v[48:51], v[140:143]
buffer_load_dwordx4 v20, s[12:15], s24 offen offset:0 lds
v_mfma_f32_16x16x32_bf16 v[144:147], v[112:115], v[48:51], v[144:147]
ds_read_b64_tr_b16 v[68:69], v27 offset:16384
ds_read_b64_tr_b16 v[70:71], v27 offset:17408
v_mfma_f32_16x16x32_bf16 v[148:151], v[116:119], v[48:51], v[148:151]
v_mfma_f32_16x16x32_bf16 v[152:155], v[120:123], v[48:51], v[152:155]
ds_read_b64_tr_b16 v[72:73], v28 offset:16384
ds_read_b64_tr_b16 v[74:75], v28 offset:17408
v_mfma_f32_16x16x32_bf16 v[156:159], v[124:127], v[48:51], v[156:159]
buffer_load_dwordx4 v21, s[12:15], s24 offen offset:1024 lds
v_mfma_f32_16x16x32_bf16 v[160:163], v[96:99], v[52:55], v[160:163]
ds_read_b128 v[48:51], v24 offset:0
v_mfma_f32_16x16x32_bf16 v[164:167], v[100:103], v[52:55], v[164:167]
v_mfma_f32_16x16x32_bf16 v[168:171], v[104:107], v[52:55], v[168:171]
ds_read_b64_tr_b16 v[76:77], v29 offset:16384
ds_read_b64_tr_b16 v[78:79], v29 offset:17408
v_mfma_f32_16x16x32_bf16 v[172:175], v[108:111], v[52:55], v[172:175]
buffer_load_dwordx4 v22, s[12:15], s24 offen offset:2048 lds
v_mfma_f32_16x16x32_bf16 v[176:179], v[112:115], v[52:55], v[176:179]
ds_read_b64_tr_b16 v[80:81], v30 offset:16384
ds_read_b64_tr_b16 v[82:83], v30 offset:17408
v_mfma_f32_16x16x32_bf16 v[180:183], v[116:119], v[52:55], v[180:183]
v_mfma_f32_16x16x32_bf16 v[184:187], v[120:123], v[52:55], v[184:187]
ds_read_b64_tr_b16 v[84:85], v31 offset:16384
ds_read_b64_tr_b16 v[86:87], v31 offset:17408
v_mfma_f32_16x16x32_bf16 v[188:191], v[124:127], v[52:55], v[188:191]
buffer_load_dwordx4 v23, s[12:15], s24 offen offset:3072 lds
v_mfma_f32_16x16x32_bf16 v[192:195], v[96:99], v[56:59], v[192:195]
ds_read_b128 v[52:55], v24 offset:2048
v_mfma_f32_16x16x32_bf16 v[196:199], v[100:103], v[56:59], v[196:199]
v_mfma_f32_16x16x32_bf16 v[200:203], v[104:107], v[56:59], v[200:203]
s_add_u32 m0, s92, 36864
ds_read_b64_tr_b16 v[88:89], v32 offset:16384
ds_read_b64_tr_b16 v[90:91], v32 offset:17408
v_mfma_f32_16x16x32_bf16 v[204:207], v[108:111], v[56:59], v[204:207]
buffer_load_dwordx4 v20, s[12:15], s25 offen offset:0 lds
v_mfma_f32_16x16x32_bf16 v[208:211], v[112:115], v[56:59], v[208:211]
ds_read_b64_tr_b16 v[92:93], v33 offset:16384
ds_read_b64_tr_b16 v[94:95], v33 offset:17408
v_mfma_f32_16x16x32_bf16 v[212:215], v[116:119], v[56:59], v[212:215]
v_mfma_f32_16x16x32_bf16 v[216:219], v[120:123], v[56:59], v[216:219]
s_waitcnt lgkmcnt(15)
v_mfma_f32_16x16x32_bf16 v[220:223], v[124:127], v[56:59], v[220:223]
v_mfma_f32_16x16x32_bf16 v[224:227], v[96:99], v[60:63], v[224:227]
ds_read_b128 v[56:59], v24 offset:4096
v_mfma_f32_16x16x32_bf16 v[228:231], v[100:103], v[60:63], v[228:231]
buffer_load_dwordx4 v21, s[12:15], s25 offen offset:1024 lds
v_mfma_f32_16x16x32_bf16 v[232:235], v[104:107], v[60:63], v[232:235]
v_mfma_f32_16x16x32_bf16 v[236:239], v[108:111], v[60:63], v[236:239]
v_mfma_f32_16x16x32_bf16 v[240:243], v[112:115], v[60:63], v[240:243]
v_mfma_f32_16x16x32_bf16 v[244:247], v[116:119], v[60:63], v[244:247]
v_mfma_f32_16x16x32_bf16 v[248:251], v[120:123], v[60:63], v[248:251]
v_mfma_f32_16x16x32_bf16 v[252:255], v[124:127], v[60:63], v[252:255]
s_waitcnt lgkmcnt(0)
v_mfma_f32_16x16x32_bf16 v[128:131], v[64:67], v[48:51], v[128:131]
ds_read_b128 v[60:63], v24 offset:6144
v_mfma_f32_16x16x32_bf16 v[132:135], v[68:71], v[48:51], v[132:135]
v_add_u32 v24, 32768, v24
v_mfma_f32_16x16x32_bf16 v[136:139], v[72:75], v[48:51], v[136:139]
ds_read_b64_tr_b16 v[96:97], v26 offset:24576
ds_read_b64_tr_b16 v[98:99], v26 offset:25600
v_mfma_f32_16x16x32_bf16 v[140:143], v[76:79], v[48:51], v[140:143]
buffer_load_dwordx4 v22, s[12:15], s25 offen offset:2048 lds
v_mfma_f32_16x16x32_bf16 v[144:147], v[80:83], v[48:51], v[144:147]
ds_read_b64_tr_b16 v[100:101], v27 offset:24576
ds_read_b64_tr_b16 v[102:103], v27 offset:25600
v_mfma_f32_16x16x32_bf16 v[148:151], v[84:87], v[48:51], v[148:151]
v_mfma_f32_16x16x32_bf16 v[152:155], v[88:91], v[48:51], v[152:155]
ds_read_b64_tr_b16 v[104:105], v28 offset:24576
ds_read_b64_tr_b16 v[106:107], v28 offset:25600
v_mfma_f32_16x16x32_bf16 v[156:159], v[92:95], v[48:51], v[156:159]
buffer_load_dwordx4 v23, s[12:15], s25 offen offset:3072 lds
v_mfma_f32_16x16x32_bf16 v[160:163], v[64:67], v[52:55], v[160:163]
ds_read_b128 v[48:51], v25 offset:0
v_mfma_f32_16x16x32_bf16 v[164:167], v[68:71], v[52:55], v[164:167]
s_add_u32 s24, s24, 128
s_add_u32 s25, s25, 128
v_mfma_f32_16x16x32_bf16 v[168:171], v[72:75], v[52:55], v[168:171]
ds_read_b64_tr_b16 v[108:109], v29 offset:24576
ds_read_b64_tr_b16 v[110:111], v29 offset:25600
v_mfma_f32_16x16x32_bf16 v[172:175], v[76:79], v[52:55], v[172:175]
v_mfma_f32_16x16x32_bf16 v[176:179], v[80:83], v[52:55], v[176:179]
ds_read_b64_tr_b16 v[112:113], v30 offset:24576
ds_read_b64_tr_b16 v[114:115], v30 offset:25600
v_mfma_f32_16x16x32_bf16 v[180:183], v[84:87], v[52:55], v[180:183]
v_mfma_f32_16x16x32_bf16 v[184:187], v[88:91], v[52:55], v[184:187]
ds_read_b64_tr_b16 v[116:117], v31 offset:24576
ds_read_b64_tr_b16 v[118:119], v31 offset:25600
v_mfma_f32_16x16x32_bf16 v[188:191], v[92:95], v[52:55], v[188:191]
v_mfma_f32_16x16x32_bf16 v[192:195], v[64:67], v[56:59], v[192:195]
ds_read_b128 v[52:55], v25 offset:2048
v_mfma_f32_16x16x32_bf16 v[196:199], v[68:71], v[56:59], v[196:199]
v_mfma_f32_16x16x32_bf16 v[200:203], v[72:75], v[56:59], v[200:203]
ds_read_b64_tr_b16 v[120:121], v32 offset:24576
ds_read_b64_tr_b16 v[122:123], v32 offset:25600
v_mfma_f32_16x16x32_bf16 v[204:207], v[76:79], v[56:59], v[204:207]
v_mfma_f32_16x16x32_bf16 v[208:211], v[80:83], v[56:59], v[208:211]
ds_read_b64_tr_b16 v[124:125], v33 offset:24576
ds_read_b64_tr_b16 v[126:127], v33 offset:25600
v_mfma_f32_16x16x32_bf16 v[212:215], v[84:87], v[56:59], v[212:215]
s_waitcnt vmcnt(8)
v_and_b32 v0, 7, v8
v_cmp_lt_u32 s[28:29], v0, v14
v_mfma_f32_16x16x32_bf16 v[216:219], v[88:91], v[56:59], v[216:219]
s_waitcnt lgkmcnt(15)
v_cmp_eq_u32 s[30:31], v8, v2
v_mfma_f32_16x16x32_bf16 v[220:223], v[92:95], v[56:59], v[220:223]
v_xor_b32 v26, 0x10000, v26
v_mfma_f32_16x16x32_bf16 v[224:227], v[64:67], v[60:63], v[224:227]
ds_read_b128 v[56:59], v25 offset:4096
v_xor_b32 v27, 0x10000, v27
v_mfma_f32_16x16x32_bf16 v[228:231], v[68:71], v[60:63], v[228:231]
v_xor_b32 v28, 0x10000, v28
v_mfma_f32_16x16x32_bf16 v[232:235], v[72:75], v[60:63], v[232:235]
ds_read_b128 v[20:23], v25 offset:6144
v_xor_b32 v29, 0x10000, v29
v_mfma_f32_16x16x32_bf16 v[236:239], v[76:79], v[60:63], v[236:239]
v_xor_b32 v30, 0x10000, v30
v_mfma_f32_16x16x32_bf16 v[240:243], v[80:83], v[60:63], v[240:243]
v_xor_b32 v31, 0x10000, v31
v_mfma_f32_16x16x32_bf16 v[244:247], v[84:87], v[60:63], v[244:247]
v_xor_b32 v32, 0x10000, v32
v_mfma_f32_16x16x32_bf16 v[248:251], v[88:91], v[60:63], v[248:251]
s_or_b64 s[28:29], s[28:29], s[30:31]
s_cbranch_scc1 .Lspin1
.Lspin1_ret:
s_add_u32 s28, s39, 0
s_and_b32 s28, s28, 0xffff
v_xor_b32 v33, 0x10000, v33
v_mfma_f32_16x16x32_bf16 v[252:255], v[92:95], v[60:63], v[252:255]
s_waitcnt vmcnt(13) lgkmcnt(0)
s_barrier
s_mov_b32 s40, 0
v_mfma_f32_16x16x32_bf16 v[128:131], v[96:99], v[48:51], v[128:131]
s_add_u32 m0, s28, 65536
v_mfma_f32_16x16x32_bf16 v[132:135], v[100:103], v[48:51], v[132:135]
buffer_load_dwordx4 v7, s[4:7], s40 offen offset:0 sc1 lds
v_add_u32 v25, 32768, v25
v_mfma_f32_16x16x32_bf16 v[136:139], v[104:107], v[48:51], v[136:139]
ds_read_b64_tr_b16 v[64:65], v26 offset:0
ds_read_b64_tr_b16 v[66:67], v26 offset:1024
v_mfma_f32_16x16x32_bf16 v[140:143], v[108:111], v[48:51], v[140:143]
v_mfma_f32_16x16x32_bf16 v[144:147], v[112:115], v[48:51], v[144:147]
ds_read_b64_tr_b16 v[68:69], v27 offset:0
ds_read_b64_tr_b16 v[70:71], v27 offset:1024
v_mfma_f32_16x16x32_bf16 v[148:151], v[116:119], v[48:51], v[148:151]
buffer_load_dwordx4 v7, s[4:7], s40 offen offset:1024 sc1 lds
v_mfma_f32_16x16x32_bf16 v[152:155], v[120:123], v[48:51], v[152:155]
ds_read_b64_tr_b16 v[72:73], v28 offset:0
ds_read_b64_tr_b16 v[74:75], v28 offset:1024
v_mfma_f32_16x16x32_bf16 v[156:159], v[124:127], v[48:51], v[156:159]
v_mfma_f32_16x16x32_bf16 v[160:163], v[96:99], v[52:55], v[160:163]
ds_read_b128 v[48:51], v24 offset:0
v_mfma_f32_16x16x32_bf16 v[164:167], v[100:103], v[52:55], v[164:167]
buffer_load_dwordx4 v7, s[4:7], s40 offen offset:2048 sc1 lds
v_mfma_f32_16x16x32_bf16 v[168:171], v[104:107], v[52:55], v[168:171]
ds_read_b64_tr_b16 v[76:77], v29 offset:0
ds_read_b64_tr_b16 v[78:79], v29 offset:1024
v_mfma_f32_16x16x32_bf16 v[172:175], v[108:111], v[52:55], v[172:175]
v_mfma_f32_16x16x32_bf16 v[176:179], v[112:115], v[52:55], v[176:179]
ds_read_b64_tr_b16 v[80:81], v30 offset:0
ds_read_b64_tr_b16 v[82:83], v30 offset:1024
v_mfma_f32_16x16x32_bf16 v[180:183], v[116:119], v[52:55], v[180:183]
buffer_load_dwordx4 v7, s[4:7], s40 offen offset:3072 sc1 lds
v_mfma_f32_16x16x32_bf16 v[184:187], v[120:123], v[52:55], v[184:187]
ds_read_b64_tr_b16 v[84:85], v31 offset:0
ds_read_b64_tr_b16 v[86:87], v31 offset:1024
v_mfma_f32_16x16x32_bf16 v[188:191], v[124:127], v[52:55], v[188:191]
s_add_u32 s28, s39, 16384
s_and_b32 s28, s28, 0xffff
v_mfma_f32_16x16x32_bf16 v[192:195], v[96:99], v[56:59], v[192:195]
s_mov_b32 s41, 16384
ds_read_b128 v[52:55], v24 offset:2048
v_mfma_f32_16x16x32_bf16 v[196:199], v[100:103], v[56:59], v[196:199]
s_add_u32 m0, s28, 65536
v_mfma_f32_16x16x32_bf16 v[200:203], v[104:107], v[56:59], v[200:203]
buffer_load_dwordx4 v7, s[4:7], s41 offen offset:0 sc1 lds
ds_read_b64_tr_b16 v[88:89], v32 offset:0
ds_read_b64_tr_b16 v[90:91], v32 offset:1024
v_mfma_f32_16x16x32_bf16 v[204:207], v[108:111], v[56:59], v[204:207]
v_mfma_f32_16x16x32_bf16 v[208:211], v[112:115], v[56:59], v[208:211]
ds_read_b64_tr_b16 v[92:93], v33 offset:0
ds_read_b64_tr_b16 v[94:95], v33 offset:1024
v_mfma_f32_16x16x32_bf16 v[212:215], v[116:119], v[56:59], v[212:215]
v_mfma_f32_16x16x32_bf16 v[216:219], v[120:123], v[56:59], v[216:219]
buffer_load_dwordx4 v7, s[4:7], s41 offen offset:1024 sc1 lds
v_mfma_f32_16x16x32_bf16 v[220:223], v[124:127], v[56:59], v[220:223]
v_mfma_f32_16x16x32_bf16 v[224:227], v[96:99], v[20:23], v[224:227]
ds_read_b128 v[56:59], v24 offset:4096
v_mfma_f32_16x16x32_bf16 v[228:231], v[100:103], v[20:23], v[228:231]
v_mfma_f32_16x16x32_bf16 v[232:235], v[104:107], v[20:23], v[232:235]
buffer_load_dwordx4 v7, s[4:7], s41 offen offset:2048 sc1 lds
v_mfma_f32_16x16x32_bf16 v[236:239], v[108:111], v[20:23], v[236:239]
v_mfma_f32_16x16x32_bf16 v[240:243], v[112:115], v[20:23], v[240:243]
v_mfma_f32_16x16x32_bf16 v[244:247], v[116:119], v[20:23], v[244:247]
v_mfma_f32_16x16x32_bf16 v[248:251], v[120:123], v[20:23], v[248:251]
buffer_load_dwordx4 v7, s[4:7], s41 offen offset:3072 sc1 lds
v_mfma_f32_16x16x32_bf16 v[252:255], v[124:127], v[20:23], v[252:255]
s_waitcnt lgkmcnt(0)
v_mfma_f32_16x16x32_bf16 v[128:131], v[64:67], v[48:51], v[128:131]
s_add_u32 s28, s39, 32768
s_and_b32 s28, s28, 0xffff
ds_read_b128 v[60:63], v24 offset:6144
v_mfma_f32_16x16x32_bf16 v[132:135], v[68:71], v[48:51], v[132:135]
s_mov_b32 s40, 32768
v_add_u32 v24, 4294868992, v24
v_mfma_f32_16x16x32_bf16 v[136:139], v[72:75], v[48:51], v[136:139]
s_add_u32 m0, s28, 65536
ds_read_b64_tr_b16 v[96:97], v26 offset:8192
ds_read_b64_tr_b16 v[98:99], v26 offset:9216
v_mfma_f32_16x16x32_bf16 v[140:143], v[76:79], v[48:51], v[140:143]
buffer_load_dwordx4 v7, s[4:7], s40 offen offset:0 sc1 lds
v_mfma_f32_16x16x32_bf16 v[144:147], v[80:83], v[48:51], v[144:147]
ds_read_b64_tr_b16 v[100:101], v27 offset:8192
ds_read_b64_tr_b16 v[102:103], v27 offset:9216
v_mfma_f32_16x16x32_bf16 v[148:151], v[84:87], v[48:51], v[148:151]
s_sub_u32 s32, s66, 4194304
s_subb_u32 s33, s67, 0
v_mfma_f32_16x16x32_bf16 v[152:155], v[88:91], v[48:51], v[152:155]
ds_read_b64_tr_b16 v[104:105], v28 offset:8192
ds_read_b64_tr_b16 v[106:107], v28 offset:9216
v_mfma_f32_16x16x32_bf16 v[156:159], v[92:95], v[48:51], v[156:159]
buffer_load_dwordx4 v7, s[4:7], s40 offen offset:1024 sc1 lds
v_mfma_f32_16x16x32_bf16 v[160:163], v[64:67], v[52:55], v[160:163]
ds_read_b128 v[48:51], v25 offset:0
v_mfma_f32_16x16x32_bf16 v[164:167], v[68:71], v[52:55], v[164:167]
s_sub_u32 s34, s80, 10485760
s_subb_u32 s35, s81, 0
v_mfma_f32_16x16x32_bf16 v[168:171], v[72:75], v[52:55], v[168:171]
ds_read_b64_tr_b16 v[108:109], v29 offset:8192
ds_read_b64_tr_b16 v[110:111], v29 offset:9216
v_mfma_f32_16x16x32_bf16 v[172:175], v[76:79], v[52:55], v[172:175]
buffer_load_dwordx4 v7, s[4:7], s40 offen offset:2048 sc1 lds
v_mfma_f32_16x16x32_bf16 v[176:179], v[80:83], v[52:55], v[176:179]
ds_read_b64_tr_b16 v[112:113], v30 offset:8192
ds_read_b64_tr_b16 v[114:115], v30 offset:9216
v_mfma_f32_16x16x32_bf16 v[180:183], v[84:87], v[52:55], v[180:183]
s_sub_u32 s36, s68, 18874368
s_subb_u32 s37, s69, 0
s_max_u32 s38, s89, 4
v_mfma_f32_16x16x32_bf16 v[184:187], v[88:91], v[52:55], v[184:187]
ds_read_b64_tr_b16 v[116:117], v31 offset:8192
ds_read_b64_tr_b16 v[118:119], v31 offset:9216
v_mfma_f32_16x16x32_bf16 v[188:191], v[92:95], v[52:55], v[188:191]
s_sub_u32 s38, s38, 4
buffer_load_dwordx4 v7, s[4:7], s40 offen offset:3072 sc1 lds
v_mfma_f32_16x16x32_bf16 v[192:195], v[64:67], v[56:59], v[192:195]
s_lshl_b32 s38, s38, 3
ds_read_b128 v[52:55], v25 offset:2048
v_mfma_f32_16x16x32_bf16 v[196:199], v[68:71], v[56:59], v[196:199]
s_add_u32 s28, s39, 49152
s_and_b32 s28, s28, 0xffff
s_lshr_b32 s29, 319291392, s38
v_mfma_f32_16x16x32_bf16 v[200:203], v[72:75], v[56:59], v[200:203]
s_mov_b32 s41, 49152
ds_read_b64_tr_b16 v[120:121], v32 offset:8192
ds_read_b64_tr_b16 v[122:123], v32 offset:9216
v_mfma_f32_16x16x32_bf16 v[204:207], v[76:79], v[56:59], v[204:207]
s_add_u32 m0, s28, 65536
s_and_b32 s29, s29, 0xff
s_lshl_b32 s29, s29, 7
v_mfma_f32_16x16x32_bf16 v[208:211], v[80:83], v[56:59], v[208:211]
buffer_load_dwordx4 v7, s[4:7], s41 offen offset:0 sc1 lds
ds_read_b64_tr_b16 v[124:125], v33 offset:8192
ds_read_b64_tr_b16 v[126:127], v33 offset:9216
v_mfma_f32_16x16x32_bf16 v[212:215], v[84:87], v[56:59], v[212:215]
s_lshr_b32 s52, 218826752, s38
s_and_b32 s52, s52, 0xff
v_mfma_f32_16x16x32_bf16 v[216:219], v[88:91], v[56:59], v[216:219]
s_waitcnt lgkmcnt(15)
s_cmp_lt_u32 s89, 4
s_cselect_b32 s52, 0, s52
v_mfma_f32_16x16x32_bf16 v[220:223], v[92:95], v[56:59], v[220:223]
s_sub_u32 s52, s52, 1
s_cselect_b32 s52, 0, s52
s_cselect_b64 s[30:31], -1, 0
v_mfma_f32_16x16x32_bf16 v[224:227], v[64:67], v[60:63], v[224:227]
buffer_load_dwordx4 v7, s[4:7], s41 offen offset:1024 sc1 lds
ds_read_b128 v[56:59], v25 offset:4096
v_mfma_f32_16x16x32_bf16 v[228:231], v[68:71], v[60:63], v[228:231]
s_and_b32 s38, s2, 31
s_lshl_b32 s38, s38, 2
s_add_u32 s38, s38, s3
v_mfma_f32_16x16x32_bf16 v[232:235], v[72:75], v[60:63], v[232:235]
s_add_u32 s38, s38, s29
s_lshl_b32 s38, s38, 8
v_mfma_f32_16x16x32_bf16 v[236:239], v[76:79], v[60:63], v[236:239]
v_lshrrev_b32 v9, 1, v1
v_min_u32 v9, 19, v9
v_mfma_f32_16x16x32_bf16 v[240:243], v[80:83], v[60:63], v[240:243]
buffer_load_dwordx4 v7, s[4:7], s41 offen offset:2048 sc1 lds
v_mov_b32 v2, s64
v_mfma_f32_16x16x32_bf16 v[244:247], v[84:87], v[60:63], v[244:247]
v_mov_b32 v3, s65
v_cmp_le_u32 vcc, 4, v9
v_mfma_f32_16x16x32_bf16 v[248:251], v[88:91], v[60:63], v[248:251]
v_mov_b32 v5, s32
v_mov_b32 v10, s33
v_mfma_f32_16x16x32_bf16 v[252:255], v[92:95], v[60:63], v[252:255]
s_waitcnt vmcnt(30)
s_waitcnt vmcnt(15) lgkmcnt(0)
s_barrier
v_mfma_f32_16x16x32_bf16 v[128:131], v[96:99], v[48:51], v[128:131]
buffer_load_dwordx4 v7, s[4:7], s41 offen offset:3072 sc1 lds
ds_read_b128 v[60:63], v25 offset:6144
v_mfma_f32_16x16x32_bf16 v[132:135], v[100:103], v[48:51], v[132:135]
v_add_u32 v25, 4294868992, v25
v_or_b32 v0, 2, v11
v_mfma_f32_16x16x32_bf16 v[136:139], v[104:107], v[48:51], v[136:139]
ds_read_b64_tr_b16 v[64:65], v26 offset:16384
ds_read_b64_tr_b16 v[66:67], v26 offset:17408
v_mfma_f32_16x16x32_bf16 v[140:143], v[108:111], v[48:51], v[140:143]
buffer_store_dword v0, v6, s[60:63], 0 offen sc1
v_cndmask_b32 v2, v2, v5, vcc
v_mfma_f32_16x16x32_bf16 v[144:147], v[112:115], v[48:51], v[144:147]
ds_read_b64_tr_b16 v[68:69], v27 offset:16384
ds_read_b64_tr_b16 v[70:71], v27 offset:17408
v_mfma_f32_16x16x32_bf16 v[148:151], v[116:119], v[48:51], v[148:151]
v_cndmask_b32 v3, v3, v10, vcc
v_cmp_le_u32 vcc, 10, v9
v_mfma_f32_16x16x32_bf16 v[152:155], v[120:123], v[48:51], v[152:155]
ds_read_b64_tr_b16 v[72:73], v28 offset:16384
ds_read_b64_tr_b16 v[74:75], v28 offset:17408
v_mfma_f32_16x16x32_bf16 v[156:159], v[124:127], v[48:51], v[156:159]
v_mov_b32 v5, s34
v_mov_b32 v10, s35
v_mfma_f32_16x16x32_bf16 v[160:163], v[96:99], v[52:55], v[160:163]
s_xor_b32 s48, s95, 262144
ds_read_b128 v[48:51], v24 offset:0
v_mfma_f32_16x16x32_bf16 v[164:167], v[100:103], v[52:55], v[164:167]
buffer_store_dwordx4 a[128:131], v7, s[20:23], s48 offen sc1
v_cndmask_b32 v2, v2, v5, vcc
v_mfma_f32_16x16x32_bf16 v[168:171], v[104:107], v[52:55], v[168:171]
ds_read_b64_tr_b16 v[76:77], v29 offset:16384
ds_read_b64_tr_b16 v[78:79], v29 offset:17408
v_mfma_f32_16x16x32_bf16 v[172:175], v[108:111], v[52:55], v[172:175]
v_cndmask_b32 v3, v3, v10, vcc
v_cmp_le_u32 vcc, 18, v9
v_mfma_f32_16x16x32_bf16 v[176:179], v[112:115], v[52:55], v[176:179]
ds_read_b64_tr_b16 v[80:81], v30 offset:16384
ds_read_b64_tr_b16 v[82:83], v30 offset:17408
v_mfma_f32_16x16x32_bf16 v[180:183], v[116:119], v[52:55], v[180:183]
v_mov_b32 v5, s36
v_mov_b32 v10, s37
v_mfma_f32_16x16x32_bf16 v[184:187], v[120:123], v[52:55], v[184:187]
ds_read_b64_tr_b16 v[84:85], v31 offset:16384
ds_read_b64_tr_b16 v[86:87], v31 offset:17408
v_mfma_f32_16x16x32_bf16 v[188:191], v[124:127], v[52:55], v[188:191]
v_cndmask_b32 v2, v2, v5, vcc
v_cndmask_b32 v3, v3, v10, vcc
v_mfma_f32_16x16x32_bf16 v[192:195], v[96:99], v[56:59], v[192:195]
ds_read_b128 v[52:55], v24 offset:2048
v_lshlrev_b32 v9, 20, v9
v_mfma_f32_16x16x32_bf16 v[196:199], v[100:103], v[56:59], v[196:199]
v_add_u32 v9, s38, v9
v_and_b32 v5, 1, v1
v_mfma_f32_16x16x32_bf16 v[200:203], v[104:107], v[56:59], v[200:203]
ds_read_b64_tr_b16 v[88:89], v32 offset:16384
ds_read_b64_tr_b16 v[90:91], v32 offset:17408
.Lpad9:
s_cbranch_execz .Ltch2
v_mfma_f32_16x16x32_bf16 v[204:207], v[108:111], v[56:59], v[204:207]
v_lshl_add_u32 v9, v5, 7, v9
v_add_co_u32 v2, vcc, v2, v9
v_mfma_f32_16x16x32_bf16 v[208:211], v[112:115], v[56:59], v[208:211]
ds_read_b64_tr_b16 v[92:93], v33 offset:16384
ds_read_b64_tr_b16 v[94:95], v33 offset:17408
v_mfma_f32_16x16x32_bf16 v[212:215], v[116:119], v[56:59], v[212:215]
v_nop
v_nop
v_mfma_f32_16x16x32_bf16 v[216:219], v[120:123], v[56:59], v[216:219]
s_waitcnt lgkmcnt(15)
v_addc_co_u32 v3, vcc, 0, v3, vcc
v_mov_b32 v5, s4
v_mfma_f32_16x16x32_bf16 v[220:223], v[124:127], v[56:59], v[220:223]
v_mov_b32 v10, s5
v_cndmask_b32 v2, v2, v5, s[30:31]
v_mfma_f32_16x16x32_bf16 v[224:227], v[96:99], v[60:63], v[224:227]
ds_read_b128 v[56:59], v24 offset:4096
v_cndmask_b32 v3, v3, v10, s[30:31]
v_mfma_f32_16x16x32_bf16 v[228:231], v[100:103], v[60:63], v[228:231]
s_xor_b32 s48, s95, 263168
s_lshl_b32 s93, s2, 4
s_add_u32 s93, s93, s3
v_mfma_f32_16x16x32_bf16 v[232:235], v[104:107], v[60:63], v[232:235]
s_lshl_b32 s93, s93, 2
buffer_store_dwordx4 a[132:135], v7, s[20:23], s48 offen sc1
v_mfma_f32_16x16x32_bf16 v[236:239], v[108:111], v[60:63], v[236:239]
s_add_u32 s93, s93, 0x10000
v_mfma_f32_16x16x32_bf16 v[240:243], v[112:115], v[60:63], v[240:243]
v_lshlrev_b32 v9, 20, v1
v_add_u32 v5, s93, v9
v_mfma_f32_16x16x32_bf16 v[244:247], v[116:119], v[60:63], v[244:247]
v_mfma_f32_16x16x32_bf16 v[248:251], v[120:123], v[60:63], v[248:251]
v_mfma_f32_16x16x32_bf16 v[252:255], v[124:127], v[60:63], v[252:255]
s_waitcnt lgkmcnt(0)
v_mfma_f32_16x16x32_bf16 v[128:131], v[64:67], v[48:51], v[128:131]
ds_read_b128 v[60:63], v24 offset:6144
v_mfma_f32_16x16x32_bf16 v[132:135], v[68:71], v[48:51], v[132:135]
v_mfma_f32_16x16x32_bf16 v[136:139], v[72:75], v[48:51], v[136:139]
ds_read_b64_tr_b16 v[96:97], v26 offset:24576
ds_read_b64_tr_b16 v[98:99], v26 offset:25600
v_mfma_f32_16x16x32_bf16 v[140:143], v[76:79], v[48:51], v[140:143]
v_mfma_f32_16x16x32_bf16 v[144:147], v[80:83], v[48:51], v[144:147]
ds_read_b64_tr_b16 v[100:101], v27 offset:24576
ds_read_b64_tr_b16 v[102:103], v27 offset:25600
v_mfma_f32_16x16x32_bf16 v[148:151], v[84:87], v[48:51], v[148:151]
v_mfma_f32_16x16x32_bf16 v[152:155], v[88:91], v[48:51], v[152:155]
ds_read_b64_tr_b16 v[104:105], v28 offset:24576
ds_read_b64_tr_b16 v[106:107], v28 offset:25600
v_mfma_f32_16x16x32_bf16 v[156:159], v[92:95], v[48:51], v[156:159]
v_mfma_f32_16x16x32_bf16 v[160:163], v[64:67], v[52:55], v[160:163]
ds_read_b128 v[48:51], v25 offset:0
v_mfma_f32_16x16x32_bf16 v[164:167], v[68:71], v[52:55], v[164:167]
v_mfma_f32_16x16x32_bf16 v[168:171], v[72:75], v[52:55], v[168:171]
s_xor_b32 s48, s95, 264192
ds_read_b64_tr_b16 v[108:109], v29 offset:24576
ds_read_b64_tr_b16 v[110:111], v29 offset:25600
v_mfma_f32_16x16x32_bf16 v[172:175], v[76:79], v[52:55], v[172:175]
buffer_store_dwordx4 a[136:139], v7, s[20:23], s48 offen sc1
v_mfma_f32_16x16x32_bf16 v[176:179], v[80:83], v[52:55], v[176:179]
ds_read_b64_tr_b16 v[112:113], v30 offset:24576
ds_read_b64_tr_b16 v[114:115], v30 offset:25600
v_mfma_f32_16x16x32_bf16 v[180:183], v[84:87], v[52:55], v[180:183]
v_mfma_f32_16x16x32_bf16 v[184:187], v[88:91], v[52:55], v[184:187]
ds_read_b64_tr_b16 v[116:117], v31 offset:24576
ds_read_b64_tr_b16 v[118:119], v31 offset:25600
v_mfma_f32_16x16x32_bf16 v[188:191], v[92:95], v[52:55], v[188:191]
v_mfma_f32_16x16x32_bf16 v[192:195], v[64:67], v[56:59], v[192:195]
ds_read_b128 v[52:55], v25 offset:2048
v_mfma_f32_16x16x32_bf16 v[196:199], v[68:71], v[56:59], v[196:199]
v_mfma_f32_16x16x32_bf16 v[200:203], v[72:75], v[56:59], v[200:203]
ds_read_b64_tr_b16 v[120:121], v32 offset:24576
ds_read_b64_tr_b16 v[122:123], v32 offset:25600
v_mfma_f32_16x16x32_bf16 v[204:207], v[76:79], v[56:59], v[204:207]
v_mfma_f32_16x16x32_bf16 v[208:211], v[80:83], v[56:59], v[208:211]
ds_read_b64_tr_b16 v[124:125], v33 offset:24576
ds_read_b64_tr_b16 v[126:127], v33 offset:25600
v_mfma_f32_16x16x32_bf16 v[212:215], v[84:87], v[56:59], v[212:215]
v_mfma_f32_16x16x32_bf16 v[216:219], v[88:91], v[56:59], v[216:219]
s_waitcnt lgkmcnt(15)
v_subrev_u32 v33, s91, v7
v_add_u32 v32, 0x10000, v33
v_mfma_f32_16x16x32_bf16 v[220:223], v[92:95], v[56:59], v[220:223]
v_add_u32 v12, s46, v32
v_and_b32 v8, 15, v1
v_mfma_f32_16x16x32_bf16 v[224:227], v[64:67], v[60:63], v[224:227]
ds_read_b128 v[56:59], v25 offset:4096
v_lshrrev_b32 v9, 4, v1
v_mfma_f32_16x16x32_bf16 v[228:231], v[68:71], v[60:63], v[228:231]
v_lshlrev_b32 v9, 2, v9
v_sub_u32 v8, v8, v9
v_mfma_f32_16x16x32_bf16 v[232:235], v[72:75], v[60:63], v[232:235]
ds_read_b128 v[20:23], v25 offset:6144
v_mov_b32 v34, 0xff800000
v_mfma_f32_16x16x32_bf16 v[236:239], v[76:79], v[60:63], v[236:239]
v_and_b32 v9, 3, v1
v_mov_b32 v10, 0x3f803f80
v_mfma_f32_16x16x32_bf16 v[240:243], v[80:83], v[60:63], v[240:243]
v_cmp_eq_u32 vcc, 0, v9
v_nop
v_mfma_f32_16x16x32_bf16 v[244:247], v[84:87], v[60:63], v[244:247]
v_nop
v_cndmask_b32 v14, 0, v10, vcc
v_mfma_f32_16x16x32_bf16 v[248:251], v[88:91], v[60:63], v[248:251]
s_add_u32 s28, s39, 0
s_and_b32 s28, s28, 0xffff
s_add_u32 s49, s39, 16384
v_mfma_f32_16x16x32_bf16 v[252:255], v[92:95], v[60:63], v[252:255]
s_waitcnt  lgkmcnt(0)
s_waitcnt vmcnt(4)
s_barrier
s_mov_b32 s40, 16384
v_mfma_f32_16x16x32_bf16 v[128:131], v[96:99], v[48:51], v[128:131]
s_add_u32 m0, s28, 0
s_xor_b32 s46, s39, 0x8000
s_mov_b32 s44, 65536
v_mfma_f32_16x16x32_bf16 v[132:135], v[100:103], v[48:51], v[132:135]
s_xor_b32 s50, s46, 0x18000
buffer_load_dwordx4 v7, s[8:11], s40 offen offset:0 sc1 lds
v_mfma_f32_16x16x32_bf16 v[136:139], v[104:107], v[48:51], v[136:139]
s_lshl_b32 s30, s89, 2
s_add_u32 s51, s30, s94
s_max_u32 s29, s51, 3
v_mfma_f32_16x16x32_bf16 v[140:143], v[108:111], v[48:51], v[140:143]
s_sub_u32 s42, s29, 3
s_sub_u32 s56, 57, s30
s_add_u32 s56, s56, s3
v_mfma_f32_16x16x32_bf16 v[144:147], v[112:115], v[48:51], v[144:147]
ds_read_b128 a[0:3], v12 offset:0
ds_read_b128 a[32:35], v12 offset:8192
v_mfma_f32_16x16x32_bf16 v[148:151], v[116:119], v[48:51], v[148:151]
s_mov_b32 s57, 2
buffer_load_dwordx4 v7, s[8:11], s40 offen offset:1024 sc1 lds
v_mfma_f32_16x16x32_bf16 v[152:155], v[120:123], v[48:51], v[152:155]
ds_read_b128 a[4:7], v12 offset:4096
ds_read_b128 a[36:39], v12 offset:12288
v_mfma_f32_16x16x32_bf16 v[156:159], v[124:127], v[48:51], v[156:159]
s_min_u32 s43, s42, s57
s_sub_u32 s55, s43, 1
s_sub_u32 s31, s51, s29
v_mfma_f32_16x16x32_bf16 v[160:163], v[96:99], v[52:55], v[160:163]
ds_read_b128 a[8:11], v12 offset:1024
ds_read_b128 a[40:43], v12 offset:9216
v_mfma_f32_16x16x32_bf16 v[164:167], v[100:103], v[52:55], v[164:167]
s_lshl_b32 s31, s31, 5
buffer_load_dwordx4 v7, s[8:11], s40 offen offset:2048 sc1 lds
v_mfma_f32_16x16x32_bf16 v[168:171], v[104:107], v[52:55], v[168:171]
ds_read_b128 a[12:15], v12 offset:5120
ds_read_b128 a[44:47], v12 offset:13312
v_mfma_f32_16x16x32_bf16 v[172:175], v[108:111], v[52:55], v[172:175]
v_mfma_f32_16x16x32_bf16 v[176:179], v[112:115], v[52:55], v[176:179]
ds_read_b128 a[16:19], v12 offset:2048
ds_read_b128 a[48:51], v12 offset:10240
v_mfma_f32_16x16x32_bf16 v[180:183], v[116:119], v[52:55], v[180:183]
buffer_load_dwordx4 v7, s[8:11], s40 offen offset:3072 sc1 lds
v_mfma_f32_16x16x32_bf16 v[184:187], v[120:123], v[52:55], v[184:187]
ds_read_b128 a[20:23], v12 offset:6144
ds_read_b128 a[52:55], v12 offset:14336
v_mfma_f32_16x16x32_bf16 v[188:191], v[124:127], v[52:55], v[188:191]
v_mfma_f32_16x16x32_bf16 v[192:195], v[96:99], v[56:59], v[192:195]
ds_read_b128 a[24:27], v12 offset:3072
ds_read_b128 a[56:59], v12 offset:11264
v_mfma_f32_16x16x32_bf16 v[196:199], v[100:103], v[56:59], v[196:199]
v_mfma_f32_16x16x32_bf16 v[200:203], v[104:107], v[56:59], v[200:203]
ds_read_b128 a[28:31], v12 offset:7168
ds_read_b128 a[60:63], v12 offset:15360
v_mfma_f32_16x16x32_bf16 v[204:207], v[108:111], v[56:59], v[204:207]
v_cvt_pk_bf16_f32 v64, v128, v129
v_cvt_pk_bf16_f32 v65, v130, v131
v_mfma_f32_16x16x32_bf16 v[208:211], v[112:115], v[56:59], v[208:211]
v_cvt_pk_bf16_f32 v66, v132, v133
v_cvt_pk_bf16_f32 v67, v134, v135
v_mfma_f32_16x16x32_bf16 v[212:215], v[116:119], v[56:59], v[212:215]
v_cvt_pk_bf16_f32 v80, v160, v161
v_cvt_pk_bf16_f32 v81, v162, v163
v_mfma_f32_16x16x32_bf16 v[216:219], v[120:123], v[56:59], v[216:219]
v_cvt_pk_bf16_f32 v82, v164, v165
v_cvt_pk_bf16_f32 v83, v166, v167
v_mfma_f32_16x16x32_bf16 v[220:223], v[124:127], v[56:59], v[220:223]
v_cvt_pk_bf16_f32 v68, v136, v137
v_cvt_pk_bf16_f32 v69, v138, v139
v_mfma_f32_16x16x32_bf16 v[224:227], v[96:99], v[20:23], v[224:227]
v_cvt_pk_bf16_f32 v70, v140, v141
v_cvt_pk_bf16_f32 v71, v142, v143
v_mfma_f32_16x16x32_bf16 v[228:231], v[100:103], v[20:23], v[228:231]
v_cvt_pk_bf16_f32 v84, v168, v169
v_cvt_pk_bf16_f32 v85, v170, v171
v_mfma_f32_16x16x32_bf16 v[232:235], v[104:107], v[20:23], v[232:235]
v_cvt_pk_bf16_f32 v86, v172, v173
v_cvt_pk_bf16_f32 v87, v174, v175
v_mfma_f32_16x16x32_bf16 v[236:239], v[108:111], v[20:23], v[236:239]
v_cvt_pk_bf16_f32 v72, v144, v145
v_cvt_pk_bf16_f32 v73, v146, v147
v_mfma_f32_16x16x32_bf16 v[240:243], v[112:115], v[20:23], v[240:243]
v_cvt_pk_bf16_f32 v74, v148, v149
v_cvt_pk_bf16_f32 v75, v150, v151
v_mfma_f32_16x16x32_bf16 v[244:247], v[116:119], v[20:23], v[244:247]
v_cvt_pk_bf16_f32 v88, v176, v177
v_cvt_pk_bf16_f32 v89, v178, v179
v_mfma_f32_16x16x32_bf16 v[248:251], v[120:123], v[20:23], v[248:251]
s_getpc_b64 s[58:59]
.Lpc0:
s_add_u32 s58, s58, .Ljob0-.Lpc0
s_addc_u32 s59, s59, 0
v_mfma_f32_16x16x32_bf16 v[252:255], v[124:127], v[20:23], v[252:255]
s_waitcnt lgkmcnt(0)
v_cvt_pk_bf16_f32 v90, v180, v181
v_cvt_pk_bf16_f32 v91, v182, v183
v_cvt_pk_bf16_f32 v76, v152, v153
v_cvt_pk_bf16_f32 v77, v154, v155
v_cvt_pk_bf16_f32 v78, v156, v157
v_cvt_pk_bf16_f32 v79, v158, v159
v_cvt_pk_bf16_f32 v92, v184, v185
v_cvt_pk_bf16_f32 v93, v186, v187
v_cvt_pk_bf16_f32 v94, v188, v189
v_cvt_pk_bf16_f32 v95, v190, v191
v_mfma_f32_16x16x32_bf16 v[128:131], a[0:3], v[64:67], 0
s_add_u32 s28, s39, 16384
s_and_b32 s28, s28, 0xffff
s_mov_b32 s41, 32768
v_add_u32 v62, s49, v32
v_mfma_f32_16x16x32_bf16 v[136:139], a[0:3], v[80:83], 0
s_add_u32 m0, s28, 0
v_cvt_pk_bf16_f32 v96, v192, v193
v_cvt_pk_bf16_f32 v97, v194, v195
v_mfma_f32_16x16x32_bf16 v[132:135], a[4:7], v[64:67], 0
buffer_load_dwordx4 v7, s[8:11], s41 offen offset:0 sc1 lds
v_cvt_pk_bf16_f32 v98, v196, v197
v_mfma_f32_16x16x32_bf16 v[140:143], a[4:7], v[80:83], 0
v_cvt_pk_bf16_f32 v99, v198, v199
v_cvt_pk_bf16_f32 v112, v224, v225
v_mfma_f32_16x16x32_bf16 v[128:131], a[8:11], v[68:71], v[128:131]
v_cvt_pk_bf16_f32 v113, v226, v227
v_cvt_pk_bf16_f32 v114, v228, v229
v_mfma_f32_16x16x32_bf16 v[136:139], a[8:11], v[84:87], v[136:139]
v_cvt_pk_bf16_f32 v115, v230, v231
v_cvt_pk_bf16_f32 v100, v200, v201
v_mfma_f32_16x16x32_bf16 v[132:135], a[12:15], v[68:71], v[132:135]
buffer_load_dwordx4 v7, s[8:11], s41 offen offset:1024 sc1 lds
v_mfma_f32_16x16x32_bf16 v[140:143], a[12:15], v[84:87], v[140:143]
v_cvt_pk_bf16_f32 v101, v202, v203
v_cvt_pk_bf16_f32 v102, v204, v205
v_mfma_f32_16x16x32_bf16 v[128:131], a[16:19], v[72:75], v[128:131]
v_cvt_pk_bf16_f32 v103, v206, v207
v_cvt_pk_bf16_f32 v116, v232, v233
v_mfma_f32_16x16x32_bf16 v[136:139], a[16:19], v[88:91], v[136:139]
v_cvt_pk_bf16_f32 v117, v234, v235
v_cvt_pk_bf16_f32 v118, v236, v237
v_mfma_f32_16x16x32_bf16 v[132:135], a[20:23], v[72:75], v[132:135]
buffer_load_dwordx4 v7, s[8:11], s41 offen offset:2048 sc1 lds
v_cvt_pk_bf16_f32 v119, v238, v239
v_mfma_f32_16x16x32_bf16 v[140:143], a[20:23], v[88:91], v[140:143]
v_cvt_pk_bf16_f32 v104, v208, v209
v_cvt_pk_bf16_f32 v105, v210, v211
v_mfma_f32_16x16x32_bf16 v[128:131], a[24:27], v[76:79], v[128:131]
v_cvt_pk_bf16_f32 v106, v212, v213
v_cvt_pk_bf16_f32 v107, v214, v215
v_mfma_f32_16x16x32_bf16 v[136:139], a[24:27], v[92:95], v[136:139]
v_cvt_pk_bf16_f32 v120, v240, v241
v_cvt_pk_bf16_f32 v121, v242, v243
v_mfma_f32_16x16x32_bf16 v[132:135], a[28:31], v[76:79], v[132:135]
s_add_u32 s28, s39, 32768
s_and_b32 s28, s28, 0xffff
buffer_load_dwordx4 v7, s[8:11], s41 offen offset:3072 sc1 lds
v_mfma_f32_16x16x32_bf16 v[140:143], a[28:31], v[92:95], v[140:143]
s_mov_b32 s40, 49152
v_cvt_pk_bf16_f32 v122, v244, v245
v_cvt_pk_bf16_f32 v123, v246, v247
v_mfma_f32_16x16x32_bf16 v[160:163], a[0:3], v[96:99], 0
s_add_u32 m0, s28, 0
v_cvt_pk_bf16_f32 v108, v216, v217
v_cvt_pk_bf16_f32 v109, v218, v219
v_cvt_pk_bf16_f32 v110, v220, v221
v_mfma_f32_16x16x32_bf16 v[168:171], a[0:3], v[112:115], 0
buffer_load_dwordx4 v7, s[8:11], s40 offen offset:0 sc1 lds
v_cvt_pk_bf16_f32 v111, v222, v223
v_cvt_pk_bf16_f32 v124, v248, v249
v_mfma_f32_16x16x32_bf16 v[164:167], a[4:7], v[96:99], 0
v_cvt_pk_bf16_f32 v125, v250, v251
v_cvt_pk_bf16_f32 v126, v252, v253
v_cvt_pk_bf16_f32 v127, v254, v255
v_mfma_f32_16x16x32_bf16 v[172:175], a[4:7], v[112:115], 0
ds_read_b128 a[0:3], v62 offset:0
ds_read_b128 a[4:7], v62 offset:4096
v_max3_f32 v56, v128, v129, v130
v_max3_f32 v57, v136, v137, v138
v_mfma_f32_16x16x32_bf16 v[160:163], a[8:11], v[100:103], v[160:163]
v_max3_f32 v58, v131, v132, v133
v_max3_f32 v59, v139, v140, v141
v_max3_f32 v56, v56, v134, v135
v_mfma_f32_16x16x32_bf16 v[168:171], a[8:11], v[116:119], v[168:171]
buffer_load_dwordx4 v7, s[8:11], s40 offen offset:1024 sc1 lds
v_max3_f32 v57, v57, v142, v143
v_max_f32 v56, v56, v58
v_mfma_f32_16x16x32_bf16 v[164:167], a[12:15], v[100:103], v[164:167]
v_max_f32 v57, v57, v59
v_mov_b32 v58, v56
v_mov_b32 v59, v57
v_mfma_f32_16x16x32_bf16 v[172:175], a[12:15], v[116:119], v[172:175]
ds_read_b128 a[8:11], v62 offset:1024
ds_read_b128 a[12:15], v62 offset:5120
v_nop
v_permlane16_swap_b32 v56, v58
v_mfma_f32_16x16x32_bf16 v[160:163], a[16:19], v[104:107], v[160:163]
v_permlane16_swap_b32 v57, v59
v_max_f32 v56, v56, v58
v_max_f32 v57, v57, v59
v_mfma_f32_16x16x32_bf16 v[168:171], a[16:19], v[120:123], v[168:171]
buffer_load_dwordx4 v7, s[8:11], s40 offen offset:2048 sc1 lds
v_mov_b32 v58, v56
v_mov_b32 v59, v57
v_mfma_f32_16x16x32_bf16 v[164:167], a[20:23], v[104:107], v[164:167]
v_nop
v_permlane32_swap_b32 v56, v58
v_permlane32_swap_b32 v57, v59
v_mfma_f32_16x16x32_bf16 v[172:175], a[20:23], v[120:123], v[172:175]
ds_read_b128 a[16:19], v62 offset:2048
ds_read_b128 a[20:23], v62 offset:6144
v_max_f32 v56, v56, v58
v_max_f32 v57, v57, v59
v_mfma_f32_16x16x32_bf16 v[160:163], a[24:27], v[108:111], v[160:163]
v_sub_f32 v192, 0, v56
v_sub_f32 v193, 0, v56
v_sub_f32 v194, 0, v56
v_mfma_f32_16x16x32_bf16 v[168:171], a[24:27], v[124:127], v[168:171]
buffer_load_dwordx4 v7, s[8:11], s40 offen offset:3072 sc1 lds
v_sub_f32 v195, 0, v56
v_sub_f32 v196, 0, v57
v_mfma_f32_16x16x32_bf16 v[164:167], a[28:31], v[108:111], v[164:167]
v_sub_f32 v197, 0, v57
v_sub_f32 v198, 0, v57
v_sub_f32 v199, 0, v57
v_mfma_f32_16x16x32_bf16 v[172:175], a[28:31], v[124:127], v[172:175]
ds_read_b128 a[24:27], v62 offset:3072
ds_read_b128 a[28:31], v62 offset:7168
v_add_f32 v128, v128, v192
v_add_f32 v129, v129, v192
v_mfma_f32_16x16x32_bf16 v[144:147], a[32:35], v[64:67], v[192:195]
s_xor_b32 s48, s95, 265216
v_add_f32 v130, v130, v192
v_add_f32 v131, v131, v192
v_mfma_f32_16x16x32_bf16 v[152:155], a[32:35], v[80:83], v[196:199]
buffer_store_dwordx4 a[140:143], v7, s[20:23], s48 offen sc1
v_add_f32 v132, v132, v192
v_add_f32 v133, v133, v192
v_mfma_f32_16x16x32_bf16 v[148:151], a[36:39], v[64:67], v[192:195]
s_cmp_eq_u32 s51, 0
s_cbranch_scc1 .Lj0a
.Lj0a_ret:
v_max3_f32 v56, v160, v161, v162
v_max3_f32 v57, v168, v169, v170
v_mfma_f32_16x16x32_bf16 v[156:159], a[36:39], v[80:83], v[196:199]
v_max3_f32 v58, v163, v164, v165
v_max3_f32 v59, v171, v172, v173
v_max3_f32 v56, v56, v166, v167
v_mfma_f32_16x16x32_bf16 v[144:147], a[40:43], v[68:71], v[144:147]
v_max3_f32 v57, v57, v174, v175
v_max_f32 v56, v56, v58
v_mfma_f32_16x16x32_bf16 v[152:155], a[40:43], v[84:87], v[152:155]
s_xor_b32 s48, s95, 266240
v_max_f32 v57, v57, v59
v_mov_b32 v58, v56
v_mfma_f32_16x16x32_bf16 v[148:151], a[44:47], v[68:71], v[148:151]
buffer_store_dwordx4 a[160:163], v7, s[20:23], s48 offen sc1
v_mov_b32 v59, v57
v_nop
v_mfma_f32_16x16x32_bf16 v[156:159], a[44:47], v[84:87], v[156:159]
v_permlane16_swap_b32 v56, v58
v_permlane16_swap_b32 v57, v59
v_mfma_f32_16x16x32_bf16 v[144:147], a[48:51], v[72:75], v[144:147]
v_max_f32 v56, v56, v58
v_max_f32 v57, v57, v59
v_mov_b32 v58, v56
v_mfma_f32_16x16x32_bf16 v[152:155], a[48:51], v[88:91], v[152:155]
v_mov_b32 v59, v57
v_nop
v_permlane32_swap_b32 v56, v58
v_mfma_f32_16x16x32_bf16 v[148:151], a[52:55], v[72:75], v[148:151]
s_xor_b32 s48, s95, 267264
v_permlane32_swap_b32 v57, v59
v_max_f32 v56, v56, v58
v_mfma_f32_16x16x32_bf16 v[156:159], a[52:55], v[88:91], v[156:159]
buffer_store_dwordx4 a[164:167], v7, s[20:23], s48 offen sc1
v_max_f32 v57, v57, v59
v_sub_f32 v200, 0, v56
v_mfma_f32_16x16x32_bf16 v[144:147], a[56:59], v[76:79], v[144:147]
v_sub_f32 v201, 0, v56
v_sub_f32 v202, 0, v56
v_mfma_f32_16x16x32_bf16 v[152:155], a[56:59], v[92:95], v[152:155]
v_sub_f32 v203, 0, v56
v_sub_f32 v204, 0, v57
v_sub_f32 v205, 0, v57
v_mfma_f32_16x16x32_bf16 v[148:151], a[60:63], v[76:79], v[148:151]
v_sub_f32 v206, 0, v57
v_sub_f32 v207, 0, v57
v_mfma_f32_16x16x32_bf16 v[156:159], a[60:63], v[92:95], v[156:159]
s_xor_b32 s48, s95, 268288
v_add_f32 v134, v134, v192
v_add_f32 v135, v135, v192
v_mfma_f32_16x16x32_bf16 v[176:179], a[32:35], v[96:99], v[200:203]
buffer_store_dwordx4 a[168:171], v7, s[20:23], s48 offen sc1
v_mfma_f32_16x16x32_bf16 v[184:187], a[32:35], v[112:115], v[204:207]
v_add_f32 v136, v136, v196
v_add_f32 v137, v137, v196
v_mfma_f32_16x16x32_bf16 v[180:183], a[36:39], v[96:99], v[200:203]
v_add_f32 v138, v138, v196
v_add_f32 v139, v139, v196
v_mfma_f32_16x16x32_bf16 v[188:191], a[36:39], v[112:115], v[204:207]
v_add_f32 v140, v140, v196
v_mfma_f32_16x16x32_bf16 v[176:179], a[40:43], v[100:103], v[176:179]
s_xor_b32 s48, s95, 269312
v_add_f32 v141, v141, v196
v_add_f32 v142, v142, v196
v_mfma_f32_16x16x32_bf16 v[184:187], a[40:43], v[116:119], v[184:187]
buffer_store_dwordx4 a[172:175], v7, s[20:23], s48 offen sc1
v_mfma_f32_16x16x32_bf16 v[180:183], a[44:47], v[100:103], v[180:183]
v_add_f32 v143, v143, v196
v_add_u32 v63, s39, v33
v_mfma_f32_16x16x32_bf16 v[188:191], a[44:47], v[116:119], v[188:191]
v_add_u32 v13, s31, v8
v_mov_b32 v52, v14
v_mfma_f32_16x16x32_bf16 v[176:179], a[48:51], v[104:107], v[176:179]
v_mov_b32 v53, v14
v_mfma_f32_16x16x32_bf16 v[184:187], a[48:51], v[120:123], v[184:187]
s_xor_b32 s48, s95, 393216
v_mov_b32 v54, v14
v_mov_b32 v55, v14
v_mfma_f32_16x16x32_bf16 v[180:183], a[52:55], v[104:107], v[180:183]
buffer_store_dwordx4 a[192:195], v7, s[20:23], s48 offen sc1
v_mfma_f32_16x16x32_bf16 v[188:191], a[52:55], v[120:123], v[188:191]
v_add_f32 v160, v160, v200
v_add_f32 v161, v161, v200
v_mfma_f32_16x16x32_bf16 v[176:179], a[56:59], v[108:111], v[176:179]
v_add_f32 v162, v162, v200
v_add_f32 v163, v163, v200
v_mfma_f32_16x16x32_bf16 v[184:187], a[56:59], v[124:127], v[184:187]
v_add_f32 v164, v164, v200
v_mfma_f32_16x16x32_bf16 v[180:183], a[60:63], v[108:111], v[180:183]
v_add_f32 v165, v165, v200
v_add_f32 v166, v166, v200
v_mfma_f32_16x16x32_bf16 v[188:191], a[60:63], v[124:127], v[188:191]
v_add_f32 v167, v167, v200
v_add_f32 v168, v168, v204
v_add_f32 v169, v169, v204
v_add_f32 v170, v170, v204
v_add_f32 v171, v171, v204
v_add_f32 v172, v172, v204
v_add_f32 v173, v173, v204
v_add_f32 v174, v174, v204
v_add_f32 v175, v175, v204
v_exp_f32 v128, v128
v_exp_f32 v129, v129
v_exp_f32 v160, v160
v_cvt_pk_bf16_f32 v224, v128, v129
v_exp_f32 v161, v161
v_exp_f32 v130, v130
v_cvt_pk_bf16_f32 v240, v160, v161
v_exp_f32 v131, v131
v_exp_f32 v162, v162
v_cvt_pk_bf16_f32 v225, v130, v131
v_exp_f32 v163, v163
v_exp_f32 v132, v132
v_cvt_pk_bf16_f32 v241, v162, v163
v_exp_f32 v133, v133
v_exp_f32 v164, v164
v_cvt_pk_bf16_f32 v226, v132, v133
v_exp_f32 v165, v165
v_exp_f32 v134, v134
v_cvt_pk_bf16_f32 v242, v164, v165
v_exp_f32 v135, v135
v_exp_f32 v166, v166
v_cvt_pk_bf16_f32 v227, v134, v135
v_exp_f32 v167, v167
v_exp_f32 v136, v136
v_cvt_pk_bf16_f32 v243, v166, v167
v_exp_f32 v137, v137
v_exp_f32 v168, v168
v_cvt_pk_bf16_f32 v228, v136, v137
v_exp_f32 v169, v169
v_exp_f32 v138, v138
v_cvt_pk_bf16_f32 v244, v168, v169
v_exp_f32 v139, v139
v_exp_f32 v170, v170
v_cvt_pk_bf16_f32 v229, v138, v139
v_exp_f32 v171, v171
v_exp_f32 v140, v140
v_cvt_pk_bf16_f32 v245, v170, v171
v_exp_f32 v141, v141
v_exp_f32 v172, v172
v_cvt_pk_bf16_f32 v230, v140, v141
v_exp_f32 v173, v173
v_exp_f32 v142, v142
v_cvt_pk_bf16_f32 v246, v172, v173
v_exp_f32 v143, v143
v_exp_f32 v174, v174
v_cvt_pk_bf16_f32 v231, v142, v143
v_exp_f32 v175, v175
s_nop 0
v_cvt_pk_bf16_f32 v247, v174, v175
s_cmp_lt_u32 s51, 2
s_cbranch_scc1 .Lj0b
.Lj0b_ret:
s_waitcnt vmcnt(14) lgkmcnt(0)
s_barrier
v_mfma_f32_16x16x32_bf16 v[128:131], a[0:3], v[64:67], v[192:195]
v_exp_f32 v144, v144
v_mfma_f32_16x16x32_bf16 v[136:139], a[0:3], v[80:83], v[196:199]
ds_read_b128 a[32:35], v63 offset:0
ds_read_b128 a[36:39], v63 offset:1024
v_cmp_gt_i32 s[32:33], 0, v13
v_mfma_f32_16x16x32_bf16 v[160:163], a[0:3], v[96:99], v[200:203]
v_exp_f32 v145, v145
v_mfma_f32_16x16x32_bf16 v[168:171], a[0:3], v[112:115], v[204:207]
s_xor_b32 s48, s95, 394240
ds_read_b128 a[0:3], v62 offset:8192
v_nop
v_mfma_f32_16x16x32_bf16 v[132:135], a[4:7], v[64:67], v[192:195]
buffer_store_dwordx4 a[196:199], v7, s[20:23], s48 offen sc1
v_mfma_f32_16x16x32_bf16 v[140:143], a[4:7], v[80:83], v[196:199]
ds_read_b128 a[40:43], v63 offset:2048
ds_read_b128 a[44:47], v63 offset:3072
v_cmp_gt_i32 s[34:35], 1, v13
v_mfma_f32_16x16x32_bf16 v[164:167], a[4:7], v[96:99], v[200:203]
s_sub_u32 s52, s52, 1
s_cselect_b32 s52, 0, s52
s_cselect_b32 s53, 0, 0x8000
v_cndmask_b32 v36, v200, v34, s[32:33]
v_mfma_f32_16x16x32_bf16 v[172:175], a[4:7], v[112:115], v[204:207]
ds_read_b128 a[4:7], v62 offset:12288
v_exp_f32 v176, v176
v_mfma_f32_16x16x32_bf16 v[128:131], a[8:11], v[68:71], v[128:131]
v_cvt_pk_bf16_f32 v232, v144, v145
v_cmp_gt_i32 s[32:33], 2, v13
v_mfma_f32_16x16x32_bf16 v[136:139], a[8:11], v[84:87], v[136:139]
ds_read_b128 a[48:51], v63 offset:4096
ds_read_b128 a[52:55], v63 offset:5120
v_mfma_f32_16x16x32_bf16 v[160:163], a[8:11], v[100:103], v[160:163]
v_exp_f32 v177, v177
v_mfma_f32_16x16x32_bf16 v[168:171], a[8:11], v[116:119], v[168:171]
s_xor_b32 s48, s95, 395264
ds_read_b128 a[8:11], v62 offset:9216
v_cndmask_b32 v37, v200, v34, s[34:35]
v_mfma_f32_16x16x32_bf16 v[132:135], a[12:15], v[68:71], v[132:135]
buffer_store_dwordx4 a[200:203], v7, s[20:23], s48 offen sc1
v_mfma_f32_16x16x32_bf16 v[140:143], a[12:15], v[84:87], v[140:143]
ds_read_b128 a[56:59], v63 offset:6144
ds_read_b128 a[60:63], v63 offset:7168
v_cmp_gt_i32 s[34:35], 3, v13
v_mfma_f32_16x16x32_bf16 v[164:167], a[12:15], v[100:103], v[164:167]
v_exp_f32 v146, v146
v_mfma_f32_16x16x32_bf16 v[172:175], a[12:15], v[116:119], v[172:175]
ds_read_b128 a[12:15], v62 offset:13312
v_cvt_pk_bf16_f32 v248, v176, v177
v_cndmask_b32 v38, v200, v34, s[32:33]
v_mfma_f32_16x16x32_bf16 v[128:131], a[16:19], v[72:75], v[128:131]
v_exp_f32 v147, v147
v_mfma_f32_16x16x32_bf16 v[136:139], a[16:19], v[88:91], v[136:139]
v_exp_f32 v178, v178
v_cmp_gt_i32 s[32:33], 16, v13
v_mfma_f32_16x16x32_bf16 v[160:163], a[16:19], v[104:107], v[160:163]
v_cvt_pk_bf16_f32 v233, v146, v147
v_cndmask_b32 v39, v200, v34, s[34:35]
v_mfma_f32_16x16x32_bf16 v[168:171], a[16:19], v[120:123], v[168:171]
s_xor_b32 s48, s95, 396288
ds_read_b128 a[16:19], v62 offset:10240
v_mfma_f32_16x16x32_bf16 v[132:135], a[20:23], v[72:75], v[132:135]
buffer_store_dwordx4 a[204:207], v7, s[20:23], s48 offen sc1
v_mfma_f32_16x16x32_bf16 v[140:143], a[20:23], v[88:91], v[140:143]
v_exp_f32 v179, v179
v_cmp_gt_i32 s[34:35], 17, v13
v_mfma_f32_16x16x32_bf16 v[164:167], a[20:23], v[104:107], v[164:167]
v_exp_f32 v148, v148
v_cndmask_b32 v40, v200, v34, s[32:33]
v_mfma_f32_16x16x32_bf16 v[172:175], a[20:23], v[120:123], v[172:175]
ds_read_b128 a[20:23], v62 offset:14336
v_cvt_pk_bf16_f32 v249, v178, v179
v_mfma_f32_16x16x32_bf16 v[128:131], a[24:27], v[76:79], v[128:131]
v_exp_f32 v149, v149
v_cmp_gt_i32 s[32:33], 18, v13
v_mfma_f32_16x16x32_bf16 v[136:139], a[24:27], v[92:95], v[136:139]
v_exp_f32 v180, v180
v_cvt_pk_bf16_f32 v234, v148, v149
v_mfma_f32_16x16x32_bf16 v[160:163], a[24:27], v[108:111], v[160:163]
v_exp_f32 v181, v181
v_cndmask_b32 v41, v200, v34, s[34:35]
v_mfma_f32_16x16x32_bf16 v[168:171], a[24:27], v[124:127], v[168:171]
s_xor_b32 s48, s95, 397312
ds_read_b128 a[24:27], v62 offset:11264
v_mfma_f32_16x16x32_bf16 v[132:135], a[28:31], v[76:79], v[132:135]
buffer_store_dwordx4 a[224:227], v7, s[20:23], s48 offen sc1
v_cmp_gt_i32 s[34:35], 19, v13
v_mfma_f32_16x16x32_bf16 v[140:143], a[28:31], v[92:95], v[140:143]
v_exp_f32 v150, v150
v_cvt_pk_bf16_f32 v250, v180, v181
v_mfma_f32_16x16x32_bf16 v[164:167], a[28:31], v[108:111], v[164:167]
v_exp_f32 v151, v151
v_cndmask_b32 v42, v200, v34, s[32:33]
v_mfma_f32_16x16x32_bf16 v[172:175], a[28:31], v[124:127], v[172:175]
ds_read_b128 a[28:31], v62 offset:15360
v_exp_f32 v182, v182
v_mfma_f32_16x16x32_bf16 v[208:211], v[52:55], v[224:227], 0
v_add_u32 v62, s46, v32
v_cvt_pk_bf16_f32 v235, v150, v151
v_cmp_gt_i32 s[32:33], -16, v13
v_mfma_f32_16x16x32_bf16 v[212:215], v[52:55], v[228:231], 0
v_exp_f32 v183, v183
v_mfma_f32_16x16x32_bf16 v[216:219], v[52:55], v[240:243], 0
v_exp_f32 v152, v152
v_cndmask_b32 v43, v200, v34, s[34:35]
v_mfma_f32_16x16x32_bf16 v[220:223], v[52:55], v[244:247], 0
s_waitcnt lgkmcnt(5)
s_xor_b32 s48, s95, 398336
v_cvt_pk_bf16_f32 v251, v182, v183
v_mfma_f32_16x16x32_bf16 a[64:67], a[32:35], v[224:227], 0
.Lpad10:
s_nop 0
buffer_store_dwordx4 a[228:231], v7, s[20:23], s48 offen sc1
v_cmp_gt_i32 s[34:35], -15, v13
v_mfma_f32_16x16x32_bf16 a[72:75], a[32:35], v[228:231], 0
v_exp_f32 v153, v153
v_mfma_f32_16x16x32_bf16 a[176:179], a[32:35], v[240:243], 0
v_exp_f32 v184, v184
v_cndmask_b32 v44, v204, v34, s[32:33]
v_mfma_f32_16x16x32_bf16 a[184:187], a[32:35], v[244:247], 0
ds_read_b128 a[32:35], v63 offset:8192
v_cvt_pk_bf16_f32 v236, v152, v153
v_mfma_f32_16x16x32_bf16 a[68:71], a[36:39], v[224:227], 0
v_exp_f32 v185, v185
v_cmp_gt_i32 s[32:33], -14, v13
v_mfma_f32_16x16x32_bf16 a[76:79], a[36:39], v[228:231], 0
v_exp_f32 v154, v154
v_mfma_f32_16x16x32_bf16 a[180:183], a[36:39], v[240:243], 0
v_cvt_pk_bf16_f32 v252, v184, v185
v_cndmask_b32 v45, v204, v34, s[34:35]
v_mfma_f32_16x16x32_bf16 a[188:191], a[36:39], v[244:247], 0
s_xor_b32 s48, s95, 399360
ds_read_b128 a[36:39], v63 offset:9216
v_mfma_f32_16x16x32_bf16 a[80:83], a[40:43], v[224:227], 0
buffer_store_dwordx4 a[232:235], v7, s[20:23], s48 offen sc1
v_cmp_gt_i32 s[34:35], -13, v13
v_mfma_f32_16x16x32_bf16 a[88:91], a[40:43], v[228:231], 0
v_exp_f32 v155, v155
v_mfma_f32_16x16x32_bf16 a[208:211], a[40:43], v[240:243], 0
v_exp_f32 v186, v186
v_cndmask_b32 v46, v204, v34, s[32:33]
v_mfma_f32_16x16x32_bf16 a[216:219], a[40:43], v[244:247], 0
ds_read_b128 a[40:43], v63 offset:10240
v_cvt_pk_bf16_f32 v237, v154, v155
v_mfma_f32_16x16x32_bf16 a[84:87], a[44:47], v[224:227], 0
v_exp_f32 v187, v187
v_cmp_gt_i32 s[32:33], 0, v13
v_mfma_f32_16x16x32_bf16 a[92:95], a[44:47], v[228:231], 0
v_exp_f32 v156, v156
v_mfma_f32_16x16x32_bf16 a[212:215], a[44:47], v[240:243], 0
v_cvt_pk_bf16_f32 v253, v186, v187
v_cndmask_b32 v47, v204, v34, s[34:35]
v_mfma_f32_16x16x32_bf16 a[220:223], a[44:47], v[244:247], 0
s_xor_b32 s48, s95, 400384
ds_read_b128 a[44:47], v63 offset:11264
v_mfma_f32_16x16x32_bf16 a[112:115], a[48:51], v[224:227], 0
buffer_store_dwordx4 a[236:239], v7, s[20:23], s48 offen sc1
v_cmp_gt_i32 s[34:35], 1, v13
v_mfma_f32_16x16x32_bf16 a[120:123], a[48:51], v[228:231], 0
v_exp_f32 v157, v157
v_mfma_f32_16x16x32_bf16 a[240:243], a[48:51], v[240:243], 0
global_load_dword v35, v[2:3], off
v_cndmask_b32 v48, v204, v34, s[32:33]
v_mfma_f32_16x16x32_bf16 a[248:251], a[48:51], v[244:247], 0
ds_read_b128 a[48:51], v63 offset:12288
v_add_co_u32 v2, vcc, s53, v2
v_mfma_f32_16x16x32_bf16 a[116:119], a[52:55], v[224:227], 0
v_exp_f32 v188, v188
v_cmp_gt_i32 s[32:33], 2, v13
v_mfma_f32_16x16x32_bf16 a[124:127], a[52:55], v[228:231], 0
v_addc_co_u32 v3, vcc, 0, v3, vcc
v_cvt_pk_bf16_f32 v238, v156, v157
v_mfma_f32_16x16x32_bf16 a[244:247], a[52:55], v[240:243], 0
v_exp_f32 v189, v189
v_cndmask_b32 v49, v204, v34, s[34:35]
v_mfma_f32_16x16x32_bf16 a[252:255], a[52:55], v[244:247], 0
ds_read_b128 a[52:55], v63 offset:13312
v_exp_f32 v158, v158
v_mfma_f32_16x16x32_bf16 a[144:147], a[56:59], v[224:227], 0
v_cvt_pk_bf16_f32 v254, v188, v189
v_cmp_gt_i32 s[34:35], 3, v13
v_mfma_f32_16x16x32_bf16 a[152:155], a[56:59], v[228:231], 0
v_exp_f32 v159, v159
v_mfma_f32_16x16x32_bf16 a[96:99], a[56:59], v[240:243], 0
v_exp_f32 v190, v190
v_cndmask_b32 v50, v204, v34, s[32:33]
v_mfma_f32_16x16x32_bf16 a[104:107], a[56:59], v[244:247], 0
ds_read_b128 a[56:59], v63 offset:14336
v_cvt_pk_bf16_f32 v239, v158, v159
v_mfma_f32_16x16x32_bf16 a[148:151], a[60:63], v[224:227], 0
v_exp_f32 v191, v191
v_mfma_f32_16x16x32_bf16 a[156:159], a[60:63], v[228:231], 0
s_waitcnt lgkmcnt(7)
v_nop
v_cndmask_b32 v51, v204, v34, s[34:35]
v_mfma_f32_16x16x32_bf16 a[100:103], a[60:63], v[240:243], 0
s_waitcnt vmcnt(18)
s_barrier
v_cvt_pk_bf16_f32 v255, v190, v191
v_mfma_f32_16x16x32_bf16 a[108:111], a[60:63], v[244:247], 0
ds_read_b128 a[60:63], v63 offset:15360
s_cmp_lt_u32 s51, 3
s_cbranch_scc1 .Lj0c
s_cmp_eq_u32 s29, 3
s_cbranch_scc1 .LT1
s_nop 0
s_nop 0
s_nop 0
.LD1:
v_mfma_f32_16x16x32_bf16 v[144:147], a[0:3], v[64:67], v[192:195]
s_cmp_lt_u32 s44, s54
v_exp_f32 v128, v128
v_mfma_f32_16x16x32_bf16 v[152:155], a[0:3], v[80:83], v[196:199]
s_cselect_b32 m0, s50, s27
v_exp_f32 v129, v129
v_mfma_f32_16x16x32_bf16 v[176:179], a[0:3], v[96:99], v[200:203]
v_add_u32 v63, s49, v33
v_mfma_f32_16x16x32_bf16 v[184:187], a[0:3], v[112:115], v[204:207]
ds_read_b128 a[0:3], v62 offset:0
v_mfma_f32_16x16x32_bf16 v[148:151], a[4:7], v[64:67], v[192:195]
v_exp_f32 v160, v160
v_mfma_f32_16x16x32_bf16 v[156:159], a[4:7], v[80:83], v[196:199]
v_cvt_pk_bf16_f32 v224, v128, v129
v_mfma_f32_16x16x32_bf16 v[180:183], a[4:7], v[96:99], v[200:203]
v_exp_f32 v161, v161
v_mfma_f32_16x16x32_bf16 v[188:191], a[4:7], v[112:115], v[204:207]
ds_read_b128 a[4:7], v62 offset:4096
v_mfma_f32_16x16x32_bf16 v[144:147], a[8:11], v[68:71], v[144:147]
buffer_load_dwordx4 v7, s[4:7], s44 offen offset:0 sc1 lds
v_mfma_f32_16x16x32_bf16 v[152:155], a[8:11], v[84:87], v[152:155]
v_exp_f32 v130, v130
v_mfma_f32_16x16x32_bf16 v[176:179], a[8:11], v[100:103], v[176:179]
v_cvt_pk_bf16_f32 v240, v160, v161
v_mfma_f32_16x16x32_bf16 v[184:187], a[8:11], v[116:119], v[184:187]
ds_read_b128 a[8:11], v62 offset:1024
v_mfma_f32_16x16x32_bf16 v[148:151], a[12:15], v[68:71], v[148:151]
s_mov_b32 s49, s46
v_exp_f32 v131, v131
v_mfma_f32_16x16x32_bf16 v[156:159], a[12:15], v[84:87], v[156:159]
v_exp_f32 v162, v162
v_mfma_f32_16x16x32_bf16 v[180:183], a[12:15], v[100:103], v[180:183]
v_cvt_pk_bf16_f32 v225, v130, v131
v_mfma_f32_16x16x32_bf16 v[188:191], a[12:15], v[116:119], v[188:191]
ds_read_b128 a[12:15], v62 offset:5120
v_mfma_f32_16x16x32_bf16 v[144:147], a[16:19], v[72:75], v[144:147]
v_exp_f32 v163, v163
v_mfma_f32_16x16x32_bf16 v[152:155], a[16:19], v[88:91], v[152:155]
v_exp_f32 v132, v132
v_mfma_f32_16x16x32_bf16 v[176:179], a[16:19], v[104:107], v[176:179]
s_add_u32 s46, s46, 16384
v_cvt_pk_bf16_f32 v241, v162, v163
v_mfma_f32_16x16x32_bf16 v[184:187], a[16:19], v[120:123], v[184:187]
ds_read_b128 a[16:19], v62 offset:2048
v_mfma_f32_16x16x32_bf16 v[148:151], a[20:23], v[72:75], v[148:151]
buffer_load_dwordx4 v7, s[4:7], s44 offen offset:1024 sc1 lds
v_mfma_f32_16x16x32_bf16 v[156:159], a[20:23], v[88:91], v[156:159]
v_exp_f32 v133, v133
v_mfma_f32_16x16x32_bf16 v[180:183], a[20:23], v[104:107], v[180:183]
v_exp_f32 v164, v164
v_mfma_f32_16x16x32_bf16 v[188:191], a[20:23], v[120:123], v[188:191]
ds_read_b128 a[20:23], v62 offset:6144
v_cvt_pk_bf16_f32 v226, v132, v133
v_mfma_f32_16x16x32_bf16 v[144:147], a[24:27], v[76:79], v[144:147]
v_exp_f32 v165, v165
v_mfma_f32_16x16x32_bf16 v[152:155], a[24:27], v[92:95], v[152:155]
v_exp_f32 v134, v134
v_mfma_f32_16x16x32_bf16 v[176:179], a[24:27], v[108:111], v[176:179]
s_and_b32 s46, s46, 0xffff
v_cvt_pk_bf16_f32 v242, v164, v165
v_mfma_f32_16x16x32_bf16 v[184:187], a[24:27], v[124:127], v[184:187]
ds_read_b128 a[24:27], v62 offset:3072
v_mfma_f32_16x16x32_bf16 v[148:151], a[28:31], v[76:79], v[148:151]
v_exp_f32 v135, v135
v_mfma_f32_16x16x32_bf16 v[156:159], a[28:31], v[92:95], v[156:159]
v_exp_f32 v166, v166
v_mfma_f32_16x16x32_bf16 v[180:183], a[28:31], v[108:111], v[180:183]
v_cvt_pk_bf16_f32 v227, v134, v135
v_mfma_f32_16x16x32_bf16 v[188:191], a[28:31], v[124:127], v[188:191]
ds_read_b128 a[28:31], v62 offset:7168
v_mfma_f32_16x16x32_bf16 v[208:211], v[52:55], v[232:235], v[208:211]
v_exp_f32 v167, v167
v_mfma_f32_16x16x32_bf16 v[212:215], v[52:55], v[236:239], v[212:215]
v_exp_f32 v136, v136
v_mfma_f32_16x16x32_bf16 v[216:219], v[52:55], v[248:251], v[216:219]
v_cvt_pk_bf16_f32 v243, v166, v167
v_mfma_f32_16x16x32_bf16 v[220:223], v[52:55], v[252:255], v[220:223]
s_waitcnt lgkmcnt(8)
v_exp_f32 v137, v137
v_mfma_f32_16x16x32_bf16 a[64:67], a[32:35], v[232:235], a[64:67]
buffer_load_dwordx4 v7, s[4:7], s44 offen offset:2048 sc1 lds
v_mfma_f32_16x16x32_bf16 a[72:75], a[32:35], v[236:239], a[72:75]
v_exp_f32 v168, v168
v_mfma_f32_16x16x32_bf16 a[176:179], a[32:35], v[248:251], a[176:179]
v_cvt_pk_bf16_f32 v228, v136, v137
v_mfma_f32_16x16x32_bf16 a[184:187], a[32:35], v[252:255], a[184:187]
ds_read_b128 a[32:35], v63 offset:0
v_mfma_f32_16x16x32_bf16 a[68:71], a[36:39], v[232:235], a[68:71]
v_exp_f32 v169, v169
v_mfma_f32_16x16x32_bf16 a[76:79], a[36:39], v[236:239], a[76:79]
v_exp_f32 v138, v138
v_mfma_f32_16x16x32_bf16 a[180:183], a[36:39], v[248:251], a[180:183]
v_cvt_pk_bf16_f32 v244, v168, v169
v_mfma_f32_16x16x32_bf16 a[188:191], a[36:39], v[252:255], a[188:191]
ds_read_b128 a[36:39], v63 offset:1024
v_mfma_f32_16x16x32_bf16 a[80:83], a[40:43], v[232:235], a[80:83]
v_exp_f32 v139, v139
v_mfma_f32_16x16x32_bf16 a[88:91], a[40:43], v[236:239], a[88:91]
v_exp_f32 v170, v170
v_mfma_f32_16x16x32_bf16 a[208:211], a[40:43], v[248:251], a[208:211]
v_cvt_pk_bf16_f32 v229, v138, v139
v_mfma_f32_16x16x32_bf16 a[216:219], a[40:43], v[252:255], a[216:219]
ds_read_b128 a[40:43], v63 offset:2048
v_mfma_f32_16x16x32_bf16 a[84:87], a[44:47], v[232:235], a[84:87]
buffer_load_dwordx4 v7, s[4:7], s44 offen offset:3072 sc1 lds
v_mfma_f32_16x16x32_bf16 a[92:95], a[44:47], v[236:239], a[92:95]
v_exp_f32 v171, v171
v_mfma_f32_16x16x32_bf16 a[212:215], a[44:47], v[248:251], a[212:215]
v_exp_f32 v140, v140
v_mfma_f32_16x16x32_bf16 a[220:223], a[44:47], v[252:255], a[220:223]
ds_read_b128 a[44:47], v63 offset:3072
v_cvt_pk_bf16_f32 v245, v170, v171
v_mfma_f32_16x16x32_bf16 a[112:115], a[48:51], v[232:235], a[112:115]
v_exp_f32 v141, v141
v_mfma_f32_16x16x32_bf16 a[120:123], a[48:51], v[236:239], a[120:123]
v_exp_f32 v172, v172
v_mfma_f32_16x16x32_bf16 a[240:243], a[48:51], v[248:251], a[240:243]
v_mfma_f32_16x16x32_bf16 a[248:251], a[48:51], v[252:255], a[248:251]
ds_read_b128 a[48:51], v63 offset:4096
v_cvt_pk_bf16_f32 v230, v140, v141
v_mfma_f32_16x16x32_bf16 a[116:119], a[52:55], v[232:235], a[116:119]
s_sub_u32 s55, s55, 1
v_exp_f32 v173, v173
v_mfma_f32_16x16x32_bf16 a[124:127], a[52:55], v[236:239], a[124:127]
v_exp_f32 v142, v142
v_mfma_f32_16x16x32_bf16 a[244:247], a[52:55], v[248:251], a[244:247]
v_mfma_f32_16x16x32_bf16 a[252:255], a[52:55], v[252:255], a[252:255]
ds_read_b128 a[52:55], v63 offset:5120
v_cvt_pk_bf16_f32 v246, v172, v173
v_mfma_f32_16x16x32_bf16 a[144:147], a[56:59], v[232:235], a[144:147]
v_exp_f32 v143, v143
v_mfma_f32_16x16x32_bf16 a[152:155], a[56:59], v[236:239], a[152:155]
v_mfma_f32_16x16x32_bf16 a[96:99], a[56:59], v[248:251], a[96:99]
v_exp_f32 v174, v174
v_mfma_f32_16x16x32_bf16 a[104:107], a[56:59], v[252:255], a[104:107]
ds_read_b128 a[56:59], v63 offset:6144
v_cvt_pk_bf16_f32 v231, v142, v143
v_mfma_f32_16x16x32_bf16 a[148:151], a[60:63], v[232:235], a[148:151]
v_exp_f32 v175, v175
v_mfma_f32_16x16x32_bf16 a[156:159], a[60:63], v[236:239], a[156:159]
s_waitcnt lgkmcnt(7)
v_mfma_f32_16x16x32_bf16 a[100:103], a[60:63], v[248:251], a[100:103]
v_cvt_pk_bf16_f32 v247, v174, v175
v_mfma_f32_16x16x32_bf16 a[108:111], a[60:63], v[252:255], a[108:111]
ds_read_b128 a[60:63], v63 offset:7168
s_cbranch_scc1 .Lev_d1
.LD0:
v_mfma_f32_16x16x32_bf16 v[128:131], a[0:3], v[64:67], v[192:195]
s_cmp_lt_u32 s44, s45
v_exp_f32 v144, v144
v_mfma_f32_16x16x32_bf16 v[136:139], a[0:3], v[80:83], v[196:199]
s_cselect_b32 m0, s46, s27
v_exp_f32 v145, v145
v_mfma_f32_16x16x32_bf16 v[160:163], a[0:3], v[96:99], v[200:203]
v_exp_f32 v176, v176
v_mfma_f32_16x16x32_bf16 v[168:171], a[0:3], v[112:115], v[204:207]
ds_read_b128 a[0:3], v62 offset:8192
v_cvt_pk_bf16_f32 v232, v144, v145
v_mfma_f32_16x16x32_bf16 v[132:135], a[4:7], v[64:67], v[192:195]
s_sub_u32 s52, s52, 1
s_cselect_b32 s52, 0, s52
s_cselect_b32 s53, 0, 0x8000
v_mfma_f32_16x16x32_bf16 v[140:143], a[4:7], v[80:83], v[196:199]
v_exp_f32 v177, v177
v_mfma_f32_16x16x32_bf16 v[164:167], a[4:7], v[96:99], v[200:203]
v_exp_f32 v146, v146
v_mfma_f32_16x16x32_bf16 v[172:175], a[4:7], v[112:115], v[204:207]
ds_read_b128 a[4:7], v62 offset:12288
v_cvt_pk_bf16_f32 v248, v176, v177
v_mfma_f32_16x16x32_bf16 v[128:131], a[8:11], v[68:71], v[128:131]
buffer_load_dwordx4 v7, s[8:11], s44 offen offset:0 sc1 lds
v_mfma_f32_16x16x32_bf16 v[136:139], a[8:11], v[84:87], v[136:139]
v_exp_f32 v147, v147
v_mfma_f32_16x16x32_bf16 v[160:163], a[8:11], v[100:103], v[160:163]
v_exp_f32 v178, v178
v_mfma_f32_16x16x32_bf16 v[168:171], a[8:11], v[116:119], v[168:171]
ds_read_b128 a[8:11], v62 offset:9216
v_mfma_f32_16x16x32_bf16 v[132:135], a[12:15], v[68:71], v[132:135]
s_xor_b32 s50, s46, 0x18000
v_cvt_pk_bf16_f32 v233, v146, v147
v_mfma_f32_16x16x32_bf16 v[140:143], a[12:15], v[84:87], v[140:143]
v_exp_f32 v179, v179
v_mfma_f32_16x16x32_bf16 v[164:167], a[12:15], v[100:103], v[164:167]
v_exp_f32 v148, v148
v_mfma_f32_16x16x32_bf16 v[172:175], a[12:15], v[116:119], v[172:175]
ds_read_b128 a[12:15], v62 offset:13312
v_mfma_f32_16x16x32_bf16 v[128:131], a[16:19], v[72:75], v[128:131]
v_cvt_pk_bf16_f32 v249, v178, v179
v_mfma_f32_16x16x32_bf16 v[136:139], a[16:19], v[88:91], v[136:139]
v_exp_f32 v149, v149
v_mfma_f32_16x16x32_bf16 v[160:163], a[16:19], v[104:107], v[160:163]
v_exp_f32 v180, v180
v_mfma_f32_16x16x32_bf16 v[168:171], a[16:19], v[120:123], v[168:171]
ds_read_b128 a[16:19], v62 offset:10240
v_cvt_pk_bf16_f32 v234, v148, v149
v_mfma_f32_16x16x32_bf16 v[132:135], a[20:23], v[72:75], v[132:135]
buffer_load_dwordx4 v7, s[8:11], s44 offen offset:1024 sc1 lds
v_mfma_f32_16x16x32_bf16 v[140:143], a[20:23], v[88:91], v[140:143]
v_exp_f32 v181, v181
v_mfma_f32_16x16x32_bf16 v[164:167], a[20:23], v[104:107], v[164:167]
v_exp_f32 v150, v150
v_mfma_f32_16x16x32_bf16 v[172:175], a[20:23], v[120:123], v[172:175]
ds_read_b128 a[20:23], v62 offset:14336
v_mfma_f32_16x16x32_bf16 v[128:131], a[24:27], v[76:79], v[128:131]
v_cvt_pk_bf16_f32 v250, v180, v181
v_mfma_f32_16x16x32_bf16 v[136:139], a[24:27], v[92:95], v[136:139]
v_exp_f32 v151, v151
v_mfma_f32_16x16x32_bf16 v[160:163], a[24:27], v[108:111], v[160:163]
v_exp_f32 v182, v182
v_mfma_f32_16x16x32_bf16 v[168:171], a[24:27], v[124:127], v[168:171]
ds_read_b128 a[24:27], v62 offset:11264
v_mfma_f32_16x16x32_bf16 v[132:135], a[28:31], v[76:79], v[132:135]
v_cvt_pk_bf16_f32 v235, v150, v151
v_mfma_f32_16x16x32_bf16 v[140:143], a[28:31], v[92:95], v[140:143]
v_exp_f32 v183, v183
v_mfma_f32_16x16x32_bf16 v[164:167], a[28:31], v[108:111], v[164:167]
v_exp_f32 v152, v152
v_mfma_f32_16x16x32_bf16 v[172:175], a[28:31], v[124:127], v[172:175]
ds_read_b128 a[28:31], v62 offset:15360
v_mfma_f32_16x16x32_bf16 v[208:211], v[52:55], v[224:227], v[208:211]
v_add_u32 v62, s46, v32
v_cvt_pk_bf16_f32 v251, v182, v183
v_mfma_f32_16x16x32_bf16 v[212:215], v[52:55], v[228:231], v[212:215]
v_exp_f32 v153, v153
v_mfma_f32_16x16x32_bf16 v[216:219], v[52:55], v[240:243], v[216:219]
v_exp_f32 v184, v184
v_mfma_f32_16x16x32_bf16 v[220:223], v[52:55], v[244:247], v[220:223]
s_waitcnt lgkmcnt(8)
v_cvt_pk_bf16_f32 v236, v152, v153
v_mfma_f32_16x16x32_bf16 a[64:67], a[32:35], v[224:227], a[64:67]
buffer_load_dwordx4 v7, s[8:11], s44 offen offset:2048 sc1 lds
v_mfma_f32_16x16x32_bf16 a[72:75], a[32:35], v[228:231], a[72:75]
v_exp_f32 v185, v185
v_mfma_f32_16x16x32_bf16 a[176:179], a[32:35], v[240:243], a[176:179]
v_exp_f32 v154, v154
v_mfma_f32_16x16x32_bf16 a[184:187], a[32:35], v[244:247], a[184:187]
ds_read_b128 a[32:35], v63 offset:8192
v_mfma_f32_16x16x32_bf16 a[68:71], a[36:39], v[224:227], a[68:71]
v_cvt_pk_bf16_f32 v252, v184, v185
v_mfma_f32_16x16x32_bf16 a[76:79], a[36:39], v[228:231], a[76:79]
v_exp_f32 v155, v155
v_mfma_f32_16x16x32_bf16 a[180:183], a[36:39], v[240:243], a[180:183]
v_exp_f32 v186, v186
v_mfma_f32_16x16x32_bf16 a[188:191], a[36:39], v[244:247], a[188:191]
ds_read_b128 a[36:39], v63 offset:9216
v_mfma_f32_16x16x32_bf16 a[80:83], a[40:43], v[224:227], a[80:83]
v_cvt_pk_bf16_f32 v237, v154, v155
v_mfma_f32_16x16x32_bf16 a[88:91], a[40:43], v[228:231], a[88:91]
v_exp_f32 v187, v187
v_mfma_f32_16x16x32_bf16 a[208:211], a[40:43], v[240:243], a[208:211]
v_exp_f32 v156, v156
v_mfma_f32_16x16x32_bf16 a[216:219], a[40:43], v[244:247], a[216:219]
ds_read_b128 a[40:43], v63 offset:10240
v_mfma_f32_16x16x32_bf16 a[84:87], a[44:47], v[224:227], a[84:87]
buffer_load_dwordx4 v7, s[8:11], s44 offen offset:3072 sc1 lds
v_mfma_f32_16x16x32_bf16 a[92:95], a[44:47], v[228:231], a[92:95]
v_cvt_pk_bf16_f32 v253, v186, v187
v_mfma_f32_16x16x32_bf16 a[212:215], a[44:47], v[240:243], a[212:215]
v_exp_f32 v157, v157
v_mfma_f32_16x16x32_bf16 a[220:223], a[44:47], v[244:247], a[220:223]
ds_read_b128 a[44:47], v63 offset:11264
v_mfma_f32_16x16x32_bf16 a[112:115], a[48:51], v[224:227], a[112:115]
s_add_u32 s44, s44, 16384
v_exp_f32 v188, v188
v_mfma_f32_16x16x32_bf16 a[120:123], a[48:51], v[228:231], a[120:123]
v_cvt_pk_bf16_f32 v238, v156, v157
v_mfma_f32_16x16x32_bf16 a[240:243], a[48:51], v[240:243], a[240:243]
global_load_dword v35, v[2:3], off
v_mfma_f32_16x16x32_bf16 a[248:251], a[48:51], v[244:247], a[248:251]
ds_read_b128 a[48:51], v63 offset:12288
v_add_co_u32 v2, vcc, s53, v2
v_mfma_f32_16x16x32_bf16 a[116:119], a[52:55], v[224:227], a[116:119]
s_sub_u32 s55, s55, 1
v_exp_f32 v189, v189
v_mfma_f32_16x16x32_bf16 a[124:127], a[52:55], v[228:231], a[124:127]
v_addc_co_u32 v3, vcc, 0, v3, vcc
v_mfma_f32_16x16x32_bf16 a[244:247], a[52:55], v[240:243], a[244:247]
v_exp_f32 v158, v158
v_mfma_f32_16x16x32_bf16 a[252:255], a[52:55], v[244:247], a[252:255]
ds_read_b128 a[52:55], v63 offset:13312
v_cvt_pk_bf16_f32 v254, v188, v189
v_mfma_f32_16x16x32_bf16 a[144:147], a[56:59], v[224:227], a[144:147]
v_exp_f32 v159, v159
v_mfma_f32_16x16x32_bf16 a[152:155], a[56:59], v[228:231], a[152:155]
v_mfma_f32_16x16x32_bf16 a[96:99], a[56:59], v[240:243], a[96:99]
v_exp_f32 v190, v190
v_mfma_f32_16x16x32_bf16 a[104:107], a[56:59], v[244:247], a[104:107]
ds_read_b128 a[56:59], v63 offset:14336
v_cvt_pk_bf16_f32 v239, v158, v159
v_mfma_f32_16x16x32_bf16 a[148:151], a[60:63], v[224:227], a[148:151]
v_exp_f32 v191, v191
v_mfma_f32_16x16x32_bf16 a[156:159], a[60:63], v[228:231], a[156:159]
s_waitcnt lgkmcnt(7)
v_mfma_f32_16x16x32_bf16 a[100:103], a[60:63], v[240:243], a[100:103]
s_waitcnt vmcnt(10)
s_barrier
v_cvt_pk_bf16_f32 v255, v190, v191
v_mfma_f32_16x16x32_bf16 a[108:111], a[60:63], v[244:247], a[108:111]
ds_read_b128 a[60:63], v63 offset:15360
s_cbranch_scc0 .LD1
s_cmp_eq_u32 s43, s42
s_cbranch_scc1 .LT1
s_swappc_b64 s[30:31], s[58:59]
.Lev_d0_r:
s_branch .LD1
.LT0:
v_mfma_f32_16x16x32_bf16 v[128:131], a[0:3], v[64:67], v[192:195]
s_cmp_lt_u32 s44, s45
v_exp_f32 v144, v144
v_mfma_f32_16x16x32_bf16 v[136:139], a[0:3], v[80:83], v[196:199]
s_cselect_b32 m0, s46, s27
v_exp_f32 v145, v145
v_cmp_gt_i32 s[32:33], 0, v8
v_mfma_f32_16x16x32_bf16 v[160:163], a[0:3], v[96:99], v[36:39]
s_add_u32 s43, s42, 4
v_exp_f32 v176, v176
v_mfma_f32_16x16x32_bf16 v[168:171], a[0:3], v[112:115], v[44:47]
ds_read_b128 a[0:3], v62 offset:8192
v_nop
v_mfma_f32_16x16x32_bf16 v[132:135], a[4:7], v[64:67], v[192:195]
s_mov_b32 s42, s56
v_cvt_pk_bf16_f32 v232, v144, v145
v_cmp_gt_i32 s[34:35], 1, v8
v_mfma_f32_16x16x32_bf16 v[140:143], a[4:7], v[80:83], v[196:199]
s_max_u32 s55, s57, s43
v_exp_f32 v177, v177
v_mfma_f32_16x16x32_bf16 v[164:167], a[4:7], v[96:99], v[40:43]
s_min_u32 s55, s55, s42
v_exp_f32 v146, v146
v_mfma_f32_16x16x32_bf16 v[172:175], a[4:7], v[112:115], v[48:51]
ds_read_b128 a[4:7], v62 offset:12288
v_cvt_pk_bf16_f32 v248, v176, v177
v_cndmask_b32 v16, v192, v34, s[32:33]
v_mfma_f32_16x16x32_bf16 v[128:131], a[8:11], v[68:71], v[128:131]
buffer_load_dwordx4 v7, s[8:11], s44 offen offset:0 sc1 lds
v_mfma_f32_16x16x32_bf16 v[136:139], a[8:11], v[84:87], v[136:139]
s_sub_u32 s93, s55, s43
v_exp_f32 v147, v147
v_cmp_gt_i32 s[32:33], 2, v8
v_mfma_f32_16x16x32_bf16 v[160:163], a[8:11], v[100:103], v[160:163]
s_mov_b32 s43, s55
v_exp_f32 v178, v178
v_mfma_f32_16x16x32_bf16 v[168:171], a[8:11], v[116:119], v[168:171]
ds_read_b128 a[8:11], v62 offset:9216
v_cndmask_b32 v17, v192, v34, s[34:35]
v_mfma_f32_16x16x32_bf16 v[132:135], a[12:15], v[68:71], v[132:135]
s_xor_b32 s50, s46, 0x18000
v_cvt_pk_bf16_f32 v233, v146, v147
v_cmp_gt_i32 s[34:35], 3, v8
v_mfma_f32_16x16x32_bf16 v[140:143], a[12:15], v[84:87], v[140:143]
s_mov_b32 s55, s93
v_exp_f32 v179, v179
v_mfma_f32_16x16x32_bf16 v[164:167], a[12:15], v[100:103], v[164:167]
v_exp_f32 v148, v148
v_mfma_f32_16x16x32_bf16 v[172:175], a[12:15], v[116:119], v[172:175]
ds_read_b128 a[12:15], v62 offset:13312
v_cndmask_b32 v18, v192, v34, s[32:33]
v_mfma_f32_16x16x32_bf16 v[128:131], a[16:19], v[72:75], v[128:131]
v_cvt_pk_bf16_f32 v249, v178, v179
v_cmp_gt_i32 s[32:33], 16, v8
v_mfma_f32_16x16x32_bf16 v[136:139], a[16:19], v[88:91], v[136:139]
v_exp_f32 v149, v149
v_mfma_f32_16x16x32_bf16 v[160:163], a[16:19], v[104:107], v[160:163]
v_exp_f32 v180, v180
v_mfma_f32_16x16x32_bf16 v[168:171], a[16:19], v[120:123], v[168:171]
ds_read_b128 a[16:19], v62 offset:10240
v_cndmask_b32 v19, v192, v34, s[34:35]
v_mfma_f32_16x16x32_bf16 v[132:135], a[20:23], v[72:75], v[132:135]
buffer_load_dwordx4 v7, s[8:11], s44 offen offset:1024 sc1 lds
v_mfma_f32_16x16x32_bf16 v[140:143], a[20:23], v[88:91], v[140:143]
v_cvt_pk_bf16_f32 v234, v148, v149
v_cmp_gt_i32 s[34:35], 17, v8
v_mfma_f32_16x16x32_bf16 v[164:167], a[20:23], v[104:107], v[164:167]
v_exp_f32 v181, v181
v_mfma_f32_16x16x32_bf16 v[172:175], a[20:23], v[120:123], v[172:175]
ds_read_b128 a[20:23], v62 offset:14336
v_cndmask_b32 v20, v192, v34, s[32:33]
v_mfma_f32_16x16x32_bf16 v[128:131], a[24:27], v[76:79], v[128:131]
v_exp_f32 v150, v150
v_mfma_f32_16x16x32_bf16 v[136:139], a[24:27], v[92:95], v[136:139]
v_cvt_pk_bf16_f32 v250, v180, v181
v_cmp_gt_i32 s[32:33], 18, v8
v_mfma_f32_16x16x32_bf16 v[160:163], a[24:27], v[108:111], v[160:163]
v_exp_f32 v151, v151
v_mfma_f32_16x16x32_bf16 v[168:171], a[24:27], v[124:127], v[168:171]
ds_read_b128 a[24:27], v62 offset:11264
v_cndmask_b32 v21, v192, v34, s[34:35]
v_mfma_f32_16x16x32_bf16 v[132:135], a[28:31], v[76:79], v[132:135]
v_exp_f32 v182, v182
v_mfma_f32_16x16x32_bf16 v[140:143], a[28:31], v[92:95], v[140:143]
v_cvt_pk_bf16_f32 v235, v150, v151
v_cmp_gt_i32 s[34:35], 19, v8
v_mfma_f32_16x16x32_bf16 v[164:167], a[28:31], v[108:111], v[164:167]
v_exp_f32 v183, v183
v_mfma_f32_16x16x32_bf16 v[172:175], a[28:31], v[124:127], v[172:175]
ds_read_b128 a[28:31], v62 offset:15360
v_cndmask_b32 v22, v192, v34, s[32:33]
v_mfma_f32_16x16x32_bf16 v[208:211], v[52:55], v[224:227], v[208:211]
v_add_u32 v62, s46, v32
v_cmp_gt_i32 s[32:33], -16, v8
v_mfma_f32_16x16x32_bf16 v[212:215], v[52:55], v[228:231], v[212:215]
v_exp_f32 v152, v152
v_mfma_f32_16x16x32_bf16 v[216:219], v[52:55], v[240:243], v[216:219]
v_cvt_pk_bf16_f32 v251, v182, v183
v_cndmask_b32 v23, v192, v34, s[34:35]
v_mfma_f32_16x16x32_bf16 v[220:223], v[52:55], v[244:247], v[220:223]
s_waitcnt lgkmcnt(8)
v_exp_f32 v153, v153
v_mfma_f32_16x16x32_bf16 a[64:67], a[32:35], v[224:227], a[64:67]
buffer_load_dwordx4 v7, s[8:11], s44 offen offset:2048 sc1 lds
v_cmp_gt_i32 s[34:35], -15, v8
v_mfma_f32_16x16x32_bf16 a[72:75], a[32:35], v[228:231], a[72:75]
v_exp_f32 v184, v184
v_mfma_f32_16x16x32_bf16 a[176:179], a[32:35], v[240:243], a[176:179]
v_cvt_pk_bf16_f32 v236, v152, v153
v_cndmask_b32 v24, v196, v34, s[32:33]
v_mfma_f32_16x16x32_bf16 a[184:187], a[32:35], v[244:247], a[184:187]
ds_read_b128 a[32:35], v63 offset:8192
v_cmp_gt_i32 s[32:33], -14, v8
v_mfma_f32_16x16x32_bf16 a[68:71], a[36:39], v[224:227], a[68:71]
v_exp_f32 v185, v185
v_mfma_f32_16x16x32_bf16 a[76:79], a[36:39], v[228:231], a[76:79]
v_exp_f32 v154, v154
v_mfma_f32_16x16x32_bf16 a[180:183], a[36:39], v[240:243], a[180:183]
v_cvt_pk_bf16_f32 v252, v184, v185
v_cndmask_b32 v25, v196, v34, s[34:35]
v_mfma_f32_16x16x32_bf16 a[188:191], a[36:39], v[244:247], a[188:191]
ds_read_b128 a[36:39], v63 offset:9216
v_cmp_gt_i32 s[34:35], -13, v8
v_mfma_f32_16x16x32_bf16 a[80:83], a[40:43], v[224:227], a[80:83]
v_exp_f32 v155, v155
v_mfma_f32_16x16x32_bf16 a[88:91], a[40:43], v[228:231], a[88:91]
v_exp_f32 v186, v186
v_mfma_f32_16x16x32_bf16 a[208:211], a[40:43], v[240:243], a[208:211]
v_cvt_pk_bf16_f32 v237, v154, v155
v_cndmask_b32 v26, v196, v34, s[32:33]
v_mfma_f32_16x16x32_bf16 a[216:219], a[40:43], v[244:247], a[216:219]
ds_read_b128 a[40:43], v63 offset:10240
v_cmp_gt_i32 s[32:33], 0, v8
v_mfma_f32_16x16x32_bf16 a[84:87], a[44:47], v[224:227], a[84:87]
buffer_load_dwordx4 v7, s[8:11], s44 offen offset:3072 sc1 lds
v_mfma_f32_16x16x32_bf16 a[92:95], a[44:47], v[228:231], a[92:95]
v_exp_f32 v187, v187
v_mfma_f32_16x16x32_bf16 a[212:215], a[44:47], v[240:243], a[212:215]
v_exp_f32 v156, v156
v_cndmask_b32 v27, v196, v34, s[34:35]
v_mfma_f32_16x16x32_bf16 a[220:223], a[44:47], v[244:247], a[220:223]
ds_read_b128 a[44:47], v63 offset:11264
v_cvt_pk_bf16_f32 v253, v186, v187
v_mfma_f32_16x16x32_bf16 a[112:115], a[48:51], v[224:227], a[112:115]
s_add_u32 s44, s44, 16384
v_exp_f32 v157, v157
v_mfma_f32_16x16x32_bf16 a[120:123], a[48:51], v[228:231], a[120:123]
v_exp_f32 v188, v188
v_mfma_f32_16x16x32_bf16 a[240:243], a[48:51], v[240:243], a[240:243]
v_cmp_gt_i32 s[34:35], 1, v8
v_cndmask_b32 v28, v196, v34, s[32:33]
v_mfma_f32_16x16x32_bf16 a[248:251], a[48:51], v[244:247], a[248:251]
ds_read_b128 a[48:51], v63 offset:12288
v_cvt_pk_bf16_f32 v238, v156, v157
v_mfma_f32_16x16x32_bf16 a[116:119], a[52:55], v[224:227], a[116:119]
v_exp_f32 v189, v189
v_mfma_f32_16x16x32_bf16 a[124:127], a[52:55], v[228:231], a[124:127]
v_exp_f32 v158, v158
v_mfma_f32_16x16x32_bf16 a[244:247], a[52:55], v[240:243], a[244:247]
v_cmp_gt_i32 s[32:33], 2, v8
v_cndmask_b32 v29, v196, v34, s[34:35]
v_mfma_f32_16x16x32_bf16 a[252:255], a[52:55], v[244:247], a[252:255]
ds_read_b128 a[52:55], v63 offset:13312
v_cvt_pk_bf16_f32 v254, v188, v189
v_mfma_f32_16x16x32_bf16 a[144:147], a[56:59], v[224:227], a[144:147]
v_exp_f32 v159, v159
v_mfma_f32_16x16x32_bf16 a[152:155], a[56:59], v[228:231], a[152:155]
v_cmp_gt_i32 s[34:35], 3, v8
v_cndmask_b32 v30, v196, v34, s[32:33]
v_mfma_f32_16x16x32_bf16 a[96:99], a[56:59], v[240:243], a[96:99]
v_exp_f32 v190, v190
v_mfma_f32_16x16x32_bf16 a[104:107], a[56:59], v[244:247], a[104:107]
ds_read_b128 a[56:59], v63 offset:14336
v_cvt_pk_bf16_f32 v239, v158, v159
v_mfma_f32_16x16x32_bf16 a[148:151], a[60:63], v[224:227], a[148:151]
v_exp_f32 v191, v191
v_mfma_f32_16x16x32_bf16 a[156:159], a[60:63], v[228:231], a[156:159]
s_waitcnt lgkmcnt(7)
v_nop
v_cndmask_b32 v31, v196, v34, s[34:35]
v_mfma_f32_16x16x32_bf16 a[100:103], a[60:63], v[240:243], a[100:103]
s_waitcnt vmcnt(9)
s_barrier
v_cvt_pk_bf16_f32 v255, v190, v191
v_mfma_f32_16x16x32_bf16 a[108:111], a[60:63], v[244:247], a[108:111]
ds_read_b128 a[60:63], v63 offset:15360
v_mfma_f32_16x16x32_bf16 v[144:147], a[0:3], v[64:67], v[192:195]
s_cmp_lt_u32 s44, s54
v_exp_f32 v128, v128
v_mfma_f32_16x16x32_bf16 v[152:155], a[0:3], v[80:83], v[196:199]
s_cselect_b32 m0, s50, s27
v_exp_f32 v129, v129
v_mfma_f32_16x16x32_bf16 v[148:151], a[4:7], v[64:67], v[192:195]
s_and_b32 s36, s86, 1
s_lshl_b32 s36, s36, 3
v_add_u32 v63, s49, v33
v_exp_f32 v160, v160
v_mfma_f32_16x16x32_bf16 v[156:159], a[4:7], v[80:83], v[196:199]
ds_read_b128 a[0:3], v62 offset:0
.Lpad11:
s_nop 0
ds_read_b128 a[4:7], v62 offset:4096
v_cvt_pk_bf16_f32 v224, v128, v129
v_mfma_f32_16x16x32_bf16 v[144:147], a[8:11], v[68:71], v[144:147]
s_and_b32 s28, s87, 7
s_or_b32 s36, s36, s28
v_exp_f32 v161, v161
v_mfma_f32_16x16x32_bf16 v[152:155], a[8:11], v[84:87], v[152:155]
ds_read_b128 a[8:11], v62 offset:1024
v_exp_f32 v130, v130
v_mfma_f32_16x16x32_bf16 v[148:151], a[12:15], v[68:71], v[148:151]
buffer_load_dwordx4 v7, s[4:7], s44 offen offset:0 sc1 lds
v_mfma_f32_16x16x32_bf16 v[156:159], a[12:15], v[84:87], v[156:159]
ds_read_b128 a[12:15], v62 offset:5120
v_cvt_pk_bf16_f32 v240, v160, v161
v_mfma_f32_16x16x32_bf16 v[144:147], a[16:19], v[72:75], v[144:147]
s_lshr_b32 s37, s86, 1
s_lshl_b32 s37, s37, 2
v_exp_f32 v131, v131
v_mfma_f32_16x16x32_bf16 v[152:155], a[16:19], v[88:91], v[152:155]
s_mov_b32 s49, s46
v_exp_f32 v162, v162
v_mfma_f32_16x16x32_bf16 v[148:151], a[20:23], v[72:75], v[148:151]
s_lshr_b32 s28, s87, 3
s_or_b32 s37, s37, s28
v_cvt_pk_bf16_f32 v225, v130, v131
v_mfma_f32_16x16x32_bf16 v[156:159], a[20:23], v[88:91], v[156:159]
ds_read_b128 a[16:19], v62 offset:2048
ds_read_b128 a[20:23], v62 offset:6144
v_exp_f32 v163, v163
v_mfma_f32_16x16x32_bf16 v[144:147], a[24:27], v[76:79], v[144:147]
s_lshl_b32 s24, s36, 18
s_lshl_b32 s28, s3, 16
v_exp_f32 v132, v132
v_mfma_f32_16x16x32_bf16 v[152:155], a[24:27], v[92:95], v[152:155]
ds_read_b128 a[24:27], v62 offset:3072
v_cvt_pk_bf16_f32 v241, v162, v163
v_mfma_f32_16x16x32_bf16 v[148:151], a[28:31], v[76:79], v[148:151]
s_add_u32 s46, s46, 16384
v_exp_f32 v133, v133
v_mfma_f32_16x16x32_bf16 v[156:159], a[28:31], v[92:95], v[156:159]
ds_read_b128 a[28:31], v62 offset:7168
v_exp_f32 v164, v164
v_mfma_f32_16x16x32_bf16 v[208:211], v[52:55], v[232:235], v[208:211]
buffer_load_dwordx4 v7, s[4:7], s44 offen offset:1024 sc1 lds
v_mfma_f32_16x16x32_bf16 v[212:215], v[52:55], v[236:239], v[212:215]
s_add_u32 s24, s24, s28
s_mov_b32 s25, 0
v_cvt_pk_bf16_f32 v226, v132, v133
v_mfma_f32_16x16x32_bf16 v[216:219], v[52:55], v[248:251], v[216:219]
s_mul_i32 s26, s3, 98304
s_lshl_b32 s28, s37, 7
v_exp_f32 v165, v165
v_mfma_f32_16x16x32_bf16 v[220:223], v[52:55], v[252:255], v[220:223]
s_waitcnt lgkmcnt(8)
s_and_b32 s46, s46, 0xffff
v_exp_f32 v134, v134
v_mfma_f32_16x16x32_bf16 a[64:67], a[32:35], v[232:235], a[64:67]
s_add_u32 s26, s26, s28
s_add_u32 s47, s26, 4096
v_cvt_pk_bf16_f32 v242, v164, v165
v_mfma_f32_16x16x32_bf16 a[72:75], a[32:35], v[236:239], a[72:75]
s_mul_i32 s48, s3, 7168
s_mov_b32 s12, s64
v_exp_f32 v135, v135
v_mfma_f32_16x16x32_bf16 a[176:179], a[32:35], v[248:251], a[176:179]
s_and_b32 s13, s65, 0xffff
s_mov_b32 s14, 0x400000
v_exp_f32 v166, v166
v_mfma_f32_16x16x32_bf16 a[184:187], a[32:35], v[252:255], a[184:187]
ds_read_b128 a[32:35], v63 offset:0
v_cvt_pk_bf16_f32 v227, v134, v135
v_mfma_f32_16x16x32_bf16 a[68:71], a[36:39], v[232:235], a[68:71]
s_mov_b32 s16, s66
s_and_b32 s17, s67, 0xffff
v_exp_f32 v167, v167
v_mfma_f32_16x16x32_bf16 a[76:79], a[36:39], v[236:239], a[76:79]
s_mov_b32 s18, 0x600000
s_lshl_b32 s29, s36, 18
v_exp_f32 v136, v136
v_mfma_f32_16x16x32_bf16 a[180:183], a[36:39], v[248:251], a[180:183]
s_lshl_b32 s28, s3, 13
s_add_u32 s29, s29, s28
v_cvt_pk_bf16_f32 v243, v166, v167
v_mfma_f32_16x16x32_bf16 a[188:191], a[36:39], v[252:255], a[188:191]
ds_read_b128 a[36:39], v63 offset:1024
v_exp_f32 v137, v137
v_mfma_f32_16x16x32_bf16 a[80:83], a[40:43], v[232:235], a[80:83]
buffer_load_dwordx4 v7, s[4:7], s44 offen offset:2048 sc1 lds
v_mfma_f32_16x16x32_bf16 a[88:91], a[40:43], v[236:239], a[88:91]
s_sub_u32 s29, s29, 34816
v_exp_f32 v168, v168
v_mfma_f32_16x16x32_bf16 a[208:211], a[40:43], v[248:251], a[208:211]
s_cmp_eq_u32 s36, 0
s_cselect_b32 s29, 0x7f000000, s29
v_cvt_pk_bf16_f32 v228, v136, v137
v_mfma_f32_16x16x32_bf16 a[216:219], a[40:43], v[252:255], a[216:219]
ds_read_b128 a[40:43], v63 offset:2048
v_exp_f32 v169, v169
v_mfma_f32_16x16x32_bf16 a[84:87], a[44:47], v[232:235], a[84:87]
s_lshl_b32 s32, s3, 2
s_and_b32 s33, s3, 1
v_exp_f32 v138, v138
v_mfma_f32_16x16x32_bf16 a[92:95], a[44:47], v[236:239], a[92:95]
s_lshl_b32 s33, s33, 1
s_lshr_b32 s34, s3, 1
v_cvt_pk_bf16_f32 v244, v168, v169
v_mfma_f32_16x16x32_bf16 a[212:215], a[44:47], v[248:251], a[212:215]
s_lshl_b32 s35, s34, 13
s_add_u32 s35, s35, 65536
v_exp_f32 v139, v139
v_mfma_f32_16x16x32_bf16 a[220:223], a[44:47], v[252:255], a[220:223]
ds_read_b128 a[44:47], v63 offset:3072
v_exp_f32 v170, v170
v_mfma_f32_16x16x32_bf16 a[112:115], a[48:51], v[232:235], a[112:115]
buffer_load_dwordx4 v7, s[4:7], s44 offen offset:3072 sc1 lds
v_mfma_f32_16x16x32_bf16 a[120:123], a[48:51], v[236:239], a[120:123]
s_mov_b32 s38, 131072
s_lshl_b32 s28, s88, 13
v_cvt_pk_bf16_f32 v229, v138, v139
v_mfma_f32_16x16x32_bf16 a[240:243], a[48:51], v[248:251], a[240:243]
s_add_u32 s28, s28, 33554432
v_exp_f32 v171, v171
v_mfma_f32_16x16x32_bf16 a[248:251], a[48:51], v[252:255], a[248:251]
ds_read_b128 a[48:51], v63 offset:4096
v_exp_f32 v140, v140
v_mfma_f32_16x16x32_bf16 a[116:119], a[52:55], v[232:235], a[116:119]
s_add_u32 s20, s84, s28
s_addc_u32 s21, s85, 0
v_cvt_pk_bf16_f32 v245, v170, v171
v_mfma_f32_16x16x32_bf16 a[124:127], a[52:55], v[236:239], a[124:127]
s_and_b32 s21, s21, 0xffff
s_mov_b32 s22, 16777216
v_exp_f32 v141, v141
v_mfma_f32_16x16x32_bf16 a[244:247], a[52:55], v[248:251], a[244:247]
s_sub_u32 s40, 15, s89
s_lshl_b32 s40, s40, 2
v_exp_f32 v172, v172
v_mfma_f32_16x16x32_bf16 a[252:255], a[52:55], v[252:255], a[252:255]
ds_read_b128 a[52:55], v63 offset:5120
v_cvt_pk_bf16_f32 v230, v140, v141
v_mfma_f32_16x16x32_bf16 a[144:147], a[56:59], v[232:235], a[144:147]
s_add_u32 s40, s40, s3
s_lshl_b32 s40, s40, 18
v_exp_f32 v173, v173
v_mfma_f32_16x16x32_bf16 a[152:155], a[56:59], v[236:239], a[152:155]
s_lshl_b32 s41, s89, 2
s_add_u32 s41, s41, s94
v_exp_f32 v142, v142
v_mfma_f32_16x16x32_bf16 a[96:99], a[56:59], v[248:251], a[96:99]
s_lshl_b32 s41, s41, 18
s_mov_b32 s28, 1602224128
v_cvt_pk_bf16_f32 v246, v172, v173
v_mfma_f32_16x16x32_bf16 a[104:107], a[56:59], v[252:255], a[104:107]
ds_read_b128 a[56:59], v63 offset:6144
v_exp_f32 v143, v143
v_mfma_f32_16x16x32_bf16 a[148:151], a[60:63], v[232:235], a[148:151]
v_exp_f32 v174, v174
v_cvt_pk_bf16_f32 v231, v142, v143
v_mfma_f32_16x16x32_bf16 a[156:159], a[60:63], v[236:239], a[156:159]
s_waitcnt lgkmcnt(7)
v_exp_f32 v175, v175
v_mfma_f32_16x16x32_bf16 a[100:103], a[60:63], v[248:251], a[100:103]
v_cvt_pk_bf16_f32 v247, v174, v175
v_mfma_f32_16x16x32_bf16 a[108:111], a[60:63], v[252:255], a[108:111]
ds_read_b128 a[60:63], v63 offset:7168
v_mfma_f32_16x16x32_bf16 v[128:131], a[0:3], v[64:67], v[192:195]
s_cmp_lt_u32 s44, s45
v_lshrrev_b32 v56, 3, v1
v_and_b32 v57, 7, v1
v_mfma_f32_16x16x32_bf16 v[136:139], a[0:3], v[80:83], v[196:199]
s_cselect_b32 m0, s46, s27
v_exp_f32 v144, v144
v_xor_b32 v58, v57, v56
v_mfma_f32_16x16x32_bf16 v[132:135], a[4:7], v[64:67], v[192:195]
v_exp_f32 v145, v145
v_lshlrev_b32 v58, 4, v58
v_mfma_f32_16x16x32_bf16 v[140:143], a[4:7], v[80:83], v[196:199]
ds_read_b128 a[0:3], v62 offset:8192
ds_read_b128 a[4:7], v62 offset:12288
v_lshl_add_u32 v36, v56, 11, v58
v_mfma_f32_16x16x32_bf16 v[128:131], a[8:11], v[68:71], v[128:131]
v_exp_f32 v146, v146
v_add_u32 v37, 15360, v36
v_mfma_f32_16x16x32_bf16 v[136:139], a[8:11], v[84:87], v[136:139]
ds_read_b128 a[8:11], v62 offset:9216
v_add_u32 v38, 30720, v36
v_mfma_f32_16x16x32_bf16 v[132:135], a[12:15], v[68:71], v[132:135]
buffer_load_dwordx4 v7, s[8:11], s44 offen offset:0 sc1 lds
v_add_u32 v39, 46080, v36
v_mfma_f32_16x16x32_bf16 v[140:143], a[12:15], v[84:87], v[140:143]
ds_read_b128 a[12:15], v62 offset:13312
v_cvt_pk_bf16_f32 v232, v144, v145
v_add_u32 v58, s32, v56
v_mfma_f32_16x16x32_bf16 v[128:131], a[16:19], v[72:75], v[128:131]
v_xor_b32 v58, v58, v57
v_and_b32 v58, 7, v58
v_mfma_f32_16x16x32_bf16 v[136:139], a[16:19], v[88:91], v[136:139]
s_xor_b32 s50, s46, 0x18000
v_exp_f32 v147, v147
v_mfma_f32_16x16x32_bf16 v[132:135], a[20:23], v[72:75], v[132:135]
v_lshlrev_b32 v58, 4, v58
v_lshl_add_u32 v58, v56, 11, v58
v_mfma_f32_16x16x32_bf16 v[140:143], a[20:23], v[88:91], v[140:143]
ds_read_b128 a[16:19], v62 offset:10240
ds_read_b128 a[20:23], v62 offset:14336
v_add_u32 v58, s29, v58
v_mfma_f32_16x16x32_bf16 v[128:131], a[24:27], v[76:79], v[128:131]
v_exp_f32 v148, v148
v_and_b32 v59, 32, v1
v_mfma_f32_16x16x32_bf16 v[136:139], a[24:27], v[92:95], v[136:139]
ds_read_b128 a[24:27], v62 offset:11264
v_cvt_pk_bf16_f32 v233, v146, v147
v_lshlrev_b32 v59, 25, v59
v_mfma_f32_16x16x32_bf16 v[132:135], a[28:31], v[76:79], v[132:135]
v_exp_f32 v149, v149
v_or_b32 v42, v59, v58
v_mfma_f32_16x16x32_bf16 v[140:143], a[28:31], v[92:95], v[140:143]
ds_read_b128 a[28:31], v62 offset:15360
v_bfe_u32 v58, v1, 4, 1
v_lshlrev_b32 v58, 1, v58
v_mfma_f32_16x16x32_bf16 v[208:211], v[52:55], v[224:227], v[208:211]
buffer_load_dwordx4 v7, s[8:11], s44 offen offset:1024 sc1 lds
v_add_u32 v62, s46, v32
v_mfma_f32_16x16x32_bf16 v[212:215], v[52:55], v[228:231], v[212:215]
v_xor_b32 v58, v58, v57
v_lshlrev_b32 v58, 4, v58
v_mfma_f32_16x16x32_bf16 v[216:219], v[52:55], v[240:243], v[216:219]
v_exp_f32 v150, v150
v_mul_u32_u24 v59, 6144, v56
v_mfma_f32_16x16x32_bf16 v[220:223], v[52:55], v[244:247], v[220:223]
s_waitcnt lgkmcnt(8)
v_add_u32 v40, v59, v58
v_xor_b32 v41, 64, v40
v_mfma_f32_16x16x32_bf16 a[64:67], a[32:35], v[224:227], a[64:67]
v_cvt_pk_bf16_f32 v234, v148, v149
v_add_u32 v41, 48128, v41
v_mfma_f32_16x16x32_bf16 a[72:75], a[32:35], v[228:231], a[72:75]
v_and_b32 v56, 15, v1
v_lshrrev_b32 v57, 4, v1
v_mfma_f32_16x16x32_bf16 a[176:179], a[32:35], v[240:243], a[176:179]
v_exp_f32 v151, v151
v_mfma_f32_16x16x32_bf16 a[184:187], a[32:35], v[244:247], a[184:187]
ds_read_b128 a[32:35], v63 offset:8192
v_and_b32 v58, 7, v56
v_mfma_f32_16x16x32_bf16 a[68:71], a[36:39], v[224:227], a[68:71]
v_exp_f32 v152, v152
v_xor_b32 v58, v58, v57
v_mfma_f32_16x16x32_bf16 a[76:79], a[36:39], v[228:231], a[76:79]
v_lshlrev_b32 v58, 4, v58
v_lshl_add_u32 v43, v56, 7, v58
v_mfma_f32_16x16x32_bf16 a[180:183], a[36:39], v[240:243], a[180:183]
v_cvt_pk_bf16_f32 v235, v150, v151
v_add_u32 v43, s35, v43
v_mfma_f32_16x16x32_bf16 a[188:191], a[36:39], v[244:247], a[188:191]
ds_read_b128 a[36:39], v63 offset:9216
v_xor_b32 v44, 64, v43
v_mfma_f32_16x16x32_bf16 a[80:83], a[40:43], v[224:227], a[80:83]
buffer_load_dwordx4 v7, s[8:11], s44 offen offset:2048 sc1 lds
v_and_b32 v56, 15, v1
v_mfma_f32_16x16x32_bf16 a[88:91], a[40:43], v[228:231], a[88:91]
v_exp_f32 v153, v153
v_lshrrev_b32 v57, 2, v56
v_mfma_f32_16x16x32_bf16 a[208:211], a[40:43], v[240:243], a[208:211]
v_exp_f32 v154, v154
v_mul_u32_u24 v57, 6656, v57
v_add_f32 v13, v216, v220
v_mfma_f32_16x16x32_bf16 a[216:219], a[40:43], v[244:247], a[216:219]
ds_read_b128 a[40:43], v63 offset:10240
v_add_u32 v57, 71680, v57
v_mov_b32 v58, -2048
v_mfma_f32_16x16x32_bf16 a[84:87], a[44:47], v[224:227], a[84:87]
v_cvt_pk_bf16_f32 v236, v152, v153
v_mov_b32 v59, s34
v_mfma_f32_16x16x32_bf16 a[92:95], a[44:47], v[228:231], a[92:95]
v_cmp_eq_u32 vcc, 0, v59
v_nop
v_mfma_f32_16x16x32_bf16 a[212:215], a[44:47], v[240:243], a[212:215]
v_exp_f32 v155, v155
v_mfma_f32_16x16x32_bf16 a[220:223], a[44:47], v[244:247], a[220:223]
ds_read_b128 a[44:47], v63 offset:11264
v_nop
v_mfma_f32_16x16x32_bf16 a[112:115], a[48:51], v[224:227], a[112:115]
buffer_load_dwordx4 v7, s[8:11], s44 offen offset:3072 sc1 lds
v_cndmask_b32 v57, v58, v57, vcc
v_mfma_f32_16x16x32_bf16 a[120:123], a[48:51], v[228:231], a[120:123]
v_exp_f32 v156, v156
v_add_u32 v45, v57, v43
v_mfma_f32_16x16x32_bf16 a[240:243], a[48:51], v[240:243], a[240:243]
v_add_u32 v46, v57, v44
v_bfe_u32 v56, v1, 2, 2
v_mfma_f32_16x16x32_bf16 a[248:251], a[48:51], v[244:247], a[248:251]
ds_read_b128 a[48:51], v63 offset:12288
v_cvt_pk_bf16_f32 v237, v154, v155
v_mfma_f32_16x16x32_bf16 a[116:119], a[52:55], v[224:227], a[116:119]
s_add_u32 s44, s44, 16384
v_bfe_u32 v57, v1, 4, 1
v_lshl_add_u32 v58, v57, 3, v56
v_mfma_f32_16x16x32_bf16 a[124:127], a[52:55], v[228:231], a[124:127]
v_exp_f32 v157, v157
v_lshlrev_b32 v58, 7, v58
v_mfma_f32_16x16x32_bf16 a[244:247], a[52:55], v[240:243], a[244:247]
v_lshrrev_b32 v59, 5, v1
v_mul_u32_u24 v59, 7168, v59
v_mfma_f32_16x16x32_bf16 a[252:255], a[52:55], v[244:247], a[252:255]
ds_read_b128 a[52:55], v63 offset:13312
v_add3_u32 v58, v58, v59, s38
v_cmp_nge_f32 s[52:53], s28, v13
v_mfma_f32_16x16x32_bf16 a[144:147], a[56:59], v[224:227], a[144:147]
v_exp_f32 v158, v158
v_lshrrev_b32 v56, 1, v56
v_mfma_f32_16x16x32_bf16 a[152:155], a[56:59], v[228:231], a[152:155]
v_cvt_pk_bf16_f32 v238, v156, v157
v_lshl_or_b32 v56, v57, 1, v56
v_mfma_f32_16x16x32_bf16 a[96:99], a[56:59], v[240:243], a[96:99]
v_xor_b32 v56, s33, v56
v_lshl_add_u32 v58, v56, 5, v58
v_mfma_f32_16x16x32_bf16 a[104:107], a[56:59], v[244:247], a[104:107]
ds_read_b128 a[56:59], v63 offset:14336
v_and_b32 v56, 3, v1
v_mfma_f32_16x16x32_bf16 a[148:151], a[60:63], v[224:227], a[148:151]
v_exp_f32 v159, v159
v_lshl_add_u32 v47, v56, 3, v58
v_mfma_f32_16x16x32_bf16 a[156:159], a[60:63], v[228:231], a[156:159]
s_waitcnt lgkmcnt(7)
v_cvt_pk_bf16_f32 v239, v158, v159
v_xor_b32 v48, 32, v47
v_mfma_f32_16x16x32_bf16 a[100:103], a[60:63], v[240:243], a[100:103]
s_waitcnt vmcnt(8)
s_barrier
v_add_u32 v51, 4096, v7
v_mfma_f32_16x16x32_bf16 a[108:111], a[60:63], v[244:247], a[108:111]
ds_read_b128 a[60:63], v63 offset:15360
s_cmp_le_u32 s94, 3
s_cbranch_scc1 .LTf0
.LTc0:
s_cmp_lt_i32 s55, 0
s_cbranch_scc1 .LE1
s_branch .LS1
.LTf0:
s_cmp_lg_u64 s[52:53], 0
s_cbranch_scc1 .LTc0
s_getpc_b64 s[30:31]
.LTp0:
s_sub_u32 s30, s30, .LTp0-.LTc0
s_subb_u32 s31, s31, 0
s_branch .Lfb
.LT1:
v_mfma_f32_16x16x32_bf16 v[144:147], a[0:3], v[64:67], v[192:195]
s_cmp_lt_u32 s44, s54
v_exp_f32 v128, v128
v_mfma_f32_16x16x32_bf16 v[152:155], a[0:3], v[80:83], v[196:199]
s_cselect_b32 m0, s50, s27
v_exp_f32 v129, v129
v_mfma_f32_16x16x32_bf16 v[176:179], a[0:3], v[96:99], v[36:39]
s_add_u32 s43, s42, 4
v_add_u32 v63, s49, v33
v_cmp_gt_i32 s[32:33], 0, v8
v_mfma_f32_16x16x32_bf16 v[184:187], a[0:3], v[112:115], v[44:47]
ds_read_b128 a[0:3], v62 offset:0
v_nop
v_mfma_f32_16x16x32_bf16 v[148:151], a[4:7], v[64:67], v[192:195]
s_mov_b32 s42, s56
v_exp_f32 v160, v160
v_mfma_f32_16x16x32_bf16 v[156:159], a[4:7], v[80:83], v[196:199]
s_max_u32 s55, s57, s43
v_cvt_pk_bf16_f32 v224, v128, v129
v_cmp_gt_i32 s[34:35], 1, v8
v_mfma_f32_16x16x32_bf16 v[180:183], a[4:7], v[96:99], v[40:43]
s_min_u32 s55, s55, s42
v_exp_f32 v161, v161
v_mfma_f32_16x16x32_bf16 v[188:191], a[4:7], v[112:115], v[48:51]
ds_read_b128 a[4:7], v62 offset:4096
v_cndmask_b32 v16, v192, v34, s[32:33]
v_mfma_f32_16x16x32_bf16 v[144:147], a[8:11], v[68:71], v[144:147]
buffer_load_dwordx4 v7, s[4:7], s44 offen offset:0 sc1 lds
v_mfma_f32_16x16x32_bf16 v[152:155], a[8:11], v[84:87], v[152:155]
s_sub_u32 s93, s55, s43
v_exp_f32 v130, v130
v_cmp_gt_i32 s[32:33], 2, v8
v_mfma_f32_16x16x32_bf16 v[176:179], a[8:11], v[100:103], v[176:179]
s_mov_b32 s43, s55
v_cvt_pk_bf16_f32 v240, v160, v161
v_cndmask_b32 v17, v192, v34, s[34:35]
v_mfma_f32_16x16x32_bf16 v[184:187], a[8:11], v[116:119], v[184:187]
ds_read_b128 a[8:11], v62 offset:1024
v_cmp_gt_i32 s[34:35], 3, v8
v_mfma_f32_16x16x32_bf16 v[148:151], a[12:15], v[68:71], v[148:151]
s_mov_b32 s49, s46
v_exp_f32 v131, v131
v_mfma_f32_16x16x32_bf16 v[156:159], a[12:15], v[84:87], v[156:159]
s_mov_b32 s55, s93
v_exp_f32 v162, v162
v_mfma_f32_16x16x32_bf16 v[180:183], a[12:15], v[100:103], v[180:183]
v_cvt_pk_bf16_f32 v225, v130, v131
v_cndmask_b32 v18, v192, v34, s[32:33]
v_mfma_f32_16x16x32_bf16 v[188:191], a[12:15], v[116:119], v[188:191]
ds_read_b128 a[12:15], v62 offset:5120
v_cmp_gt_i32 s[32:33], 16, v8
v_mfma_f32_16x16x32_bf16 v[144:147], a[16:19], v[72:75], v[144:147]
v_exp_f32 v163, v163
v_mfma_f32_16x16x32_bf16 v[152:155], a[16:19], v[88:91], v[152:155]
v_exp_f32 v132, v132
v_mfma_f32_16x16x32_bf16 v[176:179], a[16:19], v[104:107], v[176:179]
s_add_u32 s46, s46, 16384
v_cvt_pk_bf16_f32 v241, v162, v163
v_cndmask_b32 v19, v192, v34, s[34:35]
v_mfma_f32_16x16x32_bf16 v[184:187], a[16:19], v[120:123], v[184:187]
ds_read_b128 a[16:19], v62 offset:2048
v_cmp_gt_i32 s[34:35], 17, v8
v_mfma_f32_16x16x32_bf16 v[148:151], a[20:23], v[72:75], v[148:151]
buffer_load_dwordx4 v7, s[4:7], s44 offen offset:1024 sc1 lds
v_mfma_f32_16x16x32_bf16 v[156:159], a[20:23], v[88:91], v[156:159]
v_exp_f32 v133, v133
v_mfma_f32_16x16x32_bf16 v[180:183], a[20:23], v[104:107], v[180:183]
v_exp_f32 v164, v164
v_cndmask_b32 v20, v192, v34, s[32:33]
v_mfma_f32_16x16x32_bf16 v[188:191], a[20:23], v[120:123], v[188:191]
ds_read_b128 a[20:23], v62 offset:6144
v_cvt_pk_bf16_f32 v226, v132, v133
v_mfma_f32_16x16x32_bf16 v[144:147], a[24:27], v[76:79], v[144:147]
v_exp_f32 v165, v165
v_cmp_gt_i32 s[32:33], 18, v8
v_mfma_f32_16x16x32_bf16 v[152:155], a[24:27], v[92:95], v[152:155]
v_exp_f32 v134, v134
v_mfma_f32_16x16x32_bf16 v[176:179], a[24:27], v[108:111], v[176:179]
s_and_b32 s46, s46, 0xffff
v_cvt_pk_bf16_f32 v242, v164, v165
v_cndmask_b32 v21, v192, v34, s[34:35]
v_mfma_f32_16x16x32_bf16 v[184:187], a[24:27], v[124:127], v[184:187]
ds_read_b128 a[24:27], v62 offset:3072
v_cmp_gt_i32 s[34:35], 19, v8
v_mfma_f32_16x16x32_bf16 v[148:151], a[28:31], v[76:79], v[148:151]
v_exp_f32 v135, v135
v_mfma_f32_16x16x32_bf16 v[156:159], a[28:31], v[92:95], v[156:159]
v_exp_f32 v166, v166
v_mfma_f32_16x16x32_bf16 v[180:183], a[28:31], v[108:111], v[180:183]
v_cvt_pk_bf16_f32 v227, v134, v135
v_cndmask_b32 v22, v192, v34, s[32:33]
v_mfma_f32_16x16x32_bf16 v[188:191], a[28:31], v[124:127], v[188:191]
ds_read_b128 a[28:31], v62 offset:7168
v_cmp_gt_i32 s[32:33], -16, v8
v_mfma_f32_16x16x32_bf16 v[208:211], v[52:55], v[232:235], v[208:211]
v_exp_f32 v167, v167
v_mfma_f32_16x16x32_bf16 v[212:215], v[52:55], v[236:239], v[212:215]
v_exp_f32 v136, v136
v_mfma_f32_16x16x32_bf16 v[216:219], v[52:55], v[248:251], v[216:219]
v_cvt_pk_bf16_f32 v243, v166, v167
v_cndmask_b32 v23, v192, v34, s[34:35]
v_mfma_f32_16x16x32_bf16 v[220:223], v[52:55], v[252:255], v[220:223]
s_waitcnt lgkmcnt(8)
v_exp_f32 v137, v137
v_mfma_f32_16x16x32_bf16 a[64:67], a[32:35], v[232:235], a[64:67]
buffer_load_dwordx4 v7, s[4:7], s44 offen offset:2048 sc1 lds
v_cmp_gt_i32 s[34:35], -15, v8
v_mfma_f32_16x16x32_bf16 a[72:75], a[32:35], v[236:239], a[72:75]
v_exp_f32 v168, v168
v_mfma_f32_16x16x32_bf16 a[176:179], a[32:35], v[248:251], a[176:179]
v_cvt_pk_bf16_f32 v228, v136, v137
v_cndmask_b32 v24, v196, v34, s[32:33]
v_mfma_f32_16x16x32_bf16 a[184:187], a[32:35], v[252:255], a[184:187]
ds_read_b128 a[32:35], v63 offset:0
v_cmp_gt_i32 s[32:33], -14, v8
v_mfma_f32_16x16x32_bf16 a[68:71], a[36:39], v[232:235], a[68:71]
v_exp_f32 v169, v169
v_mfma_f32_16x16x32_bf16 a[76:79], a[36:39], v[236:239], a[76:79]
v_exp_f32 v138, v138
v_mfma_f32_16x16x32_bf16 a[180:183], a[36:39], v[248:251], a[180:183]
v_cvt_pk_bf16_f32 v244, v168, v169
v_cndmask_b32 v25, v196, v34, s[34:35]
v_mfma_f32_16x16x32_bf16 a[188:191], a[36:39], v[252:255], a[188:191]
ds_read_b128 a[36:39], v63 offset:1024
v_cmp_gt_i32 s[34:35], -13, v8
v_mfma_f32_16x16x32_bf16 a[80:83], a[40:43], v[232:235], a[80:83]
v_exp_f32 v139, v139
v_mfma_f32_16x16x32_bf16 a[88:91], a[40:43], v[236:239], a[88:91]
v_exp_f32 v170, v170
v_mfma_f32_16x16x32_bf16 a[208:211], a[40:43], v[248:251], a[208:211]
v_cvt_pk_bf16_f32 v229, v138, v139
v_cndmask_b32 v26, v196, v34, s[32:33]
v_mfma_f32_16x16x32_bf16 a[216:219], a[40:43], v[252:255], a[216:219]
ds_read_b128 a[40:43], v63 offset:2048
v_cmp_gt_i32 s[32:33], 0, v8
v_mfma_f32_16x16x32_bf16 a[84:87], a[44:47], v[232:235], a[84:87]
buffer_load_dwordx4 v7, s[4:7], s44 offen offset:3072 sc1 lds
v_mfma_f32_16x16x32_bf16 a[92:95], a[44:47], v[236:239], a[92:95]
v_exp_f32 v171, v171
v_mfma_f32_16x16x32_bf16 a[212:215], a[44:47], v[248:251], a[212:215]
v_exp_f32 v140, v140
v_mfma_f32_16x16x32_bf16 a[220:223], a[44:47], v[252:255], a[220:223]
ds_read_b128 a[44:47], v63 offset:3072
v_cvt_pk_bf16_f32 v245, v170, v171
v_mfma_f32_16x16x32_bf16 a[112:115], a[48:51], v[232:235], a[112:115]
v_exp_f32 v141, v141
v_mfma_f32_16x16x32_bf16 a[120:123], a[48:51], v[236:239], a[120:123]
v_exp_f32 v172, v172
v_mfma_f32_16x16x32_bf16 a[240:243], a[48:51], v[248:251], a[240:243]
v_cndmask_b32 v27, v196, v34, s[34:35]
v_cmp_gt_i32 s[34:35], 1, v8
v_mfma_f32_16x16x32_bf16 a[248:251], a[48:51], v[252:255], a[248:251]
ds_read_b128 a[48:51], v63 offset:4096
v_cvt_pk_bf16_f32 v230, v140, v141
v_mfma_f32_16x16x32_bf16 a[116:119], a[52:55], v[232:235], a[116:119]
v_exp_f32 v173, v173
v_mfma_f32_16x16x32_bf16 a[124:127], a[52:55], v[236:239], a[124:127]
v_exp_f32 v142, v142
v_mfma_f32_16x16x32_bf16 a[244:247], a[52:55], v[248:251], a[244:247]
v_cndmask_b32 v28, v196, v34, s[32:33]
v_cmp_gt_i32 s[32:33], 2, v8
v_mfma_f32_16x16x32_bf16 a[252:255], a[52:55], v[252:255], a[252:255]
ds_read_b128 a[52:55], v63 offset:5120
v_cvt_pk_bf16_f32 v246, v172, v173
v_mfma_f32_16x16x32_bf16 a[144:147], a[56:59], v[232:235], a[144:147]
v_exp_f32 v143, v143
v_mfma_f32_16x16x32_bf16 a[152:155], a[56:59], v[236:239], a[152:155]
v_cndmask_b32 v29, v196, v34, s[34:35]
v_cmp_gt_i32 s[34:35], 3, v8
v_mfma_f32_16x16x32_bf16 a[96:99], a[56:59], v[248:251], a[96:99]
v_exp_f32 v174, v174
v_mfma_f32_16x16x32_bf16 a[104:107], a[56:59], v[252:255], a[104:107]
ds_read_b128 a[56:59], v63 offset:6144
v_cvt_pk_bf16_f32 v231, v142, v143
v_mfma_f32_16x16x32_bf16 a[148:151], a[60:63], v[232:235], a[148:151]
v_exp_f32 v175, v175
v_mfma_f32_16x16x32_bf16 a[156:159], a[60:63], v[236:239], a[156:159]
s_waitcnt lgkmcnt(7)
v_cndmask_b32 v30, v196, v34, s[32:33]
v_nop
v_mfma_f32_16x16x32_bf16 a[100:103], a[60:63], v[248:251], a[100:103]
v_cvt_pk_bf16_f32 v247, v174, v175
v_cndmask_b32 v31, v196, v34, s[34:35]
v_mfma_f32_16x16x32_bf16 a[108:111], a[60:63], v[252:255], a[108:111]
ds_read_b128 a[60:63], v63 offset:7168
v_mfma_f32_16x16x32_bf16 v[128:131], a[0:3], v[64:67], v[192:195]
s_cmp_lt_u32 s44, s45
v_exp_f32 v144, v144
v_mfma_f32_16x16x32_bf16 v[136:139], a[0:3], v[80:83], v[196:199]
s_cselect_b32 m0, s46, s27
v_exp_f32 v145, v145
v_mfma_f32_16x16x32_bf16 v[132:135], a[4:7], v[64:67], v[192:195]
s_and_b32 s36, s86, 1
s_lshl_b32 s36, s36, 3
v_exp_f32 v176, v176
v_mfma_f32_16x16x32_bf16 v[140:143], a[4:7], v[80:83], v[196:199]
ds_read_b128 a[0:3], v62 offset:8192
ds_read_b128 a[4:7], v62 offset:12288
v_cvt_pk_bf16_f32 v232, v144, v145
v_mfma_f32_16x16x32_bf16 v[128:131], a[8:11], v[68:71], v[128:131]
s_and_b32 s28, s87, 7
s_or_b32 s36, s36, s28
v_exp_f32 v177, v177
v_mfma_f32_16x16x32_bf16 v[136:139], a[8:11], v[84:87], v[136:139]
ds_read_b128 a[8:11], v62 offset:9216
v_exp_f32 v146, v146
v_mfma_f32_16x16x32_bf16 v[132:135], a[12:15], v[68:71], v[132:135]
buffer_load_dwordx4 v7, s[8:11], s44 offen offset:0 sc1 lds
v_mfma_f32_16x16x32_bf16 v[140:143], a[12:15], v[84:87], v[140:143]
ds_read_b128 a[12:15], v62 offset:13312
v_cvt_pk_bf16_f32 v248, v176, v177
v_mfma_f32_16x16x32_bf16 v[128:131], a[16:19], v[72:75], v[128:131]
s_lshr_b32 s37, s86, 1
s_lshl_b32 s37, s37, 2
v_exp_f32 v147, v147
v_mfma_f32_16x16x32_bf16 v[136:139], a[16:19], v[88:91], v[136:139]
s_xor_b32 s50, s46, 0x18000
v_exp_f32 v178, v178
v_mfma_f32_16x16x32_bf16 v[132:135], a[20:23], v[72:75], v[132:135]
s_lshr_b32 s28, s87, 3
s_or_b32 s37, s37, s28
v_cvt_pk_bf16_f32 v233, v146, v147
v_mfma_f32_16x16x32_bf16 v[140:143], a[20:23], v[88:91], v[140:143]
ds_read_b128 a[16:19], v62 offset:10240
ds_read_b128 a[20:23], v62 offset:14336
v_mfma_f32_16x16x32_bf16 v[128:131], a[24:27], v[76:79], v[128:131]
s_lshl_b32 s24, s36, 18
s_lshl_b32 s28, s3, 16
v_exp_f32 v179, v179
v_mfma_f32_16x16x32_bf16 v[136:139], a[24:27], v[92:95], v[136:139]
ds_read_b128 a[24:27], v62 offset:11264
v_exp_f32 v148, v148
v_mfma_f32_16x16x32_bf16 v[132:135], a[28:31], v[76:79], v[132:135]
s_add_u32 s24, s24, s28
s_mov_b32 s25, 0
v_cvt_pk_bf16_f32 v249, v178, v179
v_mfma_f32_16x16x32_bf16 v[140:143], a[28:31], v[92:95], v[140:143]
ds_read_b128 a[28:31], v62 offset:15360
v_exp_f32 v149, v149
v_mfma_f32_16x16x32_bf16 v[208:211], v[52:55], v[224:227], v[208:211]
buffer_load_dwordx4 v7, s[8:11], s44 offen offset:1024 sc1 lds
v_add_u32 v62, s46, v32
v_mfma_f32_16x16x32_bf16 v[212:215], v[52:55], v[228:231], v[212:215]
s_mul_i32 s26, s3, 98304
s_lshl_b32 s28, s37, 7
v_exp_f32 v180, v180
v_mfma_f32_16x16x32_bf16 v[216:219], v[52:55], v[240:243], v[216:219]
s_add_u32 s26, s26, s28
s_add_u32 s47, s26, 4096
v_cvt_pk_bf16_f32 v234, v148, v149
v_mfma_f32_16x16x32_bf16 v[220:223], v[52:55], v[244:247], v[220:223]
s_waitcnt lgkmcnt(8)
v_exp_f32 v181, v181
v_mfma_f32_16x16x32_bf16 a[64:67], a[32:35], v[224:227], a[64:67]
s_mul_i32 s48, s3, 7168
s_mov_b32 s12, s64
v_exp_f32 v150, v150
v_mfma_f32_16x16x32_bf16 a[72:75], a[32:35], v[228:231], a[72:75]
s_and_b32 s13, s65, 0xffff
s_mov_b32 s14, 0x400000
v_cvt_pk_bf16_f32 v250, v180, v181
v_mfma_f32_16x16x32_bf16 a[176:179], a[32:35], v[240:243], a[176:179]
s_mov_b32 s16, s66
s_and_b32 s17, s67, 0xffff
v_exp_f32 v151, v151
v_mfma_f32_16x16x32_bf16 a[184:187], a[32:35], v[244:247], a[184:187]
ds_read_b128 a[32:35], v63 offset:8192
v_exp_f32 v182, v182
v_mfma_f32_16x16x32_bf16 a[68:71], a[36:39], v[224:227], a[68:71]
s_mov_b32 s18, 0x600000
s_lshl_b32 s29, s36, 18
v_cvt_pk_bf16_f32 v235, v150, v151
v_mfma_f32_16x16x32_bf16 a[76:79], a[36:39], v[228:231], a[76:79]
s_lshl_b32 s28, s3, 13
s_add_u32 s29, s29, s28
v_exp_f32 v183, v183
v_mfma_f32_16x16x32_bf16 a[180:183], a[36:39], v[240:243], a[180:183]
s_sub_u32 s29, s29, 34816
v_exp_f32 v152, v152
v_mfma_f32_16x16x32_bf16 a[188:191], a[36:39], v[244:247], a[188:191]
ds_read_b128 a[36:39], v63 offset:9216
.Lpad12:
s_nop 0
v_cvt_pk_bf16_f32 v251, v182, v183
v_mfma_f32_16x16x32_bf16 a[80:83], a[40:43], v[224:227], a[80:83]
buffer_load_dwordx4 v7, s[8:11], s44 offen offset:2048 sc1 lds
v_mfma_f32_16x16x32_bf16 a[88:91], a[40:43], v[228:231], a[88:91]
s_cmp_eq_u32 s36, 0
s_cselect_b32 s29, 0x7f000000, s29
v_exp_f32 v153, v153
v_mfma_f32_16x16x32_bf16 a[208:211], a[40:43], v[240:243], a[208:211]
s_lshl_b32 s32, s3, 2
s_and_b32 s33, s3, 1
v_exp_f32 v184, v184
v_mfma_f32_16x16x32_bf16 a[216:219], a[40:43], v[244:247], a[216:219]
ds_read_b128 a[40:43], v63 offset:10240
v_cvt_pk_bf16_f32 v236, v152, v153
v_mfma_f32_16x16x32_bf16 a[84:87], a[44:47], v[224:227], a[84:87]
s_lshl_b32 s33, s33, 1
s_lshr_b32 s34, s3, 1
v_exp_f32 v185, v185
v_mfma_f32_16x16x32_bf16 a[92:95], a[44:47], v[228:231], a[92:95]
s_lshl_b32 s35, s34, 13
s_add_u32 s35, s35, 65536
v_exp_f32 v154, v154
v_mfma_f32_16x16x32_bf16 a[212:215], a[44:47], v[240:243], a[212:215]
s_mov_b32 s38, 131072
s_lshl_b32 s28, s88, 13
v_cvt_pk_bf16_f32 v252, v184, v185
v_mfma_f32_16x16x32_bf16 a[220:223], a[44:47], v[244:247], a[220:223]
ds_read_b128 a[44:47], v63 offset:11264
v_exp_f32 v155, v155
v_mfma_f32_16x16x32_bf16 a[112:115], a[48:51], v[224:227], a[112:115]
buffer_load_dwordx4 v7, s[8:11], s44 offen offset:3072 sc1 lds
v_mfma_f32_16x16x32_bf16 a[120:123], a[48:51], v[228:231], a[120:123]
s_add_u32 s28, s28, 33554432
v_exp_f32 v186, v186
v_mfma_f32_16x16x32_bf16 a[240:243], a[48:51], v[240:243], a[240:243]
s_add_u32 s20, s84, s28
s_addc_u32 s21, s85, 0
v_cvt_pk_bf16_f32 v237, v154, v155
v_mfma_f32_16x16x32_bf16 a[248:251], a[48:51], v[244:247], a[248:251]
ds_read_b128 a[48:51], v63 offset:12288
v_exp_f32 v187, v187
v_mfma_f32_16x16x32_bf16 a[116:119], a[52:55], v[224:227], a[116:119]
s_add_u32 s44, s44, 16384
v_exp_f32 v156, v156
v_cvt_pk_bf16_f32 v253, v186, v187
v_mfma_f32_16x16x32_bf16 a[124:127], a[52:55], v[228:231], a[124:127]
s_and_b32 s21, s21, 0xffff
s_mov_b32 s22, 16777216
v_exp_f32 v157, v157
v_mfma_f32_16x16x32_bf16 a[244:247], a[52:55], v[240:243], a[244:247]
s_sub_u32 s40, 15, s89
s_lshl_b32 s40, s40, 2
v_exp_f32 v188, v188
v_mfma_f32_16x16x32_bf16 a[252:255], a[52:55], v[244:247], a[252:255]
ds_read_b128 a[52:55], v63 offset:13312
v_cvt_pk_bf16_f32 v238, v156, v157
v_mfma_f32_16x16x32_bf16 a[144:147], a[56:59], v[224:227], a[144:147]
s_add_u32 s40, s40, s3
s_lshl_b32 s40, s40, 18
v_exp_f32 v189, v189
v_mfma_f32_16x16x32_bf16 a[152:155], a[56:59], v[228:231], a[152:155]
s_lshl_b32 s41, s89, 2
s_add_u32 s41, s41, s94
v_exp_f32 v158, v158
v_mfma_f32_16x16x32_bf16 a[96:99], a[56:59], v[240:243], a[96:99]
s_lshl_b32 s41, s41, 18
s_mov_b32 s28, 1602224128
v_cvt_pk_bf16_f32 v254, v188, v189
v_mfma_f32_16x16x32_bf16 a[104:107], a[56:59], v[244:247], a[104:107]
ds_read_b128 a[56:59], v63 offset:14336
v_exp_f32 v159, v159
v_mfma_f32_16x16x32_bf16 a[148:151], a[60:63], v[224:227], a[148:151]
v_exp_f32 v190, v190
v_cvt_pk_bf16_f32 v239, v158, v159
v_mfma_f32_16x16x32_bf16 a[156:159], a[60:63], v[228:231], a[156:159]
s_waitcnt lgkmcnt(7)
v_exp_f32 v191, v191
v_mfma_f32_16x16x32_bf16 a[100:103], a[60:63], v[240:243], a[100:103]
s_waitcnt vmcnt(9)
s_barrier
v_cvt_pk_bf16_f32 v255, v190, v191
v_mfma_f32_16x16x32_bf16 a[108:111], a[60:63], v[244:247], a[108:111]
ds_read_b128 a[60:63], v63 offset:15360
v_mfma_f32_16x16x32_bf16 v[144:147], a[0:3], v[64:67], v[192:195]
s_cmp_lt_u32 s44, s54
v_exp_f32 v128, v128
v_lshrrev_b32 v56, 3, v1
v_mfma_f32_16x16x32_bf16 v[152:155], a[0:3], v[80:83], v[196:199]
s_cselect_b32 m0, s50, s27
v_exp_f32 v129, v129
v_and_b32 v57, 7, v1
v_mfma_f32_16x16x32_bf16 v[148:151], a[4:7], v[64:67], v[192:195]
v_add_u32 v63, s49, v33
v_xor_b32 v58, v57, v56
v_lshlrev_b32 v58, 4, v58
v_mfma_f32_16x16x32_bf16 v[156:159], a[4:7], v[80:83], v[196:199]
ds_read_b128 a[0:3], v62 offset:0
ds_read_b128 a[4:7], v62 offset:4096
v_lshl_add_u32 v36, v56, 11, v58
v_mfma_f32_16x16x32_bf16 v[144:147], a[8:11], v[68:71], v[144:147]
v_exp_f32 v130, v130
v_add_u32 v37, 15360, v36
v_mfma_f32_16x16x32_bf16 v[152:155], a[8:11], v[84:87], v[152:155]
ds_read_b128 a[8:11], v62 offset:1024
v_add_u32 v38, 30720, v36
v_mfma_f32_16x16x32_bf16 v[148:151], a[12:15], v[68:71], v[148:151]
buffer_load_dwordx4 v7, s[4:7], s44 offen offset:0 sc1 lds
v_add_u32 v39, 46080, v36
v_mfma_f32_16x16x32_bf16 v[156:159], a[12:15], v[84:87], v[156:159]
ds_read_b128 a[12:15], v62 offset:5120
v_cvt_pk_bf16_f32 v224, v128, v129
v_add_u32 v58, s32, v56
v_mfma_f32_16x16x32_bf16 v[144:147], a[16:19], v[72:75], v[144:147]
v_xor_b32 v58, v58, v57
v_and_b32 v58, 7, v58
v_mfma_f32_16x16x32_bf16 v[152:155], a[16:19], v[88:91], v[152:155]
s_mov_b32 s49, s46
v_exp_f32 v131, v131
v_mfma_f32_16x16x32_bf16 v[148:151], a[20:23], v[72:75], v[148:151]
v_lshlrev_b32 v58, 4, v58
v_lshl_add_u32 v58, v56, 11, v58
v_mfma_f32_16x16x32_bf16 v[156:159], a[20:23], v[88:91], v[156:159]
ds_read_b128 a[16:19], v62 offset:2048
ds_read_b128 a[20:23], v62 offset:6144
v_add_u32 v58, s29, v58
v_mfma_f32_16x16x32_bf16 v[144:147], a[24:27], v[76:79], v[144:147]
v_exp_f32 v132, v132
v_and_b32 v59, 32, v1
v_mfma_f32_16x16x32_bf16 v[152:155], a[24:27], v[92:95], v[152:155]
ds_read_b128 a[24:27], v62 offset:3072
v_cvt_pk_bf16_f32 v225, v130, v131
v_lshlrev_b32 v59, 25, v59
v_mfma_f32_16x16x32_bf16 v[148:151], a[28:31], v[76:79], v[148:151]
s_add_u32 s46, s46, 16384
v_exp_f32 v133, v133
v_or_b32 v42, v59, v58
v_mfma_f32_16x16x32_bf16 v[156:159], a[28:31], v[92:95], v[156:159]
ds_read_b128 a[28:31], v62 offset:7168
v_bfe_u32 v58, v1, 4, 1
v_lshlrev_b32 v58, 1, v58
v_mfma_f32_16x16x32_bf16 v[208:211], v[52:55], v[232:235], v[208:211]
buffer_load_dwordx4 v7, s[4:7], s44 offen offset:1024 sc1 lds
v_mfma_f32_16x16x32_bf16 v[212:215], v[52:55], v[236:239], v[212:215]
v_xor_b32 v58, v58, v57
v_lshlrev_b32 v58, 4, v58
v_mfma_f32_16x16x32_bf16 v[216:219], v[52:55], v[248:251], v[216:219]
v_exp_f32 v134, v134
v_mul_u32_u24 v59, 6144, v56
v_mfma_f32_16x16x32_bf16 v[220:223], v[52:55], v[252:255], v[220:223]
s_waitcnt lgkmcnt(8)
s_and_b32 s46, s46, 0xffff
v_add_u32 v40, v59, v58
v_mfma_f32_16x16x32_bf16 a[64:67], a[32:35], v[232:235], a[64:67]
v_cvt_pk_bf16_f32 v226, v132, v133
v_xor_b32 v41, 64, v40
v_mfma_f32_16x16x32_bf16 a[72:75], a[32:35], v[236:239], a[72:75]
v_add_u32 v41, 48128, v41
v_and_b32 v56, 15, v1
v_mfma_f32_16x16x32_bf16 a[176:179], a[32:35], v[248:251], a[176:179]
v_exp_f32 v135, v135
v_lshrrev_b32 v57, 4, v1
v_mfma_f32_16x16x32_bf16 a[184:187], a[32:35], v[252:255], a[184:187]
ds_read_b128 a[32:35], v63 offset:0
v_and_b32 v58, 7, v56
v_mfma_f32_16x16x32_bf16 a[68:71], a[36:39], v[232:235], a[68:71]
v_exp_f32 v136, v136
v_xor_b32 v58, v58, v57
v_mfma_f32_16x16x32_bf16 a[76:79], a[36:39], v[236:239], a[76:79]
v_lshlrev_b32 v58, 4, v58
v_lshl_add_u32 v43, v56, 7, v58
v_mfma_f32_16x16x32_bf16 a[180:183], a[36:39], v[248:251], a[180:183]
v_cvt_pk_bf16_f32 v227, v134, v135
v_add_u32 v43, s35, v43
v_mfma_f32_16x16x32_bf16 a[188:191], a[36:39], v[252:255], a[188:191]
ds_read_b128 a[36:39], v63 offset:1024
v_xor_b32 v44, 64, v43
v_mfma_f32_16x16x32_bf16 a[80:83], a[40:43], v[232:235], a[80:83]
buffer_load_dwordx4 v7, s[4:7], s44 offen offset:2048 sc1 lds
v_and_b32 v56, 15, v1
v_mfma_f32_16x16x32_bf16 a[88:91], a[40:43], v[236:239], a[88:91]
v_exp_f32 v137, v137
v_lshrrev_b32 v57, 2, v56
v_mfma_f32_16x16x32_bf16 a[208:211], a[40:43], v[248:251], a[208:211]
v_exp_f32 v138, v138
v_mul_u32_u24 v57, 6656, v57
v_add_f32 v13, v216, v220
v_mfma_f32_16x16x32_bf16 a[216:219], a[40:43], v[252:255], a[216:219]
ds_read_b128 a[40:43], v63 offset:2048
v_add_u32 v57, 71680, v57
v_mov_b32 v58, -2048
v_mfma_f32_16x16x32_bf16 a[84:87], a[44:47], v[232:235], a[84:87]
v_cvt_pk_bf16_f32 v228, v136, v137
v_mov_b32 v59, s34
v_mfma_f32_16x16x32_bf16 a[92:95], a[44:47], v[236:239], a[92:95]
v_cmp_eq_u32 vcc, 0, v59
v_nop
v_mfma_f32_16x16x32_bf16 a[212:215], a[44:47], v[248:251], a[212:215]
v_exp_f32 v139, v139
v_mfma_f32_16x16x32_bf16 a[220:223], a[44:47], v[252:255], a[220:223]
ds_read_b128 a[44:47], v63 offset:3072
v_nop
v_mfma_f32_16x16x32_bf16 a[112:115], a[48:51], v[232:235], a[112:115]
buffer_load_dwordx4 v7, s[4:7], s44 offen offset:3072 sc1 lds
v_cndmask_b32 v57, v58, v57, vcc
v_mfma_f32_16x16x32_bf16 a[120:123], a[48:51], v[236:239], a[120:123]
v_exp_f32 v140, v140
v_add_u32 v45, v57, v43
v_mfma_f32_16x16x32_bf16 a[240:243], a[48:51], v[248:251], a[240:243]
v_add_u32 v46, v57, v44
v_bfe_u32 v56, v1, 2, 2
v_mfma_f32_16x16x32_bf16 a[248:251], a[48:51], v[252:255], a[248:251]
ds_read_b128 a[48:51], v63 offset:4096
v_cvt_pk_bf16_f32 v229, v138, v139
v_mfma_f32_16x16x32_bf16 a[116:119], a[52:55], v[232:235], a[116:119]
v_bfe_u32 v57, v1, 4, 1
v_lshl_add_u32 v58, v57, 3, v56
v_mfma_f32_16x16x32_bf16 a[124:127], a[52:55], v[236:239], a[124:127]
v_exp_f32 v141, v141
v_lshlrev_b32 v58, 7, v58
v_mfma_f32_16x16x32_bf16 a[244:247], a[52:55], v[248:251], a[244:247]
v_lshrrev_b32 v59, 5, v1
v_mul_u32_u24 v59, 7168, v59
v_mfma_f32_16x16x32_bf16 a[252:255], a[52:55], v[252:255], a[252:255]
ds_read_b128 a[52:55], v63 offset:5120
v_add3_u32 v58, v58, v59, s38
v_cmp_nge_f32 s[52:53], s28, v13
v_mfma_f32_16x16x32_bf16 a[144:147], a[56:59], v[232:235], a[144:147]
v_exp_f32 v142, v142
v_lshrrev_b32 v56, 1, v56
v_mfma_f32_16x16x32_bf16 a[152:155], a[56:59], v[236:239], a[152:155]
v_cvt_pk_bf16_f32 v230, v140, v141
v_lshl_or_b32 v56, v57, 1, v56
v_mfma_f32_16x16x32_bf16 a[96:99], a[56:59], v[248:251], a[96:99]
v_xor_b32 v56, s33, v56
v_lshl_add_u32 v58, v56, 5, v58
v_mfma_f32_16x16x32_bf16 a[104:107], a[56:59], v[252:255], a[104:107]
ds_read_b128 a[56:59], v63 offset:6144
v_and_b32 v56, 3, v1
v_mfma_f32_16x16x32_bf16 a[148:151], a[60:63], v[232:235], a[148:151]
v_exp_f32 v143, v143
v_mfma_f32_16x16x32_bf16 a[156:159], a[60:63], v[236:239], a[156:159]
s_waitcnt lgkmcnt(7)
v_cvt_pk_bf16_f32 v231, v142, v143
v_lshl_add_u32 v47, v56, 3, v58
v_mfma_f32_16x16x32_bf16 a[100:103], a[60:63], v[248:251], a[100:103]
v_xor_b32 v48, 32, v47
v_add_u32 v51, 4096, v7
v_mfma_f32_16x16x32_bf16 a[108:111], a[60:63], v[252:255], a[108:111]
ds_read_b128 a[60:63], v63 offset:7168
s_cmp_le_u32 s94, 3
s_cbranch_scc1 .LTf1
.LTc1:
s_cmp_lt_i32 s55, 0
s_cbranch_scc1 .LE0
s_branch .LS0
.LTf1:
s_cmp_lg_u64 s[52:53], 0
s_cbranch_scc1 .LTc1
s_getpc_b64 s[30:31]
.LTp1:
s_sub_u32 s30, s30, .LTp1-.LTc1
s_subb_u32 s31, s31, 0
s_branch .Lfb
s_nop 0
s_nop 0
s_nop 0
s_nop 0
s_nop 0
s_nop 0
.LS1:
v_mfma_f32_16x16x32_bf16 v[144:147], a[0:3], v[64:67], v[192:195]
s_cmp_lt_u32 s44, s54
v_exp_f32 v128, v128
v_mfma_f32_16x16x32_bf16 v[152:155], a[0:3], v[80:83], v[196:199]
s_cselect_b32 m0, s50, s27
v_exp_f32 v129, v129
v_mfma_f32_16x16x32_bf16 v[148:151], a[4:7], v[64:67], v[192:195]
v_add_u32 v63, s49, v33
v_exp_f32 v130, v130
v_mfma_f32_16x16x32_bf16 v[156:159], a[4:7], v[80:83], v[196:199]
ds_read_b128 a[0:3], v62 offset:0
ds_read_b128 a[4:7], v62 offset:4096
v_mfma_f32_16x16x32_bf16 v[144:147], a[8:11], v[68:71], v[144:147]
buffer_load_dwordx4 v7, s[4:7], s44 offen offset:0 sc1 lds
v_mfma_f32_16x16x32_bf16 v[152:155], a[8:11], v[84:87], v[152:155]
ds_read_b128 a[8:11], v62 offset:1024
v_cvt_pk_bf16_f32 v224, v128, v129
v_mfma_f32_16x16x32_bf16 v[148:151], a[12:15], v[68:71], v[148:151]
s_mov_b32 s49, s46
v_exp_f32 v131, v131
v_mfma_f32_16x16x32_bf16 v[156:159], a[12:15], v[84:87], v[156:159]
ds_read_b128 a[12:15], v62 offset:5120
v_exp_f32 v132, v132
v_mfma_f32_16x16x32_bf16 v[144:147], a[16:19], v[72:75], v[144:147]
v_cvt_pk_bf16_f32 v225, v130, v131
v_mfma_f32_16x16x32_bf16 v[152:155], a[16:19], v[88:91], v[152:155]
s_add_u32 s46, s46, 16384
v_exp_f32 v133, v133
v_mfma_f32_16x16x32_bf16 v[148:151], a[20:23], v[72:75], v[148:151]
buffer_load_dwordx4 v7, s[4:7], s44 offen offset:1024 sc1 lds
v_mfma_f32_16x16x32_bf16 v[156:159], a[20:23], v[88:91], v[156:159]
ds_read_b128 a[16:19], v62 offset:2048
ds_read_b128 a[20:23], v62 offset:6144
v_mfma_f32_16x16x32_bf16 v[144:147], a[24:27], v[76:79], v[144:147]
v_exp_f32 v134, v134
v_mfma_f32_16x16x32_bf16 v[152:155], a[24:27], v[92:95], v[152:155]
ds_read_b128 a[24:27], v62 offset:3072
v_cvt_pk_bf16_f32 v226, v132, v133
v_mfma_f32_16x16x32_bf16 v[148:151], a[28:31], v[76:79], v[148:151]
s_and_b32 s46, s46, 0xffff
v_exp_f32 v135, v135
v_mfma_f32_16x16x32_bf16 v[156:159], a[28:31], v[92:95], v[156:159]
ds_read_b128 a[28:31], v62 offset:7168
v_mfma_f32_16x16x32_bf16 v[208:211], v[52:55], v[232:235], v[208:211]
v_exp_f32 v136, v136
v_mfma_f32_16x16x32_bf16 v[212:215], v[52:55], v[236:239], v[212:215]
s_waitcnt lgkmcnt(8)
v_cvt_pk_bf16_f32 v227, v134, v135
v_mfma_f32_16x16x32_bf16 a[64:67], a[32:35], v[232:235], a[64:67]
buffer_load_dwordx4 v7, s[4:7], s44 offen offset:2048 sc1 lds
v_mfma_f32_16x16x32_bf16 a[72:75], a[32:35], v[236:239], a[72:75]
v_exp_f32 v137, v137
v_mfma_f32_16x16x32_bf16 a[68:71], a[36:39], v[232:235], a[68:71]
v_exp_f32 v138, v138
v_mfma_f32_16x16x32_bf16 a[76:79], a[36:39], v[236:239], a[76:79]
ds_read_b128 a[32:35], v63 offset:0
ds_read_b128 a[36:39], v63 offset:1024
v_cvt_pk_bf16_f32 v228, v136, v137
v_mfma_f32_16x16x32_bf16 a[80:83], a[40:43], v[232:235], a[80:83]
v_exp_f32 v139, v139
v_mfma_f32_16x16x32_bf16 a[88:91], a[40:43], v[236:239], a[88:91]
ds_read_b128 a[40:43], v63 offset:2048
v_exp_f32 v140, v140
v_mfma_f32_16x16x32_bf16 a[84:87], a[44:47], v[232:235], a[84:87]
buffer_load_dwordx4 v7, s[4:7], s44 offen offset:3072 sc1 lds
v_mfma_f32_16x16x32_bf16 a[92:95], a[44:47], v[236:239], a[92:95]
ds_read_b128 a[44:47], v63 offset:3072
v_cvt_pk_bf16_f32 v229, v138, v139
v_mfma_f32_16x16x32_bf16 a[112:115], a[48:51], v[232:235], a[112:115]
v_exp_f32 v141, v141
v_mfma_f32_16x16x32_bf16 a[120:123], a[48:51], v[236:239], a[120:123]
v_exp_f32 v142, v142
v_mfma_f32_16x16x32_bf16 a[116:119], a[52:55], v[232:235], a[116:119]
s_sub_u32 s55, s55, 1
v_cvt_pk_bf16_f32 v230, v140, v141
v_mfma_f32_16x16x32_bf16 a[124:127], a[52:55], v[236:239], a[124:127]
ds_read_b128 a[48:51], v63 offset:4096
ds_read_b128 a[52:55], v63 offset:5120
v_mfma_f32_16x16x32_bf16 a[144:147], a[56:59], v[232:235], a[144:147]
v_exp_f32 v143, v143
v_mfma_f32_16x16x32_bf16 a[152:155], a[56:59], v[236:239], a[152:155]
s_waitcnt lgkmcnt(6)
ds_read_b128 a[56:59], v63 offset:6144
v_mfma_f32_16x16x32_bf16 a[148:151], a[60:63], v[232:235], a[148:151]
v_cvt_pk_bf16_f32 v231, v142, v143
v_mfma_f32_16x16x32_bf16 a[156:159], a[60:63], v[236:239], a[156:159]
ds_read_b128 a[60:63], v63 offset:7168
s_cbranch_scc1 .Lev_s1
.LS0:
v_mfma_f32_16x16x32_bf16 v[128:131], a[0:3], v[64:67], v[192:195]
s_cmp_lt_u32 s44, s45
v_exp_f32 v144, v144
v_mfma_f32_16x16x32_bf16 v[136:139], a[0:3], v[80:83], v[196:199]
s_cselect_b32 m0, s46, s27
v_exp_f32 v145, v145
v_mfma_f32_16x16x32_bf16 v[132:135], a[4:7], v[64:67], v[192:195]
v_exp_f32 v146, v146
v_mfma_f32_16x16x32_bf16 v[140:143], a[4:7], v[80:83], v[196:199]
ds_read_b128 a[0:3], v62 offset:8192
ds_read_b128 a[4:7], v62 offset:12288
v_mfma_f32_16x16x32_bf16 v[128:131], a[8:11], v[68:71], v[128:131]
buffer_load_dwordx4 v7, s[8:11], s44 offen offset:0 sc1 lds
v_mfma_f32_16x16x32_bf16 v[136:139], a[8:11], v[84:87], v[136:139]
ds_read_b128 a[8:11], v62 offset:9216
v_cvt_pk_bf16_f32 v232, v144, v145
v_mfma_f32_16x16x32_bf16 v[132:135], a[12:15], v[68:71], v[132:135]
s_xor_b32 s50, s46, 0x18000
v_exp_f32 v147, v147
v_mfma_f32_16x16x32_bf16 v[140:143], a[12:15], v[84:87], v[140:143]
ds_read_b128 a[12:15], v62 offset:13312
v_exp_f32 v148, v148
v_mfma_f32_16x16x32_bf16 v[128:131], a[16:19], v[72:75], v[128:131]
v_cvt_pk_bf16_f32 v233, v146, v147
v_mfma_f32_16x16x32_bf16 v[136:139], a[16:19], v[88:91], v[136:139]
v_exp_f32 v149, v149
v_mfma_f32_16x16x32_bf16 v[132:135], a[20:23], v[72:75], v[132:135]
buffer_load_dwordx4 v7, s[8:11], s44 offen offset:1024 sc1 lds
v_mfma_f32_16x16x32_bf16 v[140:143], a[20:23], v[88:91], v[140:143]
ds_read_b128 a[16:19], v62 offset:10240
ds_read_b128 a[20:23], v62 offset:14336
v_mfma_f32_16x16x32_bf16 v[128:131], a[24:27], v[76:79], v[128:131]
v_exp_f32 v150, v150
v_mfma_f32_16x16x32_bf16 v[136:139], a[24:27], v[92:95], v[136:139]
ds_read_b128 a[24:27], v62 offset:11264
v_cvt_pk_bf16_f32 v234, v148, v149
v_mfma_f32_16x16x32_bf16 v[132:135], a[28:31], v[76:79], v[132:135]
v_exp_f32 v151, v151
v_mfma_f32_16x16x32_bf16 v[140:143], a[28:31], v[92:95], v[140:143]
ds_read_b128 a[28:31], v62 offset:15360
v_exp_f32 v152, v152
v_mfma_f32_16x16x32_bf16 v[208:211], v[52:55], v[224:227], v[208:211]
v_add_u32 v62, s46, v32
v_cvt_pk_bf16_f32 v235, v150, v151
v_mfma_f32_16x16x32_bf16 v[212:215], v[52:55], v[228:231], v[212:215]
s_waitcnt lgkmcnt(8)
v_exp_f32 v153, v153
v_mfma_f32_16x16x32_bf16 a[64:67], a[32:35], v[224:227], a[64:67]
buffer_load_dwordx4 v7, s[8:11], s44 offen offset:2048 sc1 lds
v_mfma_f32_16x16x32_bf16 a[72:75], a[32:35], v[228:231], a[72:75]
v_exp_f32 v154, v154
v_mfma_f32_16x16x32_bf16 a[68:71], a[36:39], v[224:227], a[68:71]
v_cvt_pk_bf16_f32 v236, v152, v153
v_mfma_f32_16x16x32_bf16 a[76:79], a[36:39], v[228:231], a[76:79]
ds_read_b128 a[32:35], v63 offset:8192
ds_read_b128 a[36:39], v63 offset:9216
v_mfma_f32_16x16x32_bf16 a[80:83], a[40:43], v[224:227], a[80:83]
v_exp_f32 v155, v155
v_mfma_f32_16x16x32_bf16 a[88:91], a[40:43], v[228:231], a[88:91]
ds_read_b128 a[40:43], v63 offset:10240
v_exp_f32 v156, v156
v_mfma_f32_16x16x32_bf16 a[84:87], a[44:47], v[224:227], a[84:87]
buffer_load_dwordx4 v7, s[8:11], s44 offen offset:3072 sc1 lds
v_mfma_f32_16x16x32_bf16 a[92:95], a[44:47], v[228:231], a[92:95]
ds_read_b128 a[44:47], v63 offset:11264
v_cvt_pk_bf16_f32 v237, v154, v155
v_mfma_f32_16x16x32_bf16 a[112:115], a[48:51], v[224:227], a[112:115]
s_add_u32 s44, s44, 16384
v_exp_f32 v157, v157
v_mfma_f32_16x16x32_bf16 a[120:123], a[48:51], v[228:231], a[120:123]
v_exp_f32 v158, v158
v_mfma_f32_16x16x32_bf16 a[116:119], a[52:55], v[224:227], a[116:119]
s_sub_u32 s55, s55, 1
v_cvt_pk_bf16_f32 v238, v156, v157
v_mfma_f32_16x16x32_bf16 a[124:127], a[52:55], v[228:231], a[124:127]
ds_read_b128 a[48:51], v63 offset:12288
ds_read_b128 a[52:55], v63 offset:13312
v_mfma_f32_16x16x32_bf16 a[144:147], a[56:59], v[224:227], a[144:147]
v_exp_f32 v159, v159
v_mfma_f32_16x16x32_bf16 a[152:155], a[56:59], v[228:231], a[152:155]
s_waitcnt lgkmcnt(6)
ds_read_b128 a[56:59], v63 offset:14336
v_mfma_f32_16x16x32_bf16 a[148:151], a[60:63], v[224:227], a[148:151]
s_waitcnt vmcnt(8)
s_barrier
v_cvt_pk_bf16_f32 v239, v158, v159
v_mfma_f32_16x16x32_bf16 a[156:159], a[60:63], v[228:231], a[156:159]
ds_read_b128 a[60:63], v63 offset:15360
s_cbranch_scc0 .LS1
s_cmp_eq_u32 s43, s42
s_cbranch_scc1 .LE1
s_swappc_b64 s[30:31], s[58:59]
.Lev_s0_r:
s_branch .LS1
.LE0:
v_mfma_f32_16x16x32_bf16 v[128:131], a[0:3], v[64:67], v[16:19]
s_cmp_lt_u32 s44, s45
v_exp_f32 v144, v144
v_mfma_f32_16x16x32_bf16 v[136:139], a[0:3], v[80:83], v[24:27]
s_cselect_b32 m0, s46, s27
v_exp_f32 v145, v145
v_mfma_f32_16x16x32_bf16 v[132:135], a[4:7], v[64:67], v[20:23]
buffer_load_dwordx4 v7, s[8:11], s44 offen offset:0 sc1 lds
v_mfma_f32_16x16x32_bf16 v[140:143], a[4:7], v[80:83], v[28:31]
buffer_load_dwordx4 v7, s[8:11], s44 offen offset:1024 sc1 lds
v_exp_f32 v146, v146
v_mfma_f32_16x16x32_bf16 v[128:131], a[8:11], v[68:71], v[128:131]
buffer_load_dwordx4 v7, s[8:11], s44 offen offset:2048 sc1 lds
v_cvt_pk_bf16_f32 v232, v144, v145
v_mfma_f32_16x16x32_bf16 v[136:139], a[8:11], v[84:87], v[136:139]
buffer_load_dwordx4 v7, s[8:11], s44 offen offset:3072 sc1 lds
v_exp_f32 v147, v147
v_mfma_f32_16x16x32_bf16 v[132:135], a[12:15], v[68:71], v[132:135]
s_xor_b32 s50, s46, 0x18000
buffer_load_dword v35, v4, s[96:99], 0 offen
v_mfma_f32_16x16x32_bf16 v[140:143], a[12:15], v[84:87], v[140:143]
s_add_u32 m0, s48, 131072
v_exp_f32 v148, v148
v_mfma_f32_16x16x32_bf16 v[128:131], a[16:19], v[72:75], v[128:131]
buffer_load_dwordx4 v40, s[16:19], s26 offen offset:0 lds
v_cvt_pk_bf16_f32 v233, v146, v147
v_mfma_f32_16x16x32_bf16 v[136:139], a[16:19], v[88:91], v[136:139]
v_exp_f32 v149, v149
v_mfma_f32_16x16x32_bf16 v[132:135], a[20:23], v[72:75], v[132:135]
buffer_load_dwordx4 v41, s[16:19], s26 offen offset:1024 lds
v_mfma_f32_16x16x32_bf16 v[140:143], a[20:23], v[88:91], v[140:143]
v_exp_f32 v150, v150
v_mfma_f32_16x16x32_bf16 v[128:131], a[24:27], v[76:79], v[128:131]
buffer_load_dwordx4 v40, s[16:19], s26 offen offset:2048 lds
v_mfma_f32_16x16x32_bf16 v[136:139], a[24:27], v[92:95], v[136:139]
v_cvt_pk_bf16_f32 v234, v148, v149
v_exp_f32 v151, v151
v_mfma_f32_16x16x32_bf16 v[132:135], a[28:31], v[76:79], v[132:135]
buffer_load_dwordx4 v41, s[16:19], s26 offen offset:3072 lds
v_mfma_f32_16x16x32_bf16 v[140:143], a[28:31], v[92:95], v[140:143]
s_add_u32 m0, s48, 135168
v_exp_f32 v152, v152
v_mfma_f32_16x16x32_bf16 v[208:211], v[52:55], v[224:227], v[208:211]
buffer_load_dwordx4 v40, s[16:19], s47 offen offset:0 lds
v_mfma_f32_16x16x32_bf16 v[212:215], v[52:55], v[228:231], v[212:215]
s_waitcnt lgkmcnt(0)
v_cvt_pk_bf16_f32 v235, v150, v151
v_exp_f32 v153, v153
v_mfma_f32_16x16x32_bf16 a[64:67], a[32:35], v[224:227], a[64:67]
buffer_load_dwordx4 v41, s[16:19], s47 offen offset:1024 lds
v_mfma_f32_16x16x32_bf16 a[72:75], a[32:35], v[228:231], a[72:75]
v_exp_f32 v154, v154
v_mfma_f32_16x16x32_bf16 a[68:71], a[36:39], v[224:227], a[68:71]
buffer_load_dwordx4 v42, s[12:15], s25 offen offset:2048 lds
v_mfma_f32_16x16x32_bf16 a[76:79], a[36:39], v[228:231], a[76:79]
ds_read_b128 a[32:35], v63 offset:8192
ds_read_b128 a[36:39], v63 offset:9216
v_cvt_pk_bf16_f32 v236, v152, v153
v_mfma_f32_16x16x32_bf16 a[80:83], a[40:43], v[224:227], a[80:83]
s_add_u32 m0, s91, 65536
v_exp_f32 v155, v155
v_mfma_f32_16x16x32_bf16 a[88:91], a[40:43], v[228:231], a[88:91]
buffer_load_dwordx4 v36, s[12:15], s24 offen offset:0 lds
ds_read_b128 a[40:43], v63 offset:10240
v_mfma_f32_16x16x32_bf16 a[84:87], a[44:47], v[224:227], a[84:87]
buffer_load_dwordx4 v37, s[12:15], s24 offen offset:1024 lds
v_exp_f32 v156, v156
v_mfma_f32_16x16x32_bf16 a[92:95], a[44:47], v[228:231], a[92:95]
ds_read_b128 a[44:47], v63 offset:11264
v_cvt_pk_bf16_f32 v237, v154, v155
v_mfma_f32_16x16x32_bf16 a[112:115], a[48:51], v[224:227], a[112:115]
s_add_u32 s44, s44, 16384
v_exp_f32 v157, v157
v_mfma_f32_16x16x32_bf16 a[120:123], a[48:51], v[228:231], a[120:123]
buffer_load_dwordx4 v38, s[12:15], s24 offen offset:2048 lds
v_mfma_f32_16x16x32_bf16 a[116:119], a[52:55], v[224:227], a[116:119]
buffer_load_dwordx4 v39, s[12:15], s24 offen offset:3072 lds
v_exp_f32 v158, v158
v_mfma_f32_16x16x32_bf16 a[124:127], a[52:55], v[228:231], a[124:127]
ds_read_b128 a[48:51], v63 offset:12288
ds_read_b128 a[52:55], v63 offset:13312
v_cvt_pk_bf16_f32 v238, v156, v157
v_mfma_f32_16x16x32_bf16 a[144:147], a[56:59], v[224:227], a[144:147]
s_add_u32 s26, s26, 393216
s_add_u32 s47, s47, 393216
s_add_u32 s25, s25, 128
s_add_u32 s24, s24, 128
v_mfma_f32_16x16x32_bf16 a[152:155], a[56:59], v[228:231], a[152:155]
ds_read_b128 a[56:59], v63 offset:14336
v_exp_f32 v159, v159
v_mfma_f32_16x16x32_bf16 a[148:151], a[60:63], v[224:227], a[148:151]
s_waitcnt vmcnt(20)
s_barrier
v_cvt_pk_bf16_f32 v239, v158, v159
v_mfma_f32_16x16x32_bf16 a[156:159], a[60:63], v[228:231], a[156:159]
ds_read_b128 a[60:63], v63 offset:15360
v_add_u32 v63, s49, v33
s_waitcnt lgkmcnt(4)
v_mfma_f32_16x16x32_bf16 v[208:211], v[52:55], v[232:235], v[208:211]
v_exp_f32 v128, v128
v_mfma_f32_16x16x32_bf16 v[212:215], v[52:55], v[236:239], v[212:215]
s_add_u32 m0, s91, 81920
v_exp_f32 v129, v129
v_exp_f32 v130, v130
v_mfma_f32_16x16x32_bf16 a[64:67], a[32:35], v[232:235], a[64:67]
buffer_load_dwordx4 v36, s[12:15], s24 offen offset:0 lds
v_cvt_pk_bf16_f32 v224, v128, v129
v_mfma_f32_16x16x32_bf16 a[72:75], a[32:35], v[236:239], a[72:75]
v_exp_f32 v131, v131
v_exp_f32 v132, v132
v_mfma_f32_16x16x32_bf16 a[68:71], a[36:39], v[232:235], a[68:71]
buffer_load_dwordx4 v37, s[12:15], s24 offen offset:1024 lds
v_cvt_pk_bf16_f32 v225, v130, v131
v_mfma_f32_16x16x32_bf16 a[76:79], a[36:39], v[236:239], a[76:79]
ds_read_b128 a[32:35], v63 offset:0
ds_read_b128 a[36:39], v63 offset:1024
v_exp_f32 v133, v133
v_mfma_f32_16x16x32_bf16 a[80:83], a[40:43], v[232:235], a[80:83]
v_exp_f32 v134, v134
v_cvt_pk_bf16_f32 v226, v132, v133
v_mfma_f32_16x16x32_bf16 a[88:91], a[40:43], v[236:239], a[88:91]
ds_read_b128 a[40:43], v63 offset:2048
v_exp_f32 v135, v135
v_mfma_f32_16x16x32_bf16 a[84:87], a[44:47], v[232:235], a[84:87]
buffer_load_dwordx4 v38, s[12:15], s24 offen offset:2048 lds
v_exp_f32 v136, v136
v_mfma_f32_16x16x32_bf16 a[92:95], a[44:47], v[236:239], a[92:95]
s_waitcnt lgkmcnt(3)
ds_read_b128 a[44:47], v63 offset:3072
v_cvt_pk_bf16_f32 v227, v134, v135
v_mfma_f32_16x16x32_bf16 a[112:115], a[48:51], v[232:235], a[112:115]
v_exp_f32 v137, v137
v_exp_f32 v138, v138
v_mfma_f32_16x16x32_bf16 a[120:123], a[48:51], v[236:239], a[120:123]
buffer_load_dwordx4 v39, s[12:15], s24 offen offset:3072 lds
v_cvt_pk_bf16_f32 v228, v136, v137
v_mfma_f32_16x16x32_bf16 a[116:119], a[52:55], v[232:235], a[116:119]
s_add_u32 s24, s24, 128
v_exp_f32 v139, v139
v_mfma_f32_16x16x32_bf16 a[124:127], a[52:55], v[236:239], a[124:127]
ds_read_b128 a[48:51], v63 offset:4096
ds_read_b128 a[52:55], v63 offset:5120
v_exp_f32 v140, v140
v_mfma_f32_16x16x32_bf16 a[144:147], a[56:59], v[232:235], a[144:147]
v_cvt_pk_bf16_f32 v229, v138, v139
v_exp_f32 v141, v141
v_mfma_f32_16x16x32_bf16 a[152:155], a[56:59], v[236:239], a[152:155]
ds_read_b128 a[56:59], v63 offset:6144
v_exp_f32 v142, v142
v_mfma_f32_16x16x32_bf16 a[148:151], a[60:63], v[232:235], a[148:151]
v_cvt_pk_bf16_f32 v230, v140, v141
v_exp_f32 v143, v143
v_mfma_f32_16x16x32_bf16 a[156:159], a[60:63], v[236:239], a[156:159]
ds_read_b128 a[60:63], v63 offset:7168
v_cvt_pk_bf16_f32 v231, v142, v143
s_waitcnt lgkmcnt(4)
v_mfma_f32_16x16x32_bf16 v[208:211], v[52:55], v[224:227], v[208:211]
s_mov_b32 s28, 1602224128
v_mfma_f32_16x16x32_bf16 v[212:215], v[52:55], v[228:231], v[212:215]
v_mfma_f32_16x16x32_bf16 a[64:67], a[32:35], v[224:227], a[64:67]
v_mfma_f32_16x16x32_bf16 a[72:75], a[32:35], v[228:231], a[72:75]
v_mfma_f32_16x16x32_bf16 a[68:71], a[36:39], v[224:227], a[68:71]
.Lpad13:
s_nop 0
v_mfma_f32_16x16x32_bf16 a[76:79], a[36:39], v[228:231], a[76:79]
v_mfma_f32_16x16x32_bf16 a[80:83], a[40:43], v[224:227], a[80:83]
v_mfma_f32_16x16x32_bf16 a[88:91], a[40:43], v[228:231], a[88:91]
v_mfma_f32_16x16x32_bf16 a[84:87], a[44:47], v[224:227], a[84:87]
v_mfma_f32_16x16x32_bf16 a[92:95], a[44:47], v[228:231], a[92:95]
s_waitcnt lgkmcnt(0)
v_mfma_f32_16x16x32_bf16 a[112:115], a[48:51], v[224:227], a[112:115]
v_add_f32 v13, v208, v212
v_mfma_f32_16x16x32_bf16 a[120:123], a[48:51], v[228:231], a[120:123]
v_cmp_nge_f32 s[30:31], s28, v13
v_mfma_f32_16x16x32_bf16 a[116:119], a[52:55], v[224:227], a[116:119]
v_mfma_f32_16x16x32_bf16 a[124:127], a[52:55], v[228:231], a[124:127]
v_mfma_f32_16x16x32_bf16 a[144:147], a[56:59], v[224:227], a[144:147]
v_mfma_f32_16x16x32_bf16 a[152:155], a[56:59], v[228:231], a[152:155]
v_mfma_f32_16x16x32_bf16 a[148:151], a[60:63], v[224:227], a[148:151]
v_mfma_f32_16x16x32_bf16 a[156:159], a[60:63], v[228:231], a[156:159]
s_cmp_lt_u32 s3, 2
s_cbranch_scc0 .LEb0
s_waitcnt vmcnt(16)
s_barrier
.LEb0:
s_branch .LEend
.LE1:
v_mfma_f32_16x16x32_bf16 v[144:147], a[0:3], v[64:67], v[16:19]
buffer_load_dword v35, v4, s[96:99], 0 offen
v_mfma_f32_16x16x32_bf16 v[152:155], a[0:3], v[80:83], v[24:27]
s_add_u32 m0, s48, 131072
v_exp_f32 v128, v128
v_mfma_f32_16x16x32_bf16 v[148:151], a[4:7], v[64:67], v[20:23]
buffer_load_dwordx4 v40, s[16:19], s26 offen offset:0 lds
v_add_u32 v63, s49, v33
v_mfma_f32_16x16x32_bf16 v[156:159], a[4:7], v[80:83], v[28:31]
v_exp_f32 v129, v129
v_mfma_f32_16x16x32_bf16 v[144:147], a[8:11], v[68:71], v[144:147]
buffer_load_dwordx4 v41, s[16:19], s26 offen offset:1024 lds
v_mfma_f32_16x16x32_bf16 v[152:155], a[8:11], v[84:87], v[152:155]
v_exp_f32 v130, v130
v_cvt_pk_bf16_f32 v224, v128, v129
v_mfma_f32_16x16x32_bf16 v[148:151], a[12:15], v[68:71], v[148:151]
buffer_load_dwordx4 v40, s[16:19], s26 offen offset:2048 lds
v_mfma_f32_16x16x32_bf16 v[156:159], a[12:15], v[84:87], v[156:159]
v_exp_f32 v131, v131
v_mfma_f32_16x16x32_bf16 v[144:147], a[16:19], v[72:75], v[144:147]
buffer_load_dwordx4 v41, s[16:19], s26 offen offset:3072 lds
v_mfma_f32_16x16x32_bf16 v[152:155], a[16:19], v[88:91], v[152:155]
v_exp_f32 v132, v132
v_cvt_pk_bf16_f32 v225, v130, v131
v_mfma_f32_16x16x32_bf16 v[148:151], a[20:23], v[72:75], v[148:151]
s_add_u32 m0, s48, 135168
v_exp_f32 v133, v133
v_mfma_f32_16x16x32_bf16 v[156:159], a[20:23], v[88:91], v[156:159]
buffer_load_dwordx4 v40, s[16:19], s47 offen offset:0 lds
v_mfma_f32_16x16x32_bf16 v[144:147], a[24:27], v[76:79], v[144:147]
v_exp_f32 v134, v134
v_mfma_f32_16x16x32_bf16 v[152:155], a[24:27], v[92:95], v[152:155]
buffer_load_dwordx4 v41, s[16:19], s47 offen offset:1024 lds
v_cvt_pk_bf16_f32 v226, v132, v133
v_mfma_f32_16x16x32_bf16 v[148:151], a[28:31], v[76:79], v[148:151]
v_exp_f32 v135, v135
v_mfma_f32_16x16x32_bf16 v[156:159], a[28:31], v[92:95], v[156:159]
buffer_load_dwordx4 v42, s[12:15], s25 offen offset:2048 lds
v_mfma_f32_16x16x32_bf16 v[208:211], v[52:55], v[232:235], v[208:211]
v_exp_f32 v136, v136
v_mfma_f32_16x16x32_bf16 v[212:215], v[52:55], v[236:239], v[212:215]
s_waitcnt lgkmcnt(0)
s_add_u32 m0, s91, 65536
v_cvt_pk_bf16_f32 v227, v134, v135
v_mfma_f32_16x16x32_bf16 a[64:67], a[32:35], v[232:235], a[64:67]
buffer_load_dwordx4 v36, s[12:15], s24 offen offset:0 lds
v_mfma_f32_16x16x32_bf16 a[72:75], a[32:35], v[236:239], a[72:75]
v_exp_f32 v137, v137
v_mfma_f32_16x16x32_bf16 a[68:71], a[36:39], v[232:235], a[68:71]
buffer_load_dwordx4 v37, s[12:15], s24 offen offset:1024 lds
v_mfma_f32_16x16x32_bf16 a[76:79], a[36:39], v[236:239], a[76:79]
ds_read_b128 a[32:35], v63 offset:0
ds_read_b128 a[36:39], v63 offset:1024
v_mfma_f32_16x16x32_bf16 a[80:83], a[40:43], v[232:235], a[80:83]
v_exp_f32 v138, v138
v_cvt_pk_bf16_f32 v228, v136, v137
v_mfma_f32_16x16x32_bf16 a[88:91], a[40:43], v[236:239], a[88:91]
buffer_load_dwordx4 v38, s[12:15], s24 offen offset:2048 lds
ds_read_b128 a[40:43], v63 offset:2048
v_mfma_f32_16x16x32_bf16 a[84:87], a[44:47], v[232:235], a[84:87]
buffer_load_dwordx4 v39, s[12:15], s24 offen offset:3072 lds
v_mfma_f32_16x16x32_bf16 a[92:95], a[44:47], v[236:239], a[92:95]
ds_read_b128 a[44:47], v63 offset:3072
v_exp_f32 v139, v139
v_mfma_f32_16x16x32_bf16 a[112:115], a[48:51], v[232:235], a[112:115]
v_exp_f32 v140, v140
v_cvt_pk_bf16_f32 v229, v138, v139
v_mfma_f32_16x16x32_bf16 a[120:123], a[48:51], v[236:239], a[120:123]
v_exp_f32 v141, v141
v_mfma_f32_16x16x32_bf16 a[116:119], a[52:55], v[232:235], a[116:119]
v_exp_f32 v142, v142
v_mfma_f32_16x16x32_bf16 a[124:127], a[52:55], v[236:239], a[124:127]
ds_read_b128 a[48:51], v63 offset:4096
ds_read_b128 a[52:55], v63 offset:5120
v_mfma_f32_16x16x32_bf16 a[144:147], a[56:59], v[232:235], a[144:147]
s_add_u32 s26, s26, 393216
s_add_u32 s47, s47, 393216
s_add_u32 s25, s25, 128
s_add_u32 s24, s24, 128
v_mfma_f32_16x16x32_bf16 a[152:155], a[56:59], v[236:239], a[152:155]
ds_read_b128 a[56:59], v63 offset:6144
v_cvt_pk_bf16_f32 v230, v140, v141
v_mfma_f32_16x16x32_bf16 a[148:151], a[60:63], v[232:235], a[148:151]
v_exp_f32 v143, v143
s_nop 0
v_cvt_pk_bf16_f32 v231, v142, v143
v_mfma_f32_16x16x32_bf16 a[156:159], a[60:63], v[236:239], a[156:159]
ds_read_b128 a[60:63], v63 offset:7168
s_waitcnt lgkmcnt(4)
v_mfma_f32_16x16x32_bf16 v[208:211], v[52:55], v[224:227], v[208:211]
v_exp_f32 v144, v144
v_mfma_f32_16x16x32_bf16 v[212:215], v[52:55], v[228:231], v[212:215]
s_add_u32 m0, s91, 81920
v_exp_f32 v145, v145
v_exp_f32 v146, v146
v_mfma_f32_16x16x32_bf16 a[64:67], a[32:35], v[224:227], a[64:67]
buffer_load_dwordx4 v36, s[12:15], s24 offen offset:0 lds
v_cvt_pk_bf16_f32 v232, v144, v145
v_mfma_f32_16x16x32_bf16 a[72:75], a[32:35], v[228:231], a[72:75]
v_exp_f32 v147, v147
v_exp_f32 v148, v148
v_mfma_f32_16x16x32_bf16 a[68:71], a[36:39], v[224:227], a[68:71]
buffer_load_dwordx4 v37, s[12:15], s24 offen offset:1024 lds
v_cvt_pk_bf16_f32 v233, v146, v147
v_mfma_f32_16x16x32_bf16 a[76:79], a[36:39], v[228:231], a[76:79]
ds_read_b128 a[32:35], v63 offset:8192
ds_read_b128 a[36:39], v63 offset:9216
v_exp_f32 v149, v149
v_mfma_f32_16x16x32_bf16 a[80:83], a[40:43], v[224:227], a[80:83]
v_exp_f32 v150, v150
v_cvt_pk_bf16_f32 v234, v148, v149
v_mfma_f32_16x16x32_bf16 a[88:91], a[40:43], v[228:231], a[88:91]
ds_read_b128 a[40:43], v63 offset:10240
v_exp_f32 v151, v151
v_mfma_f32_16x16x32_bf16 a[84:87], a[44:47], v[224:227], a[84:87]
buffer_load_dwordx4 v38, s[12:15], s24 offen offset:2048 lds
v_exp_f32 v152, v152
v_mfma_f32_16x16x32_bf16 a[92:95], a[44:47], v[228:231], a[92:95]
s_waitcnt lgkmcnt(3)
ds_read_b128 a[44:47], v63 offset:11264
v_cvt_pk_bf16_f32 v235, v150, v151
v_mfma_f32_16x16x32_bf16 a[112:115], a[48:51], v[224:227], a[112:115]
v_exp_f32 v153, v153
v_exp_f32 v154, v154
v_mfma_f32_16x16x32_bf16 a[120:123], a[48:51], v[228:231], a[120:123]
buffer_load_dwordx4 v39, s[12:15], s24 offen offset:3072 lds
v_cvt_pk_bf16_f32 v236, v152, v153
v_mfma_f32_16x16x32_bf16 a[116:119], a[52:55], v[224:227], a[116:119]
s_add_u32 s24, s24, 128
v_exp_f32 v155, v155
v_mfma_f32_16x16x32_bf16 a[124:127], a[52:55], v[228:231], a[124:127]
ds_read_b128 a[48:51], v63 offset:12288
ds_read_b128 a[52:55], v63 offset:13312
v_exp_f32 v156, v156
v_mfma_f32_16x16x32_bf16 a[144:147], a[56:59], v[224:227], a[144:147]
v_cvt_pk_bf16_f32 v237, v154, v155
v_exp_f32 v157, v157
v_mfma_f32_16x16x32_bf16 a[152:155], a[56:59], v[228:231], a[152:155]
ds_read_b128 a[56:59], v63 offset:14336
v_exp_f32 v158, v158
v_mfma_f32_16x16x32_bf16 a[148:151], a[60:63], v[224:227], a[148:151]
v_cvt_pk_bf16_f32 v238, v156, v157
v_exp_f32 v159, v159
v_mfma_f32_16x16x32_bf16 a[156:159], a[60:63], v[228:231], a[156:159]
ds_read_b128 a[60:63], v63 offset:15360
v_add_u32 v63, s49, v33
v_cvt_pk_bf16_f32 v239, v158, v159
s_cmp_lt_u32 s3, 2
s_cbranch_scc0 .LEb1
s_waitcnt vmcnt(16)
s_barrier
.LEb1:
s_waitcnt lgkmcnt(4)
v_mfma_f32_16x16x32_bf16 v[208:211], v[52:55], v[232:235], v[208:211]
s_mov_b32 s28, 1602224128
v_mfma_f32_16x16x32_bf16 v[212:215], v[52:55], v[236:239], v[212:215]
v_mfma_f32_16x16x32_bf16 a[64:67], a[32:35], v[232:235], a[64:67]
v_mfma_f32_16x16x32_bf16 a[72:75], a[32:35], v[236:239], a[72:75]
v_mfma_f32_16x16x32_bf16 a[68:71], a[36:39], v[232:235], a[68:71]
v_mfma_f32_16x16x32_bf16 a[76:79], a[36:39], v[236:239], a[76:79]
v_mfma_f32_16x16x32_bf16 a[80:83], a[40:43], v[232:235], a[80:83]
v_mfma_f32_16x16x32_bf16 a[88:91], a[40:43], v[236:239], a[88:91]
v_mfma_f32_16x16x32_bf16 a[84:87], a[44:47], v[232:235], a[84:87]
v_mfma_f32_16x16x32_bf16 a[92:95], a[44:47], v[236:239], a[92:95]
s_waitcnt lgkmcnt(0)
v_mfma_f32_16x16x32_bf16 a[112:115], a[48:51], v[232:235], a[112:115]
v_add_f32 v13, v208, v212
v_mfma_f32_16x16x32_bf16 a[120:123], a[48:51], v[236:239], a[120:123]
v_cmp_nge_f32 s[30:31], s28, v13
v_mfma_f32_16x16x32_bf16 a[116:119], a[52:55], v[232:235], a[116:119]
v_mfma_f32_16x16x32_bf16 a[124:127], a[52:55], v[236:239], a[124:127]
v_mfma_f32_16x16x32_bf16 a[144:147], a[56:59], v[232:235], a[144:147]
v_mfma_f32_16x16x32_bf16 a[152:155], a[56:59], v[236:239], a[152:155]
v_mfma_f32_16x16x32_bf16 a[148:151], a[60:63], v[232:235], a[148:151]
v_mfma_f32_16x16x32_bf16 a[156:159], a[60:63], v[236:239], a[156:159]
.LEend:
s_or_b64 s[34:35], s[30:31], s[52:53]
s_cbranch_scc1 .Lx
.Lx_ret:
v_rcp_f32 v160, v208
v_rcp_f32 v162, v212
v_mov_b32 v161, v160
v_mov_b32 v163, v162
v_accvgpr_read_b32 v164, a64
v_accvgpr_read_b32 v165, a65
v_accvgpr_read_b32 v166, a66
v_accvgpr_read_b32 v167, a67
v_accvgpr_read_b32 v168, a68
v_accvgpr_read_b32 v169, a69
v_accvgpr_read_b32 v170, a70
v_accvgpr_read_b32 v171, a71
v_pk_mul_f32 v[164:165], v[164:165], v[160:161]
v_pk_mul_f32 v[166:167], v[166:167], v[160:161]
v_pk_mul_f32 v[168:169], v[168:169], v[160:161]
v_pk_mul_f32 v[170:171], v[170:171], v[160:161]
v_cvt_pk_bf16_f32 v172, v164, v165
v_cvt_pk_bf16_f32 v173, v166, v167
v_cvt_pk_bf16_f32 v174, v168, v169
v_cvt_pk_bf16_f32 v175, v170, v171
s_add_u32 m0, s48, 98304
s_nop 0
buffer_load_dwordx4 v40, s[16:19], s26 offen offset:0 lds
buffer_load_dwordx4 v41, s[16:19], s26 offen offset:1024 lds
buffer_store_dwordx4 v[172:175], v7, s[20:23], s40 offen offset:0 sc1
v_accvgpr_read_b32 v176, a72
v_accvgpr_read_b32 v177, a73
v_accvgpr_read_b32 v178, a74
v_accvgpr_read_b32 v179, a75
v_accvgpr_read_b32 v180, a76
v_accvgpr_read_b32 v181, a77
v_accvgpr_read_b32 v182, a78
v_accvgpr_read_b32 v183, a79
v_pk_mul_f32 v[176:177], v[176:177], v[162:163]
v_pk_mul_f32 v[178:179], v[178:179], v[162:163]
v_pk_mul_f32 v[180:181], v[180:181], v[162:163]
v_pk_mul_f32 v[182:183], v[182:183], v[162:163]
v_cvt_pk_bf16_f32 v184, v176, v177
v_cvt_pk_bf16_f32 v185, v178, v179
v_cvt_pk_bf16_f32 v186, v180, v181
v_cvt_pk_bf16_f32 v187, v182, v183
buffer_load_dwordx4 v40, s[16:19], s26 offen offset:2048 lds
buffer_load_dwordx4 v41, s[16:19], s26 offen offset:3072 lds
buffer_store_dwordx4 v[184:187], v7, s[20:23], s40 offen offset:1024 sc1
v_accvgpr_read_b32 v164, a80
v_accvgpr_read_b32 v165, a81
v_accvgpr_read_b32 v166, a82
v_accvgpr_read_b32 v167, a83
v_accvgpr_read_b32 v168, a84
v_accvgpr_read_b32 v169, a85
v_accvgpr_read_b32 v170, a86
v_accvgpr_read_b32 v171, a87
v_pk_mul_f32 v[164:165], v[164:165], v[160:161]
v_pk_mul_f32 v[166:167], v[166:167], v[160:161]
v_pk_mul_f32 v[168:169], v[168:169], v[160:161]
v_pk_mul_f32 v[170:171], v[170:171], v[160:161]
v_cvt_pk_bf16_f32 v172, v164, v165
v_cvt_pk_bf16_f32 v173, v166, v167
v_cvt_pk_bf16_f32 v174, v168, v169
v_cvt_pk_bf16_f32 v175, v170, v171
s_add_u32 m0, s48, 102400
s_nop 0
buffer_load_dwordx4 v40, s[16:19], s47 offen offset:0 lds
buffer_load_dwordx4 v41, s[16:19], s47 offen offset:1024 lds
buffer_store_dwordx4 v[172:175], v7, s[20:23], s40 offen offset:2048 sc1
v_accvgpr_read_b32 v176, a88
v_accvgpr_read_b32 v177, a89
v_accvgpr_read_b32 v178, a90
v_accvgpr_read_b32 v179, a91
v_accvgpr_read_b32 v180, a92
v_accvgpr_read_b32 v181, a93
v_accvgpr_read_b32 v182, a94
v_accvgpr_read_b32 v183, a95
v_pk_mul_f32 v[176:177], v[176:177], v[162:163]
v_pk_mul_f32 v[178:179], v[178:179], v[162:163]
v_pk_mul_f32 v[180:181], v[180:181], v[162:163]
v_pk_mul_f32 v[182:183], v[182:183], v[162:163]
v_cvt_pk_bf16_f32 v184, v176, v177
v_cvt_pk_bf16_f32 v185, v178, v179
v_cvt_pk_bf16_f32 v186, v180, v181
v_cvt_pk_bf16_f32 v187, v182, v183
buffer_load_dwordx4 v42, s[12:15], s25 offen offset:2048 lds
s_add_u32 m0, s91, 0
s_nop 0
buffer_load_dwordx4 v36, s[12:15], s24 offen offset:0 lds
buffer_store_dwordx4 v[184:187], v7, s[20:23], s40 offen offset:3072 sc1
v_accvgpr_read_b32 v164, a112
v_accvgpr_read_b32 v165, a113
v_accvgpr_read_b32 v166, a114
v_accvgpr_read_b32 v167, a115
v_accvgpr_read_b32 v168, a116
v_accvgpr_read_b32 v169, a117
v_accvgpr_read_b32 v170, a118
v_accvgpr_read_b32 v171, a119
v_pk_mul_f32 v[164:165], v[164:165], v[160:161]
v_pk_mul_f32 v[166:167], v[166:167], v[160:161]
v_pk_mul_f32 v[168:169], v[168:169], v[160:161]
v_pk_mul_f32 v[170:171], v[170:171], v[160:161]
v_cvt_pk_bf16_f32 v172, v164, v165
v_cvt_pk_bf16_f32 v173, v166, v167
v_cvt_pk_bf16_f32 v174, v168, v169
v_cvt_pk_bf16_f32 v175, v170, v171
buffer_load_dwordx4 v37, s[12:15], s24 offen offset:1024 lds
buffer_load_dwordx4 v38, s[12:15], s24 offen offset:2048 lds
buffer_store_dwordx4 v[172:175], v51, s[20:23], s40 offen offset:0 sc1
v_accvgpr_read_b32 v176, a120
v_accvgpr_read_b32 v177, a121
v_accvgpr_read_b32 v178, a122
v_accvgpr_read_b32 v179, a123
v_accvgpr_read_b32 v180, a124
v_accvgpr_read_b32 v181, a125
v_accvgpr_read_b32 v182, a126
v_accvgpr_read_b32 v183, a127
v_pk_mul_f32 v[176:177], v[176:177], v[162:163]
v_pk_mul_f32 v[178:179], v[178:179], v[162:163]
v_pk_mul_f32 v[180:181], v[180:181], v[162:163]
v_pk_mul_f32 v[182:183], v[182:183], v[162:163]
v_cvt_pk_bf16_f32 v184, v176, v177
v_cvt_pk_bf16_f32 v185, v178, v179
v_cvt_pk_bf16_f32 v186, v180, v181
v_cvt_pk_bf16_f32 v187, v182, v183
buffer_load_dwordx4 v39, s[12:15], s24 offen offset:3072 lds
s_add_u32 s26, s26, 393216
s_add_u32 s47, s47, 393216
s_add_u32 s25, s25, 128
s_add_u32 s24, s24, 128
buffer_store_dwordx4 v[184:187], v51, s[20:23], s40 offen offset:1024 sc1
v_accvgpr_read_b32 v164, a144
v_accvgpr_read_b32 v165, a145
v_accvgpr_read_b32 v166, a146
v_accvgpr_read_b32 v167, a147
v_accvgpr_read_b32 v168, a148
v_accvgpr_read_b32 v169, a149
v_accvgpr_read_b32 v170, a150
v_accvgpr_read_b32 v171, a151
v_pk_mul_f32 v[164:165], v[164:165], v[160:161]
v_pk_mul_f32 v[166:167], v[166:167], v[160:161]
v_pk_mul_f32 v[168:169], v[168:169], v[160:161]
v_pk_mul_f32 v[170:171], v[170:171], v[160:161]
v_cvt_pk_bf16_f32 v172, v164, v165
v_cvt_pk_bf16_f32 v173, v166, v167
v_cvt_pk_bf16_f32 v174, v168, v169
v_cvt_pk_bf16_f32 v175, v170, v171
buffer_store_dwordx4 v[172:175], v51, s[20:23], s40 offen offset:2048 sc1
v_accvgpr_read_b32 v176, a152
v_accvgpr_read_b32 v177, a153
v_accvgpr_read_b32 v178, a154
v_accvgpr_read_b32 v179, a155
v_accvgpr_read_b32 v180, a156
v_accvgpr_read_b32 v181, a157
v_accvgpr_read_b32 v182, a158
v_accvgpr_read_b32 v183, a159
v_pk_mul_f32 v[176:177], v[176:177], v[162:163]
v_pk_mul_f32 v[178:179], v[178:179], v[162:163]
v_pk_mul_f32 v[180:181], v[180:181], v[162:163]
v_pk_mul_f32 v[182:183], v[182:183], v[162:163]
v_cvt_pk_bf16_f32 v184, v176, v177
v_cvt_pk_bf16_f32 v185, v178, v179
v_cvt_pk_bf16_f32 v186, v180, v181
v_cvt_pk_bf16_f32 v187, v182, v183
buffer_store_dwordx4 v[184:187], v51, s[20:23], s40 offen offset:3072 sc1
s_cmp_eq_u32 s41, -1
s_cbranch_scc1 .Lfb_ret2
s_getpc_b64 s[30:31]
.Lfbp:
s_add_u32 s30, s30, .Lfb_ret2-.Lfbp
s_addc_u32 s31, s31, 0
.Lfb:
v_rcp_f32 v160, v216
v_rcp_f32 v162, v220
v_mov_b32 v161, v160
v_mov_b32 v163, v162
v_accvgpr_read_b32 v164, a176
v_accvgpr_read_b32 v165, a177
v_accvgpr_read_b32 v166, a178
v_accvgpr_read_b32 v167, a179
v_accvgpr_read_b32 v168, a180
v_accvgpr_read_b32 v169, a181
v_accvgpr_read_b32 v170, a182
v_accvgpr_read_b32 v171, a183
v_pk_mul_f32 v[164:165], v[164:165], v[160:161]
v_pk_mul_f32 v[166:167], v[166:167], v[160:161]
v_pk_mul_f32 v[168:169], v[168:169], v[160:161]
v_pk_mul_f32 v[170:171], v[170:171], v[160:161]
v_cvt_pk_bf16_f32 v172, v164, v165
v_cvt_pk_bf16_f32 v173, v166, v167
v_cvt_pk_bf16_f32 v174, v168, v169
v_cvt_pk_bf16_f32 v175, v170, v171
buffer_store_dwordx4 v[172:175], v7, s[20:23], s41 offen offset:0 sc1
v_accvgpr_read_b32 v176, a184
v_accvgpr_read_b32 v177, a185
v_accvgpr_read_b32 v178, a186
v_accvgpr_read_b32 v179, a187
v_accvgpr_read_b32 v180, a188
v_accvgpr_read_b32 v181, a189
v_accvgpr_read_b32 v182, a190
v_accvgpr_read_b32 v183, a191
v_pk_mul_f32 v[176:177], v[176:177], v[162:163]
v_pk_mul_f32 v[178:179], v[178:179], v[162:163]
v_pk_mul_f32 v[180:181], v[180:181], v[162:163]
v_pk_mul_f32 v[182:183], v[182:183], v[162:163]
v_cvt_pk_bf16_f32 v184, v176, v177
v_cvt_pk_bf16_f32 v185, v178, v179
v_cvt_pk_bf16_f32 v186, v180, v181
v_cvt_pk_bf16_f32 v187, v182, v183
buffer_store_dwordx4 v[184:187], v7, s[20:23], s41 offen offset:1024 sc1
v_accvgpr_read_b32 v164, a208
v_accvgpr_read_b32 v165, a209
v_accvgpr_read_b32 v166, a210
v_accvgpr_read_b32 v167, a211
v_accvgpr_read_b32 v168, a212
v_accvgpr_read_b32 v169, a213
v_accvgpr_read_b32 v170, a214
v_accvgpr_read_b32 v171, a215
v_pk_mul_f32 v[164:165], v[164:165], v[160:161]
v_pk_mul_f32 v[166:167], v[166:167], v[160:161]
v_pk_mul_f32 v[168:169], v[168:169], v[160:161]
v_pk_mul_f32 v[170:171], v[170:171], v[160:161]
v_cvt_pk_bf16_f32 v172, v164, v165
v_cvt_pk_bf16_f32 v173, v166, v167
v_cvt_pk_bf16_f32 v174, v168, v169
v_cvt_pk_bf16_f32 v175, v170, v171
buffer_store_dwordx4 v[172:175], v7, s[20:23], s41 offen offset:2048 sc1
v_accvgpr_read_b32 v176, a216
v_accvgpr_read_b32 v177, a217
v_accvgpr_read_b32 v178, a218
v_accvgpr_read_b32 v179, a219
v_accvgpr_read_b32 v180, a220
v_accvgpr_read_b32 v181, a221
v_accvgpr_read_b32 v182, a222
v_accvgpr_read_b32 v183, a223
v_pk_mul_f32 v[176:177], v[176:177], v[162:163]
v_pk_mul_f32 v[178:179], v[178:179], v[162:163]
v_pk_mul_f32 v[180:181], v[180:181], v[162:163]
v_pk_mul_f32 v[182:183], v[182:183], v[162:163]
v_cvt_pk_bf16_f32 v184, v176, v177
v_cvt_pk_bf16_f32 v185, v178, v179
v_cvt_pk_bf16_f32 v186, v180, v181
v_cvt_pk_bf16_f32 v187, v182, v183
buffer_store_dwordx4 v[184:187], v7, s[20:23], s41 offen offset:3072 sc1
v_accvgpr_read_b32 v164, a240
v_accvgpr_read_b32 v165, a241
v_accvgpr_read_b32 v166, a242
v_accvgpr_read_b32 v167, a243
v_accvgpr_read_b32 v168, a244
v_accvgpr_read_b32 v169, a245
v_accvgpr_read_b32 v170, a246
v_accvgpr_read_b32 v171, a247
v_pk_mul_f32 v[164:165], v[164:165], v[160:161]
v_pk_mul_f32 v[166:167], v[166:167], v[160:161]
v_pk_mul_f32 v[168:169], v[168:169], v[160:161]
v_pk_mul_f32 v[170:171], v[170:171], v[160:161]
v_cvt_pk_bf16_f32 v172, v164, v165
v_cvt_pk_bf16_f32 v173, v166, v167
v_cvt_pk_bf16_f32 v174, v168, v169
v_cvt_pk_bf16_f32 v175, v170, v171
buffer_store_dwordx4 v[172:175], v51, s[20:23], s41 offen offset:0 sc1
v_accvgpr_read_b32 v176, a248
v_accvgpr_read_b32 v177, a249
v_accvgpr_read_b32 v178, a250
v_accvgpr_read_b32 v179, a251
v_accvgpr_read_b32 v180, a252
v_accvgpr_read_b32 v181, a253
v_accvgpr_read_b32 v182, a254
v_accvgpr_read_b32 v183, a255
v_pk_mul_f32 v[176:177], v[176:177], v[162:163]
v_pk_mul_f32 v[178:179], v[178:179], v[162:163]
v_pk_mul_f32 v[180:181], v[180:181], v[162:163]
v_pk_mul_f32 v[182:183], v[182:183], v[162:163]
v_cvt_pk_bf16_f32 v184, v176, v177
v_cvt_pk_bf16_f32 v185, v178, v179
v_cvt_pk_bf16_f32 v186, v180, v181
v_cvt_pk_bf16_f32 v187, v182, v183
buffer_store_dwordx4 v[184:187], v51, s[20:23], s41 offen offset:1024 sc1
v_accvgpr_read_b32 v164, a96
v_accvgpr_read_b32 v165, a97
v_accvgpr_read_b32 v166, a98
v_accvgpr_read_b32 v167, a99
v_accvgpr_read_b32 v168, a100
v_accvgpr_read_b32 v169, a101
v_accvgpr_read_b32 v170, a102
v_accvgpr_read_b32 v171, a103
v_pk_mul_f32 v[164:165], v[164:165], v[160:161]
v_pk_mul_f32 v[166:167], v[166:167], v[160:161]
v_pk_mul_f32 v[168:169], v[168:169], v[160:161]
v_pk_mul_f32 v[170:171], v[170:171], v[160:161]
v_cvt_pk_bf16_f32 v172, v164, v165
v_cvt_pk_bf16_f32 v173, v166, v167
v_cvt_pk_bf16_f32 v174, v168, v169
v_cvt_pk_bf16_f32 v175, v170, v171
buffer_store_dwordx4 v[172:175], v51, s[20:23], s41 offen offset:2048 sc1
v_accvgpr_read_b32 v176, a104
v_accvgpr_read_b32 v177, a105
v_accvgpr_read_b32 v178, a106
v_accvgpr_read_b32 v179, a107
v_accvgpr_read_b32 v180, a108
v_accvgpr_read_b32 v181, a109
v_accvgpr_read_b32 v182, a110
v_accvgpr_read_b32 v183, a111
v_pk_mul_f32 v[176:177], v[176:177], v[162:163]
v_pk_mul_f32 v[178:179], v[178:179], v[162:163]
v_pk_mul_f32 v[180:181], v[180:181], v[162:163]
v_pk_mul_f32 v[182:183], v[182:183], v[162:163]
v_cvt_pk_bf16_f32 v184, v176, v177
v_cvt_pk_bf16_f32 v185, v178, v179
v_cvt_pk_bf16_f32 v186, v180, v181
v_cvt_pk_bf16_f32 v187, v182, v183
buffer_store_dwordx4 v[184:187], v51, s[20:23], s41 offen offset:3072 sc1
s_mov_b32 s41, -1
s_setpc_b64 s[30:31]
.Lfb_ret2:
s_cmp_eq_u32 s3, 0
s_cbranch_scc0 .LEend1
s_mov_b64 exec, 0
s_branch .Lpad14
.Ltch3:
s_branch .Lpad15
.Ltch4:
s_branch .Lpad16
.Ltch5:
s_branch .Lpad17
.Ltch6:
s_mov_b64 exec, -1
s_branch .Lfb_ret
.LEend1:
s_cmp_eq_u32 s3, 1
s_cbranch_scc0 .Lfb_ret
s_mov_b64 exec, 0
s_branch .Lpad18
.Ltch7:
s_branch .Lpad19
.Ltch8:
s_mov_b64 exec, -1
.Lfb_ret:
s_cmp_lt_u32 s3, 2
s_waitcnt vmcnt(23)
s_barrier
ds_read_b128 a[128:131], v45
ds_read_b128 a[132:135], v43 offset:0
ds_read_b64_tr_b16 a[164:165], v47 offset:0
ds_read_b64_tr_b16 a[166:167], v47 offset:512
ds_read_b64_tr_b16 a[168:169], v48 offset:0
ds_read_b64_tr_b16 a[170:171], v48 offset:512
ds_read_b128 a[136:139], v43 offset:2048
ds_read_b128 a[140:143], v43 offset:4096
ds_read_b128 a[160:163], v43 offset:6144
ds_read_b64_tr_b16 a[172:173], v47 offset:2048
ds_read_b64_tr_b16 a[174:175], v47 offset:2560
ds_read_b64_tr_b16 a[192:193], v48 offset:2048
ds_read_b64_tr_b16 a[194:195], v48 offset:2560
s_cselect_b32 s39, 1, 0
s_waitcnt lgkmcnt(8)
ds_read_b64_tr_b16 a[196:197], v47 offset:4096
ds_read_b64_tr_b16 a[198:199], v47 offset:4608
ds_read_b64_tr_b16 a[200:201], v48 offset:4096
ds_read_b64_tr_b16 a[202:203], v48 offset:4608
s_waitcnt lgkmcnt(0)
v_mfma_f32_16x16x32_bf16 v[160:163], a[172:175], a[128:131], 0
s_add_u32 m0, s48, 32768
v_mfma_f32_16x16x32_bf16 v[164:167], a[192:195], a[128:131], 0
buffer_load_dwordx4 v40, s[16:19], s26 offen offset:0 lds
v_mfma_f32_16x16x32_bf16 v[200:203], a[128:131], a[196:199], 0
ds_read_b64_tr_b16 a[20:21], v47 offset:14336
ds_read_b64_tr_b16 a[22:23], v47 offset:14848
v_mfma_f32_16x16x32_bf16 v[204:207], a[128:131], a[200:203], 0
ds_read_b128 a[0:3], v46
v_mfma_f32_16x16x32_bf16 v[128:131], a[164:167], a[132:135], 0
buffer_load_dwordx4 v41, s[16:19], s26 offen offset:1024 lds
v_mfma_f32_16x16x32_bf16 v[132:135], a[168:171], a[132:135], 0
ds_read_b64_tr_b16 a[24:25], v48 offset:14336
ds_read_b64_tr_b16 a[26:27], v48 offset:14848
v_mfma_f32_16x16x32_bf16 v[168:171], a[172:175], a[132:135], 0
ds_read_b128 a[4:7], v44 offset:0
.Lpad14:
s_cbranch_execz .Ltch3
v_mfma_f32_16x16x32_bf16 v[172:175], a[192:195], a[132:135], 0
buffer_load_dwordx4 v40, s[16:19], s26 offen offset:2048 lds
v_mfma_f32_16x16x32_bf16 v[208:211], a[132:135], a[196:199], 0
ds_read_b64_tr_b16 a[28:29], v47 offset:16384
ds_read_b64_tr_b16 a[30:31], v47 offset:16896
v_mfma_f32_16x16x32_bf16 v[212:215], a[132:135], a[200:203], 0
ds_read_b128 a[8:11], v44 offset:2048
v_mfma_f32_16x16x32_bf16 v[136:139], a[164:167], a[136:139], 0
buffer_load_dwordx4 v41, s[16:19], s26 offen offset:3072 lds
v_mfma_f32_16x16x32_bf16 v[140:143], a[168:171], a[136:139], 0
ds_read_b64_tr_b16 a[32:33], v48 offset:16384
ds_read_b64_tr_b16 a[34:35], v48 offset:16896
v_mfma_f32_16x16x32_bf16 v[176:179], a[172:175], a[136:139], 0
s_add_u32 m0, s48, 36864
ds_read_b128 a[12:15], v44 offset:4096
v_mfma_f32_16x16x32_bf16 v[180:183], a[192:195], a[136:139], 0
buffer_load_dwordx4 v40, s[16:19], s47 offen offset:0 lds
v_mfma_f32_16x16x32_bf16 v[216:219], a[136:139], a[196:199], 0
ds_read_b64_tr_b16 a[36:37], v47 offset:18432
ds_read_b64_tr_b16 a[38:39], v47 offset:18944
v_mfma_f32_16x16x32_bf16 v[220:223], a[136:139], a[200:203], 0
ds_read_b128 a[16:19], v44 offset:6144
v_mfma_f32_16x16x32_bf16 v[144:147], a[164:167], a[140:143], 0
buffer_load_dwordx4 v41, s[16:19], s47 offen offset:1024 lds
v_mfma_f32_16x16x32_bf16 v[148:151], a[168:171], a[140:143], 0
ds_read_b64_tr_b16 a[40:41], v48 offset:18432
ds_read_b64_tr_b16 a[42:43], v48 offset:18944
v_mfma_f32_16x16x32_bf16 v[184:187], a[172:175], a[140:143], 0
s_cmp_eq_u32 s39, 1
s_mov_b32 s28, 16384
s_cselect_b32 s28, 4294934528, s28
v_mfma_f32_16x16x32_bf16 v[188:191], a[192:195], a[140:143], 0
buffer_load_dwordx4 v42, s[12:15], s25 offen offset:2048 lds
v_mfma_f32_16x16x32_bf16 v[224:227], a[140:143], a[196:199], 0
s_add_u32 s26, s26, 393216
s_add_u32 s47, s47, 393216
s_add_u32 s25, s25, 128
v_mfma_f32_16x16x32_bf16 v[228:231], a[140:143], a[200:203], 0
s_add_u32 m0, s91, 16384
v_add_u32 v43, 16384, v43
v_add_u32 v44, 16384, v44
v_mfma_f32_16x16x32_bf16 v[152:155], a[164:167], a[160:163], 0
buffer_load_dwordx4 v36, s[12:15], s24 offen offset:0 lds
v_mfma_f32_16x16x32_bf16 v[156:159], a[168:171], a[160:163], 0
v_add_u32 v47, 4294934528, v47
v_add_u32 v48, 4294934528, v48
v_mfma_f32_16x16x32_bf16 v[192:195], a[172:175], a[160:163], 0
buffer_load_dwordx4 v37, s[12:15], s24 offen offset:1024 lds
v_mfma_f32_16x16x32_bf16 v[196:199], a[192:195], a[160:163], 0
v_add_u32 v45, s28, v45
v_add_u32 v46, s28, v46
v_mfma_f32_16x16x32_bf16 v[232:235], a[160:163], a[196:199], 0
buffer_load_dwordx4 v38, s[12:15], s24 offen offset:2048 lds
v_mfma_f32_16x16x32_bf16 v[236:239], a[160:163], a[200:203], 0
s_waitcnt vmcnt(19) lgkmcnt(0)
s_barrier
v_mfma_f32_16x16x32_bf16 v[160:163], a[28:31], a[0:3], v[160:163]
buffer_load_dwordx4 v39, s[12:15], s24 offen offset:3072 lds
v_mfma_f32_16x16x32_bf16 v[164:167], a[32:35], a[0:3], v[164:167]
s_add_u32 s24, s24, 128
v_mfma_f32_16x16x32_bf16 v[200:203], a[0:3], a[36:39], v[200:203]
s_add_u32 m0, s48, 131072
v_mfma_f32_16x16x32_bf16 v[204:207], a[0:3], a[40:43], v[204:207]
buffer_load_dwordx4 v40, s[16:19], s26 offen offset:0 lds
v_mfma_f32_16x16x32_bf16 v[128:131], a[20:23], a[4:7], v[128:131]
ds_read_b64_tr_b16 a[164:165], v47 offset:0
ds_read_b64_tr_b16 a[166:167], v47 offset:512
v_mfma_f32_16x16x32_bf16 v[132:135], a[24:27], a[4:7], v[132:135]
ds_read_b128 a[128:131], v45
v_mfma_f32_16x16x32_bf16 v[168:171], a[28:31], a[4:7], v[168:171]
ds_read_b64_tr_b16 a[168:169], v48 offset:0
ds_read_b64_tr_b16 a[170:171], v48 offset:512
v_mfma_f32_16x16x32_bf16 v[172:175], a[32:35], a[4:7], v[172:175]
buffer_load_dwordx4 v41, s[16:19], s26 offen offset:1024 lds
v_mfma_f32_16x16x32_bf16 v[208:211], a[4:7], a[36:39], v[208:211]
ds_read_b128 a[132:135], v43 offset:0
v_mfma_f32_16x16x32_bf16 v[212:215], a[4:7], a[40:43], v[212:215]
ds_read_b64_tr_b16 a[172:173], v47 offset:2048
ds_read_b64_tr_b16 a[174:175], v47 offset:2560
v_mfma_f32_16x16x32_bf16 v[136:139], a[20:23], a[8:11], v[136:139]
ds_read_b128 a[136:139], v43 offset:2048
v_mfma_f32_16x16x32_bf16 v[140:143], a[24:27], a[8:11], v[140:143]
ds_read_b64_tr_b16 a[192:193], v48 offset:2048
ds_read_b64_tr_b16 a[194:195], v48 offset:2560
v_mfma_f32_16x16x32_bf16 v[176:179], a[28:31], a[8:11], v[176:179]
buffer_load_dwordx4 v40, s[16:19], s26 offen offset:2048 lds
v_mfma_f32_16x16x32_bf16 v[180:183], a[32:35], a[8:11], v[180:183]
ds_read_b128 a[140:143], v43 offset:4096
v_mfma_f32_16x16x32_bf16 v[216:219], a[8:11], a[36:39], v[216:219]
ds_read_b64_tr_b16 a[196:197], v47 offset:4096
ds_read_b64_tr_b16 a[198:199], v47 offset:4608
v_mfma_f32_16x16x32_bf16 v[220:223], a[8:11], a[40:43], v[220:223]
ds_read_b128 a[160:163], v43 offset:6144
v_mfma_f32_16x16x32_bf16 v[144:147], a[20:23], a[12:15], v[144:147]
ds_read_b64_tr_b16 a[200:201], v48 offset:4096
ds_read_b64_tr_b16 a[202:203], v48 offset:4608
v_mfma_f32_16x16x32_bf16 v[148:151], a[24:27], a[12:15], v[148:151]
buffer_load_dwordx4 v41, s[16:19], s26 offen offset:3072 lds
v_mfma_f32_16x16x32_bf16 v[184:187], a[28:31], a[12:15], v[184:187]
v_mfma_f32_16x16x32_bf16 v[188:191], a[32:35], a[12:15], v[188:191]
v_mfma_f32_16x16x32_bf16 v[224:227], a[12:15], a[36:39], v[224:227]
v_mfma_f32_16x16x32_bf16 v[228:231], a[12:15], a[40:43], v[228:231]
s_add_u32 m0, s48, 135168
v_mfma_f32_16x16x32_bf16 v[152:155], a[20:23], a[16:19], v[152:155]
buffer_load_dwordx4 v40, s[16:19], s47 offen offset:0 lds
v_mfma_f32_16x16x32_bf16 v[156:159], a[24:27], a[16:19], v[156:159]
v_mfma_f32_16x16x32_bf16 v[192:195], a[28:31], a[16:19], v[192:195]
v_mfma_f32_16x16x32_bf16 v[196:199], a[32:35], a[16:19], v[196:199]
v_mfma_f32_16x16x32_bf16 v[232:235], a[16:19], a[36:39], v[232:235]
buffer_load_dwordx4 v41, s[16:19], s47 offen offset:1024 lds
v_mfma_f32_16x16x32_bf16 v[236:239], a[16:19], a[40:43], v[236:239]
s_waitcnt lgkmcnt(0)
v_mfma_f32_16x16x32_bf16 v[160:163], a[172:175], a[128:131], v[160:163]
ds_read_b64_tr_b16 a[20:21], v47 offset:14336
ds_read_b64_tr_b16 a[22:23], v47 offset:14848
v_mfma_f32_16x16x32_bf16 v[164:167], a[192:195], a[128:131], v[164:167]
ds_read_b128 a[0:3], v46
v_mfma_f32_16x16x32_bf16 v[200:203], a[128:131], a[196:199], v[200:203]
ds_read_b64_tr_b16 a[24:25], v48 offset:14336
ds_read_b64_tr_b16 a[26:27], v48 offset:14848
v_mfma_f32_16x16x32_bf16 v[204:207], a[128:131], a[200:203], v[204:207]
buffer_load_dwordx4 v42, s[12:15], s25 offen offset:2048 lds
v_mfma_f32_16x16x32_bf16 v[128:131], a[164:167], a[132:135], v[128:131]
s_add_u32 s26, s26, 393216
s_add_u32 s47, s47, 393216
v_mfma_f32_16x16x32_bf16 v[132:135], a[168:171], a[132:135], v[132:135]
s_add_u32 s25, s25, 128
ds_read_b128 a[4:7], v44 offset:0
v_mfma_f32_16x16x32_bf16 v[168:171], a[172:175], a[132:135], v[168:171]
ds_read_b64_tr_b16 a[28:29], v47 offset:16384
ds_read_b64_tr_b16 a[30:31], v47 offset:16896
v_mfma_f32_16x16x32_bf16 v[172:175], a[192:195], a[132:135], v[172:175]
ds_read_b128 a[8:11], v44 offset:2048
v_mfma_f32_16x16x32_bf16 v[208:211], a[132:135], a[196:199], v[208:211]
s_add_u32 m0, s91, 65536
v_mfma_f32_16x16x32_bf16 v[212:215], a[132:135], a[200:203], v[212:215]
buffer_load_dwordx4 v36, s[12:15], s24 offen offset:0 lds
v_mfma_f32_16x16x32_bf16 v[136:139], a[164:167], a[136:139], v[136:139]
ds_read_b64_tr_b16 a[32:33], v48 offset:16384
ds_read_b64_tr_b16 a[34:35], v48 offset:16896
v_mfma_f32_16x16x32_bf16 v[140:143], a[168:171], a[136:139], v[140:143]
ds_read_b128 a[12:15], v44 offset:4096
v_mfma_f32_16x16x32_bf16 v[176:179], a[172:175], a[136:139], v[176:179]
ds_read_b64_tr_b16 a[36:37], v47 offset:18432
ds_read_b64_tr_b16 a[38:39], v47 offset:18944
v_mfma_f32_16x16x32_bf16 v[180:183], a[192:195], a[136:139], v[180:183]
ds_read_b128 a[16:19], v44 offset:6144
v_mfma_f32_16x16x32_bf16 v[216:219], a[136:139], a[196:199], v[216:219]
buffer_load_dwordx4 v37, s[12:15], s24 offen offset:1024 lds
v_mfma_f32_16x16x32_bf16 v[220:223], a[136:139], a[200:203], v[220:223]
ds_read_b64_tr_b16 a[40:41], v48 offset:18432
ds_read_b64_tr_b16 a[42:43], v48 offset:18944
v_mfma_f32_16x16x32_bf16 v[144:147], a[164:167], a[140:143], v[144:147]
s_cmp_eq_u32 s39, 1
s_mov_b32 s28, 4294885376
s_cselect_b32 s28, 4294901760, s28
v_mfma_f32_16x16x32_bf16 v[148:151], a[168:171], a[140:143], v[148:151]
v_add_u32 v43, 4294885376, v43
v_add_u32 v44, 4294885376, v44
v_mfma_f32_16x16x32_bf16 v[184:187], a[172:175], a[140:143], v[184:187]
v_add_u32 v47, 4294901760, v47
v_add_u32 v48, 4294901760, v48
v_mfma_f32_16x16x32_bf16 v[188:191], a[192:195], a[140:143], v[188:191]
buffer_load_dwordx4 v38, s[12:15], s24 offen offset:2048 lds
v_mfma_f32_16x16x32_bf16 v[224:227], a[140:143], a[196:199], v[224:227]
v_add_u32 v45, s28, v45
v_add_u32 v46, s28, v46
v_mfma_f32_16x16x32_bf16 v[228:231], a[140:143], a[200:203], v[228:231]
v_mfma_f32_16x16x32_bf16 v[152:155], a[164:167], a[160:163], v[152:155]
v_mfma_f32_16x16x32_bf16 v[156:159], a[168:171], a[160:163], v[156:159]
v_mfma_f32_16x16x32_bf16 v[192:195], a[172:175], a[160:163], v[192:195]
buffer_load_dwordx4 v39, s[12:15], s24 offen offset:3072 lds
v_mfma_f32_16x16x32_bf16 v[196:199], a[192:195], a[160:163], v[196:199]
s_add_u32 s24, s24, 128
v_mfma_f32_16x16x32_bf16 v[232:235], a[160:163], a[196:199], v[232:235]
v_mfma_f32_16x16x32_bf16 v[236:239], a[160:163], a[200:203], v[236:239]
s_waitcnt vmcnt(15) lgkmcnt(0)
s_barrier
v_mfma_f32_16x16x32_bf16 v[160:163], a[28:31], a[0:3], v[160:163]
ds_read_b64_tr_b16 a[164:165], v47 offset:0
ds_read_b64_tr_b16 a[166:167], v47 offset:512
v_mfma_f32_16x16x32_bf16 v[164:167], a[32:35], a[0:3], v[164:167]
s_add_u32 m0, s48, 98304
ds_read_b128 a[128:131], v45
v_mfma_f32_16x16x32_bf16 v[200:203], a[0:3], a[36:39], v[200:203]
buffer_load_dwordx4 v40, s[16:19], s26 offen offset:0 lds
v_mfma_f32_16x16x32_bf16 v[204:207], a[0:3], a[40:43], v[204:207]
ds_read_b64_tr_b16 a[168:169], v48 offset:0
ds_read_b64_tr_b16 a[170:171], v48 offset:512
v_mfma_f32_16x16x32_bf16 v[128:131], a[20:23], a[4:7], v[128:131]
ds_read_b128 a[132:135], v43 offset:0
v_mfma_f32_16x16x32_bf16 v[132:135], a[24:27], a[4:7], v[132:135]
ds_read_b64_tr_b16 a[172:173], v47 offset:2048
ds_read_b64_tr_b16 a[174:175], v47 offset:2560
v_mfma_f32_16x16x32_bf16 v[168:171], a[28:31], a[4:7], v[168:171]
ds_read_b128 a[136:139], v43 offset:2048
v_mfma_f32_16x16x32_bf16 v[172:175], a[32:35], a[4:7], v[172:175]
buffer_load_dwordx4 v41, s[16:19], s26 offen offset:1024 lds
v_mfma_f32_16x16x32_bf16 v[208:211], a[4:7], a[36:39], v[208:211]
ds_read_b64_tr_b16 a[192:193], v48 offset:2048
ds_read_b64_tr_b16 a[194:195], v48 offset:2560
v_mfma_f32_16x16x32_bf16 v[212:215], a[4:7], a[40:43], v[212:215]
ds_read_b128 a[140:143], v43 offset:4096
v_mfma_f32_16x16x32_bf16 v[136:139], a[20:23], a[8:11], v[136:139]
ds_read_b64_tr_b16 a[196:197], v47 offset:4096
ds_read_b64_tr_b16 a[198:199], v47 offset:4608
v_mfma_f32_16x16x32_bf16 v[140:143], a[24:27], a[8:11], v[140:143]
ds_read_b128 a[160:163], v43 offset:6144
v_mfma_f32_16x16x32_bf16 v[176:179], a[28:31], a[8:11], v[176:179]
buffer_load_dwordx4 v40, s[16:19], s26 offen offset:2048 lds
v_mfma_f32_16x16x32_bf16 v[180:183], a[32:35], a[8:11], v[180:183]
ds_read_b64_tr_b16 a[200:201], v48 offset:4096
ds_read_b64_tr_b16 a[202:203], v48 offset:4608
v_mfma_f32_16x16x32_bf16 v[216:219], a[8:11], a[36:39], v[216:219]
v_mfma_f32_16x16x32_bf16 v[220:223], a[8:11], a[40:43], v[220:223]
v_mfma_f32_16x16x32_bf16 v[144:147], a[20:23], a[12:15], v[144:147]
v_mfma_f32_16x16x32_bf16 v[148:151], a[24:27], a[12:15], v[148:151]
buffer_load_dwordx4 v41, s[16:19], s26 offen offset:3072 lds
v_mfma_f32_16x16x32_bf16 v[184:187], a[28:31], a[12:15], v[184:187]
v_mfma_f32_16x16x32_bf16 v[188:191], a[32:35], a[12:15], v[188:191]
v_mfma_f32_16x16x32_bf16 v[224:227], a[12:15], a[36:39], v[224:227]
v_mfma_f32_16x16x32_bf16 v[228:231], a[12:15], a[40:43], v[228:231]
s_add_u32 m0, s48, 102400
v_mfma_f32_16x16x32_bf16 v[152:155], a[20:23], a[16:19], v[152:155]
buffer_load_dwordx4 v40, s[16:19], s47 offen offset:0 lds
v_mfma_f32_16x16x32_bf16 v[156:159], a[24:27], a[16:19], v[156:159]
v_mfma_f32_16x16x32_bf16 v[192:195], a[28:31], a[16:19], v[192:195]
v_mfma_f32_16x16x32_bf16 v[196:199], a[32:35], a[16:19], v[196:199]
v_mfma_f32_16x16x32_bf16 v[232:235], a[16:19], a[36:39], v[232:235]
buffer_load_dwordx4 v41, s[16:19], s47 offen offset:1024 lds
v_mfma_f32_16x16x32_bf16 v[236:239], a[16:19], a[40:43], v[236:239]
s_waitcnt lgkmcnt(0)
v_mfma_f32_16x16x32_bf16 v[160:163], a[172:175], a[128:131], v[160:163]
ds_read_b64_tr_b16 a[20:21], v47 offset:14336
ds_read_b64_tr_b16 a[22:23], v47 offset:14848
v_mfma_f32_16x16x32_bf16 v[164:167], a[192:195], a[128:131], v[164:167]
ds_read_b128 a[0:3], v46
v_mfma_f32_16x16x32_bf16 v[200:203], a[128:131], a[196:199], v[200:203]
ds_read_b64_tr_b16 a[24:25], v48 offset:14336
ds_read_b64_tr_b16 a[26:27], v48 offset:14848
v_mfma_f32_16x16x32_bf16 v[204:207], a[128:131], a[200:203], v[204:207]
buffer_load_dwordx4 v42, s[12:15], s25 offen offset:2048 lds
v_mfma_f32_16x16x32_bf16 v[128:131], a[164:167], a[132:135], v[128:131]
s_add_u32 s26, s26, 393216
s_add_u32 s47, s47, 393216
v_mfma_f32_16x16x32_bf16 v[132:135], a[168:171], a[132:135], v[132:135]
s_add_u32 s25, s25, 128
ds_read_b128 a[4:7], v44 offset:0
v_mfma_f32_16x16x32_bf16 v[168:171], a[172:175], a[132:135], v[168:171]
ds_read_b64_tr_b16 a[28:29], v47 offset:16384
ds_read_b64_tr_b16 a[30:31], v47 offset:16896
v_mfma_f32_16x16x32_bf16 v[172:175], a[192:195], a[132:135], v[172:175]
ds_read_b128 a[8:11], v44 offset:2048
v_mfma_f32_16x16x32_bf16 v[208:211], a[132:135], a[196:199], v[208:211]
s_add_u32 m0, s91, 81920
v_mfma_f32_16x16x32_bf16 v[212:215], a[132:135], a[200:203], v[212:215]
buffer_load_dwordx4 v36, s[12:15], s24 offen offset:0 lds
v_mfma_f32_16x16x32_bf16 v[136:139], a[164:167], a[136:139], v[136:139]
ds_read_b64_tr_b16 a[32:33], v48 offset:16384
ds_read_b64_tr_b16 a[34:35], v48 offset:16896
v_mfma_f32_16x16x32_bf16 v[140:143], a[168:171], a[136:139], v[140:143]
ds_read_b128 a[12:15], v44 offset:4096
v_mfma_f32_16x16x32_bf16 v[176:179], a[172:175], a[136:139], v[176:179]
ds_read_b64_tr_b16 a[36:37], v47 offset:18432
ds_read_b64_tr_b16 a[38:39], v47 offset:18944
v_mfma_f32_16x16x32_bf16 v[180:183], a[192:195], a[136:139], v[180:183]
ds_read_b128 a[16:19], v44 offset:6144
v_mfma_f32_16x16x32_bf16 v[216:219], a[136:139], a[196:199], v[216:219]
buffer_load_dwordx4 v37, s[12:15], s24 offen offset:1024 lds
v_mfma_f32_16x16x32_bf16 v[220:223], a[136:139], a[200:203], v[220:223]
ds_read_b64_tr_b16 a[40:41], v48 offset:18432
ds_read_b64_tr_b16 a[42:43], v48 offset:18944
v_mfma_f32_16x16x32_bf16 v[144:147], a[164:167], a[140:143], v[144:147]
s_cmp_eq_u32 s39, 1
s_mov_b32 s28, 16384
s_cselect_b32 s28, 98304, s28
v_mfma_f32_16x16x32_bf16 v[148:151], a[168:171], a[140:143], v[148:151]
v_add_u32 v43, 16384, v43
v_add_u32 v44, 16384, v44
v_mfma_f32_16x16x32_bf16 v[184:187], a[172:175], a[140:143], v[184:187]
v_add_u32 v47, 98304, v47
v_add_u32 v48, 98304, v48
v_mfma_f32_16x16x32_bf16 v[188:191], a[192:195], a[140:143], v[188:191]
buffer_load_dwordx4 v38, s[12:15], s24 offen offset:2048 lds
v_mfma_f32_16x16x32_bf16 v[224:227], a[140:143], a[196:199], v[224:227]
v_add_u32 v45, s28, v45
v_add_u32 v46, s28, v46
v_mfma_f32_16x16x32_bf16 v[228:231], a[140:143], a[200:203], v[228:231]
v_mfma_f32_16x16x32_bf16 v[152:155], a[164:167], a[160:163], v[152:155]
v_mfma_f32_16x16x32_bf16 v[156:159], a[168:171], a[160:163], v[156:159]
v_mfma_f32_16x16x32_bf16 v[192:195], a[172:175], a[160:163], v[192:195]
buffer_load_dwordx4 v39, s[12:15], s24 offen offset:3072 lds
v_mfma_f32_16x16x32_bf16 v[196:199], a[192:195], a[160:163], v[196:199]
s_add_u32 s24, s24, 128
v_mfma_f32_16x16x32_bf16 v[232:235], a[160:163], a[196:199], v[232:235]
v_or_b32 v0, 3, v11
v_mfma_f32_16x16x32_bf16 v[236:239], a[160:163], a[200:203], v[236:239]
s_waitcnt vmcnt(33)
s_waitcnt vmcnt(15) lgkmcnt(0)
s_barrier
v_mfma_f32_16x16x32_bf16 v[160:163], a[28:31], a[0:3], v[160:163]
buffer_store_dword v0, v6, s[60:63], 0 offen sc1
ds_read_b64_tr_b16 a[164:165], v47 offset:0
ds_read_b64_tr_b16 a[166:167], v47 offset:512
v_mfma_f32_16x16x32_bf16 v[164:167], a[32:35], a[0:3], v[164:167]
s_add_u32 m0, s48, 32768
ds_read_b128 a[128:131], v45
v_mfma_f32_16x16x32_bf16 v[200:203], a[0:3], a[36:39], v[200:203]
buffer_load_dwordx4 v40, s[16:19], s26 offen offset:0 lds
v_mfma_f32_16x16x32_bf16 v[204:207], a[0:3], a[40:43], v[204:207]
ds_read_b64_tr_b16 a[168:169], v48 offset:0
ds_read_b64_tr_b16 a[170:171], v48 offset:512
v_mfma_f32_16x16x32_bf16 v[128:131], a[20:23], a[4:7], v[128:131]
ds_read_b128 a[132:135], v43 offset:0
v_mfma_f32_16x16x32_bf16 v[132:135], a[24:27], a[4:7], v[132:135]
ds_read_b64_tr_b16 a[172:173], v47 offset:2048
ds_read_b64_tr_b16 a[174:175], v47 offset:2560
v_mfma_f32_16x16x32_bf16 v[168:171], a[28:31], a[4:7], v[168:171]
ds_read_b128 a[136:139], v43 offset:2048
v_mfma_f32_16x16x32_bf16 v[172:175], a[32:35], a[4:7], v[172:175]
buffer_load_dwordx4 v41, s[16:19], s26 offen offset:1024 lds
v_mfma_f32_16x16x32_bf16 v[208:211], a[4:7], a[36:39], v[208:211]
ds_read_b64_tr_b16 a[192:193], v48 offset:2048
ds_read_b64_tr_b16 a[194:195], v48 offset:2560
v_mfma_f32_16x16x32_bf16 v[212:215], a[4:7], a[40:43], v[212:215]
ds_read_b128 a[140:143], v43 offset:4096
v_mfma_f32_16x16x32_bf16 v[136:139], a[20:23], a[8:11], v[136:139]
ds_read_b64_tr_b16 a[196:197], v47 offset:4096
ds_read_b64_tr_b16 a[198:199], v47 offset:4608
v_mfma_f32_16x16x32_bf16 v[140:143], a[24:27], a[8:11], v[140:143]
ds_read_b128 a[160:163], v43 offset:6144
v_mfma_f32_16x16x32_bf16 v[176:179], a[28:31], a[8:11], v[176:179]
buffer_load_dwordx4 v40, s[16:19], s26 offen offset:2048 lds
v_mfma_f32_16x16x32_bf16 v[180:183], a[32:35], a[8:11], v[180:183]
ds_read_b64_tr_b16 a[200:201], v48 offset:4096
ds_read_b64_tr_b16 a[202:203], v48 offset:4608
v_mfma_f32_16x16x32_bf16 v[216:219], a[8:11], a[36:39], v[216:219]
v_mfma_f32_16x16x32_bf16 v[220:223], a[8:11], a[40:43], v[220:223]
v_mfma_f32_16x16x32_bf16 v[144:147], a[20:23], a[12:15], v[144:147]
v_mfma_f32_16x16x32_bf16 v[148:151], a[24:27], a[12:15], v[148:151]
buffer_load_dwordx4 v41, s[16:19], s26 offen offset:3072 lds
v_mfma_f32_16x16x32_bf16 v[184:187], a[28:31], a[12:15], v[184:187]
v_mfma_f32_16x16x32_bf16 v[188:191], a[32:35], a[12:15], v[188:191]
v_mfma_f32_16x16x32_bf16 v[224:227], a[12:15], a[36:39], v[224:227]
v_mfma_f32_16x16x32_bf16 v[228:231], a[12:15], a[40:43], v[228:231]
s_add_u32 m0, s48, 36864
v_mfma_f32_16x16x32_bf16 v[152:155], a[20:23], a[16:19], v[152:155]
buffer_load_dwordx4 v40, s[16:19], s47 offen offset:0 lds
v_mfma_f32_16x16x32_bf16 v[156:159], a[24:27], a[16:19], v[156:159]
v_mfma_f32_16x16x32_bf16 v[192:195], a[28:31], a[16:19], v[192:195]
v_mfma_f32_16x16x32_bf16 v[196:199], a[32:35], a[16:19], v[196:199]
v_mfma_f32_16x16x32_bf16 v[232:235], a[16:19], a[36:39], v[232:235]
buffer_load_dwordx4 v41, s[16:19], s47 offen offset:1024 lds
v_mfma_f32_16x16x32_bf16 v[236:239], a[16:19], a[40:43], v[236:239]
s_waitcnt lgkmcnt(0)
v_mfma_f32_16x16x32_bf16 v[160:163], a[172:175], a[128:131], v[160:163]
ds_read_b64_tr_b16 a[20:21], v47 offset:14336
ds_read_b64_tr_b16 a[22:23], v47 offset:14848
v_mfma_f32_16x16x32_bf16 v[164:167], a[192:195], a[128:131], v[164:167]
ds_read_b128 a[0:3], v46
v_mfma_f32_16x16x32_bf16 v[200:203], a[128:131], a[196:199], v[200:203]
ds_read_b64_tr_b16 a[24:25], v48 offset:14336
ds_read_b64_tr_b16 a[26:27], v48 offset:14848
v_mfma_f32_16x16x32_bf16 v[204:207], a[128:131], a[200:203], v[204:207]
buffer_load_dwordx4 v42, s[12:15], s25 offen offset:2048 lds
v_mfma_f32_16x16x32_bf16 v[128:131], a[164:167], a[132:135], v[128:131]
s_add_u32 s26, s26, 393216
s_add_u32 s47, s47, 393216
v_mfma_f32_16x16x32_bf16 v[132:135], a[168:171], a[132:135], v[132:135]
s_add_u32 s25, s25, 128
ds_read_b128 a[4:7], v44 offset:0
v_mfma_f32_16x16x32_bf16 v[168:171], a[172:175], a[132:135], v[168:171]
ds_read_b64_tr_b16 a[28:29], v47 offset:16384
ds_read_b64_tr_b16 a[30:31], v47 offset:16896
v_mfma_f32_16x16x32_bf16 v[172:175], a[192:195], a[132:135], v[172:175]
ds_read_b128 a[8:11], v44 offset:2048
v_mfma_f32_16x16x32_bf16 v[208:211], a[132:135], a[196:199], v[208:211]
s_add_u32 m0, s91, 0
v_mfma_f32_16x16x32_bf16 v[212:215], a[132:135], a[200:203], v[212:215]
buffer_load_dwordx4 v36, s[12:15], s24 offen offset:0 lds
v_mfma_f32_16x16x32_bf16 v[136:139], a[164:167], a[136:139], v[136:139]
ds_read_b64_tr_b16 a[32:33], v48 offset:16384
ds_read_b64_tr_b16 a[34:35], v48 offset:16896
v_mfma_f32_16x16x32_bf16 v[140:143], a[168:171], a[136:139], v[140:143]
ds_read_b128 a[12:15], v44 offset:4096
v_mfma_f32_16x16x32_bf16 v[176:179], a[172:175], a[136:139], v[176:179]
ds_read_b64_tr_b16 a[36:37], v47 offset:18432
ds_read_b64_tr_b16 a[38:39], v47 offset:18944
v_mfma_f32_16x16x32_bf16 v[180:183], a[192:195], a[136:139], v[180:183]
ds_read_b128 a[16:19], v44 offset:6144
v_mfma_f32_16x16x32_bf16 v[216:219], a[136:139], a[196:199], v[216:219]
buffer_load_dwordx4 v37, s[12:15], s24 offen offset:1024 lds
v_mfma_f32_16x16x32_bf16 v[220:223], a[136:139], a[200:203], v[220:223]
ds_read_b64_tr_b16 a[40:41], v48 offset:18432
ds_read_b64_tr_b16 a[42:43], v48 offset:18944
v_mfma_f32_16x16x32_bf16 v[144:147], a[164:167], a[140:143], v[144:147]
s_cmp_eq_u32 s39, 1
s_mov_b32 s28, 49152
s_cselect_b32 s28, 4294934528, s28
v_mfma_f32_16x16x32_bf16 v[148:151], a[168:171], a[140:143], v[148:151]
v_add_u32 v43, 49152, v43
v_add_u32 v44, 49152, v44
v_mfma_f32_16x16x32_bf16 v[184:187], a[172:175], a[140:143], v[184:187]
v_add_u32 v47, 4294934528, v47
v_add_u32 v48, 4294934528, v48
v_mfma_f32_16x16x32_bf16 v[188:191], a[192:195], a[140:143], v[188:191]
buffer_load_dwordx4 v38, s[12:15], s24 offen offset:2048 lds
v_mfma_f32_16x16x32_bf16 v[224:227], a[140:143], a[196:199], v[224:227]
v_add_u32 v45, s28, v45
v_add_u32 v46, s28, v46
v_mfma_f32_16x16x32_bf16 v[228:231], a[140:143], a[200:203], v[228:231]
v_mfma_f32_16x16x32_bf16 v[152:155], a[164:167], a[160:163], v[152:155]
v_mfma_f32_16x16x32_bf16 v[156:159], a[168:171], a[160:163], v[156:159]
v_mfma_f32_16x16x32_bf16 v[192:195], a[172:175], a[160:163], v[192:195]
buffer_load_dwordx4 v39, s[12:15], s24 offen offset:3072 lds
v_mfma_f32_16x16x32_bf16 v[196:199], a[192:195], a[160:163], v[196:199]
s_add_u32 s24, s24, 128
v_mfma_f32_16x16x32_bf16 v[232:235], a[160:163], a[196:199], v[232:235]
v_mfma_f32_16x16x32_bf16 v[236:239], a[160:163], a[200:203], v[236:239]
s_waitcnt vmcnt(16) lgkmcnt(0)
s_barrier
v_mfma_f32_16x16x32_bf16 v[160:163], a[28:31], a[0:3], v[160:163]
ds_read_b64_tr_b16 a[164:165], v47 offset:0
ds_read_b64_tr_b16 a[166:167], v47 offset:512
v_mfma_f32_16x16x32_bf16 v[164:167], a[32:35], a[0:3], v[164:167]
s_add_u32 m0, s48, 131072
ds_read_b128 a[128:131], v45
v_mfma_f32_16x16x32_bf16 v[200:203], a[0:3], a[36:39], v[200:203]
buffer_load_dwordx4 v40, s[16:19], s26 offen offset:0 lds
v_mfma_f32_16x16x32_bf16 v[204:207], a[0:3], a[40:43], v[204:207]
ds_read_b64_tr_b16 a[168:169], v48 offset:0
ds_read_b64_tr_b16 a[170:171], v48 offset:512
v_mfma_f32_16x16x32_bf16 v[128:131], a[20:23], a[4:7], v[128:131]
ds_read_b128 a[132:135], v43 offset:0
v_mfma_f32_16x16x32_bf16 v[132:135], a[24:27], a[4:7], v[132:135]
ds_read_b64_tr_b16 a[172:173], v47 offset:2048
ds_read_b64_tr_b16 a[174:175], v47 offset:2560
v_mfma_f32_16x16x32_bf16 v[168:171], a[28:31], a[4:7], v[168:171]
ds_read_b128 a[136:139], v43 offset:2048
v_mfma_f32_16x16x32_bf16 v[172:175], a[32:35], a[4:7], v[172:175]
buffer_load_dwordx4 v41, s[16:19], s26 offen offset:1024 lds
v_mfma_f32_16x16x32_bf16 v[208:211], a[4:7], a[36:39], v[208:211]
ds_read_b64_tr_b16 a[192:193], v48 offset:2048
ds_read_b64_tr_b16 a[194:195], v48 offset:2560
v_mfma_f32_16x16x32_bf16 v[212:215], a[4:7], a[40:43], v[212:215]
ds_read_b128 a[140:143], v43 offset:4096
v_mfma_f32_16x16x32_bf16 v[136:139], a[20:23], a[8:11], v[136:139]
ds_read_b64_tr_b16 a[196:197], v47 offset:4096
ds_read_b64_tr_b16 a[198:199], v47 offset:4608
v_mfma_f32_16x16x32_bf16 v[140:143], a[24:27], a[8:11], v[140:143]
ds_read_b128 a[160:163], v43 offset:6144
v_mfma_f32_16x16x32_bf16 v[176:179], a[28:31], a[8:11], v[176:179]
buffer_load_dwordx4 v40, s[16:19], s26 offen offset:2048 lds
v_mfma_f32_16x16x32_bf16 v[180:183], a[32:35], a[8:11], v[180:183]
ds_read_b64_tr_b16 a[200:201], v48 offset:4096
ds_read_b64_tr_b16 a[202:203], v48 offset:4608
v_mfma_f32_16x16x32_bf16 v[216:219], a[8:11], a[36:39], v[216:219]
v_mfma_f32_16x16x32_bf16 v[220:223], a[8:11], a[40:43], v[220:223]
v_mfma_f32_16x16x32_bf16 v[144:147], a[20:23], a[12:15], v[144:147]
v_mfma_f32_16x16x32_bf16 v[148:151], a[24:27], a[12:15], v[148:151]
buffer_load_dwordx4 v41, s[16:19], s26 offen offset:3072 lds
v_mfma_f32_16x16x32_bf16 v[184:187], a[28:31], a[12:15], v[184:187]
v_mfma_f32_16x16x32_bf16 v[188:191], a[32:35], a[12:15], v[188:191]
v_mfma_f32_16x16x32_bf16 v[224:227], a[12:15], a[36:39], v[224:227]
v_mfma_f32_16x16x32_bf16 v[228:231], a[12:15], a[40:43], v[228:231]
s_add_u32 m0, s48, 135168
v_mfma_f32_16x16x32_bf16 v[152:155], a[20:23], a[16:19], v[152:155]
buffer_load_dwordx4 v40, s[16:19], s47 offen offset:0 lds
v_mfma_f32_16x16x32_bf16 v[156:159], a[24:27], a[16:19], v[156:159]
v_mfma_f32_16x16x32_bf16 v[192:195], a[28:31], a[16:19], v[192:195]
v_mfma_f32_16x16x32_bf16 v[196:199], a[32:35], a[16:19], v[196:199]
v_mfma_f32_16x16x32_bf16 v[232:235], a[16:19], a[36:39], v[232:235]
buffer_load_dwordx4 v41, s[16:19], s47 offen offset:1024 lds
v_mfma_f32_16x16x32_bf16 v[236:239], a[16:19], a[40:43], v[236:239]
s_waitcnt lgkmcnt(0)
v_mfma_f32_16x16x32_bf16 v[160:163], a[172:175], a[128:131], v[160:163]
ds_read_b64_tr_b16 a[20:21], v47 offset:14336
ds_read_b64_tr_b16 a[22:23], v47 offset:14848
v_mfma_f32_16x16x32_bf16 v[164:167], a[192:195], a[128:131], v[164:167]
s_lshl_b32 s54, s86, 7
ds_read_b128 a[0:3], v46
v_mfma_f32_16x16x32_bf16 v[200:203], a[128:131], a[196:199], v[200:203]
ds_read_b64_tr_b16 a[24:25], v48 offset:14336
ds_read_b64_tr_b16 a[26:27], v48 offset:14848
v_mfma_f32_16x16x32_bf16 v[204:207], a[128:131], a[200:203], v[204:207]
s_add_u32 s54, s54, 0x14000
buffer_load_dwordx4 v42, s[12:15], s25 offen offset:2048 lds
v_mfma_f32_16x16x32_bf16 v[128:131], a[164:167], a[132:135], v[128:131]
s_add_u32 s26, s26, 393216
s_add_u32 s47, s47, 393216
v_and_b32 v249, 7, v1
v_mfma_f32_16x16x32_bf16 v[132:135], a[168:171], a[132:135], v[132:135]
s_add_u32 s25, s25, 128
ds_read_b128 a[4:7], v44 offset:0
v_mfma_f32_16x16x32_bf16 v[168:171], a[172:175], a[132:135], v[168:171]
ds_read_b64_tr_b16 a[28:29], v47 offset:16384
ds_read_b64_tr_b16 a[30:31], v47 offset:16896
v_mfma_f32_16x16x32_bf16 v[172:175], a[192:195], a[132:135], v[172:175]
ds_read_b128 a[8:11], v44 offset:2048
v_lshl_add_u32 v249, v249, 2, s54
v_mfma_f32_16x16x32_bf16 v[208:211], a[132:135], a[196:199], v[208:211]
s_add_u32 m0, s91, 16384
v_mfma_f32_16x16x32_bf16 v[212:215], a[132:135], a[200:203], v[212:215]
buffer_load_dwordx4 v36, s[12:15], s24 offen offset:0 lds
v_mfma_f32_16x16x32_bf16 v[136:139], a[164:167], a[136:139], v[136:139]
ds_read_b64_tr_b16 a[32:33], v48 offset:16384
ds_read_b64_tr_b16 a[34:35], v48 offset:16896
v_mfma_f32_16x16x32_bf16 v[140:143], a[168:171], a[136:139], v[140:143]
ds_read_b128 a[12:15], v44 offset:4096
v_mfma_f32_16x16x32_bf16 v[176:179], a[172:175], a[136:139], v[176:179]
ds_read_b64_tr_b16 a[36:37], v47 offset:18432
ds_read_b64_tr_b16 a[38:39], v47 offset:18944
v_mfma_f32_16x16x32_bf16 v[180:183], a[192:195], a[136:139], v[180:183]
ds_read_b128 a[16:19], v44 offset:6144
v_mfma_f32_16x16x32_bf16 v[216:219], a[136:139], a[196:199], v[216:219]
buffer_load_dwordx4 v37, s[12:15], s24 offen offset:1024 lds
v_mfma_f32_16x16x32_bf16 v[220:223], a[136:139], a[200:203], v[220:223]
ds_read_b64_tr_b16 a[40:41], v48 offset:18432
ds_read_b64_tr_b16 a[42:43], v48 offset:18944
v_mfma_f32_16x16x32_bf16 v[144:147], a[164:167], a[140:143], v[144:147]
s_cmp_eq_u32 s39, 1
s_mov_b32 s28, 16384
s_cselect_b32 s28, 4294901760, s28
v_mfma_f32_16x16x32_bf16 v[148:151], a[168:171], a[140:143], v[148:151]
v_add_u32 v43, 16384, v43
v_add_u32 v44, 16384, v44
v_mfma_f32_16x16x32_bf16 v[184:187], a[172:175], a[140:143], v[184:187]
v_add_u32 v47, 4294901760, v47
v_add_u32 v48, 4294901760, v48
v_mfma_f32_16x16x32_bf16 v[188:191], a[192:195], a[140:143], v[188:191]
buffer_load_dwordx4 v38, s[12:15], s24 offen offset:2048 lds
.Lpad15:
s_cbranch_execz .Ltch4
v_mfma_f32_16x16x32_bf16 v[224:227], a[140:143], a[196:199], v[224:227]
v_add_u32 v45, s28, v45
v_add_u32 v46, s28, v46
v_mfma_f32_16x16x32_bf16 v[228:231], a[140:143], a[200:203], v[228:231]
v_mfma_f32_16x16x32_bf16 v[152:155], a[164:167], a[160:163], v[152:155]
v_mfma_f32_16x16x32_bf16 v[156:159], a[168:171], a[160:163], v[156:159]
v_mfma_f32_16x16x32_bf16 v[192:195], a[172:175], a[160:163], v[192:195]
buffer_load_dwordx4 v39, s[12:15], s24 offen offset:3072 lds
v_mfma_f32_16x16x32_bf16 v[196:199], a[192:195], a[160:163], v[196:199]
s_add_u32 s24, s24, 128
v_mfma_f32_16x16x32_bf16 v[232:235], a[160:163], a[196:199], v[232:235]
v_mfma_f32_16x16x32_bf16 v[236:239], a[160:163], a[200:203], v[236:239]
s_waitcnt vmcnt(15) lgkmcnt(0)
s_barrier
v_mfma_f32_16x16x32_bf16 v[160:163], a[28:31], a[0:3], v[160:163]
ds_read_b64_tr_b16 a[164:165], v47 offset:0
ds_read_b64_tr_b16 a[166:167], v47 offset:512
v_mfma_f32_16x16x32_bf16 v[164:167], a[32:35], a[0:3], v[164:167]
s_add_u32 m0, s48, 98304
ds_read_b128 a[128:131], v45
v_mfma_f32_16x16x32_bf16 v[200:203], a[0:3], a[36:39], v[200:203]
buffer_load_dwordx4 v40, s[16:19], s26 offen offset:0 lds
v_mfma_f32_16x16x32_bf16 v[204:207], a[0:3], a[40:43], v[204:207]
ds_read_b64_tr_b16 a[168:169], v48 offset:0
ds_read_b64_tr_b16 a[170:171], v48 offset:512
v_mfma_f32_16x16x32_bf16 v[128:131], a[20:23], a[4:7], v[128:131]
ds_read_b128 a[132:135], v43 offset:0
v_mfma_f32_16x16x32_bf16 v[132:135], a[24:27], a[4:7], v[132:135]
ds_read_b64_tr_b16 a[172:173], v47 offset:2048
ds_read_b64_tr_b16 a[174:175], v47 offset:2560
v_mfma_f32_16x16x32_bf16 v[168:171], a[28:31], a[4:7], v[168:171]
ds_read_b128 a[136:139], v43 offset:2048
v_mfma_f32_16x16x32_bf16 v[172:175], a[32:35], a[4:7], v[172:175]
buffer_load_dwordx4 v41, s[16:19], s26 offen offset:1024 lds
v_mfma_f32_16x16x32_bf16 v[208:211], a[4:7], a[36:39], v[208:211]
ds_read_b64_tr_b16 a[192:193], v48 offset:2048
ds_read_b64_tr_b16 a[194:195], v48 offset:2560
v_mfma_f32_16x16x32_bf16 v[212:215], a[4:7], a[40:43], v[212:215]
ds_read_b128 a[140:143], v43 offset:4096
v_mfma_f32_16x16x32_bf16 v[136:139], a[20:23], a[8:11], v[136:139]
ds_read_b64_tr_b16 a[196:197], v47 offset:4096
ds_read_b64_tr_b16 a[198:199], v47 offset:4608
v_mfma_f32_16x16x32_bf16 v[140:143], a[24:27], a[8:11], v[140:143]
ds_read_b128 a[160:163], v43 offset:6144
v_mfma_f32_16x16x32_bf16 v[176:179], a[28:31], a[8:11], v[176:179]
buffer_load_dwordx4 v40, s[16:19], s26 offen offset:2048 lds
v_mfma_f32_16x16x32_bf16 v[180:183], a[32:35], a[8:11], v[180:183]
ds_read_b64_tr_b16 a[200:201], v48 offset:4096
ds_read_b64_tr_b16 a[202:203], v48 offset:4608
v_mfma_f32_16x16x32_bf16 v[216:219], a[8:11], a[36:39], v[216:219]
v_mfma_f32_16x16x32_bf16 v[220:223], a[8:11], a[40:43], v[220:223]
v_mfma_f32_16x16x32_bf16 v[144:147], a[20:23], a[12:15], v[144:147]
v_mfma_f32_16x16x32_bf16 v[148:151], a[24:27], a[12:15], v[148:151]
buffer_load_dwordx4 v41, s[16:19], s26 offen offset:3072 lds
v_mfma_f32_16x16x32_bf16 v[184:187], a[28:31], a[12:15], v[184:187]
v_mfma_f32_16x16x32_bf16 v[188:191], a[32:35], a[12:15], v[188:191]
v_mfma_f32_16x16x32_bf16 v[224:227], a[12:15], a[36:39], v[224:227]
v_mfma_f32_16x16x32_bf16 v[228:231], a[12:15], a[40:43], v[228:231]
s_add_u32 m0, s48, 102400
v_mfma_f32_16x16x32_bf16 v[152:155], a[20:23], a[16:19], v[152:155]
buffer_load_dwordx4 v40, s[16:19], s47 offen offset:0 lds
v_mfma_f32_16x16x32_bf16 v[156:159], a[24:27], a[16:19], v[156:159]
v_mfma_f32_16x16x32_bf16 v[192:195], a[28:31], a[16:19], v[192:195]
buffer_load_dword v35, v4, s[96:99], 0 offen
v_mfma_f32_16x16x32_bf16 v[196:199], a[32:35], a[16:19], v[196:199]
v_mfma_f32_16x16x32_bf16 v[232:235], a[16:19], a[36:39], v[232:235]
buffer_load_dwordx4 v41, s[16:19], s47 offen offset:1024 lds
v_mfma_f32_16x16x32_bf16 v[236:239], a[16:19], a[40:43], v[236:239]
s_waitcnt lgkmcnt(0)
v_mfma_f32_16x16x32_bf16 v[160:163], a[172:175], a[128:131], v[160:163]
ds_read_b64_tr_b16 a[20:21], v47 offset:14336
ds_read_b64_tr_b16 a[22:23], v47 offset:14848
v_mfma_f32_16x16x32_bf16 v[164:167], a[192:195], a[128:131], v[164:167]
ds_read_b128 a[0:3], v46
v_mfma_f32_16x16x32_bf16 v[200:203], a[128:131], a[196:199], v[200:203]
ds_read_b64_tr_b16 a[24:25], v48 offset:14336
ds_read_b64_tr_b16 a[26:27], v48 offset:14848
v_mfma_f32_16x16x32_bf16 v[204:207], a[128:131], a[200:203], v[204:207]
buffer_load_dwordx4 v42, s[12:15], s25 offen offset:2048 lds
v_mfma_f32_16x16x32_bf16 v[128:131], a[164:167], a[132:135], v[128:131]
s_add_u32 s26, s26, 393216
s_add_u32 s47, s47, 393216
v_mfma_f32_16x16x32_bf16 v[132:135], a[168:171], a[132:135], v[132:135]
s_add_u32 s25, s25, 128
ds_read_b128 a[4:7], v44 offset:0
v_mfma_f32_16x16x32_bf16 v[168:171], a[172:175], a[132:135], v[168:171]
buffer_load_dword v250, v249, s[60:63], 0 offen sc1
ds_read_b64_tr_b16 a[28:29], v47 offset:16384
ds_read_b64_tr_b16 a[30:31], v47 offset:16896
v_mfma_f32_16x16x32_bf16 v[172:175], a[192:195], a[132:135], v[172:175]
ds_read_b128 a[8:11], v44 offset:2048
v_mfma_f32_16x16x32_bf16 v[208:211], a[132:135], a[196:199], v[208:211]
s_add_u32 m0, s91, 65536
v_mfma_f32_16x16x32_bf16 v[212:215], a[132:135], a[200:203], v[212:215]
buffer_load_dwordx4 v36, s[12:15], s24 offen offset:0 lds
v_mfma_f32_16x16x32_bf16 v[136:139], a[164:167], a[136:139], v[136:139]
ds_read_b64_tr_b16 a[32:33], v48 offset:16384
ds_read_b64_tr_b16 a[34:35], v48 offset:16896
v_mfma_f32_16x16x32_bf16 v[140:143], a[168:171], a[136:139], v[140:143]
ds_read_b128 a[12:15], v44 offset:4096
v_mfma_f32_16x16x32_bf16 v[176:179], a[172:175], a[136:139], v[176:179]
ds_read_b64_tr_b16 a[36:37], v47 offset:18432
ds_read_b64_tr_b16 a[38:39], v47 offset:18944
v_mfma_f32_16x16x32_bf16 v[180:183], a[192:195], a[136:139], v[180:183]
ds_read_b128 a[16:19], v44 offset:6144
v_mfma_f32_16x16x32_bf16 v[216:219], a[136:139], a[196:199], v[216:219]
buffer_load_dwordx4 v37, s[12:15], s24 offen offset:1024 lds
v_mfma_f32_16x16x32_bf16 v[220:223], a[136:139], a[200:203], v[220:223]
ds_read_b64_tr_b16 a[40:41], v48 offset:18432
ds_read_b64_tr_b16 a[42:43], v48 offset:18944
v_mfma_f32_16x16x32_bf16 v[144:147], a[164:167], a[140:143], v[144:147]
s_cmp_eq_u32 s39, 1
s_mov_b32 s28, 4294885376
s_cselect_b32 s28, 98304, s28
v_mfma_f32_16x16x32_bf16 v[148:151], a[168:171], a[140:143], v[148:151]
v_add_u32 v43, 4294885376, v43
v_add_u32 v44, 4294885376, v44
v_mfma_f32_16x16x32_bf16 v[184:187], a[172:175], a[140:143], v[184:187]
v_add_u32 v47, 98304, v47
v_add_u32 v48, 98304, v48
v_mfma_f32_16x16x32_bf16 v[188:191], a[192:195], a[140:143], v[188:191]
buffer_load_dwordx4 v38, s[12:15], s24 offen offset:2048 lds
v_mfma_f32_16x16x32_bf16 v[224:227], a[140:143], a[196:199], v[224:227]
v_add_u32 v45, s28, v45
v_add_u32 v46, s28, v46
v_mfma_f32_16x16x32_bf16 v[228:231], a[140:143], a[200:203], v[228:231]
v_mfma_f32_16x16x32_bf16 v[152:155], a[164:167], a[160:163], v[152:155]
v_mfma_f32_16x16x32_bf16 v[156:159], a[168:171], a[160:163], v[156:159]
v_mfma_f32_16x16x32_bf16 v[192:195], a[172:175], a[160:163], v[192:195]
buffer_load_dwordx4 v39, s[12:15], s24 offen offset:3072 lds
v_mfma_f32_16x16x32_bf16 v[196:199], a[192:195], a[160:163], v[196:199]
s_add_u32 s24, s24, 128
v_mfma_f32_16x16x32_bf16 v[232:235], a[160:163], a[196:199], v[232:235]
v_mfma_f32_16x16x32_bf16 v[236:239], a[160:163], a[200:203], v[236:239]
s_waitcnt vmcnt(17) lgkmcnt(0)
s_barrier
v_mfma_f32_16x16x32_bf16 v[160:163], a[28:31], a[0:3], v[160:163]
ds_read_b64_tr_b16 a[164:165], v47 offset:0
ds_read_b64_tr_b16 a[166:167], v47 offset:512
v_mfma_f32_16x16x32_bf16 v[164:167], a[32:35], a[0:3], v[164:167]
s_add_u32 m0, s48, 32768
ds_read_b128 a[128:131], v45
v_mfma_f32_16x16x32_bf16 v[200:203], a[0:3], a[36:39], v[200:203]
buffer_load_dwordx4 v40, s[16:19], s26 offen offset:0 lds
v_mfma_f32_16x16x32_bf16 v[204:207], a[0:3], a[40:43], v[204:207]
ds_read_b64_tr_b16 a[168:169], v48 offset:0
ds_read_b64_tr_b16 a[170:171], v48 offset:512
v_mfma_f32_16x16x32_bf16 v[128:131], a[20:23], a[4:7], v[128:131]
ds_read_b128 a[132:135], v43 offset:0
v_mfma_f32_16x16x32_bf16 v[132:135], a[24:27], a[4:7], v[132:135]
ds_read_b64_tr_b16 a[172:173], v47 offset:2048
ds_read_b64_tr_b16 a[174:175], v47 offset:2560
v_mfma_f32_16x16x32_bf16 v[168:171], a[28:31], a[4:7], v[168:171]
ds_read_b128 a[136:139], v43 offset:2048
v_mfma_f32_16x16x32_bf16 v[172:175], a[32:35], a[4:7], v[172:175]
buffer_load_dwordx4 v41, s[16:19], s26 offen offset:1024 lds
v_mfma_f32_16x16x32_bf16 v[208:211], a[4:7], a[36:39], v[208:211]
ds_read_b64_tr_b16 a[192:193], v48 offset:2048
ds_read_b64_tr_b16 a[194:195], v48 offset:2560
v_mfma_f32_16x16x32_bf16 v[212:215], a[4:7], a[40:43], v[212:215]
ds_read_b128 a[140:143], v43 offset:4096
v_mfma_f32_16x16x32_bf16 v[136:139], a[20:23], a[8:11], v[136:139]
ds_read_b64_tr_b16 a[196:197], v47 offset:4096
ds_read_b64_tr_b16 a[198:199], v47 offset:4608
v_mfma_f32_16x16x32_bf16 v[140:143], a[24:27], a[8:11], v[140:143]
ds_read_b128 a[160:163], v43 offset:6144
v_mfma_f32_16x16x32_bf16 v[176:179], a[28:31], a[8:11], v[176:179]
buffer_load_dwordx4 v40, s[16:19], s26 offen offset:2048 lds
v_mfma_f32_16x16x32_bf16 v[180:183], a[32:35], a[8:11], v[180:183]
ds_read_b64_tr_b16 a[200:201], v48 offset:4096
ds_read_b64_tr_b16 a[202:203], v48 offset:4608
v_mfma_f32_16x16x32_bf16 v[216:219], a[8:11], a[36:39], v[216:219]
v_mfma_f32_16x16x32_bf16 v[220:223], a[8:11], a[40:43], v[220:223]
v_mfma_f32_16x16x32_bf16 v[144:147], a[20:23], a[12:15], v[144:147]
v_mfma_f32_16x16x32_bf16 v[148:151], a[24:27], a[12:15], v[148:151]
buffer_load_dwordx4 v41, s[16:19], s26 offen offset:3072 lds
v_mfma_f32_16x16x32_bf16 v[184:187], a[28:31], a[12:15], v[184:187]
v_mfma_f32_16x16x32_bf16 v[188:191], a[32:35], a[12:15], v[188:191]
v_mfma_f32_16x16x32_bf16 v[224:227], a[12:15], a[36:39], v[224:227]
v_mfma_f32_16x16x32_bf16 v[228:231], a[12:15], a[40:43], v[228:231]
s_add_u32 m0, s48, 36864
v_mfma_f32_16x16x32_bf16 v[152:155], a[20:23], a[16:19], v[152:155]
buffer_load_dwordx4 v40, s[16:19], s47 offen offset:0 lds
v_mfma_f32_16x16x32_bf16 v[156:159], a[24:27], a[16:19], v[156:159]
v_mfma_f32_16x16x32_bf16 v[192:195], a[28:31], a[16:19], v[192:195]
v_mfma_f32_16x16x32_bf16 v[196:199], a[32:35], a[16:19], v[196:199]
v_mfma_f32_16x16x32_bf16 v[232:235], a[16:19], a[36:39], v[232:235]
buffer_load_dwordx4 v41, s[16:19], s47 offen offset:1024 lds
v_mfma_f32_16x16x32_bf16 v[236:239], a[16:19], a[40:43], v[236:239]
s_waitcnt lgkmcnt(0)
v_mfma_f32_16x16x32_bf16 v[160:163], a[172:175], a[128:131], v[160:163]
ds_read_b64_tr_b16 a[20:21], v47 offset:14336
ds_read_b64_tr_b16 a[22:23], v47 offset:14848
v_mfma_f32_16x16x32_bf16 v[164:167], a[192:195], a[128:131], v[164:167]
ds_read_b128 a[0:3], v46
v_mfma_f32_16x16x32_bf16 v[200:203], a[128:131], a[196:199], v[200:203]
ds_read_b64_tr_b16 a[24:25], v48 offset:14336
ds_read_b64_tr_b16 a[26:27], v48 offset:14848
v_mfma_f32_16x16x32_bf16 v[204:207], a[128:131], a[200:203], v[204:207]
buffer_load_dwordx4 v42, s[12:15], s25 offen offset:2048 lds
v_mfma_f32_16x16x32_bf16 v[128:131], a[164:167], a[132:135], v[128:131]
s_add_u32 s26, s26, 393216
s_add_u32 s47, s47, 393216
v_mfma_f32_16x16x32_bf16 v[132:135], a[168:171], a[132:135], v[132:135]
s_add_u32 s25, s25, 128
ds_read_b128 a[4:7], v44 offset:0
v_mfma_f32_16x16x32_bf16 v[168:171], a[172:175], a[132:135], v[168:171]
ds_read_b64_tr_b16 a[28:29], v47 offset:16384
ds_read_b64_tr_b16 a[30:31], v47 offset:16896
v_mfma_f32_16x16x32_bf16 v[172:175], a[192:195], a[132:135], v[172:175]
ds_read_b128 a[8:11], v44 offset:2048
v_mfma_f32_16x16x32_bf16 v[208:211], a[132:135], a[196:199], v[208:211]
s_add_u32 m0, s91, 81920
v_mfma_f32_16x16x32_bf16 v[212:215], a[132:135], a[200:203], v[212:215]
buffer_load_dwordx4 v36, s[12:15], s24 offen offset:0 lds
v_mfma_f32_16x16x32_bf16 v[136:139], a[164:167], a[136:139], v[136:139]
ds_read_b64_tr_b16 a[32:33], v48 offset:16384
ds_read_b64_tr_b16 a[34:35], v48 offset:16896
v_mfma_f32_16x16x32_bf16 v[140:143], a[168:171], a[136:139], v[140:143]
ds_read_b128 a[12:15], v44 offset:4096
v_mfma_f32_16x16x32_bf16 v[176:179], a[172:175], a[136:139], v[176:179]
ds_read_b64_tr_b16 a[36:37], v47 offset:18432
ds_read_b64_tr_b16 a[38:39], v47 offset:18944
v_mfma_f32_16x16x32_bf16 v[180:183], a[192:195], a[136:139], v[180:183]
ds_read_b128 a[16:19], v44 offset:6144
v_mfma_f32_16x16x32_bf16 v[216:219], a[136:139], a[196:199], v[216:219]
buffer_load_dwordx4 v37, s[12:15], s24 offen offset:1024 lds
v_mfma_f32_16x16x32_bf16 v[220:223], a[136:139], a[200:203], v[220:223]
ds_read_b64_tr_b16 a[40:41], v48 offset:18432
ds_read_b64_tr_b16 a[42:43], v48 offset:18944
v_mfma_f32_16x16x32_bf16 v[144:147], a[164:167], a[140:143], v[144:147]
s_cmp_eq_u32 s39, 1
s_mov_b32 s28, 16384
s_cselect_b32 s28, 4294934528, s28
v_mfma_f32_16x16x32_bf16 v[148:151], a[168:171], a[140:143], v[148:151]
v_add_u32 v43, 16384, v43
v_add_u32 v44, 16384, v44
v_mfma_f32_16x16x32_bf16 v[184:187], a[172:175], a[140:143], v[184:187]
v_add_u32 v47, 4294934528, v47
v_add_u32 v48, 4294934528, v48
v_mfma_f32_16x16x32_bf16 v[188:191], a[192:195], a[140:143], v[188:191]
buffer_load_dwordx4 v38, s[12:15], s24 offen offset:2048 lds
v_mfma_f32_16x16x32_bf16 v[224:227], a[140:143], a[196:199], v[224:227]
v_add_u32 v45, s28, v45
v_add_u32 v46, s28, v46
v_mfma_f32_16x16x32_bf16 v[228:231], a[140:143], a[200:203], v[228:231]
v_mfma_f32_16x16x32_bf16 v[152:155], a[164:167], a[160:163], v[152:155]
v_mfma_f32_16x16x32_bf16 v[156:159], a[168:171], a[160:163], v[156:159]
v_mfma_f32_16x16x32_bf16 v[192:195], a[172:175], a[160:163], v[192:195]
buffer_load_dwordx4 v39, s[12:15], s24 offen offset:3072 lds
v_mfma_f32_16x16x32_bf16 v[196:199], a[192:195], a[160:163], v[196:199]
s_add_u32 s24, s24, 128
v_mfma_f32_16x16x32_bf16 v[232:235], a[160:163], a[196:199], v[232:235]
v_mfma_f32_16x16x32_bf16 v[236:239], a[160:163], a[200:203], v[236:239]
s_waitcnt vmcnt(16) lgkmcnt(0)
s_barrier
v_mfma_f32_16x16x32_bf16 v[160:163], a[28:31], a[0:3], v[160:163]
ds_read_b64_tr_b16 a[164:165], v47 offset:0
ds_read_b64_tr_b16 a[166:167], v47 offset:512
v_mfma_f32_16x16x32_bf16 v[164:167], a[32:35], a[0:3], v[164:167]
s_add_u32 m0, s48, 131072
ds_read_b128 a[128:131], v45
v_mfma_f32_16x16x32_bf16 v[200:203], a[0:3], a[36:39], v[200:203]
buffer_load_dwordx4 v40, s[16:19], s26 offen offset:0 lds
v_mfma_f32_16x16x32_bf16 v[204:207], a[0:3], a[40:43], v[204:207]
ds_read_b64_tr_b16 a[168:169], v48 offset:0
ds_read_b64_tr_b16 a[170:171], v48 offset:512
v_mfma_f32_16x16x32_bf16 v[128:131], a[20:23], a[4:7], v[128:131]
ds_read_b128 a[132:135], v43 offset:0
v_mfma_f32_16x16x32_bf16 v[132:135], a[24:27], a[4:7], v[132:135]
ds_read_b64_tr_b16 a[172:173], v47 offset:2048
ds_read_b64_tr_b16 a[174:175], v47 offset:2560
v_mfma_f32_16x16x32_bf16 v[168:171], a[28:31], a[4:7], v[168:171]
ds_read_b128 a[136:139], v43 offset:2048
v_mfma_f32_16x16x32_bf16 v[172:175], a[32:35], a[4:7], v[172:175]
buffer_load_dwordx4 v41, s[16:19], s26 offen offset:1024 lds
v_mfma_f32_16x16x32_bf16 v[208:211], a[4:7], a[36:39], v[208:211]
ds_read_b64_tr_b16 a[192:193], v48 offset:2048
ds_read_b64_tr_b16 a[194:195], v48 offset:2560
v_mfma_f32_16x16x32_bf16 v[212:215], a[4:7], a[40:43], v[212:215]
ds_read_b128 a[140:143], v43 offset:4096
v_mfma_f32_16x16x32_bf16 v[136:139], a[20:23], a[8:11], v[136:139]
ds_read_b64_tr_b16 a[196:197], v47 offset:4096
ds_read_b64_tr_b16 a[198:199], v47 offset:4608
v_mfma_f32_16x16x32_bf16 v[140:143], a[24:27], a[8:11], v[140:143]
ds_read_b128 a[160:163], v43 offset:6144
v_mfma_f32_16x16x32_bf16 v[176:179], a[28:31], a[8:11], v[176:179]
buffer_load_dwordx4 v40, s[16:19], s26 offen offset:2048 lds
v_mfma_f32_16x16x32_bf16 v[180:183], a[32:35], a[8:11], v[180:183]
ds_read_b64_tr_b16 a[200:201], v48 offset:4096
ds_read_b64_tr_b16 a[202:203], v48 offset:4608
v_mfma_f32_16x16x32_bf16 v[216:219], a[8:11], a[36:39], v[216:219]
v_mfma_f32_16x16x32_bf16 v[220:223], a[8:11], a[40:43], v[220:223]
v_mfma_f32_16x16x32_bf16 v[144:147], a[20:23], a[12:15], v[144:147]
v_mfma_f32_16x16x32_bf16 v[148:151], a[24:27], a[12:15], v[148:151]
buffer_load_dwordx4 v41, s[16:19], s26 offen offset:3072 lds
v_mfma_f32_16x16x32_bf16 v[184:187], a[28:31], a[12:15], v[184:187]
v_mfma_f32_16x16x32_bf16 v[188:191], a[32:35], a[12:15], v[188:191]
v_mfma_f32_16x16x32_bf16 v[224:227], a[12:15], a[36:39], v[224:227]
v_mfma_f32_16x16x32_bf16 v[228:231], a[12:15], a[40:43], v[228:231]
s_add_u32 m0, s48, 135168
v_mfma_f32_16x16x32_bf16 v[152:155], a[20:23], a[16:19], v[152:155]
buffer_load_dwordx4 v40, s[16:19], s47 offen offset:0 lds
v_mfma_f32_16x16x32_bf16 v[156:159], a[24:27], a[16:19], v[156:159]
v_mfma_f32_16x16x32_bf16 v[192:195], a[28:31], a[16:19], v[192:195]
v_mfma_f32_16x16x32_bf16 v[196:199], a[32:35], a[16:19], v[196:199]
v_mfma_f32_16x16x32_bf16 v[232:235], a[16:19], a[36:39], v[232:235]
buffer_load_dwordx4 v41, s[16:19], s47 offen offset:1024 lds
v_mfma_f32_16x16x32_bf16 v[236:239], a[16:19], a[40:43], v[236:239]
s_waitcnt lgkmcnt(0)
v_mfma_f32_16x16x32_bf16 v[160:163], a[172:175], a[128:131], v[160:163]
ds_read_b64_tr_b16 a[20:21], v47 offset:14336
ds_read_b64_tr_b16 a[22:23], v47 offset:14848
v_mfma_f32_16x16x32_bf16 v[164:167], a[192:195], a[128:131], v[164:167]
s_waitcnt vmcnt(21)
ds_read_b128 a[0:3], v46
v_cmp_eq_u32 vcc, 0, v250
v_mfma_f32_16x16x32_bf16 v[200:203], a[128:131], a[196:199], v[200:203]
ds_read_b64_tr_b16 a[24:25], v48 offset:14336
ds_read_b64_tr_b16 a[26:27], v48 offset:14848
v_mfma_f32_16x16x32_bf16 v[204:207], a[128:131], a[200:203], v[204:207]
buffer_load_dwordx4 v42, s[12:15], s25 offen offset:2048 lds
v_mfma_f32_16x16x32_bf16 v[128:131], a[164:167], a[132:135], v[128:131]
s_add_u32 s26, s26, 393216
s_add_u32 s47, s47, 393216
v_mfma_f32_16x16x32_bf16 v[132:135], a[168:171], a[132:135], v[132:135]
s_add_u32 s25, s25, 128
s_cmp_lg_u64 vcc, 0
s_cbranch_scc1 .Lspin6
.Lspin6_ret:
ds_read_b128 a[4:7], v44 offset:0
v_mfma_f32_16x16x32_bf16 v[168:171], a[172:175], a[132:135], v[168:171]
ds_read_b64_tr_b16 a[28:29], v47 offset:16384
ds_read_b64_tr_b16 a[30:31], v47 offset:16896
v_mfma_f32_16x16x32_bf16 v[172:175], a[192:195], a[132:135], v[172:175]
ds_read_b128 a[8:11], v44 offset:2048
v_mov_b32 v251, 0
v_mfma_f32_16x16x32_bf16 v[208:211], a[132:135], a[196:199], v[208:211]
s_add_u32 m0, s91, 0
v_readlane_b32 s54, v250, 0
v_nop
v_mfma_f32_16x16x32_bf16 v[212:215], a[132:135], a[200:203], v[212:215]
buffer_load_dwordx4 v36, s[12:15], s24 offen offset:0 lds
v_nop
v_mfma_f32_16x16x32_bf16 v[136:139], a[164:167], a[136:139], v[136:139]
ds_read_b64_tr_b16 a[32:33], v48 offset:16384
ds_read_b64_tr_b16 a[34:35], v48 offset:16896
v_mfma_f32_16x16x32_bf16 v[140:143], a[168:171], a[136:139], v[140:143]
ds_read_b128 a[12:15], v44 offset:4096
v_subrev_u32 v252, s54, v250
v_mfma_f32_16x16x32_bf16 v[176:179], a[172:175], a[136:139], v[176:179]
ds_read_b64_tr_b16 a[36:37], v47 offset:18432
ds_read_b64_tr_b16 a[38:39], v47 offset:18944
v_mfma_f32_16x16x32_bf16 v[180:183], a[192:195], a[136:139], v[180:183]
ds_read_b128 a[16:19], v44 offset:6144
v_cmp_lt_i32 vcc, 0, v252
v_mfma_f32_16x16x32_bf16 v[216:219], a[136:139], a[196:199], v[216:219]
buffer_load_dwordx4 v37, s[12:15], s24 offen offset:1024 lds
v_nop
v_mfma_f32_16x16x32_bf16 v[220:223], a[136:139], a[200:203], v[220:223]
ds_read_b64_tr_b16 a[40:41], v48 offset:18432
ds_read_b64_tr_b16 a[42:43], v48 offset:18944
v_mfma_f32_16x16x32_bf16 v[144:147], a[164:167], a[140:143], v[144:147]
s_cmp_eq_u32 s39, 1
s_mov_b32 s28, 49152
s_cselect_b32 s28, 4294901760, s28
v_mfma_f32_16x16x32_bf16 v[148:151], a[168:171], a[140:143], v[148:151]
v_add_u32 v43, 49152, v43
v_add_u32 v44, 49152, v44
v_mfma_f32_16x16x32_bf16 v[184:187], a[172:175], a[140:143], v[184:187]
v_add_u32 v47, 4294901760, v47
v_add_u32 v48, 4294901760, v48
v_mfma_f32_16x16x32_bf16 v[188:191], a[192:195], a[140:143], v[188:191]
buffer_load_dwordx4 v38, s[12:15], s24 offen offset:2048 lds
v_nop
v_mfma_f32_16x16x32_bf16 v[224:227], a[140:143], a[196:199], v[224:227]
v_add_u32 v45, s28, v45
v_add_u32 v46, s28, v46
v_mfma_f32_16x16x32_bf16 v[228:231], a[140:143], a[200:203], v[228:231]
v_addc_co_u32 v251, vcc, 0, v251, vcc
v_readlane_b32 s55, v250, 1
v_mfma_f32_16x16x32_bf16 v[152:155], a[164:167], a[160:163], v[152:155]
v_nop
v_nop
v_mfma_f32_16x16x32_bf16 v[156:159], a[168:171], a[160:163], v[156:159]
v_subrev_u32 v252, s55, v250
v_cmp_lt_i32 vcc, 0, v252
v_mfma_f32_16x16x32_bf16 v[192:195], a[172:175], a[160:163], v[192:195]
buffer_load_dwordx4 v39, s[12:15], s24 offen offset:3072 lds
v_nop
v_mfma_f32_16x16x32_bf16 v[196:199], a[192:195], a[160:163], v[196:199]
s_add_u32 s24, s24, 128
v_nop
v_addc_co_u32 v251, vcc, 0, v251, vcc
v_mfma_f32_16x16x32_bf16 v[232:235], a[160:163], a[196:199], v[232:235]
v_readlane_b32 s54, v250, 2
v_nop
v_mfma_f32_16x16x32_bf16 v[236:239], a[160:163], a[200:203], v[236:239]
s_waitcnt vmcnt(15) lgkmcnt(0)
s_barrier
v_nop
v_mfma_f32_16x16x32_bf16 v[160:163], a[28:31], a[0:3], v[160:163]
ds_read_b64_tr_b16 a[164:165], v47 offset:0
ds_read_b64_tr_b16 a[166:167], v47 offset:512
v_mfma_f32_16x16x32_bf16 v[164:167], a[32:35], a[0:3], v[164:167]
s_add_u32 m0, s48, 98304
ds_read_b128 a[128:131], v45
v_mfma_f32_16x16x32_bf16 v[200:203], a[0:3], a[36:39], v[200:203]
buffer_load_dwordx4 v40, s[16:19], s26 offen offset:0 lds
v_subrev_u32 v252, s54, v250
v_mfma_f32_16x16x32_bf16 v[204:207], a[0:3], a[40:43], v[204:207]
ds_read_b64_tr_b16 a[168:169], v48 offset:0
ds_read_b64_tr_b16 a[170:171], v48 offset:512
v_mfma_f32_16x16x32_bf16 v[128:131], a[20:23], a[4:7], v[128:131]
ds_read_b128 a[132:135], v43 offset:0
v_cmp_lt_i32 vcc, 0, v252
v_mfma_f32_16x16x32_bf16 v[132:135], a[24:27], a[4:7], v[132:135]
ds_read_b64_tr_b16 a[172:173], v47 offset:2048
ds_read_b64_tr_b16 a[174:175], v47 offset:2560
v_mfma_f32_16x16x32_bf16 v[168:171], a[28:31], a[4:7], v[168:171]
ds_read_b128 a[136:139], v43 offset:2048
v_nop
v_mfma_f32_16x16x32_bf16 v[172:175], a[32:35], a[4:7], v[172:175]
buffer_load_dwordx4 v41, s[16:19], s26 offen offset:1024 lds
v_nop
v_mfma_f32_16x16x32_bf16 v[208:211], a[4:7], a[36:39], v[208:211]
ds_read_b64_tr_b16 a[192:193], v48 offset:2048
ds_read_b64_tr_b16 a[194:195], v48 offset:2560
v_mfma_f32_16x16x32_bf16 v[212:215], a[4:7], a[40:43], v[212:215]
ds_read_b128 a[140:143], v43 offset:4096
v_addc_co_u32 v251, vcc, 0, v251, vcc
v_mfma_f32_16x16x32_bf16 v[136:139], a[20:23], a[8:11], v[136:139]
ds_read_b64_tr_b16 a[196:197], v47 offset:4096
ds_read_b64_tr_b16 a[198:199], v47 offset:4608
v_mfma_f32_16x16x32_bf16 v[140:143], a[24:27], a[8:11], v[140:143]
ds_read_b128 a[160:163], v43 offset:6144
v_readlane_b32 s55, v250, 3
v_mfma_f32_16x16x32_bf16 v[176:179], a[28:31], a[8:11], v[176:179]
buffer_load_dwordx4 v40, s[16:19], s26 offen offset:2048 lds
v_nop
v_mfma_f32_16x16x32_bf16 v[180:183], a[32:35], a[8:11], v[180:183]
ds_read_b64_tr_b16 a[200:201], v48 offset:4096
ds_read_b64_tr_b16 a[202:203], v48 offset:4608
v_mfma_f32_16x16x32_bf16 v[216:219], a[8:11], a[36:39], v[216:219]
v_nop
v_subrev_u32 v252, s55, v250
v_mfma_f32_16x16x32_bf16 v[220:223], a[8:11], a[40:43], v[220:223]
v_cmp_lt_i32 vcc, 0, v252
v_nop
v_mfma_f32_16x16x32_bf16 v[144:147], a[20:23], a[12:15], v[144:147]
v_nop
v_addc_co_u32 v251, vcc, 0, v251, vcc
v_mfma_f32_16x16x32_bf16 v[148:151], a[24:27], a[12:15], v[148:151]
buffer_load_dwordx4 v41, s[16:19], s26 offen offset:3072 lds
v_readlane_b32 s54, v250, 4
v_mfma_f32_16x16x32_bf16 v[184:187], a[28:31], a[12:15], v[184:187]
v_nop
v_nop
v_mfma_f32_16x16x32_bf16 v[188:191], a[32:35], a[12:15], v[188:191]
v_subrev_u32 v252, s54, v250
v_cmp_lt_i32 vcc, 0, v252
v_mfma_f32_16x16x32_bf16 v[224:227], a[12:15], a[36:39], v[224:227]
v_nop
v_nop
v_mfma_f32_16x16x32_bf16 v[228:231], a[12:15], a[40:43], v[228:231]
s_add_u32 m0, s48, 102400
v_addc_co_u32 v251, vcc, 0, v251, vcc
v_readlane_b32 s55, v250, 5
v_mfma_f32_16x16x32_bf16 v[152:155], a[20:23], a[16:19], v[152:155]
buffer_load_dwordx4 v40, s[16:19], s47 offen offset:0 lds
v_nop
v_mfma_f32_16x16x32_bf16 v[156:159], a[24:27], a[16:19], v[156:159]
v_nop
v_subrev_u32 v252, s55, v250
v_mfma_f32_16x16x32_bf16 v[192:195], a[28:31], a[16:19], v[192:195]
v_cmp_lt_i32 vcc, 0, v252
v_nop
v_mfma_f32_16x16x32_bf16 v[196:199], a[32:35], a[16:19], v[196:199]
v_nop
v_addc_co_u32 v251, vcc, 0, v251, vcc
v_mfma_f32_16x16x32_bf16 v[232:235], a[16:19], a[36:39], v[232:235]
buffer_load_dwordx4 v41, s[16:19], s47 offen offset:1024 lds
v_readlane_b32 s54, v250, 6
v_mfma_f32_16x16x32_bf16 v[236:239], a[16:19], a[40:43], v[236:239]
s_waitcnt lgkmcnt(0)
v_nop
v_nop
v_mfma_f32_16x16x32_bf16 v[160:163], a[172:175], a[128:131], v[160:163]
ds_read_b64_tr_b16 a[20:21], v47 offset:14336
ds_read_b64_tr_b16 a[22:23], v47 offset:14848
v_mfma_f32_16x16x32_bf16 v[164:167], a[192:195], a[128:131], v[164:167]
ds_read_b128 a[0:3], v46
v_subrev_u32 v252, s54, v250
v_mfma_f32_16x16x32_bf16 v[200:203], a[128:131], a[196:199], v[200:203]
ds_read_b64_tr_b16 a[24:25], v48 offset:14336
ds_read_b64_tr_b16 a[26:27], v48 offset:14848
v_mfma_f32_16x16x32_bf16 v[204:207], a[128:131], a[200:203], v[204:207]
buffer_load_dwordx4 v42, s[12:15], s25 offen offset:2048 lds
v_cmp_lt_i32 vcc, 0, v252
v_mfma_f32_16x16x32_bf16 v[128:131], a[164:167], a[132:135], v[128:131]
s_add_u32 s26, s26, 393216
s_add_u32 s47, s47, 393216
v_nop
v_mfma_f32_16x16x32_bf16 v[132:135], a[168:171], a[132:135], v[132:135]
s_add_u32 s25, s25, 128
ds_read_b128 a[4:7], v44 offset:0
v_mfma_f32_16x16x32_bf16 v[168:171], a[172:175], a[132:135], v[168:171]
ds_read_b64_tr_b16 a[28:29], v47 offset:16384
ds_read_b64_tr_b16 a[30:31], v47 offset:16896
v_mfma_f32_16x16x32_bf16 v[172:175], a[192:195], a[132:135], v[172:175]
ds_read_b128 a[8:11], v44 offset:2048
v_nop
v_mfma_f32_16x16x32_bf16 v[208:211], a[132:135], a[196:199], v[208:211]
s_add_u32 m0, s91, 16384
v_addc_co_u32 v251, vcc, 0, v251, vcc
v_readlane_b32 s55, v250, 7
v_mfma_f32_16x16x32_bf16 v[212:215], a[132:135], a[200:203], v[212:215]
buffer_load_dwordx4 v36, s[12:15], s24 offen offset:0 lds
v_nop
v_mfma_f32_16x16x32_bf16 v[136:139], a[164:167], a[136:139], v[136:139]
ds_read_b64_tr_b16 a[32:33], v48 offset:16384
ds_read_b64_tr_b16 a[34:35], v48 offset:16896
v_mfma_f32_16x16x32_bf16 v[140:143], a[168:171], a[136:139], v[140:143]
ds_read_b128 a[12:15], v44 offset:4096
v_nop
v_mfma_f32_16x16x32_bf16 v[176:179], a[172:175], a[136:139], v[176:179]
ds_read_b64_tr_b16 a[36:37], v47 offset:18432
ds_read_b64_tr_b16 a[38:39], v47 offset:18944
v_mfma_f32_16x16x32_bf16 v[180:183], a[192:195], a[136:139], v[180:183]
ds_read_b128 a[16:19], v44 offset:6144
v_subrev_u32 v252, s55, v250
v_mfma_f32_16x16x32_bf16 v[216:219], a[136:139], a[196:199], v[216:219]
buffer_load_dwordx4 v37, s[12:15], s24 offen offset:1024 lds
v_cmp_lt_i32 vcc, 0, v252
v_mfma_f32_16x16x32_bf16 v[220:223], a[136:139], a[200:203], v[220:223]
ds_read_b64_tr_b16 a[40:41], v48 offset:18432
ds_read_b64_tr_b16 a[42:43], v48 offset:18944
v_mfma_f32_16x16x32_bf16 v[144:147], a[164:167], a[140:143], v[144:147]
s_cmp_eq_u32 s39, 1
s_mov_b32 s28, 16384
s_cselect_b32 s28, 98304, s28
v_mfma_f32_16x16x32_bf16 v[148:151], a[168:171], a[140:143], v[148:151]
v_add_u32 v43, 16384, v43
v_add_u32 v44, 16384, v44
v_mfma_f32_16x16x32_bf16 v[184:187], a[172:175], a[140:143], v[184:187]
v_add_u32 v47, 98304, v47
v_add_u32 v48, 98304, v48
v_mfma_f32_16x16x32_bf16 v[188:191], a[192:195], a[140:143], v[188:191]
buffer_load_dwordx4 v38, s[12:15], s24 offen offset:2048 lds
v_nop
v_mfma_f32_16x16x32_bf16 v[224:227], a[140:143], a[196:199], v[224:227]
v_add_u32 v45, s28, v45
v_add_u32 v46, s28, v46
v_mfma_f32_16x16x32_bf16 v[228:231], a[140:143], a[200:203], v[228:231]
v_nop
v_addc_co_u32 v251, vcc, 0, v251, vcc
.Lpad16:
s_cbranch_execz .Ltch5
v_mfma_f32_16x16x32_bf16 v[152:155], a[164:167], a[160:163], v[152:155]
v_nop
v_readlane_b32 s54, v251, s86
v_mfma_f32_16x16x32_bf16 v[156:159], a[168:171], a[160:163], v[156:159]
v_mfma_f32_16x16x32_bf16 v[192:195], a[172:175], a[160:163], v[192:195]
s_sub_u32 s55, 7, s54
buffer_load_dwordx4 v39, s[12:15], s24 offen offset:3072 lds
v_mfma_f32_16x16x32_bf16 v[196:199], a[192:195], a[160:163], v[196:199]
s_add_u32 s24, s24, 128
v_cmp_eq_u32 vcc, s55, v251
v_mfma_f32_16x16x32_bf16 v[232:235], a[160:163], a[196:199], v[232:235]
v_mfma_f32_16x16x32_bf16 v[236:239], a[160:163], a[200:203], v[236:239]
s_waitcnt vmcnt(15) lgkmcnt(0)
s_barrier
s_ff1_i32_b64 s38, vcc
v_mfma_f32_16x16x32_bf16 v[160:163], a[28:31], a[0:3], v[160:163]
ds_read_b64_tr_b16 a[164:165], v47 offset:0
ds_read_b64_tr_b16 a[166:167], v47 offset:512
v_mfma_f32_16x16x32_bf16 v[164:167], a[32:35], a[0:3], v[164:167]
s_add_u32 m0, s48, 32768
ds_read_b128 a[128:131], v45
v_mfma_f32_16x16x32_bf16 v[200:203], a[0:3], a[36:39], v[200:203]
s_and_b32 s56, s2, -8
buffer_load_dwordx4 v40, s[16:19], s26 offen offset:0 lds
v_mfma_f32_16x16x32_bf16 v[204:207], a[0:3], a[40:43], v[204:207]
ds_read_b64_tr_b16 a[168:169], v48 offset:0
ds_read_b64_tr_b16 a[170:171], v48 offset:512
v_mfma_f32_16x16x32_bf16 v[128:131], a[20:23], a[4:7], v[128:131]
s_or_b32 s38, s38, s56
ds_read_b128 a[132:135], v43 offset:0
v_mfma_f32_16x16x32_bf16 v[132:135], a[24:27], a[4:7], v[132:135]
ds_read_b64_tr_b16 a[172:173], v47 offset:2048
ds_read_b64_tr_b16 a[174:175], v47 offset:2560
v_mfma_f32_16x16x32_bf16 v[168:171], a[28:31], a[4:7], v[168:171]
s_min_u32 s55, s55, s54
ds_read_b128 a[136:139], v43 offset:2048
v_mfma_f32_16x16x32_bf16 v[172:175], a[32:35], a[4:7], v[172:175]
s_lshr_b32 s54, s54, 2
buffer_load_dwordx4 v41, s[16:19], s26 offen offset:1024 lds
v_mfma_f32_16x16x32_bf16 v[208:211], a[4:7], a[36:39], v[208:211]
ds_read_b64_tr_b16 a[192:193], v48 offset:2048
ds_read_b64_tr_b16 a[194:195], v48 offset:2560
v_mfma_f32_16x16x32_bf16 v[212:215], a[4:7], a[40:43], v[212:215]
s_lshl_b32 s42, s55, 1
ds_read_b128 a[140:143], v43 offset:4096
v_mfma_f32_16x16x32_bf16 v[136:139], a[20:23], a[8:11], v[136:139]
ds_read_b64_tr_b16 a[196:197], v47 offset:4096
ds_read_b64_tr_b16 a[198:199], v47 offset:4608
v_mfma_f32_16x16x32_bf16 v[140:143], a[24:27], a[8:11], v[140:143]
s_or_b32 s42, s42, s54
ds_read_b128 a[160:163], v43 offset:6144
v_mfma_f32_16x16x32_bf16 v[176:179], a[28:31], a[8:11], v[176:179]
s_lshl_b32 s55, s55, 3
buffer_load_dwordx4 v40, s[16:19], s26 offen offset:2048 lds
v_mfma_f32_16x16x32_bf16 v[180:183], a[32:35], a[8:11], v[180:183]
ds_read_b64_tr_b16 a[200:201], v48 offset:4096
ds_read_b64_tr_b16 a[202:203], v48 offset:4608
v_mfma_f32_16x16x32_bf16 v[216:219], a[8:11], a[36:39], v[216:219]
s_lshr_b32 s43, 707472430, s55
s_and_b32 s43, s43, 0xff
v_mfma_f32_16x16x32_bf16 v[220:223], a[8:11], a[40:43], v[220:223]
v_mfma_f32_16x16x32_bf16 v[144:147], a[20:23], a[12:15], v[144:147]
v_mfma_f32_16x16x32_bf16 v[148:151], a[24:27], a[12:15], v[148:151]
buffer_load_dwordx4 v41, s[16:19], s26 offen offset:3072 lds
v_mfma_f32_16x16x32_bf16 v[184:187], a[28:31], a[12:15], v[184:187]
v_mfma_f32_16x16x32_bf16 v[188:191], a[32:35], a[12:15], v[188:191]
v_mfma_f32_16x16x32_bf16 v[224:227], a[12:15], a[36:39], v[224:227]
v_mfma_f32_16x16x32_bf16 v[228:231], a[12:15], a[40:43], v[228:231]
s_add_u32 m0, s48, 36864
v_mfma_f32_16x16x32_bf16 v[152:155], a[20:23], a[16:19], v[152:155]
buffer_load_dwordx4 v40, s[16:19], s47 offen offset:0 lds
v_mfma_f32_16x16x32_bf16 v[156:159], a[24:27], a[16:19], v[156:159]
v_mfma_f32_16x16x32_bf16 v[192:195], a[28:31], a[16:19], v[192:195]
v_mfma_f32_16x16x32_bf16 v[196:199], a[32:35], a[16:19], v[196:199]
v_mfma_f32_16x16x32_bf16 v[232:235], a[16:19], a[36:39], v[232:235]
buffer_load_dwordx4 v41, s[16:19], s47 offen offset:1024 lds
v_mfma_f32_16x16x32_bf16 v[236:239], a[16:19], a[40:43], v[236:239]
s_waitcnt lgkmcnt(0)
v_mfma_f32_16x16x32_bf16 v[160:163], a[172:175], a[128:131], v[160:163]
ds_read_b64_tr_b16 a[20:21], v47 offset:14336
ds_read_b64_tr_b16 a[22:23], v47 offset:14848
v_mfma_f32_16x16x32_bf16 v[164:167], a[192:195], a[128:131], v[164:167]
ds_read_b128 a[0:3], v46
v_mfma_f32_16x16x32_bf16 v[200:203], a[128:131], a[196:199], v[200:203]
ds_read_b64_tr_b16 a[24:25], v48 offset:14336
ds_read_b64_tr_b16 a[26:27], v48 offset:14848
v_mfma_f32_16x16x32_bf16 v[204:207], a[128:131], a[200:203], v[204:207]
buffer_load_dwordx4 v42, s[12:15], s25 offen offset:2048 lds
v_mfma_f32_16x16x32_bf16 v[128:131], a[164:167], a[132:135], v[128:131]
s_add_u32 s26, s26, 393216
s_add_u32 s47, s47, 393216
v_mfma_f32_16x16x32_bf16 v[132:135], a[168:171], a[132:135], v[132:135]
s_add_u32 s25, s25, 128
ds_read_b128 a[4:7], v44 offset:0
v_mfma_f32_16x16x32_bf16 v[168:171], a[172:175], a[132:135], v[168:171]
ds_read_b64_tr_b16 a[28:29], v47 offset:16384
ds_read_b64_tr_b16 a[30:31], v47 offset:16896
v_mfma_f32_16x16x32_bf16 v[172:175], a[192:195], a[132:135], v[172:175]
ds_read_b128 a[8:11], v44 offset:2048
v_mfma_f32_16x16x32_bf16 v[208:211], a[132:135], a[196:199], v[208:211]
s_add_u32 m0, s91, 65536
v_mfma_f32_16x16x32_bf16 v[212:215], a[132:135], a[200:203], v[212:215]
buffer_load_dwordx4 v36, s[12:15], s24 offen offset:0 lds
v_mfma_f32_16x16x32_bf16 v[136:139], a[164:167], a[136:139], v[136:139]
ds_read_b64_tr_b16 a[32:33], v48 offset:16384
ds_read_b64_tr_b16 a[34:35], v48 offset:16896
v_mfma_f32_16x16x32_bf16 v[140:143], a[168:171], a[136:139], v[140:143]
ds_read_b128 a[12:15], v44 offset:4096
v_mfma_f32_16x16x32_bf16 v[176:179], a[172:175], a[136:139], v[176:179]
ds_read_b64_tr_b16 a[36:37], v47 offset:18432
ds_read_b64_tr_b16 a[38:39], v47 offset:18944
v_mfma_f32_16x16x32_bf16 v[180:183], a[192:195], a[136:139], v[180:183]
ds_read_b128 a[16:19], v44 offset:6144
v_mfma_f32_16x16x32_bf16 v[216:219], a[136:139], a[196:199], v[216:219]
buffer_load_dwordx4 v37, s[12:15], s24 offen offset:1024 lds
v_mfma_f32_16x16x32_bf16 v[220:223], a[136:139], a[200:203], v[220:223]
ds_read_b64_tr_b16 a[40:41], v48 offset:18432
ds_read_b64_tr_b16 a[42:43], v48 offset:18944
v_mfma_f32_16x16x32_bf16 v[144:147], a[164:167], a[140:143], v[144:147]
s_cmp_eq_u32 s39, 1
s_mov_b32 s28, 4294885376
s_cselect_b32 s28, 4294934528, s28
v_mfma_f32_16x16x32_bf16 v[148:151], a[168:171], a[140:143], v[148:151]
v_add_u32 v43, 4294885376, v43
v_add_u32 v44, 4294885376, v44
v_mfma_f32_16x16x32_bf16 v[184:187], a[172:175], a[140:143], v[184:187]
v_add_u32 v47, 4294934528, v47
v_add_u32 v48, 4294934528, v48
v_mfma_f32_16x16x32_bf16 v[188:191], a[192:195], a[140:143], v[188:191]
buffer_load_dwordx4 v38, s[12:15], s24 offen offset:2048 lds
v_mfma_f32_16x16x32_bf16 v[224:227], a[140:143], a[196:199], v[224:227]
v_add_u32 v45, s28, v45
v_add_u32 v46, s28, v46
v_mfma_f32_16x16x32_bf16 v[228:231], a[140:143], a[200:203], v[228:231]
v_mfma_f32_16x16x32_bf16 v[152:155], a[164:167], a[160:163], v[152:155]
s_bfe_u32 s51, s42, 0x10001
s_lshl_b32 s51, s51, 3
s_and_b32 s54, s87, 7
v_mfma_f32_16x16x32_bf16 v[156:159], a[168:171], a[160:163], v[156:159]
s_or_b32 s51, s51, s54
s_lshr_b32 s52, s42, 2
s_lshl_b32 s52, s52, 2
v_mfma_f32_16x16x32_bf16 v[192:195], a[172:175], a[160:163], v[192:195]
s_lshr_b32 s54, s87, 3
buffer_load_dwordx4 v39, s[12:15], s24 offen offset:3072 lds
v_mfma_f32_16x16x32_bf16 v[196:199], a[192:195], a[160:163], v[196:199]
s_add_u32 s24, s24, 128
s_or_b32 s52, s52, s54
s_lshl_b32 s52, s52, 8
v_mfma_f32_16x16x32_bf16 v[232:235], a[160:163], a[196:199], v[232:235]
s_and_b32 s55, s42, 1
s_lshl_b32 s54, s51, 2
s_add_u32 s54, s54, s3
v_mfma_f32_16x16x32_bf16 v[236:239], a[160:163], a[200:203], v[236:239]
s_waitcnt vmcnt(15) lgkmcnt(0)
s_barrier
s_lshl_b32 s54, s54, 18
v_mfma_f32_16x16x32_bf16 v[160:163], a[28:31], a[0:3], v[160:163]
ds_read_b64_tr_b16 a[164:165], v47 offset:0
ds_read_b64_tr_b16 a[166:167], v47 offset:512
v_mfma_f32_16x16x32_bf16 v[164:167], a[32:35], a[0:3], v[164:167]
s_add_u32 m0, s48, 131072
ds_read_b128 a[128:131], v45
v_mfma_f32_16x16x32_bf16 v[200:203], a[0:3], a[36:39], v[200:203]
s_add_u32 s54, s54, 33554432
buffer_load_dwordx4 v40, s[16:19], s26 offen offset:0 lds
v_mfma_f32_16x16x32_bf16 v[204:207], a[0:3], a[40:43], v[204:207]
ds_read_b64_tr_b16 a[168:169], v48 offset:0
ds_read_b64_tr_b16 a[170:171], v48 offset:512
v_mfma_f32_16x16x32_bf16 v[128:131], a[20:23], a[4:7], v[128:131]
ds_read_b128 a[132:135], v43 offset:0
v_mfma_f32_16x16x32_bf16 v[132:135], a[24:27], a[4:7], v[132:135]
ds_read_b64_tr_b16 a[172:173], v47 offset:2048
ds_read_b64_tr_b16 a[174:175], v47 offset:2560
v_mfma_f32_16x16x32_bf16 v[168:171], a[28:31], a[4:7], v[168:171]
ds_read_b128 a[136:139], v43 offset:2048
v_mfma_f32_16x16x32_bf16 v[172:175], a[32:35], a[4:7], v[172:175]
buffer_load_dwordx4 v41, s[16:19], s26 offen offset:1024 lds
v_mfma_f32_16x16x32_bf16 v[208:211], a[4:7], a[36:39], v[208:211]
ds_read_b64_tr_b16 a[192:193], v48 offset:2048
ds_read_b64_tr_b16 a[194:195], v48 offset:2560
v_mfma_f32_16x16x32_bf16 v[212:215], a[4:7], a[40:43], v[212:215]
ds_read_b128 a[140:143], v43 offset:4096
v_mfma_f32_16x16x32_bf16 v[136:139], a[20:23], a[8:11], v[136:139]
ds_read_b64_tr_b16 a[196:197], v47 offset:4096
ds_read_b64_tr_b16 a[198:199], v47 offset:4608
v_mfma_f32_16x16x32_bf16 v[140:143], a[24:27], a[8:11], v[140:143]
ds_read_b128 a[160:163], v43 offset:6144
v_mfma_f32_16x16x32_bf16 v[176:179], a[28:31], a[8:11], v[176:179]
buffer_load_dwordx4 v40, s[16:19], s26 offen offset:2048 lds
v_mfma_f32_16x16x32_bf16 v[180:183], a[32:35], a[8:11], v[180:183]
ds_read_b64_tr_b16 a[200:201], v48 offset:4096
ds_read_b64_tr_b16 a[202:203], v48 offset:4608
v_mfma_f32_16x16x32_bf16 v[216:219], a[8:11], a[36:39], v[216:219]
s_add_u32 s4, s84, s54
s_addc_u32 s5, s85, 0
s_and_b32 s5, s5, 0xffff
v_mfma_f32_16x16x32_bf16 v[220:223], a[8:11], a[40:43], v[220:223]
s_lshl_b32 s54, s3, 15
s_add_u32 s54, s54, s52
v_mfma_f32_16x16x32_bf16 v[144:147], a[20:23], a[12:15], v[144:147]
s_add_u32 s8, s80, s54
s_addc_u32 s9, s81, 0
s_and_b32 s9, s9, 0xffff
v_mfma_f32_16x16x32_bf16 v[148:151], a[24:27], a[12:15], v[148:151]
s_lshl_b32 s56, s43, 12
buffer_load_dwordx4 v41, s[16:19], s26 offen offset:3072 lds
v_mfma_f32_16x16x32_bf16 v[184:187], a[28:31], a[12:15], v[184:187]
s_lshl_b32 s57, s43, 17
s_mul_i32 s44, s55, s56
s_mul_i32 s45, s55, s57
v_mfma_f32_16x16x32_bf16 v[188:191], a[32:35], a[12:15], v[188:191]
s_cmp_eq_u32 s55, 0
s_cselect_b32 s6, s56, 0x40000
s_cselect_b32 s57, s57, 0x800000
v_mfma_f32_16x16x32_bf16 v[224:227], a[12:15], a[36:39], v[224:227]
s_sub_u32 s10, s57, s54
s_and_b32 s54, s3, 1
s_lshl_b32 s54, s54, 13
v_mfma_f32_16x16x32_bf16 v[228:231], a[12:15], a[40:43], v[228:231]
s_add_u32 m0, s48, 135168
s_add_u32 s54, s54, 98304
s_lshr_b32 s56, s3, 1
v_mfma_f32_16x16x32_bf16 v[152:155], a[20:23], a[16:19], v[152:155]
s_lshl_b32 s56, s56, 2
buffer_load_dwordx4 v40, s[16:19], s47 offen offset:0 lds
v_mfma_f32_16x16x32_bf16 v[156:159], a[24:27], a[16:19], v[156:159]
s_sub_u32 s57, 15, s51
s_min_u32 s57, s57, s51
s_lshl_b32 s57, s57, 10
v_mfma_f32_16x16x32_bf16 v[192:195], a[28:31], a[16:19], v[192:195]
v_lshrrev_b32 v252, 4, v1
v_and_b32 v253, 15, v1
v_mfma_f32_16x16x32_bf16 v[196:199], a[32:35], a[16:19], v[196:199]
v_lshlrev_b32 v254, 1, v252
v_xor_b32 v254, v254, v253
v_mfma_f32_16x16x32_bf16 v[232:235], a[16:19], a[36:39], v[232:235]
buffer_load_dwordx4 v41, s[16:19], s47 offen offset:1024 lds
v_lshlrev_b32 v254, 4, v254
v_mfma_f32_16x16x32_bf16 v[236:239], a[16:19], a[40:43], v[236:239]
s_waitcnt lgkmcnt(0)
v_lshl_add_u32 v240, v252, 11, v254
v_xor_b32 v241, 0x80, v240
v_mfma_f32_16x16x32_bf16 v[160:163], a[172:175], a[128:131], v[160:163]
ds_read_b64_tr_b16 a[20:21], v47 offset:14336
ds_read_b64_tr_b16 a[22:23], v47 offset:14848
v_mfma_f32_16x16x32_bf16 v[164:167], a[192:195], a[128:131], v[164:167]
ds_read_b128 a[0:3], v46
v_add_u32 v241, 7168, v241
v_mfma_f32_16x16x32_bf16 v[200:203], a[128:131], a[196:199], v[200:203]
ds_read_b64_tr_b16 a[24:25], v48 offset:14336
ds_read_b64_tr_b16 a[26:27], v48 offset:14848
v_mfma_f32_16x16x32_bf16 v[204:207], a[128:131], a[200:203], v[204:207]
buffer_load_dwordx4 v42, s[12:15], s25 offen offset:2048 lds
v_add_u32 v242, 14336, v240
v_mfma_f32_16x16x32_bf16 v[128:131], a[164:167], a[132:135], v[128:131]
s_add_u32 s26, s26, 393216
s_add_u32 s47, s47, 393216
v_add_u32 v243, 14336, v241
v_mfma_f32_16x16x32_bf16 v[132:135], a[168:171], a[132:135], v[132:135]
s_add_u32 s25, s25, 128
ds_read_b128 a[4:7], v44 offset:0
v_mfma_f32_16x16x32_bf16 v[168:171], a[172:175], a[132:135], v[168:171]
ds_read_b64_tr_b16 a[28:29], v47 offset:16384
ds_read_b64_tr_b16 a[30:31], v47 offset:16896
v_mfma_f32_16x16x32_bf16 v[172:175], a[192:195], a[132:135], v[172:175]
ds_read_b128 a[8:11], v44 offset:2048
v_add_u32 v244, s54, v7
v_mfma_f32_16x16x32_bf16 v[208:211], a[132:135], a[196:199], v[208:211]
s_add_u32 m0, s91, 81920
v_bfe_u32 v254, v1, 2, 3
v_lshrrev_b32 v255, 2, v1
v_mfma_f32_16x16x32_bf16 v[212:215], a[132:135], a[200:203], v[212:215]
buffer_load_dwordx4 v36, s[12:15], s24 offen offset:0 lds
v_lshlrev_b32 v255, 8, v255
v_mfma_f32_16x16x32_bf16 v[136:139], a[164:167], a[136:139], v[136:139]
ds_read_b64_tr_b16 a[32:33], v48 offset:16384
ds_read_b64_tr_b16 a[34:35], v48 offset:16896
v_mfma_f32_16x16x32_bf16 v[140:143], a[168:171], a[136:139], v[140:143]
ds_read_b128 a[12:15], v44 offset:4096
v_and_b32 v253, 3, v1
v_mfma_f32_16x16x32_bf16 v[176:179], a[172:175], a[136:139], v[176:179]
ds_read_b64_tr_b16 a[36:37], v47 offset:18432
ds_read_b64_tr_b16 a[38:39], v47 offset:18944
v_mfma_f32_16x16x32_bf16 v[180:183], a[192:195], a[136:139], v[180:183]
ds_read_b128 a[16:19], v44 offset:6144
v_lshl_add_u32 v255, v253, 3, v255
v_mfma_f32_16x16x32_bf16 v[216:219], a[136:139], a[196:199], v[216:219]
buffer_load_dwordx4 v37, s[12:15], s24 offen offset:1024 lds
v_add_u32 v255, 114688, v255
v_mfma_f32_16x16x32_bf16 v[220:223], a[136:139], a[200:203], v[220:223]
ds_read_b64_tr_b16 a[40:41], v48 offset:18432
ds_read_b64_tr_b16 a[42:43], v48 offset:18944
v_mfma_f32_16x16x32_bf16 v[144:147], a[164:167], a[140:143], v[144:147]
s_cmp_eq_u32 s39, 1
s_mov_b32 s28, 16384
s_cselect_b32 s28, 4294901760, s28
v_mfma_f32_16x16x32_bf16 v[148:151], a[168:171], a[140:143], v[148:151]
v_add_u32 v43, 16384, v43
v_add_u32 v44, 16384, v44
v_mfma_f32_16x16x32_bf16 v[184:187], a[172:175], a[140:143], v[184:187]
v_add_u32 v47, 4294901760, v47
v_add_u32 v48, 4294901760, v48
v_mfma_f32_16x16x32_bf16 v[188:191], a[192:195], a[140:143], v[188:191]
buffer_load_dwordx4 v38, s[12:15], s24 offen offset:2048 lds
v_xor_b32 v254, s56, v254
v_mfma_f32_16x16x32_bf16 v[224:227], a[140:143], a[196:199], v[224:227]
v_add_u32 v45, s28, v45
v_add_u32 v46, s28, v46
v_mfma_f32_16x16x32_bf16 v[228:231], a[140:143], a[200:203], v[228:231]
v_xor_b32 v253, 0, v254
v_lshl_add_u32 v245, v253, 5, v255
v_mfma_f32_16x16x32_bf16 v[152:155], a[164:167], a[160:163], v[152:155]
v_xor_b32 v253, 1, v254
v_lshl_add_u32 v246, v253, 5, v255
v_mfma_f32_16x16x32_bf16 v[156:159], a[168:171], a[160:163], v[156:159]
v_xor_b32 v253, 2, v254
v_lshl_add_u32 v247, v253, 5, v255
v_mfma_f32_16x16x32_bf16 v[192:195], a[172:175], a[160:163], v[192:195]
buffer_load_dwordx4 v39, s[12:15], s24 offen offset:3072 lds
v_xor_b32 v253, 3, v254
v_mfma_f32_16x16x32_bf16 v[196:199], a[192:195], a[160:163], v[196:199]
s_add_u32 s24, s24, 128
v_lshl_add_u32 v248, v253, 5, v255
v_and_b32 v253, 31, v1
v_mfma_f32_16x16x32_bf16 v[232:235], a[160:163], a[196:199], v[232:235]
v_lshl_add_u32 v249, v253, 5, s57
v_mfma_f32_16x16x32_bf16 v[236:239], a[160:163], a[200:203], v[236:239]
s_waitcnt vmcnt(15) lgkmcnt(0)
s_barrier
v_mfma_f32_16x16x32_bf16 v[160:163], a[28:31], a[0:3], v[160:163]
ds_read_b64_tr_b16 a[164:165], v47 offset:0
ds_read_b64_tr_b16 a[166:167], v47 offset:512
v_mfma_f32_16x16x32_bf16 v[164:167], a[32:35], a[0:3], v[164:167]
s_add_u32 m0, s48, 98304
ds_read_b128 a[128:131], v45
v_mfma_f32_16x16x32_bf16 v[200:203], a[0:3], a[36:39], v[200:203]
buffer_load_dwordx4 v40, s[16:19], s26 offen offset:0 lds
v_mfma_f32_16x16x32_bf16 v[204:207], a[0:3], a[40:43], v[204:207]
ds_read_b64_tr_b16 a[168:169], v48 offset:0
ds_read_b64_tr_b16 a[170:171], v48 offset:512
v_mfma_f32_16x16x32_bf16 v[128:131], a[20:23], a[4:7], v[128:131]
ds_read_b128 a[132:135], v43 offset:0
v_mfma_f32_16x16x32_bf16 v[132:135], a[24:27], a[4:7], v[132:135]
ds_read_b64_tr_b16 a[172:173], v47 offset:2048
ds_read_b64_tr_b16 a[174:175], v47 offset:2560
v_mfma_f32_16x16x32_bf16 v[168:171], a[28:31], a[4:7], v[168:171]
ds_read_b128 a[136:139], v43 offset:2048
v_mfma_f32_16x16x32_bf16 v[172:175], a[32:35], a[4:7], v[172:175]
buffer_load_dwordx4 v41, s[16:19], s26 offen offset:1024 lds
v_mfma_f32_16x16x32_bf16 v[208:211], a[4:7], a[36:39], v[208:211]
ds_read_b64_tr_b16 a[192:193], v48 offset:2048
ds_read_b64_tr_b16 a[194:195], v48 offset:2560
v_mfma_f32_16x16x32_bf16 v[212:215], a[4:7], a[40:43], v[212:215]
ds_read_b128 a[140:143], v43 offset:4096
v_mfma_f32_16x16x32_bf16 v[136:139], a[20:23], a[8:11], v[136:139]
ds_read_b64_tr_b16 a[196:197], v47 offset:4096
ds_read_b64_tr_b16 a[198:199], v47 offset:4608
v_mfma_f32_16x16x32_bf16 v[140:143], a[24:27], a[8:11], v[140:143]
ds_read_b128 a[160:163], v43 offset:6144
v_mfma_f32_16x16x32_bf16 v[176:179], a[28:31], a[8:11], v[176:179]
buffer_load_dwordx4 v40, s[16:19], s26 offen offset:2048 lds
v_mfma_f32_16x16x32_bf16 v[180:183], a[32:35], a[8:11], v[180:183]
ds_read_b64_tr_b16 a[200:201], v48 offset:4096
ds_read_b64_tr_b16 a[202:203], v48 offset:4608
v_mfma_f32_16x16x32_bf16 v[216:219], a[8:11], a[36:39], v[216:219]
v_mfma_f32_16x16x32_bf16 v[220:223], a[8:11], a[40:43], v[220:223]
v_mfma_f32_16x16x32_bf16 v[144:147], a[20:23], a[12:15], v[144:147]
v_mfma_f32_16x16x32_bf16 v[148:151], a[24:27], a[12:15], v[148:151]
buffer_load_dwordx4 v41, s[16:19], s26 offen offset:3072 lds
v_mfma_f32_16x16x32_bf16 v[184:187], a[28:31], a[12:15], v[184:187]
v_mfma_f32_16x16x32_bf16 v[188:191], a[32:35], a[12:15], v[188:191]
v_mfma_f32_16x16x32_bf16 v[224:227], a[12:15], a[36:39], v[224:227]
v_mfma_f32_16x16x32_bf16 v[228:231], a[12:15], a[40:43], v[228:231]
s_add_u32 m0, s48, 102400
v_mfma_f32_16x16x32_bf16 v[152:155], a[20:23], a[16:19], v[152:155]
buffer_load_dwordx4 v40, s[16:19], s47 offen offset:0 lds
v_mfma_f32_16x16x32_bf16 v[156:159], a[24:27], a[16:19], v[156:159]
v_mfma_f32_16x16x32_bf16 v[192:195], a[28:31], a[16:19], v[192:195]
buffer_load_dword v35, v4, s[96:99], 0 offen
v_mfma_f32_16x16x32_bf16 v[196:199], a[32:35], a[16:19], v[196:199]
v_mfma_f32_16x16x32_bf16 v[232:235], a[16:19], a[36:39], v[232:235]
buffer_load_dwordx4 v41, s[16:19], s47 offen offset:1024 lds
v_mfma_f32_16x16x32_bf16 v[236:239], a[16:19], a[40:43], v[236:239]
s_waitcnt lgkmcnt(0)
v_mfma_f32_16x16x32_bf16 v[160:163], a[172:175], a[128:131], v[160:163]
ds_read_b64_tr_b16 a[20:21], v47 offset:14336
ds_read_b64_tr_b16 a[22:23], v47 offset:14848
v_mfma_f32_16x16x32_bf16 v[164:167], a[192:195], a[128:131], v[164:167]
ds_read_b128 a[0:3], v46
v_mfma_f32_16x16x32_bf16 v[200:203], a[128:131], a[196:199], v[200:203]
ds_read_b64_tr_b16 a[24:25], v48 offset:14336
ds_read_b64_tr_b16 a[26:27], v48 offset:14848
v_mfma_f32_16x16x32_bf16 v[204:207], a[128:131], a[200:203], v[204:207]
buffer_load_dwordx4 v42, s[12:15], s25 offen offset:2048 lds
v_mfma_f32_16x16x32_bf16 v[128:131], a[164:167], a[132:135], v[128:131]
s_add_u32 s26, s26, 393216
s_add_u32 s47, s47, 393216
v_mfma_f32_16x16x32_bf16 v[132:135], a[168:171], a[132:135], v[132:135]
s_add_u32 s25, s25, 128
ds_read_b128 a[4:7], v44 offset:0
v_mfma_f32_16x16x32_bf16 v[168:171], a[172:175], a[132:135], v[168:171]
ds_read_b64_tr_b16 a[28:29], v47 offset:16384
ds_read_b64_tr_b16 a[30:31], v47 offset:16896
v_mfma_f32_16x16x32_bf16 v[172:175], a[192:195], a[132:135], v[172:175]
ds_read_b128 a[8:11], v44 offset:2048
v_mfma_f32_16x16x32_bf16 v[208:211], a[132:135], a[196:199], v[208:211]
s_add_u32 m0, s91, 0
v_mfma_f32_16x16x32_bf16 v[212:215], a[132:135], a[200:203], v[212:215]
buffer_load_dwordx4 v36, s[12:15], s24 offen offset:0 lds
v_mfma_f32_16x16x32_bf16 v[136:139], a[164:167], a[136:139], v[136:139]
ds_read_b64_tr_b16 a[32:33], v48 offset:16384
ds_read_b64_tr_b16 a[34:35], v48 offset:16896
v_mfma_f32_16x16x32_bf16 v[140:143], a[168:171], a[136:139], v[140:143]
ds_read_b128 a[12:15], v44 offset:4096
v_mfma_f32_16x16x32_bf16 v[176:179], a[172:175], a[136:139], v[176:179]
ds_read_b64_tr_b16 a[36:37], v47 offset:18432
ds_read_b64_tr_b16 a[38:39], v47 offset:18944
v_mfma_f32_16x16x32_bf16 v[180:183], a[192:195], a[136:139], v[180:183]
ds_read_b128 a[16:19], v44 offset:6144
v_mfma_f32_16x16x32_bf16 v[216:219], a[136:139], a[196:199], v[216:219]
buffer_load_dwordx4 v37, s[12:15], s24 offen offset:1024 lds
v_mfma_f32_16x16x32_bf16 v[220:223], a[136:139], a[200:203], v[220:223]
ds_read_b64_tr_b16 a[40:41], v48 offset:18432
ds_read_b64_tr_b16 a[42:43], v48 offset:18944
v_mfma_f32_16x16x32_bf16 v[144:147], a[164:167], a[140:143], v[144:147]
s_cmp_eq_u32 s39, 1
s_mov_b32 s28, 49152
s_cselect_b32 s28, 98304, s28
v_mfma_f32_16x16x32_bf16 v[148:151], a[168:171], a[140:143], v[148:151]
v_add_u32 v43, 49152, v43
v_add_u32 v44, 49152, v44
v_mfma_f32_16x16x32_bf16 v[184:187], a[172:175], a[140:143], v[184:187]
v_add_u32 v47, 98304, v47
v_add_u32 v48, 98304, v48
v_mfma_f32_16x16x32_bf16 v[188:191], a[192:195], a[140:143], v[188:191]
buffer_load_dwordx4 v38, s[12:15], s24 offen offset:2048 lds
v_mfma_f32_16x16x32_bf16 v[224:227], a[140:143], a[196:199], v[224:227]
v_add_u32 v45, s28, v45
v_add_u32 v46, s28, v46
v_mfma_f32_16x16x32_bf16 v[228:231], a[140:143], a[200:203], v[228:231]
v_mfma_f32_16x16x32_bf16 v[152:155], a[164:167], a[160:163], v[152:155]
v_mfma_f32_16x16x32_bf16 v[156:159], a[168:171], a[160:163], v[156:159]
v_mfma_f32_16x16x32_bf16 v[192:195], a[172:175], a[160:163], v[192:195]
buffer_load_dwordx4 v39, s[12:15], s24 offen offset:3072 lds
v_mfma_f32_16x16x32_bf16 v[196:199], a[192:195], a[160:163], v[196:199]
s_add_u32 s24, s24, 128
v_mfma_f32_16x16x32_bf16 v[232:235], a[160:163], a[196:199], v[232:235]
v_mfma_f32_16x16x32_bf16 v[236:239], a[160:163], a[200:203], v[236:239]
s_waitcnt vmcnt(16) lgkmcnt(0)
s_barrier
v_mfma_f32_16x16x32_bf16 v[160:163], a[28:31], a[0:3], v[160:163]
ds_read_b64_tr_b16 a[164:165], v47 offset:0
ds_read_b64_tr_b16 a[166:167], v47 offset:512
v_mfma_f32_16x16x32_bf16 v[164:167], a[32:35], a[0:3], v[164:167]
s_add_u32 m0, s48, 32768
ds_read_b128 a[128:131], v45
v_mfma_f32_16x16x32_bf16 v[200:203], a[0:3], a[36:39], v[200:203]
buffer_load_dwordx4 v40, s[16:19], s26 offen offset:0 lds
v_mfma_f32_16x16x32_bf16 v[204:207], a[0:3], a[40:43], v[204:207]
ds_read_b64_tr_b16 a[168:169], v48 offset:0
ds_read_b64_tr_b16 a[170:171], v48 offset:512
v_mfma_f32_16x16x32_bf16 v[128:131], a[20:23], a[4:7], v[128:131]
ds_read_b128 a[132:135], v43 offset:0
v_mfma_f32_16x16x32_bf16 v[132:135], a[24:27], a[4:7], v[132:135]
ds_read_b64_tr_b16 a[172:173], v47 offset:2048
ds_read_b64_tr_b16 a[174:175], v47 offset:2560
v_mfma_f32_16x16x32_bf16 v[168:171], a[28:31], a[4:7], v[168:171]
ds_read_b128 a[136:139], v43 offset:2048
v_mfma_f32_16x16x32_bf16 v[172:175], a[32:35], a[4:7], v[172:175]
buffer_load_dwordx4 v41, s[16:19], s26 offen offset:1024 lds
v_mfma_f32_16x16x32_bf16 v[208:211], a[4:7], a[36:39], v[208:211]
ds_read_b64_tr_b16 a[192:193], v48 offset:2048
ds_read_b64_tr_b16 a[194:195], v48 offset:2560
v_mfma_f32_16x16x32_bf16 v[212:215], a[4:7], a[40:43], v[212:215]
ds_read_b128 a[140:143], v43 offset:4096
v_mfma_f32_16x16x32_bf16 v[136:139], a[20:23], a[8:11], v[136:139]
ds_read_b64_tr_b16 a[196:197], v47 offset:4096
ds_read_b64_tr_b16 a[198:199], v47 offset:4608
v_mfma_f32_16x16x32_bf16 v[140:143], a[24:27], a[8:11], v[140:143]
ds_read_b128 a[160:163], v43 offset:6144
v_mfma_f32_16x16x32_bf16 v[176:179], a[28:31], a[8:11], v[176:179]
buffer_load_dwordx4 v40, s[16:19], s26 offen offset:2048 lds
v_mfma_f32_16x16x32_bf16 v[180:183], a[32:35], a[8:11], v[180:183]
ds_read_b64_tr_b16 a[200:201], v48 offset:4096
ds_read_b64_tr_b16 a[202:203], v48 offset:4608
v_mfma_f32_16x16x32_bf16 v[216:219], a[8:11], a[36:39], v[216:219]
v_mfma_f32_16x16x32_bf16 v[220:223], a[8:11], a[40:43], v[220:223]
v_mfma_f32_16x16x32_bf16 v[144:147], a[20:23], a[12:15], v[144:147]
v_mfma_f32_16x16x32_bf16 v[148:151], a[24:27], a[12:15], v[148:151]
buffer_load_dwordx4 v41, s[16:19], s26 offen offset:3072 lds
v_mfma_f32_16x16x32_bf16 v[184:187], a[28:31], a[12:15], v[184:187]
v_mfma_f32_16x16x32_bf16 v[188:191], a[32:35], a[12:15], v[188:191]
v_mfma_f32_16x16x32_bf16 v[224:227], a[12:15], a[36:39], v[224:227]
v_mfma_f32_16x16x32_bf16 v[228:231], a[12:15], a[40:43], v[228:231]
s_add_u32 m0, s48, 36864
v_mfma_f32_16x16x32_bf16 v[152:155], a[20:23], a[16:19], v[152:155]
buffer_load_dwordx4 v40, s[16:19], s47 offen offset:0 lds
v_mfma_f32_16x16x32_bf16 v[156:159], a[24:27], a[16:19], v[156:159]
v_mfma_f32_16x16x32_bf16 v[192:195], a[28:31], a[16:19], v[192:195]
v_mfma_f32_16x16x32_bf16 v[196:199], a[32:35], a[16:19], v[196:199]
v_mfma_f32_16x16x32_bf16 v[232:235], a[16:19], a[36:39], v[232:235]
buffer_load_dwordx4 v41, s[16:19], s47 offen offset:1024 lds
v_mfma_f32_16x16x32_bf16 v[236:239], a[16:19], a[40:43], v[236:239]
s_waitcnt lgkmcnt(0)
v_mfma_f32_16x16x32_bf16 v[160:163], a[172:175], a[128:131], v[160:163]
ds_read_b64_tr_b16 a[20:21], v47 offset:14336
ds_read_b64_tr_b16 a[22:23], v47 offset:14848
v_mfma_f32_16x16x32_bf16 v[164:167], a[192:195], a[128:131], v[164:167]
ds_read_b128 a[0:3], v46
v_and_b32 v28, 15, v1
v_mfma_f32_16x16x32_bf16 v[200:203], a[128:131], a[196:199], v[200:203]
ds_read_b64_tr_b16 a[24:25], v48 offset:14336
ds_read_b64_tr_b16 a[26:27], v48 offset:14848
v_mfma_f32_16x16x32_bf16 v[204:207], a[128:131], a[200:203], v[204:207]
buffer_load_dwordx4 v42, s[12:15], s25 offen offset:2048 lds
v_lshrrev_b32 v29, 4, v1
v_mfma_f32_16x16x32_bf16 v[128:131], a[164:167], a[132:135], v[128:131]
s_add_u32 s26, s26, 393216
s_add_u32 s47, s47, 393216
v_lshlrev_b32 v29, 2, v29
v_mfma_f32_16x16x32_bf16 v[132:135], a[168:171], a[132:135], v[132:135]
s_add_u32 s25, s25, 128
ds_read_b128 a[4:7], v44 offset:0
v_mfma_f32_16x16x32_bf16 v[168:171], a[172:175], a[132:135], v[168:171]
ds_read_b64_tr_b16 a[28:29], v47 offset:16384
ds_read_b64_tr_b16 a[30:31], v47 offset:16896
v_mfma_f32_16x16x32_bf16 v[172:175], a[192:195], a[132:135], v[172:175]
ds_read_b128 a[8:11], v44 offset:2048
v_sub_u32 v28, v28, v29
v_mfma_f32_16x16x32_bf16 v[208:211], a[132:135], a[196:199], v[208:211]
s_add_u32 m0, s91, 16384
v_mov_b32 v30, 0
v_add_u32 v29, 16, v28
v_mfma_f32_16x16x32_bf16 v[212:215], a[132:135], a[200:203], v[212:215]
buffer_load_dwordx4 v36, s[12:15], s24 offen offset:0 lds
v_and_b32 v29, 0xfffffff1, v29
v_mfma_f32_16x16x32_bf16 v[136:139], a[164:167], a[136:139], v[136:139]
ds_read_b64_tr_b16 a[32:33], v48 offset:16384
ds_read_b64_tr_b16 a[34:35], v48 offset:16896
v_mfma_f32_16x16x32_bf16 v[140:143], a[168:171], a[136:139], v[140:143]
ds_read_b128 a[12:15], v44 offset:4096
v_cmp_eq_u32 vcc, 0, v29
v_mfma_f32_16x16x32_bf16 v[176:179], a[172:175], a[136:139], v[176:179]
ds_read_b64_tr_b16 a[36:37], v47 offset:18432
ds_read_b64_tr_b16 a[38:39], v47 offset:18944
v_mfma_f32_16x16x32_bf16 v[180:183], a[192:195], a[136:139], v[180:183]
ds_read_b128 a[16:19], v44 offset:6144
v_nop
v_mfma_f32_16x16x32_bf16 v[216:219], a[136:139], a[196:199], v[216:219]
buffer_load_dwordx4 v37, s[12:15], s24 offen offset:1024 lds
v_nop
v_mfma_f32_16x16x32_bf16 v[220:223], a[136:139], a[200:203], v[220:223]
ds_read_b64_tr_b16 a[40:41], v48 offset:18432
ds_read_b64_tr_b16 a[42:43], v48 offset:18944
.Lpad17:
s_cbranch_execz .Ltch6
v_mfma_f32_16x16x32_bf16 v[144:147], a[164:167], a[140:143], v[144:147]
s_cmp_eq_u32 s39, 1
s_mov_b32 s28, 16384
s_cselect_b32 s28, 4294934528, s28
v_mfma_f32_16x16x32_bf16 v[148:151], a[168:171], a[140:143], v[148:151]
v_add_u32 v43, 16384, v43
v_add_u32 v44, 16384, v44
v_mfma_f32_16x16x32_bf16 v[184:187], a[172:175], a[140:143], v[184:187]
v_add_u32 v47, 4294934528, v47
v_add_u32 v48, 4294934528, v48
v_mfma_f32_16x16x32_bf16 v[188:191], a[192:195], a[140:143], v[188:191]
buffer_load_dwordx4 v38, s[12:15], s24 offen offset:2048 lds
v_cndmask_b32 v16, v34, v30, vcc
v_mfma_f32_16x16x32_bf16 v[224:227], a[140:143], a[196:199], v[224:227]
v_add_u32 v45, s28, v45
v_add_u32 v46, s28, v46
v_mfma_f32_16x16x32_bf16 v[228:231], a[140:143], a[200:203], v[228:231]
v_add_u32 v29, 15, v28
v_and_b32 v29, 0xfffffff1, v29
v_mfma_f32_16x16x32_bf16 v[152:155], a[164:167], a[160:163], v[152:155]
v_cmp_eq_u32 vcc, 0, v29
v_nop
v_mfma_f32_16x16x32_bf16 v[156:159], a[168:171], a[160:163], v[156:159]
v_nop
v_cndmask_b32 v17, v34, v30, vcc
v_mfma_f32_16x16x32_bf16 v[192:195], a[172:175], a[160:163], v[192:195]
buffer_load_dwordx4 v39, s[12:15], s24 offen offset:3072 lds
v_add_u32 v29, 14, v28
v_mfma_f32_16x16x32_bf16 v[196:199], a[192:195], a[160:163], v[196:199]
s_add_u32 s24, s24, 128
v_and_b32 v29, 0xfffffff1, v29
v_cmp_eq_u32 vcc, 0, v29
v_mfma_f32_16x16x32_bf16 v[232:235], a[160:163], a[196:199], v[232:235]
buffer_load_dword v250, v249, s[60:63], s90 offen sc1
v_nop
v_nop
v_mfma_f32_16x16x32_bf16 v[236:239], a[160:163], a[200:203], v[236:239]
s_waitcnt vmcnt(16) lgkmcnt(0)
s_barrier
v_cndmask_b32 v18, v34, v30, vcc
v_mfma_f32_16x16x32_bf16 v[160:163], a[28:31], a[0:3], v[160:163]
ds_read_b64_tr_b16 a[164:165], v47 offset:0
ds_read_b64_tr_b16 a[166:167], v47 offset:512
v_mfma_f32_16x16x32_bf16 v[164:167], a[32:35], a[0:3], v[164:167]
s_add_u32 m0, s48, 131072
ds_read_b128 a[128:131], v45
v_mfma_f32_16x16x32_bf16 v[200:203], a[0:3], a[36:39], v[200:203]
buffer_load_dwordx4 v40, s[16:19], s26 offen offset:0 lds
v_add_u32 v29, 13, v28
v_mfma_f32_16x16x32_bf16 v[204:207], a[0:3], a[40:43], v[204:207]
ds_read_b64_tr_b16 a[168:169], v48 offset:0
ds_read_b64_tr_b16 a[170:171], v48 offset:512
v_mfma_f32_16x16x32_bf16 v[128:131], a[20:23], a[4:7], v[128:131]
ds_read_b128 a[132:135], v43 offset:0
v_and_b32 v29, 0xfffffff1, v29
v_mfma_f32_16x16x32_bf16 v[132:135], a[24:27], a[4:7], v[132:135]
ds_read_b64_tr_b16 a[172:173], v47 offset:2048
ds_read_b64_tr_b16 a[174:175], v47 offset:2560
v_mfma_f32_16x16x32_bf16 v[168:171], a[28:31], a[4:7], v[168:171]
ds_read_b128 a[136:139], v43 offset:2048
v_cmp_eq_u32 vcc, 0, v29
v_mfma_f32_16x16x32_bf16 v[172:175], a[32:35], a[4:7], v[172:175]
buffer_load_dwordx4 v41, s[16:19], s26 offen offset:1024 lds
v_nop
v_mfma_f32_16x16x32_bf16 v[208:211], a[4:7], a[36:39], v[208:211]
ds_read_b64_tr_b16 a[192:193], v48 offset:2048
ds_read_b64_tr_b16 a[194:195], v48 offset:2560
v_mfma_f32_16x16x32_bf16 v[212:215], a[4:7], a[40:43], v[212:215]
ds_read_b128 a[140:143], v43 offset:4096
v_nop
v_mfma_f32_16x16x32_bf16 v[136:139], a[20:23], a[8:11], v[136:139]
ds_read_b64_tr_b16 a[196:197], v47 offset:4096
ds_read_b64_tr_b16 a[198:199], v47 offset:4608
v_mfma_f32_16x16x32_bf16 v[140:143], a[24:27], a[8:11], v[140:143]
ds_read_b128 a[160:163], v43 offset:6144
v_cndmask_b32 v19, v34, v30, vcc
v_mfma_f32_16x16x32_bf16 v[176:179], a[28:31], a[8:11], v[176:179]
buffer_load_dwordx4 v40, s[16:19], s26 offen offset:2048 lds
v_add_u32 v29, 0, v28
v_mfma_f32_16x16x32_bf16 v[180:183], a[32:35], a[8:11], v[180:183]
ds_read_b64_tr_b16 a[200:201], v48 offset:4096
ds_read_b64_tr_b16 a[202:203], v48 offset:4608
v_mfma_f32_16x16x32_bf16 v[216:219], a[8:11], a[36:39], v[216:219]
v_and_b32 v29, 0xfffffff1, v29
v_cmp_eq_u32 vcc, 0, v29
v_mfma_f32_16x16x32_bf16 v[220:223], a[8:11], a[40:43], v[220:223]
v_nop
v_nop
v_mfma_f32_16x16x32_bf16 v[144:147], a[20:23], a[12:15], v[144:147]
v_cndmask_b32 v20, v34, v30, vcc
v_add_u32 v29, -1, v28
v_mfma_f32_16x16x32_bf16 v[148:151], a[24:27], a[12:15], v[148:151]
buffer_load_dwordx4 v41, s[16:19], s26 offen offset:3072 lds
v_and_b32 v29, 0xfffffff1, v29
v_mfma_f32_16x16x32_bf16 v[184:187], a[28:31], a[12:15], v[184:187]
v_cmp_eq_u32 vcc, 0, v29
v_nop
v_mfma_f32_16x16x32_bf16 v[188:191], a[32:35], a[12:15], v[188:191]
v_nop
v_cndmask_b32 v21, v34, v30, vcc
v_mfma_f32_16x16x32_bf16 v[224:227], a[12:15], a[36:39], v[224:227]
v_add_u32 v29, -2, v28
v_and_b32 v29, 0xfffffff1, v29
v_mfma_f32_16x16x32_bf16 v[228:231], a[12:15], a[40:43], v[228:231]
s_add_u32 m0, s48, 135168
v_cmp_eq_u32 vcc, 0, v29
v_nop
v_mfma_f32_16x16x32_bf16 v[152:155], a[20:23], a[16:19], v[152:155]
buffer_load_dwordx4 v40, s[16:19], s47 offen offset:0 lds
v_nop
v_mfma_f32_16x16x32_bf16 v[156:159], a[24:27], a[16:19], v[156:159]
v_cndmask_b32 v22, v34, v30, vcc
v_add_u32 v29, -3, v28
v_mfma_f32_16x16x32_bf16 v[192:195], a[28:31], a[16:19], v[192:195]
v_and_b32 v29, 0xfffffff1, v29
v_cmp_eq_u32 vcc, 0, v29
v_mfma_f32_16x16x32_bf16 v[196:199], a[32:35], a[16:19], v[196:199]
v_nop
v_nop
v_mfma_f32_16x16x32_bf16 v[232:235], a[16:19], a[36:39], v[232:235]
buffer_load_dwordx4 v41, s[16:19], s47 offen offset:1024 lds
v_cndmask_b32 v23, v34, v30, vcc
v_mfma_f32_16x16x32_bf16 v[236:239], a[16:19], a[40:43], v[236:239]
s_waitcnt lgkmcnt(0)
s_lshr_b32 s29, s3, 1
s_or_b32 s29, s29, s36
v_mfma_f32_16x16x32_bf16 v[160:163], a[172:175], a[128:131], v[160:163]
ds_read_b64_tr_b16 a[20:21], v47 offset:14336
ds_read_b64_tr_b16 a[22:23], v47 offset:14848
v_mfma_f32_16x16x32_bf16 v[164:167], a[192:195], a[128:131], v[164:167]
ds_read_b128 a[0:3], v46
v_mfma_f32_16x16x32_bf16 v[200:203], a[128:131], a[196:199], v[200:203]
ds_read_b64_tr_b16 a[24:25], v48 offset:14336
ds_read_b64_tr_b16 a[26:27], v48 offset:14848
v_mfma_f32_16x16x32_bf16 v[204:207], a[128:131], a[200:203], v[204:207]
buffer_load_dwordx4 v42, s[12:15], s25 offen offset:2048 lds
v_mfma_f32_16x16x32_bf16 v[128:131], a[164:167], a[132:135], v[128:131]
s_add_u32 s26, s26, 393216
s_add_u32 s47, s47, 393216
v_mfma_f32_16x16x32_bf16 v[132:135], a[168:171], a[132:135], v[132:135]
s_add_u32 s25, s25, 128
ds_read_b128 a[4:7], v44 offset:0
v_mfma_f32_16x16x32_bf16 v[168:171], a[172:175], a[132:135], v[168:171]
ds_read_b64_tr_b16 a[28:29], v47 offset:16384
ds_read_b64_tr_b16 a[30:31], v47 offset:16896
v_mfma_f32_16x16x32_bf16 v[172:175], a[192:195], a[132:135], v[172:175]
ds_read_b128 a[8:11], v44 offset:2048
v_mfma_f32_16x16x32_bf16 v[208:211], a[132:135], a[196:199], v[208:211]
ds_read_b64_tr_b16 a[32:33], v48 offset:16384
ds_read_b64_tr_b16 a[34:35], v48 offset:16896
v_mfma_f32_16x16x32_bf16 v[212:215], a[132:135], a[200:203], v[212:215]
ds_read_b128 a[12:15], v44 offset:4096
v_mfma_f32_16x16x32_bf16 v[136:139], a[164:167], a[136:139], v[136:139]
ds_read_b64_tr_b16 a[36:37], v47 offset:18432
ds_read_b64_tr_b16 a[38:39], v47 offset:18944
v_mfma_f32_16x16x32_bf16 v[140:143], a[168:171], a[136:139], v[140:143]
ds_read_b128 a[16:19], v44 offset:6144
v_mfma_f32_16x16x32_bf16 v[176:179], a[172:175], a[136:139], v[176:179]
ds_read_b64_tr_b16 a[40:41], v48 offset:18432
ds_read_b64_tr_b16 a[42:43], v48 offset:18944
v_mfma_f32_16x16x32_bf16 v[180:183], a[192:195], a[136:139], v[180:183]
s_cmp_eq_u32 s39, 1
s_mov_b32 s28, 4294885376
s_cselect_b32 s28, 4294901760, s28
v_mfma_f32_16x16x32_bf16 v[216:219], a[136:139], a[196:199], v[216:219]
v_add_u32 v43, 4294885376, v43
v_add_u32 v44, 4294885376, v44
v_mfma_f32_16x16x32_bf16 v[220:223], a[136:139], a[200:203], v[220:223]
v_add_u32 v47, 4294901760, v47
v_add_u32 v48, 4294901760, v48
v_mfma_f32_16x16x32_bf16 v[144:147], a[164:167], a[140:143], v[144:147]
v_add_u32 v45, s28, v45
v_add_u32 v46, s28, v46
v_mfma_f32_16x16x32_bf16 v[148:151], a[168:171], a[140:143], v[148:151]
s_cmp_eq_u32 s29, 0
s_cselect_b64 s[32:33], -1, 0
v_mfma_f32_16x16x32_bf16 v[184:187], a[172:175], a[140:143], v[184:187]
v_cndmask_b32 v24, v16, v34, s[32:33]
v_cndmask_b32 v25, v17, v34, s[32:33]
v_mfma_f32_16x16x32_bf16 v[188:191], a[192:195], a[140:143], v[188:191]
v_cndmask_b32 v26, v18, v34, s[32:33]
v_cndmask_b32 v27, v19, v34, s[32:33]
v_mfma_f32_16x16x32_bf16 v[224:227], a[140:143], a[196:199], v[224:227]
s_waitcnt vmcnt(7)
v_and_b32 v250, 7, v250
v_mfma_f32_16x16x32_bf16 v[228:231], a[140:143], a[200:203], v[228:231]
v_mfma_f32_16x16x32_bf16 v[152:155], a[164:167], a[160:163], v[152:155]
v_cmp_gt_u32 vcc, 3, v250
v_mfma_f32_16x16x32_bf16 v[156:159], a[168:171], a[160:163], v[156:159]
v_mfma_f32_16x16x32_bf16 v[192:195], a[172:175], a[160:163], v[192:195]
s_cmp_lg_u64 vcc, 0
s_cbranch_scc1 .Lspin3
.Lspin3_ret:
v_mfma_f32_16x16x32_bf16 v[196:199], a[192:195], a[160:163], v[196:199]
v_mfma_f32_16x16x32_bf16 v[232:235], a[160:163], a[196:199], v[232:235]
s_lshl_b32 s54, s51, 19
s_and_b32 s56, s3, 1
s_lshl_b32 s56, s56, 18
v_mfma_f32_16x16x32_bf16 v[236:239], a[160:163], a[200:203], v[236:239]
s_waitcnt vmcnt(12) lgkmcnt(0)
s_barrier
s_add_u32 s54, s54, s56
v_mfma_f32_16x16x32_bf16 v[160:163], a[28:31], a[0:3], v[160:163]
ds_read_b64_tr_b16 a[164:165], v47 offset:0
ds_read_b64_tr_b16 a[166:167], v47 offset:512
v_mfma_f32_16x16x32_bf16 v[164:167], a[32:35], a[0:3], v[164:167]
s_add_u32 m0, s91, 114688
ds_read_b128 a[128:131], v45
v_mfma_f32_16x16x32_bf16 v[200:203], a[0:3], a[36:39], v[200:203]
buffer_load_dwordx4 v240, s[8:11], s45 offen offset:0 lds
ds_read_b64_tr_b16 a[168:169], v48 offset:0
ds_read_b64_tr_b16 a[170:171], v48 offset:512
v_mfma_f32_16x16x32_bf16 v[204:207], a[0:3], a[40:43], v[204:207]
s_add_u32 s54, s54, s52
ds_read_b128 a[132:135], v43 offset:0
v_mfma_f32_16x16x32_bf16 v[128:131], a[20:23], a[4:7], v[128:131]
ds_read_b64_tr_b16 a[172:173], v47 offset:2048
ds_read_b64_tr_b16 a[174:175], v47 offset:2560
v_mfma_f32_16x16x32_bf16 v[132:135], a[24:27], a[4:7], v[132:135]
buffer_load_dwordx4 v241, s[8:11], s45 offen offset:1024 lds
ds_read_b128 a[136:139], v43 offset:2048
v_mfma_f32_16x16x32_bf16 v[168:171], a[28:31], a[4:7], v[168:171]
ds_read_b64_tr_b16 a[192:193], v48 offset:2048
ds_read_b64_tr_b16 a[194:195], v48 offset:2560
v_mfma_f32_16x16x32_bf16 v[172:175], a[32:35], a[4:7], v[172:175]
s_lshr_b32 s56, s3, 1
ds_read_b128 a[140:143], v43 offset:4096
v_mfma_f32_16x16x32_bf16 v[208:211], a[4:7], a[36:39], v[208:211]
buffer_load_dwordx4 v242, s[8:11], s45 offen offset:2048 lds
ds_read_b64_tr_b16 a[196:197], v47 offset:4096
ds_read_b64_tr_b16 a[198:199], v47 offset:4608
v_mfma_f32_16x16x32_bf16 v[212:215], a[4:7], a[40:43], v[212:215]
s_lshl_b32 s56, s56, 7
ds_read_b128 a[160:163], v43 offset:6144
v_mfma_f32_16x16x32_bf16 v[136:139], a[20:23], a[8:11], v[136:139]
ds_read_b64_tr_b16 a[200:201], v48 offset:4096
ds_read_b64_tr_b16 a[202:203], v48 offset:4608
v_mfma_f32_16x16x32_bf16 v[140:143], a[24:27], a[8:11], v[140:143]
s_add_u32 s54, s54, s56
buffer_load_dwordx4 v243, s[8:11], s45 offen offset:3072 lds
v_mfma_f32_16x16x32_bf16 v[176:179], a[28:31], a[8:11], v[176:179]
s_add_u32 s45, s45, 131072
s_xor_b32 s56, s55, 1
s_lshl_b32 s56, s56, 11
v_mfma_f32_16x16x32_bf16 v[180:183], a[32:35], a[8:11], v[180:183]
s_add_u32 m0, s91, 98304
s_add_u32 s54, s54, s56
s_sub_u32 s18, 0x800000, s54
v_mfma_f32_16x16x32_bf16 v[216:219], a[8:11], a[36:39], v[216:219]
buffer_load_dwordx4 v7, s[4:7], s44 offen offset:0 sc1 lds
v_mfma_f32_16x16x32_bf16 v[220:223], a[8:11], a[40:43], v[220:223]
s_add_u32 s16, s82, s54
s_addc_u32 s17, s83, 0
s_and_b32 s17, s17, 0xffff
v_mfma_f32_16x16x32_bf16 v[144:147], a[20:23], a[12:15], v[144:147]
s_lshr_b32 s54, s42, 1
s_lshl_b32 s54, s54, 5
s_add_u32 s54, s54, s87
v_mfma_f32_16x16x32_bf16 v[148:151], a[24:27], a[12:15], v[148:151]
s_lshl_b32 s54, s54, 16
buffer_load_dwordx4 v7, s[4:7], s44 offen offset:1024 sc1 lds
v_mfma_f32_16x16x32_bf16 v[184:187], a[28:31], a[12:15], v[184:187]
s_lshl_b32 s56, s3, 14
s_add_u32 s54, s54, s56
s_add_u32 s54, s54, 54525952
v_mfma_f32_16x16x32_bf16 v[188:191], a[32:35], a[12:15], v[188:191]
s_add_u32 s12, s84, s54
s_addc_u32 s13, s85, 0
s_and_b32 s13, s13, 0xffff
v_mfma_f32_16x16x32_bf16 v[224:227], a[12:15], a[36:39], v[224:227]
s_mov_b32 s14, 16384
buffer_load_dwordx4 v7, s[4:7], s44 offen offset:2048 sc1 lds
v_mfma_f32_16x16x32_bf16 v[228:231], a[12:15], a[40:43], v[228:231]
s_mov_b32 s56, 131072
s_mov_b32 s57, -32768
s_mov_b32 s46, 98304
v_mfma_f32_16x16x32_bf16 v[152:155], a[20:23], a[16:19], v[152:155]
s_lshr_b32 s30, s51, 3
s_and_b32 s31, s51, 7
s_lshl_b32 s31, s31, 3
v_mfma_f32_16x16x32_bf16 v[156:159], a[24:27], a[16:19], v[156:159]
s_or_b32 s30, s30, s31
buffer_load_dwordx4 v7, s[4:7], s44 offen offset:3072 sc1 lds
v_mfma_f32_16x16x32_bf16 v[192:195], a[28:31], a[16:19], v[192:195]
s_add_u32 s44, s44, 4096
s_mov_b32 s31, s38
s_sub_u32 s50, s43, 14
v_mfma_f32_16x16x32_bf16 v[196:199], a[32:35], a[16:19], v[196:199]
s_sub_u32 s54, 48, s43
v_mfma_f32_16x16x32_bf16 v[232:235], a[16:19], a[36:39], v[232:235]
s_add_u32 m0, s91, 81920
v_mfma_f32_16x16x32_bf16 v[236:239], a[16:19], a[40:43], v[236:239]
s_waitcnt lgkmcnt(0)
buffer_load_dwordx4 v240, s[8:11], s45 offen offset:0 lds
v_mfma_f32_16x16x32_bf16 v[160:163], a[172:175], a[128:131], v[160:163]
ds_read_b64_tr_b16 a[20:21], v47 offset:14336
ds_read_b64_tr_b16 a[22:23], v47 offset:14848
v_mfma_f32_16x16x32_bf16 v[164:167], a[192:195], a[128:131], v[164:167]
s_lshl_b32 s34, s36, 18
ds_read_b128 a[0:3], v46
v_mfma_f32_16x16x32_bf16 v[200:203], a[128:131], a[196:199], v[200:203]
buffer_load_dwordx4 v241, s[8:11], s45 offen offset:1024 lds
ds_read_b64_tr_b16 a[24:25], v48 offset:14336
ds_read_b64_tr_b16 a[26:27], v48 offset:14848
v_mfma_f32_16x16x32_bf16 v[204:207], a[128:131], a[200:203], v[204:207]
s_lshr_b32 s35, s3, 1
ds_read_b128 a[4:7], v44 offset:0
v_mfma_f32_16x16x32_bf16 v[128:131], a[164:167], a[132:135], v[128:131]
ds_read_b64_tr_b16 a[28:29], v47 offset:16384
ds_read_b64_tr_b16 a[30:31], v47 offset:16896
v_mfma_f32_16x16x32_bf16 v[132:135], a[168:171], a[132:135], v[132:135]
s_lshl_b32 s35, s35, 17
buffer_load_dwordx4 v242, s[8:11], s45 offen offset:2048 lds
ds_read_b128 a[8:11], v44 offset:2048
v_mfma_f32_16x16x32_bf16 v[168:171], a[172:175], a[132:135], v[168:171]
ds_read_b64_tr_b16 a[32:33], v48 offset:16384
ds_read_b64_tr_b16 a[34:35], v48 offset:16896
v_mfma_f32_16x16x32_bf16 v[172:175], a[192:195], a[132:135], v[172:175]
s_add_u32 s34, s34, s35
ds_read_b128 a[12:15], v44 offset:4096
v_mfma_f32_16x16x32_bf16 v[208:211], a[132:135], a[196:199], v[208:211]
buffer_load_dwordx4 v243, s[8:11], s45 offen offset:3072 lds
ds_read_b64_tr_b16 a[36:37], v47 offset:18432
ds_read_b64_tr_b16 a[38:39], v47 offset:18944
v_mfma_f32_16x16x32_bf16 v[212:215], a[132:135], a[200:203], v[212:215]
s_lshl_b32 s35, s37, 12
s_add_u32 s45, s45, 131072
ds_read_b128 a[16:19], v44 offset:6144
v_mfma_f32_16x16x32_bf16 v[136:139], a[164:167], a[136:139], v[136:139]
s_add_u32 m0, s91, 65536
ds_read_b64_tr_b16 a[40:41], v48 offset:18432
ds_read_b64_tr_b16 a[42:43], v48 offset:18944
v_mfma_f32_16x16x32_bf16 v[140:143], a[168:171], a[136:139], v[140:143]
s_cmp_eq_u32 s39, 1
s_mov_b32 s28, 16384
s_cselect_b32 s28, 98304, s28
buffer_load_dwordx4 v7, s[4:7], s44 offen offset:0 sc1 lds
v_mfma_f32_16x16x32_bf16 v[176:179], a[172:175], a[136:139], v[176:179]
v_add_u32 v43, 16384, v43
v_add_u32 v44, 16384, v44
v_mfma_f32_16x16x32_bf16 v[180:183], a[192:195], a[136:139], v[180:183]
v_add_u32 v47, 98304, v47
v_add_u32 v48, 98304, v48
v_mfma_f32_16x16x32_bf16 v[216:219], a[136:139], a[196:199], v[216:219]
buffer_load_dwordx4 v7, s[4:7], s44 offen offset:1024 sc1 lds
v_add_u32 v45, s28, v45
v_add_u32 v46, s28, v46
v_mfma_f32_16x16x32_bf16 v[220:223], a[136:139], a[200:203], v[220:223]
s_add_u32 s34, s34, s35
s_and_b32 s35, s3, 1
s_lshl_b32 s35, s35, 11
v_mfma_f32_16x16x32_bf16 v[144:147], a[164:167], a[140:143], v[144:147]
s_add_u32 s34, s34, s35
s_add_u32 s34, s34, 50331648
v_mfma_f32_16x16x32_bf16 v[148:151], a[168:171], a[140:143], v[148:151]
s_add_u32 s20, s84, s34
s_addc_u32 s21, s85, 0
s_and_b32 s21, s21, 0xffff
buffer_load_dwordx4 v7, s[4:7], s44 offen offset:2048 sc1 lds
v_mfma_f32_16x16x32_bf16 v[184:187], a[172:175], a[140:143], v[184:187]
s_mov_b32 s22, 131072
s_mov_b32 s40, 0
s_mov_b32 s41, 65536
v_mfma_f32_16x16x32_bf16 v[188:191], a[192:195], a[140:143], v[188:191]
s_cmp_eq_u32 s55, 0
s_cselect_b64 s[58:59], -1, 0
s_cselect_b32 s50, s50, s54
v_mfma_f32_16x16x32_bf16 v[224:227], a[140:143], a[196:199], v[224:227]
buffer_load_dwordx4 v7, s[4:7], s44 offen offset:3072 sc1 lds
v_and_b32 v252, 7, v1
v_mfma_f32_16x16x32_bf16 v[228:231], a[140:143], a[200:203], v[228:231]
s_add_u32 s44, s44, 4096
v_bfe_u32 v253, v1, 3, 1
v_bfe_u32 v254, v1, 4, 1
v_mfma_f32_16x16x32_bf16 v[152:155], a[164:167], a[160:163], v[152:155]
v_lshrrev_b32 v255, 5, v1
v_lshlrev_b32 v252, 12, v252
v_mfma_f32_16x16x32_bf16 v[156:159], a[168:171], a[160:163], v[156:159]
v_lshl_add_u32 v252, v253, 6, v252
v_lshl_add_u32 v252, v254, 5, v252
v_mfma_f32_16x16x32_bf16 v[192:195], a[172:175], a[160:163], v[192:195]
v_lshl_add_u32 v251, v255, 4, v252
v_bfe_u32 v252, v1, 2, 2
v_mfma_f32_16x16x32_bf16 v[196:199], a[192:195], a[160:163], v[196:199]
v_and_b32 v253, 3, v1
v_lshlrev_b32 v252, 1, v252
v_mfma_f32_16x16x32_bf16 v[232:235], a[160:163], a[196:199], v[232:235]
v_lshl_or_b32 v252, v253, 6, v252
v_or_b32 v252, s30, v252
v_mfma_f32_16x16x32_bf16 v[236:239], a[160:163], a[200:203], v[236:239]
s_waitcnt vmcnt(16) lgkmcnt(0)
s_barrier
v_mov_b32 v253, s31
v_mfma_f32_16x16x32_bf16 v[160:163], a[28:31], a[0:3], v[160:163]
ds_read_b64_tr_b16 a[164:165], v47 offset:0
ds_read_b64_tr_b16 a[166:167], v47 offset:512
v_mfma_f32_16x16x32_bf16 v[164:167], a[32:35], a[0:3], v[164:167]
s_add_u32 m0, s91, 49152
ds_read_b128 a[128:131], v45
v_mfma_f32_16x16x32_bf16 v[200:203], a[0:3], a[36:39], v[200:203]
buffer_load_dwordx4 v240, s[8:11], s45 offen offset:0 lds
ds_read_b64_tr_b16 a[168:169], v48 offset:0
ds_read_b64_tr_b16 a[170:171], v48 offset:512
v_mfma_f32_16x16x32_bf16 v[204:207], a[0:3], a[40:43], v[204:207]
ds_read_b128 a[132:135], v43 offset:0
v_cndmask_b32 v252, v252, v253, s[58:59]
v_mfma_f32_16x16x32_bf16 v[128:131], a[20:23], a[4:7], v[128:131]
ds_read_b64_tr_b16 a[172:173], v47 offset:2048
ds_read_b64_tr_b16 a[174:175], v47 offset:2560
v_mfma_f32_16x16x32_bf16 v[132:135], a[24:27], a[4:7], v[132:135]
ds_read_b128 a[136:139], v43 offset:2048
v_lshlrev_b32 v249, 5, v252
v_mfma_f32_16x16x32_bf16 v[168:171], a[28:31], a[4:7], v[168:171]
ds_read_b64_tr_b16 a[192:193], v48 offset:2048
ds_read_b64_tr_b16 a[194:195], v48 offset:2560
v_mfma_f32_16x16x32_bf16 v[172:175], a[32:35], a[4:7], v[172:175]
buffer_load_dwordx4 v241, s[8:11], s45 offen offset:1024 lds
ds_read_b128 a[140:143], v43 offset:4096
v_mfma_f32_16x16x32_bf16 v[208:211], a[4:7], a[36:39], v[208:211]
ds_read_b64_tr_b16 a[196:197], v47 offset:4096
ds_read_b64_tr_b16 a[198:199], v47 offset:4608
v_mfma_f32_16x16x32_bf16 v[212:215], a[4:7], a[40:43], v[212:215]
ds_read_b128 a[160:163], v43 offset:6144
v_mfma_f32_16x16x32_bf16 v[136:139], a[20:23], a[8:11], v[136:139]
ds_read_b64_tr_b16 a[200:201], v48 offset:4096
ds_read_b64_tr_b16 a[202:203], v48 offset:4608
v_mfma_f32_16x16x32_bf16 v[140:143], a[24:27], a[8:11], v[140:143]
v_mfma_f32_16x16x32_bf16 v[176:179], a[28:31], a[8:11], v[176:179]
buffer_load_dwordx4 v242, s[8:11], s45 offen offset:2048 lds
v_mfma_f32_16x16x32_bf16 v[180:183], a[32:35], a[8:11], v[180:183]
v_mfma_f32_16x16x32_bf16 v[216:219], a[8:11], a[36:39], v[216:219]
v_mfma_f32_16x16x32_bf16 v[220:223], a[8:11], a[40:43], v[220:223]
v_mfma_f32_16x16x32_bf16 v[144:147], a[20:23], a[12:15], v[144:147]
v_mfma_f32_16x16x32_bf16 v[148:151], a[24:27], a[12:15], v[148:151]
buffer_load_dwordx4 v243, s[8:11], s45 offen offset:3072 lds
v_mfma_f32_16x16x32_bf16 v[184:187], a[28:31], a[12:15], v[184:187]
s_add_u32 s45, s45, 131072
v_mfma_f32_16x16x32_bf16 v[188:191], a[32:35], a[12:15], v[188:191]
v_mfma_f32_16x16x32_bf16 v[224:227], a[12:15], a[36:39], v[224:227]
v_mfma_f32_16x16x32_bf16 v[228:231], a[12:15], a[40:43], v[228:231]
s_add_u32 m0, s91, 32768
v_mfma_f32_16x16x32_bf16 v[152:155], a[20:23], a[16:19], v[152:155]
buffer_load_dwordx4 v7, s[4:7], s44 offen offset:0 sc1 lds
v_mfma_f32_16x16x32_bf16 v[156:159], a[24:27], a[16:19], v[156:159]
v_mfma_f32_16x16x32_bf16 v[192:195], a[28:31], a[16:19], v[192:195]
v_mfma_f32_16x16x32_bf16 v[196:199], a[32:35], a[16:19], v[196:199]
v_mfma_f32_16x16x32_bf16 v[232:235], a[16:19], a[36:39], v[232:235]
v_mfma_f32_16x16x32_bf16 v[236:239], a[16:19], a[40:43], v[236:239]
s_waitcnt lgkmcnt(0)
buffer_load_dwordx4 v7, s[4:7], s44 offen offset:1024 sc1 lds
v_mfma_f32_16x16x32_bf16 v[160:163], a[172:175], a[128:131], v[160:163]
ds_read_b64_tr_b16 a[20:21], v47 offset:14336
ds_read_b64_tr_b16 a[22:23], v47 offset:14848
v_mfma_f32_16x16x32_bf16 v[164:167], a[192:195], a[128:131], v[164:167]
ds_read_b128 a[0:3], v46
v_mfma_f32_16x16x32_bf16 v[200:203], a[128:131], a[196:199], v[200:203]
ds_read_b64_tr_b16 a[24:25], v48 offset:14336
ds_read_b64_tr_b16 a[26:27], v48 offset:14848
v_mfma_f32_16x16x32_bf16 v[204:207], a[128:131], a[200:203], v[204:207]
ds_read_b128 a[4:7], v44 offset:0
v_mfma_f32_16x16x32_bf16 v[128:131], a[164:167], a[132:135], v[128:131]
buffer_load_dwordx4 v7, s[4:7], s44 offen offset:2048 sc1 lds
ds_read_b64_tr_b16 a[28:29], v47 offset:16384
ds_read_b64_tr_b16 a[30:31], v47 offset:16896
v_mfma_f32_16x16x32_bf16 v[132:135], a[168:171], a[132:135], v[132:135]
ds_read_b128 a[8:11], v44 offset:2048
v_mfma_f32_16x16x32_bf16 v[168:171], a[172:175], a[132:135], v[168:171]
ds_read_b64_tr_b16 a[32:33], v48 offset:16384
ds_read_b64_tr_b16 a[34:35], v48 offset:16896
v_mfma_f32_16x16x32_bf16 v[172:175], a[192:195], a[132:135], v[172:175]
ds_read_b128 a[12:15], v44 offset:4096
v_mfma_f32_16x16x32_bf16 v[208:211], a[132:135], a[196:199], v[208:211]
ds_read_b64_tr_b16 a[36:37], v47 offset:18432
ds_read_b64_tr_b16 a[38:39], v47 offset:18944
v_mfma_f32_16x16x32_bf16 v[212:215], a[132:135], a[200:203], v[212:215]
buffer_load_dwordx4 v7, s[4:7], s44 offen offset:3072 sc1 lds
ds_read_b128 a[16:19], v44 offset:6144
v_mfma_f32_16x16x32_bf16 v[136:139], a[164:167], a[136:139], v[136:139]
s_add_u32 s44, s44, 4096
ds_read_b64_tr_b16 a[40:41], v48 offset:18432
ds_read_b64_tr_b16 a[42:43], v48 offset:18944
v_mfma_f32_16x16x32_bf16 v[140:143], a[168:171], a[136:139], v[140:143]
v_mfma_f32_16x16x32_bf16 v[176:179], a[172:175], a[136:139], v[176:179]
v_mfma_f32_16x16x32_bf16 v[180:183], a[192:195], a[136:139], v[180:183]
v_mfma_f32_16x16x32_bf16 v[216:219], a[136:139], a[196:199], v[216:219]
v_mfma_f32_16x16x32_bf16 v[220:223], a[136:139], a[200:203], v[220:223]
v_mfma_f32_16x16x32_bf16 v[144:147], a[164:167], a[140:143], v[144:147]
v_mfma_f32_16x16x32_bf16 v[148:151], a[168:171], a[140:143], v[148:151]
v_mfma_f32_16x16x32_bf16 v[184:187], a[172:175], a[140:143], v[184:187]
v_mfma_f32_16x16x32_bf16 v[188:191], a[192:195], a[140:143], v[188:191]
v_mfma_f32_16x16x32_bf16 v[224:227], a[140:143], a[196:199], v[224:227]
v_mfma_f32_16x16x32_bf16 v[228:231], a[140:143], a[200:203], v[228:231]
v_mfma_f32_16x16x32_bf16 v[152:155], a[164:167], a[160:163], v[152:155]
v_mfma_f32_16x16x32_bf16 v[156:159], a[168:171], a[160:163], v[156:159]
v_mfma_f32_16x16x32_bf16 v[192:195], a[172:175], a[160:163], v[192:195]
v_mfma_f32_16x16x32_bf16 v[196:199], a[192:195], a[160:163], v[196:199]
v_mfma_f32_16x16x32_bf16 v[232:235], a[160:163], a[196:199], v[232:235]
v_mfma_f32_16x16x32_bf16 v[236:239], a[160:163], a[200:203], v[236:239]
s_waitcnt vmcnt(16)
s_waitcnt lgkmcnt(0)
s_barrier
v_mfma_f32_16x16x32_bf16 v[160:163], a[28:31], a[0:3], v[160:163]
v_mfma_f32_16x16x32_bf16 v[164:167], a[32:35], a[0:3], v[164:167]
s_add_u32 m0, s91, 16384
ds_read_b64_tr_b16 a[80:81], v245 offset:0
ds_read_b64_tr_b16 a[82:83], v245 offset:4096
v_mfma_f32_16x16x32_bf16 v[200:203], a[0:3], a[36:39], v[200:203]
buffer_load_dwordx4 v240, s[8:11], s45 offen offset:0 lds
v_mfma_f32_16x16x32_bf16 v[204:207], a[0:3], a[40:43], v[204:207]
v_mfma_f32_16x16x32_bf16 v[128:131], a[20:23], a[4:7], v[128:131]
ds_read_b128 a[64:67], v244 offset:0
ds_read_b128 a[68:71], v244 offset:1024
v_mfma_f32_16x16x32_bf16 v[132:135], a[24:27], a[4:7], v[132:135]
buffer_load_dwordx4 v241, s[8:11], s45 offen offset:1024 lds
v_mfma_f32_16x16x32_bf16 v[168:171], a[28:31], a[4:7], v[168:171]
v_mfma_f32_16x16x32_bf16 v[172:175], a[32:35], a[4:7], v[172:175]
ds_read_b64_tr_b16 a[84:85], v246 offset:0
ds_read_b64_tr_b16 a[86:87], v246 offset:4096
v_mfma_f32_16x16x32_bf16 v[208:211], a[4:7], a[36:39], v[208:211]
buffer_load_dwordx4 v242, s[8:11], s45 offen offset:2048 lds
v_mfma_f32_16x16x32_bf16 v[212:215], a[4:7], a[40:43], v[212:215]
v_mfma_f32_16x16x32_bf16 v[136:139], a[20:23], a[8:11], v[136:139]
ds_read_b64_tr_b16 a[88:89], v247 offset:0
ds_read_b64_tr_b16 a[90:91], v247 offset:4096
v_mfma_f32_16x16x32_bf16 v[140:143], a[24:27], a[8:11], v[140:143]
buffer_load_dwordx4 v243, s[8:11], s45 offen offset:3072 lds
v_mfma_f32_16x16x32_bf16 v[176:179], a[28:31], a[8:11], v[176:179]
s_add_u32 s45, s45, 131072
v_mfma_f32_16x16x32_bf16 v[180:183], a[32:35], a[8:11], v[180:183]
s_add_u32 m0, s91, 0
ds_read_b128 a[72:75], v244 offset:4096
ds_read_b128 a[76:79], v244 offset:5120
v_mfma_f32_16x16x32_bf16 v[216:219], a[8:11], a[36:39], v[216:219]
buffer_load_dwordx4 v7, s[4:7], s44 offen offset:0 sc1 lds
v_mfma_f32_16x16x32_bf16 v[220:223], a[8:11], a[40:43], v[220:223]
v_mfma_f32_16x16x32_bf16 v[144:147], a[20:23], a[12:15], v[144:147]
ds_read_b64_tr_b16 a[92:93], v248 offset:0
ds_read_b64_tr_b16 a[94:95], v248 offset:4096
v_mfma_f32_16x16x32_bf16 v[148:151], a[24:27], a[12:15], v[148:151]
buffer_load_dwordx4 v7, s[4:7], s44 offen offset:1024 sc1 lds
v_mfma_f32_16x16x32_bf16 v[184:187], a[28:31], a[12:15], v[184:187]
v_mfma_f32_16x16x32_bf16 v[188:191], a[32:35], a[12:15], v[188:191]
v_mfma_f32_16x16x32_bf16 v[224:227], a[12:15], a[36:39], v[224:227]
buffer_load_dwordx4 v7, s[4:7], s44 offen offset:2048 sc1 lds
v_mfma_f32_16x16x32_bf16 v[228:231], a[12:15], a[40:43], v[228:231]
v_mfma_f32_16x16x32_bf16 v[152:155], a[20:23], a[16:19], v[152:155]
v_mfma_f32_16x16x32_bf16 v[156:159], a[24:27], a[16:19], v[156:159]
buffer_load_dwordx4 v7, s[4:7], s44 offen offset:3072 sc1 lds
v_mfma_f32_16x16x32_bf16 v[192:195], a[28:31], a[16:19], v[192:195]
s_add_u32 s44, s44, 4096
v_mfma_f32_16x16x32_bf16 v[196:199], a[32:35], a[16:19], v[196:199]
v_mfma_f32_16x16x32_bf16 v[232:235], a[16:19], a[36:39], v[232:235]
v_mfma_f32_16x16x32_bf16 v[236:239], a[16:19], a[40:43], v[236:239]
s_mov_b32 s39, 1048744942
s_waitcnt lgkmcnt(0)
v_mfma_f32_16x16x32_bf16 v[64:67], a[80:83], a[64:67], 0
s_add_u32 m0, s91, 147456
ds_read_b64_tr_b16 a[112:113], v245 offset:8192
ds_read_b64_tr_b16 a[114:115], v245 offset:12288
v_mfma_f32_16x16x32_bf16 v[68:71], a[84:87], a[64:67], 0
buffer_load_dwordx4 v240, s[8:11], s45 offen offset:0 lds
v_mfma_f32_16x16x32_bf16 v[72:75], a[88:91], a[64:67], 0
ds_read_b128 a[96:99], v244 offset:2048
ds_read_b128 a[100:103], v244 offset:3072
v_mfma_f32_16x16x32_bf16 v[76:79], a[92:95], a[64:67], 0
buffer_load_dwordx4 v241, s[8:11], s45 offen offset:1024 lds
ds_read_b64_tr_b16 a[116:117], v246 offset:8192
ds_read_b64_tr_b16 a[118:119], v246 offset:12288
v_mfma_f32_16x16x32_bf16 v[80:83], a[80:83], a[68:71], 0
v_cvt_pk_bf16_f32 v160, v160, v161
v_cvt_pk_bf16_f32 v161, v162, v163
v_cvt_pk_bf16_f32 v162, v164, v165
v_cvt_pk_bf16_f32 v163, v166, v167
v_mfma_f32_16x16x32_bf16 v[84:87], a[84:87], a[68:71], 0
buffer_load_dwordx4 v242, s[8:11], s45 offen offset:2048 lds
ds_read_b64_tr_b16 a[120:121], v247 offset:8192
ds_read_b64_tr_b16 a[122:123], v247 offset:12288
.Lpad18:
s_cbranch_execz .Ltch7
v_mfma_f32_16x16x32_bf16 v[88:91], a[88:91], a[68:71], 0
ds_read_b128 a[104:107], v244 offset:6144
ds_read_b128 a[108:111], v244 offset:7168
v_cvt_pk_bf16_f32 v36, v200, v201
v_cvt_pk_bf16_f32 v37, v202, v203
v_mfma_f32_16x16x32_bf16 v[92:95], a[92:95], a[68:71], 0
buffer_load_dwordx4 v243, s[8:11], s45 offen offset:3072 lds
v_cvt_pk_bf16_f32 v46, v204, v205
v_cvt_pk_bf16_f32 v47, v206, v207
v_cvt_pk_bf16_f32 v168, v168, v169
v_mfma_f32_16x16x32_bf16 v[96:99], a[80:83], a[72:75], 0
s_cmp_eq_u32 s46, 0
s_cselect_b32 s53, s56, s57
s_add_u32 s45, s45, 131072
s_add_u32 m0, s91, 131072
ds_read_b64_tr_b16 a[124:125], v248 offset:8192
ds_read_b64_tr_b16 a[126:127], v248 offset:12288
v_mfma_f32_16x16x32_bf16 v[100:103], a[84:87], a[72:75], 0
buffer_load_dwordx4 v7, s[4:7], s44 offen offset:0 sc1 lds
v_cvt_pk_bf16_f32 v169, v170, v171
v_cvt_pk_bf16_f32 v170, v172, v173
v_cvt_pk_bf16_f32 v171, v174, v175
v_mfma_f32_16x16x32_bf16 v[104:107], a[88:91], a[72:75], 0
s_add_u32 s49, s46, s91
s_add_u32 s46, s46, s53
v_add_u32 v244, s53, v244
v_cvt_pk_bf16_f32 v128, v128, v129
v_cvt_pk_bf16_f32 v129, v130, v131
v_mfma_f32_16x16x32_bf16 v[108:111], a[92:95], a[72:75], 0
buffer_load_dwordx4 v7, s[4:7], s44 offen offset:1024 sc1 lds
v_cvt_pk_bf16_f32 v130, v132, v133
v_cvt_pk_bf16_f32 v131, v134, v135
v_cvt_pk_bf16_f32 v38, v208, v209
v_mfma_f32_16x16x32_bf16 v[112:115], a[80:83], a[76:79], 0
v_add_u32 v245, s53, v245
v_add_u32 v246, s53, v246
v_cvt_pk_bf16_f32 v39, v210, v211
v_cvt_pk_bf16_f32 v48, v212, v213
v_mfma_f32_16x16x32_bf16 v[116:119], a[84:87], a[76:79], 0
buffer_load_dwordx4 v7, s[4:7], s44 offen offset:2048 sc1 lds
v_cvt_pk_bf16_f32 v49, v214, v215
v_cvt_pk_bf16_f32 v176, v176, v177
v_cvt_pk_bf16_f32 v177, v178, v179
v_mfma_f32_16x16x32_bf16 v[120:123], a[88:91], a[76:79], 0
buffer_load_dwordx4 v7, s[4:7], s44 offen offset:3072 sc1 lds
v_add_u32 v247, s53, v247
v_add_u32 v248, s53, v248
v_cvt_pk_bf16_f32 v178, v180, v181
v_mfma_f32_16x16x32_bf16 v[124:127], a[92:95], a[76:79], 0
s_waitcnt vmcnt(24) lgkmcnt(0)
s_barrier
v_cvt_pk_bf16_f32 v179, v182, v183
v_cvt_pk_bf16_f32 v136, v136, v137
v_cvt_pk_bf16_f32 v137, v138, v139
v_mfma_f32_16x16x32_bf16 v[64:67], a[112:115], a[96:99], v[64:67]
ds_read_b64_tr_b16 a[80:81], v245 offset:0
ds_read_b64_tr_b16 a[82:83], v245 offset:4096
v_cvt_pk_bf16_f32 v138, v140, v141
v_cvt_pk_bf16_f32 v139, v142, v143
v_mfma_f32_16x16x32_bf16 v[68:71], a[116:119], a[96:99], v[68:71]
s_add_u32 m0, s49, 16384
ds_read_b128 a[64:67], v244 offset:0
v_cvt_pk_bf16_f32 v40, v216, v217
v_cvt_pk_bf16_f32 v41, v218, v219
v_mfma_f32_16x16x32_bf16 v[72:75], a[120:123], a[96:99], v[72:75]
buffer_load_dwordx4 v240, s[8:11], s45 offen offset:0 lds
v_cvt_pk_bf16_f32 v50, v220, v221
v_cvt_pk_bf16_f32 v51, v222, v223
v_cvt_pk_bf16_f32 v184, v184, v185
v_mfma_f32_16x16x32_bf16 v[76:79], a[124:127], a[96:99], v[76:79]
ds_read_b64_tr_b16 a[84:85], v246 offset:0
ds_read_b64_tr_b16 a[86:87], v246 offset:4096
v_cvt_pk_bf16_f32 v185, v186, v187
v_cvt_pk_bf16_f32 v186, v188, v189
v_mfma_f32_16x16x32_bf16 v[80:83], a[112:115], a[100:103], v[80:83]
s_add_u32 s44, s44, 4096
ds_read_b128 a[68:71], v244 offset:1024
v_cvt_pk_bf16_f32 v187, v190, v191
v_cvt_pk_bf16_f32 v144, v144, v145
v_mfma_f32_16x16x32_bf16 v[84:87], a[116:119], a[100:103], v[84:87]
ds_read_b64_tr_b16 a[88:89], v247 offset:0
ds_read_b64_tr_b16 a[90:91], v247 offset:4096
v_cvt_pk_bf16_f32 v145, v146, v147
v_cvt_pk_bf16_f32 v146, v148, v149
v_mfma_f32_16x16x32_bf16 v[88:91], a[120:123], a[100:103], v[88:91]
buffer_load_dwordx4 v241, s[8:11], s45 offen offset:1024 lds
v_cvt_pk_bf16_f32 v147, v150, v151
v_cvt_pk_bf16_f32 v42, v224, v225
v_cvt_pk_bf16_f32 v43, v226, v227
v_mfma_f32_16x16x32_bf16 v[92:95], a[124:127], a[100:103], v[92:95]
ds_read_b128 a[72:75], v244 offset:4096
v_cvt_pk_bf16_f32 v52, v228, v229
v_cvt_pk_bf16_f32 v53, v230, v231
v_cvt_pk_bf16_f32 v192, v192, v193
v_mfma_f32_16x16x32_bf16 v[96:99], a[112:115], a[104:107], v[96:99]
ds_read_b64_tr_b16 a[92:93], v248 offset:0
ds_read_b64_tr_b16 a[94:95], v248 offset:4096
v_cvt_pk_bf16_f32 v193, v194, v195
v_cvt_pk_bf16_f32 v194, v196, v197
v_mfma_f32_16x16x32_bf16 v[100:103], a[116:119], a[104:107], v[100:103]
ds_read_b128 a[76:79], v244 offset:5120
v_cvt_pk_bf16_f32 v195, v198, v199
v_cvt_pk_bf16_f32 v152, v152, v153
v_cvt_pk_bf16_f32 v153, v154, v155
v_mfma_f32_16x16x32_bf16 v[104:107], a[120:123], a[104:107], v[104:107]
buffer_load_dwordx4 v242, s[8:11], s45 offen offset:2048 lds
v_cvt_pk_bf16_f32 v154, v156, v157
v_cvt_pk_bf16_f32 v155, v158, v159
v_cvt_pk_bf16_f32 v44, v232, v233
v_mfma_f32_16x16x32_bf16 v[108:111], a[124:127], a[104:107], v[108:111]
s_cmp_eq_u32 s46, 0
s_cselect_b32 s53, s56, s57
v_cvt_pk_bf16_f32 v45, v234, v235
v_cvt_pk_bf16_f32 v54, v236, v237
v_cvt_pk_bf16_f32 v55, v238, v239
v_mfma_f32_16x16x32_bf16 v[200:203], v[160:163], v[128:131], v[24:27]
v_mfma_f32_16x16x32_bf16 v[112:115], a[112:115], a[108:111], v[112:115]
v_mfma_f32_16x16x32_bf16 v[204:207], v[168:171], v[128:131], v[20:23]
v_mfma_f32_16x16x32_bf16 v[116:119], a[116:119], a[108:111], v[116:119]
v_mfma_f32_16x16x32_bf16 v[208:211], v[168:171], v[136:139], v[16:19]
v_mfma_f32_16x16x32_bf16 v[120:123], a[120:123], a[108:111], v[120:123]
buffer_load_dwordx4 v243, s[8:11], s45 offen offset:3072 lds
v_mfma_f32_16x16x32_bf16 v[212:215], v[176:179], v[136:139], v[20:23]
v_mfma_f32_16x16x32_bf16 v[124:127], a[124:127], a[108:111], v[124:127]
s_waitcnt lgkmcnt(0)
s_mov_b32 m0, s49
s_add_u32 s45, s45, 131072
v_mfma_f32_16x16x32_bf16 v[216:219], v[176:179], v[144:147], v[16:19]
v_mfma_f32_16x16x32_bf16 v[64:67], a[80:83], a[64:67], v[64:67]
ds_read_b64_tr_b16 a[112:113], v245 offset:8192
ds_read_b64_tr_b16 a[114:115], v245 offset:12288
v_mfma_f32_16x16x32_bf16 v[220:223], v[184:187], v[144:147], v[20:23]
v_mfma_f32_16x16x32_bf16 v[68:71], a[84:87], a[64:67], v[68:71]
ds_read_b128 a[96:99], v244 offset:2048
v_add_u32 v245, s53, v245
v_mfma_f32_16x16x32_bf16 v[224:227], v[184:187], v[152:155], v[16:19]
v_mfma_f32_16x16x32_bf16 v[72:75], a[88:91], a[64:67], v[72:75]
buffer_load_dwordx4 v7, s[4:7], s44 offen offset:0 sc1 lds
v_mfma_f32_16x16x32_bf16 v[228:231], v[192:195], v[152:155], v[20:23]
v_mfma_f32_16x16x32_bf16 v[76:79], a[92:95], a[64:67], v[76:79]
ds_read_b64_tr_b16 a[116:117], v246 offset:8192
ds_read_b64_tr_b16 a[118:119], v246 offset:12288
v_max3_f32 v56, v200, v201, v202
v_max3_f32 v58, v208, v209, v210
v_mfma_f32_16x16x32_bf16 v[80:83], a[80:83], a[68:71], v[80:83]
ds_read_b128 a[100:103], v244 offset:3072
v_add_u32 v246, s53, v246
v_max3_f32 v60, v216, v217, v218
v_max3_f32 v62, v224, v225, v226
v_mfma_f32_16x16x32_bf16 v[84:87], a[84:87], a[68:71], v[84:87]
ds_read_b64_tr_b16 a[120:121], v247 offset:8192
ds_read_b64_tr_b16 a[122:123], v247 offset:12288
v_max3_f32 v57, v203, v204, v205
v_max3_f32 v59, v211, v212, v213
v_mfma_f32_16x16x32_bf16 v[88:91], a[88:91], a[68:71], v[88:91]
buffer_load_dwordx4 v7, s[4:7], s44 offen offset:1024 sc1 lds
v_max3_f32 v61, v219, v220, v221
v_max3_f32 v63, v227, v228, v229
v_max3_f32 v56, v56, v206, v207
v_mfma_f32_16x16x32_bf16 v[92:95], a[92:95], a[68:71], v[92:95]
ds_read_b128 a[104:107], v244 offset:6144
v_add_u32 v247, s53, v247
v_max3_f32 v58, v58, v214, v215
v_max3_f32 v60, v60, v222, v223
v_mfma_f32_16x16x32_bf16 v[96:99], a[80:83], a[72:75], v[96:99]
ds_read_b64_tr_b16 a[124:125], v248 offset:8192
ds_read_b64_tr_b16 a[126:127], v248 offset:12288
v_max3_f32 v62, v62, v230, v231
v_max_f32 v56, v56, v57
v_mfma_f32_16x16x32_bf16 v[100:103], a[84:87], a[72:75], v[100:103]
ds_read_b128 a[108:111], v244 offset:7168
v_add_u32 v248, s53, v248
v_max_f32 v58, v58, v59
v_max_f32 v60, v60, v61
v_mfma_f32_16x16x32_bf16 v[104:107], a[88:91], a[72:75], v[104:107]
buffer_load_dwordx4 v7, s[4:7], s44 offen offset:2048 sc1 lds
v_max_f32 v62, v62, v63
v_mov_b32 v57, v56
v_mov_b32 v59, v58
v_mfma_f32_16x16x32_bf16 v[108:111], a[92:95], a[72:75], v[108:111]
s_add_u32 s49, s46, s91
v_add_u32 v244, s53, v244
v_mov_b32 v61, v60
v_mov_b32 v63, v62
v_mfma_f32_16x16x32_bf16 v[112:115], a[80:83], a[76:79], v[112:115]
s_add_u32 s46, s46, s53
v_permlane16_swap_b32 v56, v57
v_permlane16_swap_b32 v58, v59
v_mfma_f32_16x16x32_bf16 v[116:119], a[84:87], a[76:79], v[116:119]
v_permlane16_swap_b32 v60, v61
v_permlane16_swap_b32 v62, v63
v_mfma_f32_16x16x32_bf16 v[120:123], a[88:91], a[76:79], v[120:123]
buffer_load_dwordx4 v7, s[4:7], s44 offen offset:3072 sc1 lds
v_max_f32 v56, v56, v57
v_max_f32 v58, v58, v59
v_max_f32 v60, v60, v61
v_mfma_f32_16x16x32_bf16 v[124:127], a[92:95], a[76:79], v[124:127]
s_waitcnt vmcnt(24) lgkmcnt(0)
s_barrier
v_max_f32 v62, v62, v63
v_mov_b32 v57, v56
v_mov_b32 v59, v58
v_mfma_f32_16x16x32_bf16 v[64:67], a[112:115], a[96:99], v[64:67]
ds_read_b64_tr_b16 a[80:81], v245 offset:0
ds_read_b64_tr_b16 a[82:83], v245 offset:4096
v_mov_b32 v61, v60
v_mov_b32 v63, v62
v_mfma_f32_16x16x32_bf16 v[68:71], a[116:119], a[96:99], v[68:71]
s_add_u32 m0, s49, 16384
ds_read_b128 a[64:67], v244 offset:0
v_permlane32_swap_b32 v56, v57
v_mfma_f32_16x16x32_bf16 v[72:75], a[120:123], a[96:99], v[72:75]
buffer_load_dwordx4 v240, s[8:11], s45 offen offset:0 lds
v_permlane32_swap_b32 v58, v59
v_mfma_f32_16x16x32_bf16 v[76:79], a[124:127], a[96:99], v[76:79]
ds_read_b64_tr_b16 a[84:85], v246 offset:0
ds_read_b64_tr_b16 a[86:87], v246 offset:4096
v_permlane32_swap_b32 v60, v61
v_mfma_f32_16x16x32_bf16 v[80:83], a[112:115], a[100:103], v[80:83]
s_add_u32 s44, s44, 4096
ds_read_b128 a[68:71], v244 offset:1024
v_permlane32_swap_b32 v62, v63
v_mfma_f32_16x16x32_bf16 v[84:87], a[116:119], a[100:103], v[84:87]
ds_read_b64_tr_b16 a[88:89], v247 offset:0
ds_read_b64_tr_b16 a[90:91], v247 offset:4096
v_max_f32 v56, v56, v57
v_max_f32 v58, v58, v59
v_mfma_f32_16x16x32_bf16 v[88:91], a[120:123], a[100:103], v[88:91]
buffer_load_dwordx4 v241, s[8:11], s45 offen offset:1024 lds
v_max_f32 v60, v60, v61
v_max_f32 v62, v62, v63
v_mul_f32 v56, s39, v56
v_mfma_f32_16x16x32_bf16 v[92:95], a[124:127], a[100:103], v[92:95]
ds_read_b128 a[72:75], v244 offset:4096
v_mul_f32 v58, s39, v58
v_mul_f32 v60, s39, v60
v_mul_f32 v62, s39, v62
v_mfma_f32_16x16x32_bf16 v[96:99], a[112:115], a[104:107], v[96:99]
ds_read_b64_tr_b16 a[92:93], v248 offset:0
ds_read_b64_tr_b16 a[94:95], v248 offset:4096
v_fma_f32 v200, v200, s39, -v56
v_fma_f32 v208, v208, s39, -v58
v_mfma_f32_16x16x32_bf16 v[100:103], a[116:119], a[104:107], v[100:103]
ds_read_b128 a[76:79], v244 offset:5120
v_fma_f32 v216, v216, s39, -v60
v_fma_f32 v224, v224, s39, -v62
v_fma_f32 v201, v201, s39, -v56
v_mfma_f32_16x16x32_bf16 v[104:107], a[120:123], a[104:107], v[104:107]
buffer_load_dwordx4 v242, s[8:11], s45 offen offset:2048 lds
v_fma_f32 v209, v209, s39, -v58
v_fma_f32 v217, v217, s39, -v60
v_fma_f32 v225, v225, s39, -v62
v_mfma_f32_16x16x32_bf16 v[108:111], a[124:127], a[104:107], v[108:111]
s_cmp_eq_u32 s46, 0
s_cselect_b32 s53, s56, s57
v_fma_f32 v202, v202, s39, -v56
v_fma_f32 v210, v210, s39, -v58
v_fma_f32 v218, v218, s39, -v60
v_mfma_f32_16x16x32_bf16 v[112:115], a[112:115], a[108:111], v[112:115]
v_fma_f32 v226, v226, s39, -v62
v_fma_f32 v203, v203, s39, -v56
v_fma_f32 v211, v211, s39, -v58
v_fma_f32 v219, v219, s39, -v60
v_mfma_f32_16x16x32_bf16 v[116:119], a[116:119], a[108:111], v[116:119]
v_fma_f32 v227, v227, s39, -v62
v_fma_f32 v204, v204, s39, -v56
v_fma_f32 v212, v212, s39, -v58
v_fma_f32 v220, v220, s39, -v60
v_mfma_f32_16x16x32_bf16 v[120:123], a[120:123], a[108:111], v[120:123]
buffer_load_dwordx4 v243, s[8:11], s45 offen offset:3072 lds
v_fma_f32 v228, v228, s39, -v62
v_fma_f32 v205, v205, s39, -v56
v_fma_f32 v213, v213, s39, -v58
v_mfma_f32_16x16x32_bf16 v[124:127], a[124:127], a[108:111], v[124:127]
s_waitcnt lgkmcnt(0)
s_mov_b32 m0, s49
s_add_u32 s45, s45, 131072
v_fma_f32 v221, v221, s39, -v60
v_fma_f32 v229, v229, s39, -v62
v_mfma_f32_16x16x32_bf16 v[64:67], a[80:83], a[64:67], v[64:67]
ds_read_b64_tr_b16 a[112:113], v245 offset:8192
ds_read_b64_tr_b16 a[114:115], v245 offset:12288
v_fma_f32 v206, v206, s39, -v56
v_fma_f32 v214, v214, s39, -v58
v_mfma_f32_16x16x32_bf16 v[68:71], a[84:87], a[64:67], v[68:71]
ds_read_b128 a[96:99], v244 offset:2048
v_add_u32 v245, s53, v245
v_fma_f32 v222, v222, s39, -v60
v_fma_f32 v230, v230, s39, -v62
v_mfma_f32_16x16x32_bf16 v[72:75], a[88:91], a[64:67], v[72:75]
buffer_load_dwordx4 v7, s[4:7], s44 offen offset:0 sc1 lds
v_fma_f32 v207, v207, s39, -v56
v_fma_f32 v215, v215, s39, -v58
v_fma_f32 v223, v223, s39, -v60
v_mfma_f32_16x16x32_bf16 v[76:79], a[92:95], a[64:67], v[76:79]
ds_read_b64_tr_b16 a[116:117], v246 offset:8192
ds_read_b64_tr_b16 a[118:119], v246 offset:12288
v_fma_f32 v231, v231, s39, -v62
v_mfma_f32_16x16x32_bf16 v[80:83], a[80:83], a[68:71], v[80:83]
ds_read_b128 a[100:103], v244 offset:3072
v_add_u32 v246, s53, v246
v_exp_f32 v200, v200
v_mfma_f32_16x16x32_bf16 v[84:87], a[84:87], a[68:71], v[84:87]
ds_read_b64_tr_b16 a[120:121], v247 offset:8192
ds_read_b64_tr_b16 a[122:123], v247 offset:12288
v_exp_f32 v208, v208
v_mfma_f32_16x16x32_bf16 v[88:91], a[88:91], a[68:71], v[88:91]
buffer_load_dwordx4 v7, s[4:7], s44 offen offset:1024 sc1 lds
v_exp_f32 v216, v216
v_mfma_f32_16x16x32_bf16 v[92:95], a[92:95], a[68:71], v[92:95]
ds_read_b128 a[104:107], v244 offset:6144
v_add_u32 v247, s53, v247
v_exp_f32 v224, v224
v_mfma_f32_16x16x32_bf16 v[96:99], a[80:83], a[72:75], v[96:99]
ds_read_b64_tr_b16 a[124:125], v248 offset:8192
ds_read_b64_tr_b16 a[126:127], v248 offset:12288
v_exp_f32 v201, v201
v_mfma_f32_16x16x32_bf16 v[100:103], a[84:87], a[72:75], v[100:103]
ds_read_b128 a[108:111], v244 offset:7168
v_add_u32 v248, s53, v248
v_exp_f32 v209, v209
v_mfma_f32_16x16x32_bf16 v[104:107], a[88:91], a[72:75], v[104:107]
buffer_load_dwordx4 v7, s[4:7], s44 offen offset:2048 sc1 lds
v_exp_f32 v217, v217
v_mfma_f32_16x16x32_bf16 v[108:111], a[92:95], a[72:75], v[108:111]
s_add_u32 s49, s46, s91
v_add_u32 v244, s53, v244
v_exp_f32 v225, v225
v_mfma_f32_16x16x32_bf16 v[112:115], a[80:83], a[76:79], v[112:115]
s_add_u32 s46, s46, s53
v_exp_f32 v202, v202
v_exp_f32 v210, v210
v_mfma_f32_16x16x32_bf16 v[116:119], a[84:87], a[76:79], v[116:119]
v_exp_f32 v218, v218
v_exp_f32 v226, v226
v_mfma_f32_16x16x32_bf16 v[120:123], a[88:91], a[76:79], v[120:123]
buffer_load_dwordx4 v7, s[4:7], s44 offen offset:3072 sc1 lds
v_exp_f32 v203, v203
v_mfma_f32_16x16x32_bf16 v[124:127], a[92:95], a[76:79], v[124:127]
s_waitcnt vmcnt(24) lgkmcnt(0)
s_barrier
v_exp_f32 v211, v211
v_mfma_f32_16x16x32_bf16 v[64:67], a[112:115], a[96:99], v[64:67]
ds_read_b64_tr_b16 a[80:81], v245 offset:0
ds_read_b64_tr_b16 a[82:83], v245 offset:4096
v_exp_f32 v219, v219
v_mfma_f32_16x16x32_bf16 v[68:71], a[116:119], a[96:99], v[68:71]
s_add_u32 m0, s49, 16384
ds_read_b128 a[64:67], v244 offset:0
v_exp_f32 v227, v227
v_mfma_f32_16x16x32_bf16 v[72:75], a[120:123], a[96:99], v[72:75]
buffer_load_dwordx4 v240, s[8:11], s45 offen offset:0 lds
v_exp_f32 v204, v204
v_mfma_f32_16x16x32_bf16 v[76:79], a[124:127], a[96:99], v[76:79]
ds_read_b64_tr_b16 a[84:85], v246 offset:0
ds_read_b64_tr_b16 a[86:87], v246 offset:4096
v_exp_f32 v212, v212
v_mfma_f32_16x16x32_bf16 v[80:83], a[112:115], a[100:103], v[80:83]
s_add_u32 s44, s44, 4096
ds_read_b128 a[68:71], v244 offset:1024
v_exp_f32 v220, v220
v_mfma_f32_16x16x32_bf16 v[84:87], a[116:119], a[100:103], v[84:87]
ds_read_b64_tr_b16 a[88:89], v247 offset:0
ds_read_b64_tr_b16 a[90:91], v247 offset:4096
v_exp_f32 v228, v228
v_mfma_f32_16x16x32_bf16 v[88:91], a[120:123], a[100:103], v[88:91]
buffer_load_dwordx4 v241, s[8:11], s45 offen offset:1024 lds
v_exp_f32 v205, v205
v_mfma_f32_16x16x32_bf16 v[92:95], a[124:127], a[100:103], v[92:95]
ds_read_b128 a[72:75], v244 offset:4096
v_exp_f32 v213, v213
v_mfma_f32_16x16x32_bf16 v[96:99], a[112:115], a[104:107], v[96:99]
ds_read_b64_tr_b16 a[92:93], v248 offset:0
ds_read_b64_tr_b16 a[94:95], v248 offset:4096
v_exp_f32 v221, v221
v_mfma_f32_16x16x32_bf16 v[100:103], a[116:119], a[104:107], v[100:103]
ds_read_b128 a[76:79], v244 offset:5120
v_exp_f32 v229, v229
v_mfma_f32_16x16x32_bf16 v[104:107], a[120:123], a[104:107], v[104:107]
buffer_load_dwordx4 v242, s[8:11], s45 offen offset:2048 lds
v_exp_f32 v206, v206
v_mfma_f32_16x16x32_bf16 v[108:111], a[124:127], a[104:107], v[108:111]
s_cmp_eq_u32 s46, 0
s_cselect_b32 s53, s56, s57
v_exp_f32 v214, v214
v_mfma_f32_16x16x32_bf16 v[112:115], a[112:115], a[108:111], v[112:115]
v_exp_f32 v222, v222
v_exp_f32 v230, v230
v_mfma_f32_16x16x32_bf16 v[116:119], a[116:119], a[108:111], v[116:119]
v_exp_f32 v207, v207
v_exp_f32 v215, v215
v_mfma_f32_16x16x32_bf16 v[120:123], a[120:123], a[108:111], v[120:123]
buffer_load_dwordx4 v243, s[8:11], s45 offen offset:3072 lds
v_exp_f32 v223, v223
v_mfma_f32_16x16x32_bf16 v[124:127], a[124:127], a[108:111], v[124:127]
s_waitcnt lgkmcnt(0)
s_mov_b32 m0, s49
s_add_u32 s45, s45, 131072
v_exp_f32 v231, v231
v_mfma_f32_16x16x32_bf16 v[64:67], a[80:83], a[64:67], v[64:67]
ds_read_b64_tr_b16 a[112:113], v245 offset:8192
ds_read_b64_tr_b16 a[114:115], v245 offset:12288
v_cvt_pk_bf16_f32 v132, v200, v201
v_cvt_pk_bf16_f32 v140, v208, v209
v_mfma_f32_16x16x32_bf16 v[68:71], a[84:87], a[64:67], v[68:71]
ds_read_b128 a[96:99], v244 offset:2048
v_add_u32 v245, s53, v245
v_cvt_pk_bf16_f32 v148, v216, v217
v_cvt_pk_bf16_f32 v156, v224, v225
v_mfma_f32_16x16x32_bf16 v[72:75], a[88:91], a[64:67], v[72:75]
buffer_load_dwordx4 v7, s[4:7], s44 offen offset:0 sc1 lds
v_cvt_pk_bf16_f32 v133, v202, v203
v_cvt_pk_bf16_f32 v141, v210, v211
v_cvt_pk_bf16_f32 v149, v218, v219
v_mfma_f32_16x16x32_bf16 v[76:79], a[92:95], a[64:67], v[76:79]
ds_read_b64_tr_b16 a[116:117], v246 offset:8192
ds_read_b64_tr_b16 a[118:119], v246 offset:12288
v_cvt_pk_bf16_f32 v157, v226, v227
v_cvt_pk_bf16_f32 v134, v204, v205
v_mfma_f32_16x16x32_bf16 v[80:83], a[80:83], a[68:71], v[80:83]
ds_read_b128 a[100:103], v244 offset:3072
v_add_u32 v246, s53, v246
v_cvt_pk_bf16_f32 v142, v212, v213
v_cvt_pk_bf16_f32 v150, v220, v221
v_mfma_f32_16x16x32_bf16 v[84:87], a[84:87], a[68:71], v[84:87]
ds_read_b64_tr_b16 a[120:121], v247 offset:8192
ds_read_b64_tr_b16 a[122:123], v247 offset:12288
v_cvt_pk_bf16_f32 v158, v228, v229
v_cvt_pk_bf16_f32 v135, v206, v207
v_mfma_f32_16x16x32_bf16 v[88:91], a[88:91], a[68:71], v[88:91]
buffer_load_dwordx4 v7, s[4:7], s44 offen offset:1024 sc1 lds
v_cvt_pk_bf16_f32 v143, v214, v215
v_cvt_pk_bf16_f32 v151, v222, v223
v_cvt_pk_bf16_f32 v159, v230, v231
v_mfma_f32_16x16x32_bf16 v[92:95], a[92:95], a[68:71], v[92:95]
ds_read_b128 a[104:107], v244 offset:6144
v_add_u32 v247, s53, v247
v_add_f32 v56, v200, v201
v_add_f32 v58, v208, v209
v_mfma_f32_16x16x32_bf16 v[96:99], a[80:83], a[72:75], v[96:99]
ds_read_b64_tr_b16 a[124:125], v248 offset:8192
ds_read_b64_tr_b16 a[126:127], v248 offset:12288
v_add_f32 v60, v216, v217
v_add_f32 v62, v224, v225
v_mfma_f32_16x16x32_bf16 v[160:163], v[36:39], v[132:135], 0
v_mfma_f32_16x16x32_bf16 v[100:103], a[84:87], a[72:75], v[100:103]
ds_read_b128 a[108:111], v244 offset:7168
v_add_u32 v248, s53, v248
v_mfma_f32_16x16x32_bf16 v[164:167], v[46:49], v[132:135], 0
v_mfma_f32_16x16x32_bf16 v[104:107], a[88:91], a[72:75], v[104:107]
buffer_load_dwordx4 v7, s[4:7], s44 offen offset:2048 sc1 lds
v_mfma_f32_16x16x32_bf16 v[168:171], v[38:41], v[140:143], 0
v_mfma_f32_16x16x32_bf16 v[108:111], a[92:95], a[72:75], v[108:111]
s_add_u32 s49, s46, s91
v_add_u32 v244, s53, v244
v_mfma_f32_16x16x32_bf16 v[172:175], v[48:51], v[140:143], 0
v_mfma_f32_16x16x32_bf16 v[112:115], a[80:83], a[76:79], v[112:115]
s_add_u32 s46, s46, s53
v_mfma_f32_16x16x32_bf16 v[176:179], v[40:43], v[148:151], 0
v_mfma_f32_16x16x32_bf16 v[116:119], a[84:87], a[76:79], v[116:119]
v_mfma_f32_16x16x32_bf16 v[180:183], v[50:53], v[148:151], 0
v_mfma_f32_16x16x32_bf16 v[120:123], a[88:91], a[76:79], v[120:123]
buffer_load_dwordx4 v7, s[4:7], s44 offen offset:3072 sc1 lds
v_mfma_f32_16x16x32_bf16 v[184:187], v[42:45], v[156:159], 0
v_mfma_f32_16x16x32_bf16 v[124:127], a[92:95], a[76:79], v[124:127]
s_waitcnt vmcnt(24) lgkmcnt(0)
s_barrier
v_mfma_f32_16x16x32_bf16 v[188:191], v[52:55], v[156:159], 0
v_mfma_f32_16x16x32_bf16 v[64:67], a[112:115], a[96:99], v[64:67]
ds_read_b64_tr_b16 a[80:81], v245 offset:0
ds_read_b64_tr_b16 a[82:83], v245 offset:4096
v_add_f32 v57, v202, v203
v_add_f32 v59, v210, v211
v_mfma_f32_16x16x32_bf16 v[68:71], a[116:119], a[96:99], v[68:71]
s_add_u32 m0, s49, 16384
ds_read_b128 a[64:67], v244 offset:0
v_add_f32 v61, v218, v219
v_add_f32 v63, v226, v227
v_mfma_f32_16x16x32_bf16 v[72:75], a[120:123], a[96:99], v[72:75]
buffer_load_dwordx4 v240, s[8:11], s45 offen offset:0 lds
v_add_f32 v56, v56, v204
v_add_f32 v58, v58, v212
v_add_f32 v60, v60, v220
v_mfma_f32_16x16x32_bf16 v[76:79], a[124:127], a[96:99], v[76:79]
ds_read_b64_tr_b16 a[84:85], v246 offset:0
ds_read_b64_tr_b16 a[86:87], v246 offset:4096
v_add_f32 v62, v62, v228
v_add_f32 v57, v57, v205
v_mfma_f32_16x16x32_bf16 v[80:83], a[112:115], a[100:103], v[80:83]
s_add_u32 s44, s44, 4096
ds_read_b128 a[68:71], v244 offset:1024
v_add_f32 v59, v59, v213
v_add_f32 v61, v61, v221
v_mfma_f32_16x16x32_bf16 v[84:87], a[116:119], a[100:103], v[84:87]
ds_read_b64_tr_b16 a[88:89], v247 offset:0
ds_read_b64_tr_b16 a[90:91], v247 offset:4096
v_add_f32 v63, v63, v229
v_add_f32 v56, v56, v206
v_mfma_f32_16x16x32_bf16 v[88:91], a[120:123], a[100:103], v[88:91]
buffer_load_dwordx4 v241, s[8:11], s45 offen offset:1024 lds
v_add_f32 v58, v58, v214
v_add_f32 v60, v60, v222
v_add_f32 v62, v62, v230
v_mfma_f32_16x16x32_bf16 v[92:95], a[124:127], a[100:103], v[92:95]
ds_read_b128 a[72:75], v244 offset:4096
v_add_f32 v57, v57, v207
v_add_f32 v59, v59, v215
v_add_f32 v61, v61, v223
v_mfma_f32_16x16x32_bf16 v[96:99], a[112:115], a[104:107], v[96:99]
ds_read_b64_tr_b16 a[92:93], v248 offset:0
ds_read_b64_tr_b16 a[94:95], v248 offset:4096
v_add_f32 v63, v63, v231
v_add_f32 v56, v56, v57
v_mfma_f32_16x16x32_bf16 v[100:103], a[116:119], a[104:107], v[100:103]
ds_read_b128 a[76:79], v244 offset:5120
v_add_f32 v58, v58, v59
v_add_f32 v60, v60, v61
v_add_f32 v62, v62, v63
v_mfma_f32_16x16x32_bf16 v[104:107], a[120:123], a[104:107], v[104:107]
buffer_load_dwordx4 v242, s[8:11], s45 offen offset:2048 lds
v_mov_b32 v57, v56
v_mov_b32 v59, v58
v_mov_b32 v61, v60
v_mfma_f32_16x16x32_bf16 v[108:111], a[124:127], a[104:107], v[108:111]
s_cmp_eq_u32 s46, 0
s_cselect_b32 s53, s56, s57
v_mov_b32 v63, v62
v_permlane16_swap_b32 v56, v57
v_mfma_f32_16x16x32_bf16 v[112:115], a[112:115], a[108:111], v[112:115]
v_permlane16_swap_b32 v58, v59
v_permlane16_swap_b32 v60, v61
v_mfma_f32_16x16x32_bf16 v[116:119], a[116:119], a[108:111], v[116:119]
v_permlane16_swap_b32 v62, v63
v_add_f32 v56, v56, v57
v_add_f32 v58, v58, v59
v_mfma_f32_16x16x32_bf16 v[120:123], a[120:123], a[108:111], v[120:123]
buffer_load_dwordx4 v243, s[8:11], s45 offen offset:3072 lds
v_add_f32 v60, v60, v61
v_add_f32 v62, v62, v63
v_mov_b32 v57, v56
v_mfma_f32_16x16x32_bf16 v[124:127], a[124:127], a[108:111], v[124:127]
s_waitcnt lgkmcnt(0)
s_mov_b32 m0, s49
s_add_u32 s45, s45, 131072
v_mov_b32 v59, v58
v_mov_b32 v61, v60
v_mfma_f32_16x16x32_bf16 v[64:67], a[80:83], a[64:67], v[64:67]
ds_read_b64_tr_b16 a[112:113], v245 offset:8192
ds_read_b64_tr_b16 a[114:115], v245 offset:12288
v_mov_b32 v63, v62
v_mfma_f32_16x16x32_bf16 v[68:71], a[84:87], a[64:67], v[68:71]
ds_read_b128 a[96:99], v244 offset:2048
v_add_u32 v245, s53, v245
v_permlane32_swap_b32 v56, v57
v_mfma_f32_16x16x32_bf16 v[72:75], a[88:91], a[64:67], v[72:75]
buffer_load_dwordx4 v7, s[4:7], s44 offen offset:0 sc1 lds
v_permlane32_swap_b32 v58, v59
v_mfma_f32_16x16x32_bf16 v[76:79], a[92:95], a[64:67], v[76:79]
ds_read_b64_tr_b16 a[116:117], v246 offset:8192
ds_read_b64_tr_b16 a[118:119], v246 offset:12288
v_permlane32_swap_b32 v60, v61
v_mfma_f32_16x16x32_bf16 v[80:83], a[80:83], a[68:71], v[80:83]
ds_read_b128 a[100:103], v244 offset:3072
v_add_u32 v246, s53, v246
v_permlane32_swap_b32 v62, v63
v_mfma_f32_16x16x32_bf16 v[84:87], a[84:87], a[68:71], v[84:87]
ds_read_b64_tr_b16 a[120:121], v247 offset:8192
ds_read_b64_tr_b16 a[122:123], v247 offset:12288
v_add_f32 v56, v56, v57
v_add_f32 v58, v58, v59
v_mfma_f32_16x16x32_bf16 v[88:91], a[88:91], a[68:71], v[88:91]
buffer_load_dwordx4 v7, s[4:7], s44 offen offset:1024 sc1 lds
v_add_f32 v60, v60, v61
v_add_f32 v62, v62, v63
v_mfma_f32_16x16x32_bf16 v[92:95], a[92:95], a[68:71], v[92:95]
ds_read_b128 a[104:107], v244 offset:6144
v_add_u32 v247, s53, v247
v_rcp_f32 v56, v56
v_mfma_f32_16x16x32_bf16 v[96:99], a[80:83], a[72:75], v[96:99]
ds_read_b64_tr_b16 a[124:125], v248 offset:8192
ds_read_b64_tr_b16 a[126:127], v248 offset:12288
v_rcp_f32 v58, v58
v_mfma_f32_16x16x32_bf16 v[100:103], a[84:87], a[72:75], v[100:103]
ds_read_b128 a[108:111], v244 offset:7168
v_add_u32 v248, s53, v248
v_rcp_f32 v60, v60
v_mfma_f32_16x16x32_bf16 v[104:107], a[88:91], a[72:75], v[104:107]
buffer_load_dwordx4 v7, s[4:7], s44 offen offset:2048 sc1 lds
v_rcp_f32 v62, v62
v_mul_f32 v160, v56, v160
v_mfma_f32_16x16x32_bf16 v[108:111], a[92:95], a[72:75], v[108:111]
s_add_u32 s49, s46, s91
v_add_u32 v244, s53, v244
v_mul_f32 v161, v56, v161
v_mul_f32 v162, v56, v162
v_mul_f32 v163, v56, v163
v_mfma_f32_16x16x32_bf16 v[112:115], a[80:83], a[76:79], v[112:115]
s_add_u32 s46, s46, s53
v_mul_f32 v164, v56, v164
v_mul_f32 v165, v56, v165
v_mul_f32 v166, v56, v166
v_mul_f32 v167, v56, v167
v_mfma_f32_16x16x32_bf16 v[116:119], a[84:87], a[76:79], v[116:119]
v_cvt_pk_bf16_f32 v128, v160, v161
v_cvt_pk_bf16_f32 v129, v162, v163
v_cvt_pk_bf16_f32 v130, v164, v165
.Lpad19:
s_cbranch_execz .Ltch8
v_cvt_pk_bf16_f32 v131, v166, v167
v_mfma_f32_16x16x32_bf16 v[120:123], a[88:91], a[76:79], v[120:123]
buffer_load_dwordx4 v7, s[4:7], s44 offen offset:3072 sc1 lds
buffer_store_dwordx4 v[128:131], v7, s[20:23], s40 offen offset:0 sc1
v_mul_f32 v168, v58, v168
v_mfma_f32_16x16x32_bf16 v[124:127], a[92:95], a[76:79], v[124:127]
s_waitcnt vmcnt(25) lgkmcnt(0)
s_barrier
v_mul_f32 v169, v58, v169
v_mul_f32 v170, v58, v170
v_mul_f32 v171, v58, v171
v_mfma_f32_16x16x32_bf16 v[64:67], a[112:115], a[96:99], v[64:67]
ds_read_b64_tr_b16 a[80:81], v245 offset:0
ds_read_b64_tr_b16 a[82:83], v245 offset:4096
v_mul_f32 v172, v58, v172
v_mul_f32 v173, v58, v173
v_mfma_f32_16x16x32_bf16 v[68:71], a[116:119], a[96:99], v[68:71]
s_add_u32 m0, s49, 16384
ds_read_b128 a[64:67], v244 offset:0
v_mul_f32 v174, v58, v174
v_mul_f32 v175, v58, v175
v_mfma_f32_16x16x32_bf16 v[72:75], a[120:123], a[96:99], v[72:75]
buffer_load_dwordx4 v240, s[8:11], s45 offen offset:0 lds
v_cvt_pk_bf16_f32 v136, v168, v169
v_cvt_pk_bf16_f32 v137, v170, v171
v_cvt_pk_bf16_f32 v138, v172, v173
v_mfma_f32_16x16x32_bf16 v[76:79], a[124:127], a[96:99], v[76:79]
ds_read_b64_tr_b16 a[84:85], v246 offset:0
ds_read_b64_tr_b16 a[86:87], v246 offset:4096
v_cvt_pk_bf16_f32 v139, v174, v175
v_mfma_f32_16x16x32_bf16 v[80:83], a[112:115], a[100:103], v[80:83]
s_add_u32 s44, s44, 4096
buffer_store_dwordx4 v[136:139], v7, s[20:23], s40 offen offset:1024 sc1
ds_read_b128 a[68:71], v244 offset:1024
v_mfma_f32_16x16x32_bf16 v[84:87], a[116:119], a[100:103], v[84:87]
ds_read_b64_tr_b16 a[88:89], v247 offset:0
ds_read_b64_tr_b16 a[90:91], v247 offset:4096
v_mul_f32 v176, v60, v176
v_mul_f32 v177, v60, v177
v_mfma_f32_16x16x32_bf16 v[88:91], a[120:123], a[100:103], v[88:91]
buffer_load_dwordx4 v241, s[8:11], s45 offen offset:1024 lds
v_mul_f32 v178, v60, v178
v_mul_f32 v179, v60, v179
v_mul_f32 v180, v60, v180
v_mfma_f32_16x16x32_bf16 v[92:95], a[124:127], a[100:103], v[92:95]
ds_read_b128 a[72:75], v244 offset:4096
v_mul_f32 v181, v60, v181
v_mul_f32 v182, v60, v182
v_mul_f32 v183, v60, v183
v_mfma_f32_16x16x32_bf16 v[96:99], a[112:115], a[104:107], v[96:99]
ds_read_b64_tr_b16 a[92:93], v248 offset:0
ds_read_b64_tr_b16 a[94:95], v248 offset:4096
v_cvt_pk_bf16_f32 v144, v176, v177
v_cvt_pk_bf16_f32 v145, v178, v179
v_mfma_f32_16x16x32_bf16 v[100:103], a[116:119], a[104:107], v[100:103]
ds_read_b128 a[76:79], v244 offset:5120
v_cvt_pk_bf16_f32 v146, v180, v181
v_cvt_pk_bf16_f32 v147, v182, v183
v_mfma_f32_16x16x32_bf16 v[104:107], a[120:123], a[104:107], v[104:107]
buffer_load_dwordx4 v242, s[8:11], s45 offen offset:2048 lds
buffer_store_dwordx4 v[144:147], v7, s[20:23], s41 offen offset:0 sc1
v_mul_f32 v184, v62, v184
v_mfma_f32_16x16x32_bf16 v[108:111], a[124:127], a[104:107], v[108:111]
s_cmp_eq_u32 s46, 0
s_cselect_b32 s53, s56, s57
v_mul_f32 v185, v62, v185
v_mul_f32 v186, v62, v186
v_mul_f32 v187, v62, v187
v_mfma_f32_16x16x32_bf16 v[112:115], a[112:115], a[108:111], v[112:115]
v_mul_f32 v188, v62, v188
v_mul_f32 v189, v62, v189
v_mul_f32 v190, v62, v190
v_mul_f32 v191, v62, v191
v_mfma_f32_16x16x32_bf16 v[116:119], a[116:119], a[108:111], v[116:119]
v_cvt_pk_bf16_f32 v152, v184, v185
v_cvt_pk_bf16_f32 v153, v186, v187
v_cvt_pk_bf16_f32 v154, v188, v189
v_cvt_pk_bf16_f32 v155, v190, v191
v_mfma_f32_16x16x32_bf16 v[120:123], a[120:123], a[108:111], v[120:123]
buffer_load_dwordx4 v243, s[8:11], s45 offen offset:3072 lds
buffer_store_dwordx4 v[152:155], v7, s[20:23], s41 offen offset:1024 sc1
v_mfma_f32_16x16x32_bf16 v[124:127], a[124:127], a[108:111], v[124:127]
s_waitcnt lgkmcnt(0)
s_mov_b32 m0, s49
s_add_u32 s45, s45, 131072
v_mfma_f32_16x16x32_bf16 v[64:67], a[80:83], a[64:67], v[64:67]
ds_read_b64_tr_b16 a[112:113], v245 offset:8192
ds_read_b64_tr_b16 a[114:115], v245 offset:12288
v_mfma_f32_16x16x32_bf16 v[68:71], a[84:87], a[64:67], v[68:71]
ds_read_b128 a[96:99], v244 offset:2048
v_add_u32 v245, s53, v245
v_mfma_f32_16x16x32_bf16 v[72:75], a[88:91], a[64:67], v[72:75]
buffer_load_dwordx4 v7, s[4:7], s44 offen offset:0 sc1 lds
v_mfma_f32_16x16x32_bf16 v[76:79], a[92:95], a[64:67], v[76:79]
ds_read_b64_tr_b16 a[116:117], v246 offset:8192
ds_read_b64_tr_b16 a[118:119], v246 offset:12288
v_mfma_f32_16x16x32_bf16 v[80:83], a[80:83], a[68:71], v[80:83]
ds_read_b128 a[100:103], v244 offset:3072
v_add_u32 v246, s53, v246
v_mfma_f32_16x16x32_bf16 v[84:87], a[84:87], a[68:71], v[84:87]
ds_read_b64_tr_b16 a[120:121], v247 offset:8192
ds_read_b64_tr_b16 a[122:123], v247 offset:12288
v_mfma_f32_16x16x32_bf16 v[88:91], a[88:91], a[68:71], v[88:91]
buffer_load_dwordx4 v7, s[4:7], s44 offen offset:1024 sc1 lds
v_mfma_f32_16x16x32_bf16 v[92:95], a[92:95], a[68:71], v[92:95]
ds_read_b128 a[104:107], v244 offset:6144
v_add_u32 v247, s53, v247
v_mfma_f32_16x16x32_bf16 v[96:99], a[80:83], a[72:75], v[96:99]
ds_read_b64_tr_b16 a[124:125], v248 offset:8192
ds_read_b64_tr_b16 a[126:127], v248 offset:12288
v_mfma_f32_16x16x32_bf16 v[100:103], a[84:87], a[72:75], v[100:103]
ds_read_b128 a[108:111], v244 offset:7168
v_add_u32 v248, s53, v248
v_mfma_f32_16x16x32_bf16 v[104:107], a[88:91], a[72:75], v[104:107]
buffer_load_dwordx4 v7, s[4:7], s44 offen offset:2048 sc1 lds
v_mfma_f32_16x16x32_bf16 v[108:111], a[92:95], a[72:75], v[108:111]
s_add_u32 s49, s46, s91
v_add_u32 v244, s53, v244
v_mfma_f32_16x16x32_bf16 v[112:115], a[80:83], a[76:79], v[112:115]
s_add_u32 s46, s46, s53
v_mfma_f32_16x16x32_bf16 v[116:119], a[84:87], a[76:79], v[116:119]
v_mfma_f32_16x16x32_bf16 v[120:123], a[88:91], a[76:79], v[120:123]
buffer_load_dwordx4 v7, s[4:7], s44 offen offset:3072 sc1 lds
v_mfma_f32_16x16x32_bf16 v[124:127], a[92:95], a[76:79], v[124:127]
s_waitcnt vmcnt(28) lgkmcnt(0)
s_barrier
v_mfma_f32_16x16x32_bf16 v[64:67], a[112:115], a[96:99], v[64:67]
ds_read_b64_tr_b16 a[80:81], v245 offset:0
ds_read_b64_tr_b16 a[82:83], v245 offset:4096
v_mfma_f32_16x16x32_bf16 v[68:71], a[116:119], a[96:99], v[68:71]
s_add_u32 m0, s49, 16384
ds_read_b128 a[64:67], v244 offset:0
v_mfma_f32_16x16x32_bf16 v[72:75], a[120:123], a[96:99], v[72:75]
buffer_load_dwordx4 v240, s[8:11], s45 offen offset:0 lds
v_mfma_f32_16x16x32_bf16 v[76:79], a[124:127], a[96:99], v[76:79]
ds_read_b64_tr_b16 a[84:85], v246 offset:0
ds_read_b64_tr_b16 a[86:87], v246 offset:4096
v_mfma_f32_16x16x32_bf16 v[80:83], a[112:115], a[100:103], v[80:83]
s_add_u32 s44, s44, 4096
ds_read_b128 a[68:71], v244 offset:1024
v_mfma_f32_16x16x32_bf16 v[84:87], a[116:119], a[100:103], v[84:87]
ds_read_b64_tr_b16 a[88:89], v247 offset:0
ds_read_b64_tr_b16 a[90:91], v247 offset:4096
v_mfma_f32_16x16x32_bf16 v[88:91], a[120:123], a[100:103], v[88:91]
buffer_load_dwordx4 v241, s[8:11], s45 offen offset:1024 lds
v_mfma_f32_16x16x32_bf16 v[92:95], a[124:127], a[100:103], v[92:95]
ds_read_b128 a[72:75], v244 offset:4096
v_mfma_f32_16x16x32_bf16 v[96:99], a[112:115], a[104:107], v[96:99]
ds_read_b64_tr_b16 a[92:93], v248 offset:0
ds_read_b64_tr_b16 a[94:95], v248 offset:4096
v_mfma_f32_16x16x32_bf16 v[100:103], a[116:119], a[104:107], v[100:103]
ds_read_b128 a[76:79], v244 offset:5120
v_mfma_f32_16x16x32_bf16 v[104:107], a[120:123], a[104:107], v[104:107]
buffer_load_dwordx4 v242, s[8:11], s45 offen offset:2048 lds
v_mfma_f32_16x16x32_bf16 v[108:111], a[124:127], a[104:107], v[108:111]
s_cmp_eq_u32 s46, 0
s_cselect_b32 s53, s56, s57
v_mfma_f32_16x16x32_bf16 v[112:115], a[112:115], a[108:111], v[112:115]
v_mfma_f32_16x16x32_bf16 v[116:119], a[116:119], a[108:111], v[116:119]
v_mfma_f32_16x16x32_bf16 v[120:123], a[120:123], a[108:111], v[120:123]
buffer_load_dwordx4 v243, s[8:11], s45 offen offset:3072 lds
v_mfma_f32_16x16x32_bf16 v[124:127], a[124:127], a[108:111], v[124:127]
s_waitcnt lgkmcnt(0)
s_mov_b32 m0, s49
s_add_u32 s45, s45, 131072
v_mfma_f32_16x16x32_bf16 v[64:67], a[80:83], a[64:67], v[64:67]
ds_read_b64_tr_b16 a[112:113], v245 offset:8192
ds_read_b64_tr_b16 a[114:115], v245 offset:12288
v_mfma_f32_16x16x32_bf16 v[68:71], a[84:87], a[64:67], v[68:71]
ds_read_b128 a[96:99], v244 offset:2048
v_add_u32 v245, s53, v245
v_mfma_f32_16x16x32_bf16 v[72:75], a[88:91], a[64:67], v[72:75]
buffer_load_dwordx4 v7, s[4:7], s44 offen offset:0 sc1 lds
v_mfma_f32_16x16x32_bf16 v[76:79], a[92:95], a[64:67], v[76:79]
ds_read_b64_tr_b16 a[116:117], v246 offset:8192
ds_read_b64_tr_b16 a[118:119], v246 offset:12288
v_mfma_f32_16x16x32_bf16 v[80:83], a[80:83], a[68:71], v[80:83]
ds_read_b128 a[100:103], v244 offset:3072
v_add_u32 v246, s53, v246
v_mfma_f32_16x16x32_bf16 v[84:87], a[84:87], a[68:71], v[84:87]
ds_read_b64_tr_b16 a[120:121], v247 offset:8192
ds_read_b64_tr_b16 a[122:123], v247 offset:12288
v_mfma_f32_16x16x32_bf16 v[88:91], a[88:91], a[68:71], v[88:91]
buffer_load_dwordx4 v7, s[4:7], s44 offen offset:1024 sc1 lds
v_mfma_f32_16x16x32_bf16 v[92:95], a[92:95], a[68:71], v[92:95]
ds_read_b128 a[104:107], v244 offset:6144
v_add_u32 v247, s53, v247
v_mfma_f32_16x16x32_bf16 v[96:99], a[80:83], a[72:75], v[96:99]
ds_read_b64_tr_b16 a[124:125], v248 offset:8192
ds_read_b64_tr_b16 a[126:127], v248 offset:12288
v_mfma_f32_16x16x32_bf16 v[100:103], a[84:87], a[72:75], v[100:103]
ds_read_b128 a[108:111], v244 offset:7168
v_add_u32 v248, s53, v248
v_mfma_f32_16x16x32_bf16 v[104:107], a[88:91], a[72:75], v[104:107]
buffer_load_dwordx4 v7, s[4:7], s44 offen offset:2048 sc1 lds
v_mfma_f32_16x16x32_bf16 v[108:111], a[92:95], a[72:75], v[108:111]
s_add_u32 s49, s46, s91
v_add_u32 v244, s53, v244
v_mfma_f32_16x16x32_bf16 v[112:115], a[80:83], a[76:79], v[112:115]
s_add_u32 s46, s46, s53
v_mfma_f32_16x16x32_bf16 v[116:119], a[84:87], a[76:79], v[116:119]
v_mfma_f32_16x16x32_bf16 v[120:123], a[88:91], a[76:79], v[120:123]
buffer_load_dwordx4 v7, s[4:7], s44 offen offset:3072 sc1 lds
v_mfma_f32_16x16x32_bf16 v[124:127], a[92:95], a[76:79], v[124:127]
s_waitcnt vmcnt(28) lgkmcnt(0)
s_barrier
v_mfma_f32_16x16x32_bf16 v[64:67], a[112:115], a[96:99], v[64:67]
ds_read_b64_tr_b16 a[80:81], v245 offset:0
ds_read_b64_tr_b16 a[82:83], v245 offset:4096
v_mfma_f32_16x16x32_bf16 v[68:71], a[116:119], a[96:99], v[68:71]
s_add_u32 m0, s49, 16384
ds_read_b128 a[64:67], v244 offset:0
v_mfma_f32_16x16x32_bf16 v[72:75], a[120:123], a[96:99], v[72:75]
buffer_load_dwordx4 v240, s[8:11], s45 offen offset:0 lds
v_mfma_f32_16x16x32_bf16 v[76:79], a[124:127], a[96:99], v[76:79]
ds_read_b64_tr_b16 a[84:85], v246 offset:0
ds_read_b64_tr_b16 a[86:87], v246 offset:4096
v_mfma_f32_16x16x32_bf16 v[80:83], a[112:115], a[100:103], v[80:83]
s_add_u32 s44, s44, 4096
ds_read_b128 a[68:71], v244 offset:1024
v_mfma_f32_16x16x32_bf16 v[84:87], a[116:119], a[100:103], v[84:87]
ds_read_b64_tr_b16 a[88:89], v247 offset:0
ds_read_b64_tr_b16 a[90:91], v247 offset:4096
v_mfma_f32_16x16x32_bf16 v[88:91], a[120:123], a[100:103], v[88:91]
buffer_load_dwordx4 v241, s[8:11], s45 offen offset:1024 lds
v_mfma_f32_16x16x32_bf16 v[92:95], a[124:127], a[100:103], v[92:95]
ds_read_b128 a[72:75], v244 offset:4096
v_mfma_f32_16x16x32_bf16 v[96:99], a[112:115], a[104:107], v[96:99]
ds_read_b64_tr_b16 a[92:93], v248 offset:0
ds_read_b64_tr_b16 a[94:95], v248 offset:4096
v_mfma_f32_16x16x32_bf16 v[100:103], a[116:119], a[104:107], v[100:103]
ds_read_b128 a[76:79], v244 offset:5120
v_mfma_f32_16x16x32_bf16 v[104:107], a[120:123], a[104:107], v[104:107]
buffer_load_dwordx4 v242, s[8:11], s45 offen offset:2048 lds
v_mfma_f32_16x16x32_bf16 v[108:111], a[124:127], a[104:107], v[108:111]
s_cmp_eq_u32 s46, 0
s_cselect_b32 s53, s56, s57
v_mfma_f32_16x16x32_bf16 v[112:115], a[112:115], a[108:111], v[112:115]
v_mfma_f32_16x16x32_bf16 v[116:119], a[116:119], a[108:111], v[116:119]
v_mfma_f32_16x16x32_bf16 v[120:123], a[120:123], a[108:111], v[120:123]
buffer_load_dwordx4 v243, s[8:11], s45 offen offset:3072 lds
v_mfma_f32_16x16x32_bf16 v[124:127], a[124:127], a[108:111], v[124:127]
s_waitcnt lgkmcnt(0)
s_mov_b32 m0, s49
s_add_u32 s45, s45, 131072
v_mfma_f32_16x16x32_bf16 v[64:67], a[80:83], a[64:67], v[64:67]
ds_read_b64_tr_b16 a[112:113], v245 offset:8192
ds_read_b64_tr_b16 a[114:115], v245 offset:12288
v_mfma_f32_16x16x32_bf16 v[68:71], a[84:87], a[64:67], v[68:71]
ds_read_b128 a[96:99], v244 offset:2048
v_add_u32 v245, s53, v245
v_mfma_f32_16x16x32_bf16 v[72:75], a[88:91], a[64:67], v[72:75]
buffer_load_dwordx4 v7, s[4:7], s44 offen offset:0 sc1 lds
v_mfma_f32_16x16x32_bf16 v[76:79], a[92:95], a[64:67], v[76:79]
ds_read_b64_tr_b16 a[116:117], v246 offset:8192
ds_read_b64_tr_b16 a[118:119], v246 offset:12288
v_mfma_f32_16x16x32_bf16 v[80:83], a[80:83], a[68:71], v[80:83]
ds_read_b128 a[100:103], v244 offset:3072
v_add_u32 v246, s53, v246
v_mfma_f32_16x16x32_bf16 v[84:87], a[84:87], a[68:71], v[84:87]
ds_read_b64_tr_b16 a[120:121], v247 offset:8192
ds_read_b64_tr_b16 a[122:123], v247 offset:12288
v_mfma_f32_16x16x32_bf16 v[88:91], a[88:91], a[68:71], v[88:91]
buffer_load_dwordx4 v7, s[4:7], s44 offen offset:1024 sc1 lds
v_mfma_f32_16x16x32_bf16 v[92:95], a[92:95], a[68:71], v[92:95]
ds_read_b128 a[104:107], v244 offset:6144
v_add_u32 v247, s53, v247
v_mfma_f32_16x16x32_bf16 v[96:99], a[80:83], a[72:75], v[96:99]
ds_read_b64_tr_b16 a[124:125], v248 offset:8192
ds_read_b64_tr_b16 a[126:127], v248 offset:12288
v_mfma_f32_16x16x32_bf16 v[100:103], a[84:87], a[72:75], v[100:103]
ds_read_b128 a[108:111], v244 offset:7168
v_add_u32 v248, s53, v248
v_mfma_f32_16x16x32_bf16 v[104:107], a[88:91], a[72:75], v[104:107]
buffer_load_dwordx4 v7, s[4:7], s44 offen offset:2048 sc1 lds
v_mfma_f32_16x16x32_bf16 v[108:111], a[92:95], a[72:75], v[108:111]
s_add_u32 s49, s46, s91
v_add_u32 v244, s53, v244
v_mfma_f32_16x16x32_bf16 v[112:115], a[80:83], a[76:79], v[112:115]
s_add_u32 s46, s46, s53
v_mfma_f32_16x16x32_bf16 v[116:119], a[84:87], a[76:79], v[116:119]
v_mfma_f32_16x16x32_bf16 v[120:123], a[88:91], a[76:79], v[120:123]
buffer_load_dwordx4 v7, s[4:7], s44 offen offset:3072 sc1 lds
v_mfma_f32_16x16x32_bf16 v[124:127], a[92:95], a[76:79], v[124:127]
s_waitcnt vmcnt(20)
s_waitcnt vmcnt(28) lgkmcnt(0)
s_barrier
v_mfma_f32_16x16x32_bf16 v[64:67], a[112:115], a[96:99], v[64:67]
ds_read_b64_tr_b16 a[80:81], v245 offset:0
ds_read_b64_tr_b16 a[82:83], v245 offset:4096
v_mfma_f32_16x16x32_bf16 v[68:71], a[116:119], a[96:99], v[68:71]
s_add_u32 m0, s49, 16384
ds_read_b128 a[64:67], v244 offset:0
v_mfma_f32_16x16x32_bf16 v[72:75], a[120:123], a[96:99], v[72:75]
buffer_load_dwordx4 v240, s[8:11], s45 offen offset:0 lds
v_mfma_f32_16x16x32_bf16 v[76:79], a[124:127], a[96:99], v[76:79]
ds_read_b64_tr_b16 a[84:85], v246 offset:0
ds_read_b64_tr_b16 a[86:87], v246 offset:4096
v_mfma_f32_16x16x32_bf16 v[80:83], a[112:115], a[100:103], v[80:83]
s_add_u32 s44, s44, 4096
ds_read_b128 a[68:71], v244 offset:1024
v_mfma_f32_16x16x32_bf16 v[84:87], a[116:119], a[100:103], v[84:87]
ds_read_b64_tr_b16 a[88:89], v247 offset:0
ds_read_b64_tr_b16 a[90:91], v247 offset:4096
v_mfma_f32_16x16x32_bf16 v[88:91], a[120:123], a[100:103], v[88:91]
buffer_load_dwordx4 v241, s[8:11], s45 offen offset:1024 lds
v_mfma_f32_16x16x32_bf16 v[92:95], a[124:127], a[100:103], v[92:95]
ds_read_b128 a[72:75], v244 offset:4096
v_mfma_f32_16x16x32_bf16 v[96:99], a[112:115], a[104:107], v[96:99]
ds_read_b64_tr_b16 a[92:93], v248 offset:0
ds_read_b64_tr_b16 a[94:95], v248 offset:4096
v_mfma_f32_16x16x32_bf16 v[100:103], a[116:119], a[104:107], v[100:103]
ds_read_b128 a[76:79], v244 offset:5120
v_mfma_f32_16x16x32_bf16 v[104:107], a[120:123], a[104:107], v[104:107]
s_nop 0
buffer_load_dwordx4 v242, s[8:11], s45 offen offset:2048 lds
v_or_b32 v0, 4, v11
v_mfma_f32_16x16x32_bf16 v[108:111], a[124:127], a[104:107], v[108:111]
s_cmp_eq_u32 s46, 0
s_cselect_b32 s53, s56, s57
v_mfma_f32_16x16x32_bf16 v[112:115], a[112:115], a[108:111], v[112:115]
v_mfma_f32_16x16x32_bf16 v[116:119], a[116:119], a[108:111], v[116:119]
v_mfma_f32_16x16x32_bf16 v[120:123], a[120:123], a[108:111], v[120:123]
buffer_load_dwordx4 v243, s[8:11], s45 offen offset:3072 lds
buffer_store_dword v0, v6, s[60:63], 0 offen sc1
v_mfma_f32_16x16x32_bf16 v[124:127], a[124:127], a[108:111], v[124:127]
s_waitcnt lgkmcnt(0)
s_mov_b32 m0, s49
s_add_u32 s45, s45, 131072
v_mfma_f32_16x16x32_bf16 v[64:67], a[80:83], a[64:67], v[64:67]
ds_read_b64_tr_b16 a[112:113], v245 offset:8192
ds_read_b64_tr_b16 a[114:115], v245 offset:12288
v_mfma_f32_16x16x32_bf16 v[68:71], a[84:87], a[64:67], v[68:71]
ds_read_b128 a[96:99], v244 offset:2048
v_add_u32 v245, s53, v245
v_mfma_f32_16x16x32_bf16 v[72:75], a[88:91], a[64:67], v[72:75]
buffer_load_dwordx4 v7, s[4:7], s44 offen offset:0 sc1 lds
v_mfma_f32_16x16x32_bf16 v[76:79], a[92:95], a[64:67], v[76:79]
ds_read_b64_tr_b16 a[116:117], v246 offset:8192
ds_read_b64_tr_b16 a[118:119], v246 offset:12288
v_mfma_f32_16x16x32_bf16 v[80:83], a[80:83], a[68:71], v[80:83]
ds_read_b128 a[100:103], v244 offset:3072
v_add_u32 v246, s53, v246
v_mfma_f32_16x16x32_bf16 v[84:87], a[84:87], a[68:71], v[84:87]
ds_read_b64_tr_b16 a[120:121], v247 offset:8192
ds_read_b64_tr_b16 a[122:123], v247 offset:12288
v_mfma_f32_16x16x32_bf16 v[88:91], a[88:91], a[68:71], v[88:91]
buffer_load_dwordx4 v7, s[4:7], s44 offen offset:1024 sc1 lds
v_mfma_f32_16x16x32_bf16 v[92:95], a[92:95], a[68:71], v[92:95]
ds_read_b128 a[104:107], v244 offset:6144
v_add_u32 v247, s53, v247
v_mfma_f32_16x16x32_bf16 v[96:99], a[80:83], a[72:75], v[96:99]
buffer_load_dword v35, v4, s[96:99], 0 offen
ds_read_b64_tr_b16 a[124:125], v248 offset:8192
ds_read_b64_tr_b16 a[126:127], v248 offset:12288
v_mfma_f32_16x16x32_bf16 v[100:103], a[84:87], a[72:75], v[100:103]
ds_read_b128 a[108:111], v244 offset:7168
v_add_u32 v248, s53, v248
v_mfma_f32_16x16x32_bf16 v[104:107], a[88:91], a[72:75], v[104:107]
buffer_load_dwordx4 v7, s[4:7], s44 offen offset:2048 sc1 lds
v_mfma_f32_16x16x32_bf16 v[108:111], a[92:95], a[72:75], v[108:111]
s_add_u32 s49, s46, s91
v_add_u32 v244, s53, v244
v_mfma_f32_16x16x32_bf16 v[112:115], a[80:83], a[76:79], v[112:115]
s_add_u32 s46, s46, s53
v_mfma_f32_16x16x32_bf16 v[116:119], a[84:87], a[76:79], v[116:119]
v_mfma_f32_16x16x32_bf16 v[120:123], a[88:91], a[76:79], v[120:123]
buffer_load_dwordx4 v7, s[4:7], s44 offen offset:3072 sc1 lds
v_mfma_f32_16x16x32_bf16 v[124:127], a[92:95], a[76:79], v[124:127]
s_waitcnt vmcnt(26) lgkmcnt(0)
s_barrier
s_mov_b64 exec, 0
s_cmp_eq_u32 s3, 0
s_cbranch_scc1 .Lpad20
s_cmp_eq_u32 s3, 1
s_cbranch_scc1 .L5odd_pad
s_cmp_eq_u32 s3, 2
s_cbranch_scc1 .Lpad21
s_branch .Lpad22
.Ltch9:
s_mov_b64 exec, -1
s_nop 0
s_nop 0
s_nop 0
s_nop 0
s_nop 0
s_nop 0
.L5b:
v_mfma_f32_16x16x32_bf16 v[64:67], a[112:115], a[96:99], v[64:67]
ds_read_b64_tr_b16 a[80:81], v245 offset:0
ds_read_b64_tr_b16 a[82:83], v245 offset:4096
v_mfma_f32_16x16x32_bf16 v[68:71], a[116:119], a[96:99], v[68:71]
s_add_u32 m0, s49, 16384
ds_read_b128 a[64:67], v244 offset:0
v_mfma_f32_16x16x32_bf16 v[72:75], a[120:123], a[96:99], v[72:75]
buffer_load_dwordx4 v240, s[8:11], s45 offen offset:0 lds
v_mfma_f32_16x16x32_bf16 v[76:79], a[124:127], a[96:99], v[76:79]
ds_read_b64_tr_b16 a[84:85], v246 offset:0
ds_read_b64_tr_b16 a[86:87], v246 offset:4096
v_mfma_f32_16x16x32_bf16 v[80:83], a[112:115], a[100:103], v[80:83]
s_add_u32 s44, s44, 4096
ds_read_b128 a[68:71], v244 offset:1024
v_mfma_f32_16x16x32_bf16 v[84:87], a[116:119], a[100:103], v[84:87]
ds_read_b64_tr_b16 a[88:89], v247 offset:0
ds_read_b64_tr_b16 a[90:91], v247 offset:4096
v_mfma_f32_16x16x32_bf16 v[88:91], a[120:123], a[100:103], v[88:91]
buffer_load_dwordx4 v241, s[8:11], s45 offen offset:1024 lds
v_mfma_f32_16x16x32_bf16 v[92:95], a[124:127], a[100:103], v[92:95]
ds_read_b128 a[72:75], v244 offset:4096
v_mfma_f32_16x16x32_bf16 v[96:99], a[112:115], a[104:107], v[96:99]
ds_read_b64_tr_b16 a[92:93], v248 offset:0
ds_read_b64_tr_b16 a[94:95], v248 offset:4096
v_mfma_f32_16x16x32_bf16 v[100:103], a[116:119], a[104:107], v[100:103]
ds_read_b128 a[76:79], v244 offset:5120
v_mfma_f32_16x16x32_bf16 v[104:107], a[120:123], a[104:107], v[104:107]
buffer_load_dwordx4 v242, s[8:11], s45 offen offset:2048 lds
v_mfma_f32_16x16x32_bf16 v[108:111], a[124:127], a[104:107], v[108:111]
s_cmp_eq_u32 s46, 0
s_cselect_b32 s53, s56, s57
v_mfma_f32_16x16x32_bf16 v[112:115], a[112:115], a[108:111], v[112:115]
v_mfma_f32_16x16x32_bf16 v[116:119], a[116:119], a[108:111], v[116:119]
v_mfma_f32_16x16x32_bf16 v[120:123], a[120:123], a[108:111], v[120:123]
buffer_load_dwordx4 v243, s[8:11], s45 offen offset:3072 lds
v_mfma_f32_16x16x32_bf16 v[124:127], a[124:127], a[108:111], v[124:127]
s_waitcnt lgkmcnt(0)
s_mov_b32 m0, s49
s_add_u32 s45, s45, 131072
v_mfma_f32_16x16x32_bf16 v[64:67], a[80:83], a[64:67], v[64:67]
ds_read_b64_tr_b16 a[112:113], v245 offset:8192
ds_read_b64_tr_b16 a[114:115], v245 offset:12288
v_mfma_f32_16x16x32_bf16 v[68:71], a[84:87], a[64:67], v[68:71]
ds_read_b128 a[96:99], v244 offset:2048
v_add_u32 v245, s53, v245
v_mfma_f32_16x16x32_bf16 v[72:75], a[88:91], a[64:67], v[72:75]
buffer_load_dwordx4 v7, s[4:7], s44 offen offset:0 sc1 lds
v_mfma_f32_16x16x32_bf16 v[76:79], a[92:95], a[64:67], v[76:79]
ds_read_b64_tr_b16 a[116:117], v246 offset:8192
ds_read_b64_tr_b16 a[118:119], v246 offset:12288
v_mfma_f32_16x16x32_bf16 v[80:83], a[80:83], a[68:71], v[80:83]
ds_read_b128 a[100:103], v244 offset:3072
v_add_u32 v246, s53, v246
v_mfma_f32_16x16x32_bf16 v[84:87], a[84:87], a[68:71], v[84:87]
ds_read_b64_tr_b16 a[120:121], v247 offset:8192
ds_read_b64_tr_b16 a[122:123], v247 offset:12288
v_mfma_f32_16x16x32_bf16 v[88:91], a[88:91], a[68:71], v[88:91]
buffer_load_dwordx4 v7, s[4:7], s44 offen offset:1024 sc1 lds
v_mfma_f32_16x16x32_bf16 v[92:95], a[92:95], a[68:71], v[92:95]
ds_read_b128 a[104:107], v244 offset:6144
v_add_u32 v247, s53, v247
v_mfma_f32_16x16x32_bf16 v[96:99], a[80:83], a[72:75], v[96:99]
ds_read_b64_tr_b16 a[124:125], v248 offset:8192
ds_read_b64_tr_b16 a[126:127], v248 offset:12288
v_mfma_f32_16x16x32_bf16 v[100:103], a[84:87], a[72:75], v[100:103]
ds_read_b128 a[108:111], v244 offset:7168
v_add_u32 v248, s53, v248
v_mfma_f32_16x16x32_bf16 v[104:107], a[88:91], a[72:75], v[104:107]
buffer_load_dwordx4 v7, s[4:7], s44 offen offset:2048 sc1 lds
v_mfma_f32_16x16x32_bf16 v[108:111], a[92:95], a[72:75], v[108:111]
s_add_u32 s49, s46, s91
v_add_u32 v244, s53, v244
v_mfma_f32_16x16x32_bf16 v[112:115], a[80:83], a[76:79], v[112:115]
s_add_u32 s46, s46, s53
v_mfma_f32_16x16x32_bf16 v[116:119], a[84:87], a[76:79], v[116:119]
s_sub_u32 s50, s50, 1
v_mfma_f32_16x16x32_bf16 v[120:123], a[88:91], a[76:79], v[120:123]
buffer_load_dwordx4 v7, s[4:7], s44 offen offset:3072 sc1 lds
v_mfma_f32_16x16x32_bf16 v[124:127], a[92:95], a[76:79], v[124:127]
s_waitcnt vmcnt(24) lgkmcnt(0)
s_barrier
s_cbranch_scc0 .L5b
s_cmp_eq_u32 s55, 0
s_cbranch_scc0 .L5odd
v_mfma_f32_16x16x32_bf16 v[64:67], a[112:115], a[96:99], v[64:67]
ds_read_b64_tr_b16 a[80:81], v245 offset:0
ds_read_b64_tr_b16 a[82:83], v245 offset:4096
v_mfma_f32_16x16x32_bf16 v[68:71], a[116:119], a[96:99], v[68:71]
buffer_load_dword v250, v249, s[60:63], s90 offen sc1
ds_read_b128 a[64:67], v244 offset:0
v_mfma_f32_16x16x32_bf16 v[72:75], a[120:123], a[96:99], v[72:75]
v_and_b32 v168, 15, v1
v_lshrrev_b32 v169, 4, v1
v_mfma_f32_16x16x32_bf16 v[76:79], a[124:127], a[96:99], v[76:79]
ds_read_b64_tr_b16 a[84:85], v246 offset:0
ds_read_b64_tr_b16 a[86:87], v246 offset:4096
v_mfma_f32_16x16x32_bf16 v[80:83], a[112:115], a[100:103], v[80:83]
s_mov_b32 s32, 4096
ds_read_b128 a[68:71], v244 offset:1024
v_lshlrev_b32 v169, 2, v169
v_mfma_f32_16x16x32_bf16 v[84:87], a[116:119], a[100:103], v[84:87]
ds_read_b64_tr_b16 a[88:89], v247 offset:0
ds_read_b64_tr_b16 a[90:91], v247 offset:4096
v_mfma_f32_16x16x32_bf16 v[88:91], a[120:123], a[100:103], v[88:91]
v_sub_u32 v168, v168, v169
v_mov_b32 v169, 0x3f80
v_mfma_f32_16x16x32_bf16 v[92:95], a[124:127], a[100:103], v[92:95]
ds_read_b128 a[72:75], v244 offset:4096
v_mov_b32 v170, 0x3f800000
v_mfma_f32_16x16x32_bf16 v[96:99], a[112:115], a[104:107], v[96:99]
ds_read_b64_tr_b16 a[92:93], v248 offset:0
ds_read_b64_tr_b16 a[94:95], v248 offset:4096
v_mfma_f32_16x16x32_bf16 v[100:103], a[116:119], a[104:107], v[100:103]
ds_read_b128 a[76:79], v244 offset:5120
v_cmp_eq_u32 s[24:25], 0, v168
v_mfma_f32_16x16x32_bf16 v[104:107], a[120:123], a[104:107], v[104:107]
v_cmp_eq_u32 s[26:27], 1, v168
v_cmp_eq_u32 s[28:29], 2, v168
v_mfma_f32_16x16x32_bf16 v[108:111], a[124:127], a[104:107], v[108:111]
s_cmp_eq_u32 s46, 0
s_cselect_b32 s53, s56, s57
v_cmp_eq_u32 s[30:31], 3, v168
v_mfma_f32_16x16x32_bf16 v[112:115], a[112:115], a[108:111], v[112:115]
v_cndmask_b32 v160, 0, v169, s[24:25]
v_cndmask_b32 v161, 0, v169, s[28:29]
v_mfma_f32_16x16x32_bf16 v[116:119], a[116:119], a[108:111], v[116:119]
v_cndmask_b32 v160, v160, v170, s[26:27]
v_cndmask_b32 v161, v161, v170, s[30:31]
v_mfma_f32_16x16x32_bf16 v[120:123], a[120:123], a[108:111], v[120:123]
v_mov_b32 v162, 0
v_mov_b32 v163, 0
v_mfma_f32_16x16x32_bf16 v[124:127], a[124:127], a[108:111], v[124:127]
s_waitcnt lgkmcnt(0)
v_mov_b32 v164, 0
v_mov_b32 v165, 0
v_mfma_f32_16x16x32_bf16 v[64:67], a[80:83], a[64:67], v[64:67]
ds_read_b64_tr_b16 a[112:113], v245 offset:8192
ds_read_b64_tr_b16 a[114:115], v245 offset:12288
v_mfma_f32_16x16x32_bf16 v[68:71], a[84:87], a[64:67], v[68:71]
ds_read_b128 a[96:99], v244 offset:2048
v_add_u32 v245, s53, v245
v_mfma_f32_16x16x32_bf16 v[72:75], a[88:91], a[64:67], v[72:75]
v_mov_b32 v166, v160
v_mov_b32 v167, v161
v_mfma_f32_16x16x32_bf16 v[76:79], a[92:95], a[64:67], v[76:79]
ds_read_b64_tr_b16 a[116:117], v246 offset:8192
ds_read_b64_tr_b16 a[118:119], v246 offset:12288
v_mfma_f32_16x16x32_bf16 v[80:83], a[80:83], a[68:71], v[80:83]
ds_read_b128 a[100:103], v244 offset:3072
v_add_u32 v246, s53, v246
v_mfma_f32_16x16x32_bf16 v[84:87], a[84:87], a[68:71], v[84:87]
ds_read_b64_tr_b16 a[120:121], v247 offset:8192
ds_read_b64_tr_b16 a[122:123], v247 offset:12288
v_mfma_f32_16x16x32_bf16 v[88:91], a[88:91], a[68:71], v[88:91]
v_mfma_f32_16x16x32_bf16 v[92:95], a[92:95], a[68:71], v[92:95]
ds_read_b128 a[104:107], v244 offset:6144
v_add_u32 v247, s53, v247
v_mfma_f32_16x16x32_bf16 v[96:99], a[80:83], a[72:75], v[96:99]
ds_read_b64_tr_b16 a[124:125], v248 offset:8192
ds_read_b64_tr_b16 a[126:127], v248 offset:12288
v_mfma_f32_16x16x32_bf16 v[100:103], a[84:87], a[72:75], v[100:103]
ds_read_b128 a[108:111], v244 offset:7168
v_add_u32 v248, s53, v248
v_mfma_f32_16x16x32_bf16 v[104:107], a[88:91], a[72:75], v[104:107]
v_mfma_f32_16x16x32_bf16 v[108:111], a[92:95], a[72:75], v[108:111]
s_add_u32 s49, s46, s91
v_add_u32 v244, s53, v244
v_mfma_f32_16x16x32_bf16 v[112:115], a[80:83], a[76:79], v[112:115]
s_add_u32 s46, s46, s53
v_mfma_f32_16x16x32_bf16 v[116:119], a[84:87], a[76:79], v[116:119]
v_mfma_f32_16x16x32_bf16 v[120:123], a[88:91], a[76:79], v[120:123]
v_mfma_f32_16x16x32_bf16 v[124:127], a[92:95], a[76:79], v[124:127]
s_waitcnt vmcnt(17) lgkmcnt(0)
s_barrier
v_mfma_f32_16x16x32_bf16 v[64:67], a[112:115], a[96:99], v[64:67]
ds_read_b64_tr_b16 a[80:81], v245 offset:0
ds_read_b64_tr_b16 a[82:83], v245 offset:4096
v_mfma_f32_16x16x32_bf16 v[68:71], a[116:119], a[96:99], v[68:71]
ds_read_b128 a[64:67], v244 offset:0
v_mfma_f32_16x16x32_bf16 v[72:75], a[120:123], a[96:99], v[72:75]
v_mfma_f32_16x16x32_bf16 v[76:79], a[124:127], a[96:99], v[76:79]
ds_read_b64_tr_b16 a[84:85], v246 offset:0
ds_read_b64_tr_b16 a[86:87], v246 offset:4096
v_mfma_f32_16x16x32_bf16 v[80:83], a[112:115], a[100:103], v[80:83]
ds_read_b128 a[68:71], v244 offset:1024
v_mfma_f32_16x16x32_bf16 v[84:87], a[116:119], a[100:103], v[84:87]
ds_read_b64_tr_b16 a[88:89], v247 offset:0
ds_read_b64_tr_b16 a[90:91], v247 offset:4096
.Lpad20:
s_cbranch_execz .Ltch9
v_mfma_f32_16x16x32_bf16 v[88:91], a[120:123], a[100:103], v[88:91]
v_mfma_f32_16x16x32_bf16 v[92:95], a[124:127], a[100:103], v[92:95]
ds_read_b128 a[72:75], v244 offset:4096
v_mfma_f32_16x16x32_bf16 v[96:99], a[112:115], a[104:107], v[96:99]
ds_read_b64_tr_b16 a[92:93], v248 offset:0
ds_read_b64_tr_b16 a[94:95], v248 offset:4096
v_mfma_f32_16x16x32_bf16 v[100:103], a[116:119], a[104:107], v[100:103]
ds_read_b128 a[76:79], v244 offset:5120
v_mfma_f32_16x16x32_bf16 v[104:107], a[120:123], a[104:107], v[104:107]
s_waitcnt vmcnt(0)
v_and_b32 v250, 7, v250
v_mfma_f32_16x16x32_bf16 v[108:111], a[124:127], a[104:107], v[108:111]
s_cmp_eq_u32 s46, 0
s_cselect_b32 s53, s56, s57
v_cmp_gt_u32 vcc, 5, v250
v_mfma_f32_16x16x32_bf16 v[112:115], a[112:115], a[108:111], v[112:115]
v_mfma_f32_16x16x32_bf16 v[116:119], a[116:119], a[108:111], v[116:119]
s_cmp_lg_u64 vcc, 0
s_cbranch_scc1 .Lspin5
.Lspin5_ret:
v_mfma_f32_16x16x32_bf16 v[120:123], a[120:123], a[108:111], v[120:123]
v_mfma_f32_16x16x32_bf16 v[124:127], a[124:127], a[108:111], v[124:127]
s_waitcnt lgkmcnt(0)
v_mfma_f32_16x16x32_bf16 v[64:67], a[80:83], a[64:67], v[64:67]
ds_read_b64_tr_b16 a[112:113], v245 offset:8192
ds_read_b64_tr_b16 a[114:115], v245 offset:12288
v_mfma_f32_16x16x32_bf16 v[68:71], a[84:87], a[64:67], v[68:71]
buffer_load_dwordx4 v[128:131], v7, s[12:15], 0 offen offset:0 sc1
ds_read_b128 a[96:99], v244 offset:2048
v_add_u32 v245, s53, v245
v_mfma_f32_16x16x32_bf16 v[72:75], a[88:91], a[64:67], v[72:75]
v_mfma_f32_16x16x32_bf16 v[76:79], a[92:95], a[64:67], v[76:79]
ds_read_b64_tr_b16 a[116:117], v246 offset:8192
ds_read_b64_tr_b16 a[118:119], v246 offset:12288
v_mfma_f32_16x16x32_bf16 v[80:83], a[80:83], a[68:71], v[80:83]
buffer_load_dwordx4 v[132:135], v7, s[12:15], 0 offen offset:1024 sc1
ds_read_b128 a[100:103], v244 offset:3072
v_add_u32 v246, s53, v246
v_mfma_f32_16x16x32_bf16 v[84:87], a[84:87], a[68:71], v[84:87]
ds_read_b64_tr_b16 a[120:121], v247 offset:8192
ds_read_b64_tr_b16 a[122:123], v247 offset:12288
v_mfma_f32_16x16x32_bf16 v[88:91], a[88:91], a[68:71], v[88:91]
v_mfma_f32_16x16x32_bf16 v[92:95], a[92:95], a[68:71], v[92:95]
buffer_load_dwordx4 v[136:139], v7, s[12:15], 0 offen offset:2048 sc1
ds_read_b128 a[104:107], v244 offset:6144
v_add_u32 v247, s53, v247
v_mfma_f32_16x16x32_bf16 v[96:99], a[80:83], a[72:75], v[96:99]
ds_read_b64_tr_b16 a[124:125], v248 offset:8192
ds_read_b64_tr_b16 a[126:127], v248 offset:12288
v_mfma_f32_16x16x32_bf16 v[100:103], a[84:87], a[72:75], v[100:103]
ds_read_b128 a[108:111], v244 offset:7168
v_add_u32 v248, s53, v248
v_mfma_f32_16x16x32_bf16 v[104:107], a[88:91], a[72:75], v[104:107]
buffer_load_dwordx4 v[140:143], v7, s[12:15], 0 offen offset:3072 sc1
v_mfma_f32_16x16x32_bf16 v[108:111], a[92:95], a[72:75], v[108:111]
s_add_u32 s49, s46, s91
v_add_u32 v244, s53, v244
v_mfma_f32_16x16x32_bf16 v[112:115], a[80:83], a[76:79], v[112:115]
s_add_u32 s46, s46, s53
v_mfma_f32_16x16x32_bf16 v[116:119], a[84:87], a[76:79], v[116:119]
v_mfma_f32_16x16x32_bf16 v[120:123], a[88:91], a[76:79], v[120:123]
v_mfma_f32_16x16x32_bf16 v[124:127], a[92:95], a[76:79], v[124:127]
s_waitcnt vmcnt(13) lgkmcnt(0)
s_barrier
v_mfma_f32_16x16x32_bf16 v[64:67], a[112:115], a[96:99], v[64:67]
ds_read_b64_tr_b16 a[80:81], v245 offset:0
ds_read_b64_tr_b16 a[82:83], v245 offset:4096
v_mfma_f32_16x16x32_bf16 v[68:71], a[116:119], a[96:99], v[68:71]
buffer_load_dwordx4 v[144:147], v7, s[12:15], s32 offen offset:0 sc1
ds_read_b128 a[64:67], v244 offset:0
v_mfma_f32_16x16x32_bf16 v[72:75], a[120:123], a[96:99], v[72:75]
v_mfma_f32_16x16x32_bf16 v[76:79], a[124:127], a[96:99], v[76:79]
ds_read_b64_tr_b16 a[84:85], v246 offset:0
ds_read_b64_tr_b16 a[86:87], v246 offset:4096
v_mfma_f32_16x16x32_bf16 v[80:83], a[112:115], a[100:103], v[80:83]
buffer_load_dwordx4 v[148:151], v7, s[12:15], s32 offen offset:1024 sc1
ds_read_b128 a[68:71], v244 offset:1024
v_mfma_f32_16x16x32_bf16 v[84:87], a[116:119], a[100:103], v[84:87]
ds_read_b64_tr_b16 a[88:89], v247 offset:0
ds_read_b64_tr_b16 a[90:91], v247 offset:4096
v_mfma_f32_16x16x32_bf16 v[88:91], a[120:123], a[100:103], v[88:91]
v_mfma_f32_16x16x32_bf16 v[92:95], a[124:127], a[100:103], v[92:95]
ds_read_b128 a[72:75], v244 offset:4096
v_mfma_f32_16x16x32_bf16 v[96:99], a[112:115], a[104:107], v[96:99]
ds_read_b64_tr_b16 a[92:93], v248 offset:0
ds_read_b64_tr_b16 a[94:95], v248 offset:4096
v_mfma_f32_16x16x32_bf16 v[100:103], a[116:119], a[104:107], v[100:103]
buffer_load_dwordx4 v[152:155], v7, s[12:15], s32 offen offset:2048 sc1
ds_read_b128 a[76:79], v244 offset:5120
v_mfma_f32_16x16x32_bf16 v[104:107], a[120:123], a[104:107], v[104:107]
v_mfma_f32_16x16x32_bf16 v[108:111], a[124:127], a[104:107], v[108:111]
s_cmp_eq_u32 s46, 0
s_cselect_b32 s53, s56, s57
v_mfma_f32_16x16x32_bf16 v[112:115], a[112:115], a[108:111], v[112:115]
buffer_load_dwordx4 v[156:159], v7, s[12:15], s32 offen offset:3072 sc1
v_mfma_f32_16x16x32_bf16 v[116:119], a[116:119], a[108:111], v[116:119]
v_mfma_f32_16x16x32_bf16 v[120:123], a[120:123], a[108:111], v[120:123]
v_mfma_f32_16x16x32_bf16 v[124:127], a[124:127], a[108:111], v[124:127]
s_waitcnt lgkmcnt(0)
v_mfma_f32_16x16x32_bf16 v[64:67], a[80:83], a[64:67], v[64:67]
ds_read_b64_tr_b16 a[112:113], v245 offset:8192
ds_read_b64_tr_b16 a[114:115], v245 offset:12288
v_mfma_f32_16x16x32_bf16 v[68:71], a[84:87], a[64:67], v[68:71]
ds_read_b128 a[96:99], v244 offset:2048
v_add_u32 v245, s53, v245
v_mfma_f32_16x16x32_bf16 v[72:75], a[88:91], a[64:67], v[72:75]
v_mfma_f32_16x16x32_bf16 v[76:79], a[92:95], a[64:67], v[76:79]
ds_read_b64_tr_b16 a[116:117], v246 offset:8192
ds_read_b64_tr_b16 a[118:119], v246 offset:12288
v_mfma_f32_16x16x32_bf16 v[80:83], a[80:83], a[68:71], v[80:83]
ds_read_b128 a[100:103], v244 offset:3072
v_add_u32 v246, s53, v246
v_mfma_f32_16x16x32_bf16 v[84:87], a[84:87], a[68:71], v[84:87]
ds_read_b64_tr_b16 a[120:121], v247 offset:8192
ds_read_b64_tr_b16 a[122:123], v247 offset:12288
v_mfma_f32_16x16x32_bf16 v[88:91], a[88:91], a[68:71], v[88:91]
v_mfma_f32_16x16x32_bf16 v[92:95], a[92:95], a[68:71], v[92:95]
ds_read_b128 a[104:107], v244 offset:6144
v_add_u32 v247, s53, v247
v_mfma_f32_16x16x32_bf16 v[96:99], a[80:83], a[72:75], v[96:99]
ds_read_b64_tr_b16 a[124:125], v248 offset:8192
ds_read_b64_tr_b16 a[126:127], v248 offset:12288
v_mfma_f32_16x16x32_bf16 v[100:103], a[84:87], a[72:75], v[100:103]
ds_read_b128 a[108:111], v244 offset:7168
v_add_u32 v248, s53, v248
v_mfma_f32_16x16x32_bf16 v[104:107], a[88:91], a[72:75], v[104:107]
v_mfma_f32_16x16x32_bf16 v[108:111], a[92:95], a[72:75], v[108:111]
s_add_u32 s49, s46, s91
v_add_u32 v244, s53, v244
v_mfma_f32_16x16x32_bf16 v[112:115], a[80:83], a[76:79], v[112:115]
s_add_u32 s46, s46, s53
v_mfma_f32_16x16x32_bf16 v[116:119], a[84:87], a[76:79], v[116:119]
v_mfma_f32_16x16x32_bf16 v[120:123], a[88:91], a[76:79], v[120:123]
v_mfma_f32_16x16x32_bf16 v[124:127], a[92:95], a[76:79], v[124:127]
s_waitcnt vmcnt(9) lgkmcnt(0)
s_barrier
v_mfma_f32_16x16x32_bf16 v[64:67], a[112:115], a[96:99], v[64:67]
ds_read_b64_tr_b16 a[80:81], v245 offset:0
ds_read_b64_tr_b16 a[82:83], v245 offset:4096
v_mfma_f32_16x16x32_bf16 v[68:71], a[116:119], a[96:99], v[68:71]
ds_read_b128 a[64:67], v244 offset:0
ds_read_b128 a[68:71], v244 offset:1024
v_mfma_f32_16x16x32_bf16 v[72:75], a[120:123], a[96:99], v[72:75]
ds_read_b64_tr_b16 a[84:85], v246 offset:0
ds_read_b64_tr_b16 a[86:87], v246 offset:4096
v_mfma_f32_16x16x32_bf16 v[76:79], a[124:127], a[96:99], v[76:79]
ds_read_b64_tr_b16 a[88:89], v247 offset:0
ds_read_b64_tr_b16 a[90:91], v247 offset:4096
v_mfma_f32_16x16x32_bf16 v[80:83], a[112:115], a[100:103], v[80:83]
ds_read_b64_tr_b16 a[92:93], v248 offset:0
ds_read_b64_tr_b16 a[94:95], v248 offset:4096
v_mfma_f32_16x16x32_bf16 v[84:87], a[116:119], a[100:103], v[84:87]
ds_read_b64_tr_b16 a[144:145], v245 offset:8192
ds_read_b64_tr_b16 a[146:147], v245 offset:12288
v_mfma_f32_16x16x32_bf16 v[88:91], a[120:123], a[100:103], v[88:91]
ds_read_b128 a[128:131], v244 offset:2048
ds_read_b128 a[132:135], v244 offset:3072
v_mfma_f32_16x16x32_bf16 v[92:95], a[124:127], a[100:103], v[92:95]
ds_read_b64_tr_b16 a[148:149], v246 offset:8192
ds_read_b64_tr_b16 a[150:151], v246 offset:12288
v_mfma_f32_16x16x32_bf16 v[96:99], a[112:115], a[104:107], v[96:99]
ds_read_b64_tr_b16 a[152:153], v247 offset:8192
ds_read_b64_tr_b16 a[154:155], v247 offset:12288
v_mfma_f32_16x16x32_bf16 v[100:103], a[116:119], a[104:107], v[100:103]
ds_read_b64_tr_b16 a[156:157], v248 offset:8192
ds_read_b64_tr_b16 a[158:159], v248 offset:12288
v_mfma_f32_16x16x32_bf16 v[104:107], a[120:123], a[104:107], v[104:107]
ds_read_b128 a[72:75], v244 offset:4096
ds_read_b128 a[76:79], v244 offset:5120
v_mfma_f32_16x16x32_bf16 v[108:111], a[124:127], a[104:107], v[108:111]
ds_read_b128 a[136:139], v244 offset:6144
ds_read_b128 a[140:143], v244 offset:7168
v_mfma_f32_16x16x32_bf16 v[112:115], a[112:115], a[108:111], v[112:115]
s_mov_b32 s24, 0
s_mov_b32 s25, 32768
v_mfma_f32_16x16x32_bf16 v[116:119], a[116:119], a[108:111], v[116:119]
s_mov_b32 s26, 65536
s_mov_b32 s27, 98304
v_mfma_f32_16x16x32_bf16 v[120:123], a[120:123], a[108:111], v[120:123]
s_mov_b32 s28, 131072
s_mov_b32 s29, 163840
v_mfma_f32_16x16x32_bf16 v[124:127], a[124:127], a[108:111], v[124:127]
s_mov_b32 s30, 196608
s_mov_b32 s31, 229376
s_waitcnt vmcnt(0) lgkmcnt(0)
v_mfma_f32_16x16x32_bf16 v[64:67], v[160:163], v[128:131], v[64:67]
v_mfma_f32_16x16x32_bf16 v[68:71], v[164:167], v[128:131], v[68:71]
v_mfma_f32_16x16x32_bf16 v[72:75], v[160:163], v[132:135], v[72:75]
v_mfma_f32_16x16x32_bf16 v[76:79], v[164:167], v[132:135], v[76:79]
v_mfma_f32_16x16x32_bf16 v[64:67], a[80:83], a[64:67], v[64:67]
v_mfma_f32_16x16x32_bf16 v[68:71], a[84:87], a[64:67], v[68:71]
v_mfma_f32_16x16x32_bf16 v[72:75], a[88:91], a[64:67], v[72:75]
v_mfma_f32_16x16x32_bf16 v[76:79], a[92:95], a[64:67], v[76:79]
v_mfma_f32_16x16x32_bf16 v[64:67], a[144:147], a[128:131], v[64:67]
v_mfma_f32_16x16x32_bf16 v[68:71], a[148:151], a[128:131], v[68:71]
v_mfma_f32_16x16x32_bf16 v[72:75], a[152:155], a[128:131], v[72:75]
v_mfma_f32_16x16x32_bf16 v[76:79], a[156:159], a[128:131], v[76:79]
v_mfma_f32_16x16x32_bf16 v[80:83], v[160:163], v[136:139], v[80:83]
v_mfma_f32_16x16x32_bf16 v[84:87], v[164:167], v[136:139], v[84:87]
v_mfma_f32_16x16x32_bf16 v[88:91], v[160:163], v[140:143], v[88:91]
s_nop 1
v_cvt_pk_bf16_f32 v192, v64, v65
v_cvt_pk_bf16_f32 v194, v68, v69
v_cvt_pk_bf16_f32 v193, v66, v67
v_mfma_f32_16x16x32_bf16 v[92:95], v[164:167], v[140:143], v[92:95]
v_cvt_pk_bf16_f32 v195, v70, v71
v_cvt_pk_bf16_f32 v196, v72, v73
v_cvt_pk_bf16_f32 v198, v76, v77
v_mfma_f32_16x16x32_bf16 v[80:83], a[80:83], a[68:71], v[80:83]
v_cvt_pk_bf16_f32 v197, v74, v75
v_cvt_pk_bf16_f32 v199, v78, v79
v_mfma_f32_16x16x32_bf16 v[84:87], a[84:87], a[68:71], v[84:87]
v_permlane16_swap_b32 v192, v194
v_mfma_f32_16x16x32_bf16 v[88:91], a[88:91], a[68:71], v[88:91]
v_permlane16_swap_b32 v193, v195
v_mfma_f32_16x16x32_bf16 v[92:95], a[92:95], a[68:71], v[92:95]
v_permlane16_swap_b32 v196, v198
v_mfma_f32_16x16x32_bf16 v[80:83], a[144:147], a[132:135], v[80:83]
v_permlane16_swap_b32 v197, v199
v_mov_b32 v200, v192
v_mfma_f32_16x16x32_bf16 v[84:87], a[148:151], a[132:135], v[84:87]
v_mov_b32 v201, v193
v_mov_b32 v202, v194
v_mov_b32 v203, v195
v_mfma_f32_16x16x32_bf16 v[88:91], a[152:155], a[132:135], v[88:91]
v_mov_b32_dpp v192, v196 row_ror:8 row_mask:0xf bank_mask:0xc
v_mov_b32_dpp v193, v197 row_ror:8 row_mask:0xf bank_mask:0xc
v_mov_b32_dpp v194, v198 row_ror:8 row_mask:0xf bank_mask:0xc
v_mfma_f32_16x16x32_bf16 v[92:95], a[156:159], a[132:135], v[92:95]
v_mov_b32_dpp v195, v199 row_ror:8 row_mask:0xf bank_mask:0xc
v_mfma_f32_16x16x32_bf16 v[96:99], v[160:163], v[144:147], v[96:99]
buffer_store_dwordx4 v[192:195], v251, s[16:19], s24 offen nt
v_mov_b32_dpp v196, v200 row_ror:8 row_mask:0xf bank_mask:0x3
v_mov_b32_dpp v197, v201 row_ror:8 row_mask:0xf bank_mask:0x3
v_mfma_f32_16x16x32_bf16 v[100:103], v[164:167], v[144:147], v[100:103]
v_mov_b32_dpp v198, v202 row_ror:8 row_mask:0xf bank_mask:0x3
v_mov_b32_dpp v199, v203 row_ror:8 row_mask:0xf bank_mask:0x3
v_mfma_f32_16x16x32_bf16 v[104:107], v[160:163], v[148:151], v[104:107]
buffer_store_dwordx4 v[196:199], v251, s[16:19], s25 offen nt
v_cvt_pk_bf16_f32 v204, v80, v81
v_cvt_pk_bf16_f32 v206, v84, v85
v_mfma_f32_16x16x32_bf16 v[108:111], v[164:167], v[148:151], v[108:111]
v_cvt_pk_bf16_f32 v205, v82, v83
v_cvt_pk_bf16_f32 v207, v86, v87
v_cvt_pk_bf16_f32 v208, v88, v89
v_mfma_f32_16x16x32_bf16 v[96:99], a[80:83], a[72:75], v[96:99]
v_cvt_pk_bf16_f32 v210, v92, v93
v_cvt_pk_bf16_f32 v209, v90, v91
v_cvt_pk_bf16_f32 v211, v94, v95
v_mfma_f32_16x16x32_bf16 v[100:103], a[84:87], a[72:75], v[100:103]
v_permlane16_swap_b32 v204, v206
v_mfma_f32_16x16x32_bf16 v[104:107], a[88:91], a[72:75], v[104:107]
v_permlane16_swap_b32 v205, v207
v_mfma_f32_16x16x32_bf16 v[108:111], a[92:95], a[72:75], v[108:111]
v_permlane16_swap_b32 v208, v210
v_mfma_f32_16x16x32_bf16 v[96:99], a[144:147], a[136:139], v[96:99]
v_permlane16_swap_b32 v209, v211
v_mov_b32 v212, v204
v_mfma_f32_16x16x32_bf16 v[100:103], a[148:151], a[136:139], v[100:103]
v_mov_b32 v213, v205
v_mov_b32 v214, v206
v_mov_b32 v215, v207
v_mfma_f32_16x16x32_bf16 v[104:107], a[152:155], a[136:139], v[104:107]
v_mov_b32_dpp v204, v208 row_ror:8 row_mask:0xf bank_mask:0xc
v_mov_b32_dpp v205, v209 row_ror:8 row_mask:0xf bank_mask:0xc
v_mov_b32_dpp v206, v210 row_ror:8 row_mask:0xf bank_mask:0xc
v_mfma_f32_16x16x32_bf16 v[108:111], a[156:159], a[136:139], v[108:111]
v_mov_b32_dpp v207, v211 row_ror:8 row_mask:0xf bank_mask:0xc
v_mfma_f32_16x16x32_bf16 v[112:115], v[160:163], v[152:155], v[112:115]
buffer_store_dwordx4 v[204:207], v251, s[16:19], s26 offen nt
v_mov_b32_dpp v208, v212 row_ror:8 row_mask:0xf bank_mask:0x3
v_mov_b32_dpp v209, v213 row_ror:8 row_mask:0xf bank_mask:0x3
v_mfma_f32_16x16x32_bf16 v[116:119], v[164:167], v[152:155], v[116:119]
v_mov_b32_dpp v210, v214 row_ror:8 row_mask:0xf bank_mask:0x3
v_mov_b32_dpp v211, v215 row_ror:8 row_mask:0xf bank_mask:0x3
v_mfma_f32_16x16x32_bf16 v[120:123], v[160:163], v[156:159], v[120:123]
buffer_store_dwordx4 v[208:211], v251, s[16:19], s27 offen nt
v_cvt_pk_bf16_f32 v192, v96, v97
v_cvt_pk_bf16_f32 v194, v100, v101
v_mfma_f32_16x16x32_bf16 v[124:127], v[164:167], v[156:159], v[124:127]
v_cvt_pk_bf16_f32 v193, v98, v99
v_cvt_pk_bf16_f32 v195, v102, v103
v_cvt_pk_bf16_f32 v196, v104, v105
v_mfma_f32_16x16x32_bf16 v[112:115], a[80:83], a[76:79], v[112:115]
v_cvt_pk_bf16_f32 v198, v108, v109
v_cvt_pk_bf16_f32 v197, v106, v107
v_cvt_pk_bf16_f32 v199, v110, v111
v_mfma_f32_16x16x32_bf16 v[116:119], a[84:87], a[76:79], v[116:119]
v_permlane16_swap_b32 v192, v194
v_mfma_f32_16x16x32_bf16 v[120:123], a[88:91], a[76:79], v[120:123]
v_permlane16_swap_b32 v193, v195
v_mfma_f32_16x16x32_bf16 v[124:127], a[92:95], a[76:79], v[124:127]
v_permlane16_swap_b32 v196, v198
v_mfma_f32_16x16x32_bf16 v[112:115], a[144:147], a[140:143], v[112:115]
v_permlane16_swap_b32 v197, v199
v_mov_b32 v200, v192
v_mfma_f32_16x16x32_bf16 v[116:119], a[148:151], a[140:143], v[116:119]
v_mov_b32 v201, v193
v_mov_b32 v202, v194
v_mov_b32 v203, v195
v_mfma_f32_16x16x32_bf16 v[120:123], a[152:155], a[140:143], v[120:123]
v_mov_b32_dpp v192, v196 row_ror:8 row_mask:0xf bank_mask:0xc
v_mov_b32_dpp v193, v197 row_ror:8 row_mask:0xf bank_mask:0xc
v_mov_b32_dpp v194, v198 row_ror:8 row_mask:0xf bank_mask:0xc
v_mfma_f32_16x16x32_bf16 v[124:127], a[156:159], a[140:143], v[124:127]
v_mov_b32_dpp v195, v199 row_ror:8 row_mask:0xf bank_mask:0xc
buffer_store_dwordx4 v[192:195], v251, s[16:19], s28 offen nt
v_mov_b32_dpp v196, v200 row_ror:8 row_mask:0xf bank_mask:0x3
v_mov_b32_dpp v197, v201 row_ror:8 row_mask:0xf bank_mask:0x3
v_mov_b32_dpp v198, v202 row_ror:8 row_mask:0xf bank_mask:0x3
v_mov_b32_dpp v199, v203 row_ror:8 row_mask:0xf bank_mask:0x3
buffer_store_dwordx4 v[196:199], v251, s[16:19], s29 offen nt
v_cvt_pk_bf16_f32 v204, v112, v113
v_cvt_pk_bf16_f32 v206, v116, v117
v_cvt_pk_bf16_f32 v205, v114, v115
v_cvt_pk_bf16_f32 v207, v118, v119
v_cvt_pk_bf16_f32 v208, v120, v121
v_cvt_pk_bf16_f32 v210, v124, v125
v_cvt_pk_bf16_f32 v209, v122, v123
v_cvt_pk_bf16_f32 v211, v126, v127
v_permlane16_swap_b32 v204, v206
v_permlane16_swap_b32 v205, v207
v_permlane16_swap_b32 v208, v210
v_permlane16_swap_b32 v209, v211
v_mov_b32 v212, v204
v_mov_b32 v213, v205
v_mov_b32 v214, v206
v_mov_b32 v215, v207
v_mov_b32_dpp v204, v208 row_ror:8 row_mask:0xf bank_mask:0xc
v_mov_b32_dpp v205, v209 row_ror:8 row_mask:0xf bank_mask:0xc
v_mov_b32_dpp v206, v210 row_ror:8 row_mask:0xf bank_mask:0xc
v_mov_b32_dpp v207, v211 row_ror:8 row_mask:0xf bank_mask:0xc
buffer_store_dwordx4 v[204:207], v251, s[16:19], s30 offen nt
v_mov_b32_dpp v208, v212 row_ror:8 row_mask:0xf bank_mask:0x3
v_mov_b32_dpp v209, v213 row_ror:8 row_mask:0xf bank_mask:0x3
v_mov_b32_dpp v210, v214 row_ror:8 row_mask:0xf bank_mask:0x3
v_mov_b32_dpp v211, v215 row_ror:8 row_mask:0xf bank_mask:0x3
buffer_store_dwordx4 v[208:211], v251, s[16:19], s31 offen nt
s_branch .Lend
.L5odd_pad:
s_cbranch_execz .Ltch9
.L5odd:
v_mfma_f32_16x16x32_bf16 v[64:67], a[112:115], a[96:99], v[64:67]
ds_read_b64_tr_b16 a[80:81], v245 offset:0
ds_read_b64_tr_b16 a[82:83], v245 offset:4096
v_mfma_f32_16x16x32_bf16 v[68:71], a[116:119], a[96:99], v[68:71]
s_add_u32 m0, s49, 16384
ds_read_b128 a[64:67], v244 offset:0
v_mfma_f32_16x16x32_bf16 v[72:75], a[120:123], a[96:99], v[72:75]
buffer_load_dwordx4 v240, s[8:11], s45 offen offset:0 lds
v_mfma_f32_16x16x32_bf16 v[76:79], a[124:127], a[96:99], v[76:79]
ds_read_b64_tr_b16 a[84:85], v246 offset:0
ds_read_b64_tr_b16 a[86:87], v246 offset:4096
v_mfma_f32_16x16x32_bf16 v[80:83], a[112:115], a[100:103], v[80:83]
s_add_u32 s44, s44, 4096
ds_read_b128 a[68:71], v244 offset:1024
v_mfma_f32_16x16x32_bf16 v[84:87], a[116:119], a[100:103], v[84:87]
ds_read_b64_tr_b16 a[88:89], v247 offset:0
ds_read_b64_tr_b16 a[90:91], v247 offset:4096
v_mfma_f32_16x16x32_bf16 v[88:91], a[120:123], a[100:103], v[88:91]
buffer_load_dwordx4 v241, s[8:11], s45 offen offset:1024 lds
v_mfma_f32_16x16x32_bf16 v[92:95], a[124:127], a[100:103], v[92:95]
buffer_load_dword v250, v249, s[60:63], s90 offen sc1
ds_read_b128 a[72:75], v244 offset:4096
v_mfma_f32_16x16x32_bf16 v[96:99], a[112:115], a[104:107], v[96:99]
ds_read_b64_tr_b16 a[92:93], v248 offset:0
ds_read_b64_tr_b16 a[94:95], v248 offset:4096
v_mfma_f32_16x16x32_bf16 v[100:103], a[116:119], a[104:107], v[100:103]
ds_read_b128 a[76:79], v244 offset:5120
v_mfma_f32_16x16x32_bf16 v[104:107], a[120:123], a[104:107], v[104:107]
buffer_load_dwordx4 v242, s[8:11], s45 offen offset:2048 lds
v_mfma_f32_16x16x32_bf16 v[108:111], a[124:127], a[104:107], v[108:111]
s_cmp_eq_u32 s46, 0
s_cselect_b32 s53, s56, s57
v_mfma_f32_16x16x32_bf16 v[112:115], a[112:115], a[108:111], v[112:115]
v_mfma_f32_16x16x32_bf16 v[116:119], a[116:119], a[108:111], v[116:119]
v_mfma_f32_16x16x32_bf16 v[120:123], a[120:123], a[108:111], v[120:123]
buffer_load_dwordx4 v243, s[8:11], s45 offen offset:3072 lds
v_mfma_f32_16x16x32_bf16 v[124:127], a[124:127], a[108:111], v[124:127]
s_waitcnt lgkmcnt(0)
s_mov_b32 m0, s49
s_add_u32 s45, s45, 131072
v_mfma_f32_16x16x32_bf16 v[64:67], a[80:83], a[64:67], v[64:67]
ds_read_b64_tr_b16 a[112:113], v245 offset:8192
ds_read_b64_tr_b16 a[114:115], v245 offset:12288
v_mfma_f32_16x16x32_bf16 v[68:71], a[84:87], a[64:67], v[68:71]
ds_read_b128 a[96:99], v244 offset:2048
v_add_u32 v245, s53, v245
v_mfma_f32_16x16x32_bf16 v[72:75], a[88:91], a[64:67], v[72:75]
buffer_load_dwordx4 v7, s[4:7], s44 offen offset:0 sc1 lds
v_mfma_f32_16x16x32_bf16 v[76:79], a[92:95], a[64:67], v[76:79]
ds_read_b64_tr_b16 a[116:117], v246 offset:8192
ds_read_b64_tr_b16 a[118:119], v246 offset:12288
v_mfma_f32_16x16x32_bf16 v[80:83], a[80:83], a[68:71], v[80:83]
ds_read_b128 a[100:103], v244 offset:3072
v_add_u32 v246, s53, v246
v_mfma_f32_16x16x32_bf16 v[84:87], a[84:87], a[68:71], v[84:87]
ds_read_b64_tr_b16 a[120:121], v247 offset:8192
ds_read_b64_tr_b16 a[122:123], v247 offset:12288
v_mfma_f32_16x16x32_bf16 v[88:91], a[88:91], a[68:71], v[88:91]
buffer_load_dwordx4 v7, s[4:7], s44 offen offset:1024 sc1 lds
v_mfma_f32_16x16x32_bf16 v[92:95], a[92:95], a[68:71], v[92:95]
ds_read_b128 a[104:107], v244 offset:6144
v_add_u32 v247, s53, v247
v_mfma_f32_16x16x32_bf16 v[96:99], a[80:83], a[72:75], v[96:99]
ds_read_b64_tr_b16 a[124:125], v248 offset:8192
ds_read_b64_tr_b16 a[126:127], v248 offset:12288
v_mfma_f32_16x16x32_bf16 v[100:103], a[84:87], a[72:75], v[100:103]
ds_read_b128 a[108:111], v244 offset:7168
v_add_u32 v248, s53, v248
v_mfma_f32_16x16x32_bf16 v[104:107], a[88:91], a[72:75], v[104:107]
buffer_load_dwordx4 v7, s[4:7], s44 offen offset:2048 sc1 lds
v_mfma_f32_16x16x32_bf16 v[108:111], a[92:95], a[72:75], v[108:111]
s_add_u32 s49, s46, s91
v_add_u32 v244, s53, v244
v_mfma_f32_16x16x32_bf16 v[112:115], a[80:83], a[76:79], v[112:115]
s_add_u32 s46, s46, s53
v_mfma_f32_16x16x32_bf16 v[116:119], a[84:87], a[76:79], v[116:119]
v_mfma_f32_16x16x32_bf16 v[120:123], a[88:91], a[76:79], v[120:123]
buffer_load_dwordx4 v7, s[4:7], s44 offen offset:3072 sc1 lds
v_mfma_f32_16x16x32_bf16 v[124:127], a[92:95], a[76:79], v[124:127]
s_waitcnt vmcnt(25) lgkmcnt(0)
s_barrier
v_mfma_f32_16x16x32_bf16 v[64:67], a[112:115], a[96:99], v[64:67]
ds_read_b64_tr_b16 a[80:81], v245 offset:0
ds_read_b64_tr_b16 a[82:83], v245 offset:4096
v_mfma_f32_16x16x32_bf16 v[68:71], a[116:119], a[96:99], v[68:71]
s_add_u32 m0, s49, 16384
ds_read_b128 a[64:67], v244 offset:0
v_mfma_f32_16x16x32_bf16 v[72:75], a[120:123], a[96:99], v[72:75]
buffer_load_dwordx4 v240, s[8:11], s45 offen offset:0 lds
v_mfma_f32_16x16x32_bf16 v[76:79], a[124:127], a[96:99], v[76:79]
ds_read_b64_tr_b16 a[84:85], v246 offset:0
ds_read_b64_tr_b16 a[86:87], v246 offset:4096
v_mfma_f32_16x16x32_bf16 v[80:83], a[112:115], a[100:103], v[80:83]
s_add_u32 s44, s44, 4096
ds_read_b128 a[68:71], v244 offset:1024
v_mfma_f32_16x16x32_bf16 v[84:87], a[116:119], a[100:103], v[84:87]
ds_read_b64_tr_b16 a[88:89], v247 offset:0
ds_read_b64_tr_b16 a[90:91], v247 offset:4096
v_mfma_f32_16x16x32_bf16 v[88:91], a[120:123], a[100:103], v[88:91]
buffer_load_dwordx4 v241, s[8:11], s45 offen offset:1024 lds
v_mfma_f32_16x16x32_bf16 v[92:95], a[124:127], a[100:103], v[92:95]
ds_read_b128 a[72:75], v244 offset:4096
v_mfma_f32_16x16x32_bf16 v[96:99], a[112:115], a[104:107], v[96:99]
ds_read_b64_tr_b16 a[92:93], v248 offset:0
ds_read_b64_tr_b16 a[94:95], v248 offset:4096
v_mfma_f32_16x16x32_bf16 v[100:103], a[116:119], a[104:107], v[100:103]
ds_read_b128 a[76:79], v244 offset:5120
v_mfma_f32_16x16x32_bf16 v[104:107], a[120:123], a[104:107], v[104:107]
buffer_load_dwordx4 v242, s[8:11], s45 offen offset:2048 lds
v_mfma_f32_16x16x32_bf16 v[108:111], a[124:127], a[104:107], v[108:111]
s_cmp_eq_u32 s46, 0
s_cselect_b32 s53, s56, s57
v_mfma_f32_16x16x32_bf16 v[112:115], a[112:115], a[108:111], v[112:115]
v_mfma_f32_16x16x32_bf16 v[116:119], a[116:119], a[108:111], v[116:119]
v_mfma_f32_16x16x32_bf16 v[120:123], a[120:123], a[108:111], v[120:123]
buffer_load_dwordx4 v243, s[8:11], s45 offen offset:3072 lds
v_mfma_f32_16x16x32_bf16 v[124:127], a[124:127], a[108:111], v[124:127]
s_waitcnt lgkmcnt(0)
s_mov_b32 m0, s49
s_add_u32 s45, s45, 131072
v_mfma_f32_16x16x32_bf16 v[64:67], a[80:83], a[64:67], v[64:67]
ds_read_b64_tr_b16 a[112:113], v245 offset:8192
ds_read_b64_tr_b16 a[114:115], v245 offset:12288
v_mfma_f32_16x16x32_bf16 v[68:71], a[84:87], a[64:67], v[68:71]
ds_read_b128 a[96:99], v244 offset:2048
v_add_u32 v245, s53, v245
v_mfma_f32_16x16x32_bf16 v[72:75], a[88:91], a[64:67], v[72:75]
buffer_load_dwordx4 v7, s[4:7], s44 offen offset:0 sc1 lds
v_mfma_f32_16x16x32_bf16 v[76:79], a[92:95], a[64:67], v[76:79]
ds_read_b64_tr_b16 a[116:117], v246 offset:8192
ds_read_b64_tr_b16 a[118:119], v246 offset:12288
v_mfma_f32_16x16x32_bf16 v[80:83], a[80:83], a[68:71], v[80:83]
ds_read_b128 a[100:103], v244 offset:3072
v_add_u32 v246, s53, v246
v_mfma_f32_16x16x32_bf16 v[84:87], a[84:87], a[68:71], v[84:87]
ds_read_b64_tr_b16 a[120:121], v247 offset:8192
ds_read_b64_tr_b16 a[122:123], v247 offset:12288
v_mfma_f32_16x16x32_bf16 v[88:91], a[88:91], a[68:71], v[88:91]
buffer_load_dwordx4 v7, s[4:7], s44 offen offset:1024 sc1 lds
v_mfma_f32_16x16x32_bf16 v[92:95], a[92:95], a[68:71], v[92:95]
ds_read_b128 a[104:107], v244 offset:6144
v_add_u32 v247, s53, v247
v_mfma_f32_16x16x32_bf16 v[96:99], a[80:83], a[72:75], v[96:99]
ds_read_b64_tr_b16 a[124:125], v248 offset:8192
ds_read_b64_tr_b16 a[126:127], v248 offset:12288
v_mfma_f32_16x16x32_bf16 v[100:103], a[84:87], a[72:75], v[100:103]
ds_read_b128 a[108:111], v244 offset:7168
v_add_u32 v248, s53, v248
v_mfma_f32_16x16x32_bf16 v[104:107], a[88:91], a[72:75], v[104:107]
buffer_load_dwordx4 v7, s[4:7], s44 offen offset:2048 sc1 lds
v_mfma_f32_16x16x32_bf16 v[108:111], a[92:95], a[72:75], v[108:111]
s_add_u32 s49, s46, s91
v_add_u32 v244, s53, v244
v_mfma_f32_16x16x32_bf16 v[112:115], a[80:83], a[76:79], v[112:115]
s_add_u32 s46, s46, s53
v_mfma_f32_16x16x32_bf16 v[116:119], a[84:87], a[76:79], v[116:119]
v_mfma_f32_16x16x32_bf16 v[120:123], a[88:91], a[76:79], v[120:123]
buffer_load_dwordx4 v7, s[4:7], s44 offen offset:3072 sc1 lds
v_mfma_f32_16x16x32_bf16 v[124:127], a[92:95], a[76:79], v[124:127]
s_waitcnt vmcnt(25) lgkmcnt(0)
s_barrier
s_lshl_b32 s54, s51, 2
s_add_u32 s54, s54, s3
s_lshl_b32 s54, s54, 16
s_add_u32 s54, s54, 50331648
s_add_u32 s4, s84, s54
s_addc_u32 s5, s85, 0
s_and_b32 s5, s5, 0xffff
s_mov_b32 s6, 65536
s_lshl_b32 s54, s3, 15
s_add_u32 s54, s54, s52
s_sub_u32 s28, 0x200000, s54
s_add_u32 s8, s68, s54
s_addc_u32 s9, s69, 0
s_and_b32 s9, s9, 0xffff
s_mov_b32 s10, s28
s_mov_b32 s44, -4096
s_mov_b32 s45, 0
v_mfma_f32_16x16x32_bf16 v[64:67], a[112:115], a[96:99], v[64:67]
ds_read_b64_tr_b16 a[80:81], v245 offset:0
ds_read_b64_tr_b16 a[82:83], v245 offset:4096
v_mfma_f32_16x16x32_bf16 v[68:71], a[116:119], a[96:99], v[68:71]
s_add_u32 m0, s49, 16384
ds_read_b128 a[64:67], v244 offset:0
v_mfma_f32_16x16x32_bf16 v[72:75], a[120:123], a[96:99], v[72:75]
buffer_load_dwordx4 v240, s[8:11], s45 offen offset:0 lds
v_mfma_f32_16x16x32_bf16 v[76:79], a[124:127], a[96:99], v[76:79]
ds_read_b64_tr_b16 a[84:85], v246 offset:0
ds_read_b64_tr_b16 a[86:87], v246 offset:4096
v_mfma_f32_16x16x32_bf16 v[80:83], a[112:115], a[100:103], v[80:83]
s_add_u32 s44, s44, 4096
ds_read_b128 a[68:71], v244 offset:1024
v_mfma_f32_16x16x32_bf16 v[84:87], a[116:119], a[100:103], v[84:87]
ds_read_b64_tr_b16 a[88:89], v247 offset:0
ds_read_b64_tr_b16 a[90:91], v247 offset:4096
v_mfma_f32_16x16x32_bf16 v[88:91], a[120:123], a[100:103], v[88:91]
buffer_load_dwordx4 v241, s[8:11], s45 offen offset:1024 lds
v_mfma_f32_16x16x32_bf16 v[92:95], a[124:127], a[100:103], v[92:95]
ds_read_b128 a[72:75], v244 offset:4096
v_mfma_f32_16x16x32_bf16 v[96:99], a[112:115], a[104:107], v[96:99]
ds_read_b64_tr_b16 a[92:93], v248 offset:0
ds_read_b64_tr_b16 a[94:95], v248 offset:4096
v_mfma_f32_16x16x32_bf16 v[100:103], a[116:119], a[104:107], v[100:103]
ds_read_b128 a[76:79], v244 offset:5120
v_mfma_f32_16x16x32_bf16 v[104:107], a[120:123], a[104:107], v[104:107]
s_waitcnt vmcnt(16)
buffer_load_dwordx4 v242, s[8:11], s45 offen offset:2048 lds
v_and_b32 v250, 7, v250
v_mfma_f32_16x16x32_bf16 v[108:111], a[124:127], a[104:107], v[108:111]
s_cmp_eq_u32 s46, 0
s_cselect_b32 s53, s56, s57
v_cmp_gt_u32 vcc, 4, v250
v_mfma_f32_16x16x32_bf16 v[112:115], a[112:115], a[108:111], v[112:115]
v_mfma_f32_16x16x32_bf16 v[116:119], a[116:119], a[108:111], v[116:119]
s_cmp_lg_u64 vcc, 0
s_cbranch_scc1 .Lspin4
.Lspin4_ret:
v_mfma_f32_16x16x32_bf16 v[120:123], a[120:123], a[108:111], v[120:123]
buffer_load_dwordx4 v243, s[8:11], s45 offen offset:3072 lds
v_mfma_f32_16x16x32_bf16 v[124:127], a[124:127], a[108:111], v[124:127]
s_waitcnt lgkmcnt(0)
s_mov_b32 m0, s49
s_add_u32 s45, s45, 131072
v_mfma_f32_16x16x32_bf16 v[64:67], a[80:83], a[64:67], v[64:67]
ds_read_b64_tr_b16 a[112:113], v245 offset:8192
ds_read_b64_tr_b16 a[114:115], v245 offset:12288
v_mfma_f32_16x16x32_bf16 v[68:71], a[84:87], a[64:67], v[68:71]
ds_read_b128 a[96:99], v244 offset:2048
v_add_u32 v245, s53, v245
v_mfma_f32_16x16x32_bf16 v[72:75], a[88:91], a[64:67], v[72:75]
buffer_load_dwordx4 v7, s[4:7], s44 offen offset:0 sc1 lds
v_mfma_f32_16x16x32_bf16 v[76:79], a[92:95], a[64:67], v[76:79]
ds_read_b64_tr_b16 a[116:117], v246 offset:8192
ds_read_b64_tr_b16 a[118:119], v246 offset:12288
.Lpad21:
s_cbranch_execz .Ltch9
v_mfma_f32_16x16x32_bf16 v[80:83], a[80:83], a[68:71], v[80:83]
ds_read_b128 a[100:103], v244 offset:3072
v_add_u32 v246, s53, v246
v_mfma_f32_16x16x32_bf16 v[84:87], a[84:87], a[68:71], v[84:87]
ds_read_b64_tr_b16 a[120:121], v247 offset:8192
ds_read_b64_tr_b16 a[122:123], v247 offset:12288
v_mfma_f32_16x16x32_bf16 v[88:91], a[88:91], a[68:71], v[88:91]
buffer_load_dwordx4 v7, s[4:7], s44 offen offset:1024 sc1 lds
v_mfma_f32_16x16x32_bf16 v[92:95], a[92:95], a[68:71], v[92:95]
ds_read_b128 a[104:107], v244 offset:6144
v_add_u32 v247, s53, v247
v_mfma_f32_16x16x32_bf16 v[96:99], a[80:83], a[72:75], v[96:99]
ds_read_b64_tr_b16 a[124:125], v248 offset:8192
ds_read_b64_tr_b16 a[126:127], v248 offset:12288
v_mfma_f32_16x16x32_bf16 v[100:103], a[84:87], a[72:75], v[100:103]
ds_read_b128 a[108:111], v244 offset:7168
v_add_u32 v248, s53, v248
v_mfma_f32_16x16x32_bf16 v[104:107], a[88:91], a[72:75], v[104:107]
buffer_load_dwordx4 v7, s[4:7], s44 offen offset:2048 sc1 lds
v_mfma_f32_16x16x32_bf16 v[108:111], a[92:95], a[72:75], v[108:111]
s_add_u32 s49, s46, s91
v_add_u32 v244, s53, v244
v_mfma_f32_16x16x32_bf16 v[112:115], a[80:83], a[76:79], v[112:115]
s_add_u32 s46, s46, s53
v_mfma_f32_16x16x32_bf16 v[116:119], a[84:87], a[76:79], v[116:119]
v_mfma_f32_16x16x32_bf16 v[120:123], a[88:91], a[76:79], v[120:123]
buffer_load_dwordx4 v7, s[4:7], s44 offen offset:3072 sc1 lds
v_mfma_f32_16x16x32_bf16 v[124:127], a[92:95], a[76:79], v[124:127]
s_waitcnt vmcnt(25) lgkmcnt(0)
s_barrier
v_mfma_f32_16x16x32_bf16 v[64:67], a[112:115], a[96:99], v[64:67]
ds_read_b64_tr_b16 a[80:81], v245 offset:0
ds_read_b64_tr_b16 a[82:83], v245 offset:4096
v_mfma_f32_16x16x32_bf16 v[68:71], a[116:119], a[96:99], v[68:71]
s_add_u32 m0, s49, 16384
ds_read_b128 a[64:67], v244 offset:0
v_mfma_f32_16x16x32_bf16 v[72:75], a[120:123], a[96:99], v[72:75]
buffer_load_dwordx4 v240, s[8:11], s45 offen offset:0 lds
v_mfma_f32_16x16x32_bf16 v[76:79], a[124:127], a[96:99], v[76:79]
ds_read_b64_tr_b16 a[84:85], v246 offset:0
ds_read_b64_tr_b16 a[86:87], v246 offset:4096
v_mfma_f32_16x16x32_bf16 v[80:83], a[112:115], a[100:103], v[80:83]
s_add_u32 s44, s44, 4096
ds_read_b128 a[68:71], v244 offset:1024
v_mfma_f32_16x16x32_bf16 v[84:87], a[116:119], a[100:103], v[84:87]
ds_read_b64_tr_b16 a[88:89], v247 offset:0
ds_read_b64_tr_b16 a[90:91], v247 offset:4096
v_mfma_f32_16x16x32_bf16 v[88:91], a[120:123], a[100:103], v[88:91]
buffer_load_dwordx4 v241, s[8:11], s45 offen offset:1024 lds
v_mfma_f32_16x16x32_bf16 v[92:95], a[124:127], a[100:103], v[92:95]
ds_read_b128 a[72:75], v244 offset:4096
v_mfma_f32_16x16x32_bf16 v[96:99], a[112:115], a[104:107], v[96:99]
ds_read_b64_tr_b16 a[92:93], v248 offset:0
ds_read_b64_tr_b16 a[94:95], v248 offset:4096
v_mfma_f32_16x16x32_bf16 v[100:103], a[116:119], a[104:107], v[100:103]
ds_read_b128 a[76:79], v244 offset:5120
v_mfma_f32_16x16x32_bf16 v[104:107], a[120:123], a[104:107], v[104:107]
buffer_load_dwordx4 v242, s[8:11], s45 offen offset:2048 lds
v_mfma_f32_16x16x32_bf16 v[108:111], a[124:127], a[104:107], v[108:111]
s_cmp_eq_u32 s46, 0
s_cselect_b32 s53, s56, s57
v_mfma_f32_16x16x32_bf16 v[112:115], a[112:115], a[108:111], v[112:115]
v_mfma_f32_16x16x32_bf16 v[116:119], a[116:119], a[108:111], v[116:119]
v_mfma_f32_16x16x32_bf16 v[120:123], a[120:123], a[108:111], v[120:123]
buffer_load_dwordx4 v243, s[8:11], s45 offen offset:3072 lds
v_mfma_f32_16x16x32_bf16 v[124:127], a[124:127], a[108:111], v[124:127]
s_waitcnt lgkmcnt(0)
s_mov_b32 m0, s49
s_add_u32 s45, s45, 131072
v_mfma_f32_16x16x32_bf16 v[64:67], a[80:83], a[64:67], v[64:67]
ds_read_b64_tr_b16 a[112:113], v245 offset:8192
ds_read_b64_tr_b16 a[114:115], v245 offset:12288
v_mfma_f32_16x16x32_bf16 v[68:71], a[84:87], a[64:67], v[68:71]
ds_read_b128 a[96:99], v244 offset:2048
v_add_u32 v245, s53, v245
v_mfma_f32_16x16x32_bf16 v[72:75], a[88:91], a[64:67], v[72:75]
buffer_load_dwordx4 v7, s[4:7], s44 offen offset:0 sc1 lds
v_mfma_f32_16x16x32_bf16 v[76:79], a[92:95], a[64:67], v[76:79]
ds_read_b64_tr_b16 a[116:117], v246 offset:8192
ds_read_b64_tr_b16 a[118:119], v246 offset:12288
v_mfma_f32_16x16x32_bf16 v[80:83], a[80:83], a[68:71], v[80:83]
ds_read_b128 a[100:103], v244 offset:3072
v_add_u32 v246, s53, v246
v_mfma_f32_16x16x32_bf16 v[84:87], a[84:87], a[68:71], v[84:87]
ds_read_b64_tr_b16 a[120:121], v247 offset:8192
ds_read_b64_tr_b16 a[122:123], v247 offset:12288
v_mfma_f32_16x16x32_bf16 v[88:91], a[88:91], a[68:71], v[88:91]
buffer_load_dwordx4 v7, s[4:7], s44 offen offset:1024 sc1 lds
v_mfma_f32_16x16x32_bf16 v[92:95], a[92:95], a[68:71], v[92:95]
ds_read_b128 a[104:107], v244 offset:6144
v_add_u32 v247, s53, v247
v_mfma_f32_16x16x32_bf16 v[96:99], a[80:83], a[72:75], v[96:99]
ds_read_b64_tr_b16 a[124:125], v248 offset:8192
ds_read_b64_tr_b16 a[126:127], v248 offset:12288
v_mfma_f32_16x16x32_bf16 v[100:103], a[84:87], a[72:75], v[100:103]
ds_read_b128 a[108:111], v244 offset:7168
v_add_u32 v248, s53, v248
v_mfma_f32_16x16x32_bf16 v[104:107], a[88:91], a[72:75], v[104:107]
buffer_load_dwordx4 v7, s[4:7], s44 offen offset:2048 sc1 lds
v_mfma_f32_16x16x32_bf16 v[108:111], a[92:95], a[72:75], v[108:111]
s_add_u32 s49, s46, s91
v_add_u32 v244, s53, v244
v_mfma_f32_16x16x32_bf16 v[112:115], a[80:83], a[76:79], v[112:115]
s_add_u32 s46, s46, s53
v_mfma_f32_16x16x32_bf16 v[116:119], a[84:87], a[76:79], v[116:119]
v_mfma_f32_16x16x32_bf16 v[120:123], a[88:91], a[76:79], v[120:123]
buffer_load_dwordx4 v7, s[4:7], s44 offen offset:3072 sc1 lds
v_mfma_f32_16x16x32_bf16 v[124:127], a[92:95], a[76:79], v[124:127]
s_waitcnt vmcnt(24) lgkmcnt(0)
s_barrier
v_mfma_f32_16x16x32_bf16 v[64:67], a[112:115], a[96:99], v[64:67]
ds_read_b64_tr_b16 a[80:81], v245 offset:0
ds_read_b64_tr_b16 a[82:83], v245 offset:4096
v_mfma_f32_16x16x32_bf16 v[68:71], a[116:119], a[96:99], v[68:71]
s_add_u32 m0, s49, 16384
ds_read_b128 a[64:67], v244 offset:0
v_mfma_f32_16x16x32_bf16 v[72:75], a[120:123], a[96:99], v[72:75]
buffer_load_dwordx4 v240, s[8:11], s45 offen offset:0 lds
v_mfma_f32_16x16x32_bf16 v[76:79], a[124:127], a[96:99], v[76:79]
ds_read_b64_tr_b16 a[84:85], v246 offset:0
ds_read_b64_tr_b16 a[86:87], v246 offset:4096
v_mfma_f32_16x16x32_bf16 v[80:83], a[112:115], a[100:103], v[80:83]
s_add_u32 s44, s44, 4096
ds_read_b128 a[68:71], v244 offset:1024
v_mfma_f32_16x16x32_bf16 v[84:87], a[116:119], a[100:103], v[84:87]
ds_read_b64_tr_b16 a[88:89], v247 offset:0
ds_read_b64_tr_b16 a[90:91], v247 offset:4096
v_mfma_f32_16x16x32_bf16 v[88:91], a[120:123], a[100:103], v[88:91]
buffer_load_dwordx4 v241, s[8:11], s45 offen offset:1024 lds
v_mfma_f32_16x16x32_bf16 v[92:95], a[124:127], a[100:103], v[92:95]
ds_read_b128 a[72:75], v244 offset:4096
v_mfma_f32_16x16x32_bf16 v[96:99], a[112:115], a[104:107], v[96:99]
ds_read_b64_tr_b16 a[92:93], v248 offset:0
ds_read_b64_tr_b16 a[94:95], v248 offset:4096
v_mfma_f32_16x16x32_bf16 v[100:103], a[116:119], a[104:107], v[100:103]
ds_read_b128 a[76:79], v244 offset:5120
v_mfma_f32_16x16x32_bf16 v[104:107], a[120:123], a[104:107], v[104:107]
buffer_load_dwordx4 v242, s[8:11], s45 offen offset:2048 lds
v_mfma_f32_16x16x32_bf16 v[108:111], a[124:127], a[104:107], v[108:111]
s_cmp_eq_u32 s46, 0
s_cselect_b32 s53, s56, s57
v_mfma_f32_16x16x32_bf16 v[112:115], a[112:115], a[108:111], v[112:115]
v_mfma_f32_16x16x32_bf16 v[116:119], a[116:119], a[108:111], v[116:119]
v_mfma_f32_16x16x32_bf16 v[120:123], a[120:123], a[108:111], v[120:123]
buffer_load_dwordx4 v243, s[8:11], s45 offen offset:3072 lds
v_mfma_f32_16x16x32_bf16 v[124:127], a[124:127], a[108:111], v[124:127]
s_waitcnt lgkmcnt(0)
s_mov_b32 m0, s49
s_add_u32 s45, s45, 131072
v_mfma_f32_16x16x32_bf16 v[64:67], a[80:83], a[64:67], v[64:67]
ds_read_b64_tr_b16 a[112:113], v245 offset:8192
ds_read_b64_tr_b16 a[114:115], v245 offset:12288
v_mfma_f32_16x16x32_bf16 v[68:71], a[84:87], a[64:67], v[68:71]
ds_read_b128 a[96:99], v244 offset:2048
v_add_u32 v245, s53, v245
v_mfma_f32_16x16x32_bf16 v[72:75], a[88:91], a[64:67], v[72:75]
buffer_load_dwordx4 v7, s[4:7], s44 offen offset:0 sc1 lds
v_mfma_f32_16x16x32_bf16 v[76:79], a[92:95], a[64:67], v[76:79]
ds_read_b64_tr_b16 a[116:117], v246 offset:8192
ds_read_b64_tr_b16 a[118:119], v246 offset:12288
v_mfma_f32_16x16x32_bf16 v[80:83], a[80:83], a[68:71], v[80:83]
ds_read_b128 a[100:103], v244 offset:3072
v_add_u32 v246, s53, v246
v_mfma_f32_16x16x32_bf16 v[84:87], a[84:87], a[68:71], v[84:87]
ds_read_b64_tr_b16 a[120:121], v247 offset:8192
ds_read_b64_tr_b16 a[122:123], v247 offset:12288
v_mfma_f32_16x16x32_bf16 v[88:91], a[88:91], a[68:71], v[88:91]
buffer_load_dwordx4 v7, s[4:7], s44 offen offset:1024 sc1 lds
v_mfma_f32_16x16x32_bf16 v[92:95], a[92:95], a[68:71], v[92:95]
ds_read_b128 a[104:107], v244 offset:6144
v_add_u32 v247, s53, v247
v_mfma_f32_16x16x32_bf16 v[96:99], a[80:83], a[72:75], v[96:99]
ds_read_b64_tr_b16 a[124:125], v248 offset:8192
ds_read_b64_tr_b16 a[126:127], v248 offset:12288
v_mfma_f32_16x16x32_bf16 v[100:103], a[84:87], a[72:75], v[100:103]
ds_read_b128 a[108:111], v244 offset:7168
v_add_u32 v248, s53, v248
v_mfma_f32_16x16x32_bf16 v[104:107], a[88:91], a[72:75], v[104:107]
buffer_load_dwordx4 v7, s[4:7], s44 offen offset:2048 sc1 lds
v_mfma_f32_16x16x32_bf16 v[108:111], a[92:95], a[72:75], v[108:111]
s_add_u32 s49, s46, s91
v_add_u32 v244, s53, v244
v_mfma_f32_16x16x32_bf16 v[112:115], a[80:83], a[76:79], v[112:115]
s_add_u32 s46, s46, s53
v_mfma_f32_16x16x32_bf16 v[116:119], a[84:87], a[76:79], v[116:119]
v_mfma_f32_16x16x32_bf16 v[120:123], a[88:91], a[76:79], v[120:123]
buffer_load_dwordx4 v7, s[4:7], s44 offen offset:3072 sc1 lds
v_mfma_f32_16x16x32_bf16 v[124:127], a[92:95], a[76:79], v[124:127]
s_waitcnt vmcnt(24) lgkmcnt(0)
s_barrier
v_mfma_f32_16x16x32_bf16 v[64:67], a[112:115], a[96:99], v[64:67]
ds_read_b64_tr_b16 a[80:81], v245 offset:0
ds_read_b64_tr_b16 a[82:83], v245 offset:4096
v_mfma_f32_16x16x32_bf16 v[68:71], a[116:119], a[96:99], v[68:71]
s_add_u32 m0, s49, 16384
ds_read_b128 a[64:67], v244 offset:0
v_mfma_f32_16x16x32_bf16 v[72:75], a[120:123], a[96:99], v[72:75]
buffer_load_dwordx4 v240, s[8:11], s45 offen offset:0 lds
v_mfma_f32_16x16x32_bf16 v[76:79], a[124:127], a[96:99], v[76:79]
ds_read_b64_tr_b16 a[84:85], v246 offset:0
ds_read_b64_tr_b16 a[86:87], v246 offset:4096
v_mfma_f32_16x16x32_bf16 v[80:83], a[112:115], a[100:103], v[80:83]
s_add_u32 s44, s44, 4096
ds_read_b128 a[68:71], v244 offset:1024
v_mfma_f32_16x16x32_bf16 v[84:87], a[116:119], a[100:103], v[84:87]
ds_read_b64_tr_b16 a[88:89], v247 offset:0
ds_read_b64_tr_b16 a[90:91], v247 offset:4096
v_mfma_f32_16x16x32_bf16 v[88:91], a[120:123], a[100:103], v[88:91]
buffer_load_dwordx4 v241, s[8:11], s45 offen offset:1024 lds
v_mfma_f32_16x16x32_bf16 v[92:95], a[124:127], a[100:103], v[92:95]
ds_read_b128 a[72:75], v244 offset:4096
v_mfma_f32_16x16x32_bf16 v[96:99], a[112:115], a[104:107], v[96:99]
ds_read_b64_tr_b16 a[92:93], v248 offset:0
ds_read_b64_tr_b16 a[94:95], v248 offset:4096
v_mfma_f32_16x16x32_bf16 v[100:103], a[116:119], a[104:107], v[100:103]
ds_read_b128 a[76:79], v244 offset:5120
v_mfma_f32_16x16x32_bf16 v[104:107], a[120:123], a[104:107], v[104:107]
buffer_load_dwordx4 v242, s[8:11], s45 offen offset:2048 lds
v_mfma_f32_16x16x32_bf16 v[108:111], a[124:127], a[104:107], v[108:111]
s_cmp_eq_u32 s46, 0
s_cselect_b32 s53, s56, s57
v_mfma_f32_16x16x32_bf16 v[112:115], a[112:115], a[108:111], v[112:115]
v_mfma_f32_16x16x32_bf16 v[116:119], a[116:119], a[108:111], v[116:119]
v_mfma_f32_16x16x32_bf16 v[120:123], a[120:123], a[108:111], v[120:123]
buffer_load_dwordx4 v243, s[8:11], s45 offen offset:3072 lds
v_mfma_f32_16x16x32_bf16 v[124:127], a[124:127], a[108:111], v[124:127]
s_waitcnt lgkmcnt(0)
s_mov_b32 m0, s49
s_add_u32 s45, s45, 131072
v_mfma_f32_16x16x32_bf16 v[64:67], a[80:83], a[64:67], v[64:67]
ds_read_b64_tr_b16 a[112:113], v245 offset:8192
ds_read_b64_tr_b16 a[114:115], v245 offset:12288
v_mfma_f32_16x16x32_bf16 v[68:71], a[84:87], a[64:67], v[68:71]
ds_read_b128 a[96:99], v244 offset:2048
v_add_u32 v245, s53, v245
v_mfma_f32_16x16x32_bf16 v[72:75], a[88:91], a[64:67], v[72:75]
buffer_load_dwordx4 v7, s[4:7], s44 offen offset:0 sc1 lds
v_mfma_f32_16x16x32_bf16 v[76:79], a[92:95], a[64:67], v[76:79]
ds_read_b64_tr_b16 a[116:117], v246 offset:8192
ds_read_b64_tr_b16 a[118:119], v246 offset:12288
v_mfma_f32_16x16x32_bf16 v[80:83], a[80:83], a[68:71], v[80:83]
ds_read_b128 a[100:103], v244 offset:3072
v_add_u32 v246, s53, v246
v_mfma_f32_16x16x32_bf16 v[84:87], a[84:87], a[68:71], v[84:87]
ds_read_b64_tr_b16 a[120:121], v247 offset:8192
ds_read_b64_tr_b16 a[122:123], v247 offset:12288
v_mfma_f32_16x16x32_bf16 v[88:91], a[88:91], a[68:71], v[88:91]
buffer_load_dwordx4 v7, s[4:7], s44 offen offset:1024 sc1 lds
v_mfma_f32_16x16x32_bf16 v[92:95], a[92:95], a[68:71], v[92:95]
ds_read_b128 a[104:107], v244 offset:6144
v_add_u32 v247, s53, v247
v_mfma_f32_16x16x32_bf16 v[96:99], a[80:83], a[72:75], v[96:99]
ds_read_b64_tr_b16 a[124:125], v248 offset:8192
ds_read_b64_tr_b16 a[126:127], v248 offset:12288
v_mfma_f32_16x16x32_bf16 v[100:103], a[84:87], a[72:75], v[100:103]
ds_read_b128 a[108:111], v244 offset:7168
v_add_u32 v248, s53, v248
v_mfma_f32_16x16x32_bf16 v[104:107], a[88:91], a[72:75], v[104:107]
buffer_load_dwordx4 v7, s[4:7], s44 offen offset:2048 sc1 lds
v_mfma_f32_16x16x32_bf16 v[108:111], a[92:95], a[72:75], v[108:111]
s_add_u32 s49, s46, s91
v_add_u32 v244, s53, v244
v_mfma_f32_16x16x32_bf16 v[112:115], a[80:83], a[76:79], v[112:115]
s_add_u32 s46, s46, s53
v_mfma_f32_16x16x32_bf16 v[116:119], a[84:87], a[76:79], v[116:119]
v_mfma_f32_16x16x32_bf16 v[120:123], a[88:91], a[76:79], v[120:123]
buffer_load_dwordx4 v7, s[4:7], s44 offen offset:3072 sc1 lds
v_mfma_f32_16x16x32_bf16 v[124:127], a[92:95], a[76:79], v[124:127]
s_waitcnt vmcnt(24) lgkmcnt(0)
s_barrier
v_mfma_f32_16x16x32_bf16 v[64:67], a[112:115], a[96:99], v[64:67]
ds_read_b64_tr_b16 a[80:81], v245 offset:0
ds_read_b64_tr_b16 a[82:83], v245 offset:4096
v_mfma_f32_16x16x32_bf16 v[68:71], a[116:119], a[96:99], v[68:71]
s_add_u32 m0, s49, 16384
ds_read_b128 a[64:67], v244 offset:0
v_mfma_f32_16x16x32_bf16 v[72:75], a[120:123], a[96:99], v[72:75]
buffer_load_dwordx4 v240, s[8:11], s45 offen offset:0 lds
v_mfma_f32_16x16x32_bf16 v[76:79], a[124:127], a[96:99], v[76:79]
ds_read_b64_tr_b16 a[84:85], v246 offset:0
ds_read_b64_tr_b16 a[86:87], v246 offset:4096
v_mfma_f32_16x16x32_bf16 v[80:83], a[112:115], a[100:103], v[80:83]
s_add_u32 s44, s44, 4096
ds_read_b128 a[68:71], v244 offset:1024
v_mfma_f32_16x16x32_bf16 v[84:87], a[116:119], a[100:103], v[84:87]
ds_read_b64_tr_b16 a[88:89], v247 offset:0
ds_read_b64_tr_b16 a[90:91], v247 offset:4096
v_mfma_f32_16x16x32_bf16 v[88:91], a[120:123], a[100:103], v[88:91]
buffer_load_dwordx4 v241, s[8:11], s45 offen offset:1024 lds
v_mfma_f32_16x16x32_bf16 v[92:95], a[124:127], a[100:103], v[92:95]
ds_read_b128 a[72:75], v244 offset:4096
v_mfma_f32_16x16x32_bf16 v[96:99], a[112:115], a[104:107], v[96:99]
ds_read_b64_tr_b16 a[92:93], v248 offset:0
ds_read_b64_tr_b16 a[94:95], v248 offset:4096
v_mfma_f32_16x16x32_bf16 v[100:103], a[116:119], a[104:107], v[100:103]
ds_read_b128 a[76:79], v244 offset:5120
v_mfma_f32_16x16x32_bf16 v[104:107], a[120:123], a[104:107], v[104:107]
buffer_load_dwordx4 v242, s[8:11], s45 offen offset:2048 lds
v_mfma_f32_16x16x32_bf16 v[108:111], a[124:127], a[104:107], v[108:111]
s_cmp_eq_u32 s46, 0
s_cselect_b32 s53, s56, s57
v_mfma_f32_16x16x32_bf16 v[112:115], a[112:115], a[108:111], v[112:115]
v_mfma_f32_16x16x32_bf16 v[116:119], a[116:119], a[108:111], v[116:119]
v_mfma_f32_16x16x32_bf16 v[120:123], a[120:123], a[108:111], v[120:123]
buffer_load_dwordx4 v243, s[8:11], s45 offen offset:3072 lds
v_mfma_f32_16x16x32_bf16 v[124:127], a[124:127], a[108:111], v[124:127]
s_waitcnt lgkmcnt(0)
s_mov_b32 m0, s49
s_add_u32 s45, s45, 131072
v_mfma_f32_16x16x32_bf16 v[128:131], a[80:83], a[64:67], 0
ds_read_b64_tr_b16 a[112:113], v245 offset:8192
ds_read_b64_tr_b16 a[114:115], v245 offset:12288
v_mfma_f32_16x16x32_bf16 v[132:135], a[84:87], a[64:67], 0
ds_read_b128 a[96:99], v244 offset:2048
v_add_u32 v245, s53, v245
v_mfma_f32_16x16x32_bf16 v[136:139], a[88:91], a[64:67], 0
buffer_load_dwordx4 v7, s[4:7], s44 offen offset:0 sc1 lds
v_mfma_f32_16x16x32_bf16 v[140:143], a[92:95], a[64:67], 0
ds_read_b64_tr_b16 a[116:117], v246 offset:8192
ds_read_b64_tr_b16 a[118:119], v246 offset:12288
v_mfma_f32_16x16x32_bf16 v[144:147], a[80:83], a[68:71], 0
ds_read_b128 a[100:103], v244 offset:3072
v_add_u32 v246, s53, v246
v_mfma_f32_16x16x32_bf16 v[148:151], a[84:87], a[68:71], 0
ds_read_b64_tr_b16 a[120:121], v247 offset:8192
ds_read_b64_tr_b16 a[122:123], v247 offset:12288
v_mfma_f32_16x16x32_bf16 v[152:155], a[88:91], a[68:71], 0
buffer_load_dwordx4 v7, s[4:7], s44 offen offset:1024 sc1 lds
v_mfma_f32_16x16x32_bf16 v[156:159], a[92:95], a[68:71], 0
ds_read_b128 a[104:107], v244 offset:6144
v_add_u32 v247, s53, v247
v_mfma_f32_16x16x32_bf16 v[160:163], a[80:83], a[72:75], 0
ds_read_b64_tr_b16 a[124:125], v248 offset:8192
ds_read_b64_tr_b16 a[126:127], v248 offset:12288
v_mfma_f32_16x16x32_bf16 v[164:167], a[84:87], a[72:75], 0
ds_read_b128 a[108:111], v244 offset:7168
v_add_u32 v248, s53, v248
v_mfma_f32_16x16x32_bf16 v[168:171], a[88:91], a[72:75], 0
buffer_load_dwordx4 v7, s[4:7], s44 offen offset:2048 sc1 lds
v_mfma_f32_16x16x32_bf16 v[172:175], a[92:95], a[72:75], 0
s_add_u32 s49, s46, s91
v_add_u32 v244, s53, v244
v_mfma_f32_16x16x32_bf16 v[176:179], a[80:83], a[76:79], 0
s_add_u32 s46, s46, s53
v_mfma_f32_16x16x32_bf16 v[180:183], a[84:87], a[76:79], 0
v_mfma_f32_16x16x32_bf16 v[184:187], a[88:91], a[76:79], 0
buffer_load_dwordx4 v7, s[4:7], s44 offen offset:3072 sc1 lds
v_mfma_f32_16x16x32_bf16 v[188:191], a[92:95], a[76:79], 0
s_waitcnt vmcnt(24) lgkmcnt(0)
s_barrier
s_mov_b32 s32, 4096
v_mfma_f32_16x16x32_bf16 v[128:131], a[112:115], a[96:99], v[128:131]
ds_read_b64_tr_b16 a[80:81], v245 offset:0
ds_read_b64_tr_b16 a[82:83], v245 offset:4096
v_mfma_f32_16x16x32_bf16 v[132:135], a[116:119], a[96:99], v[132:135]
s_add_u32 m0, s49, 16384
ds_read_b128 a[64:67], v244 offset:0
v_cvt_pk_bf16_f32 v64, v64, v65
v_mfma_f32_16x16x32_bf16 v[136:139], a[120:123], a[96:99], v[136:139]
buffer_load_dwordx4 v240, s[8:11], s45 offen offset:0 lds
v_mfma_f32_16x16x32_bf16 v[140:143], a[124:127], a[96:99], v[140:143]
ds_read_b64_tr_b16 a[84:85], v246 offset:0
ds_read_b64_tr_b16 a[86:87], v246 offset:4096
v_mfma_f32_16x16x32_bf16 v[144:147], a[112:115], a[100:103], v[144:147]
s_add_u32 s44, s44, 4096
ds_read_b128 a[68:71], v244 offset:1024
v_cvt_pk_bf16_f32 v65, v66, v67
v_mfma_f32_16x16x32_bf16 v[148:151], a[116:119], a[100:103], v[148:151]
ds_read_b64_tr_b16 a[88:89], v247 offset:0
ds_read_b64_tr_b16 a[90:91], v247 offset:4096
v_cvt_pk_bf16_f32 v66, v68, v69
v_mfma_f32_16x16x32_bf16 v[152:155], a[120:123], a[100:103], v[152:155]
buffer_load_dwordx4 v241, s[8:11], s45 offen offset:1024 lds
v_cvt_pk_bf16_f32 v67, v70, v71
v_mfma_f32_16x16x32_bf16 v[156:159], a[124:127], a[100:103], v[156:159]
buffer_store_dwordx4 v[64:67], v7, s[12:15], 0 offen offset:0 sc1
ds_read_b128 a[72:75], v244 offset:4096
v_mfma_f32_16x16x32_bf16 v[160:163], a[112:115], a[104:107], v[160:163]
ds_read_b64_tr_b16 a[92:93], v248 offset:0
ds_read_b64_tr_b16 a[94:95], v248 offset:4096
v_mfma_f32_16x16x32_bf16 v[164:167], a[116:119], a[104:107], v[164:167]
ds_read_b128 a[76:79], v244 offset:5120
v_mfma_f32_16x16x32_bf16 v[168:171], a[120:123], a[104:107], v[168:171]
buffer_load_dwordx4 v242, s[8:11], s45 offen offset:2048 lds
v_mfma_f32_16x16x32_bf16 v[172:175], a[124:127], a[104:107], v[172:175]
s_cmp_eq_u32 s46, 0
s_cselect_b32 s53, s56, s57
v_mfma_f32_16x16x32_bf16 v[176:179], a[112:115], a[108:111], v[176:179]
v_mfma_f32_16x16x32_bf16 v[180:183], a[116:119], a[108:111], v[180:183]
v_mfma_f32_16x16x32_bf16 v[184:187], a[120:123], a[108:111], v[184:187]
buffer_load_dwordx4 v243, s[8:11], s45 offen offset:3072 lds
v_mfma_f32_16x16x32_bf16 v[188:191], a[124:127], a[108:111], v[188:191]
s_waitcnt lgkmcnt(0)
s_mov_b32 m0, s49
s_add_u32 s45, s45, 131072
v_mfma_f32_16x16x32_bf16 v[128:131], a[80:83], a[64:67], v[128:131]
ds_read_b64_tr_b16 a[112:113], v245 offset:8192
ds_read_b64_tr_b16 a[114:115], v245 offset:12288
v_mfma_f32_16x16x32_bf16 v[132:135], a[84:87], a[64:67], v[132:135]
ds_read_b128 a[96:99], v244 offset:2048
v_add_u32 v245, s53, v245
v_cvt_pk_bf16_f32 v72, v72, v73
v_mfma_f32_16x16x32_bf16 v[136:139], a[88:91], a[64:67], v[136:139]
buffer_load_dwordx4 v7, s[4:7], s44 offen offset:0 sc1 lds
v_mfma_f32_16x16x32_bf16 v[140:143], a[92:95], a[64:67], v[140:143]
ds_read_b64_tr_b16 a[116:117], v246 offset:8192
ds_read_b64_tr_b16 a[118:119], v246 offset:12288
v_mfma_f32_16x16x32_bf16 v[144:147], a[80:83], a[68:71], v[144:147]
ds_read_b128 a[100:103], v244 offset:3072
v_add_u32 v246, s53, v246
v_cvt_pk_bf16_f32 v73, v74, v75
v_mfma_f32_16x16x32_bf16 v[148:151], a[84:87], a[68:71], v[148:151]
ds_read_b64_tr_b16 a[120:121], v247 offset:8192
ds_read_b64_tr_b16 a[122:123], v247 offset:12288
v_cvt_pk_bf16_f32 v74, v76, v77
v_mfma_f32_16x16x32_bf16 v[152:155], a[88:91], a[68:71], v[152:155]
buffer_load_dwordx4 v7, s[4:7], s44 offen offset:1024 sc1 lds
v_cvt_pk_bf16_f32 v75, v78, v79
v_mfma_f32_16x16x32_bf16 v[156:159], a[92:95], a[68:71], v[156:159]
buffer_store_dwordx4 v[72:75], v7, s[12:15], 0 offen offset:1024 sc1
ds_read_b128 a[104:107], v244 offset:6144
v_add_u32 v247, s53, v247
v_mfma_f32_16x16x32_bf16 v[160:163], a[80:83], a[72:75], v[160:163]
ds_read_b64_tr_b16 a[124:125], v248 offset:8192
ds_read_b64_tr_b16 a[126:127], v248 offset:12288
v_mfma_f32_16x16x32_bf16 v[164:167], a[84:87], a[72:75], v[164:167]
ds_read_b128 a[108:111], v244 offset:7168
v_add_u32 v248, s53, v248
v_mfma_f32_16x16x32_bf16 v[168:171], a[88:91], a[72:75], v[168:171]
buffer_load_dwordx4 v7, s[4:7], s44 offen offset:2048 sc1 lds
v_mfma_f32_16x16x32_bf16 v[172:175], a[92:95], a[72:75], v[172:175]
s_add_u32 s49, s46, s91
v_add_u32 v244, s53, v244
v_mfma_f32_16x16x32_bf16 v[176:179], a[80:83], a[76:79], v[176:179]
s_add_u32 s46, s46, s53
v_mfma_f32_16x16x32_bf16 v[180:183], a[84:87], a[76:79], v[180:183]
v_mfma_f32_16x16x32_bf16 v[184:187], a[88:91], a[76:79], v[184:187]
buffer_load_dwordx4 v7, s[4:7], s44 offen offset:3072 sc1 lds
v_mfma_f32_16x16x32_bf16 v[188:191], a[92:95], a[76:79], v[188:191]
s_waitcnt vmcnt(26) lgkmcnt(0)
s_barrier
v_mfma_f32_16x16x32_bf16 v[128:131], a[112:115], a[96:99], v[128:131]
ds_read_b64_tr_b16 a[80:81], v245 offset:0
ds_read_b64_tr_b16 a[82:83], v245 offset:4096
v_mfma_f32_16x16x32_bf16 v[132:135], a[116:119], a[96:99], v[132:135]
s_add_u32 m0, s49, 16384
ds_read_b128 a[64:67], v244 offset:0
v_cvt_pk_bf16_f32 v80, v80, v81
v_mfma_f32_16x16x32_bf16 v[136:139], a[120:123], a[96:99], v[136:139]
buffer_load_dwordx4 v240, s[8:11], s45 offen offset:0 lds
v_mfma_f32_16x16x32_bf16 v[140:143], a[124:127], a[96:99], v[140:143]
ds_read_b64_tr_b16 a[84:85], v246 offset:0
ds_read_b64_tr_b16 a[86:87], v246 offset:4096
v_mfma_f32_16x16x32_bf16 v[144:147], a[112:115], a[100:103], v[144:147]
s_add_u32 s44, s44, 4096
ds_read_b128 a[68:71], v244 offset:1024
v_cvt_pk_bf16_f32 v81, v82, v83
v_mfma_f32_16x16x32_bf16 v[148:151], a[116:119], a[100:103], v[148:151]
ds_read_b64_tr_b16 a[88:89], v247 offset:0
ds_read_b64_tr_b16 a[90:91], v247 offset:4096
v_cvt_pk_bf16_f32 v82, v84, v85
v_mfma_f32_16x16x32_bf16 v[152:155], a[120:123], a[100:103], v[152:155]
buffer_load_dwordx4 v241, s[8:11], s45 offen offset:1024 lds
v_cvt_pk_bf16_f32 v83, v86, v87
v_mfma_f32_16x16x32_bf16 v[156:159], a[124:127], a[100:103], v[156:159]
buffer_store_dwordx4 v[80:83], v7, s[12:15], 0 offen offset:2048 sc1
ds_read_b128 a[72:75], v244 offset:4096
v_mfma_f32_16x16x32_bf16 v[160:163], a[112:115], a[104:107], v[160:163]
ds_read_b64_tr_b16 a[92:93], v248 offset:0
ds_read_b64_tr_b16 a[94:95], v248 offset:4096
v_mfma_f32_16x16x32_bf16 v[164:167], a[116:119], a[104:107], v[164:167]
ds_read_b128 a[76:79], v244 offset:5120
v_mfma_f32_16x16x32_bf16 v[168:171], a[120:123], a[104:107], v[168:171]
buffer_load_dwordx4 v242, s[8:11], s45 offen offset:2048 lds
v_mfma_f32_16x16x32_bf16 v[172:175], a[124:127], a[104:107], v[172:175]
s_cmp_eq_u32 s46, 0
s_cselect_b32 s53, s56, s57
v_mfma_f32_16x16x32_bf16 v[176:179], a[112:115], a[108:111], v[176:179]
v_mfma_f32_16x16x32_bf16 v[180:183], a[116:119], a[108:111], v[180:183]
v_mfma_f32_16x16x32_bf16 v[184:187], a[120:123], a[108:111], v[184:187]
buffer_load_dwordx4 v243, s[8:11], s45 offen offset:3072 lds
v_mfma_f32_16x16x32_bf16 v[188:191], a[124:127], a[108:111], v[188:191]
s_waitcnt lgkmcnt(0)
s_mov_b32 m0, s49
s_add_u32 s45, s45, 131072
v_mfma_f32_16x16x32_bf16 v[128:131], a[80:83], a[64:67], v[128:131]
ds_read_b64_tr_b16 a[112:113], v245 offset:8192
ds_read_b64_tr_b16 a[114:115], v245 offset:12288
v_mfma_f32_16x16x32_bf16 v[132:135], a[84:87], a[64:67], v[132:135]
ds_read_b128 a[96:99], v244 offset:2048
v_add_u32 v245, s53, v245
v_cvt_pk_bf16_f32 v88, v88, v89
v_mfma_f32_16x16x32_bf16 v[136:139], a[88:91], a[64:67], v[136:139]
buffer_load_dwordx4 v7, s[4:7], s44 offen offset:0 sc1 lds
v_mfma_f32_16x16x32_bf16 v[140:143], a[92:95], a[64:67], v[140:143]
ds_read_b64_tr_b16 a[116:117], v246 offset:8192
ds_read_b64_tr_b16 a[118:119], v246 offset:12288
v_mfma_f32_16x16x32_bf16 v[144:147], a[80:83], a[68:71], v[144:147]
ds_read_b128 a[100:103], v244 offset:3072
v_add_u32 v246, s53, v246
v_cvt_pk_bf16_f32 v89, v90, v91
v_mfma_f32_16x16x32_bf16 v[148:151], a[84:87], a[68:71], v[148:151]
ds_read_b64_tr_b16 a[120:121], v247 offset:8192
ds_read_b64_tr_b16 a[122:123], v247 offset:12288
v_cvt_pk_bf16_f32 v90, v92, v93
v_mfma_f32_16x16x32_bf16 v[152:155], a[88:91], a[68:71], v[152:155]
buffer_load_dwordx4 v7, s[4:7], s44 offen offset:1024 sc1 lds
v_cvt_pk_bf16_f32 v91, v94, v95
v_mfma_f32_16x16x32_bf16 v[156:159], a[92:95], a[68:71], v[156:159]
buffer_store_dwordx4 v[88:91], v7, s[12:15], 0 offen offset:3072 sc1
ds_read_b128 a[104:107], v244 offset:6144
v_add_u32 v247, s53, v247
v_mfma_f32_16x16x32_bf16 v[160:163], a[80:83], a[72:75], v[160:163]
ds_read_b64_tr_b16 a[124:125], v248 offset:8192
ds_read_b64_tr_b16 a[126:127], v248 offset:12288
v_mfma_f32_16x16x32_bf16 v[164:167], a[84:87], a[72:75], v[164:167]
ds_read_b128 a[108:111], v244 offset:7168
v_add_u32 v248, s53, v248
v_mfma_f32_16x16x32_bf16 v[168:171], a[88:91], a[72:75], v[168:171]
buffer_load_dwordx4 v7, s[4:7], s44 offen offset:2048 sc1 lds
v_mfma_f32_16x16x32_bf16 v[172:175], a[92:95], a[72:75], v[172:175]
s_add_u32 s49, s46, s91
v_add_u32 v244, s53, v244
v_mfma_f32_16x16x32_bf16 v[176:179], a[80:83], a[76:79], v[176:179]
s_add_u32 s46, s46, s53
v_mfma_f32_16x16x32_bf16 v[180:183], a[84:87], a[76:79], v[180:183]
v_mfma_f32_16x16x32_bf16 v[184:187], a[88:91], a[76:79], v[184:187]
buffer_load_dwordx4 v7, s[4:7], s44 offen offset:3072 sc1 lds
v_mfma_f32_16x16x32_bf16 v[188:191], a[92:95], a[76:79], v[188:191]
s_waitcnt vmcnt(28) lgkmcnt(0)
s_barrier
v_mfma_f32_16x16x32_bf16 v[128:131], a[112:115], a[96:99], v[128:131]
ds_read_b64_tr_b16 a[80:81], v245 offset:0
ds_read_b64_tr_b16 a[82:83], v245 offset:4096
v_mfma_f32_16x16x32_bf16 v[132:135], a[116:119], a[96:99], v[132:135]
s_add_u32 m0, s49, 16384
ds_read_b128 a[64:67], v244 offset:0
v_cvt_pk_bf16_f32 v96, v96, v97
v_mfma_f32_16x16x32_bf16 v[136:139], a[120:123], a[96:99], v[136:139]
buffer_load_dwordx4 v240, s[8:11], s45 offen offset:0 lds
v_mfma_f32_16x16x32_bf16 v[140:143], a[124:127], a[96:99], v[140:143]
ds_read_b64_tr_b16 a[84:85], v246 offset:0
ds_read_b64_tr_b16 a[86:87], v246 offset:4096
v_mfma_f32_16x16x32_bf16 v[144:147], a[112:115], a[100:103], v[144:147]
s_add_u32 s44, s44, 4096
ds_read_b128 a[68:71], v244 offset:1024
v_cvt_pk_bf16_f32 v97, v98, v99
v_mfma_f32_16x16x32_bf16 v[148:151], a[116:119], a[100:103], v[148:151]
ds_read_b64_tr_b16 a[88:89], v247 offset:0
ds_read_b64_tr_b16 a[90:91], v247 offset:4096
v_cvt_pk_bf16_f32 v98, v100, v101
v_mfma_f32_16x16x32_bf16 v[152:155], a[120:123], a[100:103], v[152:155]
buffer_load_dwordx4 v241, s[8:11], s45 offen offset:1024 lds
v_cvt_pk_bf16_f32 v99, v102, v103
v_mfma_f32_16x16x32_bf16 v[156:159], a[124:127], a[100:103], v[156:159]
buffer_store_dwordx4 v[96:99], v7, s[12:15], s32 offen offset:0 sc1
.Lpad22:
s_cbranch_execz .Ltch9
ds_read_b128 a[72:75], v244 offset:4096
v_mfma_f32_16x16x32_bf16 v[160:163], a[112:115], a[104:107], v[160:163]
ds_read_b64_tr_b16 a[92:93], v248 offset:0
ds_read_b64_tr_b16 a[94:95], v248 offset:4096
v_mfma_f32_16x16x32_bf16 v[164:167], a[116:119], a[104:107], v[164:167]
ds_read_b128 a[76:79], v244 offset:5120
v_mfma_f32_16x16x32_bf16 v[168:171], a[120:123], a[104:107], v[168:171]
buffer_load_dwordx4 v242, s[8:11], s45 offen offset:2048 lds
v_mfma_f32_16x16x32_bf16 v[172:175], a[124:127], a[104:107], v[172:175]
s_cmp_eq_u32 s46, 0
s_cselect_b32 s53, s56, s57
v_mfma_f32_16x16x32_bf16 v[176:179], a[112:115], a[108:111], v[176:179]
v_mfma_f32_16x16x32_bf16 v[180:183], a[116:119], a[108:111], v[180:183]
v_mfma_f32_16x16x32_bf16 v[184:187], a[120:123], a[108:111], v[184:187]
buffer_load_dwordx4 v243, s[8:11], s45 offen offset:3072 lds
v_mfma_f32_16x16x32_bf16 v[188:191], a[124:127], a[108:111], v[188:191]
s_waitcnt lgkmcnt(0)
s_mov_b32 m0, s49
s_add_u32 s45, s45, 131072
v_mfma_f32_16x16x32_bf16 v[128:131], a[80:83], a[64:67], v[128:131]
ds_read_b64_tr_b16 a[112:113], v245 offset:8192
ds_read_b64_tr_b16 a[114:115], v245 offset:12288
v_mfma_f32_16x16x32_bf16 v[132:135], a[84:87], a[64:67], v[132:135]
ds_read_b128 a[96:99], v244 offset:2048
v_add_u32 v245, s53, v245
v_cvt_pk_bf16_f32 v104, v104, v105
v_mfma_f32_16x16x32_bf16 v[136:139], a[88:91], a[64:67], v[136:139]
buffer_load_dwordx4 v7, s[4:7], s44 offen offset:0 sc1 lds
v_mfma_f32_16x16x32_bf16 v[140:143], a[92:95], a[64:67], v[140:143]
ds_read_b64_tr_b16 a[116:117], v246 offset:8192
ds_read_b64_tr_b16 a[118:119], v246 offset:12288
v_mfma_f32_16x16x32_bf16 v[144:147], a[80:83], a[68:71], v[144:147]
ds_read_b128 a[100:103], v244 offset:3072
v_add_u32 v246, s53, v246
v_cvt_pk_bf16_f32 v105, v106, v107
v_mfma_f32_16x16x32_bf16 v[148:151], a[84:87], a[68:71], v[148:151]
ds_read_b64_tr_b16 a[120:121], v247 offset:8192
ds_read_b64_tr_b16 a[122:123], v247 offset:12288
v_cvt_pk_bf16_f32 v106, v108, v109
v_mfma_f32_16x16x32_bf16 v[152:155], a[88:91], a[68:71], v[152:155]
buffer_load_dwordx4 v7, s[4:7], s44 offen offset:1024 sc1 lds
v_cvt_pk_bf16_f32 v107, v110, v111
v_mfma_f32_16x16x32_bf16 v[156:159], a[92:95], a[68:71], v[156:159]
buffer_store_dwordx4 v[104:107], v7, s[12:15], s32 offen offset:1024 sc1
ds_read_b128 a[104:107], v244 offset:6144
v_add_u32 v247, s53, v247
v_mfma_f32_16x16x32_bf16 v[160:163], a[80:83], a[72:75], v[160:163]
ds_read_b64_tr_b16 a[124:125], v248 offset:8192
ds_read_b64_tr_b16 a[126:127], v248 offset:12288
v_mfma_f32_16x16x32_bf16 v[164:167], a[84:87], a[72:75], v[164:167]
ds_read_b128 a[108:111], v244 offset:7168
v_add_u32 v248, s53, v248
v_mfma_f32_16x16x32_bf16 v[168:171], a[88:91], a[72:75], v[168:171]
buffer_load_dwordx4 v7, s[4:7], s44 offen offset:2048 sc1 lds
v_mfma_f32_16x16x32_bf16 v[172:175], a[92:95], a[72:75], v[172:175]
s_add_u32 s49, s46, s91
v_add_u32 v244, s53, v244
v_mfma_f32_16x16x32_bf16 v[176:179], a[80:83], a[76:79], v[176:179]
s_add_u32 s46, s46, s53
v_mfma_f32_16x16x32_bf16 v[180:183], a[84:87], a[76:79], v[180:183]
v_mfma_f32_16x16x32_bf16 v[184:187], a[88:91], a[76:79], v[184:187]
buffer_load_dwordx4 v7, s[4:7], s44 offen offset:3072 sc1 lds
v_mfma_f32_16x16x32_bf16 v[188:191], a[92:95], a[76:79], v[188:191]
s_waitcnt vmcnt(30) lgkmcnt(0)
s_barrier
v_mfma_f32_16x16x32_bf16 v[128:131], a[112:115], a[96:99], v[128:131]
ds_read_b64_tr_b16 a[80:81], v245 offset:0
ds_read_b64_tr_b16 a[82:83], v245 offset:4096
v_mfma_f32_16x16x32_bf16 v[132:135], a[116:119], a[96:99], v[132:135]
s_add_u32 m0, s49, 16384
ds_read_b128 a[64:67], v244 offset:0
v_cvt_pk_bf16_f32 v112, v112, v113
v_mfma_f32_16x16x32_bf16 v[136:139], a[120:123], a[96:99], v[136:139]
buffer_load_dwordx4 v240, s[8:11], s45 offen offset:0 lds
v_mfma_f32_16x16x32_bf16 v[140:143], a[124:127], a[96:99], v[140:143]
ds_read_b64_tr_b16 a[84:85], v246 offset:0
ds_read_b64_tr_b16 a[86:87], v246 offset:4096
v_mfma_f32_16x16x32_bf16 v[144:147], a[112:115], a[100:103], v[144:147]
s_add_u32 s44, s44, 4096
ds_read_b128 a[68:71], v244 offset:1024
v_cvt_pk_bf16_f32 v113, v114, v115
v_mfma_f32_16x16x32_bf16 v[148:151], a[116:119], a[100:103], v[148:151]
ds_read_b64_tr_b16 a[88:89], v247 offset:0
ds_read_b64_tr_b16 a[90:91], v247 offset:4096
v_cvt_pk_bf16_f32 v114, v116, v117
v_mfma_f32_16x16x32_bf16 v[152:155], a[120:123], a[100:103], v[152:155]
buffer_load_dwordx4 v241, s[8:11], s45 offen offset:1024 lds
v_cvt_pk_bf16_f32 v115, v118, v119
v_mfma_f32_16x16x32_bf16 v[156:159], a[124:127], a[100:103], v[156:159]
buffer_store_dwordx4 v[112:115], v7, s[12:15], s32 offen offset:2048 sc1
ds_read_b128 a[72:75], v244 offset:4096
v_mfma_f32_16x16x32_bf16 v[160:163], a[112:115], a[104:107], v[160:163]
ds_read_b64_tr_b16 a[92:93], v248 offset:0
ds_read_b64_tr_b16 a[94:95], v248 offset:4096
v_mfma_f32_16x16x32_bf16 v[164:167], a[116:119], a[104:107], v[164:167]
ds_read_b128 a[76:79], v244 offset:5120
v_mfma_f32_16x16x32_bf16 v[168:171], a[120:123], a[104:107], v[168:171]
buffer_load_dwordx4 v242, s[8:11], s45 offen offset:2048 lds
v_mfma_f32_16x16x32_bf16 v[172:175], a[124:127], a[104:107], v[172:175]
s_cmp_eq_u32 s46, 0
s_cselect_b32 s53, s56, s57
v_mfma_f32_16x16x32_bf16 v[176:179], a[112:115], a[108:111], v[176:179]
v_mfma_f32_16x16x32_bf16 v[180:183], a[116:119], a[108:111], v[180:183]
v_mfma_f32_16x16x32_bf16 v[184:187], a[120:123], a[108:111], v[184:187]
buffer_load_dwordx4 v243, s[8:11], s45 offen offset:3072 lds
v_mfma_f32_16x16x32_bf16 v[188:191], a[124:127], a[108:111], v[188:191]
s_waitcnt lgkmcnt(0)
s_mov_b32 m0, s49
s_add_u32 s45, s45, 131072
v_mfma_f32_16x16x32_bf16 v[128:131], a[80:83], a[64:67], v[128:131]
ds_read_b64_tr_b16 a[112:113], v245 offset:8192
ds_read_b64_tr_b16 a[114:115], v245 offset:12288
v_mfma_f32_16x16x32_bf16 v[132:135], a[84:87], a[64:67], v[132:135]
ds_read_b128 a[96:99], v244 offset:2048
v_add_u32 v245, s53, v245
v_cvt_pk_bf16_f32 v120, v120, v121
v_mfma_f32_16x16x32_bf16 v[136:139], a[88:91], a[64:67], v[136:139]
buffer_load_dwordx4 v7, s[4:7], s44 offen offset:0 sc1 lds
v_mfma_f32_16x16x32_bf16 v[140:143], a[92:95], a[64:67], v[140:143]
ds_read_b64_tr_b16 a[116:117], v246 offset:8192
ds_read_b64_tr_b16 a[118:119], v246 offset:12288
v_mfma_f32_16x16x32_bf16 v[144:147], a[80:83], a[68:71], v[144:147]
ds_read_b128 a[100:103], v244 offset:3072
v_add_u32 v246, s53, v246
v_cvt_pk_bf16_f32 v121, v122, v123
v_mfma_f32_16x16x32_bf16 v[148:151], a[84:87], a[68:71], v[148:151]
ds_read_b64_tr_b16 a[120:121], v247 offset:8192
ds_read_b64_tr_b16 a[122:123], v247 offset:12288
v_cvt_pk_bf16_f32 v122, v124, v125
v_mfma_f32_16x16x32_bf16 v[152:155], a[88:91], a[68:71], v[152:155]
buffer_load_dwordx4 v7, s[4:7], s44 offen offset:1024 sc1 lds
v_cvt_pk_bf16_f32 v123, v126, v127
v_mfma_f32_16x16x32_bf16 v[156:159], a[92:95], a[68:71], v[156:159]
buffer_store_dwordx4 v[120:123], v7, s[12:15], s32 offen offset:3072 sc1
ds_read_b128 a[104:107], v244 offset:6144
v_add_u32 v247, s53, v247
v_mfma_f32_16x16x32_bf16 v[160:163], a[80:83], a[72:75], v[160:163]
ds_read_b64_tr_b16 a[124:125], v248 offset:8192
ds_read_b64_tr_b16 a[126:127], v248 offset:12288
v_mfma_f32_16x16x32_bf16 v[164:167], a[84:87], a[72:75], v[164:167]
ds_read_b128 a[108:111], v244 offset:7168
v_add_u32 v248, s53, v248
v_mfma_f32_16x16x32_bf16 v[168:171], a[88:91], a[72:75], v[168:171]
buffer_load_dwordx4 v7, s[4:7], s44 offen offset:2048 sc1 lds
v_mfma_f32_16x16x32_bf16 v[172:175], a[92:95], a[72:75], v[172:175]
s_add_u32 s49, s46, s91
v_add_u32 v244, s53, v244
v_mfma_f32_16x16x32_bf16 v[176:179], a[80:83], a[76:79], v[176:179]
s_add_u32 s46, s46, s53
v_mfma_f32_16x16x32_bf16 v[180:183], a[84:87], a[76:79], v[180:183]
v_mfma_f32_16x16x32_bf16 v[184:187], a[88:91], a[76:79], v[184:187]
buffer_load_dwordx4 v7, s[4:7], s44 offen offset:3072 sc1 lds
v_mfma_f32_16x16x32_bf16 v[188:191], a[92:95], a[76:79], v[188:191]
s_waitcnt vmcnt(30) lgkmcnt(0)
s_barrier
v_mfma_f32_16x16x32_bf16 v[128:131], a[112:115], a[96:99], v[128:131]
ds_read_b64_tr_b16 a[80:81], v245 offset:0
ds_read_b64_tr_b16 a[82:83], v245 offset:4096
v_mfma_f32_16x16x32_bf16 v[132:135], a[116:119], a[96:99], v[132:135]
s_add_u32 m0, s49, 16384
ds_read_b128 a[64:67], v244 offset:0
v_mfma_f32_16x16x32_bf16 v[136:139], a[120:123], a[96:99], v[136:139]
buffer_load_dwordx4 v240, s[8:11], s45 offen offset:0 lds
v_mfma_f32_16x16x32_bf16 v[140:143], a[124:127], a[96:99], v[140:143]
ds_read_b64_tr_b16 a[84:85], v246 offset:0
ds_read_b64_tr_b16 a[86:87], v246 offset:4096
v_mfma_f32_16x16x32_bf16 v[144:147], a[112:115], a[100:103], v[144:147]
s_add_u32 s44, s44, 4096
ds_read_b128 a[68:71], v244 offset:1024
v_mfma_f32_16x16x32_bf16 v[148:151], a[116:119], a[100:103], v[148:151]
ds_read_b64_tr_b16 a[88:89], v247 offset:0
ds_read_b64_tr_b16 a[90:91], v247 offset:4096
v_mfma_f32_16x16x32_bf16 v[152:155], a[120:123], a[100:103], v[152:155]
buffer_load_dwordx4 v241, s[8:11], s45 offen offset:1024 lds
v_mfma_f32_16x16x32_bf16 v[156:159], a[124:127], a[100:103], v[156:159]
ds_read_b128 a[72:75], v244 offset:4096
v_mfma_f32_16x16x32_bf16 v[160:163], a[112:115], a[104:107], v[160:163]
ds_read_b64_tr_b16 a[92:93], v248 offset:0
ds_read_b64_tr_b16 a[94:95], v248 offset:4096
v_mfma_f32_16x16x32_bf16 v[164:167], a[116:119], a[104:107], v[164:167]
ds_read_b128 a[76:79], v244 offset:5120
v_mfma_f32_16x16x32_bf16 v[168:171], a[120:123], a[104:107], v[168:171]
buffer_load_dwordx4 v242, s[8:11], s45 offen offset:2048 lds
v_mfma_f32_16x16x32_bf16 v[172:175], a[124:127], a[104:107], v[172:175]
s_cmp_eq_u32 s46, 0
s_cselect_b32 s53, s56, s57
v_mfma_f32_16x16x32_bf16 v[176:179], a[112:115], a[108:111], v[176:179]
v_mfma_f32_16x16x32_bf16 v[180:183], a[116:119], a[108:111], v[180:183]
v_mfma_f32_16x16x32_bf16 v[184:187], a[120:123], a[108:111], v[184:187]
buffer_load_dwordx4 v243, s[8:11], s45 offen offset:3072 lds
v_mfma_f32_16x16x32_bf16 v[188:191], a[124:127], a[108:111], v[188:191]
s_waitcnt lgkmcnt(0)
s_mov_b32 m0, s49
s_add_u32 s45, s45, 131072
v_mfma_f32_16x16x32_bf16 v[128:131], a[80:83], a[64:67], v[128:131]
ds_read_b64_tr_b16 a[112:113], v245 offset:8192
ds_read_b64_tr_b16 a[114:115], v245 offset:12288
v_mfma_f32_16x16x32_bf16 v[132:135], a[84:87], a[64:67], v[132:135]
ds_read_b128 a[96:99], v244 offset:2048
v_add_u32 v245, s53, v245
v_mfma_f32_16x16x32_bf16 v[136:139], a[88:91], a[64:67], v[136:139]
buffer_load_dwordx4 v7, s[4:7], s44 offen offset:0 sc1 lds
v_mfma_f32_16x16x32_bf16 v[140:143], a[92:95], a[64:67], v[140:143]
ds_read_b64_tr_b16 a[116:117], v246 offset:8192
ds_read_b64_tr_b16 a[118:119], v246 offset:12288
v_mfma_f32_16x16x32_bf16 v[144:147], a[80:83], a[68:71], v[144:147]
ds_read_b128 a[100:103], v244 offset:3072
v_add_u32 v246, s53, v246
v_mfma_f32_16x16x32_bf16 v[148:151], a[84:87], a[68:71], v[148:151]
ds_read_b64_tr_b16 a[120:121], v247 offset:8192
ds_read_b64_tr_b16 a[122:123], v247 offset:12288
v_mfma_f32_16x16x32_bf16 v[152:155], a[88:91], a[68:71], v[152:155]
buffer_load_dwordx4 v7, s[4:7], s44 offen offset:1024 sc1 lds
v_mfma_f32_16x16x32_bf16 v[156:159], a[92:95], a[68:71], v[156:159]
ds_read_b128 a[104:107], v244 offset:6144
v_add_u32 v247, s53, v247
v_mfma_f32_16x16x32_bf16 v[160:163], a[80:83], a[72:75], v[160:163]
ds_read_b64_tr_b16 a[124:125], v248 offset:8192
ds_read_b64_tr_b16 a[126:127], v248 offset:12288
v_mfma_f32_16x16x32_bf16 v[164:167], a[84:87], a[72:75], v[164:167]
ds_read_b128 a[108:111], v244 offset:7168
v_add_u32 v248, s53, v248
v_mfma_f32_16x16x32_bf16 v[168:171], a[88:91], a[72:75], v[168:171]
buffer_load_dwordx4 v7, s[4:7], s44 offen offset:2048 sc1 lds
v_mfma_f32_16x16x32_bf16 v[172:175], a[92:95], a[72:75], v[172:175]
s_add_u32 s49, s46, s91
v_add_u32 v244, s53, v244
v_mfma_f32_16x16x32_bf16 v[176:179], a[80:83], a[76:79], v[176:179]
s_add_u32 s46, s46, s53
v_mfma_f32_16x16x32_bf16 v[180:183], a[84:87], a[76:79], v[180:183]
v_mfma_f32_16x16x32_bf16 v[184:187], a[88:91], a[76:79], v[184:187]
buffer_load_dwordx4 v7, s[4:7], s44 offen offset:3072 sc1 lds
v_mfma_f32_16x16x32_bf16 v[188:191], a[92:95], a[76:79], v[188:191]
s_waitcnt vmcnt(28) lgkmcnt(0)
s_barrier
v_mfma_f32_16x16x32_bf16 v[128:131], a[112:115], a[96:99], v[128:131]
ds_read_b64_tr_b16 a[80:81], v245 offset:0
ds_read_b64_tr_b16 a[82:83], v245 offset:4096
v_mfma_f32_16x16x32_bf16 v[132:135], a[116:119], a[96:99], v[132:135]
s_add_u32 m0, s49, 16384
ds_read_b128 a[64:67], v244 offset:0
v_mfma_f32_16x16x32_bf16 v[136:139], a[120:123], a[96:99], v[136:139]
buffer_load_dwordx4 v240, s[8:11], s45 offen offset:0 lds
v_mfma_f32_16x16x32_bf16 v[140:143], a[124:127], a[96:99], v[140:143]
ds_read_b64_tr_b16 a[84:85], v246 offset:0
ds_read_b64_tr_b16 a[86:87], v246 offset:4096
v_mfma_f32_16x16x32_bf16 v[144:147], a[112:115], a[100:103], v[144:147]
s_add_u32 s44, s44, 4096
ds_read_b128 a[68:71], v244 offset:1024
v_mfma_f32_16x16x32_bf16 v[148:151], a[116:119], a[100:103], v[148:151]
ds_read_b64_tr_b16 a[88:89], v247 offset:0
ds_read_b64_tr_b16 a[90:91], v247 offset:4096
v_mfma_f32_16x16x32_bf16 v[152:155], a[120:123], a[100:103], v[152:155]
buffer_load_dwordx4 v241, s[8:11], s45 offen offset:1024 lds
v_mfma_f32_16x16x32_bf16 v[156:159], a[124:127], a[100:103], v[156:159]
ds_read_b128 a[72:75], v244 offset:4096
v_mfma_f32_16x16x32_bf16 v[160:163], a[112:115], a[104:107], v[160:163]
ds_read_b64_tr_b16 a[92:93], v248 offset:0
ds_read_b64_tr_b16 a[94:95], v248 offset:4096
v_mfma_f32_16x16x32_bf16 v[164:167], a[116:119], a[104:107], v[164:167]
ds_read_b128 a[76:79], v244 offset:5120
v_mfma_f32_16x16x32_bf16 v[168:171], a[120:123], a[104:107], v[168:171]
buffer_load_dwordx4 v242, s[8:11], s45 offen offset:2048 lds
v_mfma_f32_16x16x32_bf16 v[172:175], a[124:127], a[104:107], v[172:175]
s_cmp_eq_u32 s46, 0
s_cselect_b32 s53, s56, s57
v_mfma_f32_16x16x32_bf16 v[176:179], a[112:115], a[108:111], v[176:179]
v_mfma_f32_16x16x32_bf16 v[180:183], a[116:119], a[108:111], v[180:183]
v_mfma_f32_16x16x32_bf16 v[184:187], a[120:123], a[108:111], v[184:187]
buffer_load_dwordx4 v243, s[8:11], s45 offen offset:3072 lds
v_mfma_f32_16x16x32_bf16 v[188:191], a[124:127], a[108:111], v[188:191]
s_waitcnt lgkmcnt(0)
s_mov_b32 m0, s49
s_add_u32 s45, s45, 131072
v_mfma_f32_16x16x32_bf16 v[128:131], a[80:83], a[64:67], v[128:131]
ds_read_b64_tr_b16 a[112:113], v245 offset:8192
ds_read_b64_tr_b16 a[114:115], v245 offset:12288
v_mfma_f32_16x16x32_bf16 v[132:135], a[84:87], a[64:67], v[132:135]
ds_read_b128 a[96:99], v244 offset:2048
v_add_u32 v245, s53, v245
v_mfma_f32_16x16x32_bf16 v[136:139], a[88:91], a[64:67], v[136:139]
buffer_load_dwordx4 v7, s[4:7], s44 offen offset:0 sc1 lds
v_mfma_f32_16x16x32_bf16 v[140:143], a[92:95], a[64:67], v[140:143]
ds_read_b64_tr_b16 a[116:117], v246 offset:8192
ds_read_b64_tr_b16 a[118:119], v246 offset:12288
v_mfma_f32_16x16x32_bf16 v[144:147], a[80:83], a[68:71], v[144:147]
ds_read_b128 a[100:103], v244 offset:3072
v_add_u32 v246, s53, v246
v_mfma_f32_16x16x32_bf16 v[148:151], a[84:87], a[68:71], v[148:151]
ds_read_b64_tr_b16 a[120:121], v247 offset:8192
ds_read_b64_tr_b16 a[122:123], v247 offset:12288
v_mfma_f32_16x16x32_bf16 v[152:155], a[88:91], a[68:71], v[152:155]
buffer_load_dwordx4 v7, s[4:7], s44 offen offset:1024 sc1 lds
v_mfma_f32_16x16x32_bf16 v[156:159], a[92:95], a[68:71], v[156:159]
ds_read_b128 a[104:107], v244 offset:6144
v_add_u32 v247, s53, v247
v_mfma_f32_16x16x32_bf16 v[160:163], a[80:83], a[72:75], v[160:163]
ds_read_b64_tr_b16 a[124:125], v248 offset:8192
ds_read_b64_tr_b16 a[126:127], v248 offset:12288
v_mfma_f32_16x16x32_bf16 v[164:167], a[84:87], a[72:75], v[164:167]
ds_read_b128 a[108:111], v244 offset:7168
v_add_u32 v248, s53, v248
v_mfma_f32_16x16x32_bf16 v[168:171], a[88:91], a[72:75], v[168:171]
buffer_load_dwordx4 v7, s[4:7], s44 offen offset:2048 sc1 lds
v_mfma_f32_16x16x32_bf16 v[172:175], a[92:95], a[72:75], v[172:175]
s_add_u32 s49, s46, s91
v_add_u32 v244, s53, v244
v_mfma_f32_16x16x32_bf16 v[176:179], a[80:83], a[76:79], v[176:179]
s_add_u32 s46, s46, s53
v_mfma_f32_16x16x32_bf16 v[180:183], a[84:87], a[76:79], v[180:183]
v_mfma_f32_16x16x32_bf16 v[184:187], a[88:91], a[76:79], v[184:187]
buffer_load_dwordx4 v7, s[4:7], s44 offen offset:3072 sc1 lds
v_mfma_f32_16x16x32_bf16 v[188:191], a[92:95], a[76:79], v[188:191]
s_waitcnt vmcnt(18)
s_waitcnt vmcnt(26) lgkmcnt(0)
s_barrier
v_mfma_f32_16x16x32_bf16 v[128:131], a[112:115], a[96:99], v[128:131]
ds_read_b64_tr_b16 a[80:81], v245 offset:0
ds_read_b64_tr_b16 a[82:83], v245 offset:4096
v_mfma_f32_16x16x32_bf16 v[132:135], a[116:119], a[96:99], v[132:135]
s_add_u32 m0, s49, 16384
ds_read_b128 a[64:67], v244 offset:0
v_mfma_f32_16x16x32_bf16 v[136:139], a[120:123], a[96:99], v[136:139]
buffer_load_dwordx4 v240, s[8:11], s45 offen offset:0 lds
v_mfma_f32_16x16x32_bf16 v[140:143], a[124:127], a[96:99], v[140:143]
ds_read_b64_tr_b16 a[84:85], v246 offset:0
ds_read_b64_tr_b16 a[86:87], v246 offset:4096
v_mfma_f32_16x16x32_bf16 v[144:147], a[112:115], a[100:103], v[144:147]
s_add_u32 s44, s44, 4096
ds_read_b128 a[68:71], v244 offset:1024
v_mfma_f32_16x16x32_bf16 v[148:151], a[116:119], a[100:103], v[148:151]
ds_read_b64_tr_b16 a[88:89], v247 offset:0
ds_read_b64_tr_b16 a[90:91], v247 offset:4096
v_mfma_f32_16x16x32_bf16 v[152:155], a[120:123], a[100:103], v[152:155]
buffer_load_dwordx4 v241, s[8:11], s45 offen offset:1024 lds
v_mfma_f32_16x16x32_bf16 v[156:159], a[124:127], a[100:103], v[156:159]
ds_read_b128 a[72:75], v244 offset:4096
v_mfma_f32_16x16x32_bf16 v[160:163], a[112:115], a[104:107], v[160:163]
ds_read_b64_tr_b16 a[92:93], v248 offset:0
ds_read_b64_tr_b16 a[94:95], v248 offset:4096
v_mfma_f32_16x16x32_bf16 v[164:167], a[116:119], a[104:107], v[164:167]
ds_read_b128 a[76:79], v244 offset:5120
v_mfma_f32_16x16x32_bf16 v[168:171], a[120:123], a[104:107], v[168:171]
s_nop 0
buffer_load_dwordx4 v242, s[8:11], s45 offen offset:2048 lds
v_or_b32 v0, 5, v11
v_mfma_f32_16x16x32_bf16 v[172:175], a[124:127], a[104:107], v[172:175]
s_cmp_eq_u32 s46, 0
s_cselect_b32 s53, s56, s57
v_mfma_f32_16x16x32_bf16 v[176:179], a[112:115], a[108:111], v[176:179]
v_mfma_f32_16x16x32_bf16 v[180:183], a[116:119], a[108:111], v[180:183]
v_mfma_f32_16x16x32_bf16 v[184:187], a[120:123], a[108:111], v[184:187]
buffer_load_dwordx4 v243, s[8:11], s45 offen offset:3072 lds
buffer_store_dword v0, v6, s[60:63], 0 offen sc1
v_mfma_f32_16x16x32_bf16 v[188:191], a[124:127], a[108:111], v[188:191]
s_waitcnt lgkmcnt(0)
s_mov_b32 m0, s49
s_add_u32 s45, s45, 131072
v_mfma_f32_16x16x32_bf16 v[128:131], a[80:83], a[64:67], v[128:131]
ds_read_b64_tr_b16 a[112:113], v245 offset:8192
ds_read_b64_tr_b16 a[114:115], v245 offset:12288
v_mfma_f32_16x16x32_bf16 v[132:135], a[84:87], a[64:67], v[132:135]
ds_read_b128 a[96:99], v244 offset:2048
v_add_u32 v245, s53, v245
v_mfma_f32_16x16x32_bf16 v[136:139], a[88:91], a[64:67], v[136:139]
buffer_load_dwordx4 v7, s[4:7], s44 offen offset:0 sc1 lds
v_mfma_f32_16x16x32_bf16 v[140:143], a[92:95], a[64:67], v[140:143]
ds_read_b64_tr_b16 a[116:117], v246 offset:8192
ds_read_b64_tr_b16 a[118:119], v246 offset:12288
v_mfma_f32_16x16x32_bf16 v[144:147], a[80:83], a[68:71], v[144:147]
ds_read_b128 a[100:103], v244 offset:3072
v_add_u32 v246, s53, v246
v_mfma_f32_16x16x32_bf16 v[148:151], a[84:87], a[68:71], v[148:151]
ds_read_b64_tr_b16 a[120:121], v247 offset:8192
ds_read_b64_tr_b16 a[122:123], v247 offset:12288
v_mfma_f32_16x16x32_bf16 v[152:155], a[88:91], a[68:71], v[152:155]
buffer_load_dwordx4 v7, s[4:7], s44 offen offset:1024 sc1 lds
v_mfma_f32_16x16x32_bf16 v[156:159], a[92:95], a[68:71], v[156:159]
ds_read_b128 a[104:107], v244 offset:6144
v_add_u32 v247, s53, v247
v_mfma_f32_16x16x32_bf16 v[160:163], a[80:83], a[72:75], v[160:163]
ds_read_b64_tr_b16 a[124:125], v248 offset:8192
ds_read_b64_tr_b16 a[126:127], v248 offset:12288
v_mfma_f32_16x16x32_bf16 v[164:167], a[84:87], a[72:75], v[164:167]
ds_read_b128 a[108:111], v244 offset:7168
v_add_u32 v248, s53, v248
v_mfma_f32_16x16x32_bf16 v[168:171], a[88:91], a[72:75], v[168:171]
buffer_load_dwordx4 v7, s[4:7], s44 offen offset:2048 sc1 lds
v_mfma_f32_16x16x32_bf16 v[172:175], a[92:95], a[72:75], v[172:175]
s_add_u32 s49, s46, s91
v_add_u32 v244, s53, v244
v_mfma_f32_16x16x32_bf16 v[176:179], a[80:83], a[76:79], v[176:179]
s_add_u32 s46, s46, s53
v_mfma_f32_16x16x32_bf16 v[180:183], a[84:87], a[76:79], v[180:183]
v_mfma_f32_16x16x32_bf16 v[184:187], a[88:91], a[76:79], v[184:187]
buffer_load_dwordx4 v7, s[4:7], s44 offen offset:3072 sc1 lds
v_mfma_f32_16x16x32_bf16 v[188:191], a[92:95], a[76:79], v[188:191]
s_waitcnt vmcnt(25) lgkmcnt(0)
s_barrier
v_mfma_f32_16x16x32_bf16 v[128:131], a[112:115], a[96:99], v[128:131]
ds_read_b64_tr_b16 a[80:81], v245 offset:0
ds_read_b64_tr_b16 a[82:83], v245 offset:4096
v_mfma_f32_16x16x32_bf16 v[132:135], a[116:119], a[96:99], v[132:135]
s_add_u32 m0, s49, 16384
ds_read_b128 a[64:67], v244 offset:0
v_mfma_f32_16x16x32_bf16 v[136:139], a[120:123], a[96:99], v[136:139]
buffer_load_dwordx4 v240, s[8:11], s45 offen offset:0 lds
v_mfma_f32_16x16x32_bf16 v[140:143], a[124:127], a[96:99], v[140:143]
ds_read_b64_tr_b16 a[84:85], v246 offset:0
ds_read_b64_tr_b16 a[86:87], v246 offset:4096
v_mfma_f32_16x16x32_bf16 v[144:147], a[112:115], a[100:103], v[144:147]
s_add_u32 s44, s44, 4096
ds_read_b128 a[68:71], v244 offset:1024
v_mfma_f32_16x16x32_bf16 v[148:151], a[116:119], a[100:103], v[148:151]
ds_read_b64_tr_b16 a[88:89], v247 offset:0
ds_read_b64_tr_b16 a[90:91], v247 offset:4096
v_mfma_f32_16x16x32_bf16 v[152:155], a[120:123], a[100:103], v[152:155]
buffer_load_dwordx4 v241, s[8:11], s45 offen offset:1024 lds
v_mfma_f32_16x16x32_bf16 v[156:159], a[124:127], a[100:103], v[156:159]
ds_read_b128 a[72:75], v244 offset:4096
v_mfma_f32_16x16x32_bf16 v[160:163], a[112:115], a[104:107], v[160:163]
ds_read_b64_tr_b16 a[92:93], v248 offset:0
ds_read_b64_tr_b16 a[94:95], v248 offset:4096
v_mfma_f32_16x16x32_bf16 v[164:167], a[116:119], a[104:107], v[164:167]
ds_read_b128 a[76:79], v244 offset:5120
v_mfma_f32_16x16x32_bf16 v[168:171], a[120:123], a[104:107], v[168:171]
buffer_load_dwordx4 v242, s[8:11], s45 offen offset:2048 lds
v_mfma_f32_16x16x32_bf16 v[172:175], a[124:127], a[104:107], v[172:175]
s_cmp_eq_u32 s46, 0
s_cselect_b32 s53, s56, s57
v_mfma_f32_16x16x32_bf16 v[176:179], a[112:115], a[108:111], v[176:179]
v_mfma_f32_16x16x32_bf16 v[180:183], a[116:119], a[108:111], v[180:183]
v_mfma_f32_16x16x32_bf16 v[184:187], a[120:123], a[108:111], v[184:187]
buffer_load_dwordx4 v243, s[8:11], s45 offen offset:3072 lds
v_mfma_f32_16x16x32_bf16 v[188:191], a[124:127], a[108:111], v[188:191]
s_waitcnt lgkmcnt(0)
s_mov_b32 m0, s49
s_add_u32 s45, s45, 131072
v_mfma_f32_16x16x32_bf16 v[128:131], a[80:83], a[64:67], v[128:131]
ds_read_b64_tr_b16 a[112:113], v245 offset:8192
ds_read_b64_tr_b16 a[114:115], v245 offset:12288
v_mfma_f32_16x16x32_bf16 v[132:135], a[84:87], a[64:67], v[132:135]
ds_read_b128 a[96:99], v244 offset:2048
v_add_u32 v245, s53, v245
v_mfma_f32_16x16x32_bf16 v[136:139], a[88:91], a[64:67], v[136:139]
buffer_load_dwordx4 v7, s[4:7], s44 offen offset:0 sc1 lds
v_mfma_f32_16x16x32_bf16 v[140:143], a[92:95], a[64:67], v[140:143]
ds_read_b64_tr_b16 a[116:117], v246 offset:8192
ds_read_b64_tr_b16 a[118:119], v246 offset:12288
v_mfma_f32_16x16x32_bf16 v[144:147], a[80:83], a[68:71], v[144:147]
ds_read_b128 a[100:103], v244 offset:3072
v_add_u32 v246, s53, v246
v_mfma_f32_16x16x32_bf16 v[148:151], a[84:87], a[68:71], v[148:151]
ds_read_b64_tr_b16 a[120:121], v247 offset:8192
ds_read_b64_tr_b16 a[122:123], v247 offset:12288
v_mfma_f32_16x16x32_bf16 v[152:155], a[88:91], a[68:71], v[152:155]
buffer_load_dwordx4 v7, s[4:7], s44 offen offset:1024 sc1 lds
v_mfma_f32_16x16x32_bf16 v[156:159], a[92:95], a[68:71], v[156:159]
ds_read_b128 a[104:107], v244 offset:6144
v_add_u32 v247, s53, v247
v_mfma_f32_16x16x32_bf16 v[160:163], a[80:83], a[72:75], v[160:163]
ds_read_b64_tr_b16 a[124:125], v248 offset:8192
ds_read_b64_tr_b16 a[126:127], v248 offset:12288
v_mfma_f32_16x16x32_bf16 v[164:167], a[84:87], a[72:75], v[164:167]
ds_read_b128 a[108:111], v244 offset:7168
v_add_u32 v248, s53, v248
v_mfma_f32_16x16x32_bf16 v[168:171], a[88:91], a[72:75], v[168:171]
buffer_load_dwordx4 v7, s[4:7], s44 offen offset:2048 sc1 lds
v_mfma_f32_16x16x32_bf16 v[172:175], a[92:95], a[72:75], v[172:175]
s_add_u32 s49, s46, s91
v_add_u32 v244, s53, v244
v_mfma_f32_16x16x32_bf16 v[176:179], a[80:83], a[76:79], v[176:179]
s_add_u32 s46, s46, s53
v_mfma_f32_16x16x32_bf16 v[180:183], a[84:87], a[76:79], v[180:183]
v_mfma_f32_16x16x32_bf16 v[184:187], a[88:91], a[76:79], v[184:187]
buffer_load_dwordx4 v7, s[4:7], s44 offen offset:3072 sc1 lds
v_mfma_f32_16x16x32_bf16 v[188:191], a[92:95], a[76:79], v[188:191]
s_waitcnt vmcnt(25) lgkmcnt(0)
s_barrier
v_mfma_f32_16x16x32_bf16 v[128:131], a[112:115], a[96:99], v[128:131]
ds_read_b64_tr_b16 a[80:81], v245 offset:0
ds_read_b64_tr_b16 a[82:83], v245 offset:4096
v_mfma_f32_16x16x32_bf16 v[132:135], a[116:119], a[96:99], v[132:135]
s_add_u32 m0, s49, 16384
ds_read_b128 a[64:67], v244 offset:0
v_mfma_f32_16x16x32_bf16 v[136:139], a[120:123], a[96:99], v[136:139]
buffer_load_dwordx4 v240, s[8:11], s45 offen offset:0 lds
v_mfma_f32_16x16x32_bf16 v[140:143], a[124:127], a[96:99], v[140:143]
ds_read_b64_tr_b16 a[84:85], v246 offset:0
ds_read_b64_tr_b16 a[86:87], v246 offset:4096
v_mfma_f32_16x16x32_bf16 v[144:147], a[112:115], a[100:103], v[144:147]
s_add_u32 s44, s44, 4096
ds_read_b128 a[68:71], v244 offset:1024
v_mfma_f32_16x16x32_bf16 v[148:151], a[116:119], a[100:103], v[148:151]
ds_read_b64_tr_b16 a[88:89], v247 offset:0
ds_read_b64_tr_b16 a[90:91], v247 offset:4096
v_mfma_f32_16x16x32_bf16 v[152:155], a[120:123], a[100:103], v[152:155]
buffer_load_dwordx4 v241, s[8:11], s45 offen offset:1024 lds
v_mfma_f32_16x16x32_bf16 v[156:159], a[124:127], a[100:103], v[156:159]
ds_read_b128 a[72:75], v244 offset:4096
v_mfma_f32_16x16x32_bf16 v[160:163], a[112:115], a[104:107], v[160:163]
ds_read_b64_tr_b16 a[92:93], v248 offset:0
ds_read_b64_tr_b16 a[94:95], v248 offset:4096
v_mfma_f32_16x16x32_bf16 v[164:167], a[116:119], a[104:107], v[164:167]
ds_read_b128 a[76:79], v244 offset:5120
v_mfma_f32_16x16x32_bf16 v[168:171], a[120:123], a[104:107], v[168:171]
buffer_load_dwordx4 v242, s[8:11], s45 offen offset:2048 lds
v_mfma_f32_16x16x32_bf16 v[172:175], a[124:127], a[104:107], v[172:175]
s_cmp_eq_u32 s46, 0
s_cselect_b32 s53, s56, s57
v_mfma_f32_16x16x32_bf16 v[176:179], a[112:115], a[108:111], v[176:179]
v_mfma_f32_16x16x32_bf16 v[180:183], a[116:119], a[108:111], v[180:183]
v_mfma_f32_16x16x32_bf16 v[184:187], a[120:123], a[108:111], v[184:187]
buffer_load_dwordx4 v243, s[8:11], s45 offen offset:3072 lds
v_mfma_f32_16x16x32_bf16 v[188:191], a[124:127], a[108:111], v[188:191]
s_waitcnt lgkmcnt(0)
s_mov_b32 m0, s49
s_add_u32 s45, s45, 131072
v_mfma_f32_16x16x32_bf16 v[128:131], a[80:83], a[64:67], v[128:131]
ds_read_b64_tr_b16 a[112:113], v245 offset:8192
ds_read_b64_tr_b16 a[114:115], v245 offset:12288
v_mfma_f32_16x16x32_bf16 v[132:135], a[84:87], a[64:67], v[132:135]
ds_read_b128 a[96:99], v244 offset:2048
v_add_u32 v245, s53, v245
v_mfma_f32_16x16x32_bf16 v[136:139], a[88:91], a[64:67], v[136:139]
buffer_load_dwordx4 v7, s[4:7], s44 offen offset:0 sc1 lds
v_mfma_f32_16x16x32_bf16 v[140:143], a[92:95], a[64:67], v[140:143]
ds_read_b64_tr_b16 a[116:117], v246 offset:8192
ds_read_b64_tr_b16 a[118:119], v246 offset:12288
v_mfma_f32_16x16x32_bf16 v[144:147], a[80:83], a[68:71], v[144:147]
ds_read_b128 a[100:103], v244 offset:3072
v_add_u32 v246, s53, v246
v_mfma_f32_16x16x32_bf16 v[148:151], a[84:87], a[68:71], v[148:151]
ds_read_b64_tr_b16 a[120:121], v247 offset:8192
ds_read_b64_tr_b16 a[122:123], v247 offset:12288
v_mfma_f32_16x16x32_bf16 v[152:155], a[88:91], a[68:71], v[152:155]
buffer_load_dwordx4 v7, s[4:7], s44 offen offset:1024 sc1 lds
v_mfma_f32_16x16x32_bf16 v[156:159], a[92:95], a[68:71], v[156:159]
ds_read_b128 a[104:107], v244 offset:6144
v_add_u32 v247, s53, v247
v_mfma_f32_16x16x32_bf16 v[160:163], a[80:83], a[72:75], v[160:163]
ds_read_b64_tr_b16 a[124:125], v248 offset:8192
ds_read_b64_tr_b16 a[126:127], v248 offset:12288
v_mfma_f32_16x16x32_bf16 v[164:167], a[84:87], a[72:75], v[164:167]
ds_read_b128 a[108:111], v244 offset:7168
v_add_u32 v248, s53, v248
v_mfma_f32_16x16x32_bf16 v[168:171], a[88:91], a[72:75], v[168:171]
buffer_load_dwordx4 v7, s[4:7], s44 offen offset:2048 sc1 lds
v_mfma_f32_16x16x32_bf16 v[172:175], a[92:95], a[72:75], v[172:175]
s_add_u32 s49, s46, s91
v_add_u32 v244, s53, v244
v_mfma_f32_16x16x32_bf16 v[176:179], a[80:83], a[76:79], v[176:179]
s_add_u32 s46, s46, s53
v_mfma_f32_16x16x32_bf16 v[180:183], a[84:87], a[76:79], v[180:183]
.Lpad23:
s_nop 0
v_mfma_f32_16x16x32_bf16 v[184:187], a[88:91], a[76:79], v[184:187]
buffer_load_dwordx4 v7, s[4:7], s44 offen offset:3072 sc1 lds
v_mfma_f32_16x16x32_bf16 v[188:191], a[92:95], a[76:79], v[188:191]
s_waitcnt vmcnt(25) lgkmcnt(0)
s_barrier
v_mfma_f32_16x16x32_bf16 v[128:131], a[112:115], a[96:99], v[128:131]
ds_read_b64_tr_b16 a[80:81], v245 offset:0
ds_read_b64_tr_b16 a[82:83], v245 offset:4096
v_mfma_f32_16x16x32_bf16 v[132:135], a[116:119], a[96:99], v[132:135]
s_add_u32 m0, s49, 16384
ds_read_b128 a[64:67], v244 offset:0
v_mfma_f32_16x16x32_bf16 v[136:139], a[120:123], a[96:99], v[136:139]
buffer_load_dwordx4 v240, s[8:11], s45 offen offset:0 lds
v_mfma_f32_16x16x32_bf16 v[140:143], a[124:127], a[96:99], v[140:143]
ds_read_b64_tr_b16 a[84:85], v246 offset:0
ds_read_b64_tr_b16 a[86:87], v246 offset:4096
v_mfma_f32_16x16x32_bf16 v[144:147], a[112:115], a[100:103], v[144:147]
s_add_u32 s44, s44, 4096
ds_read_b128 a[68:71], v244 offset:1024
v_mfma_f32_16x16x32_bf16 v[148:151], a[116:119], a[100:103], v[148:151]
ds_read_b64_tr_b16 a[88:89], v247 offset:0
ds_read_b64_tr_b16 a[90:91], v247 offset:4096
v_mfma_f32_16x16x32_bf16 v[152:155], a[120:123], a[100:103], v[152:155]
buffer_load_dwordx4 v241, s[8:11], s45 offen offset:1024 lds
v_mfma_f32_16x16x32_bf16 v[156:159], a[124:127], a[100:103], v[156:159]
ds_read_b128 a[72:75], v244 offset:4096
v_mfma_f32_16x16x32_bf16 v[160:163], a[112:115], a[104:107], v[160:163]
ds_read_b64_tr_b16 a[92:93], v248 offset:0
ds_read_b64_tr_b16 a[94:95], v248 offset:4096
v_mfma_f32_16x16x32_bf16 v[164:167], a[116:119], a[104:107], v[164:167]
ds_read_b128 a[76:79], v244 offset:5120
v_mfma_f32_16x16x32_bf16 v[168:171], a[120:123], a[104:107], v[168:171]
buffer_load_dwordx4 v242, s[8:11], s45 offen offset:2048 lds
v_mfma_f32_16x16x32_bf16 v[172:175], a[124:127], a[104:107], v[172:175]
s_cmp_eq_u32 s46, 0
s_cselect_b32 s53, s56, s57
v_mfma_f32_16x16x32_bf16 v[176:179], a[112:115], a[108:111], v[176:179]
v_mfma_f32_16x16x32_bf16 v[180:183], a[116:119], a[108:111], v[180:183]
v_mfma_f32_16x16x32_bf16 v[184:187], a[120:123], a[108:111], v[184:187]
buffer_load_dwordx4 v243, s[8:11], s45 offen offset:3072 lds
v_mfma_f32_16x16x32_bf16 v[188:191], a[124:127], a[108:111], v[188:191]
s_waitcnt lgkmcnt(0)
s_mov_b32 m0, s49
s_add_u32 s45, s45, 131072
v_mfma_f32_16x16x32_bf16 v[128:131], a[80:83], a[64:67], v[128:131]
ds_read_b64_tr_b16 a[112:113], v245 offset:8192
ds_read_b64_tr_b16 a[114:115], v245 offset:12288
v_mfma_f32_16x16x32_bf16 v[132:135], a[84:87], a[64:67], v[132:135]
ds_read_b128 a[96:99], v244 offset:2048
v_add_u32 v245, s53, v245
v_mfma_f32_16x16x32_bf16 v[136:139], a[88:91], a[64:67], v[136:139]
buffer_load_dwordx4 v7, s[4:7], s44 offen offset:0 sc1 lds
v_mfma_f32_16x16x32_bf16 v[140:143], a[92:95], a[64:67], v[140:143]
ds_read_b64_tr_b16 a[116:117], v246 offset:8192
ds_read_b64_tr_b16 a[118:119], v246 offset:12288
v_mfma_f32_16x16x32_bf16 v[144:147], a[80:83], a[68:71], v[144:147]
ds_read_b128 a[100:103], v244 offset:3072
v_add_u32 v246, s53, v246
v_mfma_f32_16x16x32_bf16 v[148:151], a[84:87], a[68:71], v[148:151]
ds_read_b64_tr_b16 a[120:121], v247 offset:8192
ds_read_b64_tr_b16 a[122:123], v247 offset:12288
v_mfma_f32_16x16x32_bf16 v[152:155], a[88:91], a[68:71], v[152:155]
buffer_load_dwordx4 v7, s[4:7], s44 offen offset:1024 sc1 lds
v_mfma_f32_16x16x32_bf16 v[156:159], a[92:95], a[68:71], v[156:159]
ds_read_b128 a[104:107], v244 offset:6144
v_add_u32 v247, s53, v247
v_mfma_f32_16x16x32_bf16 v[160:163], a[80:83], a[72:75], v[160:163]
ds_read_b64_tr_b16 a[124:125], v248 offset:8192
ds_read_b64_tr_b16 a[126:127], v248 offset:12288
v_mfma_f32_16x16x32_bf16 v[164:167], a[84:87], a[72:75], v[164:167]
ds_read_b128 a[108:111], v244 offset:7168
v_add_u32 v248, s53, v248
v_mfma_f32_16x16x32_bf16 v[168:171], a[88:91], a[72:75], v[168:171]
buffer_load_dwordx4 v7, s[4:7], s44 offen offset:2048 sc1 lds
v_mfma_f32_16x16x32_bf16 v[172:175], a[92:95], a[72:75], v[172:175]
s_add_u32 s49, s46, s91
v_add_u32 v244, s53, v244
v_mfma_f32_16x16x32_bf16 v[176:179], a[80:83], a[76:79], v[176:179]
s_add_u32 s46, s46, s53
v_mfma_f32_16x16x32_bf16 v[180:183], a[84:87], a[76:79], v[180:183]
v_mfma_f32_16x16x32_bf16 v[184:187], a[88:91], a[76:79], v[184:187]
buffer_load_dwordx4 v7, s[4:7], s44 offen offset:3072 sc1 lds
v_mfma_f32_16x16x32_bf16 v[188:191], a[92:95], a[76:79], v[188:191]
s_waitcnt vmcnt(24) lgkmcnt(0)
s_barrier
v_mfma_f32_16x16x32_bf16 v[128:131], a[112:115], a[96:99], v[128:131]
ds_read_b64_tr_b16 a[80:81], v245 offset:0
ds_read_b64_tr_b16 a[82:83], v245 offset:4096
v_mfma_f32_16x16x32_bf16 v[132:135], a[116:119], a[96:99], v[132:135]
s_add_u32 m0, s49, 16384
ds_read_b128 a[64:67], v244 offset:0
v_mfma_f32_16x16x32_bf16 v[136:139], a[120:123], a[96:99], v[136:139]
buffer_load_dwordx4 v240, s[8:11], s45 offen offset:0 lds
v_mfma_f32_16x16x32_bf16 v[140:143], a[124:127], a[96:99], v[140:143]
ds_read_b64_tr_b16 a[84:85], v246 offset:0
ds_read_b64_tr_b16 a[86:87], v246 offset:4096
v_mfma_f32_16x16x32_bf16 v[144:147], a[112:115], a[100:103], v[144:147]
s_add_u32 s44, s44, 4096
ds_read_b128 a[68:71], v244 offset:1024
v_mfma_f32_16x16x32_bf16 v[148:151], a[116:119], a[100:103], v[148:151]
ds_read_b64_tr_b16 a[88:89], v247 offset:0
ds_read_b64_tr_b16 a[90:91], v247 offset:4096
v_mfma_f32_16x16x32_bf16 v[152:155], a[120:123], a[100:103], v[152:155]
buffer_load_dwordx4 v241, s[8:11], s45 offen offset:1024 lds
v_mfma_f32_16x16x32_bf16 v[156:159], a[124:127], a[100:103], v[156:159]
ds_read_b128 a[72:75], v244 offset:4096
v_mfma_f32_16x16x32_bf16 v[160:163], a[112:115], a[104:107], v[160:163]
ds_read_b64_tr_b16 a[92:93], v248 offset:0
ds_read_b64_tr_b16 a[94:95], v248 offset:4096
v_mfma_f32_16x16x32_bf16 v[164:167], a[116:119], a[104:107], v[164:167]
ds_read_b128 a[76:79], v244 offset:5120
v_mfma_f32_16x16x32_bf16 v[168:171], a[120:123], a[104:107], v[168:171]
buffer_load_dwordx4 v242, s[8:11], s45 offen offset:2048 lds
v_mfma_f32_16x16x32_bf16 v[172:175], a[124:127], a[104:107], v[172:175]
s_cmp_eq_u32 s46, 0
s_cselect_b32 s53, s56, s57
v_mfma_f32_16x16x32_bf16 v[176:179], a[112:115], a[108:111], v[176:179]
v_mfma_f32_16x16x32_bf16 v[180:183], a[116:119], a[108:111], v[180:183]
v_mfma_f32_16x16x32_bf16 v[184:187], a[120:123], a[108:111], v[184:187]
buffer_load_dwordx4 v243, s[8:11], s45 offen offset:3072 lds
v_mfma_f32_16x16x32_bf16 v[188:191], a[124:127], a[108:111], v[188:191]
s_waitcnt lgkmcnt(0)
s_mov_b32 m0, s49
s_add_u32 s45, s45, 131072
v_mfma_f32_16x16x32_bf16 v[128:131], a[80:83], a[64:67], v[128:131]
ds_read_b64_tr_b16 a[112:113], v245 offset:8192
ds_read_b64_tr_b16 a[114:115], v245 offset:12288
v_mfma_f32_16x16x32_bf16 v[132:135], a[84:87], a[64:67], v[132:135]
ds_read_b128 a[96:99], v244 offset:2048
v_add_u32 v245, s53, v245
v_mfma_f32_16x16x32_bf16 v[136:139], a[88:91], a[64:67], v[136:139]
buffer_load_dwordx4 v7, s[4:7], s44 offen offset:0 sc1 lds
v_mfma_f32_16x16x32_bf16 v[140:143], a[92:95], a[64:67], v[140:143]
ds_read_b64_tr_b16 a[116:117], v246 offset:8192
ds_read_b64_tr_b16 a[118:119], v246 offset:12288
v_mfma_f32_16x16x32_bf16 v[144:147], a[80:83], a[68:71], v[144:147]
ds_read_b128 a[100:103], v244 offset:3072
v_add_u32 v246, s53, v246
v_mfma_f32_16x16x32_bf16 v[148:151], a[84:87], a[68:71], v[148:151]
ds_read_b64_tr_b16 a[120:121], v247 offset:8192
ds_read_b64_tr_b16 a[122:123], v247 offset:12288
v_mfma_f32_16x16x32_bf16 v[152:155], a[88:91], a[68:71], v[152:155]
buffer_load_dwordx4 v7, s[4:7], s44 offen offset:1024 sc1 lds
v_mfma_f32_16x16x32_bf16 v[156:159], a[92:95], a[68:71], v[156:159]
ds_read_b128 a[104:107], v244 offset:6144
v_add_u32 v247, s53, v247
v_mfma_f32_16x16x32_bf16 v[160:163], a[80:83], a[72:75], v[160:163]
ds_read_b64_tr_b16 a[124:125], v248 offset:8192
ds_read_b64_tr_b16 a[126:127], v248 offset:12288
v_mfma_f32_16x16x32_bf16 v[164:167], a[84:87], a[72:75], v[164:167]
ds_read_b128 a[108:111], v244 offset:7168
v_add_u32 v248, s53, v248
v_mfma_f32_16x16x32_bf16 v[168:171], a[88:91], a[72:75], v[168:171]
buffer_load_dwordx4 v7, s[4:7], s44 offen offset:2048 sc1 lds
v_mfma_f32_16x16x32_bf16 v[172:175], a[92:95], a[72:75], v[172:175]
s_add_u32 s49, s46, s91
v_add_u32 v244, s53, v244
v_mfma_f32_16x16x32_bf16 v[176:179], a[80:83], a[76:79], v[176:179]
s_add_u32 s46, s46, s53
v_mfma_f32_16x16x32_bf16 v[180:183], a[84:87], a[76:79], v[180:183]
v_mfma_f32_16x16x32_bf16 v[184:187], a[88:91], a[76:79], v[184:187]
buffer_load_dwordx4 v7, s[4:7], s44 offen offset:3072 sc1 lds
v_mfma_f32_16x16x32_bf16 v[188:191], a[92:95], a[76:79], v[188:191]
s_waitcnt vmcnt(24) lgkmcnt(0)
s_barrier
v_mfma_f32_16x16x32_bf16 v[128:131], a[112:115], a[96:99], v[128:131]
ds_read_b64_tr_b16 a[80:81], v245 offset:0
ds_read_b64_tr_b16 a[82:83], v245 offset:4096
v_mfma_f32_16x16x32_bf16 v[132:135], a[116:119], a[96:99], v[132:135]
ds_read_b128 a[64:67], v244 offset:0
v_mfma_f32_16x16x32_bf16 v[136:139], a[120:123], a[96:99], v[136:139]
v_mfma_f32_16x16x32_bf16 v[140:143], a[124:127], a[96:99], v[140:143]
ds_read_b64_tr_b16 a[84:85], v246 offset:0
ds_read_b64_tr_b16 a[86:87], v246 offset:4096
v_mfma_f32_16x16x32_bf16 v[144:147], a[112:115], a[100:103], v[144:147]
ds_read_b128 a[68:71], v244 offset:1024
v_mfma_f32_16x16x32_bf16 v[148:151], a[116:119], a[100:103], v[148:151]
ds_read_b64_tr_b16 a[88:89], v247 offset:0
ds_read_b64_tr_b16 a[90:91], v247 offset:4096
v_mfma_f32_16x16x32_bf16 v[152:155], a[120:123], a[100:103], v[152:155]
v_mfma_f32_16x16x32_bf16 v[156:159], a[124:127], a[100:103], v[156:159]
ds_read_b128 a[72:75], v244 offset:4096
v_mfma_f32_16x16x32_bf16 v[160:163], a[112:115], a[104:107], v[160:163]
ds_read_b64_tr_b16 a[92:93], v248 offset:0
ds_read_b64_tr_b16 a[94:95], v248 offset:4096
v_mfma_f32_16x16x32_bf16 v[164:167], a[116:119], a[104:107], v[164:167]
ds_read_b128 a[76:79], v244 offset:5120
v_mfma_f32_16x16x32_bf16 v[168:171], a[120:123], a[104:107], v[168:171]
v_mfma_f32_16x16x32_bf16 v[172:175], a[124:127], a[104:107], v[172:175]
s_cmp_eq_u32 s46, 0
s_cselect_b32 s53, s56, s57
v_mfma_f32_16x16x32_bf16 v[176:179], a[112:115], a[108:111], v[176:179]
v_mfma_f32_16x16x32_bf16 v[180:183], a[116:119], a[108:111], v[180:183]
v_mfma_f32_16x16x32_bf16 v[184:187], a[120:123], a[108:111], v[184:187]
v_mfma_f32_16x16x32_bf16 v[188:191], a[124:127], a[108:111], v[188:191]
s_waitcnt lgkmcnt(0)
v_mfma_f32_16x16x32_bf16 v[128:131], a[80:83], a[64:67], v[128:131]
ds_read_b64_tr_b16 a[112:113], v245 offset:8192
ds_read_b64_tr_b16 a[114:115], v245 offset:12288
v_mfma_f32_16x16x32_bf16 v[132:135], a[84:87], a[64:67], v[132:135]
ds_read_b128 a[96:99], v244 offset:2048
v_add_u32 v245, s53, v245
v_mfma_f32_16x16x32_bf16 v[136:139], a[88:91], a[64:67], v[136:139]
v_mfma_f32_16x16x32_bf16 v[140:143], a[92:95], a[64:67], v[140:143]
ds_read_b64_tr_b16 a[116:117], v246 offset:8192
ds_read_b64_tr_b16 a[118:119], v246 offset:12288
v_mfma_f32_16x16x32_bf16 v[144:147], a[80:83], a[68:71], v[144:147]
ds_read_b128 a[100:103], v244 offset:3072
v_add_u32 v246, s53, v246
v_mfma_f32_16x16x32_bf16 v[148:151], a[84:87], a[68:71], v[148:151]
ds_read_b64_tr_b16 a[120:121], v247 offset:8192
ds_read_b64_tr_b16 a[122:123], v247 offset:12288
v_mfma_f32_16x16x32_bf16 v[152:155], a[88:91], a[68:71], v[152:155]
v_mfma_f32_16x16x32_bf16 v[156:159], a[92:95], a[68:71], v[156:159]
ds_read_b128 a[104:107], v244 offset:6144
v_add_u32 v247, s53, v247
v_mfma_f32_16x16x32_bf16 v[160:163], a[80:83], a[72:75], v[160:163]
ds_read_b64_tr_b16 a[124:125], v248 offset:8192
ds_read_b64_tr_b16 a[126:127], v248 offset:12288
v_mfma_f32_16x16x32_bf16 v[164:167], a[84:87], a[72:75], v[164:167]
ds_read_b128 a[108:111], v244 offset:7168
v_add_u32 v248, s53, v248
v_mfma_f32_16x16x32_bf16 v[168:171], a[88:91], a[72:75], v[168:171]
v_mfma_f32_16x16x32_bf16 v[172:175], a[92:95], a[72:75], v[172:175]
s_add_u32 s49, s46, s91
v_add_u32 v244, s53, v244
v_mfma_f32_16x16x32_bf16 v[176:179], a[80:83], a[76:79], v[176:179]
s_add_u32 s46, s46, s53
v_mfma_f32_16x16x32_bf16 v[180:183], a[84:87], a[76:79], v[180:183]
v_mfma_f32_16x16x32_bf16 v[184:187], a[88:91], a[76:79], v[184:187]
v_mfma_f32_16x16x32_bf16 v[188:191], a[92:95], a[76:79], v[188:191]
s_waitcnt vmcnt(16) lgkmcnt(0)
s_barrier
v_mfma_f32_16x16x32_bf16 v[128:131], a[112:115], a[96:99], v[128:131]
ds_read_b64_tr_b16 a[80:81], v245 offset:0
ds_read_b64_tr_b16 a[82:83], v245 offset:4096
v_mfma_f32_16x16x32_bf16 v[132:135], a[116:119], a[96:99], v[132:135]
ds_read_b128 a[64:67], v244 offset:0
v_mfma_f32_16x16x32_bf16 v[136:139], a[120:123], a[96:99], v[136:139]
v_mfma_f32_16x16x32_bf16 v[140:143], a[124:127], a[96:99], v[140:143]
ds_read_b64_tr_b16 a[84:85], v246 offset:0
ds_read_b64_tr_b16 a[86:87], v246 offset:4096
v_mfma_f32_16x16x32_bf16 v[144:147], a[112:115], a[100:103], v[144:147]
ds_read_b128 a[68:71], v244 offset:1024
v_mfma_f32_16x16x32_bf16 v[148:151], a[116:119], a[100:103], v[148:151]
ds_read_b64_tr_b16 a[88:89], v247 offset:0
ds_read_b64_tr_b16 a[90:91], v247 offset:4096
v_mfma_f32_16x16x32_bf16 v[152:155], a[120:123], a[100:103], v[152:155]
v_mfma_f32_16x16x32_bf16 v[156:159], a[124:127], a[100:103], v[156:159]
ds_read_b128 a[72:75], v244 offset:4096
v_mfma_f32_16x16x32_bf16 v[160:163], a[112:115], a[104:107], v[160:163]
ds_read_b64_tr_b16 a[92:93], v248 offset:0
ds_read_b64_tr_b16 a[94:95], v248 offset:4096
v_mfma_f32_16x16x32_bf16 v[164:167], a[116:119], a[104:107], v[164:167]
ds_read_b128 a[76:79], v244 offset:5120
v_mfma_f32_16x16x32_bf16 v[168:171], a[120:123], a[104:107], v[168:171]
v_mfma_f32_16x16x32_bf16 v[172:175], a[124:127], a[104:107], v[172:175]
s_cmp_eq_u32 s46, 0
s_cselect_b32 s53, s56, s57
v_mfma_f32_16x16x32_bf16 v[176:179], a[112:115], a[108:111], v[176:179]
v_mfma_f32_16x16x32_bf16 v[180:183], a[116:119], a[108:111], v[180:183]
v_mfma_f32_16x16x32_bf16 v[184:187], a[120:123], a[108:111], v[184:187]
v_mfma_f32_16x16x32_bf16 v[188:191], a[124:127], a[108:111], v[188:191]
s_waitcnt lgkmcnt(0)
v_mfma_f32_16x16x32_bf16 v[128:131], a[80:83], a[64:67], v[128:131]
ds_read_b64_tr_b16 a[112:113], v245 offset:8192
ds_read_b64_tr_b16 a[114:115], v245 offset:12288
v_mfma_f32_16x16x32_bf16 v[132:135], a[84:87], a[64:67], v[132:135]
ds_read_b128 a[96:99], v244 offset:2048
v_add_u32 v245, s53, v245
v_mfma_f32_16x16x32_bf16 v[136:139], a[88:91], a[64:67], v[136:139]
v_mfma_f32_16x16x32_bf16 v[140:143], a[92:95], a[64:67], v[140:143]
ds_read_b64_tr_b16 a[116:117], v246 offset:8192
ds_read_b64_tr_b16 a[118:119], v246 offset:12288
v_mfma_f32_16x16x32_bf16 v[144:147], a[80:83], a[68:71], v[144:147]
ds_read_b128 a[100:103], v244 offset:3072
v_add_u32 v246, s53, v246
v_mfma_f32_16x16x32_bf16 v[148:151], a[84:87], a[68:71], v[148:151]
ds_read_b64_tr_b16 a[120:121], v247 offset:8192
ds_read_b64_tr_b16 a[122:123], v247 offset:12288
v_mfma_f32_16x16x32_bf16 v[152:155], a[88:91], a[68:71], v[152:155]
v_mfma_f32_16x16x32_bf16 v[156:159], a[92:95], a[68:71], v[156:159]
ds_read_b128 a[104:107], v244 offset:6144
v_add_u32 v247, s53, v247
v_mfma_f32_16x16x32_bf16 v[160:163], a[80:83], a[72:75], v[160:163]
ds_read_b64_tr_b16 a[124:125], v248 offset:8192
ds_read_b64_tr_b16 a[126:127], v248 offset:12288
v_mfma_f32_16x16x32_bf16 v[164:167], a[84:87], a[72:75], v[164:167]
ds_read_b128 a[108:111], v244 offset:7168
v_add_u32 v248, s53, v248
v_mfma_f32_16x16x32_bf16 v[168:171], a[88:91], a[72:75], v[168:171]
v_mfma_f32_16x16x32_bf16 v[172:175], a[92:95], a[72:75], v[172:175]
s_add_u32 s49, s46, s91
v_add_u32 v244, s53, v244
v_mfma_f32_16x16x32_bf16 v[176:179], a[80:83], a[76:79], v[176:179]
s_add_u32 s46, s46, s53
v_mfma_f32_16x16x32_bf16 v[180:183], a[84:87], a[76:79], v[180:183]
v_mfma_f32_16x16x32_bf16 v[184:187], a[88:91], a[76:79], v[184:187]
v_mfma_f32_16x16x32_bf16 v[188:191], a[92:95], a[76:79], v[188:191]
s_waitcnt vmcnt(8) lgkmcnt(0)
s_barrier
v_mfma_f32_16x16x32_bf16 v[128:131], a[112:115], a[96:99], v[128:131]
ds_read_b64_tr_b16 a[80:81], v245 offset:0
ds_read_b64_tr_b16 a[82:83], v245 offset:4096
v_mfma_f32_16x16x32_bf16 v[132:135], a[116:119], a[96:99], v[132:135]
ds_read_b128 a[64:67], v244 offset:0
v_mfma_f32_16x16x32_bf16 v[136:139], a[120:123], a[96:99], v[136:139]
v_mfma_f32_16x16x32_bf16 v[140:143], a[124:127], a[96:99], v[140:143]
ds_read_b64_tr_b16 a[84:85], v246 offset:0
ds_read_b64_tr_b16 a[86:87], v246 offset:4096
v_mfma_f32_16x16x32_bf16 v[144:147], a[112:115], a[100:103], v[144:147]
ds_read_b128 a[68:71], v244 offset:1024
v_mfma_f32_16x16x32_bf16 v[148:151], a[116:119], a[100:103], v[148:151]
ds_read_b64_tr_b16 a[88:89], v247 offset:0
ds_read_b64_tr_b16 a[90:91], v247 offset:4096
v_mfma_f32_16x16x32_bf16 v[152:155], a[120:123], a[100:103], v[152:155]
v_mfma_f32_16x16x32_bf16 v[156:159], a[124:127], a[100:103], v[156:159]
ds_read_b128 a[72:75], v244 offset:4096
v_mfma_f32_16x16x32_bf16 v[160:163], a[112:115], a[104:107], v[160:163]
ds_read_b64_tr_b16 a[92:93], v248 offset:0
ds_read_b64_tr_b16 a[94:95], v248 offset:4096
v_mfma_f32_16x16x32_bf16 v[164:167], a[116:119], a[104:107], v[164:167]
ds_read_b128 a[76:79], v244 offset:5120
v_mfma_f32_16x16x32_bf16 v[168:171], a[120:123], a[104:107], v[168:171]
v_mfma_f32_16x16x32_bf16 v[172:175], a[124:127], a[104:107], v[172:175]
s_cmp_eq_u32 s46, 0
s_cselect_b32 s53, s56, s57
v_mfma_f32_16x16x32_bf16 v[176:179], a[112:115], a[108:111], v[176:179]
v_mfma_f32_16x16x32_bf16 v[180:183], a[116:119], a[108:111], v[180:183]
v_mfma_f32_16x16x32_bf16 v[184:187], a[120:123], a[108:111], v[184:187]
v_mfma_f32_16x16x32_bf16 v[188:191], a[124:127], a[108:111], v[188:191]
s_waitcnt lgkmcnt(0)
v_mfma_f32_16x16x32_bf16 v[128:131], a[80:83], a[64:67], v[128:131]
ds_read_b64_tr_b16 a[112:113], v245 offset:8192
ds_read_b64_tr_b16 a[114:115], v245 offset:12288
v_mfma_f32_16x16x32_bf16 v[132:135], a[84:87], a[64:67], v[132:135]
ds_read_b128 a[96:99], v244 offset:2048
v_add_u32 v245, s53, v245
v_mfma_f32_16x16x32_bf16 v[136:139], a[88:91], a[64:67], v[136:139]
v_mfma_f32_16x16x32_bf16 v[140:143], a[92:95], a[64:67], v[140:143]
ds_read_b64_tr_b16 a[116:117], v246 offset:8192
ds_read_b64_tr_b16 a[118:119], v246 offset:12288
v_mfma_f32_16x16x32_bf16 v[144:147], a[80:83], a[68:71], v[144:147]
ds_read_b128 a[100:103], v244 offset:3072
v_add_u32 v246, s53, v246
v_mfma_f32_16x16x32_bf16 v[148:151], a[84:87], a[68:71], v[148:151]
ds_read_b64_tr_b16 a[120:121], v247 offset:8192
ds_read_b64_tr_b16 a[122:123], v247 offset:12288
v_mfma_f32_16x16x32_bf16 v[152:155], a[88:91], a[68:71], v[152:155]
v_mfma_f32_16x16x32_bf16 v[156:159], a[92:95], a[68:71], v[156:159]
ds_read_b128 a[104:107], v244 offset:6144
v_add_u32 v247, s53, v247
v_mfma_f32_16x16x32_bf16 v[160:163], a[80:83], a[72:75], v[160:163]
ds_read_b64_tr_b16 a[124:125], v248 offset:8192
ds_read_b64_tr_b16 a[126:127], v248 offset:12288
v_mfma_f32_16x16x32_bf16 v[164:167], a[84:87], a[72:75], v[164:167]
ds_read_b128 a[108:111], v244 offset:7168
v_add_u32 v248, s53, v248
v_mfma_f32_16x16x32_bf16 v[168:171], a[88:91], a[72:75], v[168:171]
v_mfma_f32_16x16x32_bf16 v[172:175], a[92:95], a[72:75], v[172:175]
s_add_u32 s49, s46, s91
v_add_u32 v244, s53, v244
v_mfma_f32_16x16x32_bf16 v[176:179], a[80:83], a[76:79], v[176:179]
s_add_u32 s46, s46, s53
v_mfma_f32_16x16x32_bf16 v[180:183], a[84:87], a[76:79], v[180:183]
v_mfma_f32_16x16x32_bf16 v[184:187], a[88:91], a[76:79], v[184:187]
v_mfma_f32_16x16x32_bf16 v[188:191], a[92:95], a[76:79], v[188:191]
s_waitcnt vmcnt(0) lgkmcnt(0)
s_barrier
v_mfma_f32_16x16x32_bf16 v[128:131], a[112:115], a[96:99], v[128:131]
ds_read_b64_tr_b16 a[80:81], v245 offset:0
ds_read_b64_tr_b16 a[82:83], v245 offset:4096
v_mfma_f32_16x16x32_bf16 v[132:135], a[116:119], a[96:99], v[132:135]
ds_read_b128 a[64:67], v244 offset:0
ds_read_b128 a[68:71], v244 offset:1024
v_mfma_f32_16x16x32_bf16 v[136:139], a[120:123], a[96:99], v[136:139]
ds_read_b64_tr_b16 a[84:85], v246 offset:0
ds_read_b64_tr_b16 a[86:87], v246 offset:4096
v_mfma_f32_16x16x32_bf16 v[140:143], a[124:127], a[96:99], v[140:143]
ds_read_b64_tr_b16 a[88:89], v247 offset:0
ds_read_b64_tr_b16 a[90:91], v247 offset:4096
v_mfma_f32_16x16x32_bf16 v[144:147], a[112:115], a[100:103], v[144:147]
ds_read_b64_tr_b16 a[92:93], v248 offset:0
ds_read_b64_tr_b16 a[94:95], v248 offset:4096
v_mfma_f32_16x16x32_bf16 v[148:151], a[116:119], a[100:103], v[148:151]
ds_read_b64_tr_b16 a[144:145], v245 offset:8192
ds_read_b64_tr_b16 a[146:147], v245 offset:12288
v_mfma_f32_16x16x32_bf16 v[152:155], a[120:123], a[100:103], v[152:155]
ds_read_b128 a[128:131], v244 offset:2048
ds_read_b128 a[132:135], v244 offset:3072
v_mfma_f32_16x16x32_bf16 v[156:159], a[124:127], a[100:103], v[156:159]
ds_read_b64_tr_b16 a[148:149], v246 offset:8192
ds_read_b64_tr_b16 a[150:151], v246 offset:12288
v_mfma_f32_16x16x32_bf16 v[160:163], a[112:115], a[104:107], v[160:163]
ds_read_b64_tr_b16 a[152:153], v247 offset:8192
ds_read_b64_tr_b16 a[154:155], v247 offset:12288
v_mfma_f32_16x16x32_bf16 v[164:167], a[116:119], a[104:107], v[164:167]
ds_read_b64_tr_b16 a[156:157], v248 offset:8192
ds_read_b64_tr_b16 a[158:159], v248 offset:12288
v_mfma_f32_16x16x32_bf16 v[168:171], a[120:123], a[104:107], v[168:171]
ds_read_b128 a[72:75], v244 offset:4096
ds_read_b128 a[76:79], v244 offset:5120
v_mfma_f32_16x16x32_bf16 v[172:175], a[124:127], a[104:107], v[172:175]
ds_read_b128 a[136:139], v244 offset:6144
ds_read_b128 a[140:143], v244 offset:7168
v_mfma_f32_16x16x32_bf16 v[176:179], a[112:115], a[108:111], v[176:179]
s_mov_b32 s24, 0
s_mov_b32 s25, 32768
v_mfma_f32_16x16x32_bf16 v[180:183], a[116:119], a[108:111], v[180:183]
s_mov_b32 s26, 65536
s_mov_b32 s27, 98304
v_mfma_f32_16x16x32_bf16 v[184:187], a[120:123], a[108:111], v[184:187]
s_mov_b32 s28, 131072
s_mov_b32 s29, 163840
v_mfma_f32_16x16x32_bf16 v[188:191], a[124:127], a[108:111], v[188:191]
s_mov_b32 s30, 196608
s_mov_b32 s31, 229376
s_waitcnt lgkmcnt(0)
v_mfma_f32_16x16x32_bf16 v[128:131], a[80:83], a[64:67], v[128:131]
v_mfma_f32_16x16x32_bf16 v[132:135], a[84:87], a[64:67], v[132:135]
v_mfma_f32_16x16x32_bf16 v[136:139], a[88:91], a[64:67], v[136:139]
v_mfma_f32_16x16x32_bf16 v[140:143], a[92:95], a[64:67], v[140:143]
v_mfma_f32_16x16x32_bf16 v[128:131], a[144:147], a[128:131], v[128:131]
v_mfma_f32_16x16x32_bf16 v[132:135], a[148:151], a[128:131], v[132:135]
v_mfma_f32_16x16x32_bf16 v[136:139], a[152:155], a[128:131], v[136:139]
v_mfma_f32_16x16x32_bf16 v[140:143], a[156:159], a[128:131], v[140:143]
v_mfma_f32_16x16x32_bf16 v[144:147], a[80:83], a[68:71], v[144:147]
v_mfma_f32_16x16x32_bf16 v[148:151], a[84:87], a[68:71], v[148:151]
v_mfma_f32_16x16x32_bf16 v[152:155], a[88:91], a[68:71], v[152:155]
s_nop 1
v_cvt_pk_bf16_f32 v192, v128, v129
v_cvt_pk_bf16_f32 v194, v132, v133
v_cvt_pk_bf16_f32 v193, v130, v131
v_mfma_f32_16x16x32_bf16 v[156:159], a[92:95], a[68:71], v[156:159]
v_cvt_pk_bf16_f32 v195, v134, v135
v_cvt_pk_bf16_f32 v196, v136, v137
v_cvt_pk_bf16_f32 v198, v140, v141
v_mfma_f32_16x16x32_bf16 v[144:147], a[144:147], a[132:135], v[144:147]
v_cvt_pk_bf16_f32 v197, v138, v139
v_cvt_pk_bf16_f32 v199, v142, v143
v_mfma_f32_16x16x32_bf16 v[148:151], a[148:151], a[132:135], v[148:151]
v_permlane16_swap_b32 v192, v194
v_mfma_f32_16x16x32_bf16 v[152:155], a[152:155], a[132:135], v[152:155]
v_permlane16_swap_b32 v193, v195
v_mfma_f32_16x16x32_bf16 v[156:159], a[156:159], a[132:135], v[156:159]
v_permlane16_swap_b32 v196, v198
v_mfma_f32_16x16x32_bf16 v[160:163], a[80:83], a[72:75], v[160:163]
v_permlane16_swap_b32 v197, v199
v_mov_b32 v200, v192
v_mfma_f32_16x16x32_bf16 v[164:167], a[84:87], a[72:75], v[164:167]
v_mov_b32 v201, v193
v_mov_b32 v202, v194
v_mov_b32 v203, v195
v_mfma_f32_16x16x32_bf16 v[168:171], a[88:91], a[72:75], v[168:171]
v_mov_b32_dpp v192, v196 row_ror:8 row_mask:0xf bank_mask:0xc
v_mov_b32_dpp v193, v197 row_ror:8 row_mask:0xf bank_mask:0xc
v_mov_b32_dpp v194, v198 row_ror:8 row_mask:0xf bank_mask:0xc
v_mfma_f32_16x16x32_bf16 v[172:175], a[92:95], a[72:75], v[172:175]
v_mov_b32_dpp v195, v199 row_ror:8 row_mask:0xf bank_mask:0xc
v_mfma_f32_16x16x32_bf16 v[160:163], a[144:147], a[136:139], v[160:163]
buffer_store_dwordx4 v[192:195], v251, s[16:19], s24 offen nt
v_mov_b32_dpp v196, v200 row_ror:8 row_mask:0xf bank_mask:0x3
v_mov_b32_dpp v197, v201 row_ror:8 row_mask:0xf bank_mask:0x3
v_mfma_f32_16x16x32_bf16 v[164:167], a[148:151], a[136:139], v[164:167]
v_mov_b32_dpp v198, v202 row_ror:8 row_mask:0xf bank_mask:0x3
v_mov_b32_dpp v199, v203 row_ror:8 row_mask:0xf bank_mask:0x3
v_mfma_f32_16x16x32_bf16 v[168:171], a[152:155], a[136:139], v[168:171]
buffer_store_dwordx4 v[196:199], v251, s[16:19], s25 offen nt
v_cvt_pk_bf16_f32 v204, v144, v145
v_cvt_pk_bf16_f32 v206, v148, v149
v_mfma_f32_16x16x32_bf16 v[172:175], a[156:159], a[136:139], v[172:175]
v_cvt_pk_bf16_f32 v205, v146, v147
v_cvt_pk_bf16_f32 v207, v150, v151
v_cvt_pk_bf16_f32 v208, v152, v153
v_mfma_f32_16x16x32_bf16 v[176:179], a[80:83], a[76:79], v[176:179]
v_cvt_pk_bf16_f32 v210, v156, v157
v_cvt_pk_bf16_f32 v209, v154, v155
v_cvt_pk_bf16_f32 v211, v158, v159
v_mfma_f32_16x16x32_bf16 v[180:183], a[84:87], a[76:79], v[180:183]
v_permlane16_swap_b32 v204, v206
v_mfma_f32_16x16x32_bf16 v[184:187], a[88:91], a[76:79], v[184:187]
v_permlane16_swap_b32 v205, v207
v_mfma_f32_16x16x32_bf16 v[188:191], a[92:95], a[76:79], v[188:191]
v_permlane16_swap_b32 v208, v210
v_mfma_f32_16x16x32_bf16 v[176:179], a[144:147], a[140:143], v[176:179]
v_permlane16_swap_b32 v209, v211
v_mov_b32 v212, v204
v_mfma_f32_16x16x32_bf16 v[180:183], a[148:151], a[140:143], v[180:183]
v_mov_b32 v213, v205
v_mov_b32 v214, v206
v_mov_b32 v215, v207
v_mfma_f32_16x16x32_bf16 v[184:187], a[152:155], a[140:143], v[184:187]
v_mov_b32_dpp v204, v208 row_ror:8 row_mask:0xf bank_mask:0xc
v_mov_b32_dpp v205, v209 row_ror:8 row_mask:0xf bank_mask:0xc
v_mov_b32_dpp v206, v210 row_ror:8 row_mask:0xf bank_mask:0xc
v_mfma_f32_16x16x32_bf16 v[188:191], a[156:159], a[140:143], v[188:191]
v_mov_b32_dpp v207, v211 row_ror:8 row_mask:0xf bank_mask:0xc
buffer_store_dwordx4 v[204:207], v251, s[16:19], s26 offen nt
v_mov_b32_dpp v208, v212 row_ror:8 row_mask:0xf bank_mask:0x3
v_mov_b32_dpp v209, v213 row_ror:8 row_mask:0xf bank_mask:0x3
v_mov_b32_dpp v210, v214 row_ror:8 row_mask:0xf bank_mask:0x3
v_mov_b32_dpp v211, v215 row_ror:8 row_mask:0xf bank_mask:0x3
buffer_store_dwordx4 v[208:211], v251, s[16:19], s27 offen nt
v_cvt_pk_bf16_f32 v192, v160, v161
v_cvt_pk_bf16_f32 v194, v164, v165
v_cvt_pk_bf16_f32 v193, v162, v163
v_cvt_pk_bf16_f32 v195, v166, v167
v_cvt_pk_bf16_f32 v196, v168, v169
v_cvt_pk_bf16_f32 v198, v172, v173
v_cvt_pk_bf16_f32 v197, v170, v171
v_cvt_pk_bf16_f32 v199, v174, v175
v_permlane16_swap_b32 v192, v194
v_permlane16_swap_b32 v193, v195
v_permlane16_swap_b32 v196, v198
v_permlane16_swap_b32 v197, v199
v_mov_b32 v200, v192
v_mov_b32 v201, v193
v_mov_b32 v202, v194
v_mov_b32 v203, v195
v_mov_b32_dpp v192, v196 row_ror:8 row_mask:0xf bank_mask:0xc
v_mov_b32_dpp v193, v197 row_ror:8 row_mask:0xf bank_mask:0xc
v_mov_b32_dpp v194, v198 row_ror:8 row_mask:0xf bank_mask:0xc
v_mov_b32_dpp v195, v199 row_ror:8 row_mask:0xf bank_mask:0xc
buffer_store_dwordx4 v[192:195], v251, s[16:19], s28 offen nt
v_mov_b32_dpp v196, v200 row_ror:8 row_mask:0xf bank_mask:0x3
v_mov_b32_dpp v197, v201 row_ror:8 row_mask:0xf bank_mask:0x3
v_mov_b32_dpp v198, v202 row_ror:8 row_mask:0xf bank_mask:0x3
v_mov_b32_dpp v199, v203 row_ror:8 row_mask:0xf bank_mask:0x3
buffer_store_dwordx4 v[196:199], v251, s[16:19], s29 offen nt
v_cvt_pk_bf16_f32 v204, v176, v177
v_cvt_pk_bf16_f32 v206, v180, v181
v_cvt_pk_bf16_f32 v205, v178, v179
v_cvt_pk_bf16_f32 v207, v182, v183
v_cvt_pk_bf16_f32 v208, v184, v185
v_cvt_pk_bf16_f32 v210, v188, v189
v_cvt_pk_bf16_f32 v209, v186, v187
v_cvt_pk_bf16_f32 v211, v190, v191
v_permlane16_swap_b32 v204, v206
v_permlane16_swap_b32 v205, v207
v_permlane16_swap_b32 v208, v210
v_permlane16_swap_b32 v209, v211
v_mov_b32 v212, v204
v_mov_b32 v213, v205
v_mov_b32 v214, v206
v_mov_b32 v215, v207
v_mov_b32_dpp v204, v208 row_ror:8 row_mask:0xf bank_mask:0xc
v_mov_b32_dpp v205, v209 row_ror:8 row_mask:0xf bank_mask:0xc
v_mov_b32_dpp v206, v210 row_ror:8 row_mask:0xf bank_mask:0xc
v_mov_b32_dpp v207, v211 row_ror:8 row_mask:0xf bank_mask:0xc
buffer_store_dwordx4 v[204:207], v251, s[16:19], s30 offen nt
v_mov_b32_dpp v208, v212 row_ror:8 row_mask:0xf bank_mask:0x3
v_mov_b32_dpp v209, v213 row_ror:8 row_mask:0xf bank_mask:0x3
v_mov_b32_dpp v210, v214 row_ror:8 row_mask:0xf bank_mask:0x3
v_mov_b32_dpp v211, v215 row_ror:8 row_mask:0xf bank_mask:0x3
buffer_store_dwordx4 v[208:211], v251, s[16:19], s31 offen nt
.Lend:
s_endpgm
.Lspin1:
s_mov_b32 s93, 0
.Lspin1_l:
s_sleep 4
buffer_load_dword v8, v3, s[60:63], s90 offen sc1
s_waitcnt vmcnt(0)
v_and_b32 v0, 7, v8
v_cmp_lt_u32 s[28:29], v0, v14
v_cmp_eq_u32 s[30:31], v8, v2
s_nop 0
s_or_b64 s[28:29], s[28:29], s[30:31]
s_cbranch_scc0 .Lspin1_ret
s_add_u32 s93, s93, 1
s_cmp_lt_u32 s93, 0x100000
s_cbranch_scc1 .Lspin1_l
.Lpad24:
s_nop 0
s_branch .Lspin1_ret
.Lj0_pad:
s_cbranch_execz .Ltch2
.Lj0a:
v_cmp_gt_i32 s[32:33], 0, v8
v_nop
v_cmp_gt_i32 s[34:35], 1, v8
v_cndmask_b32 v160, v160, v34, s[32:33]
v_cmp_gt_i32 s[32:33], 2, v8
v_cndmask_b32 v161, v161, v34, s[34:35]
v_cmp_gt_i32 s[34:35], 3, v8
v_cndmask_b32 v162, v162, v34, s[32:33]
v_cmp_gt_i32 s[32:33], 16, v8
v_cndmask_b32 v163, v163, v34, s[34:35]
v_cmp_gt_i32 s[34:35], 17, v8
v_cndmask_b32 v164, v164, v34, s[32:33]
v_cmp_gt_i32 s[32:33], 18, v8
v_cndmask_b32 v165, v165, v34, s[34:35]
v_cmp_gt_i32 s[34:35], 19, v8
v_cndmask_b32 v166, v166, v34, s[32:33]
v_cmp_gt_i32 s[32:33], -16, v8
v_cndmask_b32 v167, v167, v34, s[34:35]
v_cmp_gt_i32 s[34:35], -15, v8
v_cndmask_b32 v168, v168, v34, s[32:33]
v_cmp_gt_i32 s[32:33], -14, v8
v_cndmask_b32 v169, v169, v34, s[34:35]
v_cmp_gt_i32 s[34:35], -13, v8
v_cndmask_b32 v170, v170, v34, s[32:33]
v_cmp_gt_i32 s[32:33], 0, v8
v_cndmask_b32 v171, v171, v34, s[34:35]
v_cmp_gt_i32 s[34:35], 1, v8
v_cndmask_b32 v172, v172, v34, s[32:33]
v_cmp_gt_i32 s[32:33], 2, v8
v_cndmask_b32 v173, v173, v34, s[34:35]
v_cmp_gt_i32 s[34:35], 3, v8
v_cndmask_b32 v174, v174, v34, s[32:33]
v_nop
v_cndmask_b32 v175, v175, v34, s[34:35]
s_branch .Lj0a_ret
.Lj0b:
s_cmp_eq_u32 s51, 1
s_cbranch_scc1 .Lj0b1
v_mov_b32 v176, v34
v_mov_b32 v177, v34
v_mov_b32 v178, v34
v_mov_b32 v179, v34
v_mov_b32 v180, v34
v_mov_b32 v181, v34
v_mov_b32 v182, v34
v_mov_b32 v183, v34
v_mov_b32 v184, v34
v_mov_b32 v185, v34
v_mov_b32 v186, v34
v_mov_b32 v187, v34
v_mov_b32 v188, v34
v_mov_b32 v189, v34
v_mov_b32 v190, v34
v_mov_b32 v191, v34
s_branch .Lj0b_ret
.Lj0b1:
v_cmp_gt_i32 s[32:33], 0, v8
v_nop
v_cmp_gt_i32 s[34:35], 1, v8
v_cndmask_b32 v176, v176, v34, s[32:33]
v_cmp_gt_i32 s[32:33], 2, v8
v_cndmask_b32 v177, v177, v34, s[34:35]
v_cmp_gt_i32 s[34:35], 3, v8
v_cndmask_b32 v178, v178, v34, s[32:33]
v_cmp_gt_i32 s[32:33], 16, v8
v_cndmask_b32 v179, v179, v34, s[34:35]
v_cmp_gt_i32 s[34:35], 17, v8
v_cndmask_b32 v180, v180, v34, s[32:33]
v_cmp_gt_i32 s[32:33], 18, v8
v_cndmask_b32 v181, v181, v34, s[34:35]
v_cmp_gt_i32 s[34:35], 19, v8
v_cndmask_b32 v182, v182, v34, s[32:33]
v_cmp_gt_i32 s[32:33], -16, v8
v_cndmask_b32 v183, v183, v34, s[34:35]
v_cmp_gt_i32 s[34:35], -15, v8
v_cndmask_b32 v184, v184, v34, s[32:33]
v_cmp_gt_i32 s[32:33], -14, v8
v_cndmask_b32 v185, v185, v34, s[34:35]
v_cmp_gt_i32 s[34:35], -13, v8
v_cndmask_b32 v186, v186, v34, s[32:33]
v_cmp_gt_i32 s[32:33], 0, v8
v_cndmask_b32 v187, v187, v34, s[34:35]
v_cmp_gt_i32 s[34:35], 1, v8
v_cndmask_b32 v188, v188, v34, s[32:33]
v_cmp_gt_i32 s[32:33], 2, v8
v_cndmask_b32 v189, v189, v34, s[34:35]
v_cmp_gt_i32 s[34:35], 3, v8
v_cndmask_b32 v190, v190, v34, s[32:33]
v_nop
v_cndmask_b32 v191, v191, v34, s[34:35]
s_branch .Lj0b_ret
.Lj0c:
s_cmp_eq_u32 s51, 2
s_cbranch_scc1 .Lj0c1
v_mov_b32 v160, v34
v_mov_b32 v161, v34
v_mov_b32 v162, v34
v_mov_b32 v163, v34
v_mov_b32 v164, v34
v_mov_b32 v165, v34
v_mov_b32 v166, v34
v_mov_b32 v167, v34
v_mov_b32 v168, v34
v_mov_b32 v169, v34
v_mov_b32 v170, v34
v_mov_b32 v171, v34
v_mov_b32 v172, v34
v_mov_b32 v173, v34
v_mov_b32 v174, v34
v_mov_b32 v175, v34
s_branch .LT1
.Lj0c1:
v_cmp_gt_i32 s[32:33], 0, v8
v_nop
v_cmp_gt_i32 s[34:35], 1, v8
v_cndmask_b32 v160, v160, v34, s[32:33]
v_cmp_gt_i32 s[32:33], 2, v8
v_cndmask_b32 v161, v161, v34, s[34:35]
v_cmp_gt_i32 s[34:35], 3, v8
v_cndmask_b32 v162, v162, v34, s[32:33]
v_cmp_gt_i32 s[32:33], 16, v8
v_cndmask_b32 v163, v163, v34, s[34:35]
v_cmp_gt_i32 s[34:35], 17, v8
v_cndmask_b32 v164, v164, v34, s[32:33]
v_cmp_gt_i32 s[32:33], 18, v8
v_cndmask_b32 v165, v165, v34, s[34:35]
v_cmp_gt_i32 s[34:35], 19, v8
v_cndmask_b32 v166, v166, v34, s[32:33]
v_cmp_gt_i32 s[32:33], -16, v8
v_cndmask_b32 v167, v167, v34, s[34:35]
v_cmp_gt_i32 s[34:35], -15, v8
v_cndmask_b32 v168, v168, v34, s[32:33]
v_cmp_gt_i32 s[32:33], -14, v8
v_cndmask_b32 v169, v169, v34, s[34:35]
v_cmp_gt_i32 s[34:35], -13, v8
v_cndmask_b32 v170, v170, v34, s[32:33]
v_cmp_gt_i32 s[32:33], 0, v8
v_cndmask_b32 v171, v171, v34, s[34:35]
v_cmp_gt_i32 s[34:35], 1, v8
v_cndmask_b32 v172, v172, v34, s[32:33]
v_cmp_gt_i32 s[32:33], 2, v8
v_cndmask_b32 v173, v173, v34, s[34:35]
v_cmp_gt_i32 s[34:35], 3, v8
v_cndmask_b32 v174, v174, v34, s[32:33]
v_nop
v_cndmask_b32 v175, v175, v34, s[34:35]
s_branch .LT1
.Lool_pad:
s_nop 0
.Lev_d1:
s_cmp_eq_u32 s43, s42
s_cbranch_scc1 .LT0
s_swappc_b64 s[30:31], s[58:59]
.Lev_d1_r:
s_branch .LD0
.Lev_s1:
s_cmp_eq_u32 s43, s42
s_cbranch_scc1 .LE0
s_swappc_b64 s[30:31], s[58:59]
.Lev_s1_r:
s_branch .LS0
.Lspin22:
s_mov_b32 s93, 0
.Lspin22_l:
s_sleep 4
buffer_load_dword v9, v10, s[60:63], s90 offen sc1
s_waitcnt vmcnt(0)
v_and_b32 v9, 7, v9
v_cmp_gt_u32 vcc, 2, v9
s_cmp_lg_u64 vcc, 0
s_cbranch_scc0 .Lspin22_ret
s_add_u32 s93, s93, 1
s_cmp_lt_u32 s93, 0x100000
s_cbranch_scc1 .Lspin22_l
s_branch .Lspin22_ret
.Lspin21:
s_mov_b32 s93, 0
.Lspin21_l:
s_sleep 4
buffer_load_dword v9, v10, s[60:63], 0 offen sc1
s_waitcnt vmcnt(0)
v_cmp_gt_u32 vcc, 1, v9
s_cmp_lg_u64 vcc, 0
s_cbranch_scc0 .Lspin21_ret
s_add_u32 s93, s93, 1
s_cmp_lt_u32 s93, 0x100000
s_cbranch_scc1 .Lspin21_l
s_branch .Lspin21_ret
.Ljob0:
s_waitcnt vmcnt(8)
v_mov_b32 v0, 1
buffer_store_dword v0, v5, s[60:63], 0 offen sc1
v_and_b32 v10, 7, v1
v_and_b32 v9, 8, v1
v_lshl_or_b32 v10, v9, 1, v10
s_and_b32 s93, s87, 2
s_lshl_b32 s93, s93, 2
v_or_b32 v10, s93, v10
v_lshlrev_b32 v10, 8, v10
s_lshl_b32 s93, s86, 5
v_add_u32 v10, s93, v10
buffer_load_dword v9, v10, s[60:63], s90 offen sc1
s_mov_b32 s57, 3
s_add_u32 s93, s43, 1
s_max_u32 s43, s57, s93
s_min_u32 s43, s43, s42
s_sub_u32 s55, s43, s93
s_swappc_b64 s[58:59], s[30:31]
.Ljob1:
s_waitcnt vmcnt(4)
v_and_b32 v9, 7, v9
v_cmp_gt_u32 vcc, 2, v9
s_cmp_lg_u64 vcc, 0
s_cbranch_scc1 .Lspin22
.Lspin22_ret:
s_mov_b32 s57, 12
s_add_u32 s93, s43, 1
s_max_u32 s43, s57, s93
s_min_u32 s43, s43, s42
s_sub_u32 s55, s43, s93
s_swappc_b64 s[58:59], s[30:31]
.Ljob2:
v_and_b32 v10, 7, v1
v_and_b32 v9, 8, v1
v_lshl_or_b32 v10, v9, 1, v10
s_and_b32 s93, s87, 2
s_lshl_b32 s93, s93, 2
v_or_b32 v10, s93, v10
v_lshlrev_b32 v10, 9, v10
v_bfe_u32 v9, v1, 4, 1
v_lshl_or_b32 v10, v9, 2, v10
s_lshl_b32 s93, s86, 6
v_add_u32 v10, s93, v10
s_and_b32 s93, s87, 1
s_lshl_b32 s93, s93, 3
s_add_u32 s93, s93, 0x10000
v_add_u32 v10, s93, v10
buffer_load_dword v9, v10, s[60:63], 0 offen sc1
s_mov_b32 s57, 14
s_add_u32 s93, s43, 1
s_max_u32 s43, s57, s93
s_min_u32 s43, s43, s42
s_sub_u32 s55, s43, s93
s_swappc_b64 s[58:59], s[30:31]
.Ljob3:
s_waitcnt vmcnt(4)
v_cmp_gt_u32 vcc, 1, v9
s_cmp_lg_u64 vcc, 0
s_cbranch_scc1 .Lspin21
.Lspin21_ret:
s_cmp_eq_u32 s87, 8
s_cselect_b32 s0, 44, 0x7fffffff
s_mov_b32 s57, s0
s_add_u32 s93, s43, 1
s_max_u32 s43, s57, s93
s_min_u32 s43, s43, s42
s_sub_u32 s55, s43, s93
s_swappc_b64 s[58:59], s[30:31]
.Ljob4:
s_memrealtime s[100:101]
s_lshl_b32 s93, s87, 2
s_add_u32 s93, s93, s3
s_xor_b32 s93, s93, 32
s_lshl_b32 s93, s93, 20
s_lshl_b32 s0, s86, 2
s_add_u32 s93, s93, s0
s_add_u32 s93, s93, 0x14000
v_lshlrev_b32 v9, 7, v1
v_add_u32 v10, s93, v9
s_waitcnt lgkmcnt(0)
s_and_b32 s100, s100, -16
s_or_b32 s100, s100, s86
s_or_b32 s100, s100, 8
v_mov_b32 v0, s100
buffer_store_dword v0, v10, s[60:63], 0 offen sc1
s_mov_b32 s57, 2147483647
s_add_u32 s93, s43, 1
s_max_u32 s43, s57, s93
s_min_u32 s43, s43, s42
s_sub_u32 s55, s43, s93
s_swappc_b64 s[58:59], s[30:31]
.Lx:
s_sub_u32 s4, s4, s91
s_subb_u32 s5, s5, 0
s_mov_b32 s6, 0x80000
s_sub_u32 s8, s8, s91
s_subb_u32 s9, s9, 0
s_mov_b32 s10, 0x84000
s_mov_b64 s[38:39], s[52:53]
s_cmp_eq_u64 s[30:31], 0
s_cbranch_scc1 .Lx_s0
v_mov_b32 v224, v64
v_mov_b32 v225, v65
v_mov_b32 v226, v66
v_mov_b32 v227, v67
v_mov_b32 v228, v68
v_mov_b32 v229, v69
v_mov_b32 v230, v70
v_mov_b32 v231, v71
v_mov_b32 v232, v72
v_mov_b32 v233, v73
v_mov_b32 v234, v74
v_mov_b32 v235, v75
v_mov_b32 v236, v76
v_mov_b32 v237, v77
v_mov_b32 v238, v78
v_mov_b32 v239, v79
v_mov_b32 v240, v80
v_mov_b32 v241, v81
v_mov_b32 v242, v82
v_mov_b32 v243, v83
v_mov_b32 v244, v84
v_mov_b32 v245, v85
v_mov_b32 v246, v86
v_mov_b32 v247, v87
v_mov_b32 v248, v88
v_mov_b32 v249, v89
v_mov_b32 v250, v90
v_mov_b32 v251, v91
v_mov_b32 v252, v92
v_mov_b32 v253, v93
v_mov_b32 v254, v94
v_mov_b32 v255, v95
s_sub_u32 s50, 15, s89
s_lshl_b32 s50, s50, 2
s_add_u32 s50, s50, s3
s_getpc_b64 s[42:43]
.Lx_p0:
s_add_u32 s42, s42, .Lx_r0-.Lx_p0
s_addc_u32 s43, s43, 0
s_branch .Lx_core
.Lx_r0:
v_accvgpr_write_b32 a64, v128
v_accvgpr_write_b32 a65, v129
v_accvgpr_write_b32 a66, v130
v_accvgpr_write_b32 a67, v131
v_accvgpr_write_b32 a68, v132
v_accvgpr_write_b32 a69, v133
v_accvgpr_write_b32 a70, v134
v_accvgpr_write_b32 a71, v135
v_accvgpr_write_b32 a72, v136
v_accvgpr_write_b32 a73, v137
v_accvgpr_write_b32 a74, v138
v_accvgpr_write_b32 a75, v139
v_accvgpr_write_b32 a76, v140
v_accvgpr_write_b32 a77, v141
v_accvgpr_write_b32 a78, v142
v_accvgpr_write_b32 a79, v143
v_accvgpr_write_b32 a80, v144
v_accvgpr_write_b32 a81, v145
v_accvgpr_write_b32 a82, v146
v_accvgpr_write_b32 a83, v147
v_accvgpr_write_b32 a84, v148
v_accvgpr_write_b32 a85, v149
v_accvgpr_write_b32 a86, v150
v_accvgpr_write_b32 a87, v151
v_accvgpr_write_b32 a88, v152
v_accvgpr_write_b32 a89, v153
v_accvgpr_write_b32 a90, v154
v_accvgpr_write_b32 a91, v155
v_accvgpr_write_b32 a92, v156
v_accvgpr_write_b32 a93, v157
v_accvgpr_write_b32 a94, v158
v_accvgpr_write_b32 a95, v159
v_accvgpr_write_b32 a112, v160
v_accvgpr_write_b32 a113, v161
v_accvgpr_write_b32 a114, v162
v_accvgpr_write_b32 a115, v163
v_accvgpr_write_b32 a116, v164
v_accvgpr_write_b32 a117, v165
v_accvgpr_write_b32 a118, v166
v_accvgpr_write_b32 a119, v167
v_accvgpr_write_b32 a120, v168
v_accvgpr_write_b32 a121, v169
v_accvgpr_write_b32 a122, v170
v_accvgpr_write_b32 a123, v171
v_accvgpr_write_b32 a124, v172
v_accvgpr_write_b32 a125, v173
v_accvgpr_write_b32 a126, v174
v_accvgpr_write_b32 a127, v175
v_accvgpr_write_b32 a144, v176
v_accvgpr_write_b32 a145, v177
v_accvgpr_write_b32 a146, v178
v_accvgpr_write_b32 a147, v179
v_accvgpr_write_b32 a148, v180
v_accvgpr_write_b32 a149, v181
v_accvgpr_write_b32 a150, v182
v_accvgpr_write_b32 a151, v183
v_accvgpr_write_b32 a152, v184
v_accvgpr_write_b32 a153, v185
v_accvgpr_write_b32 a154, v186
v_accvgpr_write_b32 a155, v187
v_accvgpr_write_b32 a156, v188
v_accvgpr_write_b32 a157, v189
v_accvgpr_write_b32 a158, v190
v_accvgpr_write_b32 a159, v191
v_mov_b32 v208, v56
v_mov_b32 v212, v57
.Lx_s0:
s_cmp_eq_u64 s[38:39], 0
s_cbranch_scc1 .Lx_s1
v_mov_b32 v224, v96
v_mov_b32 v225, v97
v_mov_b32 v226, v98
v_mov_b32 v227, v99
v_mov_b32 v228, v100
v_mov_b32 v229, v101
v_mov_b32 v230, v102
v_mov_b32 v231, v103
v_mov_b32 v232, v104
v_mov_b32 v233, v105
v_mov_b32 v234, v106
v_mov_b32 v235, v107
v_mov_b32 v236, v108
v_mov_b32 v237, v109
v_mov_b32 v238, v110
v_mov_b32 v239, v111
v_mov_b32 v240, v112
v_mov_b32 v241, v113
v_mov_b32 v242, v114
v_mov_b32 v243, v115
v_mov_b32 v244, v116
v_mov_b32 v245, v117
v_mov_b32 v246, v118
v_mov_b32 v247, v119
v_mov_b32 v248, v120
v_mov_b32 v249, v121
v_mov_b32 v250, v122
v_mov_b32 v251, v123
v_mov_b32 v252, v124
v_mov_b32 v253, v125
v_mov_b32 v254, v126
v_mov_b32 v255, v127
s_mov_b32 s50, s89
s_lshl_b32 s50, s50, 2
s_add_u32 s50, s50, s94
s_getpc_b64 s[42:43]
.Lx_p1:
s_add_u32 s42, s42, .Lx_r1-.Lx_p1
s_addc_u32 s43, s43, 0
s_branch .Lx_core
.Lx_r1:
v_accvgpr_write_b32 a176, v128
v_accvgpr_write_b32 a177, v129
v_accvgpr_write_b32 a178, v130
v_accvgpr_write_b32 a179, v131
v_accvgpr_write_b32 a180, v132
v_accvgpr_write_b32 a181, v133
v_accvgpr_write_b32 a182, v134
v_accvgpr_write_b32 a183, v135
v_accvgpr_write_b32 a184, v136
v_accvgpr_write_b32 a185, v137
v_accvgpr_write_b32 a186, v138
v_accvgpr_write_b32 a187, v139
v_accvgpr_write_b32 a188, v140
v_accvgpr_write_b32 a189, v141
v_accvgpr_write_b32 a190, v142
v_accvgpr_write_b32 a191, v143
v_accvgpr_write_b32 a208, v144
v_accvgpr_write_b32 a209, v145
v_accvgpr_write_b32 a210, v146
v_accvgpr_write_b32 a211, v147
v_accvgpr_write_b32 a212, v148
v_accvgpr_write_b32 a213, v149
v_accvgpr_write_b32 a214, v150
v_accvgpr_write_b32 a215, v151
v_accvgpr_write_b32 a216, v152
v_accvgpr_write_b32 a217, v153
v_accvgpr_write_b32 a218, v154
v_accvgpr_write_b32 a219, v155
v_accvgpr_write_b32 a220, v156
v_accvgpr_write_b32 a221, v157
v_accvgpr_write_b32 a222, v158
v_accvgpr_write_b32 a223, v159
v_accvgpr_write_b32 a240, v160
v_accvgpr_write_b32 a241, v161
v_accvgpr_write_b32 a242, v162
v_accvgpr_write_b32 a243, v163
v_accvgpr_write_b32 a244, v164
v_accvgpr_write_b32 a245, v165
v_accvgpr_write_b32 a246, v166
v_accvgpr_write_b32 a247, v167
v_accvgpr_write_b32 a248, v168
v_accvgpr_write_b32 a249, v169
v_accvgpr_write_b32 a250, v170
v_accvgpr_write_b32 a251, v171
v_accvgpr_write_b32 a252, v172
v_accvgpr_write_b32 a253, v173
v_accvgpr_write_b32 a254, v174
v_accvgpr_write_b32 a255, v175
v_accvgpr_write_b32 a96, v176
v_accvgpr_write_b32 a97, v177
v_accvgpr_write_b32 a98, v178
v_accvgpr_write_b32 a99, v179
v_accvgpr_write_b32 a100, v180
v_accvgpr_write_b32 a101, v181
v_accvgpr_write_b32 a102, v182
v_accvgpr_write_b32 a103, v183
v_accvgpr_write_b32 a104, v184
v_accvgpr_write_b32 a105, v185
v_accvgpr_write_b32 a106, v186
v_accvgpr_write_b32 a107, v187
v_accvgpr_write_b32 a108, v188
v_accvgpr_write_b32 a109, v189
v_accvgpr_write_b32 a110, v190
v_accvgpr_write_b32 a111, v191
v_mov_b32 v216, v56
v_mov_b32 v220, v57
.Lx_s1:
s_branch .Lx_ret
.Lx_core:
s_lshl_b32 s51, s50, 5
v_add_u32 v58, s51, v8
s_mov_b32 s44, 0
s_mov_b32 s45, 16384
s_mov_b32 s46, 0
v_mov_b32 v24, v34
v_mov_b32 v25, v34
v_mov_b32 v56, 0
v_mov_b32 v57, 0
v_mov_b32 v128, 0
v_mov_b32 v129, 0
v_mov_b32 v130, 0
v_mov_b32 v131, 0
v_mov_b32 v132, 0
v_mov_b32 v133, 0
v_mov_b32 v134, 0
v_mov_b32 v135, 0
v_mov_b32 v136, 0
v_mov_b32 v137, 0
v_mov_b32 v138, 0
v_mov_b32 v139, 0
v_mov_b32 v140, 0
v_mov_b32 v141, 0
v_mov_b32 v142, 0
v_mov_b32 v143, 0
v_mov_b32 v144, 0
v_mov_b32 v145, 0
v_mov_b32 v146, 0
v_mov_b32 v147, 0
v_mov_b32 v148, 0
v_mov_b32 v149, 0
v_mov_b32 v150, 0
v_mov_b32 v151, 0
v_mov_b32 v152, 0
v_mov_b32 v153, 0
v_mov_b32 v154, 0
v_mov_b32 v155, 0
v_mov_b32 v156, 0
v_mov_b32 v157, 0
v_mov_b32 v158, 0
v_mov_b32 v159, 0
v_mov_b32 v160, 0
v_mov_b32 v161, 0
v_mov_b32 v162, 0
v_mov_b32 v163, 0
v_mov_b32 v164, 0
v_mov_b32 v165, 0
v_mov_b32 v166, 0
v_mov_b32 v167, 0
v_mov_b32 v168, 0
v_mov_b32 v169, 0
v_mov_b32 v170, 0
v_mov_b32 v171, 0
v_mov_b32 v172, 0
v_mov_b32 v173, 0
v_mov_b32 v174, 0
v_mov_b32 v175, 0
v_mov_b32 v176, 0
v_mov_b32 v177, 0
v_mov_b32 v178, 0
v_mov_b32 v179, 0
v_mov_b32 v180, 0
v_mov_b32 v181, 0
v_mov_b32 v182, 0
v_mov_b32 v183, 0
v_mov_b32 v184, 0
v_mov_b32 v185, 0
v_mov_b32 v186, 0
v_mov_b32 v187, 0
v_mov_b32 v188, 0
v_mov_b32 v189, 0
v_mov_b32 v190, 0
v_mov_b32 v191, 0
.Lx_loop:
buffer_load_dwordx4 a[0:3], v7, s[4:7], s44 offen offset:0 sc1
buffer_load_dwordx4 a[4:7], v51, s[4:7], s44 offen offset:0 sc1
buffer_load_dwordx4 a[8:11], v7, s[4:7], s44 offen offset:1024 sc1
buffer_load_dwordx4 a[12:15], v51, s[4:7], s44 offen offset:1024 sc1
buffer_load_dwordx4 a[16:19], v7, s[4:7], s44 offen offset:2048 sc1
buffer_load_dwordx4 a[20:23], v51, s[4:7], s44 offen offset:2048 sc1
buffer_load_dwordx4 a[24:27], v7, s[4:7], s44 offen offset:3072 sc1
buffer_load_dwordx4 a[28:31], v51, s[4:7], s44 offen offset:3072 sc1
buffer_load_dwordx4 a[32:35], v7, s[8:11], s45 offen offset:0 sc1
buffer_load_dwordx4 a[36:39], v7, s[8:11], s45 offen offset:1024 sc1
buffer_load_dwordx4 a[40:43], v7, s[8:11], s45 offen offset:2048 sc1
buffer_load_dwordx4 a[44:47], v7, s[8:11], s45 offen offset:3072 sc1
buffer_load_dwordx4 a[48:51], v51, s[8:11], s45 offen offset:0 sc1
buffer_load_dwordx4 a[52:55], v51, s[8:11], s45 offen offset:1024 sc1
buffer_load_dwordx4 a[56:59], v51, s[8:11], s45 offen offset:2048 sc1
buffer_load_dwordx4 a[60:63], v51, s[8:11], s45 offen offset:3072 sc1
s_waitcnt vmcnt(8)
v_mfma_f32_16x16x32_bf16 v[192:195], a[0:3], v[224:227], 0
v_mfma_f32_16x16x32_bf16 v[200:203], a[0:3], v[240:243], 0
v_mfma_f32_16x16x32_bf16 v[196:199], a[4:7], v[224:227], 0
v_mfma_f32_16x16x32_bf16 v[204:207], a[4:7], v[240:243], 0
v_mfma_f32_16x16x32_bf16 v[192:195], a[8:11], v[228:231], v[192:195]
v_mfma_f32_16x16x32_bf16 v[200:203], a[8:11], v[244:247], v[200:203]
v_mfma_f32_16x16x32_bf16 v[196:199], a[12:15], v[228:231], v[196:199]
v_mfma_f32_16x16x32_bf16 v[204:207], a[12:15], v[244:247], v[204:207]
v_mfma_f32_16x16x32_bf16 v[192:195], a[16:19], v[232:235], v[192:195]
v_mfma_f32_16x16x32_bf16 v[200:203], a[16:19], v[248:251], v[200:203]
v_mfma_f32_16x16x32_bf16 v[196:199], a[20:23], v[232:235], v[196:199]
v_mfma_f32_16x16x32_bf16 v[204:207], a[20:23], v[248:251], v[204:207]
v_mfma_f32_16x16x32_bf16 v[192:195], a[24:27], v[236:239], v[192:195]
v_mfma_f32_16x16x32_bf16 v[200:203], a[24:27], v[252:255], v[200:203]
v_mfma_f32_16x16x32_bf16 v[196:199], a[28:31], v[236:239], v[196:199]
v_mfma_f32_16x16x32_bf16 v[204:207], a[28:31], v[252:255], v[204:207]
v_cmp_gt_i32 s[32:33], 0, v58
v_nop
v_cmp_gt_i32 s[34:35], 1, v58
s_nop 1
v_cndmask_b32 v192, v192, v34, s[32:33]
v_cmp_gt_i32 s[32:33], 2, v58
v_cndmask_b32 v193, v193, v34, s[34:35]
v_cmp_gt_i32 s[34:35], 3, v58
v_cndmask_b32 v194, v194, v34, s[32:33]
v_cmp_gt_i32 s[32:33], 16, v58
v_cndmask_b32 v195, v195, v34, s[34:35]
v_cmp_gt_i32 s[34:35], 17, v58
v_cndmask_b32 v196, v196, v34, s[32:33]
v_cmp_gt_i32 s[32:33], 18, v58
v_cndmask_b32 v197, v197, v34, s[34:35]
v_cmp_gt_i32 s[34:35], 19, v58
v_cndmask_b32 v198, v198, v34, s[32:33]
v_cmp_gt_i32 s[32:33], -16, v58
v_cndmask_b32 v199, v199, v34, s[34:35]
v_cmp_gt_i32 s[34:35], -15, v58
v_cndmask_b32 v200, v200, v34, s[32:33]
v_cmp_gt_i32 s[32:33], -14, v58
v_cndmask_b32 v201, v201, v34, s[34:35]
v_cmp_gt_i32 s[34:35], -13, v58
v_cndmask_b32 v202, v202, v34, s[32:33]
v_cmp_gt_i32 s[32:33], 0, v58
v_cndmask_b32 v203, v203, v34, s[34:35]
v_cmp_gt_i32 s[34:35], 1, v58
v_cndmask_b32 v204, v204, v34, s[32:33]
v_cmp_gt_i32 s[32:33], 2, v58
v_cndmask_b32 v205, v205, v34, s[34:35]
v_cmp_gt_i32 s[34:35], 3, v58
v_cndmask_b32 v206, v206, v34, s[32:33]
v_nop
v_cndmask_b32 v207, v207, v34, s[34:35]
v_max3_f32 v26, v192, v193, v194
v_max3_f32 v27, v200, v201, v202
v_max3_f32 v28, v195, v196, v197
v_max3_f32 v29, v203, v204, v205
v_max3_f32 v26, v26, v198, v199
v_max3_f32 v27, v27, v206, v207
v_max3_f32 v26, v26, v28, v24
v_max3_f32 v27, v27, v29, v25
v_mov_b32 v28, v26
v_mov_b32 v29, v27
v_nop
v_permlane16_swap_b32 v26, v28
v_permlane16_swap_b32 v27, v29
v_max_f32 v26, v26, v28
v_max_f32 v27, v27, v29
v_mov_b32 v28, v26
v_mov_b32 v29, v27
v_nop
v_permlane32_swap_b32 v26, v28
v_permlane32_swap_b32 v27, v29
v_max_f32 v26, v26, v28
v_max_f32 v27, v27, v29
v_sub_f32 v30, v24, v26
v_exp_f32 v30, v30
v_mov_b32 v24, v26
v_sub_f32 v31, v25, v27
v_exp_f32 v31, v31
v_mov_b32 v25, v27
v_sub_f32 v192, v192, v26
v_sub_f32 v193, v193, v26
v_sub_f32 v194, v194, v26
v_sub_f32 v195, v195, v26
v_sub_f32 v196, v196, v26
v_sub_f32 v197, v197, v26
v_sub_f32 v198, v198, v26
v_sub_f32 v199, v199, v26
v_sub_f32 v200, v200, v27
v_sub_f32 v201, v201, v27
v_sub_f32 v202, v202, v27
v_sub_f32 v203, v203, v27
v_sub_f32 v204, v204, v27
v_sub_f32 v205, v205, v27
v_sub_f32 v206, v206, v27
v_sub_f32 v207, v207, v27
v_exp_f32 v192, v192
v_exp_f32 v193, v193
v_exp_f32 v194, v194
v_exp_f32 v195, v195
v_exp_f32 v196, v196
v_exp_f32 v197, v197
v_exp_f32 v198, v198
v_exp_f32 v199, v199
v_exp_f32 v200, v200
v_exp_f32 v201, v201
v_exp_f32 v202, v202
v_exp_f32 v203, v203
v_exp_f32 v204, v204
v_exp_f32 v205, v205
v_exp_f32 v206, v206
v_exp_f32 v207, v207
v_mul_f32 v56, v56, v30
v_mul_f32 v57, v57, v31
v_add_f32 v56, v56, v192
v_add_f32 v56, v56, v193
v_add_f32 v56, v56, v194
v_add_f32 v56, v56, v195
v_add_f32 v56, v56, v196
v_add_f32 v56, v56, v197
v_add_f32 v56, v56, v198
v_add_f32 v56, v56, v199
v_add_f32 v57, v57, v200
v_add_f32 v57, v57, v201
v_add_f32 v57, v57, v202
v_add_f32 v57, v57, v203
v_add_f32 v57, v57, v204
v_add_f32 v57, v57, v205
v_add_f32 v57, v57, v206
v_add_f32 v57, v57, v207
v_cvt_pk_bf16_f32 v16, v192, v193
.Lpad25:
s_nop 0
v_cvt_pk_bf16_f32 v17, v194, v195
v_cvt_pk_bf16_f32 v18, v196, v197
v_cvt_pk_bf16_f32 v19, v198, v199
v_cvt_pk_bf16_f32 v20, v200, v201
v_cvt_pk_bf16_f32 v21, v202, v203
v_cvt_pk_bf16_f32 v22, v204, v205
v_cvt_pk_bf16_f32 v23, v206, v207
v_mul_f32 v128, v30, v128
v_mul_f32 v129, v30, v129
v_mul_f32 v130, v30, v130
v_mul_f32 v131, v30, v131
v_mul_f32 v132, v30, v132
v_mul_f32 v133, v30, v133
v_mul_f32 v134, v30, v134
v_mul_f32 v135, v30, v135
v_mul_f32 v136, v31, v136
v_mul_f32 v137, v31, v137
v_mul_f32 v138, v31, v138
v_mul_f32 v139, v31, v139
v_mul_f32 v140, v31, v140
v_mul_f32 v141, v31, v141
v_mul_f32 v142, v31, v142
v_mul_f32 v143, v31, v143
v_mul_f32 v144, v30, v144
v_mul_f32 v145, v30, v145
v_mul_f32 v146, v30, v146
v_mul_f32 v147, v30, v147
v_mul_f32 v148, v30, v148
v_mul_f32 v149, v30, v149
v_mul_f32 v150, v30, v150
v_mul_f32 v151, v30, v151
v_mul_f32 v152, v31, v152
v_mul_f32 v153, v31, v153
v_mul_f32 v154, v31, v154
v_mul_f32 v155, v31, v155
v_mul_f32 v156, v31, v156
v_mul_f32 v157, v31, v157
v_mul_f32 v158, v31, v158
v_mul_f32 v159, v31, v159
v_mul_f32 v160, v30, v160
v_mul_f32 v161, v30, v161
v_mul_f32 v162, v30, v162
v_mul_f32 v163, v30, v163
v_mul_f32 v164, v30, v164
v_mul_f32 v165, v30, v165
v_mul_f32 v166, v30, v166
v_mul_f32 v167, v30, v167
v_mul_f32 v168, v31, v168
v_mul_f32 v169, v31, v169
v_mul_f32 v170, v31, v170
v_mul_f32 v171, v31, v171
v_mul_f32 v172, v31, v172
v_mul_f32 v173, v31, v173
v_mul_f32 v174, v31, v174
v_mul_f32 v175, v31, v175
v_mul_f32 v176, v30, v176
v_mul_f32 v177, v30, v177
v_mul_f32 v178, v30, v178
v_mul_f32 v179, v30, v179
v_mul_f32 v180, v30, v180
v_mul_f32 v181, v30, v181
v_mul_f32 v182, v30, v182
v_mul_f32 v183, v30, v183
v_mul_f32 v184, v31, v184
v_mul_f32 v185, v31, v185
v_mul_f32 v186, v31, v186
v_mul_f32 v187, v31, v187
v_mul_f32 v188, v31, v188
v_mul_f32 v189, v31, v189
v_mul_f32 v190, v31, v190
v_mul_f32 v191, v31, v191
s_waitcnt vmcnt(0)
v_mfma_f32_16x16x32_bf16 v[128:131], a[32:35], v[16:19], v[128:131]
v_mfma_f32_16x16x32_bf16 v[136:139], a[32:35], v[20:23], v[136:139]
v_mfma_f32_16x16x32_bf16 v[132:135], a[36:39], v[16:19], v[132:135]
v_mfma_f32_16x16x32_bf16 v[140:143], a[36:39], v[20:23], v[140:143]
v_mfma_f32_16x16x32_bf16 v[144:147], a[40:43], v[16:19], v[144:147]
v_mfma_f32_16x16x32_bf16 v[152:155], a[40:43], v[20:23], v[152:155]
v_mfma_f32_16x16x32_bf16 v[148:151], a[44:47], v[16:19], v[148:151]
v_mfma_f32_16x16x32_bf16 v[156:159], a[44:47], v[20:23], v[156:159]
v_mfma_f32_16x16x32_bf16 v[160:163], a[48:51], v[16:19], v[160:163]
v_mfma_f32_16x16x32_bf16 v[168:171], a[48:51], v[20:23], v[168:171]
v_mfma_f32_16x16x32_bf16 v[164:167], a[52:55], v[16:19], v[164:167]
v_mfma_f32_16x16x32_bf16 v[172:175], a[52:55], v[20:23], v[172:175]
v_mfma_f32_16x16x32_bf16 v[176:179], a[56:59], v[16:19], v[176:179]
v_mfma_f32_16x16x32_bf16 v[184:187], a[56:59], v[20:23], v[184:187]
v_mfma_f32_16x16x32_bf16 v[180:183], a[60:63], v[16:19], v[180:183]
v_mfma_f32_16x16x32_bf16 v[188:191], a[60:63], v[20:23], v[188:191]
v_subrev_u32 v58, 32, v58
s_add_u32 s44, s44, 8192
s_add_u32 s45, s45, 8192
s_add_u32 s46, s46, 1
s_cmp_le_u32 s46, s50
s_cbranch_scc1 .Lx_loop
v_mov_b32 v28, v56
v_mov_b32 v29, v57
s_nop 0
v_permlane16_swap_b32 v56, v28
v_permlane16_swap_b32 v57, v29
v_add_f32 v56, v56, v28
v_add_f32 v57, v57, v29
v_mov_b32 v28, v56
v_mov_b32 v29, v57
s_nop 0
v_permlane32_swap_b32 v56, v28
v_permlane32_swap_b32 v57, v29
v_add_f32 v56, v56, v28
v_add_f32 v57, v57, v29
s_setpc_b64 s[42:43]
.Lspin6:
s_mov_b32 s93, 0
.Lspin6_l:
s_sleep 4
buffer_load_dword v250, v249, s[60:63], 0 offen sc1
s_waitcnt vmcnt(0)
v_cmp_eq_u32 vcc, 0, v250
s_cmp_lg_u64 vcc, 0
s_cbranch_scc0 .Lspin6_ret
s_add_u32 s93, s93, 1
s_cmp_lt_u32 s93, 0x100000
s_cbranch_scc1 .Lspin6_l
s_branch .Lspin6_ret
.Lspin3:
s_mov_b32 s93, 0
.Lspin3_l:
s_sleep 4
buffer_load_dword v250, v249, s[60:63], s90 offen sc1
s_waitcnt vmcnt(0)
v_and_b32 v250, 7, v250
v_cmp_gt_u32 vcc, 3, v250
s_cmp_lg_u64 vcc, 0
s_cbranch_scc0 .Lspin3_ret
s_add_u32 s93, s93, 1
s_cmp_lt_u32 s93, 0x100000
s_cbranch_scc1 .Lspin3_l
s_branch .Lspin3_ret
.Lspin5:
s_mov_b32 s93, 0
.Lspin5_l:
s_sleep 4
buffer_load_dword v250, v249, s[60:63], s90 offen sc1
s_waitcnt vmcnt(0)
v_and_b32 v250, 7, v250
v_cmp_gt_u32 vcc, 5, v250
s_cmp_lg_u64 vcc, 0
s_cbranch_scc0 .Lspin5_ret
s_add_u32 s93, s93, 1
s_cmp_lt_u32 s93, 0x100000
s_cbranch_scc1 .Lspin5_l
s_branch .Lspin5_ret
.Lspin4:
s_mov_b32 s93, 0
.Lspin4_l:
s_sleep 4
buffer_load_dword v250, v249, s[60:63], s90 offen sc1
s_waitcnt vmcnt(0)
v_and_b32 v250, 7, v250
v_cmp_gt_u32 vcc, 4, v250
s_cmp_lg_u64 vcc, 0
s_cbranch_scc0 .Lspin4_ret
s_add_u32 s93, s93, 1
s_cmp_lt_u32 s93, 0x100000
s_cbranch_scc1 .Lspin4_l
s_branch .Lspin4_ret
.Lpret:
.Lcode1:
)ASM"
    :: "{s[0:1]}"(__builtin_amdgcn_kernarg_segment_ptr()), "{s2}"(blockIdx.x), "{v0}"(threadIdx.x),
       "{s3}"((unsigned)(size_t)(__attribute__((address_space(3))) char*)smem)
    : "v0", "v1", "v2", "v3", "v4", "v5", "v6", "v7", "v8", "v9", "v10", "v11", "v12", "v13", "v14", "v15", "v16", "v17", "v18", "v19", "v20", "v21", "v22", "v23", "v24", "v25", "v26", "v27", "v28", "v29", "v30", "v31", "v32", "v33", "v34", "v35", "v36", "v37", "v38", "v39", "v40", "v41", "v42", "v43", "v44", "v45", "v46", "v47", "v48", "v49", "v50", "v51", "v52", "v53", "v54", "v55", "v56", "v57", "v58", "v59", "v60", "v61", "v62", "v63", "v64", "v65", "v66", "v67", "v68", "v69", "v70", "v71", "v72", "v73", "v74", "v75", "v76", "v77", "v78", "v79", "v80", "v81", "v82", "v83", "v84", "v85", "v86", "v87", "v88", "v89", "v90", "v91", "v92", "v93", "v94", "v95", "v96", "v97", "v98", "v99", "v100", "v101", "v102", "v103", "v104", "v105", "v106", "v107", "v108", "v109", "v110", "v111", "v112", "v113", "v114", "v115", "v116", "v117", "v118", "v119", "v120", "v121", "v122", "v123", "v124", "v125", "v126", "v127", "v128", "v129", "v130", "v131", "v132", "v133", "v134", "v135", "v136", "v137", "v138", "v139", "v140", "v141", "v142", "v143", "v144", "v145", "v146", "v147", "v148", "v149", "v150", "v151", "v152", "v153", "v154", "v155", "v156", "v157", "v158", "v159", "v160", "v161", "v162", "v163", "v164", "v165", "v166", "v167", "v168", "v169", "v170", "v171", "v172", "v173", "v174", "v175", "v176", "v177", "v178", "v179", "v180", "v181", "v182", "v183", "v184", "v185", "v186", "v187", "v188", "v189", "v190", "v191", "v192", "v193", "v194", "v195", "v196", "v197", "v198", "v199", "v200", "v201", "v202", "v203", "v204", "v205", "v206", "v207", "v208", "v209", "v210", "v211", "v212", "v213", "v214", "v215", "v216", "v217", "v218", "v219", "v220", "v221", "v222", "v223", "v224", "v225", "v226", "v227", "v228", "v229", "v230", "v231", "v232", "v233", "v234", "v235", "v236", "v237", "v238", "v239", "v240", "v241", "v242", "v243", "v244", "v245", "v246", "v247", "v248", "v249", "v250", "v251", "v252", "v253", "v254", "v255", "a0", "a1", "a2", "a3", "a4", "a5", "a6", "a7", "a8", "a9", "a10", "a11", "a12", "a13", "a14", "a15", "a16", "a17", "a18", "a19", "a20", "a21", "a22", "a23", "a24", "a25", "a26", "a27", "a28", "a29", "a30", "a31", "a32", "a33", "a34", "a35", "a36", "a37", "a38", "a39", "a40", "a41", "a42", "a43", "a44", "a45", "a46", "a47", "a48", "a49", "a50", "a51", "a52", "a53", "a54", "a55", "a56", "a57", "a58", "a59", "a60", "a61", "a62", "a63", "a64", "a65", "a66", "a67", "a68", "a69", "a70", "a71", "a72", "a73", "a74", "a75", "a76", "a77", "a78", "a79", "a80", "a81", "a82", "a83", "a84", "a85", "a86", "a87", "a88", "a89", "a90", "a91", "a92", "a93", "a94", "a95", "a96", "a97", "a98", "a99", "a100", "a101", "a102", "a103", "a104", "a105", "a106", "a107", "a108", "a109", "a110", "a111", "a112", "a113", "a114", "a115", "a116", "a117", "a118", "a119", "a120", "a121", "a122", "a123", "a124", "a125", "a126", "a127", "a128", "a129", "a130", "a131", "a132", "a133", "a134", "a135", "a136", "a137", "a138", "a139", "a140", "a141", "a142", "a143", "a144", "a145", "a146", "a147", "a148", "a149", "a150", "a151", "a152", "a153", "a154", "a155", "a156", "a157", "a158", "a159", "a160", "a161", "a162", "a163", "a164", "a165", "a166", "a167", "a168", "a169", "a170", "a171", "a172", "a173", "a174", "a175", "a176", "a177", "a178", "a179", "a180", "a181", "a182", "a183", "a184", "a185", "a186", "a187", "a188", "a189", "a190", "a191", "a192", "a193", "a194", "a195", "a196", "a197", "a198", "a199", "a200", "a201", "a202", "a203", "a204", "a205", "a206", "a207", "a208", "a209", "a210", "a211", "a212", "a213", "a214", "a215", "a216", "a217", "a218", "a219", "a220", "a221", "a222", "a223", "a224", "a225", "a226", "a227", "a228", "a229", "a230", "a231", "a232", "a233", "a234", "a235", "a236", "a237", "a238", "a239", "a240", "a241", "a242", "a243", "a244", "a245", "a246", "a247", "a248", "a249", "a250", "a251", "a252", "a253", "a254", "a255", "s3", "s4", "s5", "s6", "s7", "s8", "s9", "s10", "s11", "s12", "s13", "s14", "s15", "s16", "s17", "s18", "s19", "s20", "s21", "s22", "s23", "s24", "s25", "s26", "s27", "s28", "s29", "s30", "s31", "s32", "s33", "s34", "s35", "s36", "s37", "s38", "s39", "s40", "s41", "s42", "s43", "s44", "s45", "s46", "s47", "s48", "s49", "s50", "s51", "s52", "s53", "s54", "s55", "s56", "s57", "s58", "s59", "s60", "s61", "s62", "s63", "s64", "s65", "s66", "s67", "s68", "s69", "s70", "s71", "s72", "s73", "s74", "s75", "s76", "s77", "s78", "s79", "s80", "s81", "s82", "s83", "s84", "s85", "s86", "s87", "s88", "s89", "s90", "s91", "s92", "s93", "s94", "s95", "s96", "s97", "s98", "s99", "s100", "s101", "vcc", "m0", "scc", "memory");
}
extern "C" void kernel_launch(void* const* d_in, const int* in_sizes, int n_in, void* d_out, int out_size,
                              void* d_ws, size_t ws_size, hipStream_t stream) {
  Args a;
  for (int i = 0; i < 9; ++i) a.p[i] = d_in[i];
  a.out = d_out; a.ws = (char*)d_ws + (0 ? ((ws_size - (72u << 20)) & ~(size_t)0xfffff) : 0);
  mega_kernel<<<256, 256, 0, stream>>>(a);
}
```

```cpp
#include <hip/hip_runtime.h>
#include <stdint.h>
struct Args { const void* p[9]; void* out; void* ws; };
__global__ __launch_bounds__(256, 1) __attribute__((aligned(4096))) void mega_kernel(Args args) {
  __shared__ __attribute__((aligned(16))) char smem[163840];
  asm volatile(R"ASM(
s_getpc_b64 s[96:97]
.Lcode0:
s_load_dwordx16 s[64:79], s[0:1], 0
s_load_dwordx4 s[80:83], s[0:1], 64
s_load_dwordx2 s[84:85], s[0:1], 80
v_and_b32 v1, 63, v0
v_lshrrev_b32 v16, 6, v0
v_lshlrev_b32 v7, 4, v1
v_readfirstlane_b32 s3, v16
s_and_b32 s86, s2, 7
s_lshr_b32 s87, s2, 3
s_and_b32 s4, s87, 3
s_lshl_b32 s88, s86, 2
s_add_u32 s88, s88, s4
s_lshr_b32 s89, s2, 5
s_lshl_b32 s91, s3, 12
s_lshl_b32 s92, s3, 13
s_and_b32 s97, s97, 0xffff
s_mov_b32 s98, .Lcode1-.Lcode0
s_mov_b32 s99, 0x20000
s_lshl_b32 s33, s87, 2
s_add_u32 s33, s33, s3
s_lshl_b32 s33, s33, 10
v_and_b32 v4, 7, v1
v_lshlrev_b32 v4, 7, v4
v_add_u32 v4, s33, v4
buffer_load_dword v35, v4, s[96:99], 0 offen
s_and_b32 s27, s87, 7
s_bfe_u32 s28, s87, 0x10003
s_lshl_b32 s29, s86, 1
s_add_u32 s28, s28, s29
v_lshrrev_b32 v40, 3, v1
v_and_b32 v41, 7, v1
v_xor_b32 v41, v41, v40
v_lshlrev_b32 v41, 4, v41
v_lshl_add_u32 v202, v40, 10, v41
v_add_u32 v203, 7168, v202
v_add_u32 v204, 14336, v202
v_add_u32 v205, 21504, v202
v_lshl_add_u32 v20, v40, 11, v41
v_add_u32 v21, 15360, v20
v_add_u32 v22, 30720, v20
v_add_u32 v23, 46080, v20
v_lshrrev_b32 v40, 4, v1
v_and_b32 v41, 15, v1
v_lshlrev_b32 v42, 1, v40
v_xor_b32 v41, v41, v42
v_lshlrev_b32 v42, 4, v41
v_lshl_add_u32 v16, v40, 13, v42
v_add_u32 v17, 31744, v16
v_xor_b32 v18, 0x80, v16
v_add_u32 v18, 63488, v18
v_add_u32 v19, 31744, v18
s_and_b32 s30, s3, 1
s_lshr_b32 s31, s3, 1
s_lshl_b32 s32, s30, 20
s_lshl_b32 s33, s27, 16
s_add_u32 s32, s32, s33
s_lshl_b32 s33, s31, 15
s_add_u32 s32, s32, s33
s_lshl_b32 s34, s28, 9
s_lshl_b32 s33, s31, 8
s_add_u32 s34, s34, s33
s_lshl_b32 s33, s30, 18
s_add_u32 s34, s34, s33
s_sub_u32 s35, 0x200000, s32
s_sub_u32 s36, 0x400000, s34
s_cmp_lt_u32 s87, 16
s_cselect_b32 s46, 1, 0
s_waitcnt lgkmcnt(0)
s_cselect_b32 s38, s76, s78
s_cselect_b32 s39, s77, s79
s_add_u32 s8, s38, s34
s_addc_u32 s9, s39, 0
s_and_b32 s9, s9, 0xffff
s_mov_b32 s10, s36
s_mov_b32 s11, 0x20000
s_add_u32 s4, s72, s32
s_addc_u32 s5, s73, 0
s_and_b32 s5, s5, 0xffff
s_mov_b32 s6, s35
s_mov_b32 s7, 0x20000
s_add_u32 s37, s92, 0
s_mov_b32 s42, 0
s_mov_b32 s43, 131072
s_mov_b32 m0, s37
s_nop 0
buffer_load_dwordx4 v16, s[8:11], s42 offen offset:0 nt lds
buffer_load_dwordx4 v17, s[8:11], s42 offen offset:1024 nt lds
buffer_load_dwordx4 v18, s[8:11], s42 offen offset:2048 nt lds
buffer_load_dwordx4 v19, s[8:11], s42 offen offset:3072 nt lds
s_add_u32 m0, s37, 4096
s_nop 0
buffer_load_dwordx4 v16, s[8:11], s43 offen offset:0 nt lds
buffer_load_dwordx4 v17, s[8:11], s43 offen offset:1024 nt lds
buffer_load_dwordx4 v18, s[8:11], s43 offen offset:2048 nt lds
buffer_load_dwordx4 v19, s[8:11], s43 offen offset:3072 nt lds
s_add_u32 s60, s84, 62914560
s_addc_u32 s61, s85, 0
s_and_b32 s61, s61, 0xffff
s_mov_b32 s62, 82944
s_mov_b32 s63, 0x20000
s_lshl_b32 s90, s86, 13
v_lshlrev_b32 v3, 5, v0
s_lshl_b32 s93, s2, 4
s_add_u32 s93, s93, s3
s_lshl_b32 s93, s93, 2
s_add_u32 s93, s93, 0x10000
s_add_u32 s37, s92, 32768
s_mov_b32 s40, 0
s_mov_b32 s41, 524288
s_mov_b32 m0, s37
s_nop 0
buffer_load_dwordx4 v202, s[4:7], s40 offen offset:0 lds
v_lshlrev_b32 v45, 20, v1
v_add_u32 v44, s93, v45
s_lshl_b32 s93, s87, 2
s_add_u32 s93, s93, s3
s_xor_b32 s93, s93, 32
s_lshl_b32 s93, s93, 20
s_lshl_b32 s0, s86, 2
s_add_u32 s93, s93, s0
s_add_u32 s93, s93, 0x14000
v_lshlrev_b32 v45, 7, v1
v_add_u32 v46, s93, v45
buffer_load_dwordx4 v203, s[4:7], s40 offen offset:1024 lds
s_mov_b64 exec, 0
s_cmp_eq_u32 s3, 0
s_cbranch_scc1 .Lpad1
s_cmp_eq_u32 s3, 1
s_cbranch_scc1 .Lp1_v_pad
s_cmp_eq_u32 s3, 2
s_cbranch_scc1 .Lpad2
s_branch .Lpad3
.Ltch0:
s_mov_b64 exec, -1
v_mov_b32 v45, 0
s_lshl_b32 s33, s2, 5
v_lshlrev_b32 v41, 13, v0
v_lshrrev_b32 v42, 3, v0
v_lshl_add_u32 v41, v42, 20, v41
v_add_u32 v6, s33, v41
s_and_b32 s33, s87, 2
s_lshl_b32 s33, s33, 5
s_or_b32 s33, s33, s86
v_and_b32 v41, 0x47, v0
v_cmp_eq_u32 vcc, s33, v41
s_nop 1
v_cndmask_b32 v14, 0, 1, vcc
buffer_load_dwordx4 v204, s[4:7], s40 offen offset:2048 lds
s_lshl_b32 s33, s30, 14
s_add_u32 s33, s33, 32768
v_and_b32 v36, 15, v1
v_lshrrev_b32 v37, 4, v1
v_and_b32 v38, 7, v36
v_xor_b32 v38, v38, v37
v_lshlrev_b32 v38, 4, v38
v_lshl_add_u32 v192, v36, 7, v38
v_add_u32 v192, s33, v192
v_xor_b32 v193, 64, v192
s_lshl_b32 s33, s31, 14
buffer_load_dwordx4 v205, s[4:7], s40 offen offset:3072 lds
v_and_b32 v36, 15, v1
v_lshrrev_b32 v37, 4, v1
v_lshrrev_b32 v38, 2, v36
v_and_b32 v39, 3, v36
v_lshl_add_u32 v40, v37, 3, v38
v_and_b32 v41, 1, v37
v_lshl_or_b32 v41, v41, 2, v38
v_lshlrev_b32 v40, 8, v40
v_lshl_add_u32 v40, v39, 3, v40
v_add_u32 v40, s33, v40
v_xor_b32 v36, 0, v41
v_lshl_add_u32 v194, v36, 5, v40
s_add_u32 m0, s37, 4096
s_nop 0
buffer_load_dwordx4 v202, s[4:7], s41 offen offset:0 lds
s_mov_b64 exec, 0
s_cmp_eq_u32 s3, 0
s_cbranch_scc1 .Lp2_pad
s_cmp_eq_u32 s3, 1
s_cbranch_scc1 .Lpad4
s_cmp_eq_u32 s3, 2
s_cbranch_scc1 .Lpad5
s_branch .Lpad6
.Ltch1:
s_mov_b64 exec, -1
v_xor_b32 v36, 1, v41
v_lshl_add_u32 v195, v36, 5, v40
v_xor_b32 v36, 2, v41
v_lshl_add_u32 v196, v36, 5, v40
v_xor_b32 v36, 3, v41
v_lshl_add_u32 v197, v36, 5, v40
v_xor_b32 v36, 4, v41
v_lshl_add_u32 v198, v36, 5, v40
v_xor_b32 v36, 5, v41
v_lshl_add_u32 v199, v36, 5, v40
v_xor_b32 v36, 6, v41
buffer_load_dwordx4 v203, s[4:7], s41 offen offset:1024 lds
v_lshl_add_u32 v200, v36, 5, v40
v_xor_b32 v36, 7, v41
v_lshl_add_u32 v201, v36, 5, v40
s_and_b32 s95, s27, 3
s_add_u32 s34, s46, 5
s_lshl_b32 s33, s95, s34
v_xor_b32 v194, s33, v194
v_xor_b32 v195, s33, v195
v_xor_b32 v196, s33, v196
v_xor_b32 v197, s33, v197
v_xor_b32 v198, s33, v198
buffer_load_dwordx4 v204, s[4:7], s41 offen offset:2048 lds
v_xor_b32 v199, s33, v199
v_xor_b32 v200, s33, v200
v_xor_b32 v201, s33, v201
s_lshl_b32 s95, s95, 10
s_add_u32 s33, s92, 32768
v_and_b32 v36, 15, v1
v_lshrrev_b32 v37, 4, v1
v_and_b32 v38, 7, v36
v_xor_b32 v38, v38, v37
v_lshlrev_b32 v38, 4, v38
v_lshl_add_u32 v24, v36, 7, v38
v_add_u32 v24, s33, v24
buffer_load_dwordx4 v205, s[4:7], s41 offen offset:3072 lds
s_mov_b64 exec, 0
s_cmp_eq_u32 s3, 0
s_cbranch_scc1 .Lpad7
s_cmp_eq_u32 s3, 1
s_cbranch_scc1 .Lpad8
s_cmp_eq_u32 s3, 2
s_cbranch_scc1 .Lpad9
s_branch .Lj0_pad
.Ltch2:
s_mov_b64 exec, -1
v_xor_b32 v25, 64, v24
v_and_b32 v36, 15, v1
v_lshrrev_b32 v37, 4, v1
v_lshrrev_b32 v38, 2, v36
v_and_b32 v39, 3, v36
v_lshl_add_u32 v40, v37, 3, v38
v_and_b32 v41, 1, v37
v_lshl_or_b32 v41, v41, 2, v38
v_lshlrev_b32 v40, 8, v40
v_lshl_add_u32 v40, v39, 3, v40
v_add_u32 v40, 65536, v40
s_add_u32 s37, s92, 65536
s_mov_b32 s42, 524288
s_mov_b32 s43, 655360
s_mov_b32 m0, s37
s_nop 0
buffer_load_dwordx4 v16, s[8:11], s42 offen offset:0 nt lds
v_xor_b32 v36, 0, v41
v_lshl_add_u32 v26, v36, 5, v40
v_xor_b32 v36, 1, v41
v_lshl_add_u32 v27, v36, 5, v40
v_xor_b32 v36, 2, v41
v_lshl_add_u32 v28, v36, 5, v40
v_xor_b32 v36, 3, v41
v_lshl_add_u32 v29, v36, 5, v40
v_xor_b32 v36, 4, v41
v_lshl_add_u32 v30, v36, 5, v40
v_xor_b32 v36, 5, v41
v_lshl_add_u32 v31, v36, 5, v40
buffer_load_dwordx4 v17, s[8:11], s42 offen offset:1024 nt lds
v_xor_b32 v36, 6, v41
v_lshl_add_u32 v32, v36, 5, v40
v_xor_b32 v36, 7, v41
v_lshl_add_u32 v33, v36, 5, v40
s_add_u32 s12, s70, 0
s_addc_u32 s13, s71, 0
s_and_b32 s13, s13, 0xffff
s_mov_b32 s14, 4194304
s_mov_b32 s15, 0x20000
s_lshl_b32 s33, s88, 8
s_lshl_b32 s34, s3, 17
buffer_load_dwordx4 v18, s[8:11], s42 offen offset:2048 nt lds
s_mov_b64 exec, 0
s_cmp_eq_u32 s3, 0
s_cbranch_scc1 .Lpad10
s_cmp_eq_u32 s3, 1
s_cbranch_scc1 .Lpad11
s_cmp_eq_u32 s3, 2
s_cbranch_scc1 .Lpad12
s_branch .Lfb_pad
.Ltch3:
s_mov_b64 exec, -1
s_add_u32 s33, s33, s34
s_sub_u32 s34, 0x800000, s33
s_add_u32 s16, s74, s33
s_addc_u32 s17, s75, 0
s_and_b32 s17, s17, 0xffff
s_mov_b32 s18, s34
s_mov_b32 s19, 0x20000
s_sub_u32 s24, 15, s89
s_lshl_b32 s24, s24, 18
s_lshl_b32 s25, s89, 18
s_lshl_b32 s33, s3, 16
buffer_load_dwordx4 v19, s[8:11], s42 offen offset:3072 nt lds
s_add_u32 s94, s3, 1
s_and_b32 s94, s94, 3
s_add_u32 s24, s24, s33
s_lshl_b32 s33, s94, 16
s_add_u32 s25, s25, s33
s_mov_b32 s26, 0
s_lshl_b32 s33, s28, 1
s_add_u32 s33, s33, s31
s_lshl_b32 s33, s33, 19
s_lshl_b32 s34, s27, 1
s_add_u32 s34, s34, s30
s_lshl_b32 s34, s34, 13
s_add_u32 m0, s37, 4096
s_nop 0
buffer_load_dwordx4 v16, s[8:11], s43 offen offset:0 nt lds
s_add_u32 s33, s33, s34
s_xor_b32 s34, s46, 1
s_lshl_b32 s34, s34, 24
s_add_u32 s33, s33, s34
s_add_u32 s20, s84, s33
s_addc_u32 s21, s85, 0
s_and_b32 s21, s21, 0xffff
s_mov_b32 s22, 524288
s_mov_b32 s23, 0x20000
s_cmp_eq_u32 s46, 1
s_cselect_b32 s47, 1040356334, 1.0
buffer_load_dwordx4 v17, s[8:11], s43 offen offset:1024 nt lds
s_mov_b64 exec, 0
s_cmp_eq_u32 s3, 0
s_cbranch_scc1 .LT1_pad
s_cmp_eq_u32 s3, 1
s_cbranch_scc1 .LS1_pad
s_cmp_eq_u32 s3, 2
s_cbranch_scc1 .LE1_pad
s_branch .Lool_pad
.Ltch4:
s_mov_b64 exec, -1
s_lshl_b32 s33, s87, 2
s_add_u32 s33, s33, s3
s_lshl_b32 s33, s33, 13
s_lshl_b32 s34, s86, 10
s_add_u32 s33, s33, s34
v_and_b32 v36, 7, v1
v_lshlrev_b32 v36, 7, v36
v_add_u32 v206, s33, v36
s_lshl_b32 s33, s2, 2
s_add_u32 s33, s33, s3
s_lshl_b32 s33, s33, 12
v_bfe_u32 v37, v1, 1, 1
buffer_load_dwordx4 v18, s[8:11], s43 offen offset:2048 nt lds
v_and_b32 v38, 1, v1
v_lshlrev_b32 v37, 11, v37
v_lshl_add_u32 v37, v38, 7, v37
v_add_u32 v207, s33, v37
s_mov_b32 s56, s74
s_and_b32 s57, s75, 0xffff
s_mov_b32 s58, 0x800000
s_mov_b32 s59, 0x20000
s_mov_b32 s48, 0x100000
s_mov_b32 s49, 256
s_lshl_b32 s33, s2, 2
buffer_load_dwordx4 v19, s[8:11], s43 offen offset:3072 nt lds
s_add_u32 s33, s33, s3
s_mul_i32 s33, s33, 640
v_min_u32 v37, 4, v1
v_lshlrev_b32 v37, 7, v37
v_add_u32 v207, s33, v37
s_mov_b32 s49, 0
s_mov_b32 s35, s92
s_add_u32 s36, s92, 32768
s_add_u32 s39, s92, 163840
s_mov_b32 s54, 65536
s_mov_b32 s55, -98304
s_mov_b32 s38, 1
s_waitcnt vmcnt(8)
s_barrier
ds_read_b64_tr_b16 v[96:97], v194 offset:0
ds_read_b64_tr_b16 v[98:99], v194 offset:1024
ds_read_b128 v[64:67], v192 offset:0
ds_read_b64_tr_b16 v[100:101], v195 offset:0
ds_read_b64_tr_b16 v[102:103], v195 offset:1024
ds_read_b128 v[68:71], v192 offset:2048
ds_read_b64_tr_b16 v[104:105], v196 offset:0
ds_read_b64_tr_b16 v[106:107], v196 offset:1024
ds_read_b128 v[72:75], v192 offset:4096
ds_read_b64_tr_b16 v[108:109], v197 offset:0
ds_read_b64_tr_b16 v[110:111], v197 offset:1024
ds_read_b128 v[76:79], v192 offset:6144
s_waitcnt lgkmcnt(6)
ds_read_b64_tr_b16 v[112:113], v198 offset:0
ds_read_b64_tr_b16 v[114:115], v198 offset:1024
ds_read_b128 v[80:83], v192 offset:8192
ds_read_b64_tr_b16 v[116:117], v199 offset:0
ds_read_b64_tr_b16 v[118:119], v199 offset:1024
ds_read_b128 v[84:87], v192 offset:10240
ds_read_b64_tr_b16 v[120:121], v200 offset:0
ds_read_b64_tr_b16 v[122:123], v200 offset:1024
ds_read_b128 v[88:91], v192 offset:12288
ds_read_b64_tr_b16 v[124:125], v201 offset:0
ds_read_b64_tr_b16 v[126:127], v201 offset:1024
ds_read_b128 v[92:95], v192 offset:14336
s_cmp_eq_u32 s46, 1
s_waitcnt lgkmcnt(0)
s_cbranch_scc0 .Lp1_v
v_mfma_f32_16x16x32_bf16 a[0:3], v[96:99], v[64:67], 0
ds_read_b64_tr_b16 v[160:161], v194 offset:8192
v_mfma_f32_16x16x32_bf16 a[4:7], v[100:103], v[64:67], 0
s_add_u32 s37, s92, 98304
s_mov_b32 s40, 128
s_mov_b32 s41, 524416
v_mfma_f32_16x16x32_bf16 a[8:11], v[104:107], v[64:67], 0
s_mov_b32 m0, s37
ds_read_b64_tr_b16 v[162:163], v194 offset:9216
v_mfma_f32_16x16x32_bf16 a[12:15], v[108:111], v[64:67], 0
buffer_load_dwordx4 v202, s[4:7], s40 offen offset:0 lds
v_mfma_f32_16x16x32_bf16 a[16:19], v[112:115], v[64:67], 0
ds_read_b128 v[128:131], v193 offset:0
v_mfma_f32_16x16x32_bf16 a[20:23], v[116:119], v[64:67], 0
buffer_load_dwordx4 v203, s[4:7], s40 offen offset:1024 lds
v_mfma_f32_16x16x32_bf16 a[24:27], v[120:123], v[64:67], 0
ds_read_b64_tr_b16 v[164:165], v195 offset:8192
v_mfma_f32_16x16x32_bf16 a[28:31], v[124:127], v[64:67], 0
buffer_load_dwordx4 v204, s[4:7], s40 offen offset:2048 lds
v_mfma_f32_16x16x32_bf16 a[32:35], v[96:99], v[68:71], 0
ds_read_b64_tr_b16 v[166:167], v195 offset:9216
v_mfma_f32_16x16x32_bf16 a[36:39], v[100:103], v[68:71], 0
buffer_load_dwordx4 v205, s[4:7], s40 offen offset:3072 lds
v_mfma_f32_16x16x32_bf16 a[40:43], v[104:107], v[68:71], 0
s_add_u32 m0, s37, 4096
ds_read_b128 v[132:135], v193 offset:2048
v_mfma_f32_16x16x32_bf16 a[44:47], v[108:111], v[68:71], 0
buffer_load_dwordx4 v202, s[4:7], s41 offen offset:0 lds
v_mfma_f32_16x16x32_bf16 a[48:51], v[112:115], v[68:71], 0
ds_read_b64_tr_b16 v[168:169], v196 offset:8192
v_mfma_f32_16x16x32_bf16 a[52:55], v[116:119], v[68:71], 0
buffer_load_dwordx4 v203, s[4:7], s41 offen offset:1024 lds
v_mfma_f32_16x16x32_bf16 a[56:59], v[120:123], v[68:71], 0
ds_read_b64_tr_b16 v[170:171], v196 offset:9216
v_mfma_f32_16x16x32_bf16 a[60:63], v[124:127], v[68:71], 0
buffer_load_dwordx4 v204, s[4:7], s41 offen offset:2048 lds
v_mfma_f32_16x16x32_bf16 a[64:67], v[96:99], v[72:75], 0
ds_read_b128 v[136:139], v193 offset:4096
v_mfma_f32_16x16x32_bf16 a[68:71], v[100:103], v[72:75], 0
buffer_load_dwordx4 v205, s[4:7], s41 offen offset:3072 lds
v_mfma_f32_16x16x32_bf16 a[72:75], v[104:107], v[72:75], 0
ds_read_b64_tr_b16 v[172:173], v197 offset:8192
v_mfma_f32_16x16x32_bf16 a[76:79], v[108:111], v[72:75], 0
s_add_u32 s37, s92, 131072
s_mov_b32 s42, 1048576
s_mov_b32 s43, 1179648
v_mfma_f32_16x16x32_bf16 a[80:83], v[112:115], v[72:75], 0
s_mov_b32 m0, s37
ds_read_b64_tr_b16 v[174:175], v197 offset:9216
v_mfma_f32_16x16x32_bf16 a[84:87], v[116:119], v[72:75], 0
buffer_load_dwordx4 v16, s[8:11], s42 offen offset:0 nt lds
v_mfma_f32_16x16x32_bf16 a[88:91], v[120:123], v[72:75], 0
ds_read_b128 v[140:143], v193 offset:6144
v_mfma_f32_16x16x32_bf16 a[92:95], v[124:127], v[72:75], 0
buffer_load_dwordx4 v17, s[8:11], s42 offen offset:1024 nt lds
v_mfma_f32_16x16x32_bf16 a[96:99], v[96:99], v[76:79], 0
ds_read_b64_tr_b16 v[176:177], v198 offset:8192
v_mfma_f32_16x16x32_bf16 a[100:103], v[100:103], v[76:79], 0
buffer_load_dwordx4 v18, s[8:11], s42 offen offset:2048 nt lds
v_mfma_f32_16x16x32_bf16 a[104:107], v[104:107], v[76:79], 0
ds_read_b64_tr_b16 v[178:179], v198 offset:9216
v_mfma_f32_16x16x32_bf16 a[108:111], v[108:111], v[76:79], 0
buffer_load_dwordx4 v19, s[8:11], s42 offen offset:3072 nt lds
v_mfma_f32_16x16x32_bf16 a[112:115], v[112:115], v[76:79], 0
s_add_u32 m0, s37, 4096
ds_read_b128 v[144:147], v193 offset:8192
v_mfma_f32_16x16x32_bf16 a[116:119], v[116:119], v[76:79], 0
buffer_load_dwordx4 v16, s[8:11], s43 offen offset:0 nt lds
v_mfma_f32_16x16x32_bf16 a[120:123], v[120:123], v[76:79], 0
ds_read_b64_tr_b16 v[180:181], v199 offset:8192
v_mfma_f32_16x16x32_bf16 a[124:127], v[124:127], v[76:79], 0
buffer_load_dwordx4 v17, s[8:11], s43 offen offset:1024 nt lds
v_mfma_f32_16x16x32_bf16 a[128:131], v[96:99], v[80:83], 0
ds_read_b64_tr_b16 v[182:183], v199 offset:9216
v_mfma_f32_16x16x32_bf16 a[132:135], v[100:103], v[80:83], 0
buffer_load_dwordx4 v18, s[8:11], s43 offen offset:2048 nt lds
v_mfma_f32_16x16x32_bf16 a[136:139], v[104:107], v[80:83], 0
ds_read_b128 v[148:151], v193 offset:10240
v_mfma_f32_16x16x32_bf16 a[140:143], v[108:111], v[80:83], 0
buffer_load_dwordx4 v19, s[8:11], s43 offen offset:3072 nt lds
v_mfma_f32_16x16x32_bf16 a[144:147], v[112:115], v[80:83], 0
ds_read_b64_tr_b16 v[184:185], v200 offset:8192
v_mfma_f32_16x16x32_bf16 a[148:151], v[116:119], v[80:83], 0
v_mfma_f32_16x16x32_bf16 a[152:155], v[120:123], v[80:83], 0
ds_read_b64_tr_b16 v[186:187], v200 offset:9216
v_mfma_f32_16x16x32_bf16 a[156:159], v[124:127], v[80:83], 0
v_mfma_f32_16x16x32_bf16 a[160:163], v[96:99], v[84:87], 0
ds_read_b128 v[152:155], v193 offset:12288
v_mfma_f32_16x16x32_bf16 a[164:167], v[100:103], v[84:87], 0
v_mfma_f32_16x16x32_bf16 a[168:171], v[104:107], v[84:87], 0
ds_read_b64_tr_b16 v[188:189], v201 offset:8192
v_mfma_f32_16x16x32_bf16 a[172:175], v[108:111], v[84:87], 0
v_mfma_f32_16x16x32_bf16 a[176:179], v[112:115], v[84:87], 0
s_add_u32 s44, s35, 65536
ds_read_b64_tr_b16 v[190:191], v201 offset:9216
v_mfma_f32_16x16x32_bf16 a[180:183], v[116:119], v[84:87], 0
s_cmp_ge_u32 s44, s39
s_cselect_b32 s45, s55, s54
v_mfma_f32_16x16x32_bf16 a[184:187], v[120:123], v[84:87], 0
ds_read_b128 v[156:159], v193 offset:14336
v_mfma_f32_16x16x32_bf16 a[188:191], v[124:127], v[84:87], 0
s_add_u32 s44, s36, 65536
v_mfma_f32_16x16x32_bf16 a[192:195], v[96:99], v[88:91], 0
v_mfma_f32_16x16x32_bf16 a[196:199], v[100:103], v[88:91], 0
s_cmp_ge_u32 s44, s39
s_cselect_b32 s34, s55, s54
v_mfma_f32_16x16x32_bf16 a[200:203], v[104:107], v[88:91], 0
v_add_u32 v194, s45, v194
v_mfma_f32_16x16x32_bf16 a[204:207], v[108:111], v[88:91], 0
v_add_u32 v195, s45, v195
v_mfma_f32_16x16x32_bf16 a[208:211], v[112:115], v[88:91], 0
v_add_u32 v196, s45, v196
v_mfma_f32_16x16x32_bf16 a[212:215], v[116:119], v[88:91], 0
v_add_u32 v197, s45, v197
v_mfma_f32_16x16x32_bf16 a[216:219], v[120:123], v[88:91], 0
v_add_u32 v198, s45, v198
v_mfma_f32_16x16x32_bf16 a[220:223], v[124:127], v[88:91], 0
v_add_u32 v199, s45, v199
v_mfma_f32_16x16x32_bf16 a[224:227], v[96:99], v[92:95], 0
v_add_u32 v200, s45, v200
v_mfma_f32_16x16x32_bf16 a[228:231], v[100:103], v[92:95], 0
v_add_u32 v201, s45, v201
v_mfma_f32_16x16x32_bf16 a[232:235], v[104:107], v[92:95], 0
v_add_u32 v192, s34, v192
v_mfma_f32_16x16x32_bf16 a[236:239], v[108:111], v[92:95], 0
v_add_u32 v193, s34, v193
v_mfma_f32_16x16x32_bf16 a[240:243], v[112:115], v[92:95], 0
v_mfma_f32_16x16x32_bf16 a[244:247], v[116:119], v[92:95], 0
s_mov_b32 s40, 256
v_mfma_f32_16x16x32_bf16 a[248:251], v[120:123], v[92:95], 0
s_mov_b32 s41, 524544
v_mfma_f32_16x16x32_bf16 a[252:255], v[124:127], v[92:95], 0
s_waitcnt vmcnt(8) lgkmcnt(0)
s_barrier
v_mfma_f32_16x16x32_bf16 a[0:3], v[160:163], v[128:131], a[0:3]
s_mov_b32 m0, s35
ds_read_b64_tr_b16 v[96:97], v194 offset:0
v_mfma_f32_16x16x32_bf16 a[4:7], v[164:167], v[128:131], a[4:7]
buffer_load_dwordx4 v202, s[4:7], s40 offen offset:0 lds
v_mfma_f32_16x16x32_bf16 a[8:11], v[168:171], v[128:131], a[8:11]
ds_read_b64_tr_b16 v[98:99], v194 offset:1024
v_mfma_f32_16x16x32_bf16 a[12:15], v[172:175], v[128:131], a[12:15]
v_mfma_f32_16x16x32_bf16 a[16:19], v[176:179], v[128:131], a[16:19]
ds_read_b128 v[64:67], v192 offset:0
v_mfma_f32_16x16x32_bf16 a[20:23], v[180:183], v[128:131], a[20:23]
buffer_load_dwordx4 v203, s[4:7], s40 offen offset:1024 lds
v_mfma_f32_16x16x32_bf16 a[24:27], v[184:187], v[128:131], a[24:27]
ds_read_b64_tr_b16 v[100:101], v195 offset:0
v_mfma_f32_16x16x32_bf16 a[28:31], v[188:191], v[128:131], a[28:31]
v_mfma_f32_16x16x32_bf16 a[32:35], v[160:163], v[132:135], a[32:35]
ds_read_b64_tr_b16 v[102:103], v195 offset:1024
v_mfma_f32_16x16x32_bf16 a[36:39], v[164:167], v[132:135], a[36:39]
buffer_load_dwordx4 v204, s[4:7], s40 offen offset:2048 lds
v_mfma_f32_16x16x32_bf16 a[40:43], v[168:171], v[132:135], a[40:43]
ds_read_b128 v[68:71], v192 offset:2048
v_mfma_f32_16x16x32_bf16 a[44:47], v[172:175], v[132:135], a[44:47]
v_mfma_f32_16x16x32_bf16 a[48:51], v[176:179], v[132:135], a[48:51]
ds_read_b64_tr_b16 v[104:105], v196 offset:0
v_mfma_f32_16x16x32_bf16 a[52:55], v[180:183], v[132:135], a[52:55]
buffer_load_dwordx4 v205, s[4:7], s40 offen offset:3072 lds
v_mfma_f32_16x16x32_bf16 a[56:59], v[184:187], v[132:135], a[56:59]
ds_read_b64_tr_b16 v[106:107], v196 offset:1024
v_mfma_f32_16x16x32_bf16 a[60:63], v[188:191], v[132:135], a[60:63]
v_mfma_f32_16x16x32_bf16 a[64:67], v[160:163], v[136:139], a[64:67]
s_add_u32 m0, s35, 4096
ds_read_b128 v[72:75], v192 offset:4096
v_mfma_f32_16x16x32_bf16 a[68:71], v[164:167], v[136:139], a[68:71]
buffer_load_dwordx4 v202, s[4:7], s41 offen offset:0 lds
v_mfma_f32_16x16x32_bf16 a[72:75], v[168:171], v[136:139], a[72:75]
ds_read_b64_tr_b16 v[108:109], v197 offset:0
v_mfma_f32_16x16x32_bf16 a[76:79], v[172:175], v[136:139], a[76:79]
v_mfma_f32_16x16x32_bf16 a[80:83], v[176:179], v[136:139], a[80:83]
ds_read_b64_tr_b16 v[110:111], v197 offset:1024
v_mfma_f32_16x16x32_bf16 a[84:87], v[180:183], v[136:139], a[84:87]
buffer_load_dwordx4 v203, s[4:7], s41 offen offset:1024 lds
v_mfma_f32_16x16x32_bf16 a[88:91], v[184:187], v[136:139], a[88:91]
ds_read_b128 v[76:79], v192 offset:6144
v_mfma_f32_16x16x32_bf16 a[92:95], v[188:191], v[136:139], a[92:95]
v_mfma_f32_16x16x32_bf16 a[96:99], v[160:163], v[140:143], a[96:99]
ds_read_b64_tr_b16 v[112:113], v198 offset:0
v_mfma_f32_16x16x32_bf16 a[100:103], v[164:167], v[140:143], a[100:103]
buffer_load_dwordx4 v204, s[4:7], s41 offen offset:2048 lds
v_mfma_f32_16x16x32_bf16 a[104:107], v[168:171], v[140:143], a[104:107]
ds_read_b64_tr_b16 v[114:115], v198 offset:1024
v_mfma_f32_16x16x32_bf16 a[108:111], v[172:175], v[140:143], a[108:111]
v_mfma_f32_16x16x32_bf16 a[112:115], v[176:179], v[140:143], a[112:115]
ds_read_b128 v[80:83], v192 offset:8192
v_mfma_f32_16x16x32_bf16 a[116:119], v[180:183], v[140:143], a[116:119]
buffer_load_dwordx4 v205, s[4:7], s41 offen offset:3072 lds
v_mfma_f32_16x16x32_bf16 a[120:123], v[184:187], v[140:143], a[120:123]
ds_read_b64_tr_b16 v[116:117], v199 offset:0
v_mfma_f32_16x16x32_bf16 a[124:127], v[188:191], v[140:143], a[124:127]
v_mfma_f32_16x16x32_bf16 a[128:131], v[160:163], v[144:147], a[128:131]
ds_read_b64_tr_b16 v[118:119], v199 offset:1024
v_mfma_f32_16x16x32_bf16 a[132:135], v[164:167], v[144:147], a[132:135]
buffer_load_dword v35, v206, s[56:59], s48 offen
v_mfma_f32_16x16x32_bf16 a[136:139], v[168:171], v[144:147], a[136:139]
ds_read_b128 v[84:87], v192 offset:10240
v_mfma_f32_16x16x32_bf16 a[140:143], v[172:175], v[144:147], a[140:143]
buffer_load_dword v2, v3, s[60:63], s90 offen sc1
v_mfma_f32_16x16x32_bf16 a[144:147], v[176:179], v[144:147], a[144:147]
s_mov_b32 s42, 1572864
ds_read_b64_tr_b16 v[120:121], v200 offset:0
v_mfma_f32_16x16x32_bf16 a[148:151], v[180:183], v[144:147], a[148:151]
s_mov_b32 s43, 1703936
buffer_load_dword v35, v207, s[12:15], s49 offen
v_mfma_f32_16x16x32_bf16 a[152:155], v[184:187], v[144:147], a[152:155]
ds_read_b64_tr_b16 v[122:123], v200 offset:1024
v_mfma_f32_16x16x32_bf16 a[156:159], v[188:191], v[144:147], a[156:159]
buffer_load_dword v11, v6, s[60:63], 0 offen sc1
v_mfma_f32_16x16x32_bf16 a[160:163], v[160:163], v[148:151], a[160:163]
s_add_u32 s40, s40, 128
ds_read_b128 v[88:91], v192 offset:12288
v_mfma_f32_16x16x32_bf16 a[164:167], v[164:167], v[148:151], a[164:167]
s_add_u32 s41, s41, 128
v_mfma_f32_16x16x32_bf16 a[168:171], v[168:171], v[148:151], a[168:171]
ds_read_b64_tr_b16 v[124:125], v201 offset:0
v_mfma_f32_16x16x32_bf16 a[172:175], v[172:175], v[148:151], a[172:175]
s_add_u32 s48, s48, 0x100000
v_mfma_f32_16x16x32_bf16 a[176:179], v[176:179], v[148:151], a[176:179]
ds_read_b64_tr_b16 v[126:127], v201 offset:1024
v_mfma_f32_16x16x32_bf16 a[180:183], v[180:183], v[148:151], a[180:183]
s_add_u32 s49, s49, 655360
v_mfma_f32_16x16x32_bf16 a[184:187], v[184:187], v[148:151], a[184:187]
ds_read_b128 v[92:95], v192 offset:14336
v_mfma_f32_16x16x32_bf16 a[188:191], v[188:191], v[148:151], a[188:191]
buffer_store_dword v45, v44, s[60:63], 0 offen sc1
v_mfma_f32_16x16x32_bf16 a[192:195], v[160:163], v[152:155], a[192:195]
v_mfma_f32_16x16x32_bf16 a[196:199], v[164:167], v[152:155], a[196:199]
v_mfma_f32_16x16x32_bf16 a[200:203], v[168:171], v[152:155], a[200:203]
v_mfma_f32_16x16x32_bf16 a[204:207], v[172:175], v[152:155], a[204:207]
buffer_store_dword v45, v46, s[60:63], 0 offen sc1
v_mfma_f32_16x16x32_bf16 a[208:211], v[176:179], v[152:155], a[208:211]
v_mfma_f32_16x16x32_bf16 a[212:215], v[180:183], v[152:155], a[212:215]
v_mfma_f32_16x16x32_bf16 a[216:219], v[184:187], v[152:155], a[216:219]
v_mfma_f32_16x16x32_bf16 a[220:223], v[188:191], v[152:155], a[220:223]
v_mfma_f32_16x16x32_bf16 a[224:227], v[160:163], v[156:159], a[224:227]
v_mfma_f32_16x16x32_bf16 a[228:231], v[164:167], v[156:159], a[228:231]
s_mov_b32 s37, s36
v_mfma_f32_16x16x32_bf16 a[232:235], v[168:171], v[156:159], a[232:235]
s_add_u32 s35, s35, s45
v_mfma_f32_16x16x32_bf16 a[236:239], v[172:175], v[156:159], a[236:239]
s_add_u32 s36, s36, s34
v_mfma_f32_16x16x32_bf16 a[240:243], v[176:179], v[156:159], a[240:243]
v_mfma_f32_16x16x32_bf16 a[244:247], v[180:183], v[156:159], a[244:247]
v_mfma_f32_16x16x32_bf16 a[248:251], v[184:187], v[156:159], a[248:251]
v_mfma_f32_16x16x32_bf16 a[252:255], v[188:191], v[156:159], a[252:255]
s_waitcnt lgkmcnt(0)
s_nop 0
s_nop 0
s_nop 0
s_nop 0
s_nop 0
s_nop 0
s_nop 0
s_nop 0
s_nop 0
s_nop 0
s_nop 0
s_nop 0
.Lp1_kloop:
v_mfma_f32_16x16x32_bf16 a[0:3], v[96:99], v[64:67], a[0:3]
s_mov_b32 m0, s37
ds_read_b64_tr_b16 v[160:161], v194 offset:8192
.Lpad1:
s_cbranch_execz .Ltch0
v_mfma_f32_16x16x32_bf16 a[4:7], v[100:103], v[64:67], a[4:7]
buffer_load_dwordx4 v16, s[8:11], s42 offen offset:0 nt lds
v_mfma_f32_16x16x32_bf16 a[8:11], v[104:107], v[64:67], a[8:11]
ds_read_b64_tr_b16 v[162:163], v194 offset:9216
v_mfma_f32_16x16x32_bf16 a[12:15], v[108:111], v[64:67], a[12:15]
v_mfma_f32_16x16x32_bf16 a[16:19], v[112:115], v[64:67], a[16:19]
ds_read_b128 v[128:131], v193 offset:0
v_mfma_f32_16x16x32_bf16 a[20:23], v[116:119], v[64:67], a[20:23]
buffer_load_dwordx4 v17, s[8:11], s42 offen offset:1024 nt lds
v_mfma_f32_16x16x32_bf16 a[24:27], v[120:123], v[64:67], a[24:27]
ds_read_b64_tr_b16 v[164:165], v195 offset:8192
v_mfma_f32_16x16x32_bf16 a[28:31], v[124:127], v[64:67], a[28:31]
v_mfma_f32_16x16x32_bf16 a[32:35], v[96:99], v[68:71], a[32:35]
ds_read_b64_tr_b16 v[166:167], v195 offset:9216
v_mfma_f32_16x16x32_bf16 a[36:39], v[100:103], v[68:71], a[36:39]
buffer_load_dwordx4 v18, s[8:11], s42 offen offset:2048 nt lds
v_mfma_f32_16x16x32_bf16 a[40:43], v[104:107], v[68:71], a[40:43]
ds_read_b128 v[132:135], v193 offset:2048
v_mfma_f32_16x16x32_bf16 a[44:47], v[108:111], v[68:71], a[44:47]
v_mfma_f32_16x16x32_bf16 a[48:51], v[112:115], v[68:71], a[48:51]
ds_read_b64_tr_b16 v[168:169], v196 offset:8192
v_mfma_f32_16x16x32_bf16 a[52:55], v[116:119], v[68:71], a[52:55]
buffer_load_dwordx4 v19, s[8:11], s42 offen offset:3072 nt lds
v_mfma_f32_16x16x32_bf16 a[56:59], v[120:123], v[68:71], a[56:59]
ds_read_b64_tr_b16 v[170:171], v196 offset:9216
v_mfma_f32_16x16x32_bf16 a[60:63], v[124:127], v[68:71], a[60:63]
v_mfma_f32_16x16x32_bf16 a[64:67], v[96:99], v[72:75], a[64:67]
s_add_u32 m0, s37, 4096
ds_read_b128 v[136:139], v193 offset:4096
v_mfma_f32_16x16x32_bf16 a[68:71], v[100:103], v[72:75], a[68:71]
buffer_load_dwordx4 v16, s[8:11], s43 offen offset:0 nt lds
v_mfma_f32_16x16x32_bf16 a[72:75], v[104:107], v[72:75], a[72:75]
ds_read_b64_tr_b16 v[172:173], v197 offset:8192
v_mfma_f32_16x16x32_bf16 a[76:79], v[108:111], v[72:75], a[76:79]
v_mfma_f32_16x16x32_bf16 a[80:83], v[112:115], v[72:75], a[80:83]
ds_read_b64_tr_b16 v[174:175], v197 offset:9216
v_mfma_f32_16x16x32_bf16 a[84:87], v[116:119], v[72:75], a[84:87]
buffer_load_dwordx4 v17, s[8:11], s43 offen offset:1024 nt lds
v_mfma_f32_16x16x32_bf16 a[88:91], v[120:123], v[72:75], a[88:91]
ds_read_b128 v[140:143], v193 offset:6144
v_mfma_f32_16x16x32_bf16 a[92:95], v[124:127], v[72:75], a[92:95]
v_mfma_f32_16x16x32_bf16 a[96:99], v[96:99], v[76:79], a[96:99]
ds_read_b64_tr_b16 v[176:177], v198 offset:8192
v_mfma_f32_16x16x32_bf16 a[100:103], v[100:103], v[76:79], a[100:103]
buffer_load_dwordx4 v18, s[8:11], s43 offen offset:2048 nt lds
v_mfma_f32_16x16x32_bf16 a[104:107], v[104:107], v[76:79], a[104:107]
ds_read_b64_tr_b16 v[178:179], v198 offset:9216
v_mfma_f32_16x16x32_bf16 a[108:111], v[108:111], v[76:79], a[108:111]
v_mfma_f32_16x16x32_bf16 a[112:115], v[112:115], v[76:79], a[112:115]
ds_read_b128 v[144:147], v193 offset:8192
v_mfma_f32_16x16x32_bf16 a[116:119], v[116:119], v[76:79], a[116:119]
buffer_load_dwordx4 v19, s[8:11], s43 offen offset:3072 nt lds
v_mfma_f32_16x16x32_bf16 a[120:123], v[120:123], v[76:79], a[120:123]
ds_read_b64_tr_b16 v[180:181], v199 offset:8192
v_mfma_f32_16x16x32_bf16 a[124:127], v[124:127], v[76:79], a[124:127]
v_mfma_f32_16x16x32_bf16 a[128:131], v[96:99], v[80:83], a[128:131]
ds_read_b64_tr_b16 v[182:183], v199 offset:9216
v_mfma_f32_16x16x32_bf16 a[132:135], v[100:103], v[80:83], a[132:135]
v_mfma_f32_16x16x32_bf16 a[136:139], v[104:107], v[80:83], a[136:139]
ds_read_b128 v[148:151], v193 offset:10240
v_mfma_f32_16x16x32_bf16 a[140:143], v[108:111], v[80:83], a[140:143]
v_mfma_f32_16x16x32_bf16 a[144:147], v[112:115], v[80:83], a[144:147]
ds_read_b64_tr_b16 v[184:185], v200 offset:8192
v_mfma_f32_16x16x32_bf16 a[148:151], v[116:119], v[80:83], a[148:151]
v_mfma_f32_16x16x32_bf16 a[152:155], v[120:123], v[80:83], a[152:155]
ds_read_b64_tr_b16 v[186:187], v200 offset:9216
v_mfma_f32_16x16x32_bf16 a[156:159], v[124:127], v[80:83], a[156:159]
v_mfma_f32_16x16x32_bf16 a[160:163], v[96:99], v[84:87], a[160:163]
s_add_u32 s42, s42, 524288
ds_read_b128 v[152:155], v193 offset:12288
v_mfma_f32_16x16x32_bf16 a[164:167], v[100:103], v[84:87], a[164:167]
s_add_u32 s43, s43, 524288
v_mfma_f32_16x16x32_bf16 a[168:171], v[104:107], v[84:87], a[168:171]
ds_read_b64_tr_b16 v[188:189], v201 offset:8192
v_mfma_f32_16x16x32_bf16 a[172:175], v[108:111], v[84:87], a[172:175]
v_mfma_f32_16x16x32_bf16 a[176:179], v[112:115], v[84:87], a[176:179]
s_add_u32 s44, s35, 65536
ds_read_b64_tr_b16 v[190:191], v201 offset:9216
v_mfma_f32_16x16x32_bf16 a[180:183], v[116:119], v[84:87], a[180:183]
s_cmp_ge_u32 s44, s39
s_cselect_b32 s45, s55, s54
v_mfma_f32_16x16x32_bf16 a[184:187], v[120:123], v[84:87], a[184:187]
ds_read_b128 v[156:159], v193 offset:14336
v_mfma_f32_16x16x32_bf16 a[188:191], v[124:127], v[84:87], a[188:191]
s_add_u32 s44, s36, 65536
v_mfma_f32_16x16x32_bf16 a[192:195], v[96:99], v[88:91], a[192:195]
v_mfma_f32_16x16x32_bf16 a[196:199], v[100:103], v[88:91], a[196:199]
s_cmp_ge_u32 s44, s39
s_cselect_b32 s34, s55, s54
v_mfma_f32_16x16x32_bf16 a[200:203], v[104:107], v[88:91], a[200:203]
v_add_u32 v194, s45, v194
v_mfma_f32_16x16x32_bf16 a[204:207], v[108:111], v[88:91], a[204:207]
v_add_u32 v195, s45, v195
v_mfma_f32_16x16x32_bf16 a[208:211], v[112:115], v[88:91], a[208:211]
v_add_u32 v196, s45, v196
v_mfma_f32_16x16x32_bf16 a[212:215], v[116:119], v[88:91], a[212:215]
v_add_u32 v197, s45, v197
v_mfma_f32_16x16x32_bf16 a[216:219], v[120:123], v[88:91], a[216:219]
v_add_u32 v198, s45, v198
v_mfma_f32_16x16x32_bf16 a[220:223], v[124:127], v[88:91], a[220:223]
v_add_u32 v199, s45, v199
v_mfma_f32_16x16x32_bf16 a[224:227], v[96:99], v[92:95], a[224:227]
v_add_u32 v200, s45, v200
v_mfma_f32_16x16x32_bf16 a[228:231], v[100:103], v[92:95], a[228:231]
v_add_u32 v201, s45, v201
v_mfma_f32_16x16x32_bf16 a[232:235], v[104:107], v[92:95], a[232:235]
v_add_u32 v192, s34, v192
v_mfma_f32_16x16x32_bf16 a[236:239], v[108:111], v[92:95], a[236:239]
v_add_u32 v193, s34, v193
v_mfma_f32_16x16x32_bf16 a[240:243], v[112:115], v[92:95], a[240:243]
v_mfma_f32_16x16x32_bf16 a[244:247], v[116:119], v[92:95], a[244:247]
v_mfma_f32_16x16x32_bf16 a[248:251], v[120:123], v[92:95], a[248:251]
v_mfma_f32_16x16x32_bf16 a[252:255], v[124:127], v[92:95], a[252:255]
s_waitcnt vmcnt(10) lgkmcnt(0)
s_barrier
v_mfma_f32_16x16x32_bf16 a[0:3], v[160:163], v[128:131], a[0:3]
s_mov_b32 m0, s35
ds_read_b64_tr_b16 v[96:97], v194 offset:0
v_mfma_f32_16x16x32_bf16 a[4:7], v[164:167], v[128:131], a[4:7]
buffer_load_dwordx4 v202, s[4:7], s40 offen offset:0 lds
v_mfma_f32_16x16x32_bf16 a[8:11], v[168:171], v[128:131], a[8:11]
ds_read_b64_tr_b16 v[98:99], v194 offset:1024
v_mfma_f32_16x16x32_bf16 a[12:15], v[172:175], v[128:131], a[12:15]
v_mfma_f32_16x16x32_bf16 a[16:19], v[176:179], v[128:131], a[16:19]
ds_read_b128 v[64:67], v192 offset:0
v_mfma_f32_16x16x32_bf16 a[20:23], v[180:183], v[128:131], a[20:23]
buffer_load_dwordx4 v203, s[4:7], s40 offen offset:1024 lds
v_mfma_f32_16x16x32_bf16 a[24:27], v[184:187], v[128:131], a[24:27]
ds_read_b64_tr_b16 v[100:101], v195 offset:0
v_mfma_f32_16x16x32_bf16 a[28:31], v[188:191], v[128:131], a[28:31]
v_mfma_f32_16x16x32_bf16 a[32:35], v[160:163], v[132:135], a[32:35]
ds_read_b64_tr_b16 v[102:103], v195 offset:1024
v_mfma_f32_16x16x32_bf16 a[36:39], v[164:167], v[132:135], a[36:39]
buffer_load_dwordx4 v204, s[4:7], s40 offen offset:2048 lds
v_mfma_f32_16x16x32_bf16 a[40:43], v[168:171], v[132:135], a[40:43]
ds_read_b128 v[68:71], v192 offset:2048
v_mfma_f32_16x16x32_bf16 a[44:47], v[172:175], v[132:135], a[44:47]
v_mfma_f32_16x16x32_bf16 a[48:51], v[176:179], v[132:135], a[48:51]
ds_read_b64_tr_b16 v[104:105], v196 offset:0
v_mfma_f32_16x16x32_bf16 a[52:55], v[180:183], v[132:135], a[52:55]
buffer_load_dwordx4 v205, s[4:7], s40 offen offset:3072 lds
v_mfma_f32_16x16x32_bf16 a[56:59], v[184:187], v[132:135], a[56:59]
ds_read_b64_tr_b16 v[106:107], v196 offset:1024
v_mfma_f32_16x16x32_bf16 a[60:63], v[188:191], v[132:135], a[60:63]
v_mfma_f32_16x16x32_bf16 a[64:67], v[160:163], v[136:139], a[64:67]
s_add_u32 m0, s35, 4096
ds_read_b128 v[72:75], v192 offset:4096
v_mfma_f32_16x16x32_bf16 a[68:71], v[164:167], v[136:139], a[68:71]
buffer_load_dwordx4 v202, s[4:7], s41 offen offset:0 lds
v_mfma_f32_16x16x32_bf16 a[72:75], v[168:171], v[136:139], a[72:75]
ds_read_b64_tr_b16 v[108:109], v197 offset:0
v_mfma_f32_16x16x32_bf16 a[76:79], v[172:175], v[136:139], a[76:79]
v_mfma_f32_16x16x32_bf16 a[80:83], v[176:179], v[136:139], a[80:83]
ds_read_b64_tr_b16 v[110:111], v197 offset:1024
v_mfma_f32_16x16x32_bf16 a[84:87], v[180:183], v[136:139], a[84:87]
buffer_load_dwordx4 v203, s[4:7], s41 offen offset:1024 lds
v_mfma_f32_16x16x32_bf16 a[88:91], v[184:187], v[136:139], a[88:91]
ds_read_b128 v[76:79], v192 offset:6144
v_mfma_f32_16x16x32_bf16 a[92:95], v[188:191], v[136:139], a[92:95]
v_mfma_f32_16x16x32_bf16 a[96:99], v[160:163], v[140:143], a[96:99]
ds_read_b64_tr_b16 v[112:113], v198 offset:0
v_mfma_f32_16x16x32_bf16 a[100:103], v[164:167], v[140:143], a[100:103]
buffer_load_dwordx4 v204, s[4:7], s41 offen offset:2048 lds
v_mfma_f32_16x16x32_bf16 a[104:107], v[168:171], v[140:143], a[104:107]
ds_read_b64_tr_b16 v[114:115], v198 offset:1024
v_mfma_f32_16x16x32_bf16 a[108:111], v[172:175], v[140:143], a[108:111]
v_mfma_f32_16x16x32_bf16 a[112:115], v[176:179], v[140:143], a[112:115]
ds_read_b128 v[80:83], v192 offset:8192
v_mfma_f32_16x16x32_bf16 a[116:119], v[180:183], v[140:143], a[116:119]
buffer_load_dwordx4 v205, s[4:7], s41 offen offset:3072 lds
v_mfma_f32_16x16x32_bf16 a[120:123], v[184:187], v[140:143], a[120:123]
ds_read_b64_tr_b16 v[116:117], v199 offset:0
v_mfma_f32_16x16x32_bf16 a[124:127], v[188:191], v[140:143], a[124:127]
v_mfma_f32_16x16x32_bf16 a[128:131], v[160:163], v[144:147], a[128:131]
ds_read_b64_tr_b16 v[118:119], v199 offset:1024
v_mfma_f32_16x16x32_bf16 a[132:135], v[164:167], v[144:147], a[132:135]
buffer_load_dword v35, v206, s[56:59], s48 offen
v_mfma_f32_16x16x32_bf16 a[136:139], v[168:171], v[144:147], a[136:139]
ds_read_b128 v[84:87], v192 offset:10240
v_mfma_f32_16x16x32_bf16 a[140:143], v[172:175], v[144:147], a[140:143]
v_mfma_f32_16x16x32_bf16 a[144:147], v[176:179], v[144:147], a[144:147]
ds_read_b64_tr_b16 v[120:121], v200 offset:0
v_mfma_f32_16x16x32_bf16 a[148:151], v[180:183], v[144:147], a[148:151]
buffer_load_dword v35, v207, s[12:15], s49 offen
v_mfma_f32_16x16x32_bf16 a[152:155], v[184:187], v[144:147], a[152:155]
ds_read_b64_tr_b16 v[122:123], v200 offset:1024
v_mfma_f32_16x16x32_bf16 a[156:159], v[188:191], v[144:147], a[156:159]
v_mfma_f32_16x16x32_bf16 a[160:163], v[160:163], v[148:151], a[160:163]
s_add_u32 s40, s40, 128
ds_read_b128 v[88:91], v192 offset:12288
v_mfma_f32_16x16x32_bf16 a[164:167], v[164:167], v[148:151], a[164:167]
s_add_u32 s41, s41, 128
v_mfma_f32_16x16x32_bf16 a[168:171], v[168:171], v[148:151], a[168:171]
ds_read_b64_tr_b16 v[124:125], v201 offset:0
v_mfma_f32_16x16x32_bf16 a[172:175], v[172:175], v[148:151], a[172:175]
s_add_u32 s48, s48, 0x100000
v_mfma_f32_16x16x32_bf16 a[176:179], v[176:179], v[148:151], a[176:179]
ds_read_b64_tr_b16 v[126:127], v201 offset:1024
v_mfma_f32_16x16x32_bf16 a[180:183], v[180:183], v[148:151], a[180:183]
s_add_u32 s49, s49, 655360
v_mfma_f32_16x16x32_bf16 a[184:187], v[184:187], v[148:151], a[184:187]
ds_read_b128 v[92:95], v192 offset:14336
v_mfma_f32_16x16x32_bf16 a[188:191], v[188:191], v[148:151], a[188:191]
v_mfma_f32_16x16x32_bf16 a[192:195], v[160:163], v[152:155], a[192:195]
v_mfma_f32_16x16x32_bf16 a[196:199], v[164:167], v[152:155], a[196:199]
v_mfma_f32_16x16x32_bf16 a[200:203], v[168:171], v[152:155], a[200:203]
v_mfma_f32_16x16x32_bf16 a[204:207], v[172:175], v[152:155], a[204:207]
v_mfma_f32_16x16x32_bf16 a[208:211], v[176:179], v[152:155], a[208:211]
v_mfma_f32_16x16x32_bf16 a[212:215], v[180:183], v[152:155], a[212:215]
v_mfma_f32_16x16x32_bf16 a[216:219], v[184:187], v[152:155], a[216:219]
v_mfma_f32_16x16x32_bf16 a[220:223], v[188:191], v[152:155], a[220:223]
v_mfma_f32_16x16x32_bf16 a[224:227], v[160:163], v[156:159], a[224:227]
v_mfma_f32_16x16x32_bf16 a[228:231], v[164:167], v[156:159], a[228:231]
s_mov_b32 s37, s36
v_mfma_f32_16x16x32_bf16 a[232:235], v[168:171], v[156:159], a[232:235]
s_add_u32 s35, s35, s45
v_mfma_f32_16x16x32_bf16 a[236:239], v[172:175], v[156:159], a[236:239]
s_add_u32 s36, s36, s34
v_mfma_f32_16x16x32_bf16 a[240:243], v[176:179], v[156:159], a[240:243]
s_add_u32 s38, s38, 1
v_mfma_f32_16x16x32_bf16 a[244:247], v[180:183], v[156:159], a[244:247]
s_cmp_lt_u32 s38, 6
v_mfma_f32_16x16x32_bf16 a[248:251], v[184:187], v[156:159], a[248:251]
v_mfma_f32_16x16x32_bf16 a[252:255], v[188:191], v[156:159], a[252:255]
s_waitcnt lgkmcnt(0)
s_cbranch_scc1 .Lp1_kloop
v_mfma_f32_16x16x32_bf16 a[0:3], v[96:99], v[64:67], a[0:3]
s_add_u32 m0, s92, 32768
ds_read_b64_tr_b16 v[160:161], v194 offset:8192
v_mfma_f32_16x16x32_bf16 a[4:7], v[100:103], v[64:67], a[4:7]
buffer_load_dwordx4 v20, s[12:15], s24 offen offset:0 lds
v_mfma_f32_16x16x32_bf16 a[8:11], v[104:107], v[64:67], a[8:11]
ds_read_b64_tr_b16 v[162:163], v194 offset:9216
v_mfma_f32_16x16x32_bf16 a[12:15], v[108:111], v[64:67], a[12:15]
v_mfma_f32_16x16x32_bf16 a[16:19], v[112:115], v[64:67], a[16:19]
ds_read_b128 v[128:131], v193 offset:0
v_mfma_f32_16x16x32_bf16 a[20:23], v[116:119], v[64:67], a[20:23]
buffer_load_dwordx4 v21, s[12:15], s24 offen offset:1024 lds
v_mfma_f32_16x16x32_bf16 a[24:27], v[120:123], v[64:67], a[24:27]
ds_read_b64_tr_b16 v[164:165], v195 offset:8192
v_mfma_f32_16x16x32_bf16 a[28:31], v[124:127], v[64:67], a[28:31]
v_mfma_f32_16x16x32_bf16 a[32:35], v[96:99], v[68:71], a[32:35]
ds_read_b64_tr_b16 v[166:167], v195 offset:9216
v_mfma_f32_16x16x32_bf16 a[36:39], v[100:103], v[68:71], a[36:39]
buffer_load_dwordx4 v22, s[12:15], s24 offen offset:2048 lds
v_mfma_f32_16x16x32_bf16 a[40:43], v[104:107], v[68:71], a[40:43]
ds_read_b128 v[132:135], v193 offset:2048
v_mfma_f32_16x16x32_bf16 a[44:47], v[108:111], v[68:71], a[44:47]
v_mfma_f32_16x16x32_bf16 a[48:51], v[112:115], v[68:71], a[48:51]
ds_read_b64_tr_b16 v[168:169], v196 offset:8192
v_mfma_f32_16x16x32_bf16 a[52:55], v[116:119], v[68:71], a[52:55]
buffer_load_dwordx4 v23, s[12:15], s24 offen offset:3072 lds
v_mfma_f32_16x16x32_bf16 a[56:59], v[120:123], v[68:71], a[56:59]
ds_read_b64_tr_b16 v[170:171], v196 offset:9216
v_mfma_f32_16x16x32_bf16 a[60:63], v[124:127], v[68:71], a[60:63]
v_mfma_f32_16x16x32_bf16 a[64:67], v[96:99], v[72:75], a[64:67]
s_add_u32 m0, s92, 36864
ds_read_b128 v[136:139], v193 offset:4096
v_mfma_f32_16x16x32_bf16 a[68:71], v[100:103], v[72:75], a[68:71]
buffer_load_dwordx4 v20, s[12:15], s25 offen offset:0 lds
v_mfma_f32_16x16x32_bf16 a[72:75], v[104:107], v[72:75], a[72:75]
ds_read_b64_tr_b16 v[172:173], v197 offset:8192
v_mfma_f32_16x16x32_bf16 a[76:79], v[108:111], v[72:75], a[76:79]
v_mfma_f32_16x16x32_bf16 a[80:83], v[112:115], v[72:75], a[80:83]
ds_read_b64_tr_b16 v[174:175], v197 offset:9216
v_mfma_f32_16x16x32_bf16 a[84:87], v[116:119], v[72:75], a[84:87]
buffer_load_dwordx4 v21, s[12:15], s25 offen offset:1024 lds
v_mfma_f32_16x16x32_bf16 a[88:91], v[120:123], v[72:75], a[88:91]
ds_read_b128 v[140:143], v193 offset:6144
v_mfma_f32_16x16x32_bf16 a[92:95], v[124:127], v[72:75], a[92:95]
v_mfma_f32_16x16x32_bf16 a[96:99], v[96:99], v[76:79], a[96:99]
ds_read_b64_tr_b16 v[176:177], v198 offset:8192
v_mfma_f32_16x16x32_bf16 a[100:103], v[100:103], v[76:79], a[100:103]
buffer_load_dwordx4 v22, s[12:15], s25 offen offset:2048 lds
v_mfma_f32_16x16x32_bf16 a[104:107], v[104:107], v[76:79], a[104:107]
ds_read_b64_tr_b16 v[178:179], v198 offset:9216
v_mfma_f32_16x16x32_bf16 a[108:111], v[108:111], v[76:79], a[108:111]
v_mfma_f32_16x16x32_bf16 a[112:115], v[112:115], v[76:79], a[112:115]
ds_read_b128 v[144:147], v193 offset:8192
v_mfma_f32_16x16x32_bf16 a[116:119], v[116:119], v[76:79], a[116:119]
buffer_load_dwordx4 v23, s[12:15], s25 offen offset:3072 lds
v_mfma_f32_16x16x32_bf16 a[120:123], v[120:123], v[76:79], a[120:123]
ds_read_b64_tr_b16 v[180:181], v199 offset:8192
v_mfma_f32_16x16x32_bf16 a[124:127], v[124:127], v[76:79], a[124:127]
v_mfma_f32_16x16x32_bf16 a[128:131], v[96:99], v[80:83], a[128:131]
ds_read_b64_tr_b16 v[182:183], v199 offset:9216
v_mfma_f32_16x16x32_bf16 a[132:135], v[100:103], v[80:83], a[132:135]
v_mfma_f32_16x16x32_bf16 a[136:139], v[104:107], v[80:83], a[136:139]
ds_read_b128 v[148:151], v193 offset:10240
v_mfma_f32_16x16x32_bf16 a[140:143], v[108:111], v[80:83], a[140:143]
v_mfma_f32_16x16x32_bf16 a[144:147], v[112:115], v[80:83], a[144:147]
ds_read_b64_tr_b16 v[184:185], v200 offset:8192
v_mfma_f32_16x16x32_bf16 a[148:151], v[116:119], v[80:83], a[148:151]
v_mfma_f32_16x16x32_bf16 a[152:155], v[120:123], v[80:83], a[152:155]
ds_read_b64_tr_b16 v[186:187], v200 offset:9216
v_mfma_f32_16x16x32_bf16 a[156:159], v[124:127], v[80:83], a[156:159]
v_mfma_f32_16x16x32_bf16 a[160:163], v[96:99], v[84:87], a[160:163]
s_add_u32 s24, s24, 128
s_add_u32 s25, s25, 128
ds_read_b128 v[152:155], v193 offset:12288
v_mfma_f32_16x16x32_bf16 a[164:167], v[100:103], v[84:87], a[164:167]
v_mfma_f32_16x16x32_bf16 a[168:171], v[104:107], v[84:87], a[168:171]
ds_read_b64_tr_b16 v[188:189], v201 offset:8192
v_mfma_f32_16x16x32_bf16 a[172:175], v[108:111], v[84:87], a[172:175]
v_mfma_f32_16x16x32_bf16 a[176:179], v[112:115], v[84:87], a[176:179]
s_add_u32 s44, s35, 65536
ds_read_b64_tr_b16 v[190:191], v201 offset:9216
v_mfma_f32_16x16x32_bf16 a[180:183], v[116:119], v[84:87], a[180:183]
s_cmp_ge_u32 s44, s39
s_cselect_b32 s45, s55, s54
v_mfma_f32_16x16x32_bf16 a[184:187], v[120:123], v[84:87], a[184:187]
ds_read_b128 v[156:159], v193 offset:14336
v_mfma_f32_16x16x32_bf16 a[188:191], v[124:127], v[84:87], a[188:191]
s_add_u32 s44, s36, 65536
v_mfma_f32_16x16x32_bf16 a[192:195], v[96:99], v[88:91], a[192:195]
v_mfma_f32_16x16x32_bf16 a[196:199], v[100:103], v[88:91], a[196:199]
s_cmp_ge_u32 s44, s39
s_cselect_b32 s34, s55, s54
v_mfma_f32_16x16x32_bf16 a[200:203], v[104:107], v[88:91], a[200:203]
v_add_u32 v194, s45, v194
v_mfma_f32_16x16x32_bf16 a[204:207], v[108:111], v[88:91], a[204:207]
v_add_u32 v195, s45, v195
v_mfma_f32_16x16x32_bf16 a[208:211], v[112:115], v[88:91], a[208:211]
v_add_u32 v196, s45, v196
v_mfma_f32_16x16x32_bf16 a[212:215], v[116:119], v[88:91], a[212:215]
v_add_u32 v197, s45, v197
v_mfma_f32_16x16x32_bf16 a[216:219], v[120:123], v[88:91], a[216:219]
v_add_u32 v198, s45, v198
v_mfma_f32_16x16x32_bf16 a[220:223], v[124:127], v[88:91], a[220:223]
v_add_u32 v199, s45, v199
v_mfma_f32_16x16x32_bf16 a[224:227], v[96:99], v[92:95], a[224:227]
v_add_u32 v200, s45, v200
v_mfma_f32_16x16x32_bf16 a[228:231], v[100:103], v[92:95], a[228:231]
v_add_u32 v201, s45, v201
v_mfma_f32_16x16x32_bf16 a[232:235], v[104:107], v[92:95], a[232:235]
v_add_u32 v192, s34, v192
v_mfma_f32_16x16x32_bf16 a[236:239], v[108:111], v[92:95], a[236:239]
v_add_u32 v193, s34, v193
v_mfma_f32_16x16x32_bf16 a[240:243], v[112:115], v[92:95], a[240:243]
v_mfma_f32_16x16x32_bf16 a[244:247], v[116:119], v[92:95], a[244:247]
v_mfma_f32_16x16x32_bf16 a[248:251], v[120:123], v[92:95], a[248:251]
v_mfma_f32_16x16x32_bf16 a[252:255], v[124:127], v[92:95], a[252:255]
s_waitcnt vmcnt(10) lgkmcnt(0)
s_barrier
v_mfma_f32_16x16x32_bf16 a[0:3], v[160:163], v[128:131], a[0:3]
s_add_u32 m0, s91, 65536
ds_read_b64_tr_b16 v[96:97], v194 offset:0
v_mfma_f32_16x16x32_bf16 a[4:7], v[164:167], v[128:131], a[4:7]
buffer_load_dwordx4 v16, s[16:19], s26 offen offset:0 lds
v_mfma_f32_16x16x32_bf16 a[8:11], v[168:171], v[128:131], a[8:11]
ds_read_b64_tr_b16 v[98:99], v194 offset:1024
v_mfma_f32_16x16x32_bf16 a[12:15], v[172:175], v[128:131], a[12:15]
v_mfma_f32_16x16x32_bf16 a[16:19], v[176:179], v[128:131], a[16:19]
ds_read_b128 v[64:67], v192 offset:0
v_mfma_f32_16x16x32_bf16 a[20:23], v[180:183], v[128:131], a[20:23]
buffer_load_dwordx4 v17, s[16:19], s26 offen offset:1024 lds
v_mfma_f32_16x16x32_bf16 a[24:27], v[184:187], v[128:131], a[24:27]
ds_read_b64_tr_b16 v[100:101], v195 offset:0
v_mfma_f32_16x16x32_bf16 a[28:31], v[188:191], v[128:131], a[28:31]
v_mfma_f32_16x16x32_bf16 a[32:35], v[160:163], v[132:135], a[32:35]
ds_read_b64_tr_b16 v[102:103], v195 offset:1024
v_mfma_f32_16x16x32_bf16 a[36:39], v[164:167], v[132:135], a[36:39]
buffer_load_dwordx4 v18, s[16:19], s26 offen offset:2048 lds
v_mfma_f32_16x16x32_bf16 a[40:43], v[168:171], v[132:135], a[40:43]
ds_read_b128 v[68:71], v192 offset:2048
v_mfma_f32_16x16x32_bf16 a[44:47], v[172:175], v[132:135], a[44:47]
v_mfma_f32_16x16x32_bf16 a[48:51], v[176:179], v[132:135], a[48:51]
ds_read_b64_tr_b16 v[104:105], v196 offset:0
v_mfma_f32_16x16x32_bf16 a[52:55], v[180:183], v[132:135], a[52:55]
buffer_load_dwordx4 v19, s[16:19], s26 offen offset:3072 lds
v_mfma_f32_16x16x32_bf16 a[56:59], v[184:187], v[132:135], a[56:59]
ds_read_b64_tr_b16 v[106:107], v196 offset:1024
v_mfma_f32_16x16x32_bf16 a[60:63], v[188:191], v[132:135], a[60:63]
s_add_u32 s26, s26, 524288
v_mfma_f32_16x16x32_bf16 a[64:67], v[160:163], v[136:139], a[64:67]
s_add_u32 m0, s91, 81920
ds_read_b128 v[72:75], v192 offset:4096
v_mfma_f32_16x16x32_bf16 a[68:71], v[164:167], v[136:139], a[68:71]
buffer_load_dwordx4 v16, s[16:19], s26 offen offset:0 lds
v_mfma_f32_16x16x32_bf16 a[72:75], v[168:171], v[136:139], a[72:75]
ds_read_b64_tr_b16 v[108:109], v197 offset:0
v_mfma_f32_16x16x32_bf16 a[76:79], v[172:175], v[136:139], a[76:79]
v_mfma_f32_16x16x32_bf16 a[80:83], v[176:179], v[136:139], a[80:83]
ds_read_b64_tr_b16 v[110:111], v197 offset:1024
v_mfma_f32_16x16x32_bf16 a[84:87], v[180:183], v[136:139], a[84:87]
buffer_load_dwordx4 v17, s[16:19], s26 offen offset:1024 lds
v_mfma_f32_16x16x32_bf16 a[88:91], v[184:187], v[136:139], a[88:91]
ds_read_b128 v[76:79], v192 offset:6144
v_mfma_f32_16x16x32_bf16 a[92:95], v[188:191], v[136:139], a[92:95]
v_mfma_f32_16x16x32_bf16 a[96:99], v[160:163], v[140:143], a[96:99]
ds_read_b64_tr_b16 v[112:113], v198 offset:0
v_mfma_f32_16x16x32_bf16 a[100:103], v[164:167], v[140:143], a[100:103]
buffer_load_dwordx4 v18, s[16:19], s26 offen offset:2048 lds
v_mfma_f32_16x16x32_bf16 a[104:107], v[168:171], v[140:143], a[104:107]
ds_read_b64_tr_b16 v[114:115], v198 offset:1024
v_mfma_f32_16x16x32_bf16 a[108:111], v[172:175], v[140:143], a[108:111]
v_mfma_f32_16x16x32_bf16 a[112:115], v[176:179], v[140:143], a[112:115]
ds_read_b128 v[80:83], v192 offset:8192
v_mfma_f32_16x16x32_bf16 a[116:119], v[180:183], v[140:143], a[116:119]
buffer_load_dwordx4 v19, s[16:19], s26 offen offset:3072 lds
v_mfma_f32_16x16x32_bf16 a[120:123], v[184:187], v[140:143], a[120:123]
ds_read_b64_tr_b16 v[116:117], v199 offset:0
v_mfma_f32_16x16x32_bf16 a[124:127], v[188:191], v[140:143], a[124:127]
s_add_u32 s26, s26, 524288
v_mfma_f32_16x16x32_bf16 a[128:131], v[160:163], v[144:147], a[128:131]
ds_read_b64_tr_b16 v[118:119], v199 offset:1024
v_mfma_f32_16x16x32_bf16 a[132:135], v[164:167], v[144:147], a[132:135]
buffer_load_dword v35, v206, s[56:59], s48 offen
v_mfma_f32_16x16x32_bf16 a[136:139], v[168:171], v[144:147], a[136:139]
ds_read_b128 v[84:87], v192 offset:10240
v_mfma_f32_16x16x32_bf16 a[140:143], v[172:175], v[144:147], a[140:143]
v_mfma_f32_16x16x32_bf16 a[144:147], v[176:179], v[144:147], a[144:147]
ds_read_b64_tr_b16 v[120:121], v200 offset:0
v_mfma_f32_16x16x32_bf16 a[148:151], v[180:183], v[144:147], a[148:151]
buffer_load_dword v35, v207, s[12:15], s49 offen
v_add_u32 v11, 8, v11
v_and_b32 v11, -8, v11
v_mfma_f32_16x16x32_bf16 a[152:155], v[184:187], v[144:147], a[152:155]
ds_read_b64_tr_b16 v[122:123], v200 offset:1024
v_mfma_f32_16x16x32_bf16 a[156:159], v[188:191], v[144:147], a[156:159]
v_mfma_f32_16x16x32_bf16 a[160:163], v[160:163], v[148:151], a[160:163]
ds_read_b128 v[88:91], v192 offset:12288
v_mfma_f32_16x16x32_bf16 a[164:167], v[164:167], v[148:151], a[164:167]
s_nop 0
v_or_b32 v0, 0, v11
v_mfma_f32_16x16x32_bf16 a[168:171], v[168:171], v[148:151], a[168:171]
ds_read_b64_tr_b16 v[124:125], v201 offset:0
v_mfma_f32_16x16x32_bf16 a[172:175], v[172:175], v[148:151], a[172:175]
s_add_u32 s48, s48, 0x100000
v_mfma_f32_16x16x32_bf16 a[176:179], v[176:179], v[148:151], a[176:179]
ds_read_b64_tr_b16 v[126:127], v201 offset:1024
v_mfma_f32_16x16x32_bf16 a[180:183], v[180:183], v[148:151], a[180:183]
s_add_u32 s49, s49, 655360
v_mfma_f32_16x16x32_bf16 a[184:187], v[184:187], v[148:151], a[184:187]
ds_read_b128 v[92:95], v192 offset:14336
v_mfma_f32_16x16x32_bf16 a[188:191], v[188:191], v[148:151], a[188:191]
buffer_store_dword v0, v6, s[60:63], 0 offen sc1
v_mfma_f32_16x16x32_bf16 a[192:195], v[160:163], v[152:155], a[192:195]
v_mfma_f32_16x16x32_bf16 a[196:199], v[164:167], v[152:155], a[196:199]
v_mfma_f32_16x16x32_bf16 a[200:203], v[168:171], v[152:155], a[200:203]
v_mfma_f32_16x16x32_bf16 a[204:207], v[172:175], v[152:155], a[204:207]
buffer_load_dword v35, v4, s[96:99], 0 offen
v_mfma_f32_16x16x32_bf16 a[208:211], v[176:179], v[152:155], a[208:211]
v_mfma_f32_16x16x32_bf16 a[212:215], v[180:183], v[152:155], a[212:215]
v_mfma_f32_16x16x32_bf16 a[216:219], v[184:187], v[152:155], a[216:219]
v_mfma_f32_16x16x32_bf16 a[220:223], v[188:191], v[152:155], a[220:223]
v_mfma_f32_16x16x32_bf16 a[224:227], v[160:163], v[156:159], a[224:227]
v_mfma_f32_16x16x32_bf16 a[228:231], v[164:167], v[156:159], a[228:231]
s_mov_b32 s37, s36
v_mfma_f32_16x16x32_bf16 a[232:235], v[168:171], v[156:159], a[232:235]
s_add_u32 s35, s35, s45
v_mfma_f32_16x16x32_bf16 a[236:239], v[172:175], v[156:159], a[236:239]
s_add_u32 s36, s36, s34
v_mfma_f32_16x16x32_bf16 a[240:243], v[176:179], v[156:159], a[240:243]
v_mfma_f32_16x16x32_bf16 a[244:247], v[180:183], v[156:159], a[244:247]
v_mfma_f32_16x16x32_bf16 a[248:251], v[184:187], v[156:159], a[248:251]
v_mfma_f32_16x16x32_bf16 a[252:255], v[188:191], v[156:159], a[252:255]
s_waitcnt lgkmcnt(0)
v_mfma_f32_16x16x32_bf16 a[0:3], v[96:99], v[64:67], a[0:3]
s_add_u32 m0, s92, 98304
ds_read_b64_tr_b16 v[160:161], v194 offset:8192
v_mfma_f32_16x16x32_bf16 a[4:7], v[100:103], v[64:67], a[4:7]
buffer_load_dwordx4 v20, s[12:15], s24 offen offset:0 lds
v_mfma_f32_16x16x32_bf16 a[8:11], v[104:107], v[64:67], a[8:11]
ds_read_b64_tr_b16 v[162:163], v194 offset:9216
v_mfma_f32_16x16x32_bf16 a[12:15], v[108:111], v[64:67], a[12:15]
v_mfma_f32_16x16x32_bf16 a[16:19], v[112:115], v[64:67], a[16:19]
ds_read_b128 v[128:131], v193 offset:0
v_mfma_f32_16x16x32_bf16 a[20:23], v[116:119], v[64:67], a[20:23]
buffer_load_dwordx4 v21, s[12:15], s24 offen offset:1024 lds
v_mfma_f32_16x16x32_bf16 a[24:27], v[120:123], v[64:67], a[24:27]
ds_read_b64_tr_b16 v[164:165], v195 offset:8192
v_mfma_f32_16x16x32_bf16 a[28:31], v[124:127], v[64:67], a[28:31]
v_mfma_f32_16x16x32_bf16 a[32:35], v[96:99], v[68:71], a[32:35]
ds_read_b64_tr_b16 v[166:167], v195 offset:9216
v_mfma_f32_16x16x32_bf16 a[36:39], v[100:103], v[68:71], a[36:39]
buffer_load_dwordx4 v22, s[12:15], s24 offen offset:2048 lds
v_mfma_f32_16x16x32_bf16 a[40:43], v[104:107], v[68:71], a[40:43]
ds_read_b128 v[132:135], v193 offset:2048
v_mfma_f32_16x16x32_bf16 a[44:47], v[108:111], v[68:71], a[44:47]
v_mfma_f32_16x16x32_bf16 a[48:51], v[112:115], v[68:71], a[48:51]
ds_read_b64_tr_b16 v[168:169], v196 offset:8192
v_mfma_f32_16x16x32_bf16 a[52:55], v[116:119], v[68:71], a[52:55]
buffer_load_dwordx4 v23, s[12:15], s24 offen offset:3072 lds
v_mfma_f32_16x16x32_bf16 a[56:59], v[120:123], v[68:71], a[56:59]
ds_read_b64_tr_b16 v[170:171], v196 offset:9216
v_mfma_f32_16x16x32_bf16 a[60:63], v[124:127], v[68:71], a[60:63]
v_mfma_f32_16x16x32_bf16 a[64:67], v[96:99], v[72:75], a[64:67]
s_add_u32 m0, s92, 102400
ds_read_b128 v[136:139], v193 offset:4096
v_mfma_f32_16x16x32_bf16 a[68:71], v[100:103], v[72:75], a[68:71]
buffer_load_dwordx4 v20, s[12:15], s25 offen offset:0 lds
v_mfma_f32_16x16x32_bf16 a[72:75], v[104:107], v[72:75], a[72:75]
ds_read_b64_tr_b16 v[172:173], v197 offset:8192
v_mfma_f32_16x16x32_bf16 a[76:79], v[108:111], v[72:75], a[76:79]
v_mfma_f32_16x16x32_bf16 a[80:83], v[112:115], v[72:75], a[80:83]
ds_read_b64_tr_b16 v[174:175], v197 offset:9216
v_mfma_f32_16x16x32_bf16 a[84:87], v[116:119], v[72:75], a[84:87]
buffer_load_dwordx4 v21, s[12:15], s25 offen offset:1024 lds
v_mfma_f32_16x16x32_bf16 a[88:91], v[120:123], v[72:75], a[88:91]
ds_read_b128 v[140:143], v193 offset:6144
v_mfma_f32_16x16x32_bf16 a[92:95], v[124:127], v[72:75], a[92:95]
v_mfma_f32_16x16x32_bf16 a[96:99], v[96:99], v[76:79], a[96:99]
ds_read_b64_tr_b16 v[176:177], v198 offset:8192
v_mfma_f32_16x16x32_bf16 a[100:103], v[100:103], v[76:79], a[100:103]
buffer_load_dwordx4 v22, s[12:15], s25 offen offset:2048 lds
v_mfma_f32_16x16x32_bf16 a[104:107], v[104:107], v[76:79], a[104:107]
ds_read_b64_tr_b16 v[178:179], v198 offset:9216
v_mfma_f32_16x16x32_bf16 a[108:111], v[108:111], v[76:79], a[108:111]
v_mfma_f32_16x16x32_bf16 a[112:115], v[112:115], v[76:79], a[112:115]
ds_read_b128 v[144:147], v193 offset:8192
v_mfma_f32_16x16x32_bf16 a[116:119], v[116:119], v[76:79], a[116:119]
buffer_load_dwordx4 v23, s[12:15], s25 offen offset:3072 lds
v_mfma_f32_16x16x32_bf16 a[120:123], v[120:123], v[76:79], a[120:123]
ds_read_b64_tr_b16 v[180:181], v199 offset:8192
v_mfma_f32_16x16x32_bf16 a[124:127], v[124:127], v[76:79], a[124:127]
v_mfma_f32_16x16x32_bf16 a[128:131], v[96:99], v[80:83], a[128:131]
ds_read_b64_tr_b16 v[182:183], v199 offset:9216
v_mfma_f32_16x16x32_bf16 a[132:135], v[100:103], v[80:83], a[132:135]
v_mfma_f32_16x16x32_bf16 a[136:139], v[104:107], v[80:83], a[136:139]
ds_read_b128 v[148:151], v193 offset:10240
v_mfma_f32_16x16x32_bf16 a[140:143], v[108:111], v[80:83], a[140:143]
v_mfma_f32_16x16x32_bf16 a[144:147], v[112:115], v[80:83], a[144:147]
ds_read_b64_tr_b16 v[184:185], v200 offset:8192
v_mfma_f32_16x16x32_bf16 a[148:151], v[116:119], v[80:83], a[148:151]
v_mfma_f32_16x16x32_bf16 a[152:155], v[120:123], v[80:83], a[152:155]
ds_read_b64_tr_b16 v[186:187], v200 offset:9216
v_mfma_f32_16x16x32_bf16 a[156:159], v[124:127], v[80:83], a[156:159]
v_mfma_f32_16x16x32_bf16 a[160:163], v[96:99], v[84:87], a[160:163]
s_add_u32 s24, s24, 128
s_add_u32 s25, s25, 128
ds_read_b128 v[152:155], v193 offset:12288
v_mfma_f32_16x16x32_bf16 a[164:167], v[100:103], v[84:87], a[164:167]
.Lpad2:
s_cbranch_execz .Ltch0
v_mfma_f32_16x16x32_bf16 a[168:171], v[104:107], v[84:87], a[168:171]
ds_read_b64_tr_b16 v[188:189], v201 offset:8192
v_mfma_f32_16x16x32_bf16 a[172:175], v[108:111], v[84:87], a[172:175]
v_mfma_f32_16x16x32_bf16 a[176:179], v[112:115], v[84:87], a[176:179]
ds_read_b64_tr_b16 v[190:191], v201 offset:9216
v_mfma_f32_16x16x32_bf16 a[180:183], v[116:119], v[84:87], a[180:183]
v_mfma_f32_16x16x32_bf16 a[184:187], v[120:123], v[84:87], a[184:187]
ds_read_b128 v[156:159], v193 offset:14336
v_mfma_f32_16x16x32_bf16 a[188:191], v[124:127], v[84:87], a[188:191]
v_mfma_f32_16x16x32_bf16 a[192:195], v[96:99], v[88:91], a[192:195]
v_mfma_f32_16x16x32_bf16 a[196:199], v[100:103], v[88:91], a[196:199]
v_mfma_f32_16x16x32_bf16 a[200:203], v[104:107], v[88:91], a[200:203]
v_mfma_f32_16x16x32_bf16 a[204:207], v[108:111], v[88:91], a[204:207]
v_mfma_f32_16x16x32_bf16 a[208:211], v[112:115], v[88:91], a[208:211]
v_mfma_f32_16x16x32_bf16 a[212:215], v[116:119], v[88:91], a[212:215]
v_mfma_f32_16x16x32_bf16 a[216:219], v[120:123], v[88:91], a[216:219]
v_mfma_f32_16x16x32_bf16 a[220:223], v[124:127], v[88:91], a[220:223]
v_mfma_f32_16x16x32_bf16 a[224:227], v[96:99], v[92:95], a[224:227]
v_mfma_f32_16x16x32_bf16 a[228:231], v[100:103], v[92:95], a[228:231]
v_mfma_f32_16x16x32_bf16 a[232:235], v[104:107], v[92:95], a[232:235]
v_mfma_f32_16x16x32_bf16 a[236:239], v[108:111], v[92:95], a[236:239]
v_mfma_f32_16x16x32_bf16 a[240:243], v[112:115], v[92:95], a[240:243]
v_mfma_f32_16x16x32_bf16 a[244:247], v[116:119], v[92:95], a[244:247]
v_mfma_f32_16x16x32_bf16 a[248:251], v[120:123], v[92:95], a[248:251]
v_mfma_f32_16x16x32_bf16 a[252:255], v[124:127], v[92:95], a[252:255]
s_waitcnt vmcnt(16) lgkmcnt(0)
s_barrier
v_mfma_f32_16x16x32_bf16 a[0:3], v[160:163], v[128:131], a[0:3]
s_add_u32 m0, s91, 0
ds_read_b64_tr_b16 v[64:65], v26 offset:0
v_mfma_f32_16x16x32_bf16 a[4:7], v[164:167], v[128:131], a[4:7]
buffer_load_dwordx4 v16, s[16:19], s26 offen offset:0 lds
v_mfma_f32_16x16x32_bf16 a[8:11], v[168:171], v[128:131], a[8:11]
ds_read_b64_tr_b16 v[66:67], v26 offset:1024
v_mfma_f32_16x16x32_bf16 a[12:15], v[172:175], v[128:131], a[12:15]
v_mfma_f32_16x16x32_bf16 a[16:19], v[176:179], v[128:131], a[16:19]
ds_read_b64_tr_b16 v[68:69], v27 offset:0
v_mfma_f32_16x16x32_bf16 a[20:23], v[180:183], v[128:131], a[20:23]
buffer_load_dwordx4 v17, s[16:19], s26 offen offset:1024 lds
v_mfma_f32_16x16x32_bf16 a[24:27], v[184:187], v[128:131], a[24:27]
ds_read_b64_tr_b16 v[70:71], v27 offset:1024
v_mfma_f32_16x16x32_bf16 a[28:31], v[188:191], v[128:131], a[28:31]
v_mfma_f32_16x16x32_bf16 a[32:35], v[160:163], v[132:135], a[32:35]
ds_read_b64_tr_b16 v[72:73], v28 offset:0
v_mfma_f32_16x16x32_bf16 a[36:39], v[164:167], v[132:135], a[36:39]
buffer_load_dwordx4 v18, s[16:19], s26 offen offset:2048 lds
v_mfma_f32_16x16x32_bf16 a[40:43], v[168:171], v[132:135], a[40:43]
ds_read_b64_tr_b16 v[74:75], v28 offset:1024
v_mfma_f32_16x16x32_bf16 a[44:47], v[172:175], v[132:135], a[44:47]
v_mfma_f32_16x16x32_bf16 a[48:51], v[176:179], v[132:135], a[48:51]
ds_read_b64_tr_b16 v[76:77], v29 offset:0
v_mfma_f32_16x16x32_bf16 a[52:55], v[180:183], v[132:135], a[52:55]
buffer_load_dwordx4 v19, s[16:19], s26 offen offset:3072 lds
v_mfma_f32_16x16x32_bf16 a[56:59], v[184:187], v[132:135], a[56:59]
ds_read_b64_tr_b16 v[78:79], v29 offset:1024
v_mfma_f32_16x16x32_bf16 a[60:63], v[188:191], v[132:135], a[60:63]
s_add_u32 s26, s26, 524288
v_mfma_f32_16x16x32_bf16 a[64:67], v[160:163], v[136:139], a[64:67]
s_add_u32 m0, s92, 131072
ds_read_b64_tr_b16 v[80:81], v30 offset:0
v_mfma_f32_16x16x32_bf16 a[68:71], v[164:167], v[136:139], a[68:71]
buffer_load_dwordx4 v20, s[12:15], s24 offen offset:0 lds
v_mfma_f32_16x16x32_bf16 a[72:75], v[168:171], v[136:139], a[72:75]
ds_read_b64_tr_b16 v[82:83], v30 offset:1024
v_mfma_f32_16x16x32_bf16 a[76:79], v[172:175], v[136:139], a[76:79]
v_mfma_f32_16x16x32_bf16 a[80:83], v[176:179], v[136:139], a[80:83]
ds_read_b64_tr_b16 v[84:85], v31 offset:0
v_mfma_f32_16x16x32_bf16 a[84:87], v[180:183], v[136:139], a[84:87]
buffer_load_dwordx4 v21, s[12:15], s24 offen offset:1024 lds
v_mfma_f32_16x16x32_bf16 a[88:91], v[184:187], v[136:139], a[88:91]
ds_read_b64_tr_b16 v[86:87], v31 offset:1024
v_mfma_f32_16x16x32_bf16 a[92:95], v[188:191], v[136:139], a[92:95]
v_mfma_f32_16x16x32_bf16 a[96:99], v[160:163], v[140:143], a[96:99]
ds_read_b64_tr_b16 v[88:89], v32 offset:0
v_mfma_f32_16x16x32_bf16 a[100:103], v[164:167], v[140:143], a[100:103]
buffer_load_dwordx4 v22, s[12:15], s24 offen offset:2048 lds
v_mfma_f32_16x16x32_bf16 a[104:107], v[168:171], v[140:143], a[104:107]
ds_read_b64_tr_b16 v[90:91], v32 offset:1024
v_mfma_f32_16x16x32_bf16 a[108:111], v[172:175], v[140:143], a[108:111]
v_mfma_f32_16x16x32_bf16 a[112:115], v[176:179], v[140:143], a[112:115]
ds_read_b64_tr_b16 v[92:93], v33 offset:0
v_mfma_f32_16x16x32_bf16 a[116:119], v[180:183], v[140:143], a[116:119]
buffer_load_dwordx4 v23, s[12:15], s24 offen offset:3072 lds
v_mfma_f32_16x16x32_bf16 a[120:123], v[184:187], v[140:143], a[120:123]
ds_read_b64_tr_b16 v[94:95], v33 offset:1024
v_mfma_f32_16x16x32_bf16 a[124:127], v[188:191], v[140:143], a[124:127]
v_mfma_f32_16x16x32_bf16 a[128:131], v[160:163], v[144:147], a[128:131]
s_add_u32 m0, s92, 135168
v_mfma_f32_16x16x32_bf16 a[132:135], v[164:167], v[144:147], a[132:135]
buffer_load_dwordx4 v20, s[12:15], s25 offen offset:0 lds
v_mfma_f32_16x16x32_bf16 a[136:139], v[168:171], v[144:147], a[136:139]
ds_read_b128 v[48:51], v24 offset:0
v_mfma_f32_16x16x32_bf16 a[140:143], v[172:175], v[144:147], a[140:143]
v_mfma_f32_16x16x32_bf16 a[144:147], v[176:179], v[144:147], a[144:147]
ds_read_b128 v[52:55], v24 offset:2048
v_mfma_f32_16x16x32_bf16 a[148:151], v[180:183], v[144:147], a[148:151]
buffer_load_dwordx4 v21, s[12:15], s25 offen offset:1024 lds
v_mfma_f32_16x16x32_bf16 a[152:155], v[184:187], v[144:147], a[152:155]
ds_read_b128 v[56:59], v24 offset:4096
v_mfma_f32_16x16x32_bf16 a[156:159], v[188:191], v[144:147], a[156:159]
v_mfma_f32_16x16x32_bf16 a[160:163], v[160:163], v[148:151], a[160:163]
v_mfma_f32_16x16x32_bf16 a[164:167], v[164:167], v[148:151], a[164:167]
buffer_load_dwordx4 v22, s[12:15], s25 offen offset:2048 lds
v_mfma_f32_16x16x32_bf16 a[168:171], v[168:171], v[148:151], a[168:171]
v_mfma_f32_16x16x32_bf16 a[172:175], v[172:175], v[148:151], a[172:175]
v_mfma_f32_16x16x32_bf16 a[176:179], v[176:179], v[148:151], a[176:179]
v_mfma_f32_16x16x32_bf16 a[180:183], v[180:183], v[148:151], a[180:183]
buffer_load_dwordx4 v23, s[12:15], s25 offen offset:3072 lds
v_mfma_f32_16x16x32_bf16 a[184:187], v[184:187], v[148:151], a[184:187]
v_mfma_f32_16x16x32_bf16 a[188:191], v[188:191], v[148:151], a[188:191]
v_mfma_f32_16x16x32_bf16 a[192:195], v[160:163], v[152:155], a[192:195]
v_mfma_f32_16x16x32_bf16 a[196:199], v[164:167], v[152:155], a[196:199]
s_add_u32 s24, s24, 128
s_add_u32 s25, s25, 128
v_mfma_f32_16x16x32_bf16 a[200:203], v[168:171], v[152:155], a[200:203]
v_mfma_f32_16x16x32_bf16 a[204:207], v[172:175], v[152:155], a[204:207]
v_mfma_f32_16x16x32_bf16 a[208:211], v[176:179], v[152:155], a[208:211]
v_mfma_f32_16x16x32_bf16 a[212:215], v[180:183], v[152:155], a[212:215]
v_mfma_f32_16x16x32_bf16 a[216:219], v[184:187], v[152:155], a[216:219]
v_mfma_f32_16x16x32_bf16 a[220:223], v[188:191], v[152:155], a[220:223]
v_mfma_f32_16x16x32_bf16 a[224:227], v[160:163], v[156:159], a[224:227]
v_mfma_f32_16x16x32_bf16 a[228:231], v[164:167], v[156:159], a[228:231]
v_mfma_f32_16x16x32_bf16 a[232:235], v[168:171], v[156:159], a[232:235]
v_mfma_f32_16x16x32_bf16 a[236:239], v[172:175], v[156:159], a[236:239]
v_mfma_f32_16x16x32_bf16 a[240:243], v[176:179], v[156:159], a[240:243]
v_mfma_f32_16x16x32_bf16 a[244:247], v[180:183], v[156:159], a[244:247]
v_mfma_f32_16x16x32_bf16 a[248:251], v[184:187], v[156:159], a[248:251]
v_mfma_f32_16x16x32_bf16 a[252:255], v[188:191], v[156:159], a[252:255]
s_waitcnt lgkmcnt(0)
s_branch .Lp2
.Lp1_v_pad:
s_cbranch_execz .Ltch0
.Lp1_v:
v_mfma_f32_16x16x32_bf16 a[0:3], v[64:67], v[96:99], 0
ds_read_b64_tr_b16 v[160:161], v194 offset:8192
v_mfma_f32_16x16x32_bf16 a[8:11], v[64:67], v[100:103], 0
s_add_u32 s37, s92, 98304
s_mov_b32 s40, 128
s_mov_b32 s41, 524416
v_mfma_f32_16x16x32_bf16 a[16:19], v[64:67], v[104:107], 0
s_mov_b32 m0, s37
ds_read_b64_tr_b16 v[162:163], v194 offset:9216
v_mfma_f32_16x16x32_bf16 a[24:27], v[64:67], v[108:111], 0
buffer_load_dwordx4 v202, s[4:7], s40 offen offset:0 lds
v_mfma_f32_16x16x32_bf16 a[32:35], v[64:67], v[112:115], 0
ds_read_b128 v[128:131], v193 offset:0
v_mfma_f32_16x16x32_bf16 a[40:43], v[64:67], v[116:119], 0
buffer_load_dwordx4 v203, s[4:7], s40 offen offset:1024 lds
v_mfma_f32_16x16x32_bf16 a[48:51], v[64:67], v[120:123], 0
ds_read_b64_tr_b16 v[164:165], v195 offset:8192
v_mfma_f32_16x16x32_bf16 a[56:59], v[64:67], v[124:127], 0
buffer_load_dwordx4 v204, s[4:7], s40 offen offset:2048 lds
v_mfma_f32_16x16x32_bf16 a[4:7], v[68:71], v[96:99], 0
ds_read_b64_tr_b16 v[166:167], v195 offset:9216
v_mfma_f32_16x16x32_bf16 a[12:15], v[68:71], v[100:103], 0
buffer_load_dwordx4 v205, s[4:7], s40 offen offset:3072 lds
v_mfma_f32_16x16x32_bf16 a[20:23], v[68:71], v[104:107], 0
s_add_u32 m0, s37, 4096
ds_read_b128 v[132:135], v193 offset:2048
v_mfma_f32_16x16x32_bf16 a[28:31], v[68:71], v[108:111], 0
buffer_load_dwordx4 v202, s[4:7], s41 offen offset:0 lds
v_mfma_f32_16x16x32_bf16 a[36:39], v[68:71], v[112:115], 0
ds_read_b64_tr_b16 v[168:169], v196 offset:8192
v_mfma_f32_16x16x32_bf16 a[44:47], v[68:71], v[116:119], 0
buffer_load_dwordx4 v203, s[4:7], s41 offen offset:1024 lds
v_mfma_f32_16x16x32_bf16 a[52:55], v[68:71], v[120:123], 0
ds_read_b64_tr_b16 v[170:171], v196 offset:9216
v_mfma_f32_16x16x32_bf16 a[60:63], v[68:71], v[124:127], 0
buffer_load_dwordx4 v204, s[4:7], s41 offen offset:2048 lds
v_mfma_f32_16x16x32_bf16 a[64:67], v[72:75], v[96:99], 0
ds_read_b128 v[136:139], v193 offset:4096
v_mfma_f32_16x16x32_bf16 a[72:75], v[72:75], v[100:103], 0
buffer_load_dwordx4 v205, s[4:7], s41 offen offset:3072 lds
v_mfma_f32_16x16x32_bf16 a[80:83], v[72:75], v[104:107], 0
ds_read_b64_tr_b16 v[172:173], v197 offset:8192
v_mfma_f32_16x16x32_bf16 a[88:91], v[72:75], v[108:111], 0
s_add_u32 s37, s92, 131072
s_mov_b32 s42, 1048576
s_mov_b32 s43, 1179648
v_mfma_f32_16x16x32_bf16 a[96:99], v[72:75], v[112:115], 0
s_mov_b32 m0, s37
ds_read_b64_tr_b16 v[174:175], v197 offset:9216
v_mfma_f32_16x16x32_bf16 a[104:107], v[72:75], v[116:119], 0
buffer_load_dwordx4 v16, s[8:11], s42 offen offset:0 nt lds
v_mfma_f32_16x16x32_bf16 a[112:115], v[72:75], v[120:123], 0
ds_read_b128 v[140:143], v193 offset:6144
v_mfma_f32_16x16x32_bf16 a[120:123], v[72:75], v[124:127], 0
buffer_load_dwordx4 v17, s[8:11], s42 offen offset:1024 nt lds
v_mfma_f32_16x16x32_bf16 a[68:71], v[76:79], v[96:99], 0
ds_read_b64_tr_b16 v[176:177], v198 offset:8192
v_mfma_f32_16x16x32_bf16 a[76:79], v[76:79], v[100:103], 0
buffer_load_dwordx4 v18, s[8:11], s42 offen offset:2048 nt lds
v_mfma_f32_16x16x32_bf16 a[84:87], v[76:79], v[104:107], 0
ds_read_b64_tr_b16 v[178:179], v198 offset:9216
v_mfma_f32_16x16x32_bf16 a[92:95], v[76:79], v[108:111], 0
buffer_load_dwordx4 v19, s[8:11], s42 offen offset:3072 nt lds
v_mfma_f32_16x16x32_bf16 a[100:103], v[76:79], v[112:115], 0
s_add_u32 m0, s37, 4096
ds_read_b128 v[144:147], v193 offset:8192
v_mfma_f32_16x16x32_bf16 a[108:111], v[76:79], v[116:119], 0
buffer_load_dwordx4 v16, s[8:11], s43 offen offset:0 nt lds
v_mfma_f32_16x16x32_bf16 a[116:119], v[76:79], v[120:123], 0
ds_read_b64_tr_b16 v[180:181], v199 offset:8192
v_mfma_f32_16x16x32_bf16 a[124:127], v[76:79], v[124:127], 0
buffer_load_dwordx4 v17, s[8:11], s43 offen offset:1024 nt lds
v_mfma_f32_16x16x32_bf16 a[128:131], v[80:83], v[96:99], 0
ds_read_b64_tr_b16 v[182:183], v199 offset:9216
v_mfma_f32_16x16x32_bf16 a[136:139], v[80:83], v[100:103], 0
buffer_load_dwordx4 v18, s[8:11], s43 offen offset:2048 nt lds
v_mfma_f32_16x16x32_bf16 a[144:147], v[80:83], v[104:107], 0
ds_read_b128 v[148:151], v193 offset:10240
v_mfma_f32_16x16x32_bf16 a[152:155], v[80:83], v[108:111], 0
buffer_load_dwordx4 v19, s[8:11], s43 offen offset:3072 nt lds
v_mfma_f32_16x16x32_bf16 a[160:163], v[80:83], v[112:115], 0
ds_read_b64_tr_b16 v[184:185], v200 offset:8192
v_mfma_f32_16x16x32_bf16 a[168:171], v[80:83], v[116:119], 0
v_mfma_f32_16x16x32_bf16 a[176:179], v[80:83], v[120:123], 0
ds_read_b64_tr_b16 v[186:187], v200 offset:9216
v_mfma_f32_16x16x32_bf16 a[184:187], v[80:83], v[124:127], 0
v_mfma_f32_16x16x32_bf16 a[132:135], v[84:87], v[96:99], 0
ds_read_b128 v[152:155], v193 offset:12288
v_mfma_f32_16x16x32_bf16 a[140:143], v[84:87], v[100:103], 0
v_mfma_f32_16x16x32_bf16 a[148:151], v[84:87], v[104:107], 0
ds_read_b64_tr_b16 v[188:189], v201 offset:8192
v_mfma_f32_16x16x32_bf16 a[156:159], v[84:87], v[108:111], 0
v_mfma_f32_16x16x32_bf16 a[164:167], v[84:87], v[112:115], 0
s_add_u32 s44, s35, 65536
ds_read_b64_tr_b16 v[190:191], v201 offset:9216
v_mfma_f32_16x16x32_bf16 a[172:175], v[84:87], v[116:119], 0
s_cmp_ge_u32 s44, s39
s_cselect_b32 s45, s55, s54
v_mfma_f32_16x16x32_bf16 a[180:183], v[84:87], v[120:123], 0
ds_read_b128 v[156:159], v193 offset:14336
v_mfma_f32_16x16x32_bf16 a[188:191], v[84:87], v[124:127], 0
s_add_u32 s44, s36, 65536
v_mfma_f32_16x16x32_bf16 a[192:195], v[88:91], v[96:99], 0
v_mfma_f32_16x16x32_bf16 a[200:203], v[88:91], v[100:103], 0
s_cmp_ge_u32 s44, s39
s_cselect_b32 s34, s55, s54
v_mfma_f32_16x16x32_bf16 a[208:211], v[88:91], v[104:107], 0
v_add_u32 v194, s45, v194
v_mfma_f32_16x16x32_bf16 a[216:219], v[88:91], v[108:111], 0
v_add_u32 v195, s45, v195
v_mfma_f32_16x16x32_bf16 a[224:227], v[88:91], v[112:115], 0
v_add_u32 v196, s45, v196
v_mfma_f32_16x16x32_bf16 a[232:235], v[88:91], v[116:119], 0
v_add_u32 v197, s45, v197
v_mfma_f32_16x16x32_bf16 a[240:243], v[88:91], v[120:123], 0
v_add_u32 v198, s45, v198
v_mfma_f32_16x16x32_bf16 a[248:251], v[88:91], v[124:127], 0
v_add_u32 v199, s45, v199
v_mfma_f32_16x16x32_bf16 a[196:199], v[92:95], v[96:99], 0
v_add_u32 v200, s45, v200
v_mfma_f32_16x16x32_bf16 a[204:207], v[92:95], v[100:103], 0
v_add_u32 v201, s45, v201
v_mfma_f32_16x16x32_bf16 a[212:215], v[92:95], v[104:107], 0
v_add_u32 v192, s34, v192
v_mfma_f32_16x16x32_bf16 a[220:223], v[92:95], v[108:111], 0
v_add_u32 v193, s34, v193
v_mfma_f32_16x16x32_bf16 a[228:231], v[92:95], v[112:115], 0
v_mfma_f32_16x16x32_bf16 a[236:239], v[92:95], v[116:119], 0
s_mov_b32 s40, 256
v_mfma_f32_16x16x32_bf16 a[244:247], v[92:95], v[120:123], 0
s_mov_b32 s41, 524544
v_mfma_f32_16x16x32_bf16 a[252:255], v[92:95], v[124:127], 0
s_waitcnt vmcnt(8) lgkmcnt(0)
s_barrier
v_mfma_f32_16x16x32_bf16 a[0:3], v[128:131], v[160:163], a[0:3]
s_mov_b32 m0, s35
ds_read_b64_tr_b16 v[96:97], v194 offset:0
v_mfma_f32_16x16x32_bf16 a[8:11], v[128:131], v[164:167], a[8:11]
buffer_load_dwordx4 v202, s[4:7], s40 offen offset:0 lds
v_mfma_f32_16x16x32_bf16 a[16:19], v[128:131], v[168:171], a[16:19]
ds_read_b64_tr_b16 v[98:99], v194 offset:1024
v_mfma_f32_16x16x32_bf16 a[24:27], v[128:131], v[172:175], a[24:27]
v_mfma_f32_16x16x32_bf16 a[32:35], v[128:131], v[176:179], a[32:35]
ds_read_b128 v[64:67], v192 offset:0
v_mfma_f32_16x16x32_bf16 a[40:43], v[128:131], v[180:183], a[40:43]
buffer_load_dwordx4 v203, s[4:7], s40 offen offset:1024 lds
v_mfma_f32_16x16x32_bf16 a[48:51], v[128:131], v[184:187], a[48:51]
ds_read_b64_tr_b16 v[100:101], v195 offset:0
v_mfma_f32_16x16x32_bf16 a[56:59], v[128:131], v[188:191], a[56:59]
v_mfma_f32_16x16x32_bf16 a[4:7], v[132:135], v[160:163], a[4:7]
ds_read_b64_tr_b16 v[102:103], v195 offset:1024
v_mfma_f32_16x16x32_bf16 a[12:15], v[132:135], v[164:167], a[12:15]
buffer_load_dwordx4 v204, s[4:7], s40 offen offset:2048 lds
v_mfma_f32_16x16x32_bf16 a[20:23], v[132:135], v[168:171], a[20:23]
ds_read_b128 v[68:71], v192 offset:2048
v_mfma_f32_16x16x32_bf16 a[28:31], v[132:135], v[172:175], a[28:31]
v_mfma_f32_16x16x32_bf16 a[36:39], v[132:135], v[176:179], a[36:39]
ds_read_b64_tr_b16 v[104:105], v196 offset:0
v_mfma_f32_16x16x32_bf16 a[44:47], v[132:135], v[180:183], a[44:47]
buffer_load_dwordx4 v205, s[4:7], s40 offen offset:3072 lds
v_mfma_f32_16x16x32_bf16 a[52:55], v[132:135], v[184:187], a[52:55]
ds_read_b64_tr_b16 v[106:107], v196 offset:1024
v_mfma_f32_16x16x32_bf16 a[60:63], v[132:135], v[188:191], a[60:63]
v_mfma_f32_16x16x32_bf16 a[64:67], v[136:139], v[160:163], a[64:67]
s_add_u32 m0, s35, 4096
ds_read_b128 v[72:75], v192 offset:4096
v_mfma_f32_16x16x32_bf16 a[72:75], v[136:139], v[164:167], a[72:75]
buffer_load_dwordx4 v202, s[4:7], s41 offen offset:0 lds
v_mfma_f32_16x16x32_bf16 a[80:83], v[136:139], v[168:171], a[80:83]
ds_read_b64_tr_b16 v[108:109], v197 offset:0
v_mfma_f32_16x16x32_bf16 a[88:91], v[136:139], v[172:175], a[88:91]
v_mfma_f32_16x16x32_bf16 a[96:99], v[136:139], v[176:179], a[96:99]
ds_read_b64_tr_b16 v[110:111], v197 offset:1024
v_mfma_f32_16x16x32_bf16 a[104:107], v[136:139], v[180:183], a[104:107]
buffer_load_dwordx4 v203, s[4:7], s41 offen offset:1024 lds
v_mfma_f32_16x16x32_bf16 a[112:115], v[136:139], v[184:187], a[112:115]
ds_read_b128 v[76:79], v192 offset:6144
v_mfma_f32_16x16x32_bf16 a[120:123], v[136:139], v[188:191], a[120:123]
v_mfma_f32_16x16x32_bf16 a[68:71], v[140:143], v[160:163], a[68:71]
ds_read_b64_tr_b16 v[112:113], v198 offset:0
v_mfma_f32_16x16x32_bf16 a[76:79], v[140:143], v[164:167], a[76:79]
buffer_load_dwordx4 v204, s[4:7], s41 offen offset:2048 lds
v_mfma_f32_16x16x32_bf16 a[84:87], v[140:143], v[168:171], a[84:87]
ds_read_b64_tr_b16 v[114:115], v198 offset:1024
v_mfma_f32_16x16x32_bf16 a[92:95], v[140:143], v[172:175], a[92:95]
v_mfma_f32_16x16x32_bf16 a[100:103], v[140:143], v[176:179], a[100:103]
ds_read_b128 v[80:83], v192 offset:8192
v_mfma_f32_16x16x32_bf16 a[108:111], v[140:143], v[180:183], a[108:111]
buffer_load_dwordx4 v205, s[4:7], s41 offen offset:3072 lds
v_mfma_f32_16x16x32_bf16 a[116:119], v[140:143], v[184:187], a[116:119]
ds_read_b64_tr_b16 v[116:117], v199 offset:0
v_mfma_f32_16x16x32_bf16 a[124:127], v[140:143], v[188:191], a[124:127]
v_mfma_f32_16x16x32_bf16 a[128:131], v[144:147], v[160:163], a[128:131]
ds_read_b64_tr_b16 v[118:119], v199 offset:1024
v_mfma_f32_16x16x32_bf16 a[136:139], v[144:147], v[164:167], a[136:139]
buffer_load_dword v35, v206, s[56:59], s48 offen
v_mfma_f32_16x16x32_bf16 a[144:147], v[144:147], v[168:171], a[144:147]
ds_read_b128 v[84:87], v192 offset:10240
v_mfma_f32_16x16x32_bf16 a[152:155], v[144:147], v[172:175], a[152:155]
buffer_load_dword v2, v3, s[60:63], s90 offen sc1
v_mfma_f32_16x16x32_bf16 a[160:163], v[144:147], v[176:179], a[160:163]
s_mov_b32 s42, 1572864
ds_read_b64_tr_b16 v[120:121], v200 offset:0
v_mfma_f32_16x16x32_bf16 a[168:171], v[144:147], v[180:183], a[168:171]
s_mov_b32 s43, 1703936
buffer_load_dword v35, v207, s[12:15], s49 offen
v_mfma_f32_16x16x32_bf16 a[176:179], v[144:147], v[184:187], a[176:179]
ds_read_b64_tr_b16 v[122:123], v200 offset:1024
v_mfma_f32_16x16x32_bf16 a[184:187], v[144:147], v[188:191], a[184:187]
buffer_load_dword v11, v6, s[60:63], 0 offen sc1
v_mfma_f32_16x16x32_bf16 a[132:135], v[148:151], v[160:163], a[132:135]
s_add_u32 s40, s40, 128
ds_read_b128 v[88:91], v192 offset:12288
v_mfma_f32_16x16x32_bf16 a[140:143], v[148:151], v[164:167], a[140:143]
s_add_u32 s41, s41, 128
v_mfma_f32_16x16x32_bf16 a[148:151], v[148:151], v[168:171], a[148:151]
ds_read_b64_tr_b16 v[124:125], v201 offset:0
v_mfma_f32_16x16x32_bf16 a[156:159], v[148:151], v[172:175], a[156:159]
s_add_u32 s48, s48, 0x100000
v_mfma_f32_16x16x32_bf16 a[164:167], v[148:151], v[176:179], a[164:167]
ds_read_b64_tr_b16 v[126:127], v201 offset:1024
v_mfma_f32_16x16x32_bf16 a[172:175], v[148:151], v[180:183], a[172:175]
s_add_u32 s49, s49, 655360
v_mfma_f32_16x16x32_bf16 a[180:183], v[148:151], v[184:187], a[180:183]
ds_read_b128 v[92:95], v192 offset:14336
v_mfma_f32_16x16x32_bf16 a[188:191], v[148:151], v[188:191], a[188:191]
buffer_store_dword v45, v44, s[60:63], 0 offen sc1
v_mfma_f32_16x16x32_bf16 a[192:195], v[152:155], v[160:163], a[192:195]
v_mfma_f32_16x16x32_bf16 a[200:203], v[152:155], v[164:167], a[200:203]
v_mfma_f32_16x16x32_bf16 a[208:211], v[152:155], v[168:171], a[208:211]
v_mfma_f32_16x16x32_bf16 a[216:219], v[152:155], v[172:175], a[216:219]
buffer_store_dword v45, v46, s[60:63], 0 offen sc1
v_mfma_f32_16x16x32_bf16 a[224:227], v[152:155], v[176:179], a[224:227]
v_mfma_f32_16x16x32_bf16 a[232:235], v[152:155], v[180:183], a[232:235]
v_mfma_f32_16x16x32_bf16 a[240:243], v[152:155], v[184:187], a[240:243]
v_mfma_f32_16x16x32_bf16 a[248:251], v[152:155], v[188:191], a[248:251]
v_mfma_f32_16x16x32_bf16 a[196:199], v[156:159], v[160:163], a[196:199]
v_mfma_f32_16x16x32_bf16 a[204:207], v[156:159], v[164:167], a[204:207]
s_mov_b32 s37, s36
v_mfma_f32_16x16x32_bf16 a[212:215], v[156:159], v[168:171], a[212:215]
s_add_u32 s35, s35, s45
v_mfma_f32_16x16x32_bf16 a[220:223], v[156:159], v[172:175], a[220:223]
s_add_u32 s36, s36, s34
v_mfma_f32_16x16x32_bf16 a[228:231], v[156:159], v[176:179], a[228:231]
v_mfma_f32_16x16x32_bf16 a[236:239], v[156:159], v[180:183], a[236:239]
v_mfma_f32_16x16x32_bf16 a[244:247], v[156:159], v[184:187], a[244:247]
v_mfma_f32_16x16x32_bf16 a[252:255], v[156:159], v[188:191], a[252:255]
s_waitcnt lgkmcnt(0)
s_nop 0
s_nop 0
s_nop 0
s_nop 0
s_nop 0
s_nop 0
.Lp1_vloop:
v_mfma_f32_16x16x32_bf16 a[0:3], v[64:67], v[96:99], a[0:3]
s_mov_b32 m0, s37
ds_read_b64_tr_b16 v[160:161], v194 offset:8192
v_mfma_f32_16x16x32_bf16 a[8:11], v[64:67], v[100:103], a[8:11]
buffer_load_dwordx4 v16, s[8:11], s42 offen offset:0 nt lds
v_mfma_f32_16x16x32_bf16 a[16:19], v[64:67], v[104:107], a[16:19]
ds_read_b64_tr_b16 v[162:163], v194 offset:9216
v_mfma_f32_16x16x32_bf16 a[24:27], v[64:67], v[108:111], a[24:27]
v_mfma_f32_16x16x32_bf16 a[32:35], v[64:67], v[112:115], a[32:35]
ds_read_b128 v[128:131], v193 offset:0
v_mfma_f32_16x16x32_bf16 a[40:43], v[64:67], v[116:119], a[40:43]
buffer_load_dwordx4 v17, s[8:11], s42 offen offset:1024 nt lds
v_mfma_f32_16x16x32_bf16 a[48:51], v[64:67], v[120:123], a[48:51]
ds_read_b64_tr_b16 v[164:165], v195 offset:8192
v_mfma_f32_16x16x32_bf16 a[56:59], v[64:67], v[124:127], a[56:59]
v_mfma_f32_16x16x32_bf16 a[4:7], v[68:71], v[96:99], a[4:7]
ds_read_b64_tr_b16 v[166:167], v195 offset:9216
v_mfma_f32_16x16x32_bf16 a[12:15], v[68:71], v[100:103], a[12:15]
buffer_load_dwordx4 v18, s[8:11], s42 offen offset:2048 nt lds
v_mfma_f32_16x16x32_bf16 a[20:23], v[68:71], v[104:107], a[20:23]
ds_read_b128 v[132:135], v193 offset:2048
v_mfma_f32_16x16x32_bf16 a[28:31], v[68:71], v[108:111], a[28:31]
v_mfma_f32_16x16x32_bf16 a[36:39], v[68:71], v[112:115], a[36:39]
ds_read_b64_tr_b16 v[168:169], v196 offset:8192
v_mfma_f32_16x16x32_bf16 a[44:47], v[68:71], v[116:119], a[44:47]
buffer_load_dwordx4 v19, s[8:11], s42 offen offset:3072 nt lds
v_mfma_f32_16x16x32_bf16 a[52:55], v[68:71], v[120:123], a[52:55]
ds_read_b64_tr_b16 v[170:171], v196 offset:9216
v_mfma_f32_16x16x32_bf16 a[60:63], v[68:71], v[124:127], a[60:63]
v_mfma_f32_16x16x32_bf16 a[64:67], v[72:75], v[96:99], a[64:67]
s_add_u32 m0, s37, 4096
ds_read_b128 v[136:139], v193 offset:4096
v_mfma_f32_16x16x32_bf16 a[72:75], v[72:75], v[100:103], a[72:75]
buffer_load_dwordx4 v16, s[8:11], s43 offen offset:0 nt lds
v_mfma_f32_16x16x32_bf16 a[80:83], v[72:75], v[104:107], a[80:83]
ds_read_b64_tr_b16 v[172:173], v197 offset:8192
v_mfma_f32_16x16x32_bf16 a[88:91], v[72:75], v[108:111], a[88:91]
v_mfma_f32_16x16x32_bf16 a[96:99], v[72:75], v[112:115], a[96:99]
ds_read_b64_tr_b16 v[174:175], v197 offset:9216
v_mfma_f32_16x16x32_bf16 a[104:107], v[72:75], v[116:119], a[104:107]
buffer_load_dwordx4 v17, s[8:11], s43 offen offset:1024 nt lds
v_mfma_f32_16x16x32_bf16 a[112:115], v[72:75], v[120:123], a[112:115]
ds_read_b128 v[140:143], v193 offset:6144
v_mfma_f32_16x16x32_bf16 a[120:123], v[72:75], v[124:127], a[120:123]
v_mfma_f32_16x16x32_bf16 a[68:71], v[76:79], v[96:99], a[68:71]
ds_read_b64_tr_b16 v[176:177], v198 offset:8192
v_mfma_f32_16x16x32_bf16 a[76:79], v[76:79], v[100:103], a[76:79]
buffer_load_dwordx4 v18, s[8:11], s43 offen offset:2048 nt lds
v_mfma_f32_16x16x32_bf16 a[84:87], v[76:79], v[104:107], a[84:87]
ds_read_b64_tr_b16 v[178:179], v198 offset:9216
v_mfma_f32_16x16x32_bf16 a[92:95], v[76:79], v[108:111], a[92:95]
v_mfma_f32_16x16x32_bf16 a[100:103], v[76:79], v[112:115], a[100:103]
ds_read_b128 v[144:147], v193 offset:8192
v_mfma_f32_16x16x32_bf16 a[108:111], v[76:79], v[116:119], a[108:111]
buffer_load_dwordx4 v19, s[8:11], s43 offen offset:3072 nt lds
v_mfma_f32_16x16x32_bf16 a[116:119], v[76:79], v[120:123], a[116:119]
ds_read_b64_tr_b16 v[180:181], v199 offset:8192
v_mfma_f32_16x16x32_bf16 a[124:127], v[76:79], v[124:127], a[124:127]
v_mfma_f32_16x16x32_bf16 a[128:131], v[80:83], v[96:99], a[128:131]
ds_read_b64_tr_b16 v[182:183], v199 offset:9216
v_mfma_f32_16x16x32_bf16 a[136:139], v[80:83], v[100:103], a[136:139]
v_mfma_f32_16x16x32_bf16 a[144:147], v[80:83], v[104:107], a[144:147]
ds_read_b128 v[148:151], v193 offset:10240
v_mfma_f32_16x16x32_bf16 a[152:155], v[80:83], v[108:111], a[152:155]
v_mfma_f32_16x16x32_bf16 a[160:163], v[80:83], v[112:115], a[160:163]
ds_read_b64_tr_b16 v[184:185], v200 offset:8192
v_mfma_f32_16x16x32_bf16 a[168:171], v[80:83], v[116:119], a[168:171]
v_mfma_f32_16x16x32_bf16 a[176:179], v[80:83], v[120:123], a[176:179]
ds_read_b64_tr_b16 v[186:187], v200 offset:9216
v_mfma_f32_16x16x32_bf16 a[184:187], v[80:83], v[124:127], a[184:187]
v_mfma_f32_16x16x32_bf16 a[132:135], v[84:87], v[96:99], a[132:135]
s_add_u32 s42, s42, 524288
ds_read_b128 v[152:155], v193 offset:12288
v_mfma_f32_16x16x32_bf16 a[140:143], v[84:87], v[100:103], a[140:143]
s_add_u32 s43, s43, 524288
v_mfma_f32_16x16x32_bf16 a[148:151], v[84:87], v[104:107], a[148:151]
ds_read_b64_tr_b16 v[188:189], v201 offset:8192
v_mfma_f32_16x16x32_bf16 a[156:159], v[84:87], v[108:111], a[156:159]
v_mfma_f32_16x16x32_bf16 a[164:167], v[84:87], v[112:115], a[164:167]
s_add_u32 s44, s35, 65536
ds_read_b64_tr_b16 v[190:191], v201 offset:9216
v_mfma_f32_16x16x32_bf16 a[172:175], v[84:87], v[116:119], a[172:175]
s_cmp_ge_u32 s44, s39
s_cselect_b32 s45, s55, s54
v_mfma_f32_16x16x32_bf16 a[180:183], v[84:87], v[120:123], a[180:183]
ds_read_b128 v[156:159], v193 offset:14336
v_mfma_f32_16x16x32_bf16 a[188:191], v[84:87], v[124:127], a[188:191]
s_add_u32 s44, s36, 65536
v_mfma_f32_16x16x32_bf16 a[192:195], v[88:91], v[96:99], a[192:195]
v_mfma_f32_16x16x32_bf16 a[200:203], v[88:91], v[100:103], a[200:203]
s_cmp_ge_u32 s44, s39
s_cselect_b32 s34, s55, s54
v_mfma_f32_16x16x32_bf16 a[208:211], v[88:91], v[104:107], a[208:211]
v_add_u32 v194, s45, v194
v_mfma_f32_16x16x32_bf16 a[216:219], v[88:91], v[108:111], a[216:219]
v_add_u32 v195, s45, v195
v_mfma_f32_16x16x32_bf16 a[224:227], v[88:91], v[112:115], a[224:227]
v_add_u32 v196, s45, v196
v_mfma_f32_16x16x32_bf16 a[232:235], v[88:91], v[116:119], a[232:235]
v_add_u32 v197, s45, v197
v_mfma_f32_16x16x32_bf16 a[240:243], v[88:91], v[120:123], a[240:243]
v_add_u32 v198, s45, v198
v_mfma_f32_16x16x32_bf16 a[248:251], v[88:91], v[124:127], a[248:251]
v_add_u32 v199, s45, v199
v_mfma_f32_16x16x32_bf16 a[196:199], v[92:95], v[96:99], a[196:199]
v_add_u32 v200, s45, v200
v_mfma_f32_16x16x32_bf16 a[204:207], v[92:95], v[100:103], a[204:207]
v_add_u32 v201, s45, v201
v_mfma_f32_16x16x32_bf16 a[212:215], v[92:95], v[104:107], a[212:215]
v_add_u32 v192, s34, v192
v_mfma_f32_16x16x32_bf16 a[220:223], v[92:95], v[108:111], a[220:223]
v_add_u32 v193, s34, v193
v_mfma_f32_16x16x32_bf16 a[228:231], v[92:95], v[112:115], a[228:231]
v_mfma_f32_16x16x32_bf16 a[236:239], v[92:95], v[116:119], a[236:239]
v_mfma_f32_16x16x32_bf16 a[244:247], v[92:95], v[120:123], a[244:247]
v_mfma_f32_16x16x32_bf16 a[252:255], v[92:95], v[124:127], a[252:255]
s_waitcnt vmcnt(10) lgkmcnt(0)
s_barrier
v_mfma_f32_16x16x32_bf16 a[0:3], v[128:131], v[160:163], a[0:3]
s_mov_b32 m0, s35
ds_read_b64_tr_b16 v[96:97], v194 offset:0
v_mfma_f32_16x16x32_bf16 a[8:11], v[128:131], v[164:167], a[8:11]
buffer_load_dwordx4 v202, s[4:7], s40 offen offset:0 lds
v_mfma_f32_16x16x32_bf16 a[16:19], v[128:131], v[168:171], a[16:19]
ds_read_b64_tr_b16 v[98:99], v194 offset:1024
v_mfma_f32_16x16x32_bf16 a[24:27], v[128:131], v[172:175], a[24:27]
v_mfma_f32_16x16x32_bf16 a[32:35], v[128:131], v[176:179], a[32:35]
ds_read_b128 v[64:67], v192 offset:0
v_mfma_f32_16x16x32_bf16 a[40:43], v[128:131], v[180:183], a[40:43]
buffer_load_dwordx4 v203, s[4:7], s40 offen offset:1024 lds
v_mfma_f32_16x16x32_bf16 a[48:51], v[128:131], v[184:187], a[48:51]
ds_read_b64_tr_b16 v[100:101], v195 offset:0
v_mfma_f32_16x16x32_bf16 a[56:59], v[128:131], v[188:191], a[56:59]
v_mfma_f32_16x16x32_bf16 a[4:7], v[132:135], v[160:163], a[4:7]
ds_read_b64_tr_b16 v[102:103], v195 offset:1024
v_mfma_f32_16x16x32_bf16 a[12:15], v[132:135], v[164:167], a[12:15]
buffer_load_dwordx4 v204, s[4:7], s40 offen offset:2048 lds
v_mfma_f32_16x16x32_bf16 a[20:23], v[132:135], v[168:171], a[20:23]
ds_read_b128 v[68:71], v192 offset:2048
v_mfma_f32_16x16x32_bf16 a[28:31], v[132:135], v[172:175], a[28:31]
v_mfma_f32_16x16x32_bf16 a[36:39], v[132:135], v[176:179], a[36:39]
ds_read_b64_tr_b16 v[104:105], v196 offset:0
v_mfma_f32_16x16x32_bf16 a[44:47], v[132:135], v[180:183], a[44:47]
buffer_load_dwordx4 v205, s[4:7], s40 offen offset:3072 lds
v_mfma_f32_16x16x32_bf16 a[52:55], v[132:135], v[184:187], a[52:55]
ds_read_b64_tr_b16 v[106:107], v196 offset:1024
v_mfma_f32_16x16x32_bf16 a[60:63], v[132:135], v[188:191], a[60:63]
v_mfma_f32_16x16x32_bf16 a[64:67], v[136:139], v[160:163], a[64:67]
s_add_u32 m0, s35, 4096
ds_read_b128 v[72:75], v192 offset:4096
v_mfma_f32_16x16x32_bf16 a[72:75], v[136:139], v[164:167], a[72:75]
buffer_load_dwordx4 v202, s[4:7], s41 offen offset:0 lds
v_mfma_f32_16x16x32_bf16 a[80:83], v[136:139], v[168:171], a[80:83]
ds_read_b64_tr_b16 v[108:109], v197 offset:0
.Lpad3:
s_cbranch_execz .Ltch0
v_mfma_f32_16x16x32_bf16 a[88:91], v[136:139], v[172:175], a[88:91]
v_mfma_f32_16x16x32_bf16 a[96:99], v[136:139], v[176:179], a[96:99]
ds_read_b64_tr_b16 v[110:111], v197 offset:1024
v_mfma_f32_16x16x32_bf16 a[104:107], v[136:139], v[180:183], a[104:107]
buffer_load_dwordx4 v203, s[4:7], s41 offen offset:1024 lds
v_mfma_f32_16x16x32_bf16 a[112:115], v[136:139], v[184:187], a[112:115]
ds_read_b128 v[76:79], v192 offset:6144
v_mfma_f32_16x16x32_bf16 a[120:123], v[136:139], v[188:191], a[120:123]
v_mfma_f32_16x16x32_bf16 a[68:71], v[140:143], v[160:163], a[68:71]
ds_read_b64_tr_b16 v[112:113], v198 offset:0
v_mfma_f32_16x16x32_bf16 a[76:79], v[140:143], v[164:167], a[76:79]
buffer_load_dwordx4 v204, s[4:7], s41 offen offset:2048 lds
v_mfma_f32_16x16x32_bf16 a[84:87], v[140:143], v[168:171], a[84:87]
ds_read_b64_tr_b16 v[114:115], v198 offset:1024
v_mfma_f32_16x16x32_bf16 a[92:95], v[140:143], v[172:175], a[92:95]
v_mfma_f32_16x16x32_bf16 a[100:103], v[140:143], v[176:179], a[100:103]
ds_read_b128 v[80:83], v192 offset:8192
v_mfma_f32_16x16x32_bf16 a[108:111], v[140:143], v[180:183], a[108:111]
buffer_load_dwordx4 v205, s[4:7], s41 offen offset:3072 lds
v_mfma_f32_16x16x32_bf16 a[116:119], v[140:143], v[184:187], a[116:119]
ds_read_b64_tr_b16 v[116:117], v199 offset:0
v_mfma_f32_16x16x32_bf16 a[124:127], v[140:143], v[188:191], a[124:127]
v_mfma_f32_16x16x32_bf16 a[128:131], v[144:147], v[160:163], a[128:131]
ds_read_b64_tr_b16 v[118:119], v199 offset:1024
v_mfma_f32_16x16x32_bf16 a[136:139], v[144:147], v[164:167], a[136:139]
buffer_load_dword v35, v206, s[56:59], s48 offen
v_mfma_f32_16x16x32_bf16 a[144:147], v[144:147], v[168:171], a[144:147]
ds_read_b128 v[84:87], v192 offset:10240
v_mfma_f32_16x16x32_bf16 a[152:155], v[144:147], v[172:175], a[152:155]
v_mfma_f32_16x16x32_bf16 a[160:163], v[144:147], v[176:179], a[160:163]
ds_read_b64_tr_b16 v[120:121], v200 offset:0
v_mfma_f32_16x16x32_bf16 a[168:171], v[144:147], v[180:183], a[168:171]
buffer_load_dword v35, v207, s[12:15], s49 offen
v_mfma_f32_16x16x32_bf16 a[176:179], v[144:147], v[184:187], a[176:179]
ds_read_b64_tr_b16 v[122:123], v200 offset:1024
v_mfma_f32_16x16x32_bf16 a[184:187], v[144:147], v[188:191], a[184:187]
v_mfma_f32_16x16x32_bf16 a[132:135], v[148:151], v[160:163], a[132:135]
s_add_u32 s40, s40, 128
ds_read_b128 v[88:91], v192 offset:12288
v_mfma_f32_16x16x32_bf16 a[140:143], v[148:151], v[164:167], a[140:143]
s_add_u32 s41, s41, 128
v_mfma_f32_16x16x32_bf16 a[148:151], v[148:151], v[168:171], a[148:151]
ds_read_b64_tr_b16 v[124:125], v201 offset:0
v_mfma_f32_16x16x32_bf16 a[156:159], v[148:151], v[172:175], a[156:159]
s_add_u32 s48, s48, 0x100000
v_mfma_f32_16x16x32_bf16 a[164:167], v[148:151], v[176:179], a[164:167]
ds_read_b64_tr_b16 v[126:127], v201 offset:1024
v_mfma_f32_16x16x32_bf16 a[172:175], v[148:151], v[180:183], a[172:175]
s_add_u32 s49, s49, 655360
v_mfma_f32_16x16x32_bf16 a[180:183], v[148:151], v[184:187], a[180:183]
ds_read_b128 v[92:95], v192 offset:14336
v_mfma_f32_16x16x32_bf16 a[188:191], v[148:151], v[188:191], a[188:191]
v_mfma_f32_16x16x32_bf16 a[192:195], v[152:155], v[160:163], a[192:195]
v_mfma_f32_16x16x32_bf16 a[200:203], v[152:155], v[164:167], a[200:203]
v_mfma_f32_16x16x32_bf16 a[208:211], v[152:155], v[168:171], a[208:211]
v_mfma_f32_16x16x32_bf16 a[216:219], v[152:155], v[172:175], a[216:219]
v_mfma_f32_16x16x32_bf16 a[224:227], v[152:155], v[176:179], a[224:227]
v_mfma_f32_16x16x32_bf16 a[232:235], v[152:155], v[180:183], a[232:235]
v_mfma_f32_16x16x32_bf16 a[240:243], v[152:155], v[184:187], a[240:243]
v_mfma_f32_16x16x32_bf16 a[248:251], v[152:155], v[188:191], a[248:251]
v_mfma_f32_16x16x32_bf16 a[196:199], v[156:159], v[160:163], a[196:199]
v_mfma_f32_16x16x32_bf16 a[204:207], v[156:159], v[164:167], a[204:207]
s_mov_b32 s37, s36
v_mfma_f32_16x16x32_bf16 a[212:215], v[156:159], v[168:171], a[212:215]
s_add_u32 s35, s35, s45
v_mfma_f32_16x16x32_bf16 a[220:223], v[156:159], v[172:175], a[220:223]
s_add_u32 s36, s36, s34
v_mfma_f32_16x16x32_bf16 a[228:231], v[156:159], v[176:179], a[228:231]
s_add_u32 s38, s38, 1
v_mfma_f32_16x16x32_bf16 a[236:239], v[156:159], v[180:183], a[236:239]
s_cmp_lt_u32 s38, 6
v_mfma_f32_16x16x32_bf16 a[244:247], v[156:159], v[184:187], a[244:247]
v_mfma_f32_16x16x32_bf16 a[252:255], v[156:159], v[188:191], a[252:255]
s_waitcnt lgkmcnt(0)
s_cbranch_scc1 .Lp1_vloop
v_mfma_f32_16x16x32_bf16 a[0:3], v[64:67], v[96:99], a[0:3]
s_add_u32 m0, s92, 32768
ds_read_b64_tr_b16 v[160:161], v194 offset:8192
v_mfma_f32_16x16x32_bf16 a[8:11], v[64:67], v[100:103], a[8:11]
buffer_load_dwordx4 v20, s[12:15], s24 offen offset:0 lds
v_mfma_f32_16x16x32_bf16 a[16:19], v[64:67], v[104:107], a[16:19]
ds_read_b64_tr_b16 v[162:163], v194 offset:9216
v_mfma_f32_16x16x32_bf16 a[24:27], v[64:67], v[108:111], a[24:27]
v_mfma_f32_16x16x32_bf16 a[32:35], v[64:67], v[112:115], a[32:35]
ds_read_b128 v[128:131], v193 offset:0
v_mfma_f32_16x16x32_bf16 a[40:43], v[64:67], v[116:119], a[40:43]
buffer_load_dwordx4 v21, s[12:15], s24 offen offset:1024 lds
v_mfma_f32_16x16x32_bf16 a[48:51], v[64:67], v[120:123], a[48:51]
ds_read_b64_tr_b16 v[164:165], v195 offset:8192
v_mfma_f32_16x16x32_bf16 a[56:59], v[64:67], v[124:127], a[56:59]
v_mfma_f32_16x16x32_bf16 a[4:7], v[68:71], v[96:99], a[4:7]
ds_read_b64_tr_b16 v[166:167], v195 offset:9216
v_mfma_f32_16x16x32_bf16 a[12:15], v[68:71], v[100:103], a[12:15]
buffer_load_dwordx4 v22, s[12:15], s24 offen offset:2048 lds
v_mfma_f32_16x16x32_bf16 a[20:23], v[68:71], v[104:107], a[20:23]
ds_read_b128 v[132:135], v193 offset:2048
v_mfma_f32_16x16x32_bf16 a[28:31], v[68:71], v[108:111], a[28:31]
v_mfma_f32_16x16x32_bf16 a[36:39], v[68:71], v[112:115], a[36:39]
ds_read_b64_tr_b16 v[168:169], v196 offset:8192
v_mfma_f32_16x16x32_bf16 a[44:47], v[68:71], v[116:119], a[44:47]
buffer_load_dwordx4 v23, s[12:15], s24 offen offset:3072 lds
v_mfma_f32_16x16x32_bf16 a[52:55], v[68:71], v[120:123], a[52:55]
ds_read_b64_tr_b16 v[170:171], v196 offset:9216
v_mfma_f32_16x16x32_bf16 a[60:63], v[68:71], v[124:127], a[60:63]
v_mfma_f32_16x16x32_bf16 a[64:67], v[72:75], v[96:99], a[64:67]
s_add_u32 m0, s92, 36864
ds_read_b128 v[136:139], v193 offset:4096
v_mfma_f32_16x16x32_bf16 a[72:75], v[72:75], v[100:103], a[72:75]
buffer_load_dwordx4 v20, s[12:15], s25 offen offset:0 lds
v_mfma_f32_16x16x32_bf16 a[80:83], v[72:75], v[104:107], a[80:83]
ds_read_b64_tr_b16 v[172:173], v197 offset:8192
v_mfma_f32_16x16x32_bf16 a[88:91], v[72:75], v[108:111], a[88:91]
v_mfma_f32_16x16x32_bf16 a[96:99], v[72:75], v[112:115], a[96:99]
ds_read_b64_tr_b16 v[174:175], v197 offset:9216
v_mfma_f32_16x16x32_bf16 a[104:107], v[72:75], v[116:119], a[104:107]
buffer_load_dwordx4 v21, s[12:15], s25 offen offset:1024 lds
v_mfma_f32_16x16x32_bf16 a[112:115], v[72:75], v[120:123], a[112:115]
ds_read_b128 v[140:143], v193 offset:6144
v_mfma_f32_16x16x32_bf16 a[120:123], v[72:75], v[124:127], a[120:123]
v_mfma_f32_16x16x32_bf16 a[68:71], v[76:79], v[96:99], a[68:71]
ds_read_b64_tr_b16 v[176:177], v198 offset:8192
v_mfma_f32_16x16x32_bf16 a[76:79], v[76:79], v[100:103], a[76:79]
buffer_load_dwordx4 v22, s[12:15], s25 offen offset:2048 lds
v_mfma_f32_16x16x32_bf16 a[84:87], v[76:79], v[104:107], a[84:87]
ds_read_b64_tr_b16 v[178:179], v198 offset:9216
v_mfma_f32_16x16x32_bf16 a[92:95], v[76:79], v[108:111], a[92:95]
v_mfma_f32_16x16x32_bf16 a[100:103], v[76:79], v[112:115], a[100:103]
ds_read_b128 v[144:147], v193 offset:8192
v_mfma_f32_16x16x32_bf16 a[108:111], v[76:79], v[116:119], a[108:111]
buffer_load_dwordx4 v23, s[12:15], s25 offen offset:3072 lds
v_mfma_f32_16x16x32_bf16 a[116:119], v[76:79], v[120:123], a[116:119]
ds_read_b64_tr_b16 v[180:181], v199 offset:8192
v_mfma_f32_16x16x32_bf16 a[124:127], v[76:79], v[124:127], a[124:127]
v_mfma_f32_16x16x32_bf16 a[128:131], v[80:83], v[96:99], a[128:131]
ds_read_b64_tr_b16 v[182:183], v199 offset:9216
v_mfma_f32_16x16x32_bf16 a[136:139], v[80:83], v[100:103], a[136:139]
v_mfma_f32_16x16x32_bf16 a[144:147], v[80:83], v[104:107], a[144:147]
ds_read_b128 v[148:151], v193 offset:10240
v_mfma_f32_16x16x32_bf16 a[152:155], v[80:83], v[108:111], a[152:155]
v_mfma_f32_16x16x32_bf16 a[160:163], v[80:83], v[112:115], a[160:163]
ds_read_b64_tr_b16 v[184:185], v200 offset:8192
v_mfma_f32_16x16x32_bf16 a[168:171], v[80:83], v[116:119], a[168:171]
v_mfma_f32_16x16x32_bf16 a[176:179], v[80:83], v[120:123], a[176:179]
ds_read_b64_tr_b16 v[186:187], v200 offset:9216
v_mfma_f32_16x16x32_bf16 a[184:187], v[80:83], v[124:127], a[184:187]
v_mfma_f32_16x16x32_bf16 a[132:135], v[84:87], v[96:99], a[132:135]
s_add_u32 s24, s24, 128
s_add_u32 s25, s25, 128
ds_read_b128 v[152:155], v193 offset:12288
v_mfma_f32_16x16x32_bf16 a[140:143], v[84:87], v[100:103], a[140:143]
v_mfma_f32_16x16x32_bf16 a[148:151], v[84:87], v[104:107], a[148:151]
ds_read_b64_tr_b16 v[188:189], v201 offset:8192
v_mfma_f32_16x16x32_bf16 a[156:159], v[84:87], v[108:111], a[156:159]
v_mfma_f32_16x16x32_bf16 a[164:167], v[84:87], v[112:115], a[164:167]
s_add_u32 s44, s35, 65536
ds_read_b64_tr_b16 v[190:191], v201 offset:9216
v_mfma_f32_16x16x32_bf16 a[172:175], v[84:87], v[116:119], a[172:175]
s_cmp_ge_u32 s44, s39
s_cselect_b32 s45, s55, s54
v_mfma_f32_16x16x32_bf16 a[180:183], v[84:87], v[120:123], a[180:183]
ds_read_b128 v[156:159], v193 offset:14336
v_mfma_f32_16x16x32_bf16 a[188:191], v[84:87], v[124:127], a[188:191]
s_add_u32 s44, s36, 65536
v_mfma_f32_16x16x32_bf16 a[192:195], v[88:91], v[96:99], a[192:195]
v_mfma_f32_16x16x32_bf16 a[200:203], v[88:91], v[100:103], a[200:203]
s_cmp_ge_u32 s44, s39
s_cselect_b32 s34, s55, s54
v_mfma_f32_16x16x32_bf16 a[208:211], v[88:91], v[104:107], a[208:211]
v_add_u32 v194, s45, v194
v_mfma_f32_16x16x32_bf16 a[216:219], v[88:91], v[108:111], a[216:219]
v_add_u32 v195, s45, v195
v_mfma_f32_16x16x32_bf16 a[224:227], v[88:91], v[112:115], a[224:227]
v_add_u32 v196, s45, v196
v_mfma_f32_16x16x32_bf16 a[232:235], v[88:91], v[116:119], a[232:235]
v_add_u32 v197, s45, v197
v_mfma_f32_16x16x32_bf16 a[240:243], v[88:91], v[120:123], a[240:243]
v_add_u32 v198, s45, v198
v_mfma_f32_16x16x32_bf16 a[248:251], v[88:91], v[124:127], a[248:251]
v_add_u32 v199, s45, v199
v_mfma_f32_16x16x32_bf16 a[196:199], v[92:95], v[96:99], a[196:199]
v_add_u32 v200, s45, v200
v_mfma_f32_16x16x32_bf16 a[204:207], v[92:95], v[100:103], a[204:207]
v_add_u32 v201, s45, v201
v_mfma_f32_16x16x32_bf16 a[212:215], v[92:95], v[104:107], a[212:215]
v_add_u32 v192, s34, v192
v_mfma_f32_16x16x32_bf16 a[220:223], v[92:95], v[108:111], a[220:223]
v_add_u32 v193, s34, v193
v_mfma_f32_16x16x32_bf16 a[228:231], v[92:95], v[112:115], a[228:231]
v_mfma_f32_16x16x32_bf16 a[236:239], v[92:95], v[116:119], a[236:239]
v_mfma_f32_16x16x32_bf16 a[244:247], v[92:95], v[120:123], a[244:247]
v_mfma_f32_16x16x32_bf16 a[252:255], v[92:95], v[124:127], a[252:255]
s_waitcnt vmcnt(10) lgkmcnt(0)
s_barrier
v_mfma_f32_16x16x32_bf16 a[0:3], v[128:131], v[160:163], a[0:3]
s_add_u32 m0, s91, 65536
ds_read_b64_tr_b16 v[96:97], v194 offset:0
v_mfma_f32_16x16x32_bf16 a[8:11], v[128:131], v[164:167], a[8:11]
buffer_load_dwordx4 v16, s[16:19], s26 offen offset:0 lds
v_mfma_f32_16x16x32_bf16 a[16:19], v[128:131], v[168:171], a[16:19]
ds_read_b64_tr_b16 v[98:99], v194 offset:1024
v_mfma_f32_16x16x32_bf16 a[24:27], v[128:131], v[172:175], a[24:27]
v_mfma_f32_16x16x32_bf16 a[32:35], v[128:131], v[176:179], a[32:35]
ds_read_b128 v[64:67], v192 offset:0
v_mfma_f32_16x16x32_bf16 a[40:43], v[128:131], v[180:183], a[40:43]
buffer_load_dwordx4 v17, s[16:19], s26 offen offset:1024 lds
v_mfma_f32_16x16x32_bf16 a[48:51], v[128:131], v[184:187], a[48:51]
ds_read_b64_tr_b16 v[100:101], v195 offset:0
v_mfma_f32_16x16x32_bf16 a[56:59], v[128:131], v[188:191], a[56:59]
v_mfma_f32_16x16x32_bf16 a[4:7], v[132:135], v[160:163], a[4:7]
ds_read_b64_tr_b16 v[102:103], v195 offset:1024
v_mfma_f32_16x16x32_bf16 a[12:15], v[132:135], v[164:167], a[12:15]
buffer_load_dwordx4 v18, s[16:19], s26 offen offset:2048 lds
v_mfma_f32_16x16x32_bf16 a[20:23], v[132:135], v[168:171], a[20:23]
ds_read_b128 v[68:71], v192 offset:2048
v_mfma_f32_16x16x32_bf16 a[28:31], v[132:135], v[172:175], a[28:31]
v_mfma_f32_16x16x32_bf16 a[36:39], v[132:135], v[176:179], a[36:39]
ds_read_b64_tr_b16 v[104:105], v196 offset:0
v_mfma_f32_16x16x32_bf16 a[44:47], v[132:135], v[180:183], a[44:47]
buffer_load_dwordx4 v19, s[16:19], s26 offen offset:3072 lds
v_mfma_f32_16x16x32_bf16 a[52:55], v[132:135], v[184:187], a[52:55]
ds_read_b64_tr_b16 v[106:107], v196 offset:1024
v_mfma_f32_16x16x32_bf16 a[60:63], v[132:135], v[188:191], a[60:63]
s_add_u32 s26, s26, 524288
v_mfma_f32_16x16x32_bf16 a[64:67], v[136:139], v[160:163], a[64:67]
s_add_u32 m0, s91, 81920
ds_read_b128 v[72:75], v192 offset:4096
v_mfma_f32_16x16x32_bf16 a[72:75], v[136:139], v[164:167], a[72:75]
buffer_load_dwordx4 v16, s[16:19], s26 offen offset:0 lds
v_mfma_f32_16x16x32_bf16 a[80:83], v[136:139], v[168:171], a[80:83]
ds_read_b64_tr_b16 v[108:109], v197 offset:0
v_mfma_f32_16x16x32_bf16 a[88:91], v[136:139], v[172:175], a[88:91]
v_mfma_f32_16x16x32_bf16 a[96:99], v[136:139], v[176:179], a[96:99]
ds_read_b64_tr_b16 v[110:111], v197 offset:1024
v_mfma_f32_16x16x32_bf16 a[104:107], v[136:139], v[180:183], a[104:107]
buffer_load_dwordx4 v17, s[16:19], s26 offen offset:1024 lds
v_mfma_f32_16x16x32_bf16 a[112:115], v[136:139], v[184:187], a[112:115]
ds_read_b128 v[76:79], v192 offset:6144
v_mfma_f32_16x16x32_bf16 a[120:123], v[136:139], v[188:191], a[120:123]
v_mfma_f32_16x16x32_bf16 a[68:71], v[140:143], v[160:163], a[68:71]
ds_read_b64_tr_b16 v[112:113], v198 offset:0
v_mfma_f32_16x16x32_bf16 a[76:79], v[140:143], v[164:167], a[76:79]
buffer_load_dwordx4 v18, s[16:19], s26 offen offset:2048 lds
v_mfma_f32_16x16x32_bf16 a[84:87], v[140:143], v[168:171], a[84:87]
ds_read_b64_tr_b16 v[114:115], v198 offset:1024
v_mfma_f32_16x16x32_bf16 a[92:95], v[140:143], v[172:175], a[92:95]
v_mfma_f32_16x16x32_bf16 a[100:103], v[140:143], v[176:179], a[100:103]
ds_read_b128 v[80:83], v192 offset:8192
v_mfma_f32_16x16x32_bf16 a[108:111], v[140:143], v[180:183], a[108:111]
buffer_load_dwordx4 v19, s[16:19], s26 offen offset:3072 lds
v_mfma_f32_16x16x32_bf16 a[116:119], v[140:143], v[184:187], a[116:119]
ds_read_b64_tr_b16 v[116:117], v199 offset:0
v_mfma_f32_16x16x32_bf16 a[124:127], v[140:143], v[188:191], a[124:127]
s_add_u32 s26, s26, 524288
v_mfma_f32_16x16x32_bf16 a[128:131], v[144:147], v[160:163], a[128:131]
ds_read_b64_tr_b16 v[118:119], v199 offset:1024
v_mfma_f32_16x16x32_bf16 a[136:139], v[144:147], v[164:167], a[136:139]
buffer_load_dword v35, v206, s[56:59], s48 offen
v_mfma_f32_16x16x32_bf16 a[144:147], v[144:147], v[168:171], a[144:147]
ds_read_b128 v[84:87], v192 offset:10240
v_mfma_f32_16x16x32_bf16 a[152:155], v[144:147], v[172:175], a[152:155]
v_mfma_f32_16x16x32_bf16 a[160:163], v[144:147], v[176:179], a[160:163]
ds_read_b64_tr_b16 v[120:121], v200 offset:0
v_mfma_f32_16x16x32_bf16 a[168:171], v[144:147], v[180:183], a[168:171]
buffer_load_dword v35, v207, s[12:15], s49 offen
v_add_u32 v11, 8, v11
v_and_b32 v11, -8, v11
v_mfma_f32_16x16x32_bf16 a[176:179], v[144:147], v[184:187], a[176:179]
ds_read_b64_tr_b16 v[122:123], v200 offset:1024
v_mfma_f32_16x16x32_bf16 a[184:187], v[144:147], v[188:191], a[184:187]
v_mfma_f32_16x16x32_bf16 a[132:135], v[148:151], v[160:163], a[132:135]
ds_read_b128 v[88:91], v192 offset:12288
v_mfma_f32_16x16x32_bf16 a[140:143], v[148:151], v[164:167], a[140:143]
s_nop 0
v_or_b32 v0, 0, v11
v_mfma_f32_16x16x32_bf16 a[148:151], v[148:151], v[168:171], a[148:151]
ds_read_b64_tr_b16 v[124:125], v201 offset:0
v_mfma_f32_16x16x32_bf16 a[156:159], v[148:151], v[172:175], a[156:159]
s_add_u32 s48, s48, 0x100000
v_mfma_f32_16x16x32_bf16 a[164:167], v[148:151], v[176:179], a[164:167]
ds_read_b64_tr_b16 v[126:127], v201 offset:1024
v_mfma_f32_16x16x32_bf16 a[172:175], v[148:151], v[180:183], a[172:175]
s_add_u32 s49, s49, 655360
v_mfma_f32_16x16x32_bf16 a[180:183], v[148:151], v[184:187], a[180:183]
ds_read_b128 v[92:95], v192 offset:14336
v_mfma_f32_16x16x32_bf16 a[188:191], v[148:151], v[188:191], a[188:191]
buffer_store_dword v0, v6, s[60:63], 0 offen sc1
v_mfma_f32_16x16x32_bf16 a[192:195], v[152:155], v[160:163], a[192:195]
v_mfma_f32_16x16x32_bf16 a[200:203], v[152:155], v[164:167], a[200:203]
v_mfma_f32_16x16x32_bf16 a[208:211], v[152:155], v[168:171], a[208:211]
v_mfma_f32_16x16x32_bf16 a[216:219], v[152:155], v[172:175], a[216:219]
buffer_load_dword v35, v4, s[96:99], 0 offen
v_mfma_f32_16x16x32_bf16 a[224:227], v[152:155], v[176:179], a[224:227]
v_mfma_f32_16x16x32_bf16 a[232:235], v[152:155], v[180:183], a[232:235]
v_mfma_f32_16x16x32_bf16 a[240:243], v[152:155], v[184:187], a[240:243]
v_mfma_f32_16x16x32_bf16 a[248:251], v[152:155], v[188:191], a[248:251]
v_mfma_f32_16x16x32_bf16 a[196:199], v[156:159], v[160:163], a[196:199]
v_mfma_f32_16x16x32_bf16 a[204:207], v[156:159], v[164:167], a[204:207]
s_mov_b32 s37, s36
v_mfma_f32_16x16x32_bf16 a[212:215], v[156:159], v[168:171], a[212:215]
s_add_u32 s35, s35, s45
v_mfma_f32_16x16x32_bf16 a[220:223], v[156:159], v[172:175], a[220:223]
s_add_u32 s36, s36, s34
v_mfma_f32_16x16x32_bf16 a[228:231], v[156:159], v[176:179], a[228:231]
v_mfma_f32_16x16x32_bf16 a[236:239], v[156:159], v[180:183], a[236:239]
v_mfma_f32_16x16x32_bf16 a[244:247], v[156:159], v[184:187], a[244:247]
v_mfma_f32_16x16x32_bf16 a[252:255], v[156:159], v[188:191], a[252:255]
s_waitcnt lgkmcnt(0)
v_mfma_f32_16x16x32_bf16 a[0:3], v[64:67], v[96:99], a[0:3]
s_add_u32 m0, s92, 98304
ds_read_b64_tr_b16 v[160:161], v194 offset:8192
v_mfma_f32_16x16x32_bf16 a[8:11], v[64:67], v[100:103], a[8:11]
buffer_load_dwordx4 v20, s[12:15], s24 offen offset:0 lds
v_mfma_f32_16x16x32_bf16 a[16:19], v[64:67], v[104:107], a[16:19]
ds_read_b64_tr_b16 v[162:163], v194 offset:9216
v_mfma_f32_16x16x32_bf16 a[24:27], v[64:67], v[108:111], a[24:27]
v_mfma_f32_16x16x32_bf16 a[32:35], v[64:67], v[112:115], a[32:35]
ds_read_b128 v[128:131], v193 offset:0
v_mfma_f32_16x16x32_bf16 a[40:43], v[64:67], v[116:119], a[40:43]
buffer_load_dwordx4 v21, s[12:15], s24 offen offset:1024 lds
v_mfma_f32_16x16x32_bf16 a[48:51], v[64:67], v[120:123], a[48:51]
ds_read_b64_tr_b16 v[164:165], v195 offset:8192
v_mfma_f32_16x16x32_bf16 a[56:59], v[64:67], v[124:127], a[56:59]
v_mfma_f32_16x16x32_bf16 a[4:7], v[68:71], v[96:99], a[4:7]
ds_read_b64_tr_b16 v[166:167], v195 offset:9216
v_mfma_f32_16x16x32_bf16 a[12:15], v[68:71], v[100:103], a[12:15]
buffer_load_dwordx4 v22, s[12:15], s24 offen offset:2048 lds
v_mfma_f32_16x16x32_bf16 a[20:23], v[68:71], v[104:107], a[20:23]
ds_read_b128 v[132:135], v193 offset:2048
v_mfma_f32_16x16x32_bf16 a[28:31], v[68:71], v[108:111], a[28:31]
v_mfma_f32_16x16x32_bf16 a[36:39], v[68:71], v[112:115], a[36:39]
ds_read_b64_tr_b16 v[168:169], v196 offset:8192
v_mfma_f32_16x16x32_bf16 a[44:47], v[68:71], v[116:119], a[44:47]
buffer_load_dwordx4 v23, s[12:15], s24 offen offset:3072 lds
v_mfma_f32_16x16x32_bf16 a[52:55], v[68:71], v[120:123], a[52:55]
ds_read_b64_tr_b16 v[170:171], v196 offset:9216
v_mfma_f32_16x16x32_bf16 a[60:63], v[68:71], v[124:127], a[60:63]
v_mfma_f32_16x16x32_bf16 a[64:67], v[72:75], v[96:99], a[64:67]
s_add_u32 m0, s92, 102400
ds_read_b128 v[136:139], v193 offset:4096
v_mfma_f32_16x16x32_bf16 a[72:75], v[72:75], v[100:103], a[72:75]
buffer_load_dwordx4 v20, s[12:15], s25 offen offset:0 lds
v_mfma_f32_16x16x32_bf16 a[80:83], v[72:75], v[104:107], a[80:83]
ds_read_b64_tr_b16 v[172:173], v197 offset:8192
v_mfma_f32_16x16x32_bf16 a[88:91], v[72:75], v[108:111], a[88:91]
v_mfma_f32_16x16x32_bf16 a[96:99], v[72:75], v[112:115], a[96:99]
ds_read_b64_tr_b16 v[174:175], v197 offset:9216
v_mfma_f32_16x16x32_bf16 a[104:107], v[72:75], v[116:119], a[104:107]
buffer_load_dwordx4 v21, s[12:15], s25 offen offset:1024 lds
v_mfma_f32_16x16x32_bf16 a[112:115], v[72:75], v[120:123], a[112:115]
ds_read_b128 v[140:143], v193 offset:6144
v_mfma_f32_16x16x32_bf16 a[120:123], v[72:75], v[124:127], a[120:123]
v_mfma_f32_16x16x32_bf16 a[68:71], v[76:79], v[96:99], a[68:71]
ds_read_b64_tr_b16 v[176:177], v198 offset:8192
v_mfma_f32_16x16x32_bf16 a[76:79], v[76:79], v[100:103], a[76:79]
buffer_load_dwordx4 v22, s[12:15], s25 offen offset:2048 lds
v_mfma_f32_16x16x32_bf16 a[84:87], v[76:79], v[104:107], a[84:87]
ds_read_b64_tr_b16 v[178:179], v198 offset:9216
v_mfma_f32_16x16x32_bf16 a[92:95], v[76:79], v[108:111], a[92:95]
v_mfma_f32_16x16x32_bf16 a[100:103], v[76:79], v[112:115], a[100:103]
ds_read_b128 v[144:147], v193 offset:8192
v_mfma_f32_16x16x32_bf16 a[108:111], v[76:79], v[116:119], a[108:111]
buffer_load_dwordx4 v23, s[12:15], s25 offen offset:3072 lds
v_mfma_f32_16x16x32_bf16 a[116:119], v[76:79], v[120:123], a[116:119]
ds_read_b64_tr_b16 v[180:181], v199 offset:8192
v_mfma_f32_16x16x32_bf16 a[124:127], v[76:79], v[124:127], a[124:127]
v_mfma_f32_16x16x32_bf16 a[128:131], v[80:83], v[96:99], a[128:131]
ds_read_b64_tr_b16 v[182:183], v199 offset:9216
v_mfma_f32_16x16x32_bf16 a[136:139], v[80:83], v[100:103], a[136:139]
v_mfma_f32_16x16x32_bf16 a[144:147], v[80:83], v[104:107], a[144:147]
ds_read_b128 v[148:151], v193 offset:10240
v_mfma_f32_16x16x32_bf16 a[152:155], v[80:83], v[108:111], a[152:155]
v_mfma_f32_16x16x32_bf16 a[160:163], v[80:83], v[112:115], a[160:163]
ds_read_b64_tr_b16 v[184:185], v200 offset:8192
v_mfma_f32_16x16x32_bf16 a[168:171], v[80:83], v[116:119], a[168:171]
v_mfma_f32_16x16x32_bf16 a[176:179], v[80:83], v[120:123], a[176:179]
ds_read_b64_tr_b16 v[186:187], v200 offset:9216
v_mfma_f32_16x16x32_bf16 a[184:187], v[80:83], v[124:127], a[184:187]
v_mfma_f32_16x16x32_bf16 a[132:135], v[84:87], v[96:99], a[132:135]
s_add_u32 s24, s24, 128
s_add_u32 s25, s25, 128
ds_read_b128 v[152:155], v193 offset:12288
v_mfma_f32_16x16x32_bf16 a[140:143], v[84:87], v[100:103], a[140:143]
v_mfma_f32_16x16x32_bf16 a[148:151], v[84:87], v[104:107], a[148:151]
ds_read_b64_tr_b16 v[188:189], v201 offset:8192
v_mfma_f32_16x16x32_bf16 a[156:159], v[84:87], v[108:111], a[156:159]
v_mfma_f32_16x16x32_bf16 a[164:167], v[84:87], v[112:115], a[164:167]
ds_read_b64_tr_b16 v[190:191], v201 offset:9216
v_mfma_f32_16x16x32_bf16 a[172:175], v[84:87], v[116:119], a[172:175]
v_mfma_f32_16x16x32_bf16 a[180:183], v[84:87], v[120:123], a[180:183]
ds_read_b128 v[156:159], v193 offset:14336
v_mfma_f32_16x16x32_bf16 a[188:191], v[84:87], v[124:127], a[188:191]
v_mfma_f32_16x16x32_bf16 a[192:195], v[88:91], v[96:99], a[192:195]
v_mfma_f32_16x16x32_bf16 a[200:203], v[88:91], v[100:103], a[200:203]
v_mfma_f32_16x16x32_bf16 a[208:211], v[88:91], v[104:107], a[208:211]
v_mfma_f32_16x16x32_bf16 a[216:219], v[88:91], v[108:111], a[216:219]
v_mfma_f32_16x16x32_bf16 a[224:227], v[88:91], v[112:115], a[224:227]
v_mfma_f32_16x16x32_bf16 a[232:235], v[88:91], v[116:119], a[232:235]
v_mfma_f32_16x16x32_bf16 a[240:243], v[88:91], v[120:123], a[240:243]
v_mfma_f32_16x16x32_bf16 a[248:251], v[88:91], v[124:127], a[248:251]
v_mfma_f32_16x16x32_bf16 a[196:199], v[92:95], v[96:99], a[196:199]
v_mfma_f32_16x16x32_bf16 a[204:207], v[92:95], v[100:103], a[204:207]
v_mfma_f32_16x16x32_bf16 a[212:215], v[92:95], v[104:107], a[212:215]
v_mfma_f32_16x16x32_bf16 a[220:223], v[92:95], v[108:111], a[220:223]
v_mfma_f32_16x16x32_bf16 a[228:231], v[92:95], v[112:115], a[228:231]
v_mfma_f32_16x16x32_bf16 a[236:239], v[92:95], v[116:119], a[236:239]
v_mfma_f32_16x16x32_bf16 a[244:247], v[92:95], v[120:123], a[244:247]
v_mfma_f32_16x16x32_bf16 a[252:255], v[92:95], v[124:127], a[252:255]
s_waitcnt vmcnt(16) lgkmcnt(0)
s_barrier
v_mfma_f32_16x16x32_bf16 a[0:3], v[128:131], v[160:163], a[0:3]
s_add_u32 m0, s91, 0
ds_read_b64_tr_b16 v[64:65], v26 offset:0
v_mfma_f32_16x16x32_bf16 a[8:11], v[128:131], v[164:167], a[8:11]
buffer_load_dwordx4 v16, s[16:19], s26 offen offset:0 lds
v_mfma_f32_16x16x32_bf16 a[16:19], v[128:131], v[168:171], a[16:19]
ds_read_b64_tr_b16 v[66:67], v26 offset:1024
v_mfma_f32_16x16x32_bf16 a[24:27], v[128:131], v[172:175], a[24:27]
v_mfma_f32_16x16x32_bf16 a[32:35], v[128:131], v[176:179], a[32:35]
ds_read_b64_tr_b16 v[68:69], v27 offset:0
v_mfma_f32_16x16x32_bf16 a[40:43], v[128:131], v[180:183], a[40:43]
buffer_load_dwordx4 v17, s[16:19], s26 offen offset:1024 lds
v_mfma_f32_16x16x32_bf16 a[48:51], v[128:131], v[184:187], a[48:51]
ds_read_b64_tr_b16 v[70:71], v27 offset:1024
v_mfma_f32_16x16x32_bf16 a[56:59], v[128:131], v[188:191], a[56:59]
v_mfma_f32_16x16x32_bf16 a[4:7], v[132:135], v[160:163], a[4:7]
ds_read_b64_tr_b16 v[72:73], v28 offset:0
v_mfma_f32_16x16x32_bf16 a[12:15], v[132:135], v[164:167], a[12:15]
buffer_load_dwordx4 v18, s[16:19], s26 offen offset:2048 lds
v_mfma_f32_16x16x32_bf16 a[20:23], v[132:135], v[168:171], a[20:23]
ds_read_b64_tr_b16 v[74:75], v28 offset:1024
v_mfma_f32_16x16x32_bf16 a[28:31], v[132:135], v[172:175], a[28:31]
v_mfma_f32_16x16x32_bf16 a[36:39], v[132:135], v[176:179], a[36:39]
ds_read_b64_tr_b16 v[76:77], v29 offset:0
v_mfma_f32_16x16x32_bf16 a[44:47], v[132:135], v[180:183], a[44:47]
buffer_load_dwordx4 v19, s[16:19], s26 offen offset:3072 lds
v_mfma_f32_16x16x32_bf16 a[52:55], v[132:135], v[184:187], a[52:55]
ds_read_b64_tr_b16 v[78:79], v29 offset:1024
v_mfma_f32_16x16x32_bf16 a[60:63], v[132:135], v[188:191], a[60:63]
s_add_u32 s26, s26, 524288
v_mfma_f32_16x16x32_bf16 a[64:67], v[136:139], v[160:163], a[64:67]
s_add_u32 m0, s92, 131072
ds_read_b64_tr_b16 v[80:81], v30 offset:0
v_mfma_f32_16x16x32_bf16 a[72:75], v[136:139], v[164:167], a[72:75]
buffer_load_dwordx4 v20, s[12:15], s24 offen offset:0 lds
v_mfma_f32_16x16x32_bf16 a[80:83], v[136:139], v[168:171], a[80:83]
ds_read_b64_tr_b16 v[82:83], v30 offset:1024
v_mfma_f32_16x16x32_bf16 a[88:91], v[136:139], v[172:175], a[88:91]
v_mfma_f32_16x16x32_bf16 a[96:99], v[136:139], v[176:179], a[96:99]
ds_read_b64_tr_b16 v[84:85], v31 offset:0
v_mfma_f32_16x16x32_bf16 a[104:107], v[136:139], v[180:183], a[104:107]
buffer_load_dwordx4 v21, s[12:15], s24 offen offset:1024 lds
v_mfma_f32_16x16x32_bf16 a[112:115], v[136:139], v[184:187], a[112:115]
ds_read_b64_tr_b16 v[86:87], v31 offset:1024
v_mfma_f32_16x16x32_bf16 a[120:123], v[136:139], v[188:191], a[120:123]
v_mfma_f32_16x16x32_bf16 a[68:71], v[140:143], v[160:163], a[68:71]
ds_read_b64_tr_b16 v[88:89], v32 offset:0
v_mfma_f32_16x16x32_bf16 a[76:79], v[140:143], v[164:167], a[76:79]
buffer_load_dwordx4 v22, s[12:15], s24 offen offset:2048 lds
v_mfma_f32_16x16x32_bf16 a[84:87], v[140:143], v[168:171], a[84:87]
ds_read_b64_tr_b16 v[90:91], v32 offset:1024
v_mfma_f32_16x16x32_bf16 a[92:95], v[140:143], v[172:175], a[92:95]
v_mfma_f32_16x16x32_bf16 a[100:103], v[140:143], v[176:179], a[100:103]
ds_read_b64_tr_b16 v[92:93], v33 offset:0
v_mfma_f32_16x16x32_bf16 a[108:111], v[140:143], v[180:183], a[108:111]
buffer_load_dwordx4 v23, s[12:15], s24 offen offset:3072 lds
v_mfma_f32_16x16x32_bf16 a[116:119], v[140:143], v[184:187], a[116:119]
ds_read_b64_tr_b16 v[94:95], v33 offset:1024
v_mfma_f32_16x16x32_bf16 a[124:127], v[140:143], v[188:191], a[124:127]
v_mfma_f32_16x16x32_bf16 a[128:131], v[144:147], v[160:163], a[128:131]
s_add_u32 m0, s92, 135168
v_mfma_f32_16x16x32_bf16 a[136:139], v[144:147], v[164:167], a[136:139]
buffer_load_dwordx4 v20, s[12:15], s25 offen offset:0 lds
v_mfma_f32_16x16x32_bf16 a[144:147], v[144:147], v[168:171], a[144:147]
ds_read_b128 v[48:51], v24 offset:0
v_mfma_f32_16x16x32_bf16 a[152:155], v[144:147], v[172:175], a[152:155]
v_mfma_f32_16x16x32_bf16 a[160:163], v[144:147], v[176:179], a[160:163]
ds_read_b128 v[52:55], v24 offset:2048
v_mfma_f32_16x16x32_bf16 a[168:171], v[144:147], v[180:183], a[168:171]
buffer_load_dwordx4 v21, s[12:15], s25 offen offset:1024 lds
v_mfma_f32_16x16x32_bf16 a[176:179], v[144:147], v[184:187], a[176:179]
ds_read_b128 v[56:59], v24 offset:4096
v_mfma_f32_16x16x32_bf16 a[184:187], v[144:147], v[188:191], a[184:187]
v_mfma_f32_16x16x32_bf16 a[132:135], v[148:151], v[160:163], a[132:135]
v_mfma_f32_16x16x32_bf16 a[140:143], v[148:151], v[164:167], a[140:143]
buffer_load_dwordx4 v22, s[12:15], s25 offen offset:2048 lds
v_mfma_f32_16x16x32_bf16 a[148:151], v[148:151], v[168:171], a[148:151]
v_mfma_f32_16x16x32_bf16 a[156:159], v[148:151], v[172:175], a[156:159]
v_mfma_f32_16x16x32_bf16 a[164:167], v[148:151], v[176:179], a[164:167]
v_mfma_f32_16x16x32_bf16 a[172:175], v[148:151], v[180:183], a[172:175]
buffer_load_dwordx4 v23, s[12:15], s25 offen offset:3072 lds
v_mfma_f32_16x16x32_bf16 a[180:183], v[148:151], v[184:187], a[180:183]
v_mfma_f32_16x16x32_bf16 a[188:191], v[148:151], v[188:191], a[188:191]
v_mfma_f32_16x16x32_bf16 a[192:195], v[152:155], v[160:163], a[192:195]
v_mfma_f32_16x16x32_bf16 a[200:203], v[152:155], v[164:167], a[200:203]
s_add_u32 s24, s24, 128
s_add_u32 s25, s25, 128
v_mfma_f32_16x16x32_bf16 a[208:211], v[152:155], v[168:171], a[208:211]
v_mfma_f32_16x16x32_bf16 a[216:219], v[152:155], v[172:175], a[216:219]
v_mfma_f32_16x16x32_bf16 a[224:227], v[152:155], v[176:179], a[224:227]
v_mfma_f32_16x16x32_bf16 a[232:235], v[152:155], v[180:183], a[232:235]
v_mfma_f32_16x16x32_bf16 a[240:243], v[152:155], v[184:187], a[240:243]
v_mfma_f32_16x16x32_bf16 a[248:251], v[152:155], v[188:191], a[248:251]
v_mfma_f32_16x16x32_bf16 a[196:199], v[156:159], v[160:163], a[196:199]
v_mfma_f32_16x16x32_bf16 a[204:207], v[156:159], v[164:167], a[204:207]
v_mfma_f32_16x16x32_bf16 a[212:215], v[156:159], v[168:171], a[212:215]
v_mfma_f32_16x16x32_bf16 a[220:223], v[156:159], v[172:175], a[220:223]
v_mfma_f32_16x16x32_bf16 a[228:231], v[156:159], v[176:179], a[228:231]
v_mfma_f32_16x16x32_bf16 a[236:239], v[156:159], v[180:183], a[236:239]
v_mfma_f32_16x16x32_bf16 a[244:247], v[156:159], v[184:187], a[244:247]
v_mfma_f32_16x16x32_bf16 a[252:255], v[156:159], v[188:191], a[252:255]
s_waitcnt lgkmcnt(0)
.Lp2_pad:
s_cbranch_execz .Ltch1
.Lp2:
v_mfma_f32_16x16x32_bf16 v[128:131], v[64:67], v[48:51], 0
ds_read_b128 v[60:63], v24 offset:6144
v_mfma_f32_16x16x32_bf16 v[132:135], v[68:71], v[48:51], 0
v_add_u32 v24, 65536, v24
v_accvgpr_read_b32 v36, a0
v_mfma_f32_16x16x32_bf16 v[136:139], v[72:75], v[48:51], 0
ds_read_b64_tr_b16 v[96:97], v26 offset:8192
ds_read_b64_tr_b16 v[98:99], v26 offset:9216
v_mfma_f32_16x16x32_bf16 v[140:143], v[76:79], v[48:51], 0
v_accvgpr_read_b32 v37, a1
v_mul_f32 v36, s47, v36
v_mfma_f32_16x16x32_bf16 v[144:147], v[80:83], v[48:51], 0
ds_read_b64_tr_b16 v[100:101], v27 offset:8192
ds_read_b64_tr_b16 v[102:103], v27 offset:9216
v_mfma_f32_16x16x32_bf16 v[148:151], v[84:87], v[48:51], 0
v_mul_f32 v37, s47, v37
v_cvt_pk_bf16_f32 v40, v36, v37
v_mfma_f32_16x16x32_bf16 v[152:155], v[88:91], v[48:51], 0
ds_read_b64_tr_b16 v[104:105], v28 offset:8192
ds_read_b64_tr_b16 v[106:107], v28 offset:9216
.Lpad4:
s_cbranch_execz .Ltch1
v_mfma_f32_16x16x32_bf16 v[156:159], v[92:95], v[48:51], 0
v_accvgpr_read_b32 v38, a2
v_accvgpr_read_b32 v39, a3
v_mfma_f32_16x16x32_bf16 v[160:163], v[64:67], v[52:55], 0
ds_read_b128 v[48:51], v25 offset:0
v_mul_f32 v38, s47, v38
v_mfma_f32_16x16x32_bf16 v[164:167], v[68:71], v[52:55], 0
v_mul_f32 v39, s47, v39
v_cvt_pk_bf16_f32 v41, v38, v39
v_mfma_f32_16x16x32_bf16 v[168:171], v[72:75], v[52:55], 0
s_add_u32 m0, s91, 16384
ds_read_b64_tr_b16 v[108:109], v29 offset:8192
ds_read_b64_tr_b16 v[110:111], v29 offset:9216
v_mfma_f32_16x16x32_bf16 v[172:175], v[76:79], v[52:55], 0
buffer_load_dwordx4 v16, s[16:19], s26 offen offset:0 lds
v_accvgpr_read_b32 v36, a4
v_mfma_f32_16x16x32_bf16 v[176:179], v[80:83], v[52:55], 0
ds_read_b64_tr_b16 v[112:113], v30 offset:8192
ds_read_b64_tr_b16 v[114:115], v30 offset:9216
v_mfma_f32_16x16x32_bf16 v[180:183], v[84:87], v[52:55], 0
v_accvgpr_read_b32 v37, a5
v_mul_f32 v36, s47, v36
v_mfma_f32_16x16x32_bf16 v[184:187], v[88:91], v[52:55], 0
ds_read_b64_tr_b16 v[116:117], v31 offset:8192
ds_read_b64_tr_b16 v[118:119], v31 offset:9216
v_mfma_f32_16x16x32_bf16 v[188:191], v[92:95], v[52:55], 0
buffer_load_dwordx4 v17, s[16:19], s26 offen offset:1024 lds
v_mul_f32 v37, s47, v37
v_mfma_f32_16x16x32_bf16 v[192:195], v[64:67], v[56:59], 0
ds_read_b128 v[52:55], v25 offset:2048
v_cvt_pk_bf16_f32 v42, v36, v37
v_mfma_f32_16x16x32_bf16 v[196:199], v[68:71], v[56:59], 0
v_accvgpr_read_b32 v38, a6
v_accvgpr_read_b32 v39, a7
v_mfma_f32_16x16x32_bf16 v[200:203], v[72:75], v[56:59], 0
ds_read_b64_tr_b16 v[120:121], v32 offset:8192
ds_read_b64_tr_b16 v[122:123], v32 offset:9216
v_mfma_f32_16x16x32_bf16 v[204:207], v[76:79], v[56:59], 0
buffer_load_dwordx4 v18, s[16:19], s26 offen offset:2048 lds
v_mul_f32 v38, s47, v38
v_mfma_f32_16x16x32_bf16 v[208:211], v[80:83], v[56:59], 0
ds_read_b64_tr_b16 v[124:125], v33 offset:8192
ds_read_b64_tr_b16 v[126:127], v33 offset:9216
v_mfma_f32_16x16x32_bf16 v[212:215], v[84:87], v[56:59], 0
v_mul_f32 v39, s47, v39
v_cvt_pk_bf16_f32 v43, v38, v39
v_mfma_f32_16x16x32_bf16 v[216:219], v[88:91], v[56:59], 0
s_waitcnt lgkmcnt(15)
s_xor_b32 s48, s95, 0
v_accvgpr_read_b32 v36, a128
v_mfma_f32_16x16x32_bf16 v[220:223], v[92:95], v[56:59], 0
buffer_store_dwordx4 v[40:43], v7, s[20:23], s48 offen sc1
v_accvgpr_read_b32 v37, a129
v_mfma_f32_16x16x32_bf16 v[224:227], v[64:67], v[60:63], 0
ds_read_b128 v[56:59], v25 offset:4096
v_mul_f32 v36, s47, v36
v_mfma_f32_16x16x32_bf16 v[228:231], v[68:71], v[60:63], 0
buffer_load_dwordx4 v19, s[16:19], s26 offen offset:3072 lds
v_mul_f32 v37, s47, v37
v_mfma_f32_16x16x32_bf16 v[232:235], v[72:75], v[60:63], 0
v_cvt_pk_bf16_f32 v44, v36, v37
v_accvgpr_read_b32 v38, a130
v_mfma_f32_16x16x32_bf16 v[236:239], v[76:79], v[60:63], 0
s_add_u32 s26, s26, 524288
v_accvgpr_read_b32 v39, a131
v_mul_f32 v38, s47, v38
v_mfma_f32_16x16x32_bf16 v[240:243], v[80:83], v[60:63], 0
v_mul_f32 v39, s47, v39
v_cvt_pk_bf16_f32 v45, v38, v39
v_mfma_f32_16x16x32_bf16 v[244:247], v[84:87], v[60:63], 0
v_accvgpr_read_b32 v36, a132
v_accvgpr_read_b32 v37, a133
v_mfma_f32_16x16x32_bf16 v[248:251], v[88:91], v[60:63], 0
v_mul_f32 v36, s47, v36
v_mul_f32 v37, s47, v37
v_mfma_f32_16x16x32_bf16 v[252:255], v[92:95], v[60:63], 0
s_waitcnt vmcnt(17) lgkmcnt(0)
s_barrier
v_cvt_pk_bf16_f32 v46, v36, v37
v_mfma_f32_16x16x32_bf16 v[128:131], v[96:99], v[48:51], v[128:131]
ds_read_b128 v[60:63], v25 offset:6144
v_accvgpr_read_b32 v38, a134
v_mfma_f32_16x16x32_bf16 v[132:135], v[100:103], v[48:51], v[132:135]
v_add_u32 v25, 65536, v25
v_accvgpr_read_b32 v39, a135
v_mfma_f32_16x16x32_bf16 v[136:139], v[104:107], v[48:51], v[136:139]
s_add_u32 m0, s92, 32768
ds_read_b64_tr_b16 v[64:65], v26 offset:16384
ds_read_b64_tr_b16 v[66:67], v26 offset:17408
v_mfma_f32_16x16x32_bf16 v[140:143], v[108:111], v[48:51], v[140:143]
buffer_load_dwordx4 v20, s[12:15], s24 offen offset:0 lds
v_mul_f32 v38, s47, v38
v_mfma_f32_16x16x32_bf16 v[144:147], v[112:115], v[48:51], v[144:147]
ds_read_b64_tr_b16 v[68:69], v27 offset:16384
ds_read_b64_tr_b16 v[70:71], v27 offset:17408
v_mfma_f32_16x16x32_bf16 v[148:151], v[116:119], v[48:51], v[148:151]
v_mul_f32 v39, s47, v39
v_cvt_pk_bf16_f32 v47, v38, v39
v_mfma_f32_16x16x32_bf16 v[152:155], v[120:123], v[48:51], v[152:155]
ds_read_b64_tr_b16 v[72:73], v28 offset:16384
ds_read_b64_tr_b16 v[74:75], v28 offset:17408
v_mfma_f32_16x16x32_bf16 v[156:159], v[124:127], v[48:51], v[156:159]
buffer_load_dwordx4 v21, s[12:15], s24 offen offset:1024 lds
v_accvgpr_write_b32 a128, v44
v_mfma_f32_16x16x32_bf16 v[160:163], v[96:99], v[52:55], v[160:163]
ds_read_b128 v[48:51], v24 offset:0
v_accvgpr_write_b32 a129, v45
v_mfma_f32_16x16x32_bf16 v[164:167], v[100:103], v[52:55], v[164:167]
v_accvgpr_write_b32 a130, v46
v_accvgpr_write_b32 a131, v47
v_mfma_f32_16x16x32_bf16 v[168:171], v[104:107], v[52:55], v[168:171]
ds_read_b64_tr_b16 v[76:77], v29 offset:16384
ds_read_b64_tr_b16 v[78:79], v29 offset:17408
v_mfma_f32_16x16x32_bf16 v[172:175], v[108:111], v[52:55], v[172:175]
buffer_load_dwordx4 v22, s[12:15], s24 offen offset:2048 lds
v_accvgpr_read_b32 v36, a136
v_mfma_f32_16x16x32_bf16 v[176:179], v[112:115], v[52:55], v[176:179]
ds_read_b64_tr_b16 v[80:81], v30 offset:16384
ds_read_b64_tr_b16 v[82:83], v30 offset:17408
v_mfma_f32_16x16x32_bf16 v[180:183], v[116:119], v[52:55], v[180:183]
v_accvgpr_read_b32 v37, a137
v_mul_f32 v36, s47, v36
v_mfma_f32_16x16x32_bf16 v[184:187], v[120:123], v[52:55], v[184:187]
ds_read_b64_tr_b16 v[84:85], v31 offset:16384
ds_read_b64_tr_b16 v[86:87], v31 offset:17408
v_mfma_f32_16x16x32_bf16 v[188:191], v[124:127], v[52:55], v[188:191]
buffer_load_dwordx4 v23, s[12:15], s24 offen offset:3072 lds
v_mul_f32 v37, s47, v37
v_mfma_f32_16x16x32_bf16 v[192:195], v[96:99], v[56:59], v[192:195]
ds_read_b128 v[52:55], v24 offset:2048
v_cvt_pk_bf16_f32 v40, v36, v37
v_mfma_f32_16x16x32_bf16 v[196:199], v[100:103], v[56:59], v[196:199]
v_accvgpr_read_b32 v38, a138
v_accvgpr_read_b32 v39, a139
v_mfma_f32_16x16x32_bf16 v[200:203], v[104:107], v[56:59], v[200:203]
s_add_u32 m0, s92, 36864
ds_read_b64_tr_b16 v[88:89], v32 offset:16384
ds_read_b64_tr_b16 v[90:91], v32 offset:17408
v_mfma_f32_16x16x32_bf16 v[204:207], v[108:111], v[56:59], v[204:207]
buffer_load_dwordx4 v20, s[12:15], s25 offen offset:0 lds
v_mul_f32 v38, s47, v38
v_mfma_f32_16x16x32_bf16 v[208:211], v[112:115], v[56:59], v[208:211]
ds_read_b64_tr_b16 v[92:93], v33 offset:16384
ds_read_b64_tr_b16 v[94:95], v33 offset:17408
v_mfma_f32_16x16x32_bf16 v[212:215], v[116:119], v[56:59], v[212:215]
v_mul_f32 v39, s47, v39
v_cvt_pk_bf16_f32 v41, v38, v39
v_mfma_f32_16x16x32_bf16 v[216:219], v[120:123], v[56:59], v[216:219]
s_waitcnt lgkmcnt(15)
v_accvgpr_read_b32 v36, a140
v_accvgpr_read_b32 v37, a141
v_mfma_f32_16x16x32_bf16 v[220:223], v[124:127], v[56:59], v[220:223]
v_mul_f32 v36, s47, v36
v_mul_f32 v37, s47, v37
v_mfma_f32_16x16x32_bf16 v[224:227], v[96:99], v[60:63], v[224:227]
ds_read_b128 v[56:59], v24 offset:4096
v_cvt_pk_bf16_f32 v42, v36, v37
v_mfma_f32_16x16x32_bf16 v[228:231], v[100:103], v[60:63], v[228:231]
buffer_load_dwordx4 v21, s[12:15], s25 offen offset:1024 lds
v_accvgpr_read_b32 v38, a142
v_mfma_f32_16x16x32_bf16 v[232:235], v[104:107], v[60:63], v[232:235]
v_accvgpr_read_b32 v39, a143
v_mul_f32 v38, s47, v38
v_mfma_f32_16x16x32_bf16 v[236:239], v[108:111], v[60:63], v[236:239]
v_mul_f32 v39, s47, v39
v_cvt_pk_bf16_f32 v43, v38, v39
v_mfma_f32_16x16x32_bf16 v[240:243], v[112:115], v[60:63], v[240:243]
v_accvgpr_write_b32 a132, v40
v_accvgpr_write_b32 a133, v41
v_mfma_f32_16x16x32_bf16 v[244:247], v[116:119], v[60:63], v[244:247]
v_accvgpr_write_b32 a134, v42
v_accvgpr_write_b32 a135, v43
v_mfma_f32_16x16x32_bf16 v[248:251], v[120:123], v[60:63], v[248:251]
v_accvgpr_read_b32 v36, a8
v_accvgpr_read_b32 v37, a9
v_mfma_f32_16x16x32_bf16 v[252:255], v[124:127], v[60:63], v[252:255]
s_waitcnt lgkmcnt(0)
v_mul_f32 v36, s47, v36
v_mul_f32 v37, s47, v37
v_mfma_f32_16x16x32_bf16 v[128:131], v[64:67], v[48:51], v[128:131]
ds_read_b128 v[60:63], v24 offset:6144
v_cvt_pk_bf16_f32 v44, v36, v37
v_mfma_f32_16x16x32_bf16 v[132:135], v[68:71], v[48:51], v[132:135]
v_add_u32 v24, 32768, v24
v_accvgpr_read_b32 v38, a10
v_mfma_f32_16x16x32_bf16 v[136:139], v[72:75], v[48:51], v[136:139]
ds_read_b64_tr_b16 v[96:97], v26 offset:24576
ds_read_b64_tr_b16 v[98:99], v26 offset:25600
v_mfma_f32_16x16x32_bf16 v[140:143], v[76:79], v[48:51], v[140:143]
buffer_load_dwordx4 v22, s[12:15], s25 offen offset:2048 lds
v_accvgpr_read_b32 v39, a11
v_mfma_f32_16x16x32_bf16 v[144:147], v[80:83], v[48:51], v[144:147]
ds_read_b64_tr_b16 v[100:101], v27 offset:24576
ds_read_b64_tr_b16 v[102:103], v27 offset:25600
v_mfma_f32_16x16x32_bf16 v[148:151], v[84:87], v[48:51], v[148:151]
v_mul_f32 v38, s47, v38
v_mul_f32 v39, s47, v39
v_mfma_f32_16x16x32_bf16 v[152:155], v[88:91], v[48:51], v[152:155]
ds_read_b64_tr_b16 v[104:105], v28 offset:24576
ds_read_b64_tr_b16 v[106:107], v28 offset:25600
v_mfma_f32_16x16x32_bf16 v[156:159], v[92:95], v[48:51], v[156:159]
buffer_load_dwordx4 v23, s[12:15], s25 offen offset:3072 lds
v_cvt_pk_bf16_f32 v45, v38, v39
v_mfma_f32_16x16x32_bf16 v[160:163], v[64:67], v[52:55], v[160:163]
ds_read_b128 v[48:51], v25 offset:0
v_accvgpr_read_b32 v36, a12
v_mfma_f32_16x16x32_bf16 v[164:167], v[68:71], v[52:55], v[164:167]
s_add_u32 s24, s24, 128
s_add_u32 s25, s25, 128
v_accvgpr_read_b32 v37, a13
v_mfma_f32_16x16x32_bf16 v[168:171], v[72:75], v[52:55], v[168:171]
s_add_u32 m0, s91, 65536
ds_read_b64_tr_b16 v[108:109], v29 offset:24576
ds_read_b64_tr_b16 v[110:111], v29 offset:25600
v_mfma_f32_16x16x32_bf16 v[172:175], v[76:79], v[52:55], v[172:175]
buffer_load_dwordx4 v16, s[16:19], s26 offen offset:0 lds
v_mul_f32 v36, s47, v36
v_mfma_f32_16x16x32_bf16 v[176:179], v[80:83], v[52:55], v[176:179]
ds_read_b64_tr_b16 v[112:113], v30 offset:24576
ds_read_b64_tr_b16 v[114:115], v30 offset:25600
v_mfma_f32_16x16x32_bf16 v[180:183], v[84:87], v[52:55], v[180:183]
v_mul_f32 v37, s47, v37
v_cvt_pk_bf16_f32 v46, v36, v37
v_mfma_f32_16x16x32_bf16 v[184:187], v[88:91], v[52:55], v[184:187]
ds_read_b64_tr_b16 v[116:117], v31 offset:24576
ds_read_b64_tr_b16 v[118:119], v31 offset:25600
v_mfma_f32_16x16x32_bf16 v[188:191], v[92:95], v[52:55], v[188:191]
buffer_load_dwordx4 v17, s[16:19], s26 offen offset:1024 lds
v_accvgpr_read_b32 v38, a14
v_mfma_f32_16x16x32_bf16 v[192:195], v[64:67], v[56:59], v[192:195]
ds_read_b128 v[52:55], v25 offset:2048
v_accvgpr_read_b32 v39, a15
v_mfma_f32_16x16x32_bf16 v[196:199], v[68:71], v[56:59], v[196:199]
v_mul_f32 v38, s47, v38
v_mul_f32 v39, s47, v39
v_mfma_f32_16x16x32_bf16 v[200:203], v[72:75], v[56:59], v[200:203]
ds_read_b64_tr_b16 v[120:121], v32 offset:24576
ds_read_b64_tr_b16 v[122:123], v32 offset:25600
v_mfma_f32_16x16x32_bf16 v[204:207], v[76:79], v[56:59], v[204:207]
buffer_load_dwordx4 v18, s[16:19], s26 offen offset:2048 lds
v_cvt_pk_bf16_f32 v47, v38, v39
v_mfma_f32_16x16x32_bf16 v[208:211], v[80:83], v[56:59], v[208:211]
ds_read_b64_tr_b16 v[124:125], v33 offset:24576
ds_read_b64_tr_b16 v[126:127], v33 offset:25600
v_mfma_f32_16x16x32_bf16 v[212:215], v[84:87], v[56:59], v[212:215]
s_xor_b32 s48, s95, 1024
buffer_store_dwordx4 v[44:47], v7, s[20:23], s48 offen sc1
v_mfma_f32_16x16x32_bf16 v[216:219], v[88:91], v[56:59], v[216:219]
s_waitcnt lgkmcnt(15)
v_accvgpr_read_b32 v36, a144
v_accvgpr_read_b32 v37, a145
v_mfma_f32_16x16x32_bf16 v[220:223], v[92:95], v[56:59], v[220:223]
v_xor_b32 v26, 0x10000, v26
v_mul_f32 v36, s47, v36
v_mfma_f32_16x16x32_bf16 v[224:227], v[64:67], v[60:63], v[224:227]
ds_read_b128 v[56:59], v25 offset:4096
v_xor_b32 v27, 0x10000, v27
v_mfma_f32_16x16x32_bf16 v[228:231], v[68:71], v[60:63], v[228:231]
buffer_load_dwordx4 v19, s[16:19], s26 offen offset:3072 lds
v_xor_b32 v28, 0x10000, v28
v_mfma_f32_16x16x32_bf16 v[232:235], v[72:75], v[60:63], v[232:235]
v_xor_b32 v29, 0x10000, v29
v_mul_f32 v37, s47, v37
v_mfma_f32_16x16x32_bf16 v[236:239], v[76:79], v[60:63], v[236:239]
s_add_u32 s26, s26, 524288
v_xor_b32 v30, 0x10000, v30
v_cvt_pk_bf16_f32 v40, v36, v37
v_mfma_f32_16x16x32_bf16 v[240:243], v[80:83], v[60:63], v[240:243]
v_xor_b32 v31, 0x10000, v31
v_accvgpr_read_b32 v38, a146
v_mfma_f32_16x16x32_bf16 v[244:247], v[84:87], v[60:63], v[244:247]
v_xor_b32 v32, 0x10000, v32
v_accvgpr_read_b32 v39, a147
v_mfma_f32_16x16x32_bf16 v[248:251], v[88:91], v[60:63], v[248:251]
v_xor_b32 v33, 0x10000, v33
v_mul_f32 v38, s47, v38
v_mfma_f32_16x16x32_bf16 v[252:255], v[92:95], v[60:63], v[252:255]
s_waitcnt vmcnt(18) lgkmcnt(0)
s_barrier
v_mul_f32 v39, s47, v39
v_mfma_f32_16x16x32_bf16 v[128:131], v[96:99], v[48:51], v[128:131]
ds_read_b128 v[60:63], v25 offset:6144
v_cvt_pk_bf16_f32 v41, v38, v39
v_mfma_f32_16x16x32_bf16 v[132:135], v[100:103], v[48:51], v[132:135]
v_add_u32 v25, 32768, v25
v_accvgpr_read_b32 v36, a148
v_mfma_f32_16x16x32_bf16 v[136:139], v[104:107], v[48:51], v[136:139]
s_add_u32 m0, s92, 98304
ds_read_b64_tr_b16 v[64:65], v26 offset:0
ds_read_b64_tr_b16 v[66:67], v26 offset:1024
v_mfma_f32_16x16x32_bf16 v[140:143], v[108:111], v[48:51], v[140:143]
buffer_load_dwordx4 v20, s[12:15], s24 offen offset:0 lds
v_accvgpr_read_b32 v37, a149
v_mfma_f32_16x16x32_bf16 v[144:147], v[112:115], v[48:51], v[144:147]
ds_read_b64_tr_b16 v[68:69], v27 offset:0
ds_read_b64_tr_b16 v[70:71], v27 offset:1024
v_mfma_f32_16x16x32_bf16 v[148:151], v[116:119], v[48:51], v[148:151]
v_mul_f32 v36, s47, v36
v_mul_f32 v37, s47, v37
v_mfma_f32_16x16x32_bf16 v[152:155], v[120:123], v[48:51], v[152:155]
ds_read_b64_tr_b16 v[72:73], v28 offset:0
ds_read_b64_tr_b16 v[74:75], v28 offset:1024
v_mfma_f32_16x16x32_bf16 v[156:159], v[124:127], v[48:51], v[156:159]
buffer_load_dwordx4 v21, s[12:15], s24 offen offset:1024 lds
v_cvt_pk_bf16_f32 v42, v36, v37
v_mfma_f32_16x16x32_bf16 v[160:163], v[96:99], v[52:55], v[160:163]
ds_read_b128 v[48:51], v24 offset:0
v_accvgpr_read_b32 v38, a150
v_mfma_f32_16x16x32_bf16 v[164:167], v[100:103], v[52:55], v[164:167]
v_accvgpr_read_b32 v39, a151
v_mul_f32 v38, s47, v38
v_mfma_f32_16x16x32_bf16 v[168:171], v[104:107], v[52:55], v[168:171]
ds_read_b64_tr_b16 v[76:77], v29 offset:0
ds_read_b64_tr_b16 v[78:79], v29 offset:1024
v_mfma_f32_16x16x32_bf16 v[172:175], v[108:111], v[52:55], v[172:175]
buffer_load_dwordx4 v22, s[12:15], s24 offen offset:2048 lds
v_mul_f32 v39, s47, v39
v_mfma_f32_16x16x32_bf16 v[176:179], v[112:115], v[52:55], v[176:179]
ds_read_b64_tr_b16 v[80:81], v30 offset:0
ds_read_b64_tr_b16 v[82:83], v30 offset:1024
v_mfma_f32_16x16x32_bf16 v[180:183], v[116:119], v[52:55], v[180:183]
v_cvt_pk_bf16_f32 v43, v38, v39
v_accvgpr_write_b32 a136, v40
v_mfma_f32_16x16x32_bf16 v[184:187], v[120:123], v[52:55], v[184:187]
ds_read_b64_tr_b16 v[84:85], v31 offset:0
ds_read_b64_tr_b16 v[86:87], v31 offset:1024
v_mfma_f32_16x16x32_bf16 v[188:191], v[124:127], v[52:55], v[188:191]
buffer_load_dwordx4 v23, s[12:15], s24 offen offset:3072 lds
v_accvgpr_write_b32 a137, v41
v_mfma_f32_16x16x32_bf16 v[192:195], v[96:99], v[56:59], v[192:195]
ds_read_b128 v[52:55], v24 offset:2048
v_accvgpr_write_b32 a138, v42
v_mfma_f32_16x16x32_bf16 v[196:199], v[100:103], v[56:59], v[196:199]
v_accvgpr_write_b32 a139, v43
v_accvgpr_read_b32 v36, a16
v_mfma_f32_16x16x32_bf16 v[200:203], v[104:107], v[56:59], v[200:203]
s_add_u32 m0, s92, 102400
ds_read_b64_tr_b16 v[88:89], v32 offset:0
ds_read_b64_tr_b16 v[90:91], v32 offset:1024
v_mfma_f32_16x16x32_bf16 v[204:207], v[108:111], v[56:59], v[204:207]
buffer_load_dwordx4 v20, s[12:15], s25 offen offset:0 lds
v_accvgpr_read_b32 v37, a17
v_mfma_f32_16x16x32_bf16 v[208:211], v[112:115], v[56:59], v[208:211]
ds_read_b64_tr_b16 v[92:93], v33 offset:0
ds_read_b64_tr_b16 v[94:95], v33 offset:1024
v_mfma_f32_16x16x32_bf16 v[212:215], v[116:119], v[56:59], v[212:215]
v_mul_f32 v36, s47, v36
v_mul_f32 v37, s47, v37
v_mfma_f32_16x16x32_bf16 v[216:219], v[120:123], v[56:59], v[216:219]
s_waitcnt lgkmcnt(15)
v_cvt_pk_bf16_f32 v44, v36, v37
v_accvgpr_read_b32 v38, a18
v_mfma_f32_16x16x32_bf16 v[220:223], v[124:127], v[56:59], v[220:223]
v_accvgpr_read_b32 v39, a19
v_mul_f32 v38, s47, v38
v_mfma_f32_16x16x32_bf16 v[224:227], v[96:99], v[60:63], v[224:227]
ds_read_b128 v[56:59], v24 offset:4096
v_mul_f32 v39, s47, v39
v_mfma_f32_16x16x32_bf16 v[228:231], v[100:103], v[60:63], v[228:231]
buffer_load_dwordx4 v21, s[12:15], s25 offen offset:1024 lds
v_cvt_pk_bf16_f32 v45, v38, v39
v_mfma_f32_16x16x32_bf16 v[232:235], v[104:107], v[60:63], v[232:235]
v_accvgpr_read_b32 v36, a20
v_accvgpr_read_b32 v37, a21
v_mfma_f32_16x16x32_bf16 v[236:239], v[108:111], v[60:63], v[236:239]
v_mul_f32 v36, s47, v36
v_mul_f32 v37, s47, v37
v_mfma_f32_16x16x32_bf16 v[240:243], v[112:115], v[60:63], v[240:243]
v_cvt_pk_bf16_f32 v46, v36, v37
v_accvgpr_read_b32 v38, a22
v_mfma_f32_16x16x32_bf16 v[244:247], v[116:119], v[60:63], v[244:247]
v_accvgpr_read_b32 v39, a23
v_mul_f32 v38, s47, v38
v_mfma_f32_16x16x32_bf16 v[248:251], v[120:123], v[60:63], v[248:251]
v_mul_f32 v39, s47, v39
v_cvt_pk_bf16_f32 v47, v38, v39
v_mfma_f32_16x16x32_bf16 v[252:255], v[124:127], v[60:63], v[252:255]
s_waitcnt lgkmcnt(0)
s_xor_b32 s48, s95, 2048
v_accvgpr_read_b32 v36, a152
v_mfma_f32_16x16x32_bf16 v[128:131], v[64:67], v[48:51], v[128:131]
ds_read_b128 v[60:63], v24 offset:6144
v_accvgpr_read_b32 v37, a153
v_mfma_f32_16x16x32_bf16 v[132:135], v[68:71], v[48:51], v[132:135]
buffer_store_dwordx4 v[44:47], v7, s[20:23], s48 offen sc1
v_add_u32 v24, 4294868992, v24
v_mfma_f32_16x16x32_bf16 v[136:139], v[72:75], v[48:51], v[136:139]
ds_read_b64_tr_b16 v[96:97], v26 offset:8192
ds_read_b64_tr_b16 v[98:99], v26 offset:9216
v_mfma_f32_16x16x32_bf16 v[140:143], v[76:79], v[48:51], v[140:143]
buffer_load_dwordx4 v22, s[12:15], s25 offen offset:2048 lds
v_mul_f32 v36, s47, v36
v_mfma_f32_16x16x32_bf16 v[144:147], v[80:83], v[48:51], v[144:147]
ds_read_b64_tr_b16 v[100:101], v27 offset:8192
ds_read_b64_tr_b16 v[102:103], v27 offset:9216
v_mfma_f32_16x16x32_bf16 v[148:151], v[84:87], v[48:51], v[148:151]
v_mul_f32 v37, s47, v37
v_cvt_pk_bf16_f32 v40, v36, v37
v_mfma_f32_16x16x32_bf16 v[152:155], v[88:91], v[48:51], v[152:155]
ds_read_b64_tr_b16 v[104:105], v28 offset:8192
ds_read_b64_tr_b16 v[106:107], v28 offset:9216
v_mfma_f32_16x16x32_bf16 v[156:159], v[92:95], v[48:51], v[156:159]
buffer_load_dwordx4 v23, s[12:15], s25 offen offset:3072 lds
v_accvgpr_read_b32 v38, a154
v_mfma_f32_16x16x32_bf16 v[160:163], v[64:67], v[52:55], v[160:163]
ds_read_b128 v[48:51], v25 offset:0
v_accvgpr_read_b32 v39, a155
v_mfma_f32_16x16x32_bf16 v[164:167], v[68:71], v[52:55], v[164:167]
s_add_u32 s24, s24, 128
s_add_u32 s25, s25, 128
v_mul_f32 v38, s47, v38
v_mfma_f32_16x16x32_bf16 v[168:171], v[72:75], v[52:55], v[168:171]
s_add_u32 m0, s91, 81920
ds_read_b64_tr_b16 v[108:109], v29 offset:8192
ds_read_b64_tr_b16 v[110:111], v29 offset:9216
v_mfma_f32_16x16x32_bf16 v[172:175], v[76:79], v[52:55], v[172:175]
buffer_load_dwordx4 v16, s[16:19], s26 offen offset:0 lds
v_mul_f32 v39, s47, v39
v_mfma_f32_16x16x32_bf16 v[176:179], v[80:83], v[52:55], v[176:179]
ds_read_b64_tr_b16 v[112:113], v30 offset:8192
ds_read_b64_tr_b16 v[114:115], v30 offset:9216
v_mfma_f32_16x16x32_bf16 v[180:183], v[84:87], v[52:55], v[180:183]
v_cvt_pk_bf16_f32 v41, v38, v39
v_accvgpr_read_b32 v36, a156
v_mfma_f32_16x16x32_bf16 v[184:187], v[88:91], v[52:55], v[184:187]
ds_read_b64_tr_b16 v[116:117], v31 offset:8192
ds_read_b64_tr_b16 v[118:119], v31 offset:9216
v_mfma_f32_16x16x32_bf16 v[188:191], v[92:95], v[52:55], v[188:191]
buffer_load_dwordx4 v17, s[16:19], s26 offen offset:1024 lds
v_accvgpr_read_b32 v37, a157
v_mfma_f32_16x16x32_bf16 v[192:195], v[64:67], v[56:59], v[192:195]
ds_read_b128 v[52:55], v25 offset:2048
v_mul_f32 v36, s47, v36
v_mfma_f32_16x16x32_bf16 v[196:199], v[68:71], v[56:59], v[196:199]
v_mul_f32 v37, s47, v37
v_cvt_pk_bf16_f32 v42, v36, v37
v_mfma_f32_16x16x32_bf16 v[200:203], v[72:75], v[56:59], v[200:203]
ds_read_b64_tr_b16 v[120:121], v32 offset:8192
ds_read_b64_tr_b16 v[122:123], v32 offset:9216
v_mfma_f32_16x16x32_bf16 v[204:207], v[76:79], v[56:59], v[204:207]
buffer_load_dwordx4 v18, s[16:19], s26 offen offset:2048 lds
v_accvgpr_read_b32 v38, a158
v_mfma_f32_16x16x32_bf16 v[208:211], v[80:83], v[56:59], v[208:211]
ds_read_b64_tr_b16 v[124:125], v33 offset:8192
ds_read_b64_tr_b16 v[126:127], v33 offset:9216
v_mfma_f32_16x16x32_bf16 v[212:215], v[84:87], v[56:59], v[212:215]
v_accvgpr_read_b32 v39, a159
v_mul_f32 v38, s47, v38
v_mfma_f32_16x16x32_bf16 v[216:219], v[88:91], v[56:59], v[216:219]
s_waitcnt lgkmcnt(15)
v_mul_f32 v39, s47, v39
v_cvt_pk_bf16_f32 v43, v38, v39
v_mfma_f32_16x16x32_bf16 v[220:223], v[92:95], v[56:59], v[220:223]
v_accvgpr_write_b32 a140, v40
v_accvgpr_write_b32 a141, v41
v_mfma_f32_16x16x32_bf16 v[224:227], v[64:67], v[60:63], v[224:227]
ds_read_b128 v[56:59], v25 offset:4096
v_accvgpr_write_b32 a142, v42
v_mfma_f32_16x16x32_bf16 v[228:231], v[68:71], v[60:63], v[228:231]
buffer_load_dwordx4 v19, s[16:19], s26 offen offset:3072 lds
v_accvgpr_write_b32 a143, v43
v_mfma_f32_16x16x32_bf16 v[232:235], v[72:75], v[60:63], v[232:235]
v_accvgpr_read_b32 v36, a24
v_accvgpr_read_b32 v37, a25
v_mfma_f32_16x16x32_bf16 v[236:239], v[76:79], v[60:63], v[236:239]
s_add_u32 s26, s26, 524288
v_mul_f32 v36, s47, v36
v_mul_f32 v37, s47, v37
v_mfma_f32_16x16x32_bf16 v[240:243], v[80:83], v[60:63], v[240:243]
v_cvt_pk_bf16_f32 v44, v36, v37
v_accvgpr_read_b32 v38, a26
v_mfma_f32_16x16x32_bf16 v[244:247], v[84:87], v[60:63], v[244:247]
v_accvgpr_read_b32 v39, a27
v_mul_f32 v38, s47, v38
v_mfma_f32_16x16x32_bf16 v[248:251], v[88:91], v[60:63], v[248:251]
v_mul_f32 v39, s47, v39
v_cvt_pk_bf16_f32 v45, v38, v39
v_mfma_f32_16x16x32_bf16 v[252:255], v[92:95], v[60:63], v[252:255]
s_waitcnt vmcnt(18) lgkmcnt(0)
s_barrier
v_accvgpr_read_b32 v36, a28
v_mfma_f32_16x16x32_bf16 v[128:131], v[96:99], v[48:51], v[128:131]
ds_read_b128 v[60:63], v25 offset:6144
v_accvgpr_read_b32 v37, a29
v_mfma_f32_16x16x32_bf16 v[132:135], v[100:103], v[48:51], v[132:135]
v_add_u32 v25, 4294868992, v25
v_mul_f32 v36, s47, v36
v_mfma_f32_16x16x32_bf16 v[136:139], v[104:107], v[48:51], v[136:139]
s_add_u32 m0, s92, 131072
ds_read_b64_tr_b16 v[64:65], v26 offset:16384
ds_read_b64_tr_b16 v[66:67], v26 offset:17408
v_mfma_f32_16x16x32_bf16 v[140:143], v[108:111], v[48:51], v[140:143]
buffer_load_dwordx4 v20, s[12:15], s24 offen offset:0 lds
v_mul_f32 v37, s47, v37
v_mfma_f32_16x16x32_bf16 v[144:147], v[112:115], v[48:51], v[144:147]
ds_read_b64_tr_b16 v[68:69], v27 offset:16384
ds_read_b64_tr_b16 v[70:71], v27 offset:17408
v_mfma_f32_16x16x32_bf16 v[148:151], v[116:119], v[48:51], v[148:151]
v_cvt_pk_bf16_f32 v46, v36, v37
v_accvgpr_read_b32 v38, a30
v_mfma_f32_16x16x32_bf16 v[152:155], v[120:123], v[48:51], v[152:155]
ds_read_b64_tr_b16 v[72:73], v28 offset:16384
ds_read_b64_tr_b16 v[74:75], v28 offset:17408
v_mfma_f32_16x16x32_bf16 v[156:159], v[124:127], v[48:51], v[156:159]
buffer_load_dwordx4 v21, s[12:15], s24 offen offset:1024 lds
v_accvgpr_read_b32 v39, a31
v_mfma_f32_16x16x32_bf16 v[160:163], v[96:99], v[52:55], v[160:163]
ds_read_b128 v[48:51], v24 offset:0
v_mul_f32 v38, s47, v38
v_mfma_f32_16x16x32_bf16 v[164:167], v[100:103], v[52:55], v[164:167]
v_mul_f32 v39, s47, v39
v_cvt_pk_bf16_f32 v47, v38, v39
v_mfma_f32_16x16x32_bf16 v[168:171], v[104:107], v[52:55], v[168:171]
ds_read_b64_tr_b16 v[76:77], v29 offset:16384
ds_read_b64_tr_b16 v[78:79], v29 offset:17408
v_mfma_f32_16x16x32_bf16 v[172:175], v[108:111], v[52:55], v[172:175]
s_xor_b32 s48, s95, 3072
buffer_load_dwordx4 v22, s[12:15], s24 offen offset:2048 lds
v_mfma_f32_16x16x32_bf16 v[176:179], v[112:115], v[52:55], v[176:179]
ds_read_b64_tr_b16 v[80:81], v30 offset:16384
ds_read_b64_tr_b16 v[82:83], v30 offset:17408
v_mfma_f32_16x16x32_bf16 v[180:183], v[116:119], v[52:55], v[180:183]
buffer_store_dwordx4 v[44:47], v7, s[20:23], s48 offen sc1
v_accvgpr_read_b32 v36, a160
v_mfma_f32_16x16x32_bf16 v[184:187], v[120:123], v[52:55], v[184:187]
ds_read_b64_tr_b16 v[84:85], v31 offset:16384
ds_read_b64_tr_b16 v[86:87], v31 offset:17408
v_mfma_f32_16x16x32_bf16 v[188:191], v[124:127], v[52:55], v[188:191]
buffer_load_dwordx4 v23, s[12:15], s24 offen offset:3072 lds
v_accvgpr_read_b32 v37, a161
v_mfma_f32_16x16x32_bf16 v[192:195], v[96:99], v[56:59], v[192:195]
ds_read_b128 v[52:55], v24 offset:2048
v_mul_f32 v36, s47, v36
v_mfma_f32_16x16x32_bf16 v[196:199], v[100:103], v[56:59], v[196:199]
v_mul_f32 v37, s47, v37
v_cvt_pk_bf16_f32 v40, v36, v37
v_mfma_f32_16x16x32_bf16 v[200:203], v[104:107], v[56:59], v[200:203]
s_add_u32 m0, s92, 135168
ds_read_b64_tr_b16 v[88:89], v32 offset:16384
ds_read_b64_tr_b16 v[90:91], v32 offset:17408
v_mfma_f32_16x16x32_bf16 v[204:207], v[108:111], v[56:59], v[204:207]
buffer_load_dwordx4 v20, s[12:15], s25 offen offset:0 lds
v_accvgpr_read_b32 v38, a162
v_mfma_f32_16x16x32_bf16 v[208:211], v[112:115], v[56:59], v[208:211]
ds_read_b64_tr_b16 v[92:93], v33 offset:16384
ds_read_b64_tr_b16 v[94:95], v33 offset:17408
v_mfma_f32_16x16x32_bf16 v[212:215], v[116:119], v[56:59], v[212:215]
v_accvgpr_read_b32 v39, a163
v_mul_f32 v38, s47, v38
v_mfma_f32_16x16x32_bf16 v[216:219], v[120:123], v[56:59], v[216:219]
s_waitcnt lgkmcnt(15)
v_mul_f32 v39, s47, v39
v_cvt_pk_bf16_f32 v41, v38, v39
v_mfma_f32_16x16x32_bf16 v[220:223], v[124:127], v[56:59], v[220:223]
v_accvgpr_read_b32 v36, a164
.Lpad5:
s_cbranch_execz .Ltch1
v_accvgpr_read_b32 v37, a165
v_mfma_f32_16x16x32_bf16 v[224:227], v[96:99], v[60:63], v[224:227]
ds_read_b128 v[56:59], v24 offset:4096
v_mul_f32 v36, s47, v36
v_mfma_f32_16x16x32_bf16 v[228:231], v[100:103], v[60:63], v[228:231]
buffer_load_dwordx4 v21, s[12:15], s25 offen offset:1024 lds
v_mul_f32 v37, s47, v37
v_mfma_f32_16x16x32_bf16 v[232:235], v[104:107], v[60:63], v[232:235]
v_cvt_pk_bf16_f32 v42, v36, v37
v_accvgpr_read_b32 v38, a166
v_mfma_f32_16x16x32_bf16 v[236:239], v[108:111], v[60:63], v[236:239]
v_accvgpr_read_b32 v39, a167
v_mul_f32 v38, s47, v38
v_mfma_f32_16x16x32_bf16 v[240:243], v[112:115], v[60:63], v[240:243]
v_mul_f32 v39, s47, v39
v_cvt_pk_bf16_f32 v43, v38, v39
v_mfma_f32_16x16x32_bf16 v[244:247], v[116:119], v[60:63], v[244:247]
v_accvgpr_write_b32 a160, v40
v_accvgpr_write_b32 a161, v41
v_mfma_f32_16x16x32_bf16 v[248:251], v[120:123], v[60:63], v[248:251]
v_accvgpr_write_b32 a162, v42
v_accvgpr_write_b32 a163, v43
v_mfma_f32_16x16x32_bf16 v[252:255], v[124:127], v[60:63], v[252:255]
s_waitcnt lgkmcnt(0)
v_accvgpr_read_b32 v36, a168
v_accvgpr_read_b32 v37, a169
v_mfma_f32_16x16x32_bf16 v[128:131], v[64:67], v[48:51], v[128:131]
ds_read_b128 v[60:63], v24 offset:6144
v_mul_f32 v36, s47, v36
v_mfma_f32_16x16x32_bf16 v[132:135], v[68:71], v[48:51], v[132:135]
v_add_u32 v24, 65536, v24
v_mul_f32 v37, s47, v37
v_mfma_f32_16x16x32_bf16 v[136:139], v[72:75], v[48:51], v[136:139]
ds_read_b64_tr_b16 v[96:97], v26 offset:24576
ds_read_b64_tr_b16 v[98:99], v26 offset:25600
v_mfma_f32_16x16x32_bf16 v[140:143], v[76:79], v[48:51], v[140:143]
buffer_load_dwordx4 v22, s[12:15], s25 offen offset:2048 lds
v_cvt_pk_bf16_f32 v44, v36, v37
v_mfma_f32_16x16x32_bf16 v[144:147], v[80:83], v[48:51], v[144:147]
ds_read_b64_tr_b16 v[100:101], v27 offset:24576
ds_read_b64_tr_b16 v[102:103], v27 offset:25600
v_mfma_f32_16x16x32_bf16 v[148:151], v[84:87], v[48:51], v[148:151]
v_accvgpr_read_b32 v38, a170
v_accvgpr_read_b32 v39, a171
v_mfma_f32_16x16x32_bf16 v[152:155], v[88:91], v[48:51], v[152:155]
ds_read_b64_tr_b16 v[104:105], v28 offset:24576
ds_read_b64_tr_b16 v[106:107], v28 offset:25600
v_mfma_f32_16x16x32_bf16 v[156:159], v[92:95], v[48:51], v[156:159]
buffer_load_dwordx4 v23, s[12:15], s25 offen offset:3072 lds
v_mul_f32 v38, s47, v38
v_mfma_f32_16x16x32_bf16 v[160:163], v[64:67], v[52:55], v[160:163]
ds_read_b128 v[48:51], v25 offset:0
v_mul_f32 v39, s47, v39
v_mfma_f32_16x16x32_bf16 v[164:167], v[68:71], v[52:55], v[164:167]
s_add_u32 s24, s24, 128
s_add_u32 s25, s25, 128
v_cvt_pk_bf16_f32 v45, v38, v39
v_mfma_f32_16x16x32_bf16 v[168:171], v[72:75], v[52:55], v[168:171]
s_add_u32 m0, s91, 0
ds_read_b64_tr_b16 v[108:109], v29 offset:24576
ds_read_b64_tr_b16 v[110:111], v29 offset:25600
v_mfma_f32_16x16x32_bf16 v[172:175], v[76:79], v[52:55], v[172:175]
buffer_load_dwordx4 v16, s[16:19], s26 offen offset:0 lds
v_accvgpr_read_b32 v36, a172
v_mfma_f32_16x16x32_bf16 v[176:179], v[80:83], v[52:55], v[176:179]
ds_read_b64_tr_b16 v[112:113], v30 offset:24576
ds_read_b64_tr_b16 v[114:115], v30 offset:25600
v_mfma_f32_16x16x32_bf16 v[180:183], v[84:87], v[52:55], v[180:183]
v_accvgpr_read_b32 v37, a173
v_mul_f32 v36, s47, v36
v_mfma_f32_16x16x32_bf16 v[184:187], v[88:91], v[52:55], v[184:187]
ds_read_b64_tr_b16 v[116:117], v31 offset:24576
ds_read_b64_tr_b16 v[118:119], v31 offset:25600
v_mfma_f32_16x16x32_bf16 v[188:191], v[92:95], v[52:55], v[188:191]
buffer_load_dwordx4 v17, s[16:19], s26 offen offset:1024 lds
v_mul_f32 v37, s47, v37
v_mfma_f32_16x16x32_bf16 v[192:195], v[64:67], v[56:59], v[192:195]
ds_read_b128 v[52:55], v25 offset:2048
v_cvt_pk_bf16_f32 v46, v36, v37
v_mfma_f32_16x16x32_bf16 v[196:199], v[68:71], v[56:59], v[196:199]
v_accvgpr_read_b32 v38, a174
v_accvgpr_read_b32 v39, a175
v_mfma_f32_16x16x32_bf16 v[200:203], v[72:75], v[56:59], v[200:203]
ds_read_b64_tr_b16 v[120:121], v32 offset:24576
ds_read_b64_tr_b16 v[122:123], v32 offset:25600
v_mfma_f32_16x16x32_bf16 v[204:207], v[76:79], v[56:59], v[204:207]
buffer_load_dwordx4 v18, s[16:19], s26 offen offset:2048 lds
v_mul_f32 v38, s47, v38
v_mfma_f32_16x16x32_bf16 v[208:211], v[80:83], v[56:59], v[208:211]
ds_read_b64_tr_b16 v[124:125], v33 offset:24576
ds_read_b64_tr_b16 v[126:127], v33 offset:25600
v_mfma_f32_16x16x32_bf16 v[212:215], v[84:87], v[56:59], v[212:215]
v_mul_f32 v39, s47, v39
v_cvt_pk_bf16_f32 v47, v38, v39
v_mfma_f32_16x16x32_bf16 v[216:219], v[88:91], v[56:59], v[216:219]
s_waitcnt lgkmcnt(15)
v_accvgpr_write_b32 a164, v44
v_accvgpr_write_b32 a165, v45
v_mfma_f32_16x16x32_bf16 v[220:223], v[92:95], v[56:59], v[220:223]
v_xor_b32 v26, 0x10000, v26
v_accvgpr_write_b32 a166, v46
v_mfma_f32_16x16x32_bf16 v[224:227], v[64:67], v[60:63], v[224:227]
ds_read_b128 v[56:59], v25 offset:4096
v_xor_b32 v27, 0x10000, v27
v_mfma_f32_16x16x32_bf16 v[228:231], v[68:71], v[60:63], v[228:231]
buffer_load_dwordx4 v19, s[16:19], s26 offen offset:3072 lds
v_xor_b32 v28, 0x10000, v28
v_mfma_f32_16x16x32_bf16 v[232:235], v[72:75], v[60:63], v[232:235]
v_xor_b32 v29, 0x10000, v29
v_accvgpr_write_b32 a167, v47
v_mfma_f32_16x16x32_bf16 v[236:239], v[76:79], v[60:63], v[236:239]
s_add_u32 s26, s26, 524288
v_xor_b32 v30, 0x10000, v30
v_accvgpr_read_b32 v36, a32
v_mfma_f32_16x16x32_bf16 v[240:243], v[80:83], v[60:63], v[240:243]
v_xor_b32 v31, 0x10000, v31
v_accvgpr_read_b32 v37, a33
v_mfma_f32_16x16x32_bf16 v[244:247], v[84:87], v[60:63], v[244:247]
v_xor_b32 v32, 0x10000, v32
v_mul_f32 v36, s47, v36
v_mfma_f32_16x16x32_bf16 v[248:251], v[88:91], v[60:63], v[248:251]
v_xor_b32 v33, 0x10000, v33
v_mul_f32 v37, s47, v37
v_mfma_f32_16x16x32_bf16 v[252:255], v[92:95], v[60:63], v[252:255]
s_waitcnt vmcnt(17) lgkmcnt(0)
s_barrier
v_cvt_pk_bf16_f32 v40, v36, v37
v_mfma_f32_16x16x32_bf16 v[128:131], v[96:99], v[48:51], v[128:131]
ds_read_b128 v[60:63], v25 offset:6144
v_accvgpr_read_b32 v38, a34
v_mfma_f32_16x16x32_bf16 v[132:135], v[100:103], v[48:51], v[132:135]
v_add_u32 v25, 65536, v25
v_accvgpr_read_b32 v39, a35
v_mfma_f32_16x16x32_bf16 v[136:139], v[104:107], v[48:51], v[136:139]
s_add_u32 m0, s92, 32768
ds_read_b64_tr_b16 v[64:65], v26 offset:0
ds_read_b64_tr_b16 v[66:67], v26 offset:1024
v_mfma_f32_16x16x32_bf16 v[140:143], v[108:111], v[48:51], v[140:143]
buffer_load_dwordx4 v20, s[12:15], s24 offen offset:0 lds
v_mul_f32 v38, s47, v38
v_mfma_f32_16x16x32_bf16 v[144:147], v[112:115], v[48:51], v[144:147]
ds_read_b64_tr_b16 v[68:69], v27 offset:0
ds_read_b64_tr_b16 v[70:71], v27 offset:1024
v_mfma_f32_16x16x32_bf16 v[148:151], v[116:119], v[48:51], v[148:151]
v_mul_f32 v39, s47, v39
v_cvt_pk_bf16_f32 v41, v38, v39
v_mfma_f32_16x16x32_bf16 v[152:155], v[120:123], v[48:51], v[152:155]
ds_read_b64_tr_b16 v[72:73], v28 offset:0
ds_read_b64_tr_b16 v[74:75], v28 offset:1024
v_mfma_f32_16x16x32_bf16 v[156:159], v[124:127], v[48:51], v[156:159]
buffer_load_dwordx4 v21, s[12:15], s24 offen offset:1024 lds
v_accvgpr_read_b32 v36, a36
v_mfma_f32_16x16x32_bf16 v[160:163], v[96:99], v[52:55], v[160:163]
ds_read_b128 v[48:51], v24 offset:0
v_accvgpr_read_b32 v37, a37
v_mfma_f32_16x16x32_bf16 v[164:167], v[100:103], v[52:55], v[164:167]
v_mul_f32 v36, s47, v36
v_mul_f32 v37, s47, v37
v_mfma_f32_16x16x32_bf16 v[168:171], v[104:107], v[52:55], v[168:171]
ds_read_b64_tr_b16 v[76:77], v29 offset:0
ds_read_b64_tr_b16 v[78:79], v29 offset:1024
v_mfma_f32_16x16x32_bf16 v[172:175], v[108:111], v[52:55], v[172:175]
buffer_load_dwordx4 v22, s[12:15], s24 offen offset:2048 lds
v_cvt_pk_bf16_f32 v42, v36, v37
v_mfma_f32_16x16x32_bf16 v[176:179], v[112:115], v[52:55], v[176:179]
ds_read_b64_tr_b16 v[80:81], v30 offset:0
ds_read_b64_tr_b16 v[82:83], v30 offset:1024
v_mfma_f32_16x16x32_bf16 v[180:183], v[116:119], v[52:55], v[180:183]
v_accvgpr_read_b32 v38, a38
v_accvgpr_read_b32 v39, a39
v_mfma_f32_16x16x32_bf16 v[184:187], v[120:123], v[52:55], v[184:187]
ds_read_b64_tr_b16 v[84:85], v31 offset:0
ds_read_b64_tr_b16 v[86:87], v31 offset:1024
v_mfma_f32_16x16x32_bf16 v[188:191], v[124:127], v[52:55], v[188:191]
buffer_load_dwordx4 v23, s[12:15], s24 offen offset:3072 lds
v_mul_f32 v38, s47, v38
v_mfma_f32_16x16x32_bf16 v[192:195], v[96:99], v[56:59], v[192:195]
ds_read_b128 v[52:55], v24 offset:2048
v_mul_f32 v39, s47, v39
v_mfma_f32_16x16x32_bf16 v[196:199], v[100:103], v[56:59], v[196:199]
v_cvt_pk_bf16_f32 v43, v38, v39
v_accvgpr_read_b32 v36, a176
v_mfma_f32_16x16x32_bf16 v[200:203], v[104:107], v[56:59], v[200:203]
s_add_u32 m0, s92, 36864
ds_read_b64_tr_b16 v[88:89], v32 offset:0
ds_read_b64_tr_b16 v[90:91], v32 offset:1024
v_mfma_f32_16x16x32_bf16 v[204:207], v[108:111], v[56:59], v[204:207]
s_xor_b32 s48, s95, 4096
buffer_load_dwordx4 v20, s[12:15], s25 offen offset:0 lds
v_mfma_f32_16x16x32_bf16 v[208:211], v[112:115], v[56:59], v[208:211]
ds_read_b64_tr_b16 v[92:93], v33 offset:0
ds_read_b64_tr_b16 v[94:95], v33 offset:1024
v_mfma_f32_16x16x32_bf16 v[212:215], v[116:119], v[56:59], v[212:215]
buffer_store_dwordx4 v[40:43], v7, s[20:23], s48 offen sc1
v_accvgpr_read_b32 v37, a177
v_mfma_f32_16x16x32_bf16 v[216:219], v[120:123], v[56:59], v[216:219]
s_waitcnt lgkmcnt(15)
v_mul_f32 v36, s47, v36
v_mul_f32 v37, s47, v37
v_mfma_f32_16x16x32_bf16 v[220:223], v[124:127], v[56:59], v[220:223]
v_cvt_pk_bf16_f32 v44, v36, v37
v_accvgpr_read_b32 v38, a178
v_mfma_f32_16x16x32_bf16 v[224:227], v[96:99], v[60:63], v[224:227]
ds_read_b128 v[56:59], v24 offset:4096
v_accvgpr_read_b32 v39, a179
v_mfma_f32_16x16x32_bf16 v[228:231], v[100:103], v[60:63], v[228:231]
buffer_load_dwordx4 v21, s[12:15], s25 offen offset:1024 lds
v_mul_f32 v38, s47, v38
v_mfma_f32_16x16x32_bf16 v[232:235], v[104:107], v[60:63], v[232:235]
v_mul_f32 v39, s47, v39
v_cvt_pk_bf16_f32 v45, v38, v39
v_mfma_f32_16x16x32_bf16 v[236:239], v[108:111], v[60:63], v[236:239]
v_accvgpr_read_b32 v36, a180
v_accvgpr_read_b32 v37, a181
v_mfma_f32_16x16x32_bf16 v[240:243], v[112:115], v[60:63], v[240:243]
v_mul_f32 v36, s47, v36
v_mul_f32 v37, s47, v37
v_mfma_f32_16x16x32_bf16 v[244:247], v[116:119], v[60:63], v[244:247]
v_cvt_pk_bf16_f32 v46, v36, v37
v_accvgpr_read_b32 v38, a182
v_mfma_f32_16x16x32_bf16 v[248:251], v[120:123], v[60:63], v[248:251]
v_accvgpr_read_b32 v39, a183
v_mul_f32 v38, s47, v38
v_mfma_f32_16x16x32_bf16 v[252:255], v[124:127], v[60:63], v[252:255]
s_waitcnt lgkmcnt(0)
v_mul_f32 v39, s47, v39
v_cvt_pk_bf16_f32 v47, v38, v39
v_mfma_f32_16x16x32_bf16 v[128:131], v[64:67], v[48:51], v[128:131]
ds_read_b128 v[60:63], v24 offset:6144
v_accvgpr_write_b32 a168, v44
v_mfma_f32_16x16x32_bf16 v[132:135], v[68:71], v[48:51], v[132:135]
v_add_u32 v24, 32768, v24
v_accvgpr_write_b32 a169, v45
v_mfma_f32_16x16x32_bf16 v[136:139], v[72:75], v[48:51], v[136:139]
ds_read_b64_tr_b16 v[96:97], v26 offset:8192
ds_read_b64_tr_b16 v[98:99], v26 offset:9216
v_mfma_f32_16x16x32_bf16 v[140:143], v[76:79], v[48:51], v[140:143]
buffer_load_dwordx4 v22, s[12:15], s25 offen offset:2048 lds
v_accvgpr_write_b32 a170, v46
v_mfma_f32_16x16x32_bf16 v[144:147], v[80:83], v[48:51], v[144:147]
ds_read_b64_tr_b16 v[100:101], v27 offset:8192
ds_read_b64_tr_b16 v[102:103], v27 offset:9216
v_mfma_f32_16x16x32_bf16 v[148:151], v[84:87], v[48:51], v[148:151]
v_accvgpr_write_b32 a171, v47
v_accvgpr_read_b32 v36, a40
v_mfma_f32_16x16x32_bf16 v[152:155], v[88:91], v[48:51], v[152:155]
ds_read_b64_tr_b16 v[104:105], v28 offset:8192
ds_read_b64_tr_b16 v[106:107], v28 offset:9216
v_mfma_f32_16x16x32_bf16 v[156:159], v[92:95], v[48:51], v[156:159]
buffer_load_dwordx4 v23, s[12:15], s25 offen offset:3072 lds
v_accvgpr_read_b32 v37, a41
v_mfma_f32_16x16x32_bf16 v[160:163], v[64:67], v[52:55], v[160:163]
ds_read_b128 v[48:51], v25 offset:0
v_mul_f32 v36, s47, v36
v_mfma_f32_16x16x32_bf16 v[164:167], v[68:71], v[52:55], v[164:167]
s_add_u32 s24, s24, 128
s_add_u32 s25, s25, 128
buffer_load_dword v35, v4, s[96:99], 0 offen
v_mfma_f32_16x16x32_bf16 v[168:171], v[72:75], v[52:55], v[168:171]
s_add_u32 m0, s91, 16384
ds_read_b64_tr_b16 v[108:109], v29 offset:8192
ds_read_b64_tr_b16 v[110:111], v29 offset:9216
v_mfma_f32_16x16x32_bf16 v[172:175], v[76:79], v[52:55], v[172:175]
buffer_load_dwordx4 v16, s[16:19], s26 offen offset:0 lds
v_mul_f32 v37, s47, v37
v_mfma_f32_16x16x32_bf16 v[176:179], v[80:83], v[52:55], v[176:179]
ds_read_b64_tr_b16 v[112:113], v30 offset:8192
ds_read_b64_tr_b16 v[114:115], v30 offset:9216
v_mfma_f32_16x16x32_bf16 v[180:183], v[84:87], v[52:55], v[180:183]
v_cvt_pk_bf16_f32 v40, v36, v37
v_accvgpr_read_b32 v38, a42
v_mfma_f32_16x16x32_bf16 v[184:187], v[88:91], v[52:55], v[184:187]
ds_read_b64_tr_b16 v[116:117], v31 offset:8192
ds_read_b64_tr_b16 v[118:119], v31 offset:9216
v_mfma_f32_16x16x32_bf16 v[188:191], v[92:95], v[52:55], v[188:191]
buffer_load_dwordx4 v17, s[16:19], s26 offen offset:1024 lds
v_accvgpr_read_b32 v39, a43
v_mfma_f32_16x16x32_bf16 v[192:195], v[64:67], v[56:59], v[192:195]
ds_read_b128 v[52:55], v25 offset:2048
v_mul_f32 v38, s47, v38
v_mfma_f32_16x16x32_bf16 v[196:199], v[68:71], v[56:59], v[196:199]
v_mul_f32 v39, s47, v39
v_cvt_pk_bf16_f32 v41, v38, v39
v_mfma_f32_16x16x32_bf16 v[200:203], v[72:75], v[56:59], v[200:203]
ds_read_b64_tr_b16 v[120:121], v32 offset:8192
ds_read_b64_tr_b16 v[122:123], v32 offset:9216
v_mfma_f32_16x16x32_bf16 v[204:207], v[76:79], v[56:59], v[204:207]
buffer_load_dwordx4 v18, s[16:19], s26 offen offset:2048 lds
v_accvgpr_read_b32 v36, a44
v_mfma_f32_16x16x32_bf16 v[208:211], v[80:83], v[56:59], v[208:211]
ds_read_b64_tr_b16 v[124:125], v33 offset:8192
ds_read_b64_tr_b16 v[126:127], v33 offset:9216
v_mfma_f32_16x16x32_bf16 v[212:215], v[84:87], v[56:59], v[212:215]
v_accvgpr_read_b32 v37, a45
v_mul_f32 v36, s47, v36
v_mfma_f32_16x16x32_bf16 v[216:219], v[88:91], v[56:59], v[216:219]
s_waitcnt lgkmcnt(15)
v_mul_f32 v37, s47, v37
v_cvt_pk_bf16_f32 v42, v36, v37
v_mfma_f32_16x16x32_bf16 v[220:223], v[92:95], v[56:59], v[220:223]
v_accvgpr_read_b32 v38, a46
v_accvgpr_read_b32 v39, a47
v_mfma_f32_16x16x32_bf16 v[224:227], v[64:67], v[60:63], v[224:227]
ds_read_b128 v[56:59], v25 offset:4096
v_mul_f32 v38, s47, v38
v_mfma_f32_16x16x32_bf16 v[228:231], v[68:71], v[60:63], v[228:231]
buffer_load_dwordx4 v19, s[16:19], s26 offen offset:3072 lds
v_mul_f32 v39, s47, v39
v_mfma_f32_16x16x32_bf16 v[232:235], v[72:75], v[60:63], v[232:235]
v_cvt_pk_bf16_f32 v43, v38, v39
v_accvgpr_read_b32 v36, a184
v_mfma_f32_16x16x32_bf16 v[236:239], v[76:79], v[60:63], v[236:239]
s_add_u32 s26, s26, 524288
s_xor_b32 s48, s95, 5120
v_accvgpr_read_b32 v37, a185
v_mfma_f32_16x16x32_bf16 v[240:243], v[80:83], v[60:63], v[240:243]
buffer_store_dwordx4 v[40:43], v7, s[20:23], s48 offen sc1
v_mul_f32 v36, s47, v36
v_mfma_f32_16x16x32_bf16 v[244:247], v[84:87], v[60:63], v[244:247]
v_mul_f32 v37, s47, v37
v_cvt_pk_bf16_f32 v44, v36, v37
v_mfma_f32_16x16x32_bf16 v[248:251], v[88:91], v[60:63], v[248:251]
v_accvgpr_read_b32 v38, a186
v_accvgpr_read_b32 v39, a187
v_mfma_f32_16x16x32_bf16 v[252:255], v[92:95], v[60:63], v[252:255]
s_waitcnt vmcnt(19) lgkmcnt(0)
s_barrier
v_mul_f32 v38, s47, v38
v_mfma_f32_16x16x32_bf16 v[128:131], v[96:99], v[48:51], v[128:131]
ds_read_b128 v[60:63], v25 offset:6144
v_mul_f32 v39, s47, v39
v_mfma_f32_16x16x32_bf16 v[132:135], v[100:103], v[48:51], v[132:135]
v_add_u32 v25, 32768, v25
v_cvt_pk_bf16_f32 v45, v38, v39
v_mfma_f32_16x16x32_bf16 v[136:139], v[104:107], v[48:51], v[136:139]
s_add_u32 m0, s92, 98304
ds_read_b64_tr_b16 v[64:65], v26 offset:16384
ds_read_b64_tr_b16 v[66:67], v26 offset:17408
v_mfma_f32_16x16x32_bf16 v[140:143], v[108:111], v[48:51], v[140:143]
buffer_load_dwordx4 v20, s[12:15], s24 offen offset:0 lds
v_accvgpr_read_b32 v36, a188
v_mfma_f32_16x16x32_bf16 v[144:147], v[112:115], v[48:51], v[144:147]
ds_read_b64_tr_b16 v[68:69], v27 offset:16384
ds_read_b64_tr_b16 v[70:71], v27 offset:17408
v_mfma_f32_16x16x32_bf16 v[148:151], v[116:119], v[48:51], v[148:151]
v_accvgpr_read_b32 v37, a189
v_mul_f32 v36, s47, v36
v_mfma_f32_16x16x32_bf16 v[152:155], v[120:123], v[48:51], v[152:155]
ds_read_b64_tr_b16 v[72:73], v28 offset:16384
ds_read_b64_tr_b16 v[74:75], v28 offset:17408
v_mfma_f32_16x16x32_bf16 v[156:159], v[124:127], v[48:51], v[156:159]
buffer_load_dwordx4 v21, s[12:15], s24 offen offset:1024 lds
v_mul_f32 v37, s47, v37
v_mfma_f32_16x16x32_bf16 v[160:163], v[96:99], v[52:55], v[160:163]
ds_read_b128 v[48:51], v24 offset:0
v_cvt_pk_bf16_f32 v46, v36, v37
v_mfma_f32_16x16x32_bf16 v[164:167], v[100:103], v[52:55], v[164:167]
v_accvgpr_read_b32 v38, a190
v_accvgpr_read_b32 v39, a191
v_mfma_f32_16x16x32_bf16 v[168:171], v[104:107], v[52:55], v[168:171]
ds_read_b64_tr_b16 v[76:77], v29 offset:16384
ds_read_b64_tr_b16 v[78:79], v29 offset:17408
v_mfma_f32_16x16x32_bf16 v[172:175], v[108:111], v[52:55], v[172:175]
buffer_load_dwordx4 v22, s[12:15], s24 offen offset:2048 lds
v_mul_f32 v38, s47, v38
v_mfma_f32_16x16x32_bf16 v[176:179], v[112:115], v[52:55], v[176:179]
ds_read_b64_tr_b16 v[80:81], v30 offset:16384
ds_read_b64_tr_b16 v[82:83], v30 offset:17408
v_mfma_f32_16x16x32_bf16 v[180:183], v[116:119], v[52:55], v[180:183]
v_mul_f32 v39, s47, v39
v_cvt_pk_bf16_f32 v47, v38, v39
v_mfma_f32_16x16x32_bf16 v[184:187], v[120:123], v[52:55], v[184:187]
ds_read_b64_tr_b16 v[84:85], v31 offset:16384
ds_read_b64_tr_b16 v[86:87], v31 offset:17408
v_mfma_f32_16x16x32_bf16 v[188:191], v[124:127], v[52:55], v[188:191]
buffer_load_dwordx4 v23, s[12:15], s24 offen offset:3072 lds
v_accvgpr_write_b32 a172, v44
v_mfma_f32_16x16x32_bf16 v[192:195], v[96:99], v[56:59], v[192:195]
ds_read_b128 v[52:55], v24 offset:2048
v_accvgpr_write_b32 a173, v45
v_mfma_f32_16x16x32_bf16 v[196:199], v[100:103], v[56:59], v[196:199]
v_accvgpr_write_b32 a174, v46
v_accvgpr_write_b32 a175, v47
v_mfma_f32_16x16x32_bf16 v[200:203], v[104:107], v[56:59], v[200:203]
s_add_u32 m0, s92, 102400
ds_read_b64_tr_b16 v[88:89], v32 offset:16384
ds_read_b64_tr_b16 v[90:91], v32 offset:17408
v_mfma_f32_16x16x32_bf16 v[204:207], v[108:111], v[56:59], v[204:207]
buffer_load_dwordx4 v20, s[12:15], s25 offen offset:0 lds
v_accvgpr_read_b32 v36, a48
v_mfma_f32_16x16x32_bf16 v[208:211], v[112:115], v[56:59], v[208:211]
ds_read_b64_tr_b16 v[92:93], v33 offset:16384
ds_read_b64_tr_b16 v[94:95], v33 offset:17408
v_mfma_f32_16x16x32_bf16 v[212:215], v[116:119], v[56:59], v[212:215]
v_accvgpr_read_b32 v37, a49
v_mul_f32 v36, s47, v36
v_mfma_f32_16x16x32_bf16 v[216:219], v[120:123], v[56:59], v[216:219]
s_waitcnt lgkmcnt(15)
v_mul_f32 v37, s47, v37
v_cvt_pk_bf16_f32 v40, v36, v37
v_mfma_f32_16x16x32_bf16 v[220:223], v[124:127], v[56:59], v[220:223]
v_accvgpr_read_b32 v38, a50
v_accvgpr_read_b32 v39, a51
v_mfma_f32_16x16x32_bf16 v[224:227], v[96:99], v[60:63], v[224:227]
ds_read_b128 v[56:59], v24 offset:4096
v_mul_f32 v38, s47, v38
v_mfma_f32_16x16x32_bf16 v[228:231], v[100:103], v[60:63], v[228:231]
buffer_load_dwordx4 v21, s[12:15], s25 offen offset:1024 lds
v_mul_f32 v39, s47, v39
v_mfma_f32_16x16x32_bf16 v[232:235], v[104:107], v[60:63], v[232:235]
v_cvt_pk_bf16_f32 v41, v38, v39
v_accvgpr_read_b32 v36, a52
v_mfma_f32_16x16x32_bf16 v[236:239], v[108:111], v[60:63], v[236:239]
v_accvgpr_read_b32 v37, a53
v_mul_f32 v36, s47, v36
v_mfma_f32_16x16x32_bf16 v[240:243], v[112:115], v[60:63], v[240:243]
v_mul_f32 v37, s47, v37
v_cvt_pk_bf16_f32 v42, v36, v37
v_mfma_f32_16x16x32_bf16 v[244:247], v[116:119], v[60:63], v[244:247]
v_accvgpr_read_b32 v38, a54
v_accvgpr_read_b32 v39, a55
v_mfma_f32_16x16x32_bf16 v[248:251], v[120:123], v[60:63], v[248:251]
v_mul_f32 v38, s47, v38
v_mul_f32 v39, s47, v39
v_mfma_f32_16x16x32_bf16 v[252:255], v[124:127], v[60:63], v[252:255]
s_waitcnt lgkmcnt(0)
v_cvt_pk_bf16_f32 v43, v38, v39
v_accvgpr_read_b32 v36, a192
v_mfma_f32_16x16x32_bf16 v[128:131], v[64:67], v[48:51], v[128:131]
s_xor_b32 s48, s95, 6144
ds_read_b128 v[60:63], v24 offset:6144
v_mfma_f32_16x16x32_bf16 v[132:135], v[68:71], v[48:51], v[132:135]
buffer_store_dwordx4 v[40:43], v7, s[20:23], s48 offen sc1
v_add_u32 v24, 4294868992, v24
v_mfma_f32_16x16x32_bf16 v[136:139], v[72:75], v[48:51], v[136:139]
ds_read_b64_tr_b16 v[96:97], v26 offset:24576
ds_read_b64_tr_b16 v[98:99], v26 offset:25600
v_mfma_f32_16x16x32_bf16 v[140:143], v[76:79], v[48:51], v[140:143]
buffer_load_dwordx4 v22, s[12:15], s25 offen offset:2048 lds
v_accvgpr_read_b32 v37, a193
v_mfma_f32_16x16x32_bf16 v[144:147], v[80:83], v[48:51], v[144:147]
ds_read_b64_tr_b16 v[100:101], v27 offset:24576
ds_read_b64_tr_b16 v[102:103], v27 offset:25600
v_mfma_f32_16x16x32_bf16 v[148:151], v[84:87], v[48:51], v[148:151]
v_mul_f32 v36, s47, v36
v_mul_f32 v37, s47, v37
v_mfma_f32_16x16x32_bf16 v[152:155], v[88:91], v[48:51], v[152:155]
ds_read_b64_tr_b16 v[104:105], v28 offset:24576
ds_read_b64_tr_b16 v[106:107], v28 offset:25600
v_mfma_f32_16x16x32_bf16 v[156:159], v[92:95], v[48:51], v[156:159]
buffer_load_dwordx4 v23, s[12:15], s25 offen offset:3072 lds
v_cvt_pk_bf16_f32 v44, v36, v37
v_mfma_f32_16x16x32_bf16 v[160:163], v[64:67], v[52:55], v[160:163]
ds_read_b128 v[48:51], v25 offset:0
v_accvgpr_read_b32 v38, a194
v_mfma_f32_16x16x32_bf16 v[164:167], v[68:71], v[52:55], v[164:167]
s_add_u32 s24, s24, 128
s_add_u32 s25, s25, 128
v_accvgpr_read_b32 v39, a195
v_mfma_f32_16x16x32_bf16 v[168:171], v[72:75], v[52:55], v[168:171]
s_add_u32 m0, s91, 65536
ds_read_b64_tr_b16 v[108:109], v29 offset:24576
ds_read_b64_tr_b16 v[110:111], v29 offset:25600
v_mfma_f32_16x16x32_bf16 v[172:175], v[76:79], v[52:55], v[172:175]
buffer_load_dwordx4 v16, s[16:19], s26 offen offset:0 lds
v_mul_f32 v38, s47, v38
v_mfma_f32_16x16x32_bf16 v[176:179], v[80:83], v[52:55], v[176:179]
ds_read_b64_tr_b16 v[112:113], v30 offset:24576
ds_read_b64_tr_b16 v[114:115], v30 offset:25600
v_mfma_f32_16x16x32_bf16 v[180:183], v[84:87], v[52:55], v[180:183]
v_mul_f32 v39, s47, v39
v_cvt_pk_bf16_f32 v45, v38, v39
v_mfma_f32_16x16x32_bf16 v[184:187], v[88:91], v[52:55], v[184:187]
ds_read_b64_tr_b16 v[116:117], v31 offset:24576
ds_read_b64_tr_b16 v[118:119], v31 offset:25600
v_mfma_f32_16x16x32_bf16 v[188:191], v[92:95], v[52:55], v[188:191]
buffer_load_dwordx4 v17, s[16:19], s26 offen offset:1024 lds
v_accvgpr_read_b32 v36, a196
v_mfma_f32_16x16x32_bf16 v[192:195], v[64:67], v[56:59], v[192:195]
ds_read_b128 v[52:55], v25 offset:2048
v_accvgpr_read_b32 v37, a197
v_mfma_f32_16x16x32_bf16 v[196:199], v[68:71], v[56:59], v[196:199]
v_mul_f32 v36, s47, v36
v_mul_f32 v37, s47, v37
v_mfma_f32_16x16x32_bf16 v[200:203], v[72:75], v[56:59], v[200:203]
ds_read_b64_tr_b16 v[120:121], v32 offset:24576
ds_read_b64_tr_b16 v[122:123], v32 offset:25600
v_mfma_f32_16x16x32_bf16 v[204:207], v[76:79], v[56:59], v[204:207]
buffer_load_dwordx4 v18, s[16:19], s26 offen offset:2048 lds
v_cvt_pk_bf16_f32 v46, v36, v37
v_mfma_f32_16x16x32_bf16 v[208:211], v[80:83], v[56:59], v[208:211]
ds_read_b64_tr_b16 v[124:125], v33 offset:24576
ds_read_b64_tr_b16 v[126:127], v33 offset:25600
v_mfma_f32_16x16x32_bf16 v[212:215], v[84:87], v[56:59], v[212:215]
v_accvgpr_read_b32 v38, a198
v_accvgpr_read_b32 v39, a199
v_mfma_f32_16x16x32_bf16 v[216:219], v[88:91], v[56:59], v[216:219]
s_waitcnt lgkmcnt(15)
v_mul_f32 v38, s47, v38
v_mul_f32 v39, s47, v39
v_mfma_f32_16x16x32_bf16 v[220:223], v[92:95], v[56:59], v[220:223]
v_xor_b32 v26, 0x10000, v26
v_cvt_pk_bf16_f32 v47, v38, v39
v_mfma_f32_16x16x32_bf16 v[224:227], v[64:67], v[60:63], v[224:227]
ds_read_b128 v[56:59], v25 offset:4096
v_xor_b32 v27, 0x10000, v27
v_mfma_f32_16x16x32_bf16 v[228:231], v[68:71], v[60:63], v[228:231]
buffer_load_dwordx4 v19, s[16:19], s26 offen offset:3072 lds
v_xor_b32 v28, 0x10000, v28
v_mfma_f32_16x16x32_bf16 v[232:235], v[72:75], v[60:63], v[232:235]
v_xor_b32 v29, 0x10000, v29
v_accvgpr_write_b32 a192, v44
v_mfma_f32_16x16x32_bf16 v[236:239], v[76:79], v[60:63], v[236:239]
s_add_u32 s26, s26, 524288
v_xor_b32 v30, 0x10000, v30
v_accvgpr_write_b32 a193, v45
v_mfma_f32_16x16x32_bf16 v[240:243], v[80:83], v[60:63], v[240:243]
v_xor_b32 v31, 0x10000, v31
v_accvgpr_write_b32 a194, v46
v_mfma_f32_16x16x32_bf16 v[244:247], v[84:87], v[60:63], v[244:247]
v_xor_b32 v32, 0x10000, v32
v_accvgpr_write_b32 a195, v47
v_mfma_f32_16x16x32_bf16 v[248:251], v[88:91], v[60:63], v[248:251]
v_xor_b32 v33, 0x10000, v33
v_mfma_f32_16x16x32_bf16 v[252:255], v[92:95], v[60:63], v[252:255]
s_waitcnt vmcnt(19) lgkmcnt(0)
s_barrier
v_accvgpr_read_b32 v36, a56
v_mfma_f32_16x16x32_bf16 v[128:131], v[96:99], v[48:51], v[128:131]
ds_read_b128 v[60:63], v25 offset:6144
v_accvgpr_read_b32 v37, a57
v_mfma_f32_16x16x32_bf16 v[132:135], v[100:103], v[48:51], v[132:135]
v_add_u32 v25, 4294868992, v25
v_mul_f32 v36, s47, v36
v_mfma_f32_16x16x32_bf16 v[136:139], v[104:107], v[48:51], v[136:139]
s_add_u32 m0, s92, 131072
ds_read_b64_tr_b16 v[64:65], v26 offset:0
ds_read_b64_tr_b16 v[66:67], v26 offset:1024
v_mfma_f32_16x16x32_bf16 v[140:143], v[108:111], v[48:51], v[140:143]
buffer_load_dwordx4 v20, s[12:15], s24 offen offset:0 lds
v_mul_f32 v37, s47, v37
v_mfma_f32_16x16x32_bf16 v[144:147], v[112:115], v[48:51], v[144:147]
ds_read_b64_tr_b16 v[68:69], v27 offset:0
ds_read_b64_tr_b16 v[70:71], v27 offset:1024
v_mfma_f32_16x16x32_bf16 v[148:151], v[116:119], v[48:51], v[148:151]
v_cvt_pk_bf16_f32 v40, v36, v37
v_accvgpr_read_b32 v38, a58
v_mfma_f32_16x16x32_bf16 v[152:155], v[120:123], v[48:51], v[152:155]
ds_read_b64_tr_b16 v[72:73], v28 offset:0
ds_read_b64_tr_b16 v[74:75], v28 offset:1024
.Lpad6:
s_cbranch_execz .Ltch1
v_mfma_f32_16x16x32_bf16 v[156:159], v[124:127], v[48:51], v[156:159]
buffer_load_dwordx4 v21, s[12:15], s24 offen offset:1024 lds
v_accvgpr_read_b32 v39, a59
v_mfma_f32_16x16x32_bf16 v[160:163], v[96:99], v[52:55], v[160:163]
ds_read_b128 v[48:51], v24 offset:0
v_mul_f32 v38, s47, v38
v_mfma_f32_16x16x32_bf16 v[164:167], v[100:103], v[52:55], v[164:167]
v_mul_f32 v39, s47, v39
v_cvt_pk_bf16_f32 v41, v38, v39
v_mfma_f32_16x16x32_bf16 v[168:171], v[104:107], v[52:55], v[168:171]
ds_read_b64_tr_b16 v[76:77], v29 offset:0
ds_read_b64_tr_b16 v[78:79], v29 offset:1024
v_mfma_f32_16x16x32_bf16 v[172:175], v[108:111], v[52:55], v[172:175]
buffer_load_dwordx4 v22, s[12:15], s24 offen offset:2048 lds
v_accvgpr_read_b32 v36, a60
v_mfma_f32_16x16x32_bf16 v[176:179], v[112:115], v[52:55], v[176:179]
ds_read_b64_tr_b16 v[80:81], v30 offset:0
ds_read_b64_tr_b16 v[82:83], v30 offset:1024
v_mfma_f32_16x16x32_bf16 v[180:183], v[116:119], v[52:55], v[180:183]
v_accvgpr_read_b32 v37, a61
v_mul_f32 v36, s47, v36
v_mfma_f32_16x16x32_bf16 v[184:187], v[120:123], v[52:55], v[184:187]
ds_read_b64_tr_b16 v[84:85], v31 offset:0
ds_read_b64_tr_b16 v[86:87], v31 offset:1024
v_mfma_f32_16x16x32_bf16 v[188:191], v[124:127], v[52:55], v[188:191]
buffer_load_dwordx4 v23, s[12:15], s24 offen offset:3072 lds
v_mul_f32 v37, s47, v37
v_mfma_f32_16x16x32_bf16 v[192:195], v[96:99], v[56:59], v[192:195]
ds_read_b128 v[52:55], v24 offset:2048
v_cvt_pk_bf16_f32 v42, v36, v37
v_mfma_f32_16x16x32_bf16 v[196:199], v[100:103], v[56:59], v[196:199]
v_accvgpr_read_b32 v38, a62
v_accvgpr_read_b32 v39, a63
v_mfma_f32_16x16x32_bf16 v[200:203], v[104:107], v[56:59], v[200:203]
s_add_u32 m0, s92, 135168
ds_read_b64_tr_b16 v[88:89], v32 offset:0
ds_read_b64_tr_b16 v[90:91], v32 offset:1024
v_mfma_f32_16x16x32_bf16 v[204:207], v[108:111], v[56:59], v[204:207]
buffer_load_dwordx4 v20, s[12:15], s25 offen offset:0 lds
v_mul_f32 v38, s47, v38
v_mfma_f32_16x16x32_bf16 v[208:211], v[112:115], v[56:59], v[208:211]
ds_read_b64_tr_b16 v[92:93], v33 offset:0
ds_read_b64_tr_b16 v[94:95], v33 offset:1024
v_mfma_f32_16x16x32_bf16 v[212:215], v[116:119], v[56:59], v[212:215]
v_mul_f32 v39, s47, v39
v_cvt_pk_bf16_f32 v43, v38, v39
v_mfma_f32_16x16x32_bf16 v[216:219], v[120:123], v[56:59], v[216:219]
s_waitcnt lgkmcnt(15)
s_xor_b32 s48, s95, 7168
v_accvgpr_read_b32 v36, a200
v_mfma_f32_16x16x32_bf16 v[220:223], v[124:127], v[56:59], v[220:223]
buffer_store_dwordx4 v[40:43], v7, s[20:23], s48 offen sc1
v_accvgpr_read_b32 v37, a201
v_mfma_f32_16x16x32_bf16 v[224:227], v[96:99], v[60:63], v[224:227]
ds_read_b128 v[56:59], v24 offset:4096
v_mul_f32 v36, s47, v36
v_mfma_f32_16x16x32_bf16 v[228:231], v[100:103], v[60:63], v[228:231]
buffer_load_dwordx4 v21, s[12:15], s25 offen offset:1024 lds
v_mul_f32 v37, s47, v37
v_mfma_f32_16x16x32_bf16 v[232:235], v[104:107], v[60:63], v[232:235]
v_cvt_pk_bf16_f32 v44, v36, v37
v_accvgpr_read_b32 v38, a202
v_mfma_f32_16x16x32_bf16 v[236:239], v[108:111], v[60:63], v[236:239]
v_accvgpr_read_b32 v39, a203
v_mul_f32 v38, s47, v38
v_mfma_f32_16x16x32_bf16 v[240:243], v[112:115], v[60:63], v[240:243]
v_mul_f32 v39, s47, v39
v_cvt_pk_bf16_f32 v45, v38, v39
v_mfma_f32_16x16x32_bf16 v[244:247], v[116:119], v[60:63], v[244:247]
v_accvgpr_read_b32 v36, a204
v_accvgpr_read_b32 v37, a205
v_mfma_f32_16x16x32_bf16 v[248:251], v[120:123], v[60:63], v[248:251]
v_mul_f32 v36, s47, v36
v_mul_f32 v37, s47, v37
v_mfma_f32_16x16x32_bf16 v[252:255], v[124:127], v[60:63], v[252:255]
s_waitcnt lgkmcnt(0)
v_cvt_pk_bf16_f32 v46, v36, v37
v_accvgpr_read_b32 v38, a206
v_mfma_f32_16x16x32_bf16 v[128:131], v[64:67], v[48:51], v[128:131]
ds_read_b128 v[60:63], v24 offset:6144
v_accvgpr_read_b32 v39, a207
v_mfma_f32_16x16x32_bf16 v[132:135], v[68:71], v[48:51], v[132:135]
v_add_u32 v24, 65536, v24
v_mul_f32 v38, s47, v38
v_mfma_f32_16x16x32_bf16 v[136:139], v[72:75], v[48:51], v[136:139]
ds_read_b64_tr_b16 v[96:97], v26 offset:8192
ds_read_b64_tr_b16 v[98:99], v26 offset:9216
v_mfma_f32_16x16x32_bf16 v[140:143], v[76:79], v[48:51], v[140:143]
buffer_load_dwordx4 v22, s[12:15], s25 offen offset:2048 lds
v_mul_f32 v39, s47, v39
v_mfma_f32_16x16x32_bf16 v[144:147], v[80:83], v[48:51], v[144:147]
ds_read_b64_tr_b16 v[100:101], v27 offset:8192
ds_read_b64_tr_b16 v[102:103], v27 offset:9216
v_mfma_f32_16x16x32_bf16 v[148:151], v[84:87], v[48:51], v[148:151]
v_cvt_pk_bf16_f32 v47, v38, v39
v_accvgpr_write_b32 a196, v44
v_mfma_f32_16x16x32_bf16 v[152:155], v[88:91], v[48:51], v[152:155]
ds_read_b64_tr_b16 v[104:105], v28 offset:8192
ds_read_b64_tr_b16 v[106:107], v28 offset:9216
v_mfma_f32_16x16x32_bf16 v[156:159], v[92:95], v[48:51], v[156:159]
buffer_load_dwordx4 v23, s[12:15], s25 offen offset:3072 lds
v_accvgpr_write_b32 a197, v45
v_mfma_f32_16x16x32_bf16 v[160:163], v[64:67], v[52:55], v[160:163]
ds_read_b128 v[48:51], v25 offset:0
v_accvgpr_write_b32 a198, v46
v_mfma_f32_16x16x32_bf16 v[164:167], v[68:71], v[52:55], v[164:167]
s_add_u32 s24, s24, 128
s_add_u32 s25, s25, 128
v_accvgpr_write_b32 a199, v47
v_mfma_f32_16x16x32_bf16 v[168:171], v[72:75], v[52:55], v[168:171]
s_add_u32 m0, s91, 81920
ds_read_b64_tr_b16 v[108:109], v29 offset:8192
ds_read_b64_tr_b16 v[110:111], v29 offset:9216
v_mfma_f32_16x16x32_bf16 v[172:175], v[76:79], v[52:55], v[172:175]
buffer_load_dwordx4 v16, s[16:19], s26 offen offset:0 lds
v_mfma_f32_16x16x32_bf16 v[176:179], v[80:83], v[52:55], v[176:179]
ds_read_b64_tr_b16 v[112:113], v30 offset:8192
ds_read_b64_tr_b16 v[114:115], v30 offset:9216
v_mfma_f32_16x16x32_bf16 v[180:183], v[84:87], v[52:55], v[180:183]
v_mfma_f32_16x16x32_bf16 v[184:187], v[88:91], v[52:55], v[184:187]
ds_read_b64_tr_b16 v[116:117], v31 offset:8192
ds_read_b64_tr_b16 v[118:119], v31 offset:9216
v_mfma_f32_16x16x32_bf16 v[188:191], v[92:95], v[52:55], v[188:191]
buffer_load_dwordx4 v17, s[16:19], s26 offen offset:1024 lds
v_mfma_f32_16x16x32_bf16 v[192:195], v[64:67], v[56:59], v[192:195]
ds_read_b128 v[52:55], v25 offset:2048
v_mfma_f32_16x16x32_bf16 v[196:199], v[68:71], v[56:59], v[196:199]
v_accvgpr_read_b32 v36, a64
v_accvgpr_read_b32 v37, a65
v_mfma_f32_16x16x32_bf16 v[200:203], v[72:75], v[56:59], v[200:203]
ds_read_b64_tr_b16 v[120:121], v32 offset:8192
ds_read_b64_tr_b16 v[122:123], v32 offset:9216
v_mfma_f32_16x16x32_bf16 v[204:207], v[76:79], v[56:59], v[204:207]
buffer_load_dwordx4 v18, s[16:19], s26 offen offset:2048 lds
v_mul_f32 v36, s47, v36
v_mfma_f32_16x16x32_bf16 v[208:211], v[80:83], v[56:59], v[208:211]
ds_read_b64_tr_b16 v[124:125], v33 offset:8192
ds_read_b64_tr_b16 v[126:127], v33 offset:9216
v_mfma_f32_16x16x32_bf16 v[212:215], v[84:87], v[56:59], v[212:215]
v_mul_f32 v37, s47, v37
v_cvt_pk_bf16_f32 v40, v36, v37
v_mfma_f32_16x16x32_bf16 v[216:219], v[88:91], v[56:59], v[216:219]
s_waitcnt lgkmcnt(15)
v_accvgpr_read_b32 v38, a66
v_accvgpr_read_b32 v39, a67
v_mfma_f32_16x16x32_bf16 v[220:223], v[92:95], v[56:59], v[220:223]
v_mul_f32 v38, s47, v38
v_mul_f32 v39, s47, v39
v_mfma_f32_16x16x32_bf16 v[224:227], v[64:67], v[60:63], v[224:227]
ds_read_b128 v[56:59], v25 offset:4096
v_cvt_pk_bf16_f32 v41, v38, v39
v_mfma_f32_16x16x32_bf16 v[228:231], v[68:71], v[60:63], v[228:231]
buffer_load_dwordx4 v19, s[16:19], s26 offen offset:3072 lds
v_accvgpr_read_b32 v36, a68
v_mfma_f32_16x16x32_bf16 v[232:235], v[72:75], v[60:63], v[232:235]
v_accvgpr_read_b32 v37, a69
v_mul_f32 v36, s47, v36
v_mfma_f32_16x16x32_bf16 v[236:239], v[76:79], v[60:63], v[236:239]
s_add_u32 s26, s26, 524288
v_mul_f32 v37, s47, v37
v_cvt_pk_bf16_f32 v42, v36, v37
v_mfma_f32_16x16x32_bf16 v[240:243], v[80:83], v[60:63], v[240:243]
v_accvgpr_read_b32 v38, a70
v_accvgpr_read_b32 v39, a71
v_mfma_f32_16x16x32_bf16 v[244:247], v[84:87], v[60:63], v[244:247]
v_mul_f32 v38, s47, v38
v_mul_f32 v39, s47, v39
v_mfma_f32_16x16x32_bf16 v[248:251], v[88:91], v[60:63], v[248:251]
v_cvt_pk_bf16_f32 v43, v38, v39
v_accvgpr_read_b32 v36, a208
v_mfma_f32_16x16x32_bf16 v[252:255], v[92:95], v[60:63], v[252:255]
s_waitcnt vmcnt(17) lgkmcnt(0)
s_barrier
s_xor_b32 s48, s95, 131072
v_mfma_f32_16x16x32_bf16 v[128:131], v[96:99], v[48:51], v[128:131]
ds_read_b128 v[60:63], v25 offset:6144
v_accvgpr_read_b32 v37, a209
v_mfma_f32_16x16x32_bf16 v[132:135], v[100:103], v[48:51], v[132:135]
buffer_store_dwordx4 v[40:43], v7, s[20:23], s48 offen sc1
v_add_u32 v25, 65536, v25
v_mfma_f32_16x16x32_bf16 v[136:139], v[104:107], v[48:51], v[136:139]
s_add_u32 m0, s92, 32768
ds_read_b64_tr_b16 v[64:65], v26 offset:16384
ds_read_b64_tr_b16 v[66:67], v26 offset:17408
v_mfma_f32_16x16x32_bf16 v[140:143], v[108:111], v[48:51], v[140:143]
buffer_load_dwordx4 v20, s[12:15], s24 offen offset:0 lds
v_mul_f32 v36, s47, v36
v_mfma_f32_16x16x32_bf16 v[144:147], v[112:115], v[48:51], v[144:147]
ds_read_b64_tr_b16 v[68:69], v27 offset:16384
ds_read_b64_tr_b16 v[70:71], v27 offset:17408
v_mfma_f32_16x16x32_bf16 v[148:151], v[116:119], v[48:51], v[148:151]
v_mul_f32 v37, s47, v37
v_cvt_pk_bf16_f32 v44, v36, v37
v_mfma_f32_16x16x32_bf16 v[152:155], v[120:123], v[48:51], v[152:155]
ds_read_b64_tr_b16 v[72:73], v28 offset:16384
ds_read_b64_tr_b16 v[74:75], v28 offset:17408
v_mfma_f32_16x16x32_bf16 v[156:159], v[124:127], v[48:51], v[156:159]
buffer_load_dwordx4 v21, s[12:15], s24 offen offset:1024 lds
v_accvgpr_read_b32 v38, a210
v_mfma_f32_16x16x32_bf16 v[160:163], v[96:99], v[52:55], v[160:163]
ds_read_b128 v[48:51], v24 offset:0
v_accvgpr_read_b32 v39, a211
v_mfma_f32_16x16x32_bf16 v[164:167], v[100:103], v[52:55], v[164:167]
v_mul_f32 v38, s47, v38
v_mul_f32 v39, s47, v39
v_mfma_f32_16x16x32_bf16 v[168:171], v[104:107], v[52:55], v[168:171]
ds_read_b64_tr_b16 v[76:77], v29 offset:16384
ds_read_b64_tr_b16 v[78:79], v29 offset:17408
v_mfma_f32_16x16x32_bf16 v[172:175], v[108:111], v[52:55], v[172:175]
buffer_load_dwordx4 v22, s[12:15], s24 offen offset:2048 lds
v_cvt_pk_bf16_f32 v45, v38, v39
v_mfma_f32_16x16x32_bf16 v[176:179], v[112:115], v[52:55], v[176:179]
ds_read_b64_tr_b16 v[80:81], v30 offset:16384
ds_read_b64_tr_b16 v[82:83], v30 offset:17408
v_mfma_f32_16x16x32_bf16 v[180:183], v[116:119], v[52:55], v[180:183]
v_accvgpr_read_b32 v36, a212
v_accvgpr_read_b32 v37, a213
v_mfma_f32_16x16x32_bf16 v[184:187], v[120:123], v[52:55], v[184:187]
ds_read_b64_tr_b16 v[84:85], v31 offset:16384
ds_read_b64_tr_b16 v[86:87], v31 offset:17408
v_mfma_f32_16x16x32_bf16 v[188:191], v[124:127], v[52:55], v[188:191]
buffer_load_dwordx4 v23, s[12:15], s24 offen offset:3072 lds
v_mul_f32 v36, s47, v36
v_mfma_f32_16x16x32_bf16 v[192:195], v[96:99], v[56:59], v[192:195]
ds_read_b128 v[52:55], v24 offset:2048
v_mul_f32 v37, s47, v37
v_mfma_f32_16x16x32_bf16 v[196:199], v[100:103], v[56:59], v[196:199]
v_cvt_pk_bf16_f32 v46, v36, v37
v_accvgpr_read_b32 v38, a214
v_mfma_f32_16x16x32_bf16 v[200:203], v[104:107], v[56:59], v[200:203]
s_add_u32 m0, s92, 36864
ds_read_b64_tr_b16 v[88:89], v32 offset:16384
ds_read_b64_tr_b16 v[90:91], v32 offset:17408
v_mfma_f32_16x16x32_bf16 v[204:207], v[108:111], v[56:59], v[204:207]
buffer_load_dwordx4 v20, s[12:15], s25 offen offset:0 lds
v_accvgpr_read_b32 v39, a215
v_mfma_f32_16x16x32_bf16 v[208:211], v[112:115], v[56:59], v[208:211]
ds_read_b64_tr_b16 v[92:93], v33 offset:16384
ds_read_b64_tr_b16 v[94:95], v33 offset:17408
v_mfma_f32_16x16x32_bf16 v[212:215], v[116:119], v[56:59], v[212:215]
v_mul_f32 v38, s47, v38
v_mul_f32 v39, s47, v39
v_mfma_f32_16x16x32_bf16 v[216:219], v[120:123], v[56:59], v[216:219]
s_waitcnt lgkmcnt(15)
v_cvt_pk_bf16_f32 v47, v38, v39
v_accvgpr_write_b32 a200, v44
v_mfma_f32_16x16x32_bf16 v[220:223], v[124:127], v[56:59], v[220:223]
v_accvgpr_write_b32 a201, v45
v_accvgpr_write_b32 a202, v46
v_mfma_f32_16x16x32_bf16 v[224:227], v[96:99], v[60:63], v[224:227]
ds_read_b128 v[56:59], v24 offset:4096
v_accvgpr_write_b32 a203, v47
v_mfma_f32_16x16x32_bf16 v[228:231], v[100:103], v[60:63], v[228:231]
buffer_load_dwordx4 v21, s[12:15], s25 offen offset:1024 lds
v_accvgpr_read_b32 v36, a216
v_mfma_f32_16x16x32_bf16 v[232:235], v[104:107], v[60:63], v[232:235]
v_accvgpr_read_b32 v37, a217
v_mul_f32 v36, s47, v36
v_mfma_f32_16x16x32_bf16 v[236:239], v[108:111], v[60:63], v[236:239]
v_mul_f32 v37, s47, v37
v_cvt_pk_bf16_f32 v40, v36, v37
v_mfma_f32_16x16x32_bf16 v[240:243], v[112:115], v[60:63], v[240:243]
v_accvgpr_read_b32 v38, a218
v_accvgpr_read_b32 v39, a219
v_mfma_f32_16x16x32_bf16 v[244:247], v[116:119], v[60:63], v[244:247]
v_mul_f32 v38, s47, v38
v_mul_f32 v39, s47, v39
v_mfma_f32_16x16x32_bf16 v[248:251], v[120:123], v[60:63], v[248:251]
v_cvt_pk_bf16_f32 v41, v38, v39
v_accvgpr_read_b32 v36, a220
v_mfma_f32_16x16x32_bf16 v[252:255], v[124:127], v[60:63], v[252:255]
s_waitcnt lgkmcnt(0)
v_accvgpr_read_b32 v37, a221
v_mul_f32 v36, s47, v36
v_mfma_f32_16x16x32_bf16 v[128:131], v[64:67], v[48:51], v[128:131]
ds_read_b128 v[60:63], v24 offset:6144
v_mul_f32 v37, s47, v37
v_mfma_f32_16x16x32_bf16 v[132:135], v[68:71], v[48:51], v[132:135]
v_add_u32 v24, 32768, v24
v_cvt_pk_bf16_f32 v42, v36, v37
v_mfma_f32_16x16x32_bf16 v[136:139], v[72:75], v[48:51], v[136:139]
ds_read_b64_tr_b16 v[96:97], v26 offset:24576
ds_read_b64_tr_b16 v[98:99], v26 offset:25600
v_mfma_f32_16x16x32_bf16 v[140:143], v[76:79], v[48:51], v[140:143]
buffer_load_dwordx4 v22, s[12:15], s25 offen offset:2048 lds
v_accvgpr_read_b32 v38, a222
v_mfma_f32_16x16x32_bf16 v[144:147], v[80:83], v[48:51], v[144:147]
ds_read_b64_tr_b16 v[100:101], v27 offset:24576
ds_read_b64_tr_b16 v[102:103], v27 offset:25600
v_mfma_f32_16x16x32_bf16 v[148:151], v[84:87], v[48:51], v[148:151]
v_accvgpr_read_b32 v39, a223
v_mul_f32 v38, s47, v38
v_mfma_f32_16x16x32_bf16 v[152:155], v[88:91], v[48:51], v[152:155]
ds_read_b64_tr_b16 v[104:105], v28 offset:24576
ds_read_b64_tr_b16 v[106:107], v28 offset:25600
v_mfma_f32_16x16x32_bf16 v[156:159], v[92:95], v[48:51], v[156:159]
buffer_load_dwordx4 v23, s[12:15], s25 offen offset:3072 lds
v_mul_f32 v39, s47, v39
v_mfma_f32_16x16x32_bf16 v[160:163], v[64:67], v[52:55], v[160:163]
ds_read_b128 v[48:51], v25 offset:0
v_cvt_pk_bf16_f32 v43, v38, v39
v_mfma_f32_16x16x32_bf16 v[164:167], v[68:71], v[52:55], v[164:167]
s_add_u32 s24, s24, 128
s_add_u32 s25, s25, 128
v_accvgpr_write_b32 a204, v40
v_mfma_f32_16x16x32_bf16 v[168:171], v[72:75], v[52:55], v[168:171]
s_add_u32 m0, s91, 0
ds_read_b64_tr_b16 v[108:109], v29 offset:24576
ds_read_b64_tr_b16 v[110:111], v29 offset:25600
v_mfma_f32_16x16x32_bf16 v[172:175], v[76:79], v[52:55], v[172:175]
buffer_load_dwordx4 v16, s[16:19], s26 offen offset:0 lds
v_accvgpr_write_b32 a205, v41
v_mfma_f32_16x16x32_bf16 v[176:179], v[80:83], v[52:55], v[176:179]
ds_read_b64_tr_b16 v[112:113], v30 offset:24576
ds_read_b64_tr_b16 v[114:115], v30 offset:25600
v_mfma_f32_16x16x32_bf16 v[180:183], v[84:87], v[52:55], v[180:183]
v_accvgpr_write_b32 a206, v42
v_accvgpr_write_b32 a207, v43
v_mfma_f32_16x16x32_bf16 v[184:187], v[88:91], v[52:55], v[184:187]
ds_read_b64_tr_b16 v[116:117], v31 offset:24576
ds_read_b64_tr_b16 v[118:119], v31 offset:25600
v_mfma_f32_16x16x32_bf16 v[188:191], v[92:95], v[52:55], v[188:191]
buffer_load_dwordx4 v17, s[16:19], s26 offen offset:1024 lds
v_accvgpr_read_b32 v36, a72
v_mfma_f32_16x16x32_bf16 v[192:195], v[64:67], v[56:59], v[192:195]
ds_read_b128 v[52:55], v25 offset:2048
v_accvgpr_read_b32 v37, a73
v_mfma_f32_16x16x32_bf16 v[196:199], v[68:71], v[56:59], v[196:199]
v_mul_f32 v36, s47, v36
v_mul_f32 v37, s47, v37
v_mfma_f32_16x16x32_bf16 v[200:203], v[72:75], v[56:59], v[200:203]
ds_read_b64_tr_b16 v[120:121], v32 offset:24576
ds_read_b64_tr_b16 v[122:123], v32 offset:25600
v_mfma_f32_16x16x32_bf16 v[204:207], v[76:79], v[56:59], v[204:207]
buffer_load_dwordx4 v18, s[16:19], s26 offen offset:2048 lds
v_cvt_pk_bf16_f32 v44, v36, v37
v_mfma_f32_16x16x32_bf16 v[208:211], v[80:83], v[56:59], v[208:211]
ds_read_b64_tr_b16 v[124:125], v33 offset:24576
ds_read_b64_tr_b16 v[126:127], v33 offset:25600
v_mfma_f32_16x16x32_bf16 v[212:215], v[84:87], v[56:59], v[212:215]
v_accvgpr_read_b32 v38, a74
v_accvgpr_read_b32 v39, a75
v_mfma_f32_16x16x32_bf16 v[216:219], v[88:91], v[56:59], v[216:219]
s_waitcnt lgkmcnt(15)
v_mul_f32 v38, s47, v38
v_mul_f32 v39, s47, v39
v_mfma_f32_16x16x32_bf16 v[220:223], v[92:95], v[56:59], v[220:223]
v_xor_b32 v26, 0x10000, v26
v_cvt_pk_bf16_f32 v45, v38, v39
v_mfma_f32_16x16x32_bf16 v[224:227], v[64:67], v[60:63], v[224:227]
ds_read_b128 v[56:59], v25 offset:4096
v_xor_b32 v27, 0x10000, v27
v_mfma_f32_16x16x32_bf16 v[228:231], v[68:71], v[60:63], v[228:231]
buffer_load_dwordx4 v19, s[16:19], s26 offen offset:3072 lds
v_xor_b32 v28, 0x10000, v28
v_mfma_f32_16x16x32_bf16 v[232:235], v[72:75], v[60:63], v[232:235]
v_xor_b32 v29, 0x10000, v29
v_accvgpr_read_b32 v36, a76
v_mfma_f32_16x16x32_bf16 v[236:239], v[76:79], v[60:63], v[236:239]
s_add_u32 s26, s26, 524288
v_xor_b32 v30, 0x10000, v30
v_accvgpr_read_b32 v37, a77
v_mfma_f32_16x16x32_bf16 v[240:243], v[80:83], v[60:63], v[240:243]
v_xor_b32 v31, 0x10000, v31
v_mul_f32 v36, s47, v36
v_mfma_f32_16x16x32_bf16 v[244:247], v[84:87], v[60:63], v[244:247]
v_xor_b32 v32, 0x10000, v32
v_mul_f32 v37, s47, v37
v_mfma_f32_16x16x32_bf16 v[248:251], v[88:91], v[60:63], v[248:251]
v_xor_b32 v33, 0x10000, v33
v_cvt_pk_bf16_f32 v46, v36, v37
v_mfma_f32_16x16x32_bf16 v[252:255], v[92:95], v[60:63], v[252:255]
s_waitcnt vmcnt(17) lgkmcnt(0)
s_barrier
v_accvgpr_read_b32 v38, a78
v_mfma_f32_16x16x32_bf16 v[128:131], v[96:99], v[48:51], v[128:131]
ds_read_b128 v[60:63], v25 offset:6144
v_accvgpr_read_b32 v39, a79
v_mfma_f32_16x16x32_bf16 v[132:135], v[100:103], v[48:51], v[132:135]
v_add_u32 v25, 32768, v25
v_mul_f32 v38, s47, v38
v_mfma_f32_16x16x32_bf16 v[136:139], v[104:107], v[48:51], v[136:139]
s_add_u32 m0, s92, 98304
ds_read_b64_tr_b16 v[64:65], v26 offset:0
ds_read_b64_tr_b16 v[66:67], v26 offset:1024
v_mfma_f32_16x16x32_bf16 v[140:143], v[108:111], v[48:51], v[140:143]
buffer_load_dwordx4 v20, s[12:15], s24 offen offset:0 lds
v_mul_f32 v39, s47, v39
v_mfma_f32_16x16x32_bf16 v[144:147], v[112:115], v[48:51], v[144:147]
ds_read_b64_tr_b16 v[68:69], v27 offset:0
ds_read_b64_tr_b16 v[70:71], v27 offset:1024
v_mfma_f32_16x16x32_bf16 v[148:151], v[116:119], v[48:51], v[148:151]
v_cvt_pk_bf16_f32 v47, v38, v39
v_accvgpr_read_b32 v36, a224
v_mfma_f32_16x16x32_bf16 v[152:155], v[120:123], v[48:51], v[152:155]
ds_read_b64_tr_b16 v[72:73], v28 offset:0
ds_read_b64_tr_b16 v[74:75], v28 offset:1024
v_mfma_f32_16x16x32_bf16 v[156:159], v[124:127], v[48:51], v[156:159]
s_xor_b32 s48, s95, 132096
buffer_load_dwordx4 v21, s[12:15], s24 offen offset:1024 lds
v_mfma_f32_16x16x32_bf16 v[160:163], v[96:99], v[52:55], v[160:163]
ds_read_b128 v[48:51], v24 offset:0
v_accvgpr_read_b32 v37, a225
v_mfma_f32_16x16x32_bf16 v[164:167], v[100:103], v[52:55], v[164:167]
buffer_store_dwordx4 v[44:47], v7, s[20:23], s48 offen sc1
v_mul_f32 v36, s47, v36
v_mfma_f32_16x16x32_bf16 v[168:171], v[104:107], v[52:55], v[168:171]
ds_read_b64_tr_b16 v[76:77], v29 offset:0
ds_read_b64_tr_b16 v[78:79], v29 offset:1024
v_mfma_f32_16x16x32_bf16 v[172:175], v[108:111], v[52:55], v[172:175]
buffer_load_dwordx4 v22, s[12:15], s24 offen offset:2048 lds
v_mul_f32 v37, s47, v37
v_mfma_f32_16x16x32_bf16 v[176:179], v[112:115], v[52:55], v[176:179]
ds_read_b64_tr_b16 v[80:81], v30 offset:0
ds_read_b64_tr_b16 v[82:83], v30 offset:1024
v_mfma_f32_16x16x32_bf16 v[180:183], v[116:119], v[52:55], v[180:183]
v_cvt_pk_bf16_f32 v40, v36, v37
v_accvgpr_read_b32 v38, a226
v_mfma_f32_16x16x32_bf16 v[184:187], v[120:123], v[52:55], v[184:187]
ds_read_b64_tr_b16 v[84:85], v31 offset:0
ds_read_b64_tr_b16 v[86:87], v31 offset:1024
v_mfma_f32_16x16x32_bf16 v[188:191], v[124:127], v[52:55], v[188:191]
buffer_load_dwordx4 v23, s[12:15], s24 offen offset:3072 lds
v_accvgpr_read_b32 v39, a227
v_mfma_f32_16x16x32_bf16 v[192:195], v[96:99], v[56:59], v[192:195]
ds_read_b128 v[52:55], v24 offset:2048
v_mul_f32 v38, s47, v38
v_mfma_f32_16x16x32_bf16 v[196:199], v[100:103], v[56:59], v[196:199]
v_mul_f32 v39, s47, v39
v_cvt_pk_bf16_f32 v41, v38, v39
v_mfma_f32_16x16x32_bf16 v[200:203], v[104:107], v[56:59], v[200:203]
s_add_u32 m0, s92, 102400
ds_read_b64_tr_b16 v[88:89], v32 offset:0
ds_read_b64_tr_b16 v[90:91], v32 offset:1024
v_mfma_f32_16x16x32_bf16 v[204:207], v[108:111], v[56:59], v[204:207]
buffer_load_dwordx4 v20, s[12:15], s25 offen offset:0 lds
v_accvgpr_read_b32 v36, a228
v_mfma_f32_16x16x32_bf16 v[208:211], v[112:115], v[56:59], v[208:211]
ds_read_b64_tr_b16 v[92:93], v33 offset:0
ds_read_b64_tr_b16 v[94:95], v33 offset:1024
v_mfma_f32_16x16x32_bf16 v[212:215], v[116:119], v[56:59], v[212:215]
v_accvgpr_read_b32 v37, a229
v_mul_f32 v36, s47, v36
v_mfma_f32_16x16x32_bf16 v[216:219], v[120:123], v[56:59], v[216:219]
s_waitcnt lgkmcnt(15)
v_mul_f32 v37, s47, v37
v_cvt_pk_bf16_f32 v42, v36, v37
v_mfma_f32_16x16x32_bf16 v[220:223], v[124:127], v[56:59], v[220:223]
v_accvgpr_read_b32 v38, a230
v_accvgpr_read_b32 v39, a231
v_mfma_f32_16x16x32_bf16 v[224:227], v[96:99], v[60:63], v[224:227]
ds_read_b128 v[56:59], v24 offset:4096
v_mul_f32 v38, s47, v38
v_mfma_f32_16x16x32_bf16 v[228:231], v[100:103], v[60:63], v[228:231]
buffer_load_dwordx4 v21, s[12:15], s25 offen offset:1024 lds
v_mul_f32 v39, s47, v39
v_mfma_f32_16x16x32_bf16 v[232:235], v[104:107], v[60:63], v[232:235]
v_cvt_pk_bf16_f32 v43, v38, v39
v_accvgpr_write_b32 a224, v40
v_mfma_f32_16x16x32_bf16 v[236:239], v[108:111], v[60:63], v[236:239]
v_accvgpr_write_b32 a225, v41
v_accvgpr_write_b32 a226, v42
v_mfma_f32_16x16x32_bf16 v[240:243], v[112:115], v[60:63], v[240:243]
v_accvgpr_write_b32 a227, v43
v_accvgpr_read_b32 v36, a80
v_mfma_f32_16x16x32_bf16 v[244:247], v[116:119], v[60:63], v[244:247]
v_accvgpr_read_b32 v37, a81
v_mul_f32 v36, s47, v36
v_mfma_f32_16x16x32_bf16 v[248:251], v[120:123], v[60:63], v[248:251]
v_mul_f32 v37, s47, v37
v_cvt_pk_bf16_f32 v44, v36, v37
v_mfma_f32_16x16x32_bf16 v[252:255], v[124:127], v[60:63], v[252:255]
s_waitcnt lgkmcnt(0)
v_accvgpr_read_b32 v38, a82
v_accvgpr_read_b32 v39, a83
v_mfma_f32_16x16x32_bf16 v[128:131], v[64:67], v[48:51], v[128:131]
ds_read_b128 v[60:63], v24 offset:6144
v_mul_f32 v38, s47, v38
v_mfma_f32_16x16x32_bf16 v[132:135], v[68:71], v[48:51], v[132:135]
v_add_u32 v24, 4294868992, v24
v_mul_f32 v39, s47, v39
v_mfma_f32_16x16x32_bf16 v[136:139], v[72:75], v[48:51], v[136:139]
ds_read_b64_tr_b16 v[96:97], v26 offset:8192
ds_read_b64_tr_b16 v[98:99], v26 offset:9216
v_mfma_f32_16x16x32_bf16 v[140:143], v[76:79], v[48:51], v[140:143]
s_lshl_b32 s28, s88, 19
buffer_load_dwordx4 v22, s[12:15], s25 offen offset:2048 lds
v_mfma_f32_16x16x32_bf16 v[144:147], v[80:83], v[48:51], v[144:147]
ds_read_b64_tr_b16 v[100:101], v27 offset:8192
ds_read_b64_tr_b16 v[102:103], v27 offset:9216
v_mfma_f32_16x16x32_bf16 v[148:151], v[84:87], v[48:51], v[148:151]
s_add_u32 s28, s28, s91
v_cvt_pk_bf16_f32 v45, v38, v39
v_accvgpr_read_b32 v36, a84
v_mfma_f32_16x16x32_bf16 v[152:155], v[88:91], v[48:51], v[152:155]
ds_read_b64_tr_b16 v[104:105], v28 offset:8192
ds_read_b64_tr_b16 v[106:107], v28 offset:9216
v_mfma_f32_16x16x32_bf16 v[156:159], v[92:95], v[48:51], v[156:159]
s_lshl_b32 s29, s89, 15
buffer_load_dwordx4 v23, s[12:15], s25 offen offset:3072 lds
v_mfma_f32_16x16x32_bf16 v[160:163], v[64:67], v[52:55], v[160:163]
s_sub_u32 s54, 0x80000, s29
ds_read_b128 v[48:51], v25 offset:0
v_mfma_f32_16x16x32_bf16 v[164:167], v[68:71], v[52:55], v[164:167]
s_add_u32 s24, s24, 128
s_add_u32 s25, s25, 128
v_accvgpr_read_b32 v37, a85
v_mfma_f32_16x16x32_bf16 v[168:171], v[72:75], v[52:55], v[168:171]
s_add_u32 m0, s91, 16384
ds_read_b64_tr_b16 v[108:109], v29 offset:8192
ds_read_b64_tr_b16 v[110:111], v29 offset:9216
v_mfma_f32_16x16x32_bf16 v[172:175], v[76:79], v[52:55], v[172:175]
s_sub_u32 s29, s54, s91
buffer_load_dwordx4 v16, s[16:19], s26 offen offset:0 lds
v_mfma_f32_16x16x32_bf16 v[176:179], v[80:83], v[52:55], v[176:179]
ds_read_b64_tr_b16 v[112:113], v30 offset:8192
ds_read_b64_tr_b16 v[114:115], v30 offset:9216
v_mfma_f32_16x16x32_bf16 v[180:183], v[84:87], v[52:55], v[180:183]
s_add_u32 s4, s84, s28
s_addc_u32 s5, s85, 0
v_mul_f32 v36, s47, v36
v_mfma_f32_16x16x32_bf16 v[184:187], v[88:91], v[52:55], v[184:187]
ds_read_b64_tr_b16 v[116:117], v31 offset:8192
ds_read_b64_tr_b16 v[118:119], v31 offset:9216
v_mfma_f32_16x16x32_bf16 v[188:191], v[92:95], v[52:55], v[188:191]
s_and_b32 s5, s5, 0xffff
buffer_load_dwordx4 v17, s[16:19], s26 offen offset:1024 lds
v_mfma_f32_16x16x32_bf16 v[192:195], v[64:67], v[56:59], v[192:195]
s_mov_b32 s6, s29
ds_read_b128 v[52:55], v25 offset:2048
v_mfma_f32_16x16x32_bf16 v[196:199], v[68:71], v[56:59], v[196:199]
s_mov_b32 s7, 0x20000
v_mul_f32 v37, s47, v37
v_cvt_pk_bf16_f32 v46, v36, v37
v_mfma_f32_16x16x32_bf16 v[200:203], v[72:75], v[56:59], v[200:203]
ds_read_b64_tr_b16 v[120:121], v32 offset:8192
ds_read_b64_tr_b16 v[122:123], v32 offset:9216
v_mfma_f32_16x16x32_bf16 v[204:207], v[76:79], v[56:59], v[204:207]
s_add_u32 s28, s28, 16760832
buffer_load_dwordx4 v18, s[16:19], s26 offen offset:2048 lds
v_mfma_f32_16x16x32_bf16 v[208:211], v[80:83], v[56:59], v[208:211]
ds_read_b64_tr_b16 v[124:125], v33 offset:8192
ds_read_b64_tr_b16 v[126:127], v33 offset:9216
v_mfma_f32_16x16x32_bf16 v[212:215], v[84:87], v[56:59], v[212:215]
s_add_u32 s29, s29, 16384
v_accvgpr_read_b32 v38, a86
v_accvgpr_read_b32 v39, a87
v_mfma_f32_16x16x32_bf16 v[216:219], v[88:91], v[56:59], v[216:219]
s_waitcnt lgkmcnt(15)
s_add_u32 s45, s54, 16384
v_mul_f32 v38, s47, v38
v_mfma_f32_16x16x32_bf16 v[220:223], v[92:95], v[56:59], v[220:223]
.Lpad7:
s_cbranch_execz .Ltch2
s_add_u32 s8, s84, s28
s_addc_u32 s9, s85, 0
v_mul_f32 v39, s47, v39
v_mfma_f32_16x16x32_bf16 v[224:227], v[64:67], v[60:63], v[224:227]
s_and_b32 s9, s9, 0xffff
ds_read_b128 v[56:59], v25 offset:4096
v_mfma_f32_16x16x32_bf16 v[228:231], v[68:71], v[60:63], v[228:231]
s_mov_b32 s10, s29
buffer_load_dwordx4 v19, s[16:19], s26 offen offset:3072 lds
v_mfma_f32_16x16x32_bf16 v[232:235], v[72:75], v[60:63], v[232:235]
s_mov_b32 s11, 0x20000
v_cvt_pk_bf16_f32 v47, v38, v39
v_accvgpr_read_b32 v36, a232
v_mfma_f32_16x16x32_bf16 v[236:239], v[76:79], v[60:63], v[236:239]
s_add_u32 s26, s26, 524288
s_xor_b32 s48, s95, 133120
v_accvgpr_read_b32 v37, a233
v_mfma_f32_16x16x32_bf16 v[240:243], v[80:83], v[60:63], v[240:243]
s_and_b32 s46, s89, 1
buffer_store_dwordx4 v[44:47], v7, s[20:23], s48 offen sc1
v_mfma_f32_16x16x32_bf16 v[244:247], v[84:87], v[60:63], v[244:247]
s_lshl_b32 s46, s46, 15
v_mul_f32 v36, s47, v36
v_mul_f32 v37, s47, v37
v_mfma_f32_16x16x32_bf16 v[248:251], v[88:91], v[60:63], v[248:251]
s_add_u32 s46, s46, s91
v_cvt_pk_bf16_f32 v40, v36, v37
v_accvgpr_read_b32 v38, a234
v_mfma_f32_16x16x32_bf16 v[252:255], v[92:95], v[60:63], v[252:255]
s_waitcnt vmcnt(34)
s_waitcnt vmcnt(18) lgkmcnt(0)
s_barrier
s_mov_b32 s39, s46
v_mfma_f32_16x16x32_bf16 v[128:131], v[96:99], v[48:51], v[128:131]
s_mov_b32 s27, 0x30000
ds_read_b128 v[60:63], v25 offset:6144
v_mfma_f32_16x16x32_bf16 v[132:135], v[100:103], v[48:51], v[132:135]
s_nop 0
v_add_u32 v25, 4294868992, v25
v_accvgpr_read_b32 v39, a235
v_or_b32 v0, 1, v11
v_mfma_f32_16x16x32_bf16 v[136:139], v[104:107], v[48:51], v[136:139]
s_add_u32 m0, s92, 131072
ds_read_b64_tr_b16 v[64:65], v26 offset:16384
ds_read_b64_tr_b16 v[66:67], v26 offset:17408
v_mfma_f32_16x16x32_bf16 v[140:143], v[108:111], v[48:51], v[140:143]
buffer_load_dwordx4 v20, s[12:15], s24 offen offset:0 lds
v_mul_f32 v38, s47, v38
v_mfma_f32_16x16x32_bf16 v[144:147], v[112:115], v[48:51], v[144:147]
ds_read_b64_tr_b16 v[68:69], v27 offset:16384
ds_read_b64_tr_b16 v[70:71], v27 offset:17408
v_mfma_f32_16x16x32_bf16 v[148:151], v[116:119], v[48:51], v[148:151]
buffer_store_dword v0, v6, s[60:63], 0 offen sc1
v_mul_f32 v39, s47, v39
v_cvt_pk_bf16_f32 v41, v38, v39
v_mfma_f32_16x16x32_bf16 v[152:155], v[120:123], v[48:51], v[152:155]
ds_read_b64_tr_b16 v[72:73], v28 offset:16384
ds_read_b64_tr_b16 v[74:75], v28 offset:17408
v_mfma_f32_16x16x32_bf16 v[156:159], v[124:127], v[48:51], v[156:159]
buffer_load_dwordx4 v21, s[12:15], s24 offen offset:1024 lds
v_accvgpr_read_b32 v36, a236
v_mfma_f32_16x16x32_bf16 v[160:163], v[96:99], v[52:55], v[160:163]
ds_read_b128 v[48:51], v24 offset:0
v_accvgpr_read_b32 v37, a237
v_mfma_f32_16x16x32_bf16 v[164:167], v[100:103], v[52:55], v[164:167]
v_mul_f32 v36, s47, v36
v_mul_f32 v37, s47, v37
v_mfma_f32_16x16x32_bf16 v[168:171], v[104:107], v[52:55], v[168:171]
ds_read_b64_tr_b16 v[76:77], v29 offset:16384
ds_read_b64_tr_b16 v[78:79], v29 offset:17408
v_mfma_f32_16x16x32_bf16 v[172:175], v[108:111], v[52:55], v[172:175]
buffer_load_dwordx4 v22, s[12:15], s24 offen offset:2048 lds
v_cvt_pk_bf16_f32 v42, v36, v37
v_mfma_f32_16x16x32_bf16 v[176:179], v[112:115], v[52:55], v[176:179]
ds_read_b64_tr_b16 v[80:81], v30 offset:16384
ds_read_b64_tr_b16 v[82:83], v30 offset:17408
v_mfma_f32_16x16x32_bf16 v[180:183], v[116:119], v[52:55], v[180:183]
v_accvgpr_read_b32 v38, a238
v_accvgpr_read_b32 v39, a239
v_mfma_f32_16x16x32_bf16 v[184:187], v[120:123], v[52:55], v[184:187]
ds_read_b64_tr_b16 v[84:85], v31 offset:16384
ds_read_b64_tr_b16 v[86:87], v31 offset:17408
v_mfma_f32_16x16x32_bf16 v[188:191], v[124:127], v[52:55], v[188:191]
buffer_load_dwordx4 v23, s[12:15], s24 offen offset:3072 lds
v_mul_f32 v38, s47, v38
v_mfma_f32_16x16x32_bf16 v[192:195], v[96:99], v[56:59], v[192:195]
ds_read_b128 v[52:55], v24 offset:2048
v_mul_f32 v39, s47, v39
v_mfma_f32_16x16x32_bf16 v[196:199], v[100:103], v[56:59], v[196:199]
v_cvt_pk_bf16_f32 v43, v38, v39
v_accvgpr_write_b32 a228, v40
v_mfma_f32_16x16x32_bf16 v[200:203], v[104:107], v[56:59], v[200:203]
s_add_u32 m0, s92, 135168
ds_read_b64_tr_b16 v[88:89], v32 offset:16384
ds_read_b64_tr_b16 v[90:91], v32 offset:17408
v_mfma_f32_16x16x32_bf16 v[204:207], v[108:111], v[56:59], v[204:207]
buffer_load_dwordx4 v20, s[12:15], s25 offen offset:0 lds
v_accvgpr_write_b32 a229, v41
v_mfma_f32_16x16x32_bf16 v[208:211], v[112:115], v[56:59], v[208:211]
ds_read_b64_tr_b16 v[92:93], v33 offset:16384
ds_read_b64_tr_b16 v[94:95], v33 offset:17408
v_mfma_f32_16x16x32_bf16 v[212:215], v[116:119], v[56:59], v[212:215]
v_accvgpr_write_b32 a230, v42
v_accvgpr_write_b32 a231, v43
v_mfma_f32_16x16x32_bf16 v[216:219], v[120:123], v[56:59], v[216:219]
s_waitcnt lgkmcnt(15)
v_accvgpr_read_b32 v36, a88
v_accvgpr_read_b32 v37, a89
v_mfma_f32_16x16x32_bf16 v[220:223], v[124:127], v[56:59], v[220:223]
v_mul_f32 v36, s47, v36
v_mul_f32 v37, s47, v37
v_mfma_f32_16x16x32_bf16 v[224:227], v[96:99], v[60:63], v[224:227]
ds_read_b128 v[56:59], v24 offset:4096
v_cvt_pk_bf16_f32 v44, v36, v37
v_mfma_f32_16x16x32_bf16 v[228:231], v[100:103], v[60:63], v[228:231]
buffer_load_dwordx4 v21, s[12:15], s25 offen offset:1024 lds
v_accvgpr_read_b32 v38, a90
v_mfma_f32_16x16x32_bf16 v[232:235], v[104:107], v[60:63], v[232:235]
v_accvgpr_read_b32 v39, a91
v_mul_f32 v38, s47, v38
v_mfma_f32_16x16x32_bf16 v[236:239], v[108:111], v[60:63], v[236:239]
v_mul_f32 v39, s47, v39
v_cvt_pk_bf16_f32 v45, v38, v39
v_mfma_f32_16x16x32_bf16 v[240:243], v[112:115], v[60:63], v[240:243]
v_accvgpr_read_b32 v36, a92
v_accvgpr_read_b32 v37, a93
v_mfma_f32_16x16x32_bf16 v[244:247], v[116:119], v[60:63], v[244:247]
v_mul_f32 v36, s47, v36
v_mul_f32 v37, s47, v37
v_mfma_f32_16x16x32_bf16 v[248:251], v[120:123], v[60:63], v[248:251]
v_cvt_pk_bf16_f32 v46, v36, v37
v_accvgpr_read_b32 v38, a94
v_mfma_f32_16x16x32_bf16 v[252:255], v[124:127], v[60:63], v[252:255]
s_waitcnt lgkmcnt(0)
v_accvgpr_read_b32 v39, a95
v_mul_f32 v38, s47, v38
v_mfma_f32_16x16x32_bf16 v[128:131], v[64:67], v[48:51], v[128:131]
ds_read_b128 v[60:63], v24 offset:6144
v_mul_f32 v39, s47, v39
v_mfma_f32_16x16x32_bf16 v[132:135], v[68:71], v[48:51], v[132:135]
v_add_u32 v24, 65536, v24
v_cvt_pk_bf16_f32 v47, v38, v39
v_mfma_f32_16x16x32_bf16 v[136:139], v[72:75], v[48:51], v[136:139]
ds_read_b64_tr_b16 v[96:97], v26 offset:24576
ds_read_b64_tr_b16 v[98:99], v26 offset:25600
v_mfma_f32_16x16x32_bf16 v[140:143], v[76:79], v[48:51], v[140:143]
s_xor_b32 s48, s95, 134144
buffer_load_dwordx4 v22, s[12:15], s25 offen offset:2048 lds
v_mfma_f32_16x16x32_bf16 v[144:147], v[80:83], v[48:51], v[144:147]
ds_read_b64_tr_b16 v[100:101], v27 offset:24576
ds_read_b64_tr_b16 v[102:103], v27 offset:25600
v_mfma_f32_16x16x32_bf16 v[148:151], v[84:87], v[48:51], v[148:151]
buffer_store_dwordx4 v[44:47], v7, s[20:23], s48 offen sc1
v_accvgpr_read_b32 v36, a240
v_mfma_f32_16x16x32_bf16 v[152:155], v[88:91], v[48:51], v[152:155]
ds_read_b64_tr_b16 v[104:105], v28 offset:24576
ds_read_b64_tr_b16 v[106:107], v28 offset:25600
v_mfma_f32_16x16x32_bf16 v[156:159], v[92:95], v[48:51], v[156:159]
buffer_load_dwordx4 v23, s[12:15], s25 offen offset:3072 lds
v_accvgpr_read_b32 v37, a241
v_mfma_f32_16x16x32_bf16 v[160:163], v[64:67], v[52:55], v[160:163]
ds_read_b128 v[48:51], v25 offset:0
v_mul_f32 v36, s47, v36
v_mfma_f32_16x16x32_bf16 v[164:167], v[68:71], v[52:55], v[164:167]
s_add_u32 s24, s24, 128
s_add_u32 s25, s25, 128
buffer_load_dword v35, v4, s[96:99], 0 offen
v_mfma_f32_16x16x32_bf16 v[168:171], v[72:75], v[52:55], v[168:171]
s_add_u32 m0, s91, 65536
ds_read_b64_tr_b16 v[108:109], v29 offset:24576
ds_read_b64_tr_b16 v[110:111], v29 offset:25600
v_mfma_f32_16x16x32_bf16 v[172:175], v[76:79], v[52:55], v[172:175]
buffer_load_dwordx4 v16, s[16:19], s26 offen offset:0 lds
v_mul_f32 v37, s47, v37
v_mfma_f32_16x16x32_bf16 v[176:179], v[80:83], v[52:55], v[176:179]
ds_read_b64_tr_b16 v[112:113], v30 offset:24576
ds_read_b64_tr_b16 v[114:115], v30 offset:25600
v_mfma_f32_16x16x32_bf16 v[180:183], v[84:87], v[52:55], v[180:183]
v_cvt_pk_bf16_f32 v40, v36, v37
v_accvgpr_read_b32 v38, a242
v_mfma_f32_16x16x32_bf16 v[184:187], v[88:91], v[52:55], v[184:187]
ds_read_b64_tr_b16 v[116:117], v31 offset:24576
ds_read_b64_tr_b16 v[118:119], v31 offset:25600
v_mfma_f32_16x16x32_bf16 v[188:191], v[92:95], v[52:55], v[188:191]
buffer_load_dwordx4 v17, s[16:19], s26 offen offset:1024 lds
v_accvgpr_read_b32 v39, a243
v_mfma_f32_16x16x32_bf16 v[192:195], v[64:67], v[56:59], v[192:195]
ds_read_b128 v[52:55], v25 offset:2048
v_mul_f32 v38, s47, v38
v_mfma_f32_16x16x32_bf16 v[196:199], v[68:71], v[56:59], v[196:199]
v_mul_f32 v39, s47, v39
v_cvt_pk_bf16_f32 v41, v38, v39
v_mfma_f32_16x16x32_bf16 v[200:203], v[72:75], v[56:59], v[200:203]
ds_read_b64_tr_b16 v[120:121], v32 offset:24576
ds_read_b64_tr_b16 v[122:123], v32 offset:25600
v_mfma_f32_16x16x32_bf16 v[204:207], v[76:79], v[56:59], v[204:207]
buffer_load_dwordx4 v18, s[16:19], s26 offen offset:2048 lds
v_accvgpr_read_b32 v36, a244
v_mfma_f32_16x16x32_bf16 v[208:211], v[80:83], v[56:59], v[208:211]
ds_read_b64_tr_b16 v[124:125], v33 offset:24576
ds_read_b64_tr_b16 v[126:127], v33 offset:25600
v_mfma_f32_16x16x32_bf16 v[212:215], v[84:87], v[56:59], v[212:215]
v_accvgpr_read_b32 v37, a245
v_mul_f32 v36, s47, v36
v_mfma_f32_16x16x32_bf16 v[216:219], v[88:91], v[56:59], v[216:219]
s_waitcnt lgkmcnt(15)
v_mul_f32 v37, s47, v37
v_cvt_pk_bf16_f32 v42, v36, v37
v_mfma_f32_16x16x32_bf16 v[220:223], v[92:95], v[56:59], v[220:223]
v_xor_b32 v26, 0x10000, v26
v_accvgpr_read_b32 v38, a246
v_mfma_f32_16x16x32_bf16 v[224:227], v[64:67], v[60:63], v[224:227]
ds_read_b128 v[56:59], v25 offset:4096
v_xor_b32 v27, 0x10000, v27
v_mfma_f32_16x16x32_bf16 v[228:231], v[68:71], v[60:63], v[228:231]
buffer_load_dwordx4 v19, s[16:19], s26 offen offset:3072 lds
v_xor_b32 v28, 0x10000, v28
v_mfma_f32_16x16x32_bf16 v[232:235], v[72:75], v[60:63], v[232:235]
v_xor_b32 v29, 0x10000, v29
v_accvgpr_read_b32 v39, a247
v_mfma_f32_16x16x32_bf16 v[236:239], v[76:79], v[60:63], v[236:239]
s_add_u32 s26, s26, 524288
v_xor_b32 v30, 0x10000, v30
v_mul_f32 v38, s47, v38
v_mfma_f32_16x16x32_bf16 v[240:243], v[80:83], v[60:63], v[240:243]
v_xor_b32 v31, 0x10000, v31
v_mul_f32 v39, s47, v39
v_mfma_f32_16x16x32_bf16 v[244:247], v[84:87], v[60:63], v[244:247]
v_xor_b32 v32, 0x10000, v32
v_cvt_pk_bf16_f32 v43, v38, v39
v_mfma_f32_16x16x32_bf16 v[248:251], v[88:91], v[60:63], v[248:251]
v_xor_b32 v33, 0x10000, v33
v_accvgpr_write_b32 a232, v40
v_mfma_f32_16x16x32_bf16 v[252:255], v[92:95], v[60:63], v[252:255]
s_waitcnt vmcnt(20) lgkmcnt(0)
s_barrier
v_accvgpr_write_b32 a233, v41
v_mfma_f32_16x16x32_bf16 v[128:131], v[96:99], v[48:51], v[128:131]
ds_read_b128 v[60:63], v25 offset:6144
v_accvgpr_write_b32 a234, v42
v_mfma_f32_16x16x32_bf16 v[132:135], v[100:103], v[48:51], v[132:135]
v_add_u32 v25, 65536, v25
v_accvgpr_write_b32 a235, v43
v_mfma_f32_16x16x32_bf16 v[136:139], v[104:107], v[48:51], v[136:139]
s_add_u32 m0, s92, 32768
ds_read_b64_tr_b16 v[64:65], v26 offset:0
ds_read_b64_tr_b16 v[66:67], v26 offset:1024
v_mfma_f32_16x16x32_bf16 v[140:143], v[108:111], v[48:51], v[140:143]
buffer_load_dwordx4 v20, s[12:15], s24 offen offset:0 lds
v_accvgpr_read_b32 v36, a96
v_mfma_f32_16x16x32_bf16 v[144:147], v[112:115], v[48:51], v[144:147]
ds_read_b64_tr_b16 v[68:69], v27 offset:0
ds_read_b64_tr_b16 v[70:71], v27 offset:1024
v_mfma_f32_16x16x32_bf16 v[148:151], v[116:119], v[48:51], v[148:151]
v_accvgpr_read_b32 v37, a97
v_mul_f32 v36, s47, v36
v_mfma_f32_16x16x32_bf16 v[152:155], v[120:123], v[48:51], v[152:155]
ds_read_b64_tr_b16 v[72:73], v28 offset:0
ds_read_b64_tr_b16 v[74:75], v28 offset:1024
v_mfma_f32_16x16x32_bf16 v[156:159], v[124:127], v[48:51], v[156:159]
buffer_load_dwordx4 v21, s[12:15], s24 offen offset:1024 lds
v_mul_f32 v37, s47, v37
v_mfma_f32_16x16x32_bf16 v[160:163], v[96:99], v[52:55], v[160:163]
ds_read_b128 v[48:51], v24 offset:0
v_cvt_pk_bf16_f32 v44, v36, v37
v_mfma_f32_16x16x32_bf16 v[164:167], v[100:103], v[52:55], v[164:167]
v_accvgpr_read_b32 v38, a98
v_accvgpr_read_b32 v39, a99
v_mfma_f32_16x16x32_bf16 v[168:171], v[104:107], v[52:55], v[168:171]
ds_read_b64_tr_b16 v[76:77], v29 offset:0
ds_read_b64_tr_b16 v[78:79], v29 offset:1024
v_mfma_f32_16x16x32_bf16 v[172:175], v[108:111], v[52:55], v[172:175]
buffer_load_dwordx4 v22, s[12:15], s24 offen offset:2048 lds
v_mul_f32 v38, s47, v38
v_mfma_f32_16x16x32_bf16 v[176:179], v[112:115], v[52:55], v[176:179]
ds_read_b64_tr_b16 v[80:81], v30 offset:0
ds_read_b64_tr_b16 v[82:83], v30 offset:1024
v_mfma_f32_16x16x32_bf16 v[180:183], v[116:119], v[52:55], v[180:183]
v_mul_f32 v39, s47, v39
v_cvt_pk_bf16_f32 v45, v38, v39
v_mfma_f32_16x16x32_bf16 v[184:187], v[120:123], v[52:55], v[184:187]
ds_read_b64_tr_b16 v[84:85], v31 offset:0
ds_read_b64_tr_b16 v[86:87], v31 offset:1024
v_mfma_f32_16x16x32_bf16 v[188:191], v[124:127], v[52:55], v[188:191]
buffer_load_dwordx4 v23, s[12:15], s24 offen offset:3072 lds
v_accvgpr_read_b32 v36, a100
v_mfma_f32_16x16x32_bf16 v[192:195], v[96:99], v[56:59], v[192:195]
ds_read_b128 v[52:55], v24 offset:2048
v_accvgpr_read_b32 v37, a101
v_mfma_f32_16x16x32_bf16 v[196:199], v[100:103], v[56:59], v[196:199]
v_mul_f32 v36, s47, v36
v_mul_f32 v37, s47, v37
v_mfma_f32_16x16x32_bf16 v[200:203], v[104:107], v[56:59], v[200:203]
s_add_u32 m0, s92, 36864
ds_read_b64_tr_b16 v[88:89], v32 offset:0
ds_read_b64_tr_b16 v[90:91], v32 offset:1024
v_mfma_f32_16x16x32_bf16 v[204:207], v[108:111], v[56:59], v[204:207]
buffer_load_dwordx4 v20, s[12:15], s25 offen offset:0 lds
v_cvt_pk_bf16_f32 v46, v36, v37
v_mfma_f32_16x16x32_bf16 v[208:211], v[112:115], v[56:59], v[208:211]
ds_read_b64_tr_b16 v[92:93], v33 offset:0
ds_read_b64_tr_b16 v[94:95], v33 offset:1024
v_mfma_f32_16x16x32_bf16 v[212:215], v[116:119], v[56:59], v[212:215]
v_accvgpr_read_b32 v38, a102
v_accvgpr_read_b32 v39, a103
v_mfma_f32_16x16x32_bf16 v[216:219], v[120:123], v[56:59], v[216:219]
s_waitcnt lgkmcnt(15)
v_mul_f32 v38, s47, v38
v_mul_f32 v39, s47, v39
v_mfma_f32_16x16x32_bf16 v[220:223], v[124:127], v[56:59], v[220:223]
v_cvt_pk_bf16_f32 v47, v38, v39
v_accvgpr_read_b32 v36, a248
v_mfma_f32_16x16x32_bf16 v[224:227], v[96:99], v[60:63], v[224:227]
s_xor_b32 s48, s95, 135168
ds_read_b128 v[56:59], v24 offset:4096
v_mfma_f32_16x16x32_bf16 v[228:231], v[100:103], v[60:63], v[228:231]
buffer_load_dwordx4 v21, s[12:15], s25 offen offset:1024 lds
v_accvgpr_read_b32 v37, a249
v_mfma_f32_16x16x32_bf16 v[232:235], v[104:107], v[60:63], v[232:235]
buffer_store_dwordx4 v[44:47], v7, s[20:23], s48 offen sc1
v_mul_f32 v36, s47, v36
v_mfma_f32_16x16x32_bf16 v[236:239], v[108:111], v[60:63], v[236:239]
v_mul_f32 v37, s47, v37
v_cvt_pk_bf16_f32 v40, v36, v37
v_mfma_f32_16x16x32_bf16 v[240:243], v[112:115], v[60:63], v[240:243]
v_accvgpr_read_b32 v38, a250
v_accvgpr_read_b32 v39, a251
v_mfma_f32_16x16x32_bf16 v[244:247], v[116:119], v[60:63], v[244:247]
v_mul_f32 v38, s47, v38
v_mul_f32 v39, s47, v39
v_mfma_f32_16x16x32_bf16 v[248:251], v[120:123], v[60:63], v[248:251]
v_cvt_pk_bf16_f32 v41, v38, v39
v_accvgpr_read_b32 v36, a252
v_mfma_f32_16x16x32_bf16 v[252:255], v[124:127], v[60:63], v[252:255]
s_waitcnt lgkmcnt(0)
v_accvgpr_read_b32 v37, a253
v_mul_f32 v36, s47, v36
v_mfma_f32_16x16x32_bf16 v[128:131], v[64:67], v[48:51], v[128:131]
ds_read_b128 v[60:63], v24 offset:6144
v_mul_f32 v37, s47, v37
v_mfma_f32_16x16x32_bf16 v[132:135], v[68:71], v[48:51], v[132:135]
v_add_u32 v24, 32768, v24
v_cvt_pk_bf16_f32 v42, v36, v37
v_mfma_f32_16x16x32_bf16 v[136:139], v[72:75], v[48:51], v[136:139]
ds_read_b64_tr_b16 v[96:97], v26 offset:8192
ds_read_b64_tr_b16 v[98:99], v26 offset:9216
v_mfma_f32_16x16x32_bf16 v[140:143], v[76:79], v[48:51], v[140:143]
buffer_load_dwordx4 v22, s[12:15], s25 offen offset:2048 lds
v_accvgpr_read_b32 v38, a254
v_mfma_f32_16x16x32_bf16 v[144:147], v[80:83], v[48:51], v[144:147]
ds_read_b64_tr_b16 v[100:101], v27 offset:8192
ds_read_b64_tr_b16 v[102:103], v27 offset:9216
v_mfma_f32_16x16x32_bf16 v[148:151], v[84:87], v[48:51], v[148:151]
v_accvgpr_read_b32 v39, a255
v_mul_f32 v38, s47, v38
v_mfma_f32_16x16x32_bf16 v[152:155], v[88:91], v[48:51], v[152:155]
ds_read_b64_tr_b16 v[104:105], v28 offset:8192
ds_read_b64_tr_b16 v[106:107], v28 offset:9216
v_mfma_f32_16x16x32_bf16 v[156:159], v[92:95], v[48:51], v[156:159]
buffer_load_dwordx4 v23, s[12:15], s25 offen offset:3072 lds
v_mul_f32 v39, s47, v39
v_mfma_f32_16x16x32_bf16 v[160:163], v[64:67], v[52:55], v[160:163]
ds_read_b128 v[48:51], v25 offset:0
v_cvt_pk_bf16_f32 v43, v38, v39
v_mfma_f32_16x16x32_bf16 v[164:167], v[68:71], v[52:55], v[164:167]
s_add_u32 s24, s24, 128
s_add_u32 s25, s25, 128
v_accvgpr_write_b32 a236, v40
v_mfma_f32_16x16x32_bf16 v[168:171], v[72:75], v[52:55], v[168:171]
s_add_u32 m0, s91, 81920
ds_read_b64_tr_b16 v[108:109], v29 offset:8192
ds_read_b64_tr_b16 v[110:111], v29 offset:9216
v_mfma_f32_16x16x32_bf16 v[172:175], v[76:79], v[52:55], v[172:175]
buffer_load_dwordx4 v16, s[16:19], s26 offen offset:0 lds
v_accvgpr_write_b32 a237, v41
v_mfma_f32_16x16x32_bf16 v[176:179], v[80:83], v[52:55], v[176:179]
ds_read_b64_tr_b16 v[112:113], v30 offset:8192
ds_read_b64_tr_b16 v[114:115], v30 offset:9216
v_mfma_f32_16x16x32_bf16 v[180:183], v[84:87], v[52:55], v[180:183]
v_accvgpr_write_b32 a238, v42
v_accvgpr_write_b32 a239, v43
v_mfma_f32_16x16x32_bf16 v[184:187], v[88:91], v[52:55], v[184:187]
ds_read_b64_tr_b16 v[116:117], v31 offset:8192
ds_read_b64_tr_b16 v[118:119], v31 offset:9216
v_mfma_f32_16x16x32_bf16 v[188:191], v[92:95], v[52:55], v[188:191]
buffer_load_dwordx4 v17, s[16:19], s26 offen offset:1024 lds
v_accvgpr_read_b32 v36, a104
v_mfma_f32_16x16x32_bf16 v[192:195], v[64:67], v[56:59], v[192:195]
ds_read_b128 v[52:55], v25 offset:2048
v_accvgpr_read_b32 v37, a105
v_mfma_f32_16x16x32_bf16 v[196:199], v[68:71], v[56:59], v[196:199]
v_mul_f32 v36, s47, v36
v_mul_f32 v37, s47, v37
v_mfma_f32_16x16x32_bf16 v[200:203], v[72:75], v[56:59], v[200:203]
ds_read_b64_tr_b16 v[120:121], v32 offset:8192
ds_read_b64_tr_b16 v[122:123], v32 offset:9216
v_mfma_f32_16x16x32_bf16 v[204:207], v[76:79], v[56:59], v[204:207]
buffer_load_dwordx4 v18, s[16:19], s26 offen offset:2048 lds
v_cvt_pk_bf16_f32 v44, v36, v37
v_mfma_f32_16x16x32_bf16 v[208:211], v[80:83], v[56:59], v[208:211]
ds_read_b64_tr_b16 v[124:125], v33 offset:8192
ds_read_b64_tr_b16 v[126:127], v33 offset:9216
v_mfma_f32_16x16x32_bf16 v[212:215], v[84:87], v[56:59], v[212:215]
v_accvgpr_read_b32 v38, a106
v_accvgpr_read_b32 v39, a107
v_mfma_f32_16x16x32_bf16 v[216:219], v[88:91], v[56:59], v[216:219]
s_waitcnt lgkmcnt(15)
v_mul_f32 v38, s47, v38
v_mul_f32 v39, s47, v39
v_mfma_f32_16x16x32_bf16 v[220:223], v[92:95], v[56:59], v[220:223]
v_cvt_pk_bf16_f32 v45, v38, v39
v_accvgpr_read_b32 v36, a108
v_mfma_f32_16x16x32_bf16 v[224:227], v[64:67], v[60:63], v[224:227]
ds_read_b128 v[56:59], v25 offset:4096
v_accvgpr_read_b32 v37, a109
v_mfma_f32_16x16x32_bf16 v[228:231], v[68:71], v[60:63], v[228:231]
buffer_load_dwordx4 v19, s[16:19], s26 offen offset:3072 lds
v_mul_f32 v36, s47, v36
v_mfma_f32_16x16x32_bf16 v[232:235], v[72:75], v[60:63], v[232:235]
v_mul_f32 v37, s47, v37
v_cvt_pk_bf16_f32 v46, v36, v37
v_mfma_f32_16x16x32_bf16 v[236:239], v[76:79], v[60:63], v[236:239]
s_add_u32 s26, s26, 524288
v_accvgpr_read_b32 v38, a110
v_accvgpr_read_b32 v39, a111
v_mfma_f32_16x16x32_bf16 v[240:243], v[80:83], v[60:63], v[240:243]
v_mul_f32 v38, s47, v38
v_mul_f32 v39, s47, v39
v_mfma_f32_16x16x32_bf16 v[244:247], v[84:87], v[60:63], v[244:247]
v_cvt_pk_bf16_f32 v47, v38, v39
v_mfma_f32_16x16x32_bf16 v[248:251], v[88:91], v[60:63], v[248:251]
s_xor_b32 s48, s95, 136192
buffer_store_dwordx4 v[44:47], v7, s[20:23], s48 offen sc1
v_mfma_f32_16x16x32_bf16 v[252:255], v[92:95], v[60:63], v[252:255]
s_waitcnt vmcnt(19) lgkmcnt(0)
s_barrier
v_mfma_f32_16x16x32_bf16 v[128:131], v[96:99], v[48:51], v[128:131]
ds_read_b128 v[60:63], v25 offset:6144
v_mfma_f32_16x16x32_bf16 v[132:135], v[100:103], v[48:51], v[132:135]
v_add_u32 v25, 32768, v25
v_mfma_f32_16x16x32_bf16 v[136:139], v[104:107], v[48:51], v[136:139]
s_add_u32 m0, s92, 98304
ds_read_b64_tr_b16 v[64:65], v26 offset:16384
ds_read_b64_tr_b16 v[66:67], v26 offset:17408
v_mfma_f32_16x16x32_bf16 v[140:143], v[108:111], v[48:51], v[140:143]
buffer_load_dwordx4 v20, s[12:15], s24 offen offset:0 lds
v_mfma_f32_16x16x32_bf16 v[144:147], v[112:115], v[48:51], v[144:147]
ds_read_b64_tr_b16 v[68:69], v27 offset:16384
ds_read_b64_tr_b16 v[70:71], v27 offset:17408
v_mfma_f32_16x16x32_bf16 v[148:151], v[116:119], v[48:51], v[148:151]
v_mfma_f32_16x16x32_bf16 v[152:155], v[120:123], v[48:51], v[152:155]
ds_read_b64_tr_b16 v[72:73], v28 offset:16384
ds_read_b64_tr_b16 v[74:75], v28 offset:17408
v_mfma_f32_16x16x32_bf16 v[156:159], v[124:127], v[48:51], v[156:159]
buffer_load_dwordx4 v21, s[12:15], s24 offen offset:1024 lds
v_mfma_f32_16x16x32_bf16 v[160:163], v[96:99], v[52:55], v[160:163]
ds_read_b128 v[48:51], v24 offset:0
v_mfma_f32_16x16x32_bf16 v[164:167], v[100:103], v[52:55], v[164:167]
v_mfma_f32_16x16x32_bf16 v[168:171], v[104:107], v[52:55], v[168:171]
ds_read_b64_tr_b16 v[76:77], v29 offset:16384
ds_read_b64_tr_b16 v[78:79], v29 offset:17408
v_mfma_f32_16x16x32_bf16 v[172:175], v[108:111], v[52:55], v[172:175]
buffer_load_dwordx4 v22, s[12:15], s24 offen offset:2048 lds
v_mfma_f32_16x16x32_bf16 v[176:179], v[112:115], v[52:55], v[176:179]
ds_read_b64_tr_b16 v[80:81], v30 offset:16384
ds_read_b64_tr_b16 v[82:83], v30 offset:17408
v_mfma_f32_16x16x32_bf16 v[180:183], v[116:119], v[52:55], v[180:183]
v_mfma_f32_16x16x32_bf16 v[184:187], v[120:123], v[52:55], v[184:187]
ds_read_b64_tr_b16 v[84:85], v31 offset:16384
ds_read_b64_tr_b16 v[86:87], v31 offset:17408
v_mfma_f32_16x16x32_bf16 v[188:191], v[124:127], v[52:55], v[188:191]
buffer_load_dwordx4 v23, s[12:15], s24 offen offset:3072 lds
v_mfma_f32_16x16x32_bf16 v[192:195], v[96:99], v[56:59], v[192:195]
ds_read_b128 v[52:55], v24 offset:2048
v_mfma_f32_16x16x32_bf16 v[196:199], v[100:103], v[56:59], v[196:199]
v_mfma_f32_16x16x32_bf16 v[200:203], v[104:107], v[56:59], v[200:203]
s_add_u32 m0, s92, 102400
ds_read_b64_tr_b16 v[88:89], v32 offset:16384
ds_read_b64_tr_b16 v[90:91], v32 offset:17408
v_mfma_f32_16x16x32_bf16 v[204:207], v[108:111], v[56:59], v[204:207]
buffer_load_dwordx4 v20, s[12:15], s25 offen offset:0 lds
v_mfma_f32_16x16x32_bf16 v[208:211], v[112:115], v[56:59], v[208:211]
ds_read_b64_tr_b16 v[92:93], v33 offset:16384
ds_read_b64_tr_b16 v[94:95], v33 offset:17408
v_mfma_f32_16x16x32_bf16 v[212:215], v[116:119], v[56:59], v[212:215]
v_mfma_f32_16x16x32_bf16 v[216:219], v[120:123], v[56:59], v[216:219]
s_waitcnt lgkmcnt(15)
v_mfma_f32_16x16x32_bf16 v[220:223], v[124:127], v[56:59], v[220:223]
v_mfma_f32_16x16x32_bf16 v[224:227], v[96:99], v[60:63], v[224:227]
ds_read_b128 v[56:59], v24 offset:4096
v_mfma_f32_16x16x32_bf16 v[228:231], v[100:103], v[60:63], v[228:231]
buffer_load_dwordx4 v21, s[12:15], s25 offen offset:1024 lds
v_mfma_f32_16x16x32_bf16 v[232:235], v[104:107], v[60:63], v[232:235]
v_mfma_f32_16x16x32_bf16 v[236:239], v[108:111], v[60:63], v[236:239]
v_mfma_f32_16x16x32_bf16 v[240:243], v[112:115], v[60:63], v[240:243]
v_mfma_f32_16x16x32_bf16 v[244:247], v[116:119], v[60:63], v[244:247]
v_accvgpr_read_b32 v36, a112
v_accvgpr_read_b32 v37, a113
v_mfma_f32_16x16x32_bf16 v[248:251], v[120:123], v[60:63], v[248:251]
v_mul_f32 v36, s47, v36
v_mul_f32 v37, s47, v37
v_mfma_f32_16x16x32_bf16 v[252:255], v[124:127], v[60:63], v[252:255]
s_waitcnt lgkmcnt(0)
v_cvt_pk_bf16_f32 v40, v36, v37
v_accvgpr_read_b32 v38, a114
v_mfma_f32_16x16x32_bf16 v[128:131], v[64:67], v[48:51], v[128:131]
ds_read_b128 v[60:63], v24 offset:6144
v_accvgpr_read_b32 v39, a115
v_mfma_f32_16x16x32_bf16 v[132:135], v[68:71], v[48:51], v[132:135]
v_add_u32 v24, 4294868992, v24
v_mul_f32 v38, s47, v38
v_mfma_f32_16x16x32_bf16 v[136:139], v[72:75], v[48:51], v[136:139]
ds_read_b64_tr_b16 v[96:97], v26 offset:24576
ds_read_b64_tr_b16 v[98:99], v26 offset:25600
v_mfma_f32_16x16x32_bf16 v[140:143], v[76:79], v[48:51], v[140:143]
buffer_load_dwordx4 v22, s[12:15], s25 offen offset:2048 lds
v_mul_f32 v39, s47, v39
v_mfma_f32_16x16x32_bf16 v[144:147], v[80:83], v[48:51], v[144:147]
ds_read_b64_tr_b16 v[100:101], v27 offset:24576
ds_read_b64_tr_b16 v[102:103], v27 offset:25600
v_mfma_f32_16x16x32_bf16 v[148:151], v[84:87], v[48:51], v[148:151]
v_cvt_pk_bf16_f32 v41, v38, v39
v_accvgpr_read_b32 v36, a116
v_mfma_f32_16x16x32_bf16 v[152:155], v[88:91], v[48:51], v[152:155]
ds_read_b64_tr_b16 v[104:105], v28 offset:24576
ds_read_b64_tr_b16 v[106:107], v28 offset:25600
v_mfma_f32_16x16x32_bf16 v[156:159], v[92:95], v[48:51], v[156:159]
buffer_load_dwordx4 v23, s[12:15], s25 offen offset:3072 lds
v_accvgpr_read_b32 v37, a117
v_mfma_f32_16x16x32_bf16 v[160:163], v[64:67], v[52:55], v[160:163]
ds_read_b128 v[48:51], v25 offset:0
v_mul_f32 v36, s47, v36
v_mfma_f32_16x16x32_bf16 v[164:167], v[68:71], v[52:55], v[164:167]
s_add_u32 s24, s24, 128
s_add_u32 s25, s25, 128
v_mul_f32 v37, s47, v37
v_mfma_f32_16x16x32_bf16 v[168:171], v[72:75], v[52:55], v[168:171]
s_add_u32 m0, s91, 0
ds_read_b64_tr_b16 v[108:109], v29 offset:24576
ds_read_b64_tr_b16 v[110:111], v29 offset:25600
v_mfma_f32_16x16x32_bf16 v[172:175], v[76:79], v[52:55], v[172:175]
buffer_load_dwordx4 v16, s[16:19], s26 offen offset:0 lds
v_cvt_pk_bf16_f32 v42, v36, v37
v_mfma_f32_16x16x32_bf16 v[176:179], v[80:83], v[52:55], v[176:179]
ds_read_b64_tr_b16 v[112:113], v30 offset:24576
ds_read_b64_tr_b16 v[114:115], v30 offset:25600
v_mfma_f32_16x16x32_bf16 v[180:183], v[84:87], v[52:55], v[180:183]
v_accvgpr_read_b32 v38, a118
v_accvgpr_read_b32 v39, a119
v_mfma_f32_16x16x32_bf16 v[184:187], v[88:91], v[52:55], v[184:187]
ds_read_b64_tr_b16 v[116:117], v31 offset:24576
ds_read_b64_tr_b16 v[118:119], v31 offset:25600
.Lpad8:
s_cbranch_execz .Ltch2
v_mfma_f32_16x16x32_bf16 v[188:191], v[92:95], v[52:55], v[188:191]
buffer_load_dwordx4 v17, s[16:19], s26 offen offset:1024 lds
v_mul_f32 v38, s47, v38
v_mfma_f32_16x16x32_bf16 v[192:195], v[64:67], v[56:59], v[192:195]
ds_read_b128 v[52:55], v25 offset:2048
v_mul_f32 v39, s47, v39
v_mfma_f32_16x16x32_bf16 v[196:199], v[68:71], v[56:59], v[196:199]
v_cvt_pk_bf16_f32 v43, v38, v39
v_mfma_f32_16x16x32_bf16 v[200:203], v[72:75], v[56:59], v[200:203]
ds_read_b64_tr_b16 v[120:121], v32 offset:24576
ds_read_b64_tr_b16 v[122:123], v32 offset:25600
v_mfma_f32_16x16x32_bf16 v[204:207], v[76:79], v[56:59], v[204:207]
s_xor_b32 s48, s95, 137216
buffer_load_dwordx4 v18, s[16:19], s26 offen offset:2048 lds
v_mfma_f32_16x16x32_bf16 v[208:211], v[80:83], v[56:59], v[208:211]
ds_read_b64_tr_b16 v[124:125], v33 offset:24576
ds_read_b64_tr_b16 v[126:127], v33 offset:25600
v_mfma_f32_16x16x32_bf16 v[212:215], v[84:87], v[56:59], v[212:215]
buffer_store_dwordx4 v[40:43], v7, s[20:23], s48 offen sc1
v_mfma_f32_16x16x32_bf16 v[216:219], v[88:91], v[56:59], v[216:219]
s_waitcnt lgkmcnt(15)
v_mfma_f32_16x16x32_bf16 v[220:223], v[92:95], v[56:59], v[220:223]
v_xor_b32 v26, 0x10000, v26
v_mfma_f32_16x16x32_bf16 v[224:227], v[64:67], v[60:63], v[224:227]
ds_read_b128 v[56:59], v25 offset:4096
v_xor_b32 v27, 0x10000, v27
v_mfma_f32_16x16x32_bf16 v[228:231], v[68:71], v[60:63], v[228:231]
buffer_load_dwordx4 v19, s[16:19], s26 offen offset:3072 lds
v_xor_b32 v28, 0x10000, v28
v_mfma_f32_16x16x32_bf16 v[232:235], v[72:75], v[60:63], v[232:235]
v_xor_b32 v29, 0x10000, v29
v_mfma_f32_16x16x32_bf16 v[236:239], v[76:79], v[60:63], v[236:239]
s_add_u32 s26, s26, 524288
v_xor_b32 v30, 0x10000, v30
v_mfma_f32_16x16x32_bf16 v[240:243], v[80:83], v[60:63], v[240:243]
v_xor_b32 v31, 0x10000, v31
v_mfma_f32_16x16x32_bf16 v[244:247], v[84:87], v[60:63], v[244:247]
v_xor_b32 v32, 0x10000, v32
v_mfma_f32_16x16x32_bf16 v[248:251], v[88:91], v[60:63], v[248:251]
v_xor_b32 v33, 0x10000, v33
v_mfma_f32_16x16x32_bf16 v[252:255], v[92:95], v[60:63], v[252:255]
s_waitcnt vmcnt(18) lgkmcnt(0)
s_barrier
v_mfma_f32_16x16x32_bf16 v[128:131], v[96:99], v[48:51], v[128:131]
ds_read_b128 v[60:63], v25 offset:6144
v_mfma_f32_16x16x32_bf16 v[132:135], v[100:103], v[48:51], v[132:135]
v_add_u32 v25, 4294868992, v25
v_mfma_f32_16x16x32_bf16 v[136:139], v[104:107], v[48:51], v[136:139]
s_add_u32 m0, s92, 131072
ds_read_b64_tr_b16 v[64:65], v26 offset:0
ds_read_b64_tr_b16 v[66:67], v26 offset:1024
v_mfma_f32_16x16x32_bf16 v[140:143], v[108:111], v[48:51], v[140:143]
buffer_load_dwordx4 v20, s[12:15], s24 offen offset:0 lds
v_mfma_f32_16x16x32_bf16 v[144:147], v[112:115], v[48:51], v[144:147]
ds_read_b64_tr_b16 v[68:69], v27 offset:0
ds_read_b64_tr_b16 v[70:71], v27 offset:1024
v_mfma_f32_16x16x32_bf16 v[148:151], v[116:119], v[48:51], v[148:151]
v_mfma_f32_16x16x32_bf16 v[152:155], v[120:123], v[48:51], v[152:155]
ds_read_b64_tr_b16 v[72:73], v28 offset:0
ds_read_b64_tr_b16 v[74:75], v28 offset:1024
v_mfma_f32_16x16x32_bf16 v[156:159], v[124:127], v[48:51], v[156:159]
buffer_load_dwordx4 v21, s[12:15], s24 offen offset:1024 lds
v_mfma_f32_16x16x32_bf16 v[160:163], v[96:99], v[52:55], v[160:163]
ds_read_b128 v[48:51], v24 offset:0
v_mfma_f32_16x16x32_bf16 v[164:167], v[100:103], v[52:55], v[164:167]
v_mfma_f32_16x16x32_bf16 v[168:171], v[104:107], v[52:55], v[168:171]
ds_read_b64_tr_b16 v[76:77], v29 offset:0
ds_read_b64_tr_b16 v[78:79], v29 offset:1024
v_mfma_f32_16x16x32_bf16 v[172:175], v[108:111], v[52:55], v[172:175]
buffer_load_dwordx4 v22, s[12:15], s24 offen offset:2048 lds
v_mfma_f32_16x16x32_bf16 v[176:179], v[112:115], v[52:55], v[176:179]
ds_read_b64_tr_b16 v[80:81], v30 offset:0
ds_read_b64_tr_b16 v[82:83], v30 offset:1024
v_mfma_f32_16x16x32_bf16 v[180:183], v[116:119], v[52:55], v[180:183]
v_mfma_f32_16x16x32_bf16 v[184:187], v[120:123], v[52:55], v[184:187]
ds_read_b64_tr_b16 v[84:85], v31 offset:0
ds_read_b64_tr_b16 v[86:87], v31 offset:1024
v_mfma_f32_16x16x32_bf16 v[188:191], v[124:127], v[52:55], v[188:191]
buffer_load_dwordx4 v23, s[12:15], s24 offen offset:3072 lds
v_accvgpr_read_b32 v36, a120
v_mfma_f32_16x16x32_bf16 v[192:195], v[96:99], v[56:59], v[192:195]
ds_read_b128 v[52:55], v24 offset:2048
v_accvgpr_read_b32 v37, a121
v_mfma_f32_16x16x32_bf16 v[196:199], v[100:103], v[56:59], v[196:199]
v_mul_f32 v36, s47, v36
v_mul_f32 v37, s47, v37
v_mfma_f32_16x16x32_bf16 v[200:203], v[104:107], v[56:59], v[200:203]
s_add_u32 m0, s92, 135168
ds_read_b64_tr_b16 v[88:89], v32 offset:0
ds_read_b64_tr_b16 v[90:91], v32 offset:1024
v_mfma_f32_16x16x32_bf16 v[204:207], v[108:111], v[56:59], v[204:207]
buffer_load_dwordx4 v20, s[12:15], s25 offen offset:0 lds
v_cvt_pk_bf16_f32 v44, v36, v37
v_mfma_f32_16x16x32_bf16 v[208:211], v[112:115], v[56:59], v[208:211]
ds_read_b64_tr_b16 v[92:93], v33 offset:0
ds_read_b64_tr_b16 v[94:95], v33 offset:1024
v_mfma_f32_16x16x32_bf16 v[212:215], v[116:119], v[56:59], v[212:215]
v_accvgpr_read_b32 v38, a122
v_accvgpr_read_b32 v39, a123
v_mfma_f32_16x16x32_bf16 v[216:219], v[120:123], v[56:59], v[216:219]
s_waitcnt lgkmcnt(15)
v_mul_f32 v38, s47, v38
v_mul_f32 v39, s47, v39
v_mfma_f32_16x16x32_bf16 v[220:223], v[124:127], v[56:59], v[220:223]
v_cvt_pk_bf16_f32 v45, v38, v39
v_accvgpr_read_b32 v36, a124
v_mfma_f32_16x16x32_bf16 v[224:227], v[96:99], v[60:63], v[224:227]
ds_read_b128 v[56:59], v24 offset:4096
v_accvgpr_read_b32 v37, a125
v_mfma_f32_16x16x32_bf16 v[228:231], v[100:103], v[60:63], v[228:231]
buffer_load_dwordx4 v21, s[12:15], s25 offen offset:1024 lds
v_mul_f32 v36, s47, v36
v_mfma_f32_16x16x32_bf16 v[232:235], v[104:107], v[60:63], v[232:235]
v_mul_f32 v37, s47, v37
v_cvt_pk_bf16_f32 v46, v36, v37
v_mfma_f32_16x16x32_bf16 v[236:239], v[108:111], v[60:63], v[236:239]
v_accvgpr_read_b32 v38, a126
v_accvgpr_read_b32 v39, a127
v_mfma_f32_16x16x32_bf16 v[240:243], v[112:115], v[60:63], v[240:243]
v_mul_f32 v38, s47, v38
v_mul_f32 v39, s47, v39
v_mfma_f32_16x16x32_bf16 v[244:247], v[116:119], v[60:63], v[244:247]
v_cvt_pk_bf16_f32 v47, v38, v39
v_mfma_f32_16x16x32_bf16 v[248:251], v[120:123], v[60:63], v[248:251]
s_xor_b32 s48, s95, 138240
buffer_store_dwordx4 v[44:47], v7, s[20:23], s48 offen sc1
v_mfma_f32_16x16x32_bf16 v[252:255], v[124:127], v[60:63], v[252:255]
s_waitcnt lgkmcnt(0)
v_mfma_f32_16x16x32_bf16 v[128:131], v[64:67], v[48:51], v[128:131]
ds_read_b128 v[60:63], v24 offset:6144
v_mfma_f32_16x16x32_bf16 v[132:135], v[68:71], v[48:51], v[132:135]
v_add_u32 v24, 65536, v24
v_mfma_f32_16x16x32_bf16 v[136:139], v[72:75], v[48:51], v[136:139]
ds_read_b64_tr_b16 v[96:97], v26 offset:8192
ds_read_b64_tr_b16 v[98:99], v26 offset:9216
v_mfma_f32_16x16x32_bf16 v[140:143], v[76:79], v[48:51], v[140:143]
buffer_load_dwordx4 v22, s[12:15], s25 offen offset:2048 lds
v_mfma_f32_16x16x32_bf16 v[144:147], v[80:83], v[48:51], v[144:147]
ds_read_b64_tr_b16 v[100:101], v27 offset:8192
ds_read_b64_tr_b16 v[102:103], v27 offset:9216
v_mfma_f32_16x16x32_bf16 v[148:151], v[84:87], v[48:51], v[148:151]
v_mfma_f32_16x16x32_bf16 v[152:155], v[88:91], v[48:51], v[152:155]
ds_read_b64_tr_b16 v[104:105], v28 offset:8192
ds_read_b64_tr_b16 v[106:107], v28 offset:9216
v_mfma_f32_16x16x32_bf16 v[156:159], v[92:95], v[48:51], v[156:159]
buffer_load_dwordx4 v23, s[12:15], s25 offen offset:3072 lds
v_mfma_f32_16x16x32_bf16 v[160:163], v[64:67], v[52:55], v[160:163]
ds_read_b128 v[48:51], v25 offset:0
v_mfma_f32_16x16x32_bf16 v[164:167], v[68:71], v[52:55], v[164:167]
s_add_u32 s24, s24, 128
s_add_u32 s25, s25, 128
v_mfma_f32_16x16x32_bf16 v[168:171], v[72:75], v[52:55], v[168:171]
s_add_u32 m0, s91, 16384
ds_read_b64_tr_b16 v[108:109], v29 offset:8192
ds_read_b64_tr_b16 v[110:111], v29 offset:9216
v_mfma_f32_16x16x32_bf16 v[172:175], v[76:79], v[52:55], v[172:175]
buffer_load_dwordx4 v16, s[16:19], s26 offen offset:0 lds
v_mfma_f32_16x16x32_bf16 v[176:179], v[80:83], v[52:55], v[176:179]
ds_read_b64_tr_b16 v[112:113], v30 offset:8192
ds_read_b64_tr_b16 v[114:115], v30 offset:9216
v_mfma_f32_16x16x32_bf16 v[180:183], v[84:87], v[52:55], v[180:183]
v_mfma_f32_16x16x32_bf16 v[184:187], v[88:91], v[52:55], v[184:187]
ds_read_b64_tr_b16 v[116:117], v31 offset:8192
ds_read_b64_tr_b16 v[118:119], v31 offset:9216
v_mfma_f32_16x16x32_bf16 v[188:191], v[92:95], v[52:55], v[188:191]
buffer_load_dwordx4 v17, s[16:19], s26 offen offset:1024 lds
v_mfma_f32_16x16x32_bf16 v[192:195], v[64:67], v[56:59], v[192:195]
ds_read_b128 v[52:55], v25 offset:2048
v_mfma_f32_16x16x32_bf16 v[196:199], v[68:71], v[56:59], v[196:199]
v_mfma_f32_16x16x32_bf16 v[200:203], v[72:75], v[56:59], v[200:203]
ds_read_b64_tr_b16 v[120:121], v32 offset:8192
ds_read_b64_tr_b16 v[122:123], v32 offset:9216
v_mfma_f32_16x16x32_bf16 v[204:207], v[76:79], v[56:59], v[204:207]
buffer_load_dwordx4 v18, s[16:19], s26 offen offset:2048 lds
v_mfma_f32_16x16x32_bf16 v[208:211], v[80:83], v[56:59], v[208:211]
ds_read_b64_tr_b16 v[124:125], v33 offset:8192
ds_read_b64_tr_b16 v[126:127], v33 offset:9216
v_mfma_f32_16x16x32_bf16 v[212:215], v[84:87], v[56:59], v[212:215]
v_mfma_f32_16x16x32_bf16 v[216:219], v[88:91], v[56:59], v[216:219]
s_waitcnt lgkmcnt(15)
v_mfma_f32_16x16x32_bf16 v[220:223], v[92:95], v[56:59], v[220:223]
v_mfma_f32_16x16x32_bf16 v[224:227], v[64:67], v[60:63], v[224:227]
ds_read_b128 v[56:59], v25 offset:4096
v_mfma_f32_16x16x32_bf16 v[228:231], v[68:71], v[60:63], v[228:231]
buffer_load_dwordx4 v19, s[16:19], s26 offen offset:3072 lds
v_mfma_f32_16x16x32_bf16 v[232:235], v[72:75], v[60:63], v[232:235]
v_mfma_f32_16x16x32_bf16 v[236:239], v[76:79], v[60:63], v[236:239]
s_add_u32 s26, s26, 524288
v_mfma_f32_16x16x32_bf16 v[240:243], v[80:83], v[60:63], v[240:243]
v_mfma_f32_16x16x32_bf16 v[244:247], v[84:87], v[60:63], v[244:247]
buffer_load_dword v8, v3, s[60:63], s90 offen sc1
v_mfma_f32_16x16x32_bf16 v[248:251], v[88:91], v[60:63], v[248:251]
v_mfma_f32_16x16x32_bf16 v[252:255], v[92:95], v[60:63], v[252:255]
s_waitcnt vmcnt(19) lgkmcnt(0)
s_barrier
v_mfma_f32_16x16x32_bf16 v[128:131], v[96:99], v[48:51], v[128:131]
ds_read_b128 v[60:63], v25 offset:6144
v_mfma_f32_16x16x32_bf16 v[132:135], v[100:103], v[48:51], v[132:135]
v_add_u32 v25, 65536, v25
v_mfma_f32_16x16x32_bf16 v[136:139], v[104:107], v[48:51], v[136:139]
s_add_u32 m0, s92, 32768
ds_read_b64_tr_b16 v[64:65], v26 offset:16384
ds_read_b64_tr_b16 v[66:67], v26 offset:17408
v_mfma_f32_16x16x32_bf16 v[140:143], v[108:111], v[48:51], v[140:143]
buffer_load_dwordx4 v20, s[12:15], s24 offen offset:0 lds
v_mfma_f32_16x16x32_bf16 v[144:147], v[112:115], v[48:51], v[144:147]
ds_read_b64_tr_b16 v[68:69], v27 offset:16384
ds_read_b64_tr_b16 v[70:71], v27 offset:17408
v_mfma_f32_16x16x32_bf16 v[148:151], v[116:119], v[48:51], v[148:151]
v_mfma_f32_16x16x32_bf16 v[152:155], v[120:123], v[48:51], v[152:155]
ds_read_b64_tr_b16 v[72:73], v28 offset:16384
ds_read_b64_tr_b16 v[74:75], v28 offset:17408
v_mfma_f32_16x16x32_bf16 v[156:159], v[124:127], v[48:51], v[156:159]
buffer_load_dwordx4 v21, s[12:15], s24 offen offset:1024 lds
v_mfma_f32_16x16x32_bf16 v[160:163], v[96:99], v[52:55], v[160:163]
ds_read_b128 v[48:51], v24 offset:0
v_mfma_f32_16x16x32_bf16 v[164:167], v[100:103], v[52:55], v[164:167]
v_mfma_f32_16x16x32_bf16 v[168:171], v[104:107], v[52:55], v[168:171]
ds_read_b64_tr_b16 v[76:77], v29 offset:16384
ds_read_b64_tr_b16 v[78:79], v29 offset:17408
v_mfma_f32_16x16x32_bf16 v[172:175], v[108:111], v[52:55], v[172:175]
buffer_load_dwordx4 v22, s[12:15], s24 offen offset:2048 lds
v_mfma_f32_16x16x32_bf16 v[176:179], v[112:115], v[52:55], v[176:179]
ds_read_b64_tr_b16 v[80:81], v30 offset:16384
ds_read_b64_tr_b16 v[82:83], v30 offset:17408
v_mfma_f32_16x16x32_bf16 v[180:183], v[116:119], v[52:55], v[180:183]
v_mfma_f32_16x16x32_bf16 v[184:187], v[120:123], v[52:55], v[184:187]
ds_read_b64_tr_b16 v[84:85], v31 offset:16384
ds_read_b64_tr_b16 v[86:87], v31 offset:17408
v_mfma_f32_16x16x32_bf16 v[188:191], v[124:127], v[52:55], v[188:191]
buffer_load_dwordx4 v23, s[12:15], s24 offen offset:3072 lds
v_mfma_f32_16x16x32_bf16 v[192:195], v[96:99], v[56:59], v[192:195]
ds_read_b128 v[52:55], v24 offset:2048
v_mfma_f32_16x16x32_bf16 v[196:199], v[100:103], v[56:59], v[196:199]
v_mfma_f32_16x16x32_bf16 v[200:203], v[104:107], v[56:59], v[200:203]
s_add_u32 m0, s92, 36864
ds_read_b64_tr_b16 v[88:89], v32 offset:16384
ds_read_b64_tr_b16 v[90:91], v32 offset:17408
v_mfma_f32_16x16x32_bf16 v[204:207], v[108:111], v[56:59], v[204:207]
buffer_load_dwordx4 v20, s[12:15], s25 offen offset:0 lds
v_mfma_f32_16x16x32_bf16 v[208:211], v[112:115], v[56:59], v[208:211]
ds_read_b64_tr_b16 v[92:93], v33 offset:16384
ds_read_b64_tr_b16 v[94:95], v33 offset:17408
v_mfma_f32_16x16x32_bf16 v[212:215], v[116:119], v[56:59], v[212:215]
v_mfma_f32_16x16x32_bf16 v[216:219], v[120:123], v[56:59], v[216:219]
s_waitcnt lgkmcnt(15)
v_mfma_f32_16x16x32_bf16 v[220:223], v[124:127], v[56:59], v[220:223]
v_mfma_f32_16x16x32_bf16 v[224:227], v[96:99], v[60:63], v[224:227]
ds_read_b128 v[56:59], v24 offset:4096
v_mfma_f32_16x16x32_bf16 v[228:231], v[100:103], v[60:63], v[228:231]
buffer_load_dwordx4 v21, s[12:15], s25 offen offset:1024 lds
v_mfma_f32_16x16x32_bf16 v[232:235], v[104:107], v[60:63], v[232:235]
v_mfma_f32_16x16x32_bf16 v[236:239], v[108:111], v[60:63], v[236:239]
v_mfma_f32_16x16x32_bf16 v[240:243], v[112:115], v[60:63], v[240:243]
v_mfma_f32_16x16x32_bf16 v[244:247], v[116:119], v[60:63], v[244:247]
v_mfma_f32_16x16x32_bf16 v[248:251], v[120:123], v[60:63], v[248:251]
v_mfma_f32_16x16x32_bf16 v[252:255], v[124:127], v[60:63], v[252:255]
s_waitcnt lgkmcnt(0)
v_mfma_f32_16x16x32_bf16 v[128:131], v[64:67], v[48:51], v[128:131]
ds_read_b128 v[60:63], v24 offset:6144
v_mfma_f32_16x16x32_bf16 v[132:135], v[68:71], v[48:51], v[132:135]
v_add_u32 v24, 32768, v24
v_mfma_f32_16x16x32_bf16 v[136:139], v[72:75], v[48:51], v[136:139]
ds_read_b64_tr_b16 v[96:97], v26 offset:24576
ds_read_b64_tr_b16 v[98:99], v26 offset:25600
v_mfma_f32_16x16x32_bf16 v[140:143], v[76:79], v[48:51], v[140:143]
buffer_load_dwordx4 v22, s[12:15], s25 offen offset:2048 lds
v_mfma_f32_16x16x32_bf16 v[144:147], v[80:83], v[48:51], v[144:147]
ds_read_b64_tr_b16 v[100:101], v27 offset:24576
ds_read_b64_tr_b16 v[102:103], v27 offset:25600
v_mfma_f32_16x16x32_bf16 v[148:151], v[84:87], v[48:51], v[148:151]
v_mfma_f32_16x16x32_bf16 v[152:155], v[88:91], v[48:51], v[152:155]
ds_read_b64_tr_b16 v[104:105], v28 offset:24576
ds_read_b64_tr_b16 v[106:107], v28 offset:25600
v_mfma_f32_16x16x32_bf16 v[156:159], v[92:95], v[48:51], v[156:159]
buffer_load_dwordx4 v23, s[12:15], s25 offen offset:3072 lds
v_mfma_f32_16x16x32_bf16 v[160:163], v[64:67], v[52:55], v[160:163]
ds_read_b128 v[48:51], v25 offset:0
v_mfma_f32_16x16x32_bf16 v[164:167], v[68:71], v[52:55], v[164:167]
s_add_u32 s24, s24, 128
s_add_u32 s25, s25, 128
v_mfma_f32_16x16x32_bf16 v[168:171], v[72:75], v[52:55], v[168:171]
ds_read_b64_tr_b16 v[108:109], v29 offset:24576
ds_read_b64_tr_b16 v[110:111], v29 offset:25600
v_mfma_f32_16x16x32_bf16 v[172:175], v[76:79], v[52:55], v[172:175]
v_mfma_f32_16x16x32_bf16 v[176:179], v[80:83], v[52:55], v[176:179]
ds_read_b64_tr_b16 v[112:113], v30 offset:24576
ds_read_b64_tr_b16 v[114:115], v30 offset:25600
v_mfma_f32_16x16x32_bf16 v[180:183], v[84:87], v[52:55], v[180:183]
v_mfma_f32_16x16x32_bf16 v[184:187], v[88:91], v[52:55], v[184:187]
ds_read_b64_tr_b16 v[116:117], v31 offset:24576
ds_read_b64_tr_b16 v[118:119], v31 offset:25600
v_mfma_f32_16x16x32_bf16 v[188:191], v[92:95], v[52:55], v[188:191]
v_mfma_f32_16x16x32_bf16 v[192:195], v[64:67], v[56:59], v[192:195]
ds_read_b128 v[52:55], v25 offset:2048
v_mfma_f32_16x16x32_bf16 v[196:199], v[68:71], v[56:59], v[196:199]
v_mfma_f32_16x16x32_bf16 v[200:203], v[72:75], v[56:59], v[200:203]
ds_read_b64_tr_b16 v[120:121], v32 offset:24576
ds_read_b64_tr_b16 v[122:123], v32 offset:25600
v_mfma_f32_16x16x32_bf16 v[204:207], v[76:79], v[56:59], v[204:207]
v_mfma_f32_16x16x32_bf16 v[208:211], v[80:83], v[56:59], v[208:211]
ds_read_b64_tr_b16 v[124:125], v33 offset:24576
ds_read_b64_tr_b16 v[126:127], v33 offset:25600
v_mfma_f32_16x16x32_bf16 v[212:215], v[84:87], v[56:59], v[212:215]
s_waitcnt vmcnt(8)
v_and_b32 v0, 7, v8
v_cmp_lt_u32 s[28:29], v0, v14
v_mfma_f32_16x16x32_bf16 v[216:219], v[88:91], v[56:59], v[216:219]
s_waitcnt lgkmcnt(15)
v_cmp_eq_u32 s[30:31], v8, v2
v_mfma_f32_16x16x32_bf16 v[220:223], v[92:95], v[56:59], v[220:223]
v_xor_b32 v26, 0x10000, v26
v_mfma_f32_16x16x32_bf16 v[224:227], v[64:67], v[60:63], v[224:227]
ds_read_b128 v[56:59], v25 offset:4096
v_xor_b32 v27, 0x10000, v27
v_mfma_f32_16x16x32_bf16 v[228:231], v[68:71], v[60:63], v[228:231]
v_xor_b32 v28, 0x10000, v28
v_mfma_f32_16x16x32_bf16 v[232:235], v[72:75], v[60:63], v[232:235]
ds_read_b128 v[20:23], v25 offset:6144
v_xor_b32 v29, 0x10000, v29
v_mfma_f32_16x16x32_bf16 v[236:239], v[76:79], v[60:63], v[236:239]
v_xor_b32 v30, 0x10000, v30
v_mfma_f32_16x16x32_bf16 v[240:243], v[80:83], v[60:63], v[240:243]
v_xor_b32 v31, 0x10000, v31
v_mfma_f32_16x16x32_bf16 v[244:247], v[84:87], v[60:63], v[244:247]
v_xor_b32 v32, 0x10000, v32
v_mfma_f32_16x16x32_bf16 v[248:251], v[88:91], v[60:63], v[248:251]
s_or_b64 s[28:29], s[28:29], s[30:31]
s_cbranch_scc1 .Lspin1
.Lspin1_ret:
s_add_u32 s28, s39, 0
s_and_b32 s28, s28, 0xffff
v_xor_b32 v33, 0x10000, v33
v_mfma_f32_16x16x32_bf16 v[252:255], v[92:95], v[60:63], v[252:255]
s_waitcnt vmcnt(13) lgkmcnt(0)
s_barrier
s_mov_b32 s40, 0
v_mfma_f32_16x16x32_bf16 v[128:131], v[96:99], v[48:51], v[128:131]
s_add_u32 m0, s28, 65536
v_mfma_f32_16x16x32_bf16 v[132:135], v[100:103], v[48:51], v[132:135]
buffer_load_dwordx4 v7, s[4:7], s40 offen offset:0 sc1 lds
v_add_u32 v25, 32768, v25
v_mfma_f32_16x16x32_bf16 v[136:139], v[104:107], v[48:51], v[136:139]
ds_read_b64_tr_b16 v[64:65], v26 offset:0
ds_read_b64_tr_b16 v[66:67], v26 offset:1024
v_mfma_f32_16x16x32_bf16 v[140:143], v[108:111], v[48:51], v[140:143]
v_mfma_f32_16x16x32_bf16 v[144:147], v[112:115], v[48:51], v[144:147]
ds_read_b64_tr_b16 v[68:69], v27 offset:0
ds_read_b64_tr_b16 v[70:71], v27 offset:1024
v_mfma_f32_16x16x32_bf16 v[148:151], v[116:119], v[48:51], v[148:151]
buffer_load_dwordx4 v7, s[4:7], s40 offen offset:1024 sc1 lds
v_mfma_f32_16x16x32_bf16 v[152:155], v[120:123], v[48:51], v[152:155]
ds_read_b64_tr_b16 v[72:73], v28 offset:0
ds_read_b64_tr_b16 v[74:75], v28 offset:1024
v_mfma_f32_16x16x32_bf16 v[156:159], v[124:127], v[48:51], v[156:159]
v_mfma_f32_16x16x32_bf16 v[160:163], v[96:99], v[52:55], v[160:163]
ds_read_b128 v[48:51], v24 offset:0
v_mfma_f32_16x16x32_bf16 v[164:167], v[100:103], v[52:55], v[164:167]
buffer_load_dwordx4 v7, s[4:7], s40 offen offset:2048 sc1 lds
v_mfma_f32_16x16x32_bf16 v[168:171], v[104:107], v[52:55], v[168:171]
ds_read_b64_tr_b16 v[76:77], v29 offset:0
ds_read_b64_tr_b16 v[78:79], v29 offset:1024
v_mfma_f32_16x16x32_bf16 v[172:175], v[108:111], v[52:55], v[172:175]
v_mfma_f32_16x16x32_bf16 v[176:179], v[112:115], v[52:55], v[176:179]
ds_read_b64_tr_b16 v[80:81], v30 offset:0
ds_read_b64_tr_b16 v[82:83], v30 offset:1024
v_mfma_f32_16x16x32_bf16 v[180:183], v[116:119], v[52:55], v[180:183]
buffer_load_dwordx4 v7, s[4:7], s40 offen offset:3072 sc1 lds
v_mfma_f32_16x16x32_bf16 v[184:187], v[120:123], v[52:55], v[184:187]
ds_read_b64_tr_b16 v[84:85], v31 offset:0
ds_read_b64_tr_b16 v[86:87], v31 offset:1024
v_mfma_f32_16x16x32_bf16 v[188:191], v[124:127], v[52:55], v[188:191]
s_add_u32 s28, s39, 16384
s_and_b32 s28, s28, 0xffff
v_mfma_f32_16x16x32_bf16 v[192:195], v[96:99], v[56:59], v[192:195]
s_mov_b32 s41, 16384
ds_read_b128 v[52:55], v24 offset:2048
v_mfma_f32_16x16x32_bf16 v[196:199], v[100:103], v[56:59], v[196:199]
s_add_u32 m0, s28, 65536
v_mfma_f32_16x16x32_bf16 v[200:203], v[104:107], v[56:59], v[200:203]
buffer_load_dwordx4 v7, s[4:7], s41 offen offset:0 sc1 lds
ds_read_b64_tr_b16 v[88:89], v32 offset:0
ds_read_b64_tr_b16 v[90:91], v32 offset:1024
v_mfma_f32_16x16x32_bf16 v[204:207], v[108:111], v[56:59], v[204:207]
v_mfma_f32_16x16x32_bf16 v[208:211], v[112:115], v[56:59], v[208:211]
ds_read_b64_tr_b16 v[92:93], v33 offset:0
ds_read_b64_tr_b16 v[94:95], v33 offset:1024
v_mfma_f32_16x16x32_bf16 v[212:215], v[116:119], v[56:59], v[212:215]
v_mfma_f32_16x16x32_bf16 v[216:219], v[120:123], v[56:59], v[216:219]
buffer_load_dwordx4 v7, s[4:7], s41 offen offset:1024 sc1 lds
v_mfma_f32_16x16x32_bf16 v[220:223], v[124:127], v[56:59], v[220:223]
v_mfma_f32_16x16x32_bf16 v[224:227], v[96:99], v[20:23], v[224:227]
ds_read_b128 v[56:59], v24 offset:4096
v_mfma_f32_16x16x32_bf16 v[228:231], v[100:103], v[20:23], v[228:231]
v_mfma_f32_16x16x32_bf16 v[232:235], v[104:107], v[20:23], v[232:235]
buffer_load_dwordx4 v7, s[4:7], s41 offen offset:2048 sc1 lds
v_mfma_f32_16x16x32_bf16 v[236:239], v[108:111], v[20:23], v[236:239]
v_mfma_f32_16x16x32_bf16 v[240:243], v[112:115], v[20:23], v[240:243]
v_mfma_f32_16x16x32_bf16 v[244:247], v[116:119], v[20:23], v[244:247]
v_mfma_f32_16x16x32_bf16 v[248:251], v[120:123], v[20:23], v[248:251]
buffer_load_dwordx4 v7, s[4:7], s41 offen offset:3072 sc1 lds
v_mfma_f32_16x16x32_bf16 v[252:255], v[124:127], v[20:23], v[252:255]
s_waitcnt lgkmcnt(0)
v_mfma_f32_16x16x32_bf16 v[128:131], v[64:67], v[48:51], v[128:131]
s_add_u32 s28, s39, 32768
s_and_b32 s28, s28, 0xffff
ds_read_b128 v[60:63], v24 offset:6144
v_mfma_f32_16x16x32_bf16 v[132:135], v[68:71], v[48:51], v[132:135]
s_mov_b32 s40, 32768
s_mov_b32 s14, 0
v_add_u32 v24, 4294868992, v24
v_mfma_f32_16x16x32_bf16 v[136:139], v[72:75], v[48:51], v[136:139]
s_add_u32 m0, s28, 65536
ds_read_b64_tr_b16 v[96:97], v26 offset:8192
ds_read_b64_tr_b16 v[98:99], v26 offset:9216
v_mfma_f32_16x16x32_bf16 v[140:143], v[76:79], v[48:51], v[140:143]
s_mov_b32 s52, 2
buffer_load_dwordx4 v7, s[4:7], s40 offen offset:0 sc1 lds
v_mfma_f32_16x16x32_bf16 v[144:147], v[80:83], v[48:51], v[144:147]
ds_read_b64_tr_b16 v[100:101], v27 offset:8192
ds_read_b64_tr_b16 v[102:103], v27 offset:9216
v_mfma_f32_16x16x32_bf16 v[148:151], v[84:87], v[48:51], v[148:151]
s_mov_b32 s16, 0
s_mov_b32 s53, 0
s_and_b32 s38, s2, 31
v_mfma_f32_16x16x32_bf16 v[152:155], v[88:91], v[48:51], v[152:155]
ds_read_b64_tr_b16 v[104:105], v28 offset:8192
ds_read_b64_tr_b16 v[106:107], v28 offset:9216
v_mfma_f32_16x16x32_bf16 v[156:159], v[92:95], v[48:51], v[156:159]
s_lshl_b32 s38, s38, 2
buffer_load_dwordx4 v7, s[4:7], s40 offen offset:1024 sc1 lds
v_mfma_f32_16x16x32_bf16 v[160:163], v[64:67], v[52:55], v[160:163]
s_add_u32 s38, s38, s3
ds_read_b128 v[48:51], v25 offset:0
v_mfma_f32_16x16x32_bf16 v[164:167], v[68:71], v[52:55], v[164:167]
s_mul_i32 s38, s38, 6144
v_mfma_f32_16x16x32_bf16 v[168:171], v[72:75], v[52:55], v[168:171]
ds_read_b64_tr_b16 v[108:109], v29 offset:8192
ds_read_b64_tr_b16 v[110:111], v29 offset:9216
v_mfma_f32_16x16x32_bf16 v[172:175], v[76:79], v[52:55], v[172:175]
buffer_load_dwordx4 v7, s[4:7], s40 offen offset:2048 sc1 lds
v_mfma_f32_16x16x32_bf16 v[176:179], v[80:83], v[52:55], v[176:179]
ds_read_b64_tr_b16 v[112:113], v30 offset:8192
ds_read_b64_tr_b16 v[114:115], v30 offset:9216
v_mfma_f32_16x16x32_bf16 v[180:183], v[84:87], v[52:55], v[180:183]
s_cmp_eq_u32 s89, 7
s_cselect_b64 s[12:13], s[80:81], s[12:13]
s_cselect_b32 s16, 8388608, s16
v_mfma_f32_16x16x32_bf16 v[184:187], v[88:91], v[52:55], v[184:187]
ds_read_b64_tr_b16 v[116:117], v31 offset:8192
ds_read_b64_tr_b16 v[118:119], v31 offset:9216
v_mfma_f32_16x16x32_bf16 v[188:191], v[92:95], v[52:55], v[188:191]
buffer_load_dwordx4 v7, s[4:7], s40 offen offset:3072 sc1 lds
v_mfma_f32_16x16x32_bf16 v[192:195], v[64:67], v[56:59], v[192:195]
ds_read_b128 v[52:55], v25 offset:2048
v_mfma_f32_16x16x32_bf16 v[196:199], v[68:71], v[56:59], v[196:199]
s_add_u32 s28, s39, 49152
s_and_b32 s28, s28, 0xffff
v_mfma_f32_16x16x32_bf16 v[200:203], v[72:75], v[56:59], v[200:203]
s_mov_b32 s41, 49152
ds_read_b64_tr_b16 v[120:121], v32 offset:8192
ds_read_b64_tr_b16 v[122:123], v32 offset:9216
v_mfma_f32_16x16x32_bf16 v[204:207], v[76:79], v[56:59], v[204:207]
s_add_u32 m0, s28, 65536
v_mfma_f32_16x16x32_bf16 v[208:211], v[80:83], v[56:59], v[208:211]
buffer_load_dwordx4 v7, s[4:7], s41 offen offset:0 sc1 lds
ds_read_b64_tr_b16 v[124:125], v33 offset:8192
ds_read_b64_tr_b16 v[126:127], v33 offset:9216
v_mfma_f32_16x16x32_bf16 v[212:215], v[84:87], v[56:59], v[212:215]
s_cmp_eq_u32 s89, 6
s_cselect_b64 s[12:13], s[66:67], s[12:13]
s_cselect_b32 s16, 6291456, s16
v_mfma_f32_16x16x32_bf16 v[216:219], v[88:91], v[56:59], v[216:219]
s_waitcnt lgkmcnt(15)
v_mfma_f32_16x16x32_bf16 v[220:223], v[92:95], v[56:59], v[220:223]
s_cmp_eq_u32 s89, 5
s_cselect_b64 s[12:13], s[64:65], s[12:13]
s_cselect_b32 s16, 4194304, s16
v_mfma_f32_16x16x32_bf16 v[224:227], v[64:67], v[60:63], v[224:227]
buffer_load_dwordx4 v7, s[4:7], s41 offen offset:1024 sc1 lds
ds_read_b128 v[56:59], v25 offset:4096
v_mfma_f32_16x16x32_bf16 v[228:231], v[68:71], v[60:63], v[228:231]
s_cmp_eq_u32 s89, 4
s_cselect_b64 s[12:13], s[68:69], s[12:13]
s_cselect_b32 s16, 2097152, s16
v_mfma_f32_16x16x32_bf16 v[232:235], v[72:75], v[60:63], v[232:235]
s_and_b32 s13, s13, 0xffff
v_mfma_f32_16x16x32_bf16 v[236:239], v[76:79], v[60:63], v[236:239]
v_min_u32 v9, 47, v1
v_lshlrev_b32 v9, 7, v9
v_mfma_f32_16x16x32_bf16 v[240:243], v[80:83], v[60:63], v[240:243]
buffer_load_dwordx4 v7, s[4:7], s41 offen offset:2048 sc1 lds
v_add_u32 v2, s38, v9
v_mfma_f32_16x16x32_bf16 v[244:247], v[84:87], v[60:63], v[244:247]
s_lshl_b32 s93, s2, 4
s_add_u32 s93, s93, s3
s_lshl_b32 s93, s93, 2
v_mfma_f32_16x16x32_bf16 v[248:251], v[88:91], v[60:63], v[248:251]
s_add_u32 s93, s93, 0x10000
v_mfma_f32_16x16x32_bf16 v[252:255], v[92:95], v[60:63], v[252:255]
s_waitcnt vmcnt(30)
s_waitcnt vmcnt(15) lgkmcnt(0)
s_barrier
v_mfma_f32_16x16x32_bf16 v[128:131], v[96:99], v[48:51], v[128:131]
buffer_load_dwordx4 v7, s[4:7], s41 offen offset:3072 sc1 lds
ds_read_b128 v[60:63], v25 offset:6144
v_mfma_f32_16x16x32_bf16 v[132:135], v[100:103], v[48:51], v[132:135]
v_add_u32 v25, 4294868992, v25
v_or_b32 v0, 2, v11
v_mfma_f32_16x16x32_bf16 v[136:139], v[104:107], v[48:51], v[136:139]
ds_read_b64_tr_b16 v[64:65], v26 offset:16384
ds_read_b64_tr_b16 v[66:67], v26 offset:17408
v_mfma_f32_16x16x32_bf16 v[140:143], v[108:111], v[48:51], v[140:143]
buffer_store_dword v0, v6, s[60:63], 0 offen sc1
v_lshlrev_b32 v9, 20, v1
v_mfma_f32_16x16x32_bf16 v[144:147], v[112:115], v[48:51], v[144:147]
ds_read_b64_tr_b16 v[68:69], v27 offset:16384
ds_read_b64_tr_b16 v[70:71], v27 offset:17408
v_mfma_f32_16x16x32_bf16 v[148:151], v[116:119], v[48:51], v[148:151]
v_add_u32 v5, s93, v9
v_mfma_f32_16x16x32_bf16 v[152:155], v[120:123], v[48:51], v[152:155]
ds_read_b64_tr_b16 v[72:73], v28 offset:16384
ds_read_b64_tr_b16 v[74:75], v28 offset:17408
v_mfma_f32_16x16x32_bf16 v[156:159], v[124:127], v[48:51], v[156:159]
v_mfma_f32_16x16x32_bf16 v[160:163], v[96:99], v[52:55], v[160:163]
s_xor_b32 s48, s95, 262144
ds_read_b128 v[48:51], v24 offset:0
v_mfma_f32_16x16x32_bf16 v[164:167], v[100:103], v[52:55], v[164:167]
buffer_store_dwordx4 a[128:131], v7, s[20:23], s48 offen sc1
v_mfma_f32_16x16x32_bf16 v[168:171], v[104:107], v[52:55], v[168:171]
ds_read_b64_tr_b16 v[76:77], v29 offset:16384
ds_read_b64_tr_b16 v[78:79], v29 offset:17408
v_mfma_f32_16x16x32_bf16 v[172:175], v[108:111], v[52:55], v[172:175]
v_mfma_f32_16x16x32_bf16 v[176:179], v[112:115], v[52:55], v[176:179]
ds_read_b64_tr_b16 v[80:81], v30 offset:16384
ds_read_b64_tr_b16 v[82:83], v30 offset:17408
.Lpad9:
s_cbranch_execz .Ltch2
v_mfma_f32_16x16x32_bf16 v[180:183], v[116:119], v[52:55], v[180:183]
v_mfma_f32_16x16x32_bf16 v[184:187], v[120:123], v[52:55], v[184:187]
ds_read_b64_tr_b16 v[84:85], v31 offset:16384
ds_read_b64_tr_b16 v[86:87], v31 offset:17408
v_mfma_f32_16x16x32_bf16 v[188:191], v[124:127], v[52:55], v[188:191]
v_mfma_f32_16x16x32_bf16 v[192:195], v[96:99], v[56:59], v[192:195]
ds_read_b128 v[52:55], v24 offset:2048
v_mfma_f32_16x16x32_bf16 v[196:199], v[100:103], v[56:59], v[196:199]
v_mfma_f32_16x16x32_bf16 v[200:203], v[104:107], v[56:59], v[200:203]
ds_read_b64_tr_b16 v[88:89], v32 offset:16384
ds_read_b64_tr_b16 v[90:91], v32 offset:17408
v_mfma_f32_16x16x32_bf16 v[204:207], v[108:111], v[56:59], v[204:207]
v_mfma_f32_16x16x32_bf16 v[208:211], v[112:115], v[56:59], v[208:211]
ds_read_b64_tr_b16 v[92:93], v33 offset:16384
ds_read_b64_tr_b16 v[94:95], v33 offset:17408
v_mfma_f32_16x16x32_bf16 v[212:215], v[116:119], v[56:59], v[212:215]
v_mfma_f32_16x16x32_bf16 v[216:219], v[120:123], v[56:59], v[216:219]
s_waitcnt lgkmcnt(15)
v_mfma_f32_16x16x32_bf16 v[220:223], v[124:127], v[56:59], v[220:223]
v_mfma_f32_16x16x32_bf16 v[224:227], v[96:99], v[60:63], v[224:227]
ds_read_b128 v[56:59], v24 offset:4096
v_mfma_f32_16x16x32_bf16 v[228:231], v[100:103], v[60:63], v[228:231]
s_xor_b32 s48, s95, 263168
v_mfma_f32_16x16x32_bf16 v[232:235], v[104:107], v[60:63], v[232:235]
buffer_store_dwordx4 a[132:135], v7, s[20:23], s48 offen sc1
v_mfma_f32_16x16x32_bf16 v[236:239], v[108:111], v[60:63], v[236:239]
v_mfma_f32_16x16x32_bf16 v[240:243], v[112:115], v[60:63], v[240:243]
v_mfma_f32_16x16x32_bf16 v[244:247], v[116:119], v[60:63], v[244:247]
v_mfma_f32_16x16x32_bf16 v[248:251], v[120:123], v[60:63], v[248:251]
v_mfma_f32_16x16x32_bf16 v[252:255], v[124:127], v[60:63], v[252:255]
s_waitcnt lgkmcnt(0)
v_mfma_f32_16x16x32_bf16 v[128:131], v[64:67], v[48:51], v[128:131]
ds_read_b128 v[60:63], v24 offset:6144
v_mfma_f32_16x16x32_bf16 v[132:135], v[68:71], v[48:51], v[132:135]
v_mfma_f32_16x16x32_bf16 v[136:139], v[72:75], v[48:51], v[136:139]
ds_read_b64_tr_b16 v[96:97], v26 offset:24576
ds_read_b64_tr_b16 v[98:99], v26 offset:25600
v_mfma_f32_16x16x32_bf16 v[140:143], v[76:79], v[48:51], v[140:143]
v_mfma_f32_16x16x32_bf16 v[144:147], v[80:83], v[48:51], v[144:147]
ds_read_b64_tr_b16 v[100:101], v27 offset:24576
ds_read_b64_tr_b16 v[102:103], v27 offset:25600
v_mfma_f32_16x16x32_bf16 v[148:151], v[84:87], v[48:51], v[148:151]
v_mfma_f32_16x16x32_bf16 v[152:155], v[88:91], v[48:51], v[152:155]
ds_read_b64_tr_b16 v[104:105], v28 offset:24576
ds_read_b64_tr_b16 v[106:107], v28 offset:25600
v_mfma_f32_16x16x32_bf16 v[156:159], v[92:95], v[48:51], v[156:159]
v_mfma_f32_16x16x32_bf16 v[160:163], v[64:67], v[52:55], v[160:163]
ds_read_b128 v[48:51], v25 offset:0
v_mfma_f32_16x16x32_bf16 v[164:167], v[68:71], v[52:55], v[164:167]
v_mfma_f32_16x16x32_bf16 v[168:171], v[72:75], v[52:55], v[168:171]
s_xor_b32 s48, s95, 264192
ds_read_b64_tr_b16 v[108:109], v29 offset:24576
ds_read_b64_tr_b16 v[110:111], v29 offset:25600
v_mfma_f32_16x16x32_bf16 v[172:175], v[76:79], v[52:55], v[172:175]
buffer_store_dwordx4 a[136:139], v7, s[20:23], s48 offen sc1
v_mfma_f32_16x16x32_bf16 v[176:179], v[80:83], v[52:55], v[176:179]
ds_read_b64_tr_b16 v[112:113], v30 offset:24576
ds_read_b64_tr_b16 v[114:115], v30 offset:25600
v_mfma_f32_16x16x32_bf16 v[180:183], v[84:87], v[52:55], v[180:183]
v_mfma_f32_16x16x32_bf16 v[184:187], v[88:91], v[52:55], v[184:187]
ds_read_b64_tr_b16 v[116:117], v31 offset:24576
ds_read_b64_tr_b16 v[118:119], v31 offset:25600
v_mfma_f32_16x16x32_bf16 v[188:191], v[92:95], v[52:55], v[188:191]
v_mfma_f32_16x16x32_bf16 v[192:195], v[64:67], v[56:59], v[192:195]
ds_read_b128 v[52:55], v25 offset:2048
v_mfma_f32_16x16x32_bf16 v[196:199], v[68:71], v[56:59], v[196:199]
v_mfma_f32_16x16x32_bf16 v[200:203], v[72:75], v[56:59], v[200:203]
ds_read_b64_tr_b16 v[120:121], v32 offset:24576
ds_read_b64_tr_b16 v[122:123], v32 offset:25600
v_mfma_f32_16x16x32_bf16 v[204:207], v[76:79], v[56:59], v[204:207]
v_mfma_f32_16x16x32_bf16 v[208:211], v[80:83], v[56:59], v[208:211]
ds_read_b64_tr_b16 v[124:125], v33 offset:24576
ds_read_b64_tr_b16 v[126:127], v33 offset:25600
v_mfma_f32_16x16x32_bf16 v[212:215], v[84:87], v[56:59], v[212:215]
v_mfma_f32_16x16x32_bf16 v[216:219], v[88:91], v[56:59], v[216:219]
s_waitcnt lgkmcnt(15)
v_subrev_u32 v33, s91, v7
v_add_u32 v32, 0x10000, v33
v_mfma_f32_16x16x32_bf16 v[220:223], v[92:95], v[56:59], v[220:223]
v_add_u32 v12, s46, v32
v_and_b32 v8, 15, v1
v_mfma_f32_16x16x32_bf16 v[224:227], v[64:67], v[60:63], v[224:227]
ds_read_b128 v[56:59], v25 offset:4096
v_lshrrev_b32 v9, 4, v1
v_mfma_f32_16x16x32_bf16 v[228:231], v[68:71], v[60:63], v[228:231]
v_lshlrev_b32 v9, 2, v9
v_sub_u32 v8, v8, v9
v_mfma_f32_16x16x32_bf16 v[232:235], v[72:75], v[60:63], v[232:235]
ds_read_b128 v[20:23], v25 offset:6144
v_mov_b32 v34, 0xff800000
v_mfma_f32_16x16x32_bf16 v[236:239], v[76:79], v[60:63], v[236:239]
v_and_b32 v9, 3, v1
v_mov_b32 v10, 0x3f803f80
v_mfma_f32_16x16x32_bf16 v[240:243], v[80:83], v[60:63], v[240:243]
v_cmp_eq_u32 vcc, 0, v9
v_nop
v_mfma_f32_16x16x32_bf16 v[244:247], v[84:87], v[60:63], v[244:247]
v_nop
v_cndmask_b32 v14, 0, v10, vcc
v_mfma_f32_16x16x32_bf16 v[248:251], v[88:91], v[60:63], v[248:251]
s_add_u32 s28, s39, 0
s_and_b32 s28, s28, 0xffff
s_add_u32 s49, s39, 16384
v_mfma_f32_16x16x32_bf16 v[252:255], v[92:95], v[60:63], v[252:255]
s_waitcnt  lgkmcnt(0)
s_waitcnt vmcnt(4)
s_barrier
s_mov_b32 s40, 16384
v_mfma_f32_16x16x32_bf16 v[128:131], v[96:99], v[48:51], v[128:131]
s_add_u32 m0, s28, 0
s_xor_b32 s46, s39, 0x8000
s_mov_b32 s44, 65536
v_mfma_f32_16x16x32_bf16 v[132:135], v[100:103], v[48:51], v[132:135]
s_xor_b32 s50, s46, 0x18000
buffer_load_dwordx4 v7, s[8:11], s40 offen offset:0 sc1 lds
v_mfma_f32_16x16x32_bf16 v[136:139], v[104:107], v[48:51], v[136:139]
s_lshl_b32 s30, s89, 2
s_add_u32 s51, s30, s94
s_max_u32 s29, s51, 3
v_mfma_f32_16x16x32_bf16 v[140:143], v[108:111], v[48:51], v[140:143]
s_sub_u32 s42, s29, 3
s_sub_u32 s56, 57, s30
s_add_u32 s56, s56, s3
v_mfma_f32_16x16x32_bf16 v[144:147], v[112:115], v[48:51], v[144:147]
ds_read_b128 a[0:3], v12 offset:0
ds_read_b128 a[32:35], v12 offset:8192
v_mfma_f32_16x16x32_bf16 v[148:151], v[116:119], v[48:51], v[148:151]
s_mov_b32 s57, 2
buffer_load_dwordx4 v7, s[8:11], s40 offen offset:1024 sc1 lds
v_mfma_f32_16x16x32_bf16 v[152:155], v[120:123], v[48:51], v[152:155]
ds_read_b128 a[4:7], v12 offset:4096
ds_read_b128 a[36:39], v12 offset:12288
v_mfma_f32_16x16x32_bf16 v[156:159], v[124:127], v[48:51], v[156:159]
s_min_u32 s43, s42, s57
s_sub_u32 s55, s43, 1
s_sub_u32 s31, s51, s29
v_mfma_f32_16x16x32_bf16 v[160:163], v[96:99], v[52:55], v[160:163]
ds_read_b128 a[8:11], v12 offset:1024
ds_read_b128 a[40:43], v12 offset:9216
v_mfma_f32_16x16x32_bf16 v[164:167], v[100:103], v[52:55], v[164:167]
s_lshl_b32 s31, s31, 5
buffer_load_dwordx4 v7, s[8:11], s40 offen offset:2048 sc1 lds
v_mfma_f32_16x16x32_bf16 v[168:171], v[104:107], v[52:55], v[168:171]
ds_read_b128 a[12:15], v12 offset:5120
ds_read_b128 a[44:47], v12 offset:13312
v_mfma_f32_16x16x32_bf16 v[172:175], v[108:111], v[52:55], v[172:175]
v_mfma_f32_16x16x32_bf16 v[176:179], v[112:115], v[52:55], v[176:179]
ds_read_b128 a[16:19], v12 offset:2048
ds_read_b128 a[48:51], v12 offset:10240
v_mfma_f32_16x16x32_bf16 v[180:183], v[116:119], v[52:55], v[180:183]
buffer_load_dwordx4 v7, s[8:11], s40 offen offset:3072 sc1 lds
v_mfma_f32_16x16x32_bf16 v[184:187], v[120:123], v[52:55], v[184:187]
ds_read_b128 a[20:23], v12 offset:6144
ds_read_b128 a[52:55], v12 offset:14336
v_mfma_f32_16x16x32_bf16 v[188:191], v[124:127], v[52:55], v[188:191]
v_mfma_f32_16x16x32_bf16 v[192:195], v[96:99], v[56:59], v[192:195]
ds_read_b128 a[24:27], v12 offset:3072
ds_read_b128 a[56:59], v12 offset:11264
v_mfma_f32_16x16x32_bf16 v[196:199], v[100:103], v[56:59], v[196:199]
v_mfma_f32_16x16x32_bf16 v[200:203], v[104:107], v[56:59], v[200:203]
ds_read_b128 a[28:31], v12 offset:7168
ds_read_b128 a[60:63], v12 offset:15360
v_mfma_f32_16x16x32_bf16 v[204:207], v[108:111], v[56:59], v[204:207]
v_cvt_pk_bf16_f32 v64, v128, v129
v_cvt_pk_bf16_f32 v65, v130, v131
v_mfma_f32_16x16x32_bf16 v[208:211], v[112:115], v[56:59], v[208:211]
v_cvt_pk_bf16_f32 v66, v132, v133
v_cvt_pk_bf16_f32 v67, v134, v135
v_mfma_f32_16x16x32_bf16 v[212:215], v[116:119], v[56:59], v[212:215]
v_cvt_pk_bf16_f32 v80, v160, v161
v_cvt_pk_bf16_f32 v81, v162, v163
v_mfma_f32_16x16x32_bf16 v[216:219], v[120:123], v[56:59], v[216:219]
v_cvt_pk_bf16_f32 v82, v164, v165
v_cvt_pk_bf16_f32 v83, v166, v167
v_mfma_f32_16x16x32_bf16 v[220:223], v[124:127], v[56:59], v[220:223]
v_cvt_pk_bf16_f32 v68, v136, v137
v_cvt_pk_bf16_f32 v69, v138, v139
v_mfma_f32_16x16x32_bf16 v[224:227], v[96:99], v[20:23], v[224:227]
v_cvt_pk_bf16_f32 v70, v140, v141
v_cvt_pk_bf16_f32 v71, v142, v143
v_mfma_f32_16x16x32_bf16 v[228:231], v[100:103], v[20:23], v[228:231]
v_cvt_pk_bf16_f32 v84, v168, v169
v_cvt_pk_bf16_f32 v85, v170, v171
v_mfma_f32_16x16x32_bf16 v[232:235], v[104:107], v[20:23], v[232:235]
v_cvt_pk_bf16_f32 v86, v172, v173
v_cvt_pk_bf16_f32 v87, v174, v175
v_mfma_f32_16x16x32_bf16 v[236:239], v[108:111], v[20:23], v[236:239]
v_cvt_pk_bf16_f32 v72, v144, v145
v_cvt_pk_bf16_f32 v73, v146, v147
v_mfma_f32_16x16x32_bf16 v[240:243], v[112:115], v[20:23], v[240:243]
v_cvt_pk_bf16_f32 v74, v148, v149
v_cvt_pk_bf16_f32 v75, v150, v151
v_mfma_f32_16x16x32_bf16 v[244:247], v[116:119], v[20:23], v[244:247]
v_cvt_pk_bf16_f32 v88, v176, v177
v_cvt_pk_bf16_f32 v89, v178, v179
v_mfma_f32_16x16x32_bf16 v[248:251], v[120:123], v[20:23], v[248:251]
s_getpc_b64 s[58:59]
.Lpc0:
s_add_u32 s58, s58, .Ljob0-.Lpc0
s_addc_u32 s59, s59, 0
v_mfma_f32_16x16x32_bf16 v[252:255], v[124:127], v[20:23], v[252:255]
s_waitcnt lgkmcnt(0)
v_cvt_pk_bf16_f32 v90, v180, v181
v_cvt_pk_bf16_f32 v91, v182, v183
v_cvt_pk_bf16_f32 v76, v152, v153
v_cvt_pk_bf16_f32 v77, v154, v155
v_cvt_pk_bf16_f32 v78, v156, v157
v_cvt_pk_bf16_f32 v79, v158, v159
v_cvt_pk_bf16_f32 v92, v184, v185
v_cvt_pk_bf16_f32 v93, v186, v187
v_cvt_pk_bf16_f32 v94, v188, v189
v_cvt_pk_bf16_f32 v95, v190, v191
v_mfma_f32_16x16x32_bf16 v[128:131], a[0:3], v[64:67], 0
s_add_u32 s28, s39, 16384
s_and_b32 s28, s28, 0xffff
s_mov_b32 s41, 32768
v_add_u32 v62, s49, v32
v_mfma_f32_16x16x32_bf16 v[136:139], a[0:3], v[80:83], 0
s_add_u32 m0, s28, 0
v_cvt_pk_bf16_f32 v96, v192, v193
v_cvt_pk_bf16_f32 v97, v194, v195
v_mfma_f32_16x16x32_bf16 v[132:135], a[4:7], v[64:67], 0
buffer_load_dwordx4 v7, s[8:11], s41 offen offset:0 sc1 lds
v_cvt_pk_bf16_f32 v98, v196, v197
v_mfma_f32_16x16x32_bf16 v[140:143], a[4:7], v[80:83], 0
v_cvt_pk_bf16_f32 v99, v198, v199
v_cvt_pk_bf16_f32 v112, v224, v225
v_mfma_f32_16x16x32_bf16 v[128:131], a[8:11], v[68:71], v[128:131]
v_cvt_pk_bf16_f32 v113, v226, v227
v_cvt_pk_bf16_f32 v114, v228, v229
v_mfma_f32_16x16x32_bf16 v[136:139], a[8:11], v[84:87], v[136:139]
v_cvt_pk_bf16_f32 v115, v230, v231
v_cvt_pk_bf16_f32 v100, v200, v201
v_mfma_f32_16x16x32_bf16 v[132:135], a[12:15], v[68:71], v[132:135]
buffer_load_dwordx4 v7, s[8:11], s41 offen offset:1024 sc1 lds
v_mfma_f32_16x16x32_bf16 v[140:143], a[12:15], v[84:87], v[140:143]
v_cvt_pk_bf16_f32 v101, v202, v203
v_cvt_pk_bf16_f32 v102, v204, v205
v_mfma_f32_16x16x32_bf16 v[128:131], a[16:19], v[72:75], v[128:131]
v_cvt_pk_bf16_f32 v103, v206, v207
v_cvt_pk_bf16_f32 v116, v232, v233
v_mfma_f32_16x16x32_bf16 v[136:139], a[16:19], v[88:91], v[136:139]
v_cvt_pk_bf16_f32 v117, v234, v235
v_cvt_pk_bf16_f32 v118, v236, v237
v_mfma_f32_16x16x32_bf16 v[132:135], a[20:23], v[72:75], v[132:135]
buffer_load_dwordx4 v7, s[8:11], s41 offen offset:2048 sc1 lds
v_cvt_pk_bf16_f32 v119, v238, v239
v_mfma_f32_16x16x32_bf16 v[140:143], a[20:23], v[88:91], v[140:143]
v_cvt_pk_bf16_f32 v104, v208, v209
v_cvt_pk_bf16_f32 v105, v210, v211
v_mfma_f32_16x16x32_bf16 v[128:131], a[24:27], v[76:79], v[128:131]
v_cvt_pk_bf16_f32 v106, v212, v213
v_cvt_pk_bf16_f32 v107, v214, v215
v_mfma_f32_16x16x32_bf16 v[136:139], a[24:27], v[92:95], v[136:139]
v_cvt_pk_bf16_f32 v120, v240, v241
v_cvt_pk_bf16_f32 v121, v242, v243
v_mfma_f32_16x16x32_bf16 v[132:135], a[28:31], v[76:79], v[132:135]
s_add_u32 s28, s39, 32768
s_and_b32 s28, s28, 0xffff
buffer_load_dwordx4 v7, s[8:11], s41 offen offset:3072 sc1 lds
v_mfma_f32_16x16x32_bf16 v[140:143], a[28:31], v[92:95], v[140:143]
s_mov_b32 s40, 49152
v_cvt_pk_bf16_f32 v122, v244, v245
v_cvt_pk_bf16_f32 v123, v246, v247
v_mfma_f32_16x16x32_bf16 v[160:163], a[0:3], v[96:99], 0
s_add_u32 m0, s28, 0
v_cvt_pk_bf16_f32 v108, v216, v217
v_cvt_pk_bf16_f32 v109, v218, v219
v_cvt_pk_bf16_f32 v110, v220, v221
v_mfma_f32_16x16x32_bf16 v[168:171], a[0:3], v[112:115], 0
buffer_load_dwordx4 v7, s[8:11], s40 offen offset:0 sc1 lds
v_cvt_pk_bf16_f32 v111, v222, v223
v_cvt_pk_bf16_f32 v124, v248, v249
v_mfma_f32_16x16x32_bf16 v[164:167], a[4:7], v[96:99], 0
v_cvt_pk_bf16_f32 v125, v250, v251
v_cvt_pk_bf16_f32 v126, v252, v253
v_cvt_pk_bf16_f32 v127, v254, v255
v_mfma_f32_16x16x32_bf16 v[172:175], a[4:7], v[112:115], 0
ds_read_b128 a[0:3], v62 offset:0
ds_read_b128 a[4:7], v62 offset:4096
v_max3_f32 v56, v128, v129, v130
v_max3_f32 v57, v136, v137, v138
v_mfma_f32_16x16x32_bf16 v[160:163], a[8:11], v[100:103], v[160:163]
v_max3_f32 v58, v131, v132, v133
v_max3_f32 v59, v139, v140, v141
v_max3_f32 v56, v56, v134, v135
v_mfma_f32_16x16x32_bf16 v[168:171], a[8:11], v[116:119], v[168:171]
buffer_load_dwordx4 v7, s[8:11], s40 offen offset:1024 sc1 lds
v_max3_f32 v57, v57, v142, v143
v_max_f32 v56, v56, v58
v_mfma_f32_16x16x32_bf16 v[164:167], a[12:15], v[100:103], v[164:167]
v_max_f32 v57, v57, v59
v_mov_b32 v58, v56
v_mov_b32 v59, v57
v_mfma_f32_16x16x32_bf16 v[172:175], a[12:15], v[116:119], v[172:175]
ds_read_b128 a[8:11], v62 offset:1024
ds_read_b128 a[12:15], v62 offset:5120
v_nop
v_permlane16_swap_b32 v56, v58
v_mfma_f32_16x16x32_bf16 v[160:163], a[16:19], v[104:107], v[160:163]
v_permlane16_swap_b32 v57, v59
v_max_f32 v56, v56, v58
v_max_f32 v57, v57, v59
v_mfma_f32_16x16x32_bf16 v[168:171], a[16:19], v[120:123], v[168:171]
buffer_load_dwordx4 v7, s[8:11], s40 offen offset:2048 sc1 lds
v_mov_b32 v58, v56
v_mov_b32 v59, v57
v_mfma_f32_16x16x32_bf16 v[164:167], a[20:23], v[104:107], v[164:167]
v_nop
v_permlane32_swap_b32 v56, v58
v_permlane32_swap_b32 v57, v59
v_mfma_f32_16x16x32_bf16 v[172:175], a[20:23], v[120:123], v[172:175]
ds_read_b128 a[16:19], v62 offset:2048
ds_read_b128 a[20:23], v62 offset:6144
v_max_f32 v56, v56, v58
v_max_f32 v57, v57, v59
v_mfma_f32_16x16x32_bf16 v[160:163], a[24:27], v[108:111], v[160:163]
v_sub_f32 v192, 0, v56
v_sub_f32 v193, 0, v56
v_sub_f32 v194, 0, v56
v_mfma_f32_16x16x32_bf16 v[168:171], a[24:27], v[124:127], v[168:171]
buffer_load_dwordx4 v7, s[8:11], s40 offen offset:3072 sc1 lds
v_sub_f32 v195, 0, v56
v_sub_f32 v196, 0, v57
v_mfma_f32_16x16x32_bf16 v[164:167], a[28:31], v[108:111], v[164:167]
v_sub_f32 v197, 0, v57
v_sub_f32 v198, 0, v57
v_sub_f32 v199, 0, v57
v_mfma_f32_16x16x32_bf16 v[172:175], a[28:31], v[124:127], v[172:175]
ds_read_b128 a[24:27], v62 offset:3072
ds_read_b128 a[28:31], v62 offset:7168
v_add_f32 v128, v128, v192
v_add_f32 v129, v129, v192
v_mfma_f32_16x16x32_bf16 v[144:147], a[32:35], v[64:67], v[192:195]
s_xor_b32 s48, s95, 265216
v_add_f32 v130, v130, v192
v_add_f32 v131, v131, v192
v_mfma_f32_16x16x32_bf16 v[152:155], a[32:35], v[80:83], v[196:199]
buffer_store_dwordx4 a[140:143], v7, s[20:23], s48 offen sc1
v_add_f32 v132, v132, v192
v_add_f32 v133, v133, v192
v_mfma_f32_16x16x32_bf16 v[148:151], a[36:39], v[64:67], v[192:195]
s_cmp_eq_u32 s51, 0
s_cbranch_scc1 .Lj0a
.Lj0a_ret:
v_max3_f32 v56, v160, v161, v162
v_max3_f32 v57, v168, v169, v170
v_mfma_f32_16x16x32_bf16 v[156:159], a[36:39], v[80:83], v[196:199]
v_max3_f32 v58, v163, v164, v165
v_max3_f32 v59, v171, v172, v173
v_max3_f32 v56, v56, v166, v167
v_mfma_f32_16x16x32_bf16 v[144:147], a[40:43], v[68:71], v[144:147]
v_max3_f32 v57, v57, v174, v175
v_max_f32 v56, v56, v58
v_mfma_f32_16x16x32_bf16 v[152:155], a[40:43], v[84:87], v[152:155]
s_xor_b32 s48, s95, 266240
v_max_f32 v57, v57, v59
v_mov_b32 v58, v56
v_mfma_f32_16x16x32_bf16 v[148:151], a[44:47], v[68:71], v[148:151]
buffer_store_dwordx4 a[160:163], v7, s[20:23], s48 offen sc1
v_mov_b32 v59, v57
v_nop
v_mfma_f32_16x16x32_bf16 v[156:159], a[44:47], v[84:87], v[156:159]
v_permlane16_swap_b32 v56, v58
v_permlane16_swap_b32 v57, v59
v_mfma_f32_16x16x32_bf16 v[144:147], a[48:51], v[72:75], v[144:147]
v_max_f32 v56, v56, v58
v_max_f32 v57, v57, v59
v_mov_b32 v58, v56
v_mfma_f32_16x16x32_bf16 v[152:155], a[48:51], v[88:91], v[152:155]
v_mov_b32 v59, v57
v_nop
v_permlane32_swap_b32 v56, v58
v_mfma_f32_16x16x32_bf16 v[148:151], a[52:55], v[72:75], v[148:151]
s_xor_b32 s48, s95, 267264
v_permlane32_swap_b32 v57, v59
v_max_f32 v56, v56, v58
v_mfma_f32_16x16x32_bf16 v[156:159], a[52:55], v[88:91], v[156:159]
buffer_store_dwordx4 a[164:167], v7, s[20:23], s48 offen sc1
v_max_f32 v57, v57, v59
v_sub_f32 v200, 0, v56
v_mfma_f32_16x16x32_bf16 v[144:147], a[56:59], v[76:79], v[144:147]
v_sub_f32 v201, 0, v56
v_sub_f32 v202, 0, v56
v_mfma_f32_16x16x32_bf16 v[152:155], a[56:59], v[92:95], v[152:155]
v_sub_f32 v203, 0, v56
v_sub_f32 v204, 0, v57
v_sub_f32 v205, 0, v57
v_mfma_f32_16x16x32_bf16 v[148:151], a[60:63], v[76:79], v[148:151]
v_sub_f32 v206, 0, v57
v_sub_f32 v207, 0, v57
v_mfma_f32_16x16x32_bf16 v[156:159], a[60:63], v[92:95], v[156:159]
s_xor_b32 s48, s95, 268288
v_add_f32 v134, v134, v192
v_add_f32 v135, v135, v192
v_mfma_f32_16x16x32_bf16 v[176:179], a[32:35], v[96:99], v[200:203]
buffer_store_dwordx4 a[168:171], v7, s[20:23], s48 offen sc1
v_mfma_f32_16x16x32_bf16 v[184:187], a[32:35], v[112:115], v[204:207]
v_add_f32 v136, v136, v196
v_add_f32 v137, v137, v196
v_mfma_f32_16x16x32_bf16 v[180:183], a[36:39], v[96:99], v[200:203]
v_add_f32 v138, v138, v196
v_add_f32 v139, v139, v196
v_mfma_f32_16x16x32_bf16 v[188:191], a[36:39], v[112:115], v[204:207]
v_add_f32 v140, v140, v196
v_mfma_f32_16x16x32_bf16 v[176:179], a[40:43], v[100:103], v[176:179]
s_xor_b32 s48, s95, 269312
v_add_f32 v141, v141, v196
v_add_f32 v142, v142, v196
v_mfma_f32_16x16x32_bf16 v[184:187], a[40:43], v[116:119], v[184:187]
buffer_store_dwordx4 a[172:175], v7, s[20:23], s48 offen sc1
v_mfma_f32_16x16x32_bf16 v[180:183], a[44:47], v[100:103], v[180:183]
v_add_f32 v143, v143, v196
v_add_u32 v63, s39, v33
v_mfma_f32_16x16x32_bf16 v[188:191], a[44:47], v[116:119], v[188:191]
v_add_u32 v13, s31, v8
v_mov_b32 v52, v14
v_mfma_f32_16x16x32_bf16 v[176:179], a[48:51], v[104:107], v[176:179]
v_mov_b32 v53, v14
v_mfma_f32_16x16x32_bf16 v[184:187], a[48:51], v[120:123], v[184:187]
s_xor_b32 s48, s95, 393216
v_mov_b32 v54, v14
v_mov_b32 v55, v14
v_mfma_f32_16x16x32_bf16 v[180:183], a[52:55], v[104:107], v[180:183]
buffer_store_dwordx4 a[192:195], v7, s[20:23], s48 offen sc1
v_mfma_f32_16x16x32_bf16 v[188:191], a[52:55], v[120:123], v[188:191]
v_add_f32 v160, v160, v200
v_add_f32 v161, v161, v200
v_mfma_f32_16x16x32_bf16 v[176:179], a[56:59], v[108:111], v[176:179]
v_add_f32 v162, v162, v200
v_add_f32 v163, v163, v200
v_mfma_f32_16x16x32_bf16 v[184:187], a[56:59], v[124:127], v[184:187]
v_add_f32 v164, v164, v200
v_mfma_f32_16x16x32_bf16 v[180:183], a[60:63], v[108:111], v[180:183]
v_add_f32 v165, v165, v200
v_add_f32 v166, v166, v200
v_mfma_f32_16x16x32_bf16 v[188:191], a[60:63], v[124:127], v[188:191]
v_add_f32 v167, v167, v200
v_add_f32 v168, v168, v204
v_add_f32 v169, v169, v204
v_add_f32 v170, v170, v204
v_add_f32 v171, v171, v204
v_add_f32 v172, v172, v204
v_add_f32 v173, v173, v204
v_add_f32 v174, v174, v204
v_add_f32 v175, v175, v204
v_exp_f32 v128, v128
v_exp_f32 v129, v129
v_exp_f32 v160, v160
v_cvt_pk_bf16_f32 v224, v128, v129
v_exp_f32 v161, v161
v_exp_f32 v130, v130
v_cvt_pk_bf16_f32 v240, v160, v161
v_exp_f32 v131, v131
v_exp_f32 v162, v162
v_cvt_pk_bf16_f32 v225, v130, v131
v_exp_f32 v163, v163
v_exp_f32 v132, v132
v_cvt_pk_bf16_f32 v241, v162, v163
v_exp_f32 v133, v133
v_exp_f32 v164, v164
v_cvt_pk_bf16_f32 v226, v132, v133
v_exp_f32 v165, v165
v_exp_f32 v134, v134
v_cvt_pk_bf16_f32 v242, v164, v165
v_exp_f32 v135, v135
v_exp_f32 v166, v166
v_cvt_pk_bf16_f32 v227, v134, v135
v_exp_f32 v167, v167
v_exp_f32 v136, v136
v_cvt_pk_bf16_f32 v243, v166, v167
v_exp_f32 v137, v137
v_exp_f32 v168, v168
v_cvt_pk_bf16_f32 v228, v136, v137
v_exp_f32 v169, v169
v_exp_f32 v138, v138
v_cvt_pk_bf16_f32 v244, v168, v169
v_exp_f32 v139, v139
v_exp_f32 v170, v170
v_cvt_pk_bf16_f32 v229, v138, v139
v_exp_f32 v171, v171
v_exp_f32 v140, v140
v_cvt_pk_bf16_f32 v245, v170, v171
v_exp_f32 v141, v141
v_exp_f32 v172, v172
v_cvt_pk_bf16_f32 v230, v140, v141
v_exp_f32 v173, v173
v_exp_f32 v142, v142
v_cvt_pk_bf16_f32 v246, v172, v173
v_exp_f32 v143, v143
v_exp_f32 v174, v174
v_cvt_pk_bf16_f32 v231, v142, v143
v_exp_f32 v175, v175
s_nop 0
v_cvt_pk_bf16_f32 v247, v174, v175
s_cmp_lt_u32 s51, 2
s_cbranch_scc1 .Lj0b
.Lj0b_ret:
s_waitcnt vmcnt(14) lgkmcnt(0)
s_barrier
v_mfma_f32_16x16x32_bf16 v[128:131], a[0:3], v[64:67], v[192:195]
v_exp_f32 v144, v144
v_mfma_f32_16x16x32_bf16 v[136:139], a[0:3], v[80:83], v[196:199]
ds_read_b128 a[32:35], v63 offset:0
ds_read_b128 a[36:39], v63 offset:1024
v_cmp_gt_i32 s[32:33], 0, v13
v_mfma_f32_16x16x32_bf16 v[160:163], a[0:3], v[96:99], v[200:203]
v_exp_f32 v145, v145
v_mfma_f32_16x16x32_bf16 v[168:171], a[0:3], v[112:115], v[204:207]
s_xor_b32 s48, s95, 394240
ds_read_b128 a[0:3], v62 offset:8192
v_nop
v_mfma_f32_16x16x32_bf16 v[132:135], a[4:7], v[64:67], v[192:195]
buffer_store_dwordx4 a[196:199], v7, s[20:23], s48 offen sc1
v_mfma_f32_16x16x32_bf16 v[140:143], a[4:7], v[80:83], v[196:199]
ds_read_b128 a[40:43], v63 offset:2048
ds_read_b128 a[44:47], v63 offset:3072
v_cmp_gt_i32 s[34:35], 1, v13
v_mfma_f32_16x16x32_bf16 v[164:167], a[4:7], v[96:99], v[200:203]
s_sub_u32 s52, s52, 1
s_cselect_b32 s52, 0, s52
s_cselect_b32 s14, s16, 0
v_mfma_f32_16x16x32_bf16 v[172:175], a[4:7], v[112:115], v[204:207]
ds_read_b128 a[4:7], v62 offset:12288
v_cndmask_b32 v36, v200, v34, s[32:33]
v_mfma_f32_16x16x32_bf16 v[128:131], a[8:11], v[68:71], v[128:131]
s_cmp_lg_u32 s14, 0
s_cselect_b32 s17, 786432, 0
v_cmp_gt_i32 s[32:33], 2, v13
v_mfma_f32_16x16x32_bf16 v[136:139], a[8:11], v[84:87], v[136:139]
ds_read_b128 a[48:51], v63 offset:4096
ds_read_b128 a[52:55], v63 offset:5120
v_mfma_f32_16x16x32_bf16 v[160:163], a[8:11], v[100:103], v[160:163]
v_exp_f32 v176, v176
v_mfma_f32_16x16x32_bf16 v[168:171], a[8:11], v[116:119], v[168:171]
s_xor_b32 s48, s95, 395264
ds_read_b128 a[8:11], v62 offset:9216
v_cndmask_b32 v37, v200, v34, s[34:35]
v_mfma_f32_16x16x32_bf16 v[132:135], a[12:15], v[68:71], v[132:135]
buffer_store_dwordx4 a[200:203], v7, s[20:23], s48 offen sc1
v_mfma_f32_16x16x32_bf16 v[140:143], a[12:15], v[84:87], v[140:143]
ds_read_b128 a[56:59], v63 offset:6144
ds_read_b128 a[60:63], v63 offset:7168
v_cmp_gt_i32 s[34:35], 3, v13
v_mfma_f32_16x16x32_bf16 v[164:167], a[12:15], v[100:103], v[164:167]
v_exp_f32 v177, v177
v_mfma_f32_16x16x32_bf16 v[172:175], a[12:15], v[116:119], v[172:175]
ds_read_b128 a[12:15], v62 offset:13312
v_cndmask_b32 v38, v200, v34, s[32:33]
v_mfma_f32_16x16x32_bf16 v[128:131], a[16:19], v[72:75], v[128:131]
v_exp_f32 v146, v146
v_mfma_f32_16x16x32_bf16 v[136:139], a[16:19], v[88:91], v[136:139]
v_exp_f32 v147, v147
v_cmp_gt_i32 s[32:33], 16, v13
v_mfma_f32_16x16x32_bf16 v[160:163], a[16:19], v[104:107], v[160:163]
v_exp_f32 v178, v178
v_mfma_f32_16x16x32_bf16 v[168:171], a[16:19], v[120:123], v[168:171]
s_xor_b32 s48, s95, 396288
ds_read_b128 a[16:19], v62 offset:10240
v_cndmask_b32 v39, v200, v34, s[34:35]
v_mfma_f32_16x16x32_bf16 v[132:135], a[20:23], v[72:75], v[132:135]
buffer_store_dwordx4 a[204:207], v7, s[20:23], s48 offen sc1
v_mfma_f32_16x16x32_bf16 v[140:143], a[20:23], v[88:91], v[140:143]
v_exp_f32 v179, v179
v_cmp_gt_i32 s[34:35], 17, v13
v_mfma_f32_16x16x32_bf16 v[164:167], a[20:23], v[104:107], v[164:167]
v_exp_f32 v148, v148
v_cndmask_b32 v40, v200, v34, s[32:33]
v_mfma_f32_16x16x32_bf16 v[172:175], a[20:23], v[120:123], v[172:175]
ds_read_b128 a[20:23], v62 offset:14336
v_cvt_pk_bf16_f32 v232, v144, v145
v_mfma_f32_16x16x32_bf16 v[128:131], a[24:27], v[76:79], v[128:131]
v_exp_f32 v149, v149
v_cmp_gt_i32 s[32:33], 18, v13
v_mfma_f32_16x16x32_bf16 v[136:139], a[24:27], v[92:95], v[136:139]
v_exp_f32 v180, v180
v_mfma_f32_16x16x32_bf16 v[160:163], a[24:27], v[108:111], v[160:163]
v_exp_f32 v181, v181
v_cndmask_b32 v41, v200, v34, s[34:35]
v_mfma_f32_16x16x32_bf16 v[168:171], a[24:27], v[124:127], v[168:171]
s_xor_b32 s48, s95, 397312
ds_read_b128 a[24:27], v62 offset:11264
v_mfma_f32_16x16x32_bf16 v[132:135], a[28:31], v[76:79], v[132:135]
buffer_store_dwordx4 a[224:227], v7, s[20:23], s48 offen sc1
v_cmp_gt_i32 s[34:35], 19, v13
v_mfma_f32_16x16x32_bf16 v[140:143], a[28:31], v[92:95], v[140:143]
v_exp_f32 v150, v150
v_mfma_f32_16x16x32_bf16 v[164:167], a[28:31], v[108:111], v[164:167]
v_exp_f32 v151, v151
v_cndmask_b32 v42, v200, v34, s[32:33]
v_mfma_f32_16x16x32_bf16 v[172:175], a[28:31], v[124:127], v[172:175]
ds_read_b128 a[28:31], v62 offset:15360
v_cvt_pk_bf16_f32 v248, v176, v177
v_mfma_f32_16x16x32_bf16 v[208:211], v[52:55], v[224:227], 0
v_add_u32 v62, s46, v32
v_cvt_pk_bf16_f32 v233, v146, v147
v_cmp_gt_i32 s[32:33], -16, v13
v_mfma_f32_16x16x32_bf16 v[212:215], v[52:55], v[228:231], 0
v_exp_f32 v182, v182
v_mfma_f32_16x16x32_bf16 v[216:219], v[52:55], v[240:243], 0
v_exp_f32 v183, v183
v_cndmask_b32 v43, v200, v34, s[34:35]
v_mfma_f32_16x16x32_bf16 v[220:223], v[52:55], v[244:247], 0
s_waitcnt lgkmcnt(5)
s_xor_b32 s48, s95, 398336
v_cvt_pk_bf16_f32 v249, v178, v179
v_mfma_f32_16x16x32_bf16 a[64:67], a[32:35], v[224:227], 0
buffer_store_dwordx4 a[228:231], v7, s[20:23], s48 offen sc1
v_cmp_gt_i32 s[34:35], -15, v13
v_mfma_f32_16x16x32_bf16 a[72:75], a[32:35], v[228:231], 0
v_exp_f32 v152, v152
v_mfma_f32_16x16x32_bf16 a[176:179], a[32:35], v[240:243], 0
.Lpad10:
s_cbranch_execz .Ltch3
v_exp_f32 v153, v153
v_cndmask_b32 v44, v204, v34, s[32:33]
v_mfma_f32_16x16x32_bf16 a[184:187], a[32:35], v[244:247], 0
ds_read_b128 a[32:35], v63 offset:8192
v_cvt_pk_bf16_f32 v234, v148, v149
v_mfma_f32_16x16x32_bf16 a[68:71], a[36:39], v[224:227], 0
v_exp_f32 v184, v184
v_cmp_gt_i32 s[32:33], -14, v13
v_mfma_f32_16x16x32_bf16 a[76:79], a[36:39], v[228:231], 0
v_exp_f32 v185, v185
v_mfma_f32_16x16x32_bf16 a[180:183], a[36:39], v[240:243], 0
v_exp_f32 v154, v154
v_cndmask_b32 v45, v204, v34, s[34:35]
v_mfma_f32_16x16x32_bf16 a[188:191], a[36:39], v[244:247], 0
s_xor_b32 s48, s95, 399360
ds_read_b128 a[36:39], v63 offset:9216
v_mfma_f32_16x16x32_bf16 a[80:83], a[40:43], v[224:227], 0
buffer_store_dwordx4 a[232:235], v7, s[20:23], s48 offen sc1
v_cmp_gt_i32 s[34:35], -13, v13
v_mfma_f32_16x16x32_bf16 a[88:91], a[40:43], v[228:231], 0
v_exp_f32 v155, v155
v_mfma_f32_16x16x32_bf16 a[208:211], a[40:43], v[240:243], 0
v_exp_f32 v186, v186
v_cndmask_b32 v46, v204, v34, s[32:33]
v_mfma_f32_16x16x32_bf16 a[216:219], a[40:43], v[244:247], 0
ds_read_b128 a[40:43], v63 offset:10240
v_cvt_pk_bf16_f32 v250, v180, v181
v_mfma_f32_16x16x32_bf16 a[84:87], a[44:47], v[224:227], 0
v_exp_f32 v187, v187
v_cmp_gt_i32 s[32:33], 0, v13
v_mfma_f32_16x16x32_bf16 a[92:95], a[44:47], v[228:231], 0
v_cvt_pk_bf16_f32 v235, v150, v151
v_cvt_pk_bf16_f32 v251, v182, v183
v_mfma_f32_16x16x32_bf16 a[212:215], a[44:47], v[240:243], 0
v_exp_f32 v156, v156
v_cndmask_b32 v47, v204, v34, s[34:35]
v_mfma_f32_16x16x32_bf16 a[220:223], a[44:47], v[244:247], 0
s_xor_b32 s48, s95, 400384
ds_read_b128 a[44:47], v63 offset:11264
v_mfma_f32_16x16x32_bf16 a[112:115], a[48:51], v[224:227], 0
buffer_store_dwordx4 a[236:239], v7, s[20:23], s48 offen sc1
v_cmp_gt_i32 s[34:35], 1, v13
v_mfma_f32_16x16x32_bf16 a[120:123], a[48:51], v[228:231], 0
v_exp_f32 v157, v157
v_mfma_f32_16x16x32_bf16 a[240:243], a[48:51], v[240:243], 0
buffer_load_dword v35, v2, s[12:15], s53 offen
v_cndmask_b32 v48, v204, v34, s[32:33]
v_mfma_f32_16x16x32_bf16 a[248:251], a[48:51], v[244:247], 0
s_add_u32 s53, s53, s17
ds_read_b128 a[48:51], v63 offset:12288
v_mfma_f32_16x16x32_bf16 a[116:119], a[52:55], v[224:227], 0
v_exp_f32 v188, v188
v_cmp_gt_i32 s[32:33], 2, v13
v_mfma_f32_16x16x32_bf16 a[124:127], a[52:55], v[228:231], 0
v_exp_f32 v189, v189
v_mfma_f32_16x16x32_bf16 a[244:247], a[52:55], v[240:243], 0
v_exp_f32 v158, v158
v_cndmask_b32 v49, v204, v34, s[34:35]
v_mfma_f32_16x16x32_bf16 a[252:255], a[52:55], v[244:247], 0
ds_read_b128 a[52:55], v63 offset:13312
v_cvt_pk_bf16_f32 v236, v152, v153
v_mfma_f32_16x16x32_bf16 a[144:147], a[56:59], v[224:227], 0
v_exp_f32 v159, v159
v_cmp_gt_i32 s[34:35], 3, v13
v_mfma_f32_16x16x32_bf16 a[152:155], a[56:59], v[228:231], 0
v_cvt_pk_bf16_f32 v252, v184, v185
v_cvt_pk_bf16_f32 v237, v154, v155
v_mfma_f32_16x16x32_bf16 a[96:99], a[56:59], v[240:243], 0
v_exp_f32 v190, v190
v_cndmask_b32 v50, v204, v34, s[32:33]
v_mfma_f32_16x16x32_bf16 a[104:107], a[56:59], v[244:247], 0
ds_read_b128 a[56:59], v63 offset:14336
v_cvt_pk_bf16_f32 v253, v186, v187
v_mfma_f32_16x16x32_bf16 a[148:151], a[60:63], v[224:227], 0
v_cvt_pk_bf16_f32 v238, v156, v157
v_cvt_pk_bf16_f32 v254, v188, v189
v_nop
v_mfma_f32_16x16x32_bf16 a[156:159], a[60:63], v[228:231], 0
s_waitcnt vmcnt(18) lgkmcnt(7)
v_exp_f32 v191, v191
v_mfma_f32_16x16x32_bf16 a[100:103], a[60:63], v[240:243], 0
s_barrier
v_cvt_pk_bf16_f32 v239, v158, v159
v_cvt_pk_bf16_f32 v255, v190, v191
v_cndmask_b32 v51, v204, v34, s[34:35]
v_mfma_f32_16x16x32_bf16 a[108:111], a[60:63], v[244:247], 0
ds_read_b128 a[60:63], v63 offset:15360
s_cmp_lt_u32 s51, 3
s_cbranch_scc1 .Lj0c
s_cmp_eq_u32 s29, 3
s_cbranch_scc1 .LT1
s_nop 0
s_nop 0
s_nop 0
s_nop 0
s_nop 0
s_nop 0
.LD1:
v_mfma_f32_16x16x32_bf16 v[144:147], a[0:3], v[64:67], v[192:195]
s_cmp_lt_u32 s44, s54
v_exp_f32 v128, v128
v_mfma_f32_16x16x32_bf16 v[152:155], a[0:3], v[80:83], v[196:199]
s_cselect_b32 m0, s50, s27
v_exp_f32 v129, v129
v_mfma_f32_16x16x32_bf16 v[176:179], a[0:3], v[96:99], v[200:203]
v_add_u32 v63, s49, v33
v_mfma_f32_16x16x32_bf16 v[184:187], a[0:3], v[112:115], v[204:207]
ds_read_b128 a[0:3], v62 offset:0
v_mfma_f32_16x16x32_bf16 v[148:151], a[4:7], v[64:67], v[192:195]
v_exp_f32 v160, v160
v_mfma_f32_16x16x32_bf16 v[156:159], a[4:7], v[80:83], v[196:199]
v_mfma_f32_16x16x32_bf16 v[180:183], a[4:7], v[96:99], v[200:203]
v_exp_f32 v161, v161
v_mfma_f32_16x16x32_bf16 v[188:191], a[4:7], v[112:115], v[204:207]
ds_read_b128 a[4:7], v62 offset:4096
v_mfma_f32_16x16x32_bf16 v[144:147], a[8:11], v[68:71], v[144:147]
buffer_load_dwordx4 v7, s[4:7], s44 offen offset:0 sc1 lds
v_mfma_f32_16x16x32_bf16 v[152:155], a[8:11], v[84:87], v[152:155]
v_exp_f32 v130, v130
v_mfma_f32_16x16x32_bf16 v[176:179], a[8:11], v[100:103], v[176:179]
v_exp_f32 v131, v131
v_mfma_f32_16x16x32_bf16 v[184:187], a[8:11], v[116:119], v[184:187]
ds_read_b128 a[8:11], v62 offset:1024
v_mfma_f32_16x16x32_bf16 v[148:151], a[12:15], v[68:71], v[148:151]
s_mov_b32 s49, s46
v_exp_f32 v162, v162
v_mfma_f32_16x16x32_bf16 v[156:159], a[12:15], v[84:87], v[156:159]
v_mfma_f32_16x16x32_bf16 v[180:183], a[12:15], v[100:103], v[180:183]
v_exp_f32 v163, v163
v_mfma_f32_16x16x32_bf16 v[188:191], a[12:15], v[116:119], v[188:191]
ds_read_b128 a[12:15], v62 offset:5120
v_mfma_f32_16x16x32_bf16 v[144:147], a[16:19], v[72:75], v[144:147]
v_exp_f32 v132, v132
v_mfma_f32_16x16x32_bf16 v[152:155], a[16:19], v[88:91], v[152:155]
v_mfma_f32_16x16x32_bf16 v[176:179], a[16:19], v[104:107], v[176:179]
s_add_u32 s46, s46, 16384
v_exp_f32 v133, v133
v_mfma_f32_16x16x32_bf16 v[184:187], a[16:19], v[120:123], v[184:187]
ds_read_b128 a[16:19], v62 offset:2048
v_mfma_f32_16x16x32_bf16 v[148:151], a[20:23], v[72:75], v[148:151]
buffer_load_dwordx4 v7, s[4:7], s44 offen offset:1024 sc1 lds
v_mfma_f32_16x16x32_bf16 v[156:159], a[20:23], v[88:91], v[156:159]
v_exp_f32 v164, v164
v_mfma_f32_16x16x32_bf16 v[180:183], a[20:23], v[104:107], v[180:183]
v_exp_f32 v165, v165
v_mfma_f32_16x16x32_bf16 v[188:191], a[20:23], v[120:123], v[188:191]
ds_read_b128 a[20:23], v62 offset:6144
v_mfma_f32_16x16x32_bf16 v[144:147], a[24:27], v[76:79], v[144:147]
v_mfma_f32_16x16x32_bf16 v[152:155], a[24:27], v[92:95], v[152:155]
v_exp_f32 v134, v134
v_mfma_f32_16x16x32_bf16 v[176:179], a[24:27], v[108:111], v[176:179]
s_and_b32 s46, s46, 0xffff
v_exp_f32 v135, v135
v_mfma_f32_16x16x32_bf16 v[184:187], a[24:27], v[124:127], v[184:187]
ds_read_b128 a[24:27], v62 offset:3072
v_mfma_f32_16x16x32_bf16 v[148:151], a[28:31], v[76:79], v[148:151]
v_mfma_f32_16x16x32_bf16 v[156:159], a[28:31], v[92:95], v[156:159]
v_exp_f32 v166, v166
v_mfma_f32_16x16x32_bf16 v[180:183], a[28:31], v[108:111], v[180:183]
v_mfma_f32_16x16x32_bf16 v[188:191], a[28:31], v[124:127], v[188:191]
ds_read_b128 a[28:31], v62 offset:7168
v_mfma_f32_16x16x32_bf16 v[208:211], v[52:55], v[232:235], v[208:211]
v_exp_f32 v167, v167
v_mfma_f32_16x16x32_bf16 v[212:215], v[52:55], v[236:239], v[212:215]
v_exp_f32 v136, v136
v_mfma_f32_16x16x32_bf16 v[216:219], v[52:55], v[248:251], v[216:219]
v_mfma_f32_16x16x32_bf16 v[220:223], v[52:55], v[252:255], v[220:223]
s_waitcnt lgkmcnt(8)
v_exp_f32 v137, v137
v_mfma_f32_16x16x32_bf16 a[64:67], a[32:35], v[232:235], a[64:67]
buffer_load_dwordx4 v7, s[4:7], s44 offen offset:2048 sc1 lds
v_mfma_f32_16x16x32_bf16 a[72:75], a[32:35], v[236:239], a[72:75]
v_exp_f32 v168, v168
v_mfma_f32_16x16x32_bf16 a[176:179], a[32:35], v[248:251], a[176:179]
v_mfma_f32_16x16x32_bf16 a[184:187], a[32:35], v[252:255], a[184:187]
ds_read_b128 a[32:35], v63 offset:0
v_mfma_f32_16x16x32_bf16 a[68:71], a[36:39], v[232:235], a[68:71]
v_exp_f32 v169, v169
v_mfma_f32_16x16x32_bf16 a[76:79], a[36:39], v[236:239], a[76:79]
v_mfma_f32_16x16x32_bf16 a[180:183], a[36:39], v[248:251], a[180:183]
v_exp_f32 v138, v138
v_mfma_f32_16x16x32_bf16 a[188:191], a[36:39], v[252:255], a[188:191]
ds_read_b128 a[36:39], v63 offset:1024
v_mfma_f32_16x16x32_bf16 a[80:83], a[40:43], v[232:235], a[80:83]
v_exp_f32 v139, v139
v_mfma_f32_16x16x32_bf16 a[88:91], a[40:43], v[236:239], a[88:91]
v_cvt_pk_bf16_f32 v224, v128, v129
v_mfma_f32_16x16x32_bf16 a[208:211], a[40:43], v[248:251], a[208:211]
v_exp_f32 v170, v170
v_mfma_f32_16x16x32_bf16 a[216:219], a[40:43], v[252:255], a[216:219]
ds_read_b128 a[40:43], v63 offset:2048
v_cvt_pk_bf16_f32 v240, v160, v161
v_mfma_f32_16x16x32_bf16 a[84:87], a[44:47], v[232:235], a[84:87]
buffer_load_dwordx4 v7, s[4:7], s44 offen offset:3072 sc1 lds
v_mfma_f32_16x16x32_bf16 a[92:95], a[44:47], v[236:239], a[92:95]
v_exp_f32 v171, v171
v_mfma_f32_16x16x32_bf16 a[212:215], a[44:47], v[248:251], a[212:215]
v_exp_f32 v140, v140
v_mfma_f32_16x16x32_bf16 a[220:223], a[44:47], v[252:255], a[220:223]
ds_read_b128 a[44:47], v63 offset:3072
v_cvt_pk_bf16_f32 v225, v130, v131
v_mfma_f32_16x16x32_bf16 a[112:115], a[48:51], v[232:235], a[112:115]
v_exp_f32 v141, v141
v_mfma_f32_16x16x32_bf16 a[120:123], a[48:51], v[236:239], a[120:123]
v_cvt_pk_bf16_f32 v241, v162, v163
v_cvt_pk_bf16_f32 v226, v132, v133
v_mfma_f32_16x16x32_bf16 a[240:243], a[48:51], v[248:251], a[240:243]
v_exp_f32 v172, v172
v_mfma_f32_16x16x32_bf16 a[248:251], a[48:51], v[252:255], a[248:251]
ds_read_b128 a[48:51], v63 offset:4096
v_cvt_pk_bf16_f32 v242, v164, v165
v_mfma_f32_16x16x32_bf16 a[116:119], a[52:55], v[232:235], a[116:119]
s_sub_u32 s55, s55, 1
v_exp_f32 v173, v173
v_mfma_f32_16x16x32_bf16 a[124:127], a[52:55], v[236:239], a[124:127]
v_cvt_pk_bf16_f32 v227, v134, v135
v_cvt_pk_bf16_f32 v243, v166, v167
v_mfma_f32_16x16x32_bf16 a[244:247], a[52:55], v[248:251], a[244:247]
v_exp_f32 v142, v142
v_mfma_f32_16x16x32_bf16 a[252:255], a[52:55], v[252:255], a[252:255]
ds_read_b128 a[52:55], v63 offset:5120
v_cvt_pk_bf16_f32 v228, v136, v137
v_mfma_f32_16x16x32_bf16 a[144:147], a[56:59], v[232:235], a[144:147]
v_exp_f32 v143, v143
v_mfma_f32_16x16x32_bf16 a[152:155], a[56:59], v[236:239], a[152:155]
v_cvt_pk_bf16_f32 v244, v168, v169
v_cvt_pk_bf16_f32 v229, v138, v139
v_mfma_f32_16x16x32_bf16 a[96:99], a[56:59], v[248:251], a[96:99]
v_exp_f32 v174, v174
v_mfma_f32_16x16x32_bf16 a[104:107], a[56:59], v[252:255], a[104:107]
ds_read_b128 a[56:59], v63 offset:6144
v_cvt_pk_bf16_f32 v245, v170, v171
v_mfma_f32_16x16x32_bf16 a[148:151], a[60:63], v[232:235], a[148:151]
v_cvt_pk_bf16_f32 v230, v140, v141
v_cvt_pk_bf16_f32 v246, v172, v173
v_mfma_f32_16x16x32_bf16 a[156:159], a[60:63], v[236:239], a[156:159]
v_exp_f32 v175, v175
v_mfma_f32_16x16x32_bf16 a[100:103], a[60:63], v[248:251], a[100:103]
s_waitcnt  lgkmcnt(7)
v_cvt_pk_bf16_f32 v231, v142, v143
v_cvt_pk_bf16_f32 v247, v174, v175
v_mfma_f32_16x16x32_bf16 a[108:111], a[60:63], v[252:255], a[108:111]
ds_read_b128 a[60:63], v63 offset:7168
s_cbranch_scc1 .Lev_d1
.LD0:
v_mfma_f32_16x16x32_bf16 v[128:131], a[0:3], v[64:67], v[192:195]
s_cmp_lt_u32 s44, s45
v_exp_f32 v144, v144
v_mfma_f32_16x16x32_bf16 v[136:139], a[0:3], v[80:83], v[196:199]
s_cselect_b32 m0, s46, s27
v_exp_f32 v145, v145
v_mfma_f32_16x16x32_bf16 v[160:163], a[0:3], v[96:99], v[200:203]
v_exp_f32 v176, v176
v_mfma_f32_16x16x32_bf16 v[168:171], a[0:3], v[112:115], v[204:207]
ds_read_b128 a[0:3], v62 offset:8192
v_mfma_f32_16x16x32_bf16 v[132:135], a[4:7], v[64:67], v[192:195]
s_sub_u32 s52, s52, 1
s_cselect_b32 s52, 0, s52
s_cselect_b32 s14, s16, 0
v_mfma_f32_16x16x32_bf16 v[140:143], a[4:7], v[80:83], v[196:199]
v_exp_f32 v177, v177
v_mfma_f32_16x16x32_bf16 v[164:167], a[4:7], v[96:99], v[200:203]
v_exp_f32 v146, v146
v_mfma_f32_16x16x32_bf16 v[172:175], a[4:7], v[112:115], v[204:207]
ds_read_b128 a[4:7], v62 offset:12288
v_mfma_f32_16x16x32_bf16 v[128:131], a[8:11], v[68:71], v[128:131]
buffer_load_dwordx4 v7, s[8:11], s44 offen offset:0 sc1 lds
v_mfma_f32_16x16x32_bf16 v[136:139], a[8:11], v[84:87], v[136:139]
s_cmp_lg_u32 s14, 0
s_cselect_b32 s17, 786432, 0
v_mfma_f32_16x16x32_bf16 v[160:163], a[8:11], v[100:103], v[160:163]
v_exp_f32 v147, v147
v_mfma_f32_16x16x32_bf16 v[168:171], a[8:11], v[116:119], v[168:171]
ds_read_b128 a[8:11], v62 offset:9216
v_mfma_f32_16x16x32_bf16 v[132:135], a[12:15], v[68:71], v[132:135]
s_xor_b32 s50, s46, 0x18000
v_exp_f32 v178, v178
v_mfma_f32_16x16x32_bf16 v[140:143], a[12:15], v[84:87], v[140:143]
v_mfma_f32_16x16x32_bf16 v[164:167], a[12:15], v[100:103], v[164:167]
v_exp_f32 v179, v179
v_mfma_f32_16x16x32_bf16 v[172:175], a[12:15], v[116:119], v[172:175]
ds_read_b128 a[12:15], v62 offset:13312
v_mfma_f32_16x16x32_bf16 v[128:131], a[16:19], v[72:75], v[128:131]
v_exp_f32 v148, v148
v_mfma_f32_16x16x32_bf16 v[136:139], a[16:19], v[88:91], v[136:139]
v_mfma_f32_16x16x32_bf16 v[160:163], a[16:19], v[104:107], v[160:163]
v_exp_f32 v149, v149
v_mfma_f32_16x16x32_bf16 v[168:171], a[16:19], v[120:123], v[168:171]
ds_read_b128 a[16:19], v62 offset:10240
v_mfma_f32_16x16x32_bf16 v[132:135], a[20:23], v[72:75], v[132:135]
buffer_load_dwordx4 v7, s[8:11], s44 offen offset:1024 sc1 lds
v_mfma_f32_16x16x32_bf16 v[140:143], a[20:23], v[88:91], v[140:143]
v_exp_f32 v180, v180
v_mfma_f32_16x16x32_bf16 v[164:167], a[20:23], v[104:107], v[164:167]
v_exp_f32 v181, v181
v_mfma_f32_16x16x32_bf16 v[172:175], a[20:23], v[120:123], v[172:175]
ds_read_b128 a[20:23], v62 offset:14336
v_mfma_f32_16x16x32_bf16 v[128:131], a[24:27], v[76:79], v[128:131]
v_mfma_f32_16x16x32_bf16 v[136:139], a[24:27], v[92:95], v[136:139]
v_exp_f32 v150, v150
v_mfma_f32_16x16x32_bf16 v[160:163], a[24:27], v[108:111], v[160:163]
v_exp_f32 v151, v151
v_mfma_f32_16x16x32_bf16 v[168:171], a[24:27], v[124:127], v[168:171]
ds_read_b128 a[24:27], v62 offset:11264
v_mfma_f32_16x16x32_bf16 v[132:135], a[28:31], v[76:79], v[132:135]
v_mfma_f32_16x16x32_bf16 v[140:143], a[28:31], v[92:95], v[140:143]
v_exp_f32 v182, v182
v_mfma_f32_16x16x32_bf16 v[164:167], a[28:31], v[108:111], v[164:167]
v_exp_f32 v183, v183
v_mfma_f32_16x16x32_bf16 v[172:175], a[28:31], v[124:127], v[172:175]
ds_read_b128 a[28:31], v62 offset:15360
v_mfma_f32_16x16x32_bf16 v[208:211], v[52:55], v[224:227], v[208:211]
v_add_u32 v62, s46, v32
v_mfma_f32_16x16x32_bf16 v[212:215], v[52:55], v[228:231], v[212:215]
v_exp_f32 v152, v152
v_mfma_f32_16x16x32_bf16 v[216:219], v[52:55], v[240:243], v[216:219]
v_mfma_f32_16x16x32_bf16 v[220:223], v[52:55], v[244:247], v[220:223]
s_waitcnt lgkmcnt(8)
v_exp_f32 v153, v153
v_mfma_f32_16x16x32_bf16 a[64:67], a[32:35], v[224:227], a[64:67]
buffer_load_dwordx4 v7, s[8:11], s44 offen offset:2048 sc1 lds
v_mfma_f32_16x16x32_bf16 a[72:75], a[32:35], v[228:231], a[72:75]
v_exp_f32 v184, v184
v_mfma_f32_16x16x32_bf16 a[176:179], a[32:35], v[240:243], a[176:179]
v_mfma_f32_16x16x32_bf16 a[184:187], a[32:35], v[244:247], a[184:187]
ds_read_b128 a[32:35], v63 offset:8192
v_mfma_f32_16x16x32_bf16 a[68:71], a[36:39], v[224:227], a[68:71]
v_exp_f32 v185, v185
v_mfma_f32_16x16x32_bf16 a[76:79], a[36:39], v[228:231], a[76:79]
v_cvt_pk_bf16_f32 v232, v144, v145
v_mfma_f32_16x16x32_bf16 a[180:183], a[36:39], v[240:243], a[180:183]
v_exp_f32 v154, v154
v_mfma_f32_16x16x32_bf16 a[188:191], a[36:39], v[244:247], a[188:191]
ds_read_b128 a[36:39], v63 offset:9216
v_cvt_pk_bf16_f32 v248, v176, v177
v_mfma_f32_16x16x32_bf16 a[80:83], a[40:43], v[224:227], a[80:83]
v_exp_f32 v155, v155
v_mfma_f32_16x16x32_bf16 a[88:91], a[40:43], v[228:231], a[88:91]
v_cvt_pk_bf16_f32 v233, v146, v147
v_cvt_pk_bf16_f32 v249, v178, v179
v_mfma_f32_16x16x32_bf16 a[208:211], a[40:43], v[240:243], a[208:211]
v_exp_f32 v186, v186
v_mfma_f32_16x16x32_bf16 a[216:219], a[40:43], v[244:247], a[216:219]
ds_read_b128 a[40:43], v63 offset:10240
v_cvt_pk_bf16_f32 v234, v148, v149
v_mfma_f32_16x16x32_bf16 a[84:87], a[44:47], v[224:227], a[84:87]
buffer_load_dwordx4 v7, s[8:11], s44 offen offset:3072 sc1 lds
v_mfma_f32_16x16x32_bf16 a[92:95], a[44:47], v[228:231], a[92:95]
v_exp_f32 v187, v187
v_mfma_f32_16x16x32_bf16 a[212:215], a[44:47], v[240:243], a[212:215]
v_exp_f32 v156, v156
v_mfma_f32_16x16x32_bf16 a[220:223], a[44:47], v[244:247], a[220:223]
ds_read_b128 a[44:47], v63 offset:11264
v_cvt_pk_bf16_f32 v250, v180, v181
v_mfma_f32_16x16x32_bf16 a[112:115], a[48:51], v[224:227], a[112:115]
s_add_u32 s44, s44, 16384
v_exp_f32 v157, v157
v_mfma_f32_16x16x32_bf16 a[120:123], a[48:51], v[228:231], a[120:123]
v_exp_f32 v188, v188
v_mfma_f32_16x16x32_bf16 a[240:243], a[48:51], v[240:243], a[240:243]
buffer_load_dword v35, v2, s[12:15], s53 offen
v_mfma_f32_16x16x32_bf16 a[248:251], a[48:51], v[244:247], a[248:251]
s_add_u32 s53, s53, s17
ds_read_b128 a[48:51], v63 offset:12288
v_mfma_f32_16x16x32_bf16 a[116:119], a[52:55], v[224:227], a[116:119]
s_sub_u32 s55, s55, 1
v_exp_f32 v189, v189
v_mfma_f32_16x16x32_bf16 a[124:127], a[52:55], v[228:231], a[124:127]
v_cvt_pk_bf16_f32 v235, v150, v151
v_cvt_pk_bf16_f32 v251, v182, v183
v_mfma_f32_16x16x32_bf16 a[244:247], a[52:55], v[240:243], a[244:247]
v_exp_f32 v158, v158
v_mfma_f32_16x16x32_bf16 a[252:255], a[52:55], v[244:247], a[252:255]
ds_read_b128 a[52:55], v63 offset:13312
v_cvt_pk_bf16_f32 v236, v152, v153
v_mfma_f32_16x16x32_bf16 a[144:147], a[56:59], v[224:227], a[144:147]
v_exp_f32 v159, v159
v_mfma_f32_16x16x32_bf16 a[152:155], a[56:59], v[228:231], a[152:155]
v_cvt_pk_bf16_f32 v252, v184, v185
v_cvt_pk_bf16_f32 v237, v154, v155
v_mfma_f32_16x16x32_bf16 a[96:99], a[56:59], v[240:243], a[96:99]
v_exp_f32 v190, v190
v_mfma_f32_16x16x32_bf16 a[104:107], a[56:59], v[244:247], a[104:107]
ds_read_b128 a[56:59], v63 offset:14336
v_cvt_pk_bf16_f32 v253, v186, v187
v_mfma_f32_16x16x32_bf16 a[148:151], a[60:63], v[224:227], a[148:151]
v_cvt_pk_bf16_f32 v238, v156, v157
v_cvt_pk_bf16_f32 v254, v188, v189
v_mfma_f32_16x16x32_bf16 a[156:159], a[60:63], v[228:231], a[156:159]
s_waitcnt vmcnt(10) lgkmcnt(7)
v_exp_f32 v191, v191
v_mfma_f32_16x16x32_bf16 a[100:103], a[60:63], v[240:243], a[100:103]
s_barrier
v_cvt_pk_bf16_f32 v239, v158, v159
v_cvt_pk_bf16_f32 v255, v190, v191
v_mfma_f32_16x16x32_bf16 a[108:111], a[60:63], v[244:247], a[108:111]
ds_read_b128 a[60:63], v63 offset:15360
s_cbranch_scc0 .LD1
s_cmp_eq_u32 s43, s42
s_cbranch_scc1 .LT1
s_swappc_b64 s[30:31], s[58:59]
.Lev_d0_r:
s_branch .LD1
.LT0:
v_mfma_f32_16x16x32_bf16 v[128:131], a[0:3], v[64:67], v[192:195]
s_cmp_lt_u32 s44, s45
v_exp_f32 v144, v144
v_mfma_f32_16x16x32_bf16 v[136:139], a[0:3], v[80:83], v[196:199]
s_cselect_b32 m0, s46, s27
v_cmp_gt_i32 s[32:33], 0, v8
v_nop
v_mfma_f32_16x16x32_bf16 v[160:163], a[0:3], v[96:99], v[36:39]
s_add_u32 s43, s42, 4
v_exp_f32 v145, v145
v_mfma_f32_16x16x32_bf16 v[168:171], a[0:3], v[112:115], v[44:47]
ds_read_b128 a[0:3], v62 offset:8192
v_cmp_gt_i32 s[34:35], 1, v8
v_mfma_f32_16x16x32_bf16 v[132:135], a[4:7], v[64:67], v[192:195]
s_mov_b32 s42, s56
v_exp_f32 v176, v176
v_mfma_f32_16x16x32_bf16 v[140:143], a[4:7], v[80:83], v[196:199]
s_max_u32 s55, s57, s43
v_cndmask_b32 v16, v192, v34, s[32:33]
v_cmp_gt_i32 s[32:33], 2, v8
v_mfma_f32_16x16x32_bf16 v[164:167], a[4:7], v[96:99], v[40:43]
s_min_u32 s55, s55, s42
v_exp_f32 v177, v177
v_mfma_f32_16x16x32_bf16 v[172:175], a[4:7], v[112:115], v[48:51]
ds_read_b128 a[4:7], v62 offset:12288
v_cndmask_b32 v17, v192, v34, s[34:35]
v_mfma_f32_16x16x32_bf16 v[128:131], a[8:11], v[68:71], v[128:131]
buffer_load_dwordx4 v7, s[8:11], s44 offen offset:0 sc1 lds
v_mfma_f32_16x16x32_bf16 v[136:139], a[8:11], v[84:87], v[136:139]
s_sub_u32 s93, s55, s43
v_exp_f32 v146, v146
v_mfma_f32_16x16x32_bf16 v[160:163], a[8:11], v[100:103], v[160:163]
s_mov_b32 s43, s55
v_exp_f32 v147, v147
v_mfma_f32_16x16x32_bf16 v[168:171], a[8:11], v[116:119], v[168:171]
ds_read_b128 a[8:11], v62 offset:9216
v_cmp_gt_i32 s[34:35], 3, v8
v_mfma_f32_16x16x32_bf16 v[132:135], a[12:15], v[68:71], v[132:135]
s_xor_b32 s50, s46, 0x18000
v_exp_f32 v178, v178
v_mfma_f32_16x16x32_bf16 v[140:143], a[12:15], v[84:87], v[140:143]
s_mov_b32 s55, s93
v_cndmask_b32 v18, v192, v34, s[32:33]
v_cmp_gt_i32 s[32:33], 16, v8
v_mfma_f32_16x16x32_bf16 v[164:167], a[12:15], v[100:103], v[164:167]
v_exp_f32 v179, v179
v_mfma_f32_16x16x32_bf16 v[172:175], a[12:15], v[116:119], v[172:175]
ds_read_b128 a[12:15], v62 offset:13312
v_cndmask_b32 v19, v192, v34, s[34:35]
v_mfma_f32_16x16x32_bf16 v[128:131], a[16:19], v[72:75], v[128:131]
v_exp_f32 v148, v148
v_mfma_f32_16x16x32_bf16 v[136:139], a[16:19], v[88:91], v[136:139]
v_cmp_gt_i32 s[34:35], 17, v8
v_cndmask_b32 v20, v192, v34, s[32:33]
v_mfma_f32_16x16x32_bf16 v[160:163], a[16:19], v[104:107], v[160:163]
v_exp_f32 v149, v149
v_mfma_f32_16x16x32_bf16 v[168:171], a[16:19], v[120:123], v[168:171]
ds_read_b128 a[16:19], v62 offset:10240
v_cmp_gt_i32 s[32:33], 18, v8
v_mfma_f32_16x16x32_bf16 v[132:135], a[20:23], v[72:75], v[132:135]
buffer_load_dwordx4 v7, s[8:11], s44 offen offset:1024 sc1 lds
v_mfma_f32_16x16x32_bf16 v[140:143], a[20:23], v[88:91], v[140:143]
v_exp_f32 v180, v180
v_mfma_f32_16x16x32_bf16 v[164:167], a[20:23], v[104:107], v[164:167]
v_exp_f32 v181, v181
v_mfma_f32_16x16x32_bf16 v[172:175], a[20:23], v[120:123], v[172:175]
ds_read_b128 a[20:23], v62 offset:14336
v_cndmask_b32 v21, v192, v34, s[34:35]
v_mfma_f32_16x16x32_bf16 v[128:131], a[24:27], v[76:79], v[128:131]
v_cmp_gt_i32 s[34:35], 19, v8
v_cndmask_b32 v22, v192, v34, s[32:33]
v_mfma_f32_16x16x32_bf16 v[136:139], a[24:27], v[92:95], v[136:139]
v_exp_f32 v150, v150
v_mfma_f32_16x16x32_bf16 v[160:163], a[24:27], v[108:111], v[160:163]
v_exp_f32 v151, v151
v_mfma_f32_16x16x32_bf16 v[168:171], a[24:27], v[124:127], v[168:171]
ds_read_b128 a[24:27], v62 offset:11264
v_cmp_gt_i32 s[32:33], -16, v8
v_mfma_f32_16x16x32_bf16 v[132:135], a[28:31], v[76:79], v[132:135]
v_cndmask_b32 v23, v192, v34, s[34:35]
v_cmp_gt_i32 s[34:35], -15, v8
v_mfma_f32_16x16x32_bf16 v[140:143], a[28:31], v[92:95], v[140:143]
v_exp_f32 v182, v182
v_mfma_f32_16x16x32_bf16 v[164:167], a[28:31], v[108:111], v[164:167]
v_exp_f32 v183, v183
v_mfma_f32_16x16x32_bf16 v[172:175], a[28:31], v[124:127], v[172:175]
ds_read_b128 a[28:31], v62 offset:15360
v_cndmask_b32 v24, v196, v34, s[32:33]
v_mfma_f32_16x16x32_bf16 v[208:211], v[52:55], v[224:227], v[208:211]
v_add_u32 v62, s46, v32
v_cmp_gt_i32 s[32:33], -14, v8
v_mfma_f32_16x16x32_bf16 v[212:215], v[52:55], v[228:231], v[212:215]
v_exp_f32 v152, v152
v_mfma_f32_16x16x32_bf16 v[216:219], v[52:55], v[240:243], v[216:219]
v_cndmask_b32 v25, v196, v34, s[34:35]
v_cmp_gt_i32 s[34:35], -13, v8
v_mfma_f32_16x16x32_bf16 v[220:223], v[52:55], v[244:247], v[220:223]
s_waitcnt lgkmcnt(8)
v_exp_f32 v153, v153
v_mfma_f32_16x16x32_bf16 a[64:67], a[32:35], v[224:227], a[64:67]
buffer_load_dwordx4 v7, s[8:11], s44 offen offset:2048 sc1 lds
v_mfma_f32_16x16x32_bf16 a[72:75], a[32:35], v[228:231], a[72:75]
v_exp_f32 v184, v184
v_mfma_f32_16x16x32_bf16 a[176:179], a[32:35], v[240:243], a[176:179]
v_cndmask_b32 v26, v196, v34, s[32:33]
v_cmp_gt_i32 s[32:33], 0, v8
v_mfma_f32_16x16x32_bf16 a[184:187], a[32:35], v[244:247], a[184:187]
ds_read_b128 a[32:35], v63 offset:8192
v_cndmask_b32 v27, v196, v34, s[34:35]
v_mfma_f32_16x16x32_bf16 a[68:71], a[36:39], v[224:227], a[68:71]
v_exp_f32 v185, v185
v_mfma_f32_16x16x32_bf16 a[76:79], a[36:39], v[228:231], a[76:79]
v_cmp_gt_i32 s[34:35], 1, v8
v_cndmask_b32 v28, v196, v34, s[32:33]
v_mfma_f32_16x16x32_bf16 a[180:183], a[36:39], v[240:243], a[180:183]
v_exp_f32 v154, v154
v_mfma_f32_16x16x32_bf16 a[188:191], a[36:39], v[244:247], a[188:191]
ds_read_b128 a[36:39], v63 offset:9216
v_cmp_gt_i32 s[32:33], 2, v8
v_mfma_f32_16x16x32_bf16 a[80:83], a[40:43], v[224:227], a[80:83]
v_exp_f32 v155, v155
v_mfma_f32_16x16x32_bf16 a[88:91], a[40:43], v[228:231], a[88:91]
v_cvt_pk_bf16_f32 v232, v144, v145
v_cndmask_b32 v29, v196, v34, s[34:35]
v_mfma_f32_16x16x32_bf16 a[208:211], a[40:43], v[240:243], a[208:211]
v_exp_f32 v186, v186
v_mfma_f32_16x16x32_bf16 a[216:219], a[40:43], v[244:247], a[216:219]
ds_read_b128 a[40:43], v63 offset:10240
v_cvt_pk_bf16_f32 v248, v176, v177
v_mfma_f32_16x16x32_bf16 a[84:87], a[44:47], v[224:227], a[84:87]
buffer_load_dwordx4 v7, s[8:11], s44 offen offset:3072 sc1 lds
v_mfma_f32_16x16x32_bf16 a[92:95], a[44:47], v[228:231], a[92:95]
v_exp_f32 v187, v187
v_mfma_f32_16x16x32_bf16 a[212:215], a[44:47], v[240:243], a[212:215]
v_exp_f32 v156, v156
v_mfma_f32_16x16x32_bf16 a[220:223], a[44:47], v[244:247], a[220:223]
ds_read_b128 a[44:47], v63 offset:11264
v_cvt_pk_bf16_f32 v233, v146, v147
v_mfma_f32_16x16x32_bf16 a[112:115], a[48:51], v[224:227], a[112:115]
s_add_u32 s44, s44, 16384
v_exp_f32 v157, v157
v_mfma_f32_16x16x32_bf16 a[120:123], a[48:51], v[228:231], a[120:123]
v_cvt_pk_bf16_f32 v249, v178, v179
v_cvt_pk_bf16_f32 v234, v148, v149
v_mfma_f32_16x16x32_bf16 a[240:243], a[48:51], v[240:243], a[240:243]
v_exp_f32 v188, v188
v_mfma_f32_16x16x32_bf16 a[248:251], a[48:51], v[244:247], a[248:251]
ds_read_b128 a[48:51], v63 offset:12288
v_cvt_pk_bf16_f32 v250, v180, v181
v_mfma_f32_16x16x32_bf16 a[116:119], a[52:55], v[224:227], a[116:119]
v_exp_f32 v189, v189
v_mfma_f32_16x16x32_bf16 a[124:127], a[52:55], v[228:231], a[124:127]
v_cvt_pk_bf16_f32 v235, v150, v151
v_cvt_pk_bf16_f32 v251, v182, v183
v_mfma_f32_16x16x32_bf16 a[244:247], a[52:55], v[240:243], a[244:247]
v_exp_f32 v158, v158
v_mfma_f32_16x16x32_bf16 a[252:255], a[52:55], v[244:247], a[252:255]
ds_read_b128 a[52:55], v63 offset:13312
v_cvt_pk_bf16_f32 v236, v152, v153
v_mfma_f32_16x16x32_bf16 a[144:147], a[56:59], v[224:227], a[144:147]
v_exp_f32 v159, v159
v_cmp_gt_i32 s[34:35], 3, v8
v_mfma_f32_16x16x32_bf16 a[152:155], a[56:59], v[228:231], a[152:155]
v_cvt_pk_bf16_f32 v252, v184, v185
v_cvt_pk_bf16_f32 v237, v154, v155
v_mfma_f32_16x16x32_bf16 a[96:99], a[56:59], v[240:243], a[96:99]
v_exp_f32 v190, v190
v_cndmask_b32 v30, v196, v34, s[32:33]
v_mfma_f32_16x16x32_bf16 a[104:107], a[56:59], v[244:247], a[104:107]
ds_read_b128 a[56:59], v63 offset:14336
v_cvt_pk_bf16_f32 v253, v186, v187
v_mfma_f32_16x16x32_bf16 a[148:151], a[60:63], v[224:227], a[148:151]
v_cvt_pk_bf16_f32 v238, v156, v157
v_cvt_pk_bf16_f32 v254, v188, v189
v_nop
v_mfma_f32_16x16x32_bf16 a[156:159], a[60:63], v[228:231], a[156:159]
s_waitcnt vmcnt(9) lgkmcnt(7)
v_exp_f32 v191, v191
v_mfma_f32_16x16x32_bf16 a[100:103], a[60:63], v[240:243], a[100:103]
s_barrier
v_cvt_pk_bf16_f32 v239, v158, v159
v_cvt_pk_bf16_f32 v255, v190, v191
v_cndmask_b32 v31, v196, v34, s[34:35]
v_mfma_f32_16x16x32_bf16 a[108:111], a[60:63], v[244:247], a[108:111]
ds_read_b128 a[60:63], v63 offset:15360
v_mfma_f32_16x16x32_bf16 v[144:147], a[0:3], v[64:67], v[192:195]
s_cmp_lt_u32 s44, s54
v_exp_f32 v128, v128
v_mfma_f32_16x16x32_bf16 v[152:155], a[0:3], v[80:83], v[196:199]
s_cselect_b32 m0, s50, s27
v_exp_f32 v129, v129
v_mfma_f32_16x16x32_bf16 v[148:151], a[4:7], v[64:67], v[192:195]
s_and_b32 s36, s86, 1
s_lshl_b32 s36, s36, 3
v_add_u32 v63, s49, v33
v_mfma_f32_16x16x32_bf16 v[156:159], a[4:7], v[80:83], v[196:199]
ds_read_b128 a[0:3], v62 offset:0
ds_read_b128 a[4:7], v62 offset:4096
.Lpad11:
s_cbranch_execz .Ltch3
v_mfma_f32_16x16x32_bf16 v[144:147], a[8:11], v[68:71], v[144:147]
s_and_b32 s28, s87, 7
s_or_b32 s36, s36, s28
v_exp_f32 v160, v160
v_mfma_f32_16x16x32_bf16 v[152:155], a[8:11], v[84:87], v[152:155]
ds_read_b128 a[8:11], v62 offset:1024
v_exp_f32 v161, v161
v_mfma_f32_16x16x32_bf16 v[148:151], a[12:15], v[68:71], v[148:151]
buffer_load_dwordx4 v7, s[4:7], s44 offen offset:0 sc1 lds
v_mfma_f32_16x16x32_bf16 v[156:159], a[12:15], v[84:87], v[156:159]
ds_read_b128 a[12:15], v62 offset:5120
v_exp_f32 v130, v130
v_mfma_f32_16x16x32_bf16 v[144:147], a[16:19], v[72:75], v[144:147]
s_lshr_b32 s37, s86, 1
s_lshl_b32 s37, s37, 2
v_exp_f32 v131, v131
v_mfma_f32_16x16x32_bf16 v[152:155], a[16:19], v[88:91], v[152:155]
s_mov_b32 s49, s46
v_exp_f32 v162, v162
v_mfma_f32_16x16x32_bf16 v[148:151], a[20:23], v[72:75], v[148:151]
s_lshr_b32 s28, s87, 3
s_or_b32 s37, s37, s28
v_exp_f32 v163, v163
v_mfma_f32_16x16x32_bf16 v[156:159], a[20:23], v[88:91], v[156:159]
ds_read_b128 a[16:19], v62 offset:2048
ds_read_b128 a[20:23], v62 offset:6144
v_mfma_f32_16x16x32_bf16 v[144:147], a[24:27], v[76:79], v[144:147]
s_lshl_b32 s24, s36, 18
s_lshl_b32 s28, s3, 16
v_exp_f32 v132, v132
v_mfma_f32_16x16x32_bf16 v[152:155], a[24:27], v[92:95], v[152:155]
ds_read_b128 a[24:27], v62 offset:3072
v_cvt_pk_bf16_f32 v224, v128, v129
v_mfma_f32_16x16x32_bf16 v[148:151], a[28:31], v[76:79], v[148:151]
s_add_u32 s46, s46, 16384
v_exp_f32 v133, v133
v_mfma_f32_16x16x32_bf16 v[156:159], a[28:31], v[92:95], v[156:159]
ds_read_b128 a[28:31], v62 offset:7168
v_exp_f32 v164, v164
v_mfma_f32_16x16x32_bf16 v[208:211], v[52:55], v[232:235], v[208:211]
buffer_load_dwordx4 v7, s[4:7], s44 offen offset:1024 sc1 lds
v_mfma_f32_16x16x32_bf16 v[212:215], v[52:55], v[236:239], v[212:215]
s_add_u32 s24, s24, s28
s_mov_b32 s25, 0
v_exp_f32 v165, v165
v_mfma_f32_16x16x32_bf16 v[216:219], v[52:55], v[248:251], v[216:219]
s_mul_i32 s26, s3, 98304
s_lshl_b32 s28, s37, 7
v_exp_f32 v134, v134
v_mfma_f32_16x16x32_bf16 v[220:223], v[52:55], v[252:255], v[220:223]
s_waitcnt lgkmcnt(8)
s_and_b32 s46, s46, 0xffff
v_cvt_pk_bf16_f32 v240, v160, v161
v_mfma_f32_16x16x32_bf16 a[64:67], a[32:35], v[232:235], a[64:67]
s_add_u32 s26, s26, s28
s_add_u32 s47, s26, 4096
v_exp_f32 v135, v135
v_mfma_f32_16x16x32_bf16 a[72:75], a[32:35], v[236:239], a[72:75]
s_mul_i32 s48, s3, 7168
s_mov_b32 s12, s64
v_cvt_pk_bf16_f32 v225, v130, v131
v_mfma_f32_16x16x32_bf16 a[176:179], a[32:35], v[248:251], a[176:179]
s_and_b32 s13, s65, 0xffff
s_mov_b32 s14, 0x400000
v_exp_f32 v166, v166
v_mfma_f32_16x16x32_bf16 a[184:187], a[32:35], v[252:255], a[184:187]
ds_read_b128 a[32:35], v63 offset:0
v_exp_f32 v167, v167
v_mfma_f32_16x16x32_bf16 a[68:71], a[36:39], v[232:235], a[68:71]
s_mov_b32 s16, s66
s_and_b32 s17, s67, 0xffff
v_cvt_pk_bf16_f32 v241, v162, v163
v_mfma_f32_16x16x32_bf16 a[76:79], a[36:39], v[236:239], a[76:79]
s_mov_b32 s18, 0x600000
s_lshl_b32 s29, s36, 18
v_exp_f32 v136, v136
v_mfma_f32_16x16x32_bf16 a[180:183], a[36:39], v[248:251], a[180:183]
s_lshl_b32 s28, s3, 13
s_add_u32 s29, s29, s28
v_exp_f32 v137, v137
v_mfma_f32_16x16x32_bf16 a[188:191], a[36:39], v[252:255], a[188:191]
ds_read_b128 a[36:39], v63 offset:1024
v_cvt_pk_bf16_f32 v226, v132, v133
v_mfma_f32_16x16x32_bf16 a[80:83], a[40:43], v[232:235], a[80:83]
buffer_load_dwordx4 v7, s[4:7], s44 offen offset:2048 sc1 lds
v_mfma_f32_16x16x32_bf16 a[88:91], a[40:43], v[236:239], a[88:91]
s_sub_u32 s29, s29, 34816
v_exp_f32 v168, v168
v_mfma_f32_16x16x32_bf16 a[208:211], a[40:43], v[248:251], a[208:211]
s_cmp_eq_u32 s36, 0
s_cselect_b32 s29, 0x7f000000, s29
v_exp_f32 v169, v169
v_mfma_f32_16x16x32_bf16 a[216:219], a[40:43], v[252:255], a[216:219]
ds_read_b128 a[40:43], v63 offset:2048
v_exp_f32 v138, v138
v_mfma_f32_16x16x32_bf16 a[84:87], a[44:47], v[232:235], a[84:87]
s_lshl_b32 s32, s3, 2
s_and_b32 s33, s3, 1
v_cvt_pk_bf16_f32 v242, v164, v165
v_mfma_f32_16x16x32_bf16 a[92:95], a[44:47], v[236:239], a[92:95]
s_lshl_b32 s33, s33, 1
s_lshr_b32 s34, s3, 1
v_exp_f32 v139, v139
v_mfma_f32_16x16x32_bf16 a[212:215], a[44:47], v[248:251], a[212:215]
s_lshl_b32 s35, s34, 13
s_add_u32 s35, s35, 65536
v_exp_f32 v170, v170
v_mfma_f32_16x16x32_bf16 a[220:223], a[44:47], v[252:255], a[220:223]
ds_read_b128 a[44:47], v63 offset:3072
v_cvt_pk_bf16_f32 v227, v134, v135
v_mfma_f32_16x16x32_bf16 a[112:115], a[48:51], v[232:235], a[112:115]
buffer_load_dwordx4 v7, s[4:7], s44 offen offset:3072 sc1 lds
v_mfma_f32_16x16x32_bf16 a[120:123], a[48:51], v[236:239], a[120:123]
s_mov_b32 s38, 131072
s_lshl_b32 s28, s88, 13
v_exp_f32 v171, v171
v_mfma_f32_16x16x32_bf16 a[240:243], a[48:51], v[248:251], a[240:243]
s_add_u32 s28, s28, 33554432
v_exp_f32 v140, v140
v_mfma_f32_16x16x32_bf16 a[248:251], a[48:51], v[252:255], a[248:251]
ds_read_b128 a[48:51], v63 offset:4096
v_exp_f32 v141, v141
v_mfma_f32_16x16x32_bf16 a[116:119], a[52:55], v[232:235], a[116:119]
s_add_u32 s20, s84, s28
s_addc_u32 s21, s85, 0
v_cvt_pk_bf16_f32 v243, v166, v167
v_mfma_f32_16x16x32_bf16 a[124:127], a[52:55], v[236:239], a[124:127]
s_and_b32 s21, s21, 0xffff
s_mov_b32 s22, 16777216
v_exp_f32 v172, v172
v_mfma_f32_16x16x32_bf16 a[244:247], a[52:55], v[248:251], a[244:247]
s_sub_u32 s40, 15, s89
s_lshl_b32 s40, s40, 2
v_exp_f32 v173, v173
v_mfma_f32_16x16x32_bf16 a[252:255], a[52:55], v[252:255], a[252:255]
ds_read_b128 a[52:55], v63 offset:5120
v_cvt_pk_bf16_f32 v228, v136, v137
v_mfma_f32_16x16x32_bf16 a[144:147], a[56:59], v[232:235], a[144:147]
s_add_u32 s40, s40, s3
s_lshl_b32 s40, s40, 18
v_exp_f32 v142, v142
v_mfma_f32_16x16x32_bf16 a[152:155], a[56:59], v[236:239], a[152:155]
s_lshl_b32 s41, s89, 2
s_add_u32 s41, s41, s94
v_cvt_pk_bf16_f32 v244, v168, v169
v_mfma_f32_16x16x32_bf16 a[96:99], a[56:59], v[248:251], a[96:99]
s_lshl_b32 s41, s41, 18
s_mov_b32 s28, 1602224128
v_exp_f32 v143, v143
v_mfma_f32_16x16x32_bf16 a[104:107], a[56:59], v[252:255], a[104:107]
ds_read_b128 a[56:59], v63 offset:6144
v_exp_f32 v174, v174
v_mfma_f32_16x16x32_bf16 a[148:151], a[60:63], v[232:235], a[148:151]
v_cvt_pk_bf16_f32 v229, v138, v139
v_cvt_pk_bf16_f32 v245, v170, v171
v_mfma_f32_16x16x32_bf16 a[156:159], a[60:63], v[236:239], a[156:159]
v_cvt_pk_bf16_f32 v230, v140, v141
v_exp_f32 v175, v175
v_mfma_f32_16x16x32_bf16 a[100:103], a[60:63], v[248:251], a[100:103]
s_waitcnt  lgkmcnt(7)
v_cvt_pk_bf16_f32 v246, v172, v173
v_cvt_pk_bf16_f32 v231, v142, v143
v_cvt_pk_bf16_f32 v247, v174, v175
v_mfma_f32_16x16x32_bf16 a[108:111], a[60:63], v[252:255], a[108:111]
ds_read_b128 a[60:63], v63 offset:7168
v_mfma_f32_16x16x32_bf16 v[128:131], a[0:3], v[64:67], v[192:195]
s_cmp_lt_u32 s44, s45
v_exp_f32 v144, v144
v_mfma_f32_16x16x32_bf16 v[136:139], a[0:3], v[80:83], v[196:199]
s_cselect_b32 m0, s46, s27
v_lshrrev_b32 v56, 3, v1
v_and_b32 v57, 7, v1
v_mfma_f32_16x16x32_bf16 v[132:135], a[4:7], v[64:67], v[192:195]
v_xor_b32 v58, v57, v56
v_lshlrev_b32 v58, 4, v58
v_mfma_f32_16x16x32_bf16 v[140:143], a[4:7], v[80:83], v[196:199]
ds_read_b128 a[0:3], v62 offset:8192
ds_read_b128 a[4:7], v62 offset:12288
v_lshl_add_u32 v36, v56, 11, v58
v_mfma_f32_16x16x32_bf16 v[128:131], a[8:11], v[68:71], v[128:131]
v_exp_f32 v145, v145
v_add_u32 v37, 15360, v36
v_mfma_f32_16x16x32_bf16 v[136:139], a[8:11], v[84:87], v[136:139]
ds_read_b128 a[8:11], v62 offset:9216
v_add_u32 v38, 30720, v36
v_mfma_f32_16x16x32_bf16 v[132:135], a[12:15], v[68:71], v[132:135]
buffer_load_dwordx4 v7, s[8:11], s44 offen offset:0 sc1 lds
v_add_u32 v39, 46080, v36
v_mfma_f32_16x16x32_bf16 v[140:143], a[12:15], v[84:87], v[140:143]
ds_read_b128 a[12:15], v62 offset:13312
v_add_u32 v58, s32, v56
v_mfma_f32_16x16x32_bf16 v[128:131], a[16:19], v[72:75], v[128:131]
v_exp_f32 v146, v146
v_xor_b32 v58, v58, v57
v_mfma_f32_16x16x32_bf16 v[136:139], a[16:19], v[88:91], v[136:139]
s_xor_b32 s50, s46, 0x18000
v_and_b32 v58, 7, v58
v_lshlrev_b32 v58, 4, v58
v_mfma_f32_16x16x32_bf16 v[132:135], a[20:23], v[72:75], v[132:135]
v_exp_f32 v147, v147
v_lshl_add_u32 v58, v56, 11, v58
v_mfma_f32_16x16x32_bf16 v[140:143], a[20:23], v[88:91], v[140:143]
ds_read_b128 a[16:19], v62 offset:10240
ds_read_b128 a[20:23], v62 offset:14336
v_add_u32 v58, s29, v58
v_mfma_f32_16x16x32_bf16 v[128:131], a[24:27], v[76:79], v[128:131]
v_exp_f32 v148, v148
v_and_b32 v59, 32, v1
v_mfma_f32_16x16x32_bf16 v[136:139], a[24:27], v[92:95], v[136:139]
ds_read_b128 a[24:27], v62 offset:11264
v_lshlrev_b32 v59, 25, v59
v_mfma_f32_16x16x32_bf16 v[132:135], a[28:31], v[76:79], v[132:135]
v_or_b32 v42, v59, v58
v_bfe_u32 v58, v1, 4, 1
v_mfma_f32_16x16x32_bf16 v[140:143], a[28:31], v[92:95], v[140:143]
ds_read_b128 a[28:31], v62 offset:15360
v_lshlrev_b32 v58, 1, v58
v_mfma_f32_16x16x32_bf16 v[208:211], v[52:55], v[224:227], v[208:211]
buffer_load_dwordx4 v7, s[8:11], s44 offen offset:1024 sc1 lds
v_add_u32 v62, s46, v32
v_mfma_f32_16x16x32_bf16 v[212:215], v[52:55], v[228:231], v[212:215]
v_exp_f32 v149, v149
v_xor_b32 v58, v58, v57
v_mfma_f32_16x16x32_bf16 v[216:219], v[52:55], v[240:243], v[216:219]
v_lshlrev_b32 v58, 4, v58
v_mul_u32_u24 v59, 6144, v56
v_mfma_f32_16x16x32_bf16 v[220:223], v[52:55], v[244:247], v[220:223]
s_waitcnt lgkmcnt(8)
v_exp_f32 v150, v150
v_add_u32 v40, v59, v58
v_mfma_f32_16x16x32_bf16 a[64:67], a[32:35], v[224:227], a[64:67]
v_xor_b32 v41, 64, v40
v_add_u32 v41, 48128, v41
v_mfma_f32_16x16x32_bf16 a[72:75], a[32:35], v[228:231], a[72:75]
v_and_b32 v56, 15, v1
v_lshrrev_b32 v57, 4, v1
v_mfma_f32_16x16x32_bf16 a[176:179], a[32:35], v[240:243], a[176:179]
v_exp_f32 v151, v151
v_mfma_f32_16x16x32_bf16 a[184:187], a[32:35], v[244:247], a[184:187]
ds_read_b128 a[32:35], v63 offset:8192
v_and_b32 v58, 7, v56
v_mfma_f32_16x16x32_bf16 a[68:71], a[36:39], v[224:227], a[68:71]
v_xor_b32 v58, v58, v57
v_lshlrev_b32 v58, 4, v58
v_mfma_f32_16x16x32_bf16 a[76:79], a[36:39], v[228:231], a[76:79]
v_lshl_add_u32 v43, v56, 7, v58
v_add_u32 v43, s35, v43
v_mfma_f32_16x16x32_bf16 a[180:183], a[36:39], v[240:243], a[180:183]
v_exp_f32 v152, v152
v_mfma_f32_16x16x32_bf16 a[188:191], a[36:39], v[244:247], a[188:191]
ds_read_b128 a[36:39], v63 offset:9216
v_xor_b32 v44, 64, v43
v_mfma_f32_16x16x32_bf16 a[80:83], a[40:43], v[224:227], a[80:83]
buffer_load_dwordx4 v7, s[8:11], s44 offen offset:2048 sc1 lds
v_and_b32 v56, 15, v1
v_mfma_f32_16x16x32_bf16 a[88:91], a[40:43], v[228:231], a[88:91]
v_exp_f32 v153, v153
v_lshrrev_b32 v57, 2, v56
v_mfma_f32_16x16x32_bf16 a[208:211], a[40:43], v[240:243], a[208:211]
v_mul_u32_u24 v57, 6656, v57
v_add_u32 v57, 71680, v57
v_add_f32 v13, v216, v220
v_mfma_f32_16x16x32_bf16 a[216:219], a[40:43], v[244:247], a[216:219]
ds_read_b128 a[40:43], v63 offset:10240
v_mov_b32 v58, -2048
v_mfma_f32_16x16x32_bf16 a[84:87], a[44:47], v[224:227], a[84:87]
v_exp_f32 v154, v154
v_mfma_f32_16x16x32_bf16 a[92:95], a[44:47], v[228:231], a[92:95]
v_mov_b32 v59, s34
v_cmp_eq_u32 vcc, 0, v59
v_mfma_f32_16x16x32_bf16 a[212:215], a[44:47], v[240:243], a[212:215]
v_exp_f32 v155, v155
v_nop
v_mfma_f32_16x16x32_bf16 a[220:223], a[44:47], v[244:247], a[220:223]
ds_read_b128 a[44:47], v63 offset:11264
v_nop
v_mfma_f32_16x16x32_bf16 a[112:115], a[48:51], v[224:227], a[112:115]
buffer_load_dwordx4 v7, s[8:11], s44 offen offset:3072 sc1 lds
v_cndmask_b32 v57, v58, v57, vcc
v_mfma_f32_16x16x32_bf16 a[120:123], a[48:51], v[228:231], a[120:123]
v_add_u32 v45, v57, v43
v_add_u32 v46, v57, v44
v_mfma_f32_16x16x32_bf16 a[240:243], a[48:51], v[240:243], a[240:243]
v_exp_f32 v156, v156
v_mfma_f32_16x16x32_bf16 a[248:251], a[48:51], v[244:247], a[248:251]
ds_read_b128 a[48:51], v63 offset:12288
v_bfe_u32 v56, v1, 2, 2
v_mfma_f32_16x16x32_bf16 a[116:119], a[52:55], v[224:227], a[116:119]
s_add_u32 s44, s44, 16384
v_bfe_u32 v57, v1, 4, 1
v_lshl_add_u32 v58, v57, 3, v56
v_mfma_f32_16x16x32_bf16 a[124:127], a[52:55], v[228:231], a[124:127]
v_lshlrev_b32 v58, 7, v58
v_lshrrev_b32 v59, 5, v1
v_mfma_f32_16x16x32_bf16 a[244:247], a[52:55], v[240:243], a[244:247]
v_exp_f32 v157, v157
v_mfma_f32_16x16x32_bf16 a[252:255], a[52:55], v[244:247], a[252:255]
ds_read_b128 a[52:55], v63 offset:13312
v_mul_u32_u24 v59, 7168, v59
v_add3_u32 v58, v58, v59, s38
v_mfma_f32_16x16x32_bf16 a[144:147], a[56:59], v[224:227], a[144:147]
v_cvt_pk_bf16_f32 v232, v144, v145
v_lshrrev_b32 v56, 1, v56
v_cmp_nge_f32 s[52:53], s28, v13
v_mfma_f32_16x16x32_bf16 a[152:155], a[56:59], v[228:231], a[152:155]
v_exp_f32 v158, v158
v_lshl_or_b32 v56, v57, 1, v56
v_mfma_f32_16x16x32_bf16 a[96:99], a[56:59], v[240:243], a[96:99]
v_cvt_pk_bf16_f32 v233, v146, v147
v_cvt_pk_bf16_f32 v234, v148, v149
v_xor_b32 v56, s33, v56
v_mfma_f32_16x16x32_bf16 a[104:107], a[56:59], v[244:247], a[104:107]
ds_read_b128 a[56:59], v63 offset:14336
v_cvt_pk_bf16_f32 v235, v150, v151
v_lshl_add_u32 v58, v56, 5, v58
v_mfma_f32_16x16x32_bf16 a[148:151], a[60:63], v[224:227], a[148:151]
v_exp_f32 v159, v159
v_and_b32 v56, 3, v1
v_lshl_add_u32 v47, v56, 3, v58
v_mfma_f32_16x16x32_bf16 a[156:159], a[60:63], v[228:231], a[156:159]
s_waitcnt vmcnt(8) lgkmcnt(7)
v_cvt_pk_bf16_f32 v236, v152, v153
v_cvt_pk_bf16_f32 v237, v154, v155
v_xor_b32 v48, 32, v47
v_mfma_f32_16x16x32_bf16 a[100:103], a[60:63], v[240:243], a[100:103]
s_barrier
v_cvt_pk_bf16_f32 v238, v156, v157
v_cvt_pk_bf16_f32 v239, v158, v159
v_add_u32 v51, 4096, v7
v_mfma_f32_16x16x32_bf16 a[108:111], a[60:63], v[244:247], a[108:111]
ds_read_b128 a[60:63], v63 offset:15360
s_cmp_le_u32 s94, 3
s_cbranch_scc1 .LTf0
.LTc0:
s_cmp_lt_i32 s55, 0
s_cbranch_scc1 .LE1
s_branch .LS1
.LTf0:
s_cmp_lg_u64 s[52:53], 0
s_cbranch_scc1 .LTc0
s_getpc_b64 s[30:31]
.LTp0:
s_sub_u32 s30, s30, .LTp0-.LTc0
s_subb_u32 s31, s31, 0
s_branch .Lfb
.LT1_pad:
s_cbranch_execz .Ltch4
.LT1:
v_mfma_f32_16x16x32_bf16 v[144:147], a[0:3], v[64:67], v[192:195]
s_cmp_lt_u32 s44, s54
v_exp_f32 v128, v128
v_mfma_f32_16x16x32_bf16 v[152:155], a[0:3], v[80:83], v[196:199]
s_cselect_b32 m0, s50, s27
v_exp_f32 v129, v129
v_mfma_f32_16x16x32_bf16 v[176:179], a[0:3], v[96:99], v[36:39]
s_add_u32 s43, s42, 4
v_add_u32 v63, s49, v33
v_cmp_gt_i32 s[32:33], 0, v8
v_mfma_f32_16x16x32_bf16 v[184:187], a[0:3], v[112:115], v[44:47]
ds_read_b128 a[0:3], v62 offset:0
v_nop
v_mfma_f32_16x16x32_bf16 v[148:151], a[4:7], v[64:67], v[192:195]
s_mov_b32 s42, s56
v_exp_f32 v160, v160
v_mfma_f32_16x16x32_bf16 v[156:159], a[4:7], v[80:83], v[196:199]
s_max_u32 s55, s57, s43
v_cmp_gt_i32 s[34:35], 1, v8
v_cndmask_b32 v16, v192, v34, s[32:33]
v_mfma_f32_16x16x32_bf16 v[180:183], a[4:7], v[96:99], v[40:43]
s_min_u32 s55, s55, s42
v_exp_f32 v161, v161
v_mfma_f32_16x16x32_bf16 v[188:191], a[4:7], v[112:115], v[48:51]
ds_read_b128 a[4:7], v62 offset:4096
v_cmp_gt_i32 s[32:33], 2, v8
v_mfma_f32_16x16x32_bf16 v[144:147], a[8:11], v[68:71], v[144:147]
buffer_load_dwordx4 v7, s[4:7], s44 offen offset:0 sc1 lds
v_mfma_f32_16x16x32_bf16 v[152:155], a[8:11], v[84:87], v[152:155]
s_sub_u32 s93, s55, s43
v_exp_f32 v130, v130
v_mfma_f32_16x16x32_bf16 v[176:179], a[8:11], v[100:103], v[176:179]
s_mov_b32 s43, s55
v_exp_f32 v131, v131
v_mfma_f32_16x16x32_bf16 v[184:187], a[8:11], v[116:119], v[184:187]
ds_read_b128 a[8:11], v62 offset:1024
v_cndmask_b32 v17, v192, v34, s[34:35]
v_mfma_f32_16x16x32_bf16 v[148:151], a[12:15], v[68:71], v[148:151]
s_mov_b32 s49, s46
v_exp_f32 v162, v162
v_mfma_f32_16x16x32_bf16 v[156:159], a[12:15], v[84:87], v[156:159]
s_mov_b32 s55, s93
v_cmp_gt_i32 s[34:35], 3, v8
v_cndmask_b32 v18, v192, v34, s[32:33]
v_mfma_f32_16x16x32_bf16 v[180:183], a[12:15], v[100:103], v[180:183]
v_exp_f32 v163, v163
v_mfma_f32_16x16x32_bf16 v[188:191], a[12:15], v[116:119], v[188:191]
ds_read_b128 a[12:15], v62 offset:5120
v_cmp_gt_i32 s[32:33], 16, v8
v_mfma_f32_16x16x32_bf16 v[144:147], a[16:19], v[72:75], v[144:147]
v_exp_f32 v132, v132
v_mfma_f32_16x16x32_bf16 v[152:155], a[16:19], v[88:91], v[152:155]
v_cndmask_b32 v19, v192, v34, s[34:35]
v_cmp_gt_i32 s[34:35], 17, v8
v_mfma_f32_16x16x32_bf16 v[176:179], a[16:19], v[104:107], v[176:179]
s_add_u32 s46, s46, 16384
v_exp_f32 v133, v133
v_mfma_f32_16x16x32_bf16 v[184:187], a[16:19], v[120:123], v[184:187]
ds_read_b128 a[16:19], v62 offset:2048
v_cndmask_b32 v20, v192, v34, s[32:33]
v_mfma_f32_16x16x32_bf16 v[148:151], a[20:23], v[72:75], v[148:151]
buffer_load_dwordx4 v7, s[4:7], s44 offen offset:1024 sc1 lds
v_mfma_f32_16x16x32_bf16 v[156:159], a[20:23], v[88:91], v[156:159]
v_exp_f32 v164, v164
v_mfma_f32_16x16x32_bf16 v[180:183], a[20:23], v[104:107], v[180:183]
v_exp_f32 v165, v165
v_mfma_f32_16x16x32_bf16 v[188:191], a[20:23], v[120:123], v[188:191]
ds_read_b128 a[20:23], v62 offset:6144
v_cmp_gt_i32 s[32:33], 18, v8
v_mfma_f32_16x16x32_bf16 v[144:147], a[24:27], v[76:79], v[144:147]
v_cndmask_b32 v21, v192, v34, s[34:35]
v_cmp_gt_i32 s[34:35], 19, v8
v_mfma_f32_16x16x32_bf16 v[152:155], a[24:27], v[92:95], v[152:155]
v_exp_f32 v134, v134
v_mfma_f32_16x16x32_bf16 v[176:179], a[24:27], v[108:111], v[176:179]
s_and_b32 s46, s46, 0xffff
v_exp_f32 v135, v135
v_mfma_f32_16x16x32_bf16 v[184:187], a[24:27], v[124:127], v[184:187]
ds_read_b128 a[24:27], v62 offset:3072
v_cndmask_b32 v22, v192, v34, s[32:33]
v_mfma_f32_16x16x32_bf16 v[148:151], a[28:31], v[76:79], v[148:151]
v_cmp_gt_i32 s[32:33], -16, v8
v_cndmask_b32 v23, v192, v34, s[34:35]
v_mfma_f32_16x16x32_bf16 v[156:159], a[28:31], v[92:95], v[156:159]
v_exp_f32 v166, v166
v_mfma_f32_16x16x32_bf16 v[180:183], a[28:31], v[108:111], v[180:183]
v_cmp_gt_i32 s[34:35], -15, v8
v_cndmask_b32 v24, v196, v34, s[32:33]
v_mfma_f32_16x16x32_bf16 v[188:191], a[28:31], v[124:127], v[188:191]
ds_read_b128 a[28:31], v62 offset:7168
v_cmp_gt_i32 s[32:33], -14, v8
v_mfma_f32_16x16x32_bf16 v[208:211], v[52:55], v[232:235], v[208:211]
v_exp_f32 v167, v167
v_mfma_f32_16x16x32_bf16 v[212:215], v[52:55], v[236:239], v[212:215]
v_exp_f32 v136, v136
v_mfma_f32_16x16x32_bf16 v[216:219], v[52:55], v[248:251], v[216:219]
v_cndmask_b32 v25, v196, v34, s[34:35]
v_cmp_gt_i32 s[34:35], -13, v8
v_mfma_f32_16x16x32_bf16 v[220:223], v[52:55], v[252:255], v[220:223]
s_waitcnt lgkmcnt(8)
v_exp_f32 v137, v137
v_mfma_f32_16x16x32_bf16 a[64:67], a[32:35], v[232:235], a[64:67]
buffer_load_dwordx4 v7, s[4:7], s44 offen offset:2048 sc1 lds
v_mfma_f32_16x16x32_bf16 a[72:75], a[32:35], v[236:239], a[72:75]
v_exp_f32 v168, v168
v_mfma_f32_16x16x32_bf16 a[176:179], a[32:35], v[248:251], a[176:179]
v_cndmask_b32 v26, v196, v34, s[32:33]
v_cmp_gt_i32 s[32:33], 0, v8
v_mfma_f32_16x16x32_bf16 a[184:187], a[32:35], v[252:255], a[184:187]
ds_read_b128 a[32:35], v63 offset:0
v_cndmask_b32 v27, v196, v34, s[34:35]
v_mfma_f32_16x16x32_bf16 a[68:71], a[36:39], v[232:235], a[68:71]
v_exp_f32 v169, v169
v_mfma_f32_16x16x32_bf16 a[76:79], a[36:39], v[236:239], a[76:79]
v_cmp_gt_i32 s[34:35], 1, v8
v_cndmask_b32 v28, v196, v34, s[32:33]
v_mfma_f32_16x16x32_bf16 a[180:183], a[36:39], v[248:251], a[180:183]
v_exp_f32 v138, v138
v_mfma_f32_16x16x32_bf16 a[188:191], a[36:39], v[252:255], a[188:191]
ds_read_b128 a[36:39], v63 offset:1024
v_cmp_gt_i32 s[32:33], 2, v8
v_mfma_f32_16x16x32_bf16 a[80:83], a[40:43], v[232:235], a[80:83]
v_exp_f32 v139, v139
v_mfma_f32_16x16x32_bf16 a[88:91], a[40:43], v[236:239], a[88:91]
v_cvt_pk_bf16_f32 v224, v128, v129
v_cndmask_b32 v29, v196, v34, s[34:35]
v_mfma_f32_16x16x32_bf16 a[208:211], a[40:43], v[248:251], a[208:211]
v_exp_f32 v170, v170
v_mfma_f32_16x16x32_bf16 a[216:219], a[40:43], v[252:255], a[216:219]
ds_read_b128 a[40:43], v63 offset:2048
v_cvt_pk_bf16_f32 v240, v160, v161
v_mfma_f32_16x16x32_bf16 a[84:87], a[44:47], v[232:235], a[84:87]
buffer_load_dwordx4 v7, s[4:7], s44 offen offset:3072 sc1 lds
v_mfma_f32_16x16x32_bf16 a[92:95], a[44:47], v[236:239], a[92:95]
v_exp_f32 v171, v171
v_mfma_f32_16x16x32_bf16 a[212:215], a[44:47], v[248:251], a[212:215]
v_exp_f32 v140, v140
v_mfma_f32_16x16x32_bf16 a[220:223], a[44:47], v[252:255], a[220:223]
ds_read_b128 a[44:47], v63 offset:3072
v_cvt_pk_bf16_f32 v225, v130, v131
v_mfma_f32_16x16x32_bf16 a[112:115], a[48:51], v[232:235], a[112:115]
v_exp_f32 v141, v141
v_mfma_f32_16x16x32_bf16 a[120:123], a[48:51], v[236:239], a[120:123]
v_cvt_pk_bf16_f32 v241, v162, v163
v_cvt_pk_bf16_f32 v226, v132, v133
v_mfma_f32_16x16x32_bf16 a[240:243], a[48:51], v[248:251], a[240:243]
v_exp_f32 v172, v172
v_mfma_f32_16x16x32_bf16 a[248:251], a[48:51], v[252:255], a[248:251]
ds_read_b128 a[48:51], v63 offset:4096
v_cvt_pk_bf16_f32 v242, v164, v165
v_mfma_f32_16x16x32_bf16 a[116:119], a[52:55], v[232:235], a[116:119]
v_exp_f32 v173, v173
v_mfma_f32_16x16x32_bf16 a[124:127], a[52:55], v[236:239], a[124:127]
v_cvt_pk_bf16_f32 v227, v134, v135
v_cvt_pk_bf16_f32 v243, v166, v167
v_mfma_f32_16x16x32_bf16 a[244:247], a[52:55], v[248:251], a[244:247]
v_exp_f32 v142, v142
v_mfma_f32_16x16x32_bf16 a[252:255], a[52:55], v[252:255], a[252:255]
ds_read_b128 a[52:55], v63 offset:5120
v_cvt_pk_bf16_f32 v228, v136, v137
v_mfma_f32_16x16x32_bf16 a[144:147], a[56:59], v[232:235], a[144:147]
v_exp_f32 v143, v143
v_cmp_gt_i32 s[34:35], 3, v8
v_mfma_f32_16x16x32_bf16 a[152:155], a[56:59], v[236:239], a[152:155]
v_cvt_pk_bf16_f32 v244, v168, v169
v_cvt_pk_bf16_f32 v229, v138, v139
v_mfma_f32_16x16x32_bf16 a[96:99], a[56:59], v[248:251], a[96:99]
v_exp_f32 v174, v174
v_cndmask_b32 v30, v196, v34, s[32:33]
v_mfma_f32_16x16x32_bf16 a[104:107], a[56:59], v[252:255], a[104:107]
ds_read_b128 a[56:59], v63 offset:6144
v_cvt_pk_bf16_f32 v245, v170, v171
v_mfma_f32_16x16x32_bf16 a[148:151], a[60:63], v[232:235], a[148:151]
v_cvt_pk_bf16_f32 v230, v140, v141
v_cvt_pk_bf16_f32 v246, v172, v173
v_nop
v_mfma_f32_16x16x32_bf16 a[156:159], a[60:63], v[236:239], a[156:159]
v_exp_f32 v175, v175
v_mfma_f32_16x16x32_bf16 a[100:103], a[60:63], v[248:251], a[100:103]
s_waitcnt  lgkmcnt(7)
v_cvt_pk_bf16_f32 v231, v142, v143
v_cvt_pk_bf16_f32 v247, v174, v175
v_cndmask_b32 v31, v196, v34, s[34:35]
v_mfma_f32_16x16x32_bf16 a[108:111], a[60:63], v[252:255], a[108:111]
ds_read_b128 a[60:63], v63 offset:7168
v_mfma_f32_16x16x32_bf16 v[128:131], a[0:3], v[64:67], v[192:195]
s_cmp_lt_u32 s44, s45
v_exp_f32 v144, v144
v_mfma_f32_16x16x32_bf16 v[136:139], a[0:3], v[80:83], v[196:199]
s_cselect_b32 m0, s46, s27
v_exp_f32 v145, v145
v_mfma_f32_16x16x32_bf16 v[132:135], a[4:7], v[64:67], v[192:195]
s_and_b32 s36, s86, 1
s_lshl_b32 s36, s36, 3
v_exp_f32 v176, v176
v_mfma_f32_16x16x32_bf16 v[140:143], a[4:7], v[80:83], v[196:199]
ds_read_b128 a[0:3], v62 offset:8192
ds_read_b128 a[4:7], v62 offset:12288
v_mfma_f32_16x16x32_bf16 v[128:131], a[8:11], v[68:71], v[128:131]
s_and_b32 s28, s87, 7
s_or_b32 s36, s36, s28
v_exp_f32 v177, v177
v_mfma_f32_16x16x32_bf16 v[136:139], a[8:11], v[84:87], v[136:139]
ds_read_b128 a[8:11], v62 offset:9216
v_exp_f32 v146, v146
v_mfma_f32_16x16x32_bf16 v[132:135], a[12:15], v[68:71], v[132:135]
buffer_load_dwordx4 v7, s[8:11], s44 offen offset:0 sc1 lds
v_mfma_f32_16x16x32_bf16 v[140:143], a[12:15], v[84:87], v[140:143]
ds_read_b128 a[12:15], v62 offset:13312
v_exp_f32 v147, v147
v_mfma_f32_16x16x32_bf16 v[128:131], a[16:19], v[72:75], v[128:131]
s_lshr_b32 s37, s86, 1
s_lshl_b32 s37, s37, 2
v_cvt_pk_bf16_f32 v232, v144, v145
v_mfma_f32_16x16x32_bf16 v[136:139], a[16:19], v[88:91], v[136:139]
s_xor_b32 s50, s46, 0x18000
v_exp_f32 v178, v178
v_mfma_f32_16x16x32_bf16 v[132:135], a[20:23], v[72:75], v[132:135]
s_lshr_b32 s28, s87, 3
s_or_b32 s37, s37, s28
v_exp_f32 v179, v179
v_mfma_f32_16x16x32_bf16 v[140:143], a[20:23], v[88:91], v[140:143]
ds_read_b128 a[16:19], v62 offset:10240
ds_read_b128 a[20:23], v62 offset:14336
v_mfma_f32_16x16x32_bf16 v[128:131], a[24:27], v[76:79], v[128:131]
s_lshl_b32 s24, s36, 18
s_lshl_b32 s28, s3, 16
v_exp_f32 v148, v148
v_mfma_f32_16x16x32_bf16 v[136:139], a[24:27], v[92:95], v[136:139]
ds_read_b128 a[24:27], v62 offset:11264
v_cvt_pk_bf16_f32 v248, v176, v177
v_mfma_f32_16x16x32_bf16 v[132:135], a[28:31], v[76:79], v[132:135]
s_add_u32 s24, s24, s28
s_mov_b32 s25, 0
v_exp_f32 v149, v149
v_mfma_f32_16x16x32_bf16 v[140:143], a[28:31], v[92:95], v[140:143]
ds_read_b128 a[28:31], v62 offset:15360
v_exp_f32 v180, v180
v_mfma_f32_16x16x32_bf16 v[208:211], v[52:55], v[224:227], v[208:211]
buffer_load_dwordx4 v7, s[8:11], s44 offen offset:1024 sc1 lds
v_add_u32 v62, s46, v32
v_mfma_f32_16x16x32_bf16 v[212:215], v[52:55], v[228:231], v[212:215]
s_mul_i32 s26, s3, 98304
s_lshl_b32 s28, s37, 7
v_exp_f32 v181, v181
v_mfma_f32_16x16x32_bf16 v[216:219], v[52:55], v[240:243], v[216:219]
s_add_u32 s26, s26, s28
s_add_u32 s47, s26, 4096
v_exp_f32 v150, v150
v_mfma_f32_16x16x32_bf16 v[220:223], v[52:55], v[244:247], v[220:223]
s_waitcnt lgkmcnt(8)
v_cvt_pk_bf16_f32 v233, v146, v147
v_cvt_pk_bf16_f32 v249, v178, v179
v_mfma_f32_16x16x32_bf16 a[64:67], a[32:35], v[224:227], a[64:67]
s_mul_i32 s48, s3, 7168
s_mov_b32 s12, s64
v_exp_f32 v151, v151
v_mfma_f32_16x16x32_bf16 a[72:75], a[32:35], v[228:231], a[72:75]
s_and_b32 s13, s65, 0xffff
s_mov_b32 s14, 0x400000
v_cvt_pk_bf16_f32 v234, v148, v149
v_mfma_f32_16x16x32_bf16 a[176:179], a[32:35], v[240:243], a[176:179]
s_mov_b32 s16, s66
s_and_b32 s17, s67, 0xffff
v_exp_f32 v182, v182
v_mfma_f32_16x16x32_bf16 a[184:187], a[32:35], v[244:247], a[184:187]
ds_read_b128 a[32:35], v63 offset:8192
v_exp_f32 v183, v183
v_mfma_f32_16x16x32_bf16 a[68:71], a[36:39], v[224:227], a[68:71]
s_mov_b32 s18, 0x600000
s_lshl_b32 s29, s36, 18
v_cvt_pk_bf16_f32 v250, v180, v181
v_mfma_f32_16x16x32_bf16 a[76:79], a[36:39], v[228:231], a[76:79]
s_lshl_b32 s28, s3, 13
s_add_u32 s29, s29, s28
v_exp_f32 v152, v152
v_mfma_f32_16x16x32_bf16 a[180:183], a[36:39], v[240:243], a[180:183]
s_sub_u32 s29, s29, 34816
v_exp_f32 v153, v153
v_mfma_f32_16x16x32_bf16 a[188:191], a[36:39], v[244:247], a[188:191]
ds_read_b128 a[36:39], v63 offset:9216
.Lpad12:
s_cbranch_execz .Ltch3
v_cvt_pk_bf16_f32 v235, v150, v151
v_mfma_f32_16x16x32_bf16 a[80:83], a[40:43], v[224:227], a[80:83]
buffer_load_dwordx4 v7, s[8:11], s44 offen offset:2048 sc1 lds
v_mfma_f32_16x16x32_bf16 a[88:91], a[40:43], v[228:231], a[88:91]
s_cmp_eq_u32 s36, 0
s_cselect_b32 s29, 0x7f000000, s29
v_exp_f32 v184, v184
v_mfma_f32_16x16x32_bf16 a[208:211], a[40:43], v[240:243], a[208:211]
s_lshl_b32 s32, s3, 2
s_and_b32 s33, s3, 1
v_exp_f32 v185, v185
v_mfma_f32_16x16x32_bf16 a[216:219], a[40:43], v[244:247], a[216:219]
ds_read_b128 a[40:43], v63 offset:10240
v_exp_f32 v154, v154
v_mfma_f32_16x16x32_bf16 a[84:87], a[44:47], v[224:227], a[84:87]
s_lshl_b32 s33, s33, 1
s_lshr_b32 s34, s3, 1
v_cvt_pk_bf16_f32 v251, v182, v183
v_mfma_f32_16x16x32_bf16 a[92:95], a[44:47], v[228:231], a[92:95]
s_lshl_b32 s35, s34, 13
s_add_u32 s35, s35, 65536
v_exp_f32 v155, v155
v_mfma_f32_16x16x32_bf16 a[212:215], a[44:47], v[240:243], a[212:215]
s_mov_b32 s38, 131072
s_lshl_b32 s28, s88, 13
v_exp_f32 v186, v186
v_mfma_f32_16x16x32_bf16 a[220:223], a[44:47], v[244:247], a[220:223]
ds_read_b128 a[44:47], v63 offset:11264
v_cvt_pk_bf16_f32 v236, v152, v153
v_mfma_f32_16x16x32_bf16 a[112:115], a[48:51], v[224:227], a[112:115]
buffer_load_dwordx4 v7, s[8:11], s44 offen offset:3072 sc1 lds
v_mfma_f32_16x16x32_bf16 a[120:123], a[48:51], v[228:231], a[120:123]
s_add_u32 s28, s28, 33554432
v_exp_f32 v187, v187
v_mfma_f32_16x16x32_bf16 a[240:243], a[48:51], v[240:243], a[240:243]
s_add_u32 s20, s84, s28
s_addc_u32 s21, s85, 0
v_exp_f32 v156, v156
v_mfma_f32_16x16x32_bf16 a[248:251], a[48:51], v[244:247], a[248:251]
ds_read_b128 a[48:51], v63 offset:12288
v_cvt_pk_bf16_f32 v252, v184, v185
v_mfma_f32_16x16x32_bf16 a[116:119], a[52:55], v[224:227], a[116:119]
s_add_u32 s44, s44, 16384
v_exp_f32 v157, v157
v_mfma_f32_16x16x32_bf16 a[124:127], a[52:55], v[228:231], a[124:127]
s_and_b32 s21, s21, 0xffff
s_mov_b32 s22, 16777216
v_exp_f32 v188, v188
v_mfma_f32_16x16x32_bf16 a[244:247], a[52:55], v[240:243], a[244:247]
s_sub_u32 s40, 15, s89
s_lshl_b32 s40, s40, 2
v_exp_f32 v189, v189
v_mfma_f32_16x16x32_bf16 a[252:255], a[52:55], v[244:247], a[252:255]
ds_read_b128 a[52:55], v63 offset:13312
v_cvt_pk_bf16_f32 v237, v154, v155
v_mfma_f32_16x16x32_bf16 a[144:147], a[56:59], v[224:227], a[144:147]
s_add_u32 s40, s40, s3
s_lshl_b32 s40, s40, 18
v_exp_f32 v158, v158
v_mfma_f32_16x16x32_bf16 a[152:155], a[56:59], v[228:231], a[152:155]
s_lshl_b32 s41, s89, 2
s_add_u32 s41, s41, s94
v_cvt_pk_bf16_f32 v253, v186, v187
v_mfma_f32_16x16x32_bf16 a[96:99], a[56:59], v[240:243], a[96:99]
s_lshl_b32 s41, s41, 18
s_mov_b32 s28, 1602224128
v_exp_f32 v159, v159
v_mfma_f32_16x16x32_bf16 a[104:107], a[56:59], v[244:247], a[104:107]
ds_read_b128 a[56:59], v63 offset:14336
v_exp_f32 v190, v190
v_mfma_f32_16x16x32_bf16 a[148:151], a[60:63], v[224:227], a[148:151]
v_cvt_pk_bf16_f32 v238, v156, v157
v_cvt_pk_bf16_f32 v254, v188, v189
v_mfma_f32_16x16x32_bf16 a[156:159], a[60:63], v[228:231], a[156:159]
s_waitcnt vmcnt(9) lgkmcnt(7)
v_exp_f32 v191, v191
v_mfma_f32_16x16x32_bf16 a[100:103], a[60:63], v[240:243], a[100:103]
s_barrier
v_cvt_pk_bf16_f32 v239, v158, v159
v_cvt_pk_bf16_f32 v255, v190, v191
v_mfma_f32_16x16x32_bf16 a[108:111], a[60:63], v[244:247], a[108:111]
ds_read_b128 a[60:63], v63 offset:15360
v_mfma_f32_16x16x32_bf16 v[144:147], a[0:3], v[64:67], v[192:195]
s_cmp_lt_u32 s44, s54
v_exp_f32 v128, v128
v_mfma_f32_16x16x32_bf16 v[152:155], a[0:3], v[80:83], v[196:199]
s_cselect_b32 m0, s50, s27
v_lshrrev_b32 v56, 3, v1
v_and_b32 v57, 7, v1
v_mfma_f32_16x16x32_bf16 v[148:151], a[4:7], v[64:67], v[192:195]
v_add_u32 v63, s49, v33
v_xor_b32 v58, v57, v56
v_lshlrev_b32 v58, 4, v58
v_mfma_f32_16x16x32_bf16 v[156:159], a[4:7], v[80:83], v[196:199]
ds_read_b128 a[0:3], v62 offset:0
ds_read_b128 a[4:7], v62 offset:4096
v_lshl_add_u32 v36, v56, 11, v58
v_mfma_f32_16x16x32_bf16 v[144:147], a[8:11], v[68:71], v[144:147]
v_exp_f32 v129, v129
v_add_u32 v37, 15360, v36
v_mfma_f32_16x16x32_bf16 v[152:155], a[8:11], v[84:87], v[152:155]
ds_read_b128 a[8:11], v62 offset:1024
v_add_u32 v38, 30720, v36
v_mfma_f32_16x16x32_bf16 v[148:151], a[12:15], v[68:71], v[148:151]
buffer_load_dwordx4 v7, s[4:7], s44 offen offset:0 sc1 lds
v_add_u32 v39, 46080, v36
v_mfma_f32_16x16x32_bf16 v[156:159], a[12:15], v[84:87], v[156:159]
ds_read_b128 a[12:15], v62 offset:5120
v_add_u32 v58, s32, v56
v_mfma_f32_16x16x32_bf16 v[144:147], a[16:19], v[72:75], v[144:147]
v_exp_f32 v130, v130
v_xor_b32 v58, v58, v57
v_mfma_f32_16x16x32_bf16 v[152:155], a[16:19], v[88:91], v[152:155]
s_mov_b32 s49, s46
v_and_b32 v58, 7, v58
v_lshlrev_b32 v58, 4, v58
v_mfma_f32_16x16x32_bf16 v[148:151], a[20:23], v[72:75], v[148:151]
v_exp_f32 v131, v131
v_lshl_add_u32 v58, v56, 11, v58
v_mfma_f32_16x16x32_bf16 v[156:159], a[20:23], v[88:91], v[156:159]
ds_read_b128 a[16:19], v62 offset:2048
ds_read_b128 a[20:23], v62 offset:6144
v_add_u32 v58, s29, v58
v_mfma_f32_16x16x32_bf16 v[144:147], a[24:27], v[76:79], v[144:147]
v_exp_f32 v132, v132
v_and_b32 v59, 32, v1
v_mfma_f32_16x16x32_bf16 v[152:155], a[24:27], v[92:95], v[152:155]
ds_read_b128 a[24:27], v62 offset:3072
v_lshlrev_b32 v59, 25, v59
v_mfma_f32_16x16x32_bf16 v[148:151], a[28:31], v[76:79], v[148:151]
s_add_u32 s46, s46, 16384
v_or_b32 v42, v59, v58
v_bfe_u32 v58, v1, 4, 1
v_mfma_f32_16x16x32_bf16 v[156:159], a[28:31], v[92:95], v[156:159]
ds_read_b128 a[28:31], v62 offset:7168
v_lshlrev_b32 v58, 1, v58
v_mfma_f32_16x16x32_bf16 v[208:211], v[52:55], v[232:235], v[208:211]
buffer_load_dwordx4 v7, s[4:7], s44 offen offset:1024 sc1 lds
v_xor_b32 v58, v58, v57
v_mfma_f32_16x16x32_bf16 v[212:215], v[52:55], v[236:239], v[212:215]
v_exp_f32 v133, v133
v_mfma_f32_16x16x32_bf16 v[216:219], v[52:55], v[248:251], v[216:219]
v_lshlrev_b32 v58, 4, v58
v_mul_u32_u24 v59, 6144, v56
v_mfma_f32_16x16x32_bf16 v[220:223], v[52:55], v[252:255], v[220:223]
s_waitcnt lgkmcnt(8)
s_and_b32 s46, s46, 0xffff
v_add_u32 v40, v59, v58
v_mfma_f32_16x16x32_bf16 a[64:67], a[32:35], v[232:235], a[64:67]
v_exp_f32 v134, v134
v_xor_b32 v41, 64, v40
v_mfma_f32_16x16x32_bf16 a[72:75], a[32:35], v[236:239], a[72:75]
v_add_u32 v41, 48128, v41
v_and_b32 v56, 15, v1
v_mfma_f32_16x16x32_bf16 a[176:179], a[32:35], v[248:251], a[176:179]
v_exp_f32 v135, v135
v_lshrrev_b32 v57, 4, v1
v_mfma_f32_16x16x32_bf16 a[184:187], a[32:35], v[252:255], a[184:187]
ds_read_b128 a[32:35], v63 offset:0
v_and_b32 v58, 7, v56
v_mfma_f32_16x16x32_bf16 a[68:71], a[36:39], v[232:235], a[68:71]
v_xor_b32 v58, v58, v57
v_lshlrev_b32 v58, 4, v58
v_mfma_f32_16x16x32_bf16 a[76:79], a[36:39], v[236:239], a[76:79]
v_lshl_add_u32 v43, v56, 7, v58
v_add_u32 v43, s35, v43
v_mfma_f32_16x16x32_bf16 a[180:183], a[36:39], v[248:251], a[180:183]
v_exp_f32 v136, v136
v_mfma_f32_16x16x32_bf16 a[188:191], a[36:39], v[252:255], a[188:191]
ds_read_b128 a[36:39], v63 offset:1024
v_xor_b32 v44, 64, v43
v_mfma_f32_16x16x32_bf16 a[80:83], a[40:43], v[232:235], a[80:83]
buffer_load_dwordx4 v7, s[4:7], s44 offen offset:2048 sc1 lds
v_and_b32 v56, 15, v1
v_mfma_f32_16x16x32_bf16 a[88:91], a[40:43], v[236:239], a[88:91]
v_exp_f32 v137, v137
v_lshrrev_b32 v57, 2, v56
v_mfma_f32_16x16x32_bf16 a[208:211], a[40:43], v[248:251], a[208:211]
v_mul_u32_u24 v57, 6656, v57
v_add_u32 v57, 71680, v57
v_add_f32 v13, v216, v220
v_mfma_f32_16x16x32_bf16 a[216:219], a[40:43], v[252:255], a[216:219]
ds_read_b128 a[40:43], v63 offset:2048
v_mov_b32 v58, -2048
v_mfma_f32_16x16x32_bf16 a[84:87], a[44:47], v[232:235], a[84:87]
v_exp_f32 v138, v138
v_mfma_f32_16x16x32_bf16 a[92:95], a[44:47], v[236:239], a[92:95]
v_mov_b32 v59, s34
v_cmp_eq_u32 vcc, 0, v59
v_mfma_f32_16x16x32_bf16 a[212:215], a[44:47], v[248:251], a[212:215]
v_exp_f32 v139, v139
v_nop
v_mfma_f32_16x16x32_bf16 a[220:223], a[44:47], v[252:255], a[220:223]
ds_read_b128 a[44:47], v63 offset:3072
v_nop
v_mfma_f32_16x16x32_bf16 a[112:115], a[48:51], v[232:235], a[112:115]
buffer_load_dwordx4 v7, s[4:7], s44 offen offset:3072 sc1 lds
v_cndmask_b32 v57, v58, v57, vcc
v_mfma_f32_16x16x32_bf16 a[120:123], a[48:51], v[236:239], a[120:123]
v_add_u32 v45, v57, v43
v_add_u32 v46, v57, v44
v_mfma_f32_16x16x32_bf16 a[240:243], a[48:51], v[248:251], a[240:243]
v_exp_f32 v140, v140
v_mfma_f32_16x16x32_bf16 a[248:251], a[48:51], v[252:255], a[248:251]
ds_read_b128 a[48:51], v63 offset:4096
v_bfe_u32 v56, v1, 2, 2
v_mfma_f32_16x16x32_bf16 a[116:119], a[52:55], v[232:235], a[116:119]
v_bfe_u32 v57, v1, 4, 1
v_lshl_add_u32 v58, v57, 3, v56
v_mfma_f32_16x16x32_bf16 a[124:127], a[52:55], v[236:239], a[124:127]
v_lshlrev_b32 v58, 7, v58
v_lshrrev_b32 v59, 5, v1
v_mfma_f32_16x16x32_bf16 a[244:247], a[52:55], v[248:251], a[244:247]
v_exp_f32 v141, v141
v_mfma_f32_16x16x32_bf16 a[252:255], a[52:55], v[252:255], a[252:255]
ds_read_b128 a[52:55], v63 offset:5120
v_mul_u32_u24 v59, 7168, v59
v_add3_u32 v58, v58, v59, s38
v_mfma_f32_16x16x32_bf16 a[144:147], a[56:59], v[232:235], a[144:147]
v_cvt_pk_bf16_f32 v224, v128, v129
v_lshrrev_b32 v56, 1, v56
v_cmp_nge_f32 s[52:53], s28, v13
v_mfma_f32_16x16x32_bf16 a[152:155], a[56:59], v[236:239], a[152:155]
v_exp_f32 v142, v142
v_lshl_or_b32 v56, v57, 1, v56
v_mfma_f32_16x16x32_bf16 a[96:99], a[56:59], v[248:251], a[96:99]
v_cvt_pk_bf16_f32 v225, v130, v131
v_cvt_pk_bf16_f32 v226, v132, v133
v_xor_b32 v56, s33, v56
v_mfma_f32_16x16x32_bf16 a[104:107], a[56:59], v[252:255], a[104:107]
ds_read_b128 a[56:59], v63 offset:6144
v_cvt_pk_bf16_f32 v227, v134, v135
v_lshl_add_u32 v58, v56, 5, v58
v_mfma_f32_16x16x32_bf16 a[148:151], a[60:63], v[232:235], a[148:151]
v_exp_f32 v143, v143
v_and_b32 v56, 3, v1
v_lshl_add_u32 v47, v56, 3, v58
v_mfma_f32_16x16x32_bf16 a[156:159], a[60:63], v[236:239], a[156:159]
v_cvt_pk_bf16_f32 v228, v136, v137
v_cvt_pk_bf16_f32 v229, v138, v139
v_xor_b32 v48, 32, v47
v_mfma_f32_16x16x32_bf16 a[100:103], a[60:63], v[248:251], a[100:103]
s_waitcnt  lgkmcnt(7)
v_cvt_pk_bf16_f32 v230, v140, v141
v_cvt_pk_bf16_f32 v231, v142, v143
v_add_u32 v51, 4096, v7
v_mfma_f32_16x16x32_bf16 a[108:111], a[60:63], v[252:255], a[108:111]
ds_read_b128 a[60:63], v63 offset:7168
s_cmp_le_u32 s94, 3
s_cbranch_scc1 .LTf1
.LTc1:
s_cmp_lt_i32 s55, 0
s_cbranch_scc1 .LE0
s_branch .LS0
.LTf1:
s_cmp_lg_u64 s[52:53], 0
s_cbranch_scc1 .LTc1
s_getpc_b64 s[30:31]
.LTp1:
s_sub_u32 s30, s30, .LTp1-.LTc1
s_subb_u32 s31, s31, 0
s_branch .Lfb
.LS1_pad:
s_cbranch_execz .Ltch4
s_nop 0
s_nop 0
s_nop 0
s_nop 0
s_nop 0
s_nop 0
s_nop 0
.LS1:
v_mfma_f32_16x16x32_bf16 v[144:147], a[0:3], v[64:67], v[192:195]
s_cmp_lt_u32 s44, s54
v_exp_f32 v128, v128
v_mfma_f32_16x16x32_bf16 v[152:155], a[0:3], v[80:83], v[196:199]
s_cselect_b32 m0, s50, s27
v_exp_f32 v129, v129
v_mfma_f32_16x16x32_bf16 v[148:151], a[4:7], v[64:67], v[192:195]
v_add_u32 v63, s49, v33
v_mfma_f32_16x16x32_bf16 v[156:159], a[4:7], v[80:83], v[196:199]
ds_read_b128 a[0:3], v62 offset:0
ds_read_b128 a[4:7], v62 offset:4096
v_mfma_f32_16x16x32_bf16 v[144:147], a[8:11], v[68:71], v[144:147]
buffer_load_dwordx4 v7, s[4:7], s44 offen offset:0 sc1 lds
v_mfma_f32_16x16x32_bf16 v[152:155], a[8:11], v[84:87], v[152:155]
ds_read_b128 a[8:11], v62 offset:1024
v_mfma_f32_16x16x32_bf16 v[148:151], a[12:15], v[68:71], v[148:151]
s_mov_b32 s49, s46
v_exp_f32 v130, v130
v_mfma_f32_16x16x32_bf16 v[156:159], a[12:15], v[84:87], v[156:159]
ds_read_b128 a[12:15], v62 offset:5120
v_cvt_pk_bf16_f32 v224, v128, v129
v_mfma_f32_16x16x32_bf16 v[144:147], a[16:19], v[72:75], v[144:147]
v_exp_f32 v131, v131
v_mfma_f32_16x16x32_bf16 v[152:155], a[16:19], v[88:91], v[152:155]
s_add_u32 s46, s46, 16384
v_exp_f32 v132, v132
v_mfma_f32_16x16x32_bf16 v[148:151], a[20:23], v[72:75], v[148:151]
buffer_load_dwordx4 v7, s[4:7], s44 offen offset:1024 sc1 lds
v_mfma_f32_16x16x32_bf16 v[156:159], a[20:23], v[88:91], v[156:159]
ds_read_b128 a[16:19], v62 offset:2048
ds_read_b128 a[20:23], v62 offset:6144
v_mfma_f32_16x16x32_bf16 v[144:147], a[24:27], v[76:79], v[144:147]
v_exp_f32 v133, v133
v_mfma_f32_16x16x32_bf16 v[152:155], a[24:27], v[92:95], v[152:155]
ds_read_b128 a[24:27], v62 offset:3072
v_cvt_pk_bf16_f32 v225, v130, v131
v_mfma_f32_16x16x32_bf16 v[148:151], a[28:31], v[76:79], v[148:151]
s_and_b32 s46, s46, 0xffff
v_exp_f32 v134, v134
v_mfma_f32_16x16x32_bf16 v[156:159], a[28:31], v[92:95], v[156:159]
ds_read_b128 a[28:31], v62 offset:7168
v_cvt_pk_bf16_f32 v226, v132, v133
v_mfma_f32_16x16x32_bf16 v[208:211], v[52:55], v[232:235], v[208:211]
v_exp_f32 v135, v135
v_mfma_f32_16x16x32_bf16 v[212:215], v[52:55], v[236:239], v[212:215]
s_waitcnt lgkmcnt(8)
v_exp_f32 v136, v136
v_mfma_f32_16x16x32_bf16 a[64:67], a[32:35], v[232:235], a[64:67]
buffer_load_dwordx4 v7, s[4:7], s44 offen offset:2048 sc1 lds
v_mfma_f32_16x16x32_bf16 a[72:75], a[32:35], v[236:239], a[72:75]
v_exp_f32 v137, v137
v_mfma_f32_16x16x32_bf16 a[68:71], a[36:39], v[232:235], a[68:71]
v_exp_f32 v138, v138
v_mfma_f32_16x16x32_bf16 a[76:79], a[36:39], v[236:239], a[76:79]
ds_read_b128 a[32:35], v63 offset:0
ds_read_b128 a[36:39], v63 offset:1024
v_mfma_f32_16x16x32_bf16 a[80:83], a[40:43], v[232:235], a[80:83]
v_exp_f32 v139, v139
v_mfma_f32_16x16x32_bf16 a[88:91], a[40:43], v[236:239], a[88:91]
ds_read_b128 a[40:43], v63 offset:2048
v_cvt_pk_bf16_f32 v227, v134, v135
v_mfma_f32_16x16x32_bf16 a[84:87], a[44:47], v[232:235], a[84:87]
buffer_load_dwordx4 v7, s[4:7], s44 offen offset:3072 sc1 lds
v_mfma_f32_16x16x32_bf16 a[92:95], a[44:47], v[236:239], a[92:95]
ds_read_b128 a[44:47], v63 offset:3072
v_cvt_pk_bf16_f32 v228, v136, v137
v_mfma_f32_16x16x32_bf16 a[112:115], a[48:51], v[232:235], a[112:115]
v_exp_f32 v140, v140
v_mfma_f32_16x16x32_bf16 a[120:123], a[48:51], v[236:239], a[120:123]
v_exp_f32 v141, v141
v_mfma_f32_16x16x32_bf16 a[116:119], a[52:55], v[232:235], a[116:119]
s_sub_u32 s55, s55, 1
v_exp_f32 v142, v142
v_mfma_f32_16x16x32_bf16 a[124:127], a[52:55], v[236:239], a[124:127]
ds_read_b128 a[48:51], v63 offset:4096
ds_read_b128 a[52:55], v63 offset:5120
v_mfma_f32_16x16x32_bf16 a[144:147], a[56:59], v[232:235], a[144:147]
v_exp_f32 v143, v143
v_mfma_f32_16x16x32_bf16 a[152:155], a[56:59], v[236:239], a[152:155]
ds_read_b128 a[56:59], v63 offset:6144
v_cvt_pk_bf16_f32 v229, v138, v139
v_mfma_f32_16x16x32_bf16 a[148:151], a[60:63], v[232:235], a[148:151]
s_waitcnt  lgkmcnt(7)
v_cvt_pk_bf16_f32 v230, v140, v141
v_cvt_pk_bf16_f32 v231, v142, v143
v_mfma_f32_16x16x32_bf16 a[156:159], a[60:63], v[236:239], a[156:159]
ds_read_b128 a[60:63], v63 offset:7168
s_cbranch_scc1 .Lev_s1
.LS0:
v_mfma_f32_16x16x32_bf16 v[128:131], a[0:3], v[64:67], v[192:195]
s_cmp_lt_u32 s44, s45
v_exp_f32 v144, v144
v_mfma_f32_16x16x32_bf16 v[136:139], a[0:3], v[80:83], v[196:199]
s_cselect_b32 m0, s46, s27
v_exp_f32 v145, v145
v_mfma_f32_16x16x32_bf16 v[132:135], a[4:7], v[64:67], v[192:195]
v_exp_f32 v146, v146
v_mfma_f32_16x16x32_bf16 v[140:143], a[4:7], v[80:83], v[196:199]
ds_read_b128 a[0:3], v62 offset:8192
ds_read_b128 a[4:7], v62 offset:12288
v_mfma_f32_16x16x32_bf16 v[128:131], a[8:11], v[68:71], v[128:131]
buffer_load_dwordx4 v7, s[8:11], s44 offen offset:0 sc1 lds
v_mfma_f32_16x16x32_bf16 v[136:139], a[8:11], v[84:87], v[136:139]
ds_read_b128 a[8:11], v62 offset:9216
v_mfma_f32_16x16x32_bf16 v[132:135], a[12:15], v[68:71], v[132:135]
s_xor_b32 s50, s46, 0x18000
v_exp_f32 v147, v147
v_mfma_f32_16x16x32_bf16 v[140:143], a[12:15], v[84:87], v[140:143]
ds_read_b128 a[12:15], v62 offset:13312
v_cvt_pk_bf16_f32 v232, v144, v145
v_mfma_f32_16x16x32_bf16 v[128:131], a[16:19], v[72:75], v[128:131]
v_exp_f32 v148, v148
v_mfma_f32_16x16x32_bf16 v[136:139], a[16:19], v[88:91], v[136:139]
v_exp_f32 v149, v149
v_mfma_f32_16x16x32_bf16 v[132:135], a[20:23], v[72:75], v[132:135]
buffer_load_dwordx4 v7, s[8:11], s44 offen offset:1024 sc1 lds
v_mfma_f32_16x16x32_bf16 v[140:143], a[20:23], v[88:91], v[140:143]
ds_read_b128 a[16:19], v62 offset:10240
ds_read_b128 a[20:23], v62 offset:14336
v_mfma_f32_16x16x32_bf16 v[128:131], a[24:27], v[76:79], v[128:131]
v_exp_f32 v150, v150
v_mfma_f32_16x16x32_bf16 v[136:139], a[24:27], v[92:95], v[136:139]
ds_read_b128 a[24:27], v62 offset:11264
v_cvt_pk_bf16_f32 v233, v146, v147
v_mfma_f32_16x16x32_bf16 v[132:135], a[28:31], v[76:79], v[132:135]
v_exp_f32 v151, v151
v_mfma_f32_16x16x32_bf16 v[140:143], a[28:31], v[92:95], v[140:143]
ds_read_b128 a[28:31], v62 offset:15360
v_cvt_pk_bf16_f32 v234, v148, v149
v_mfma_f32_16x16x32_bf16 v[208:211], v[52:55], v[224:227], v[208:211]
v_add_u32 v62, s46, v32
v_cvt_pk_bf16_f32 v235, v150, v151
v_mfma_f32_16x16x32_bf16 v[212:215], v[52:55], v[228:231], v[212:215]
s_waitcnt lgkmcnt(8)
v_exp_f32 v152, v152
v_mfma_f32_16x16x32_bf16 a[64:67], a[32:35], v[224:227], a[64:67]
buffer_load_dwordx4 v7, s[8:11], s44 offen offset:2048 sc1 lds
v_mfma_f32_16x16x32_bf16 a[72:75], a[32:35], v[228:231], a[72:75]
v_exp_f32 v153, v153
v_mfma_f32_16x16x32_bf16 a[68:71], a[36:39], v[224:227], a[68:71]
v_exp_f32 v154, v154
v_mfma_f32_16x16x32_bf16 a[76:79], a[36:39], v[228:231], a[76:79]
ds_read_b128 a[32:35], v63 offset:8192
ds_read_b128 a[36:39], v63 offset:9216
v_mfma_f32_16x16x32_bf16 a[80:83], a[40:43], v[224:227], a[80:83]
v_exp_f32 v155, v155
v_mfma_f32_16x16x32_bf16 a[88:91], a[40:43], v[228:231], a[88:91]
ds_read_b128 a[40:43], v63 offset:10240
v_cvt_pk_bf16_f32 v236, v152, v153
v_mfma_f32_16x16x32_bf16 a[84:87], a[44:47], v[224:227], a[84:87]
buffer_load_dwordx4 v7, s[8:11], s44 offen offset:3072 sc1 lds
v_mfma_f32_16x16x32_bf16 a[92:95], a[44:47], v[228:231], a[92:95]
ds_read_b128 a[44:47], v63 offset:11264
v_cvt_pk_bf16_f32 v237, v154, v155
v_mfma_f32_16x16x32_bf16 a[112:115], a[48:51], v[224:227], a[112:115]
s_add_u32 s44, s44, 16384
v_exp_f32 v156, v156
v_mfma_f32_16x16x32_bf16 a[120:123], a[48:51], v[228:231], a[120:123]
v_exp_f32 v157, v157
v_mfma_f32_16x16x32_bf16 a[116:119], a[52:55], v[224:227], a[116:119]
s_sub_u32 s55, s55, 1
v_exp_f32 v158, v158
v_mfma_f32_16x16x32_bf16 a[124:127], a[52:55], v[228:231], a[124:127]
ds_read_b128 a[48:51], v63 offset:12288
ds_read_b128 a[52:55], v63 offset:13312
v_mfma_f32_16x16x32_bf16 a[144:147], a[56:59], v[224:227], a[144:147]
v_exp_f32 v159, v159
v_mfma_f32_16x16x32_bf16 a[152:155], a[56:59], v[228:231], a[152:155]
s_waitcnt vmcnt(8) lgkmcnt(6)
ds_read_b128 a[56:59], v63 offset:14336
v_mfma_f32_16x16x32_bf16 a[148:151], a[60:63], v[224:227], a[148:151]
s_barrier
v_cvt_pk_bf16_f32 v238, v156, v157
v_cvt_pk_bf16_f32 v239, v158, v159
v_mfma_f32_16x16x32_bf16 a[156:159], a[60:63], v[228:231], a[156:159]
ds_read_b128 a[60:63], v63 offset:15360
s_cbranch_scc0 .LS1
s_cmp_eq_u32 s43, s42
s_cbranch_scc1 .LE1
s_swappc_b64 s[30:31], s[58:59]
.Lev_s0_r:
s_branch .LS1
.LE0:
v_mfma_f32_16x16x32_bf16 v[128:131], a[0:3], v[64:67], v[16:19]
s_cmp_lt_u32 s44, s45
v_exp_f32 v144, v144
v_mfma_f32_16x16x32_bf16 v[136:139], a[0:3], v[80:83], v[24:27]
s_cselect_b32 m0, s46, s27
v_exp_f32 v145, v145
v_mfma_f32_16x16x32_bf16 v[132:135], a[4:7], v[64:67], v[20:23]
buffer_load_dwordx4 v7, s[8:11], s44 offen offset:0 sc1 lds
v_mfma_f32_16x16x32_bf16 v[140:143], a[4:7], v[80:83], v[28:31]
buffer_load_dwordx4 v7, s[8:11], s44 offen offset:1024 sc1 lds
v_mfma_f32_16x16x32_bf16 v[128:131], a[8:11], v[68:71], v[128:131]
buffer_load_dwordx4 v7, s[8:11], s44 offen offset:2048 sc1 lds
v_exp_f32 v146, v146
v_mfma_f32_16x16x32_bf16 v[136:139], a[8:11], v[84:87], v[136:139]
buffer_load_dwordx4 v7, s[8:11], s44 offen offset:3072 sc1 lds
v_exp_f32 v147, v147
v_mfma_f32_16x16x32_bf16 v[132:135], a[12:15], v[68:71], v[132:135]
s_xor_b32 s50, s46, 0x18000
buffer_load_dword v35, v4, s[96:99], 0 offen
v_mfma_f32_16x16x32_bf16 v[140:143], a[12:15], v[84:87], v[140:143]
s_add_u32 m0, s48, 131072
v_exp_f32 v148, v148
v_mfma_f32_16x16x32_bf16 v[128:131], a[16:19], v[72:75], v[128:131]
buffer_load_dwordx4 v40, s[16:19], s26 offen offset:0 lds
v_mfma_f32_16x16x32_bf16 v[136:139], a[16:19], v[88:91], v[136:139]
v_exp_f32 v149, v149
v_mfma_f32_16x16x32_bf16 v[132:135], a[20:23], v[72:75], v[132:135]
buffer_load_dwordx4 v41, s[16:19], s26 offen offset:1024 lds
v_mfma_f32_16x16x32_bf16 v[140:143], a[20:23], v[88:91], v[140:143]
v_exp_f32 v150, v150
v_mfma_f32_16x16x32_bf16 v[128:131], a[24:27], v[76:79], v[128:131]
buffer_load_dwordx4 v40, s[16:19], s26 offen offset:2048 lds
v_mfma_f32_16x16x32_bf16 v[136:139], a[24:27], v[92:95], v[136:139]
v_exp_f32 v151, v151
v_mfma_f32_16x16x32_bf16 v[132:135], a[28:31], v[76:79], v[132:135]
buffer_load_dwordx4 v41, s[16:19], s26 offen offset:3072 lds
v_mfma_f32_16x16x32_bf16 v[140:143], a[28:31], v[92:95], v[140:143]
s_add_u32 m0, s48, 135168
v_exp_f32 v152, v152
v_mfma_f32_16x16x32_bf16 v[208:211], v[52:55], v[224:227], v[208:211]
buffer_load_dwordx4 v40, s[16:19], s47 offen offset:0 lds
v_mfma_f32_16x16x32_bf16 v[212:215], v[52:55], v[228:231], v[212:215]
s_waitcnt lgkmcnt(0)
v_exp_f32 v153, v153
v_mfma_f32_16x16x32_bf16 a[64:67], a[32:35], v[224:227], a[64:67]
buffer_load_dwordx4 v41, s[16:19], s47 offen offset:1024 lds
v_mfma_f32_16x16x32_bf16 a[72:75], a[32:35], v[228:231], a[72:75]
v_exp_f32 v154, v154
v_mfma_f32_16x16x32_bf16 a[68:71], a[36:39], v[224:227], a[68:71]
buffer_load_dwordx4 v42, s[12:15], s25 offen offset:2048 lds
v_mfma_f32_16x16x32_bf16 a[76:79], a[36:39], v[228:231], a[76:79]
ds_read_b128 a[32:35], v63 offset:8192
ds_read_b128 a[36:39], v63 offset:9216
v_cvt_pk_bf16_f32 v232, v144, v145
v_mfma_f32_16x16x32_bf16 a[80:83], a[40:43], v[224:227], a[80:83]
s_add_u32 m0, s91, 65536
v_cvt_pk_bf16_f32 v233, v146, v147
v_exp_f32 v155, v155
v_mfma_f32_16x16x32_bf16 a[88:91], a[40:43], v[228:231], a[88:91]
buffer_load_dwordx4 v36, s[12:15], s24 offen offset:0 lds
ds_read_b128 a[40:43], v63 offset:10240
v_mfma_f32_16x16x32_bf16 a[84:87], a[44:47], v[224:227], a[84:87]
buffer_load_dwordx4 v37, s[12:15], s24 offen offset:1024 lds
v_cvt_pk_bf16_f32 v234, v148, v149
v_mfma_f32_16x16x32_bf16 a[92:95], a[44:47], v[228:231], a[92:95]
ds_read_b128 a[44:47], v63 offset:11264
v_exp_f32 v156, v156
v_mfma_f32_16x16x32_bf16 a[112:115], a[48:51], v[224:227], a[112:115]
s_add_u32 s44, s44, 16384
v_cvt_pk_bf16_f32 v235, v150, v151
v_exp_f32 v157, v157
v_mfma_f32_16x16x32_bf16 a[120:123], a[48:51], v[228:231], a[120:123]
buffer_load_dwordx4 v38, s[12:15], s24 offen offset:2048 lds
v_cvt_pk_bf16_f32 v236, v152, v153
v_mfma_f32_16x16x32_bf16 a[116:119], a[52:55], v[224:227], a[116:119]
buffer_load_dwordx4 v39, s[12:15], s24 offen offset:3072 lds
v_exp_f32 v158, v158
v_mfma_f32_16x16x32_bf16 a[124:127], a[52:55], v[228:231], a[124:127]
ds_read_b128 a[48:51], v63 offset:12288
ds_read_b128 a[52:55], v63 offset:13312
v_cvt_pk_bf16_f32 v237, v154, v155
v_mfma_f32_16x16x32_bf16 a[144:147], a[56:59], v[224:227], a[144:147]
s_add_u32 s26, s26, 393216
s_add_u32 s47, s47, 393216
s_add_u32 s25, s25, 128
s_add_u32 s24, s24, 128
v_mfma_f32_16x16x32_bf16 a[152:155], a[56:59], v[228:231], a[152:155]
s_waitcnt vmcnt(20)
ds_read_b128 a[56:59], v63 offset:14336
v_cvt_pk_bf16_f32 v238, v156, v157
v_mfma_f32_16x16x32_bf16 a[148:151], a[60:63], v[224:227], a[148:151]
s_barrier
v_exp_f32 v159, v159
s_nop 0
v_cvt_pk_bf16_f32 v239, v158, v159
v_mfma_f32_16x16x32_bf16 a[156:159], a[60:63], v[228:231], a[156:159]
ds_read_b128 a[60:63], v63 offset:15360
v_add_u32 v63, s49, v33
s_waitcnt lgkmcnt(4)
v_mfma_f32_16x16x32_bf16 v[208:211], v[52:55], v[232:235], v[208:211]
v_exp_f32 v128, v128
v_exp_f32 v129, v129
v_mfma_f32_16x16x32_bf16 v[212:215], v[52:55], v[236:239], v[212:215]
s_add_u32 m0, s91, 81920
v_exp_f32 v130, v130
v_mfma_f32_16x16x32_bf16 a[64:67], a[32:35], v[232:235], a[64:67]
buffer_load_dwordx4 v36, s[12:15], s24 offen offset:0 lds
v_mfma_f32_16x16x32_bf16 a[72:75], a[32:35], v[236:239], a[72:75]
v_exp_f32 v131, v131
v_mfma_f32_16x16x32_bf16 a[68:71], a[36:39], v[232:235], a[68:71]
buffer_load_dwordx4 v37, s[12:15], s24 offen offset:1024 lds
v_exp_f32 v132, v132
v_mfma_f32_16x16x32_bf16 a[76:79], a[36:39], v[236:239], a[76:79]
ds_read_b128 a[32:35], v63 offset:0
ds_read_b128 a[36:39], v63 offset:1024
v_exp_f32 v133, v133
v_mfma_f32_16x16x32_bf16 a[80:83], a[40:43], v[232:235], a[80:83]
v_cvt_pk_bf16_f32 v224, v128, v129
v_exp_f32 v134, v134
v_mfma_f32_16x16x32_bf16 a[88:91], a[40:43], v[236:239], a[88:91]
ds_read_b128 a[40:43], v63 offset:2048
v_exp_f32 v135, v135
v_mfma_f32_16x16x32_bf16 a[84:87], a[44:47], v[232:235], a[84:87]
buffer_load_dwordx4 v38, s[12:15], s24 offen offset:2048 lds
v_exp_f32 v136, v136
v_mfma_f32_16x16x32_bf16 a[92:95], a[44:47], v[236:239], a[92:95]
s_waitcnt lgkmcnt(3)
ds_read_b128 a[44:47], v63 offset:3072
v_exp_f32 v137, v137
v_mfma_f32_16x16x32_bf16 a[112:115], a[48:51], v[232:235], a[112:115]
v_cvt_pk_bf16_f32 v225, v130, v131
v_exp_f32 v138, v138
v_mfma_f32_16x16x32_bf16 a[120:123], a[48:51], v[236:239], a[120:123]
buffer_load_dwordx4 v39, s[12:15], s24 offen offset:3072 lds
v_cvt_pk_bf16_f32 v226, v132, v133
v_mfma_f32_16x16x32_bf16 a[116:119], a[52:55], v[232:235], a[116:119]
s_add_u32 s24, s24, 128
v_cvt_pk_bf16_f32 v227, v134, v135
v_exp_f32 v139, v139
v_mfma_f32_16x16x32_bf16 a[124:127], a[52:55], v[236:239], a[124:127]
ds_read_b128 a[48:51], v63 offset:4096
ds_read_b128 a[52:55], v63 offset:5120
v_exp_f32 v140, v140
v_mfma_f32_16x16x32_bf16 a[144:147], a[56:59], v[232:235], a[144:147]
v_cvt_pk_bf16_f32 v228, v136, v137
v_exp_f32 v141, v141
v_mfma_f32_16x16x32_bf16 a[152:155], a[56:59], v[236:239], a[152:155]
ds_read_b128 a[56:59], v63 offset:6144
v_exp_f32 v142, v142
v_mfma_f32_16x16x32_bf16 a[148:151], a[60:63], v[232:235], a[148:151]
v_cvt_pk_bf16_f32 v229, v138, v139
v_exp_f32 v143, v143
v_mfma_f32_16x16x32_bf16 a[156:159], a[60:63], v[236:239], a[156:159]
ds_read_b128 a[60:63], v63 offset:7168
v_cvt_pk_bf16_f32 v230, v140, v141
v_cvt_pk_bf16_f32 v231, v142, v143
s_waitcnt lgkmcnt(4)
v_mfma_f32_16x16x32_bf16 v[208:211], v[52:55], v[224:227], v[208:211]
s_mov_b32 s28, 1602224128
v_mfma_f32_16x16x32_bf16 v[212:215], v[52:55], v[228:231], v[212:215]
v_mfma_f32_16x16x32_bf16 a[64:67], a[32:35], v[224:227], a[64:67]
v_mfma_f32_16x16x32_bf16 a[72:75], a[32:35], v[228:231], a[72:75]
v_mfma_f32_16x16x32_bf16 a[68:71], a[36:39], v[224:227], a[68:71]
.Lpad13:
s_nop 0
v_mfma_f32_16x16x32_bf16 a[76:79], a[36:39], v[228:231], a[76:79]
v_mfma_f32_16x16x32_bf16 a[80:83], a[40:43], v[224:227], a[80:83]
v_mfma_f32_16x16x32_bf16 a[88:91], a[40:43], v[228:231], a[88:91]
v_mfma_f32_16x16x32_bf16 a[84:87], a[44:47], v[224:227], a[84:87]
v_mfma_f32_16x16x32_bf16 a[92:95], a[44:47], v[228:231], a[92:95]
s_waitcnt lgkmcnt(0)
v_mfma_f32_16x16x32_bf16 a[112:115], a[48:51], v[224:227], a[112:115]
v_add_f32 v13, v208, v212
v_mfma_f32_16x16x32_bf16 a[120:123], a[48:51], v[228:231], a[120:123]
v_cmp_nge_f32 s[30:31], s28, v13
v_mfma_f32_16x16x32_bf16 a[116:119], a[52:55], v[224:227], a[116:119]
v_mfma_f32_16x16x32_bf16 a[124:127], a[52:55], v[228:231], a[124:127]
v_mfma_f32_16x16x32_bf16 a[144:147], a[56:59], v[224:227], a[144:147]
v_mfma_f32_16x16x32_bf16 a[152:155], a[56:59], v[228:231], a[152:155]
v_mfma_f32_16x16x32_bf16 a[148:151], a[60:63], v[224:227], a[148:151]
v_mfma_f32_16x16x32_bf16 a[156:159], a[60:63], v[228:231], a[156:159]
s_cmp_lt_u32 s3, 2
s_cbranch_scc0 .LEb0
s_waitcnt vmcnt(16)
s_barrier
.LEb0:
s_branch .LEend
.LE1_pad:
s_cbranch_execz .Ltch4
.LE1:
v_mfma_f32_16x16x32_bf16 v[144:147], a[0:3], v[64:67], v[16:19]
buffer_load_dword v35, v4, s[96:99], 0 offen
v_mfma_f32_16x16x32_bf16 v[152:155], a[0:3], v[80:83], v[24:27]
s_add_u32 m0, s48, 131072
v_exp_f32 v128, v128
v_mfma_f32_16x16x32_bf16 v[148:151], a[4:7], v[64:67], v[20:23]
buffer_load_dwordx4 v40, s[16:19], s26 offen offset:0 lds
v_add_u32 v63, s49, v33
v_mfma_f32_16x16x32_bf16 v[156:159], a[4:7], v[80:83], v[28:31]
v_exp_f32 v129, v129
v_mfma_f32_16x16x32_bf16 v[144:147], a[8:11], v[68:71], v[144:147]
buffer_load_dwordx4 v41, s[16:19], s26 offen offset:1024 lds
v_mfma_f32_16x16x32_bf16 v[152:155], a[8:11], v[84:87], v[152:155]
v_exp_f32 v130, v130
v_mfma_f32_16x16x32_bf16 v[148:151], a[12:15], v[68:71], v[148:151]
buffer_load_dwordx4 v40, s[16:19], s26 offen offset:2048 lds
v_mfma_f32_16x16x32_bf16 v[156:159], a[12:15], v[84:87], v[156:159]
v_exp_f32 v131, v131
v_mfma_f32_16x16x32_bf16 v[144:147], a[16:19], v[72:75], v[144:147]
buffer_load_dwordx4 v41, s[16:19], s26 offen offset:3072 lds
v_mfma_f32_16x16x32_bf16 v[152:155], a[16:19], v[88:91], v[152:155]
v_exp_f32 v132, v132
v_mfma_f32_16x16x32_bf16 v[148:151], a[20:23], v[72:75], v[148:151]
s_add_u32 m0, s48, 135168
v_exp_f32 v133, v133
v_mfma_f32_16x16x32_bf16 v[156:159], a[20:23], v[88:91], v[156:159]
buffer_load_dwordx4 v40, s[16:19], s47 offen offset:0 lds
v_mfma_f32_16x16x32_bf16 v[144:147], a[24:27], v[76:79], v[144:147]
v_exp_f32 v134, v134
v_mfma_f32_16x16x32_bf16 v[152:155], a[24:27], v[92:95], v[152:155]
buffer_load_dwordx4 v41, s[16:19], s47 offen offset:1024 lds
v_mfma_f32_16x16x32_bf16 v[148:151], a[28:31], v[76:79], v[148:151]
v_exp_f32 v135, v135
v_mfma_f32_16x16x32_bf16 v[156:159], a[28:31], v[92:95], v[156:159]
buffer_load_dwordx4 v42, s[12:15], s25 offen offset:2048 lds
v_mfma_f32_16x16x32_bf16 v[208:211], v[52:55], v[232:235], v[208:211]
v_exp_f32 v136, v136
v_mfma_f32_16x16x32_bf16 v[212:215], v[52:55], v[236:239], v[212:215]
s_waitcnt lgkmcnt(0)
s_add_u32 m0, s91, 65536
v_exp_f32 v137, v137
v_mfma_f32_16x16x32_bf16 a[64:67], a[32:35], v[232:235], a[64:67]
buffer_load_dwordx4 v36, s[12:15], s24 offen offset:0 lds
v_mfma_f32_16x16x32_bf16 a[72:75], a[32:35], v[236:239], a[72:75]
v_exp_f32 v138, v138
v_mfma_f32_16x16x32_bf16 a[68:71], a[36:39], v[232:235], a[68:71]
buffer_load_dwordx4 v37, s[12:15], s24 offen offset:1024 lds
v_mfma_f32_16x16x32_bf16 a[76:79], a[36:39], v[236:239], a[76:79]
ds_read_b128 a[32:35], v63 offset:0
ds_read_b128 a[36:39], v63 offset:1024
v_mfma_f32_16x16x32_bf16 a[80:83], a[40:43], v[232:235], a[80:83]
v_exp_f32 v139, v139
v_mfma_f32_16x16x32_bf16 a[88:91], a[40:43], v[236:239], a[88:91]
buffer_load_dwordx4 v38, s[12:15], s24 offen offset:2048 lds
ds_read_b128 a[40:43], v63 offset:2048
v_mfma_f32_16x16x32_bf16 a[84:87], a[44:47], v[232:235], a[84:87]
buffer_load_dwordx4 v39, s[12:15], s24 offen offset:3072 lds
v_mfma_f32_16x16x32_bf16 a[92:95], a[44:47], v[236:239], a[92:95]
ds_read_b128 a[44:47], v63 offset:3072
v_exp_f32 v140, v140
v_mfma_f32_16x16x32_bf16 a[112:115], a[48:51], v[232:235], a[112:115]
v_cvt_pk_bf16_f32 v224, v128, v129
v_cvt_pk_bf16_f32 v225, v130, v131
v_mfma_f32_16x16x32_bf16 a[120:123], a[48:51], v[236:239], a[120:123]
v_cvt_pk_bf16_f32 v226, v132, v133
v_exp_f32 v141, v141
v_mfma_f32_16x16x32_bf16 a[116:119], a[52:55], v[232:235], a[116:119]
v_cvt_pk_bf16_f32 v227, v134, v135
v_exp_f32 v142, v142
v_mfma_f32_16x16x32_bf16 a[124:127], a[52:55], v[236:239], a[124:127]
ds_read_b128 a[48:51], v63 offset:4096
ds_read_b128 a[52:55], v63 offset:5120
v_cvt_pk_bf16_f32 v228, v136, v137
v_mfma_f32_16x16x32_bf16 a[144:147], a[56:59], v[232:235], a[144:147]
s_add_u32 s26, s26, 393216
s_add_u32 s47, s47, 393216
s_add_u32 s25, s25, 128
s_add_u32 s24, s24, 128
v_mfma_f32_16x16x32_bf16 a[152:155], a[56:59], v[236:239], a[152:155]
ds_read_b128 a[56:59], v63 offset:6144
v_exp_f32 v143, v143
v_mfma_f32_16x16x32_bf16 a[148:151], a[60:63], v[232:235], a[148:151]
v_cvt_pk_bf16_f32 v229, v138, v139
v_cvt_pk_bf16_f32 v230, v140, v141
v_cvt_pk_bf16_f32 v231, v142, v143
v_mfma_f32_16x16x32_bf16 a[156:159], a[60:63], v[236:239], a[156:159]
ds_read_b128 a[60:63], v63 offset:7168
s_waitcnt lgkmcnt(4)
v_mfma_f32_16x16x32_bf16 v[208:211], v[52:55], v[224:227], v[208:211]
v_exp_f32 v144, v144
v_exp_f32 v145, v145
v_mfma_f32_16x16x32_bf16 v[212:215], v[52:55], v[228:231], v[212:215]
s_add_u32 m0, s91, 81920
v_exp_f32 v146, v146
v_mfma_f32_16x16x32_bf16 a[64:67], a[32:35], v[224:227], a[64:67]
buffer_load_dwordx4 v36, s[12:15], s24 offen offset:0 lds
v_mfma_f32_16x16x32_bf16 a[72:75], a[32:35], v[228:231], a[72:75]
v_cvt_pk_bf16_f32 v232, v144, v145
v_exp_f32 v147, v147
v_mfma_f32_16x16x32_bf16 a[68:71], a[36:39], v[224:227], a[68:71]
buffer_load_dwordx4 v37, s[12:15], s24 offen offset:1024 lds
v_exp_f32 v148, v148
v_mfma_f32_16x16x32_bf16 a[76:79], a[36:39], v[228:231], a[76:79]
ds_read_b128 a[32:35], v63 offset:8192
ds_read_b128 a[36:39], v63 offset:9216
v_exp_f32 v149, v149
v_mfma_f32_16x16x32_bf16 a[80:83], a[40:43], v[224:227], a[80:83]
v_cvt_pk_bf16_f32 v233, v146, v147
v_exp_f32 v150, v150
v_mfma_f32_16x16x32_bf16 a[88:91], a[40:43], v[228:231], a[88:91]
ds_read_b128 a[40:43], v63 offset:10240
v_exp_f32 v151, v151
v_mfma_f32_16x16x32_bf16 a[84:87], a[44:47], v[224:227], a[84:87]
buffer_load_dwordx4 v38, s[12:15], s24 offen offset:2048 lds
v_exp_f32 v152, v152
v_mfma_f32_16x16x32_bf16 a[92:95], a[44:47], v[228:231], a[92:95]
s_waitcnt lgkmcnt(3)
ds_read_b128 a[44:47], v63 offset:11264
v_exp_f32 v153, v153
v_mfma_f32_16x16x32_bf16 a[112:115], a[48:51], v[224:227], a[112:115]
v_cvt_pk_bf16_f32 v234, v148, v149
v_exp_f32 v154, v154
v_mfma_f32_16x16x32_bf16 a[120:123], a[48:51], v[228:231], a[120:123]
buffer_load_dwordx4 v39, s[12:15], s24 offen offset:3072 lds
v_cvt_pk_bf16_f32 v235, v150, v151
v_mfma_f32_16x16x32_bf16 a[116:119], a[52:55], v[224:227], a[116:119]
s_add_u32 s24, s24, 128
v_exp_f32 v155, v155
v_exp_f32 v156, v156
v_mfma_f32_16x16x32_bf16 a[124:127], a[52:55], v[228:231], a[124:127]
ds_read_b128 a[48:51], v63 offset:12288
ds_read_b128 a[52:55], v63 offset:13312
v_cvt_pk_bf16_f32 v236, v152, v153
v_mfma_f32_16x16x32_bf16 a[144:147], a[56:59], v[224:227], a[144:147]
v_cvt_pk_bf16_f32 v237, v154, v155
v_exp_f32 v157, v157
v_mfma_f32_16x16x32_bf16 a[152:155], a[56:59], v[228:231], a[152:155]
ds_read_b128 a[56:59], v63 offset:14336
v_exp_f32 v158, v158
v_mfma_f32_16x16x32_bf16 a[148:151], a[60:63], v[224:227], a[148:151]
v_cvt_pk_bf16_f32 v238, v156, v157
v_exp_f32 v159, v159
v_mfma_f32_16x16x32_bf16 a[156:159], a[60:63], v[228:231], a[156:159]
ds_read_b128 a[60:63], v63 offset:15360
v_add_u32 v63, s49, v33
v_cvt_pk_bf16_f32 v239, v158, v159
s_cmp_lt_u32 s3, 2
s_cbranch_scc0 .LEb1
s_waitcnt vmcnt(16)
s_barrier
.LEb1:
s_waitcnt lgkmcnt(4)
v_mfma_f32_16x16x32_bf16 v[208:211], v[52:55], v[232:235], v[208:211]
s_mov_b32 s28, 1602224128
v_mfma_f32_16x16x32_bf16 v[212:215], v[52:55], v[236:239], v[212:215]
v_mfma_f32_16x16x32_bf16 a[64:67], a[32:35], v[232:235], a[64:67]
v_mfma_f32_16x16x32_bf16 a[72:75], a[32:35], v[236:239], a[72:75]
v_mfma_f32_16x16x32_bf16 a[68:71], a[36:39], v[232:235], a[68:71]
v_mfma_f32_16x16x32_bf16 a[76:79], a[36:39], v[236:239], a[76:79]
v_mfma_f32_16x16x32_bf16 a[80:83], a[40:43], v[232:235], a[80:83]
v_mfma_f32_16x16x32_bf16 a[88:91], a[40:43], v[236:239], a[88:91]
v_mfma_f32_16x16x32_bf16 a[84:87], a[44:47], v[232:235], a[84:87]
v_mfma_f32_16x16x32_bf16 a[92:95], a[44:47], v[236:239], a[92:95]
s_waitcnt lgkmcnt(0)
v_mfma_f32_16x16x32_bf16 a[112:115], a[48:51], v[232:235], a[112:115]
v_add_f32 v13, v208, v212
v_mfma_f32_16x16x32_bf16 a[120:123], a[48:51], v[236:239], a[120:123]
v_cmp_nge_f32 s[30:31], s28, v13
v_mfma_f32_16x16x32_bf16 a[116:119], a[52:55], v[232:235], a[116:119]
v_mfma_f32_16x16x32_bf16 a[124:127], a[52:55], v[236:239], a[124:127]
v_mfma_f32_16x16x32_bf16 a[144:147], a[56:59], v[232:235], a[144:147]
v_mfma_f32_16x16x32_bf16 a[152:155], a[56:59], v[236:239], a[152:155]
v_mfma_f32_16x16x32_bf16 a[148:151], a[60:63], v[232:235], a[148:151]
v_mfma_f32_16x16x32_bf16 a[156:159], a[60:63], v[236:239], a[156:159]
.LEend:
s_or_b64 s[34:35], s[30:31], s[52:53]
s_cbranch_scc1 .Lx
.Lx_ret:
v_rcp_f32 v160, v208
v_rcp_f32 v162, v212
v_mov_b32 v161, v160
v_mov_b32 v163, v162
v_accvgpr_read_b32 v164, a64
v_accvgpr_read_b32 v165, a65
v_accvgpr_read_b32 v166, a66
v_accvgpr_read_b32 v167, a67
v_accvgpr_read_b32 v168, a68
v_accvgpr_read_b32 v169, a69
v_accvgpr_read_b32 v170, a70
v_accvgpr_read_b32 v171, a71
v_pk_mul_f32 v[164:165], v[164:165], v[160:161]
v_pk_mul_f32 v[166:167], v[166:167], v[160:161]
v_pk_mul_f32 v[168:169], v[168:169], v[160:161]
v_pk_mul_f32 v[170:171], v[170:171], v[160:161]
v_cvt_pk_bf16_f32 v172, v164, v165
v_cvt_pk_bf16_f32 v173, v166, v167
v_cvt_pk_bf16_f32 v174, v168, v169
v_cvt_pk_bf16_f32 v175, v170, v171
s_add_u32 m0, s48, 98304
s_nop 0
buffer_load_dwordx4 v40, s[16:19], s26 offen offset:0 lds
buffer_load_dwordx4 v41, s[16:19], s26 offen offset:1024 lds
buffer_store_dwordx4 v[172:175], v7, s[20:23], s40 offen offset:0 sc1
v_accvgpr_read_b32 v176, a72
v_accvgpr_read_b32 v177, a73
v_accvgpr_read_b32 v178, a74
v_accvgpr_read_b32 v179, a75
v_accvgpr_read_b32 v180, a76
v_accvgpr_read_b32 v181, a77
v_accvgpr_read_b32 v182, a78
v_accvgpr_read_b32 v183, a79
v_pk_mul_f32 v[176:177], v[176:177], v[162:163]
v_pk_mul_f32 v[178:179], v[178:179], v[162:163]
v_pk_mul_f32 v[180:181], v[180:181], v[162:163]
v_pk_mul_f32 v[182:183], v[182:183], v[162:163]
v_cvt_pk_bf16_f32 v184, v176, v177
v_cvt_pk_bf16_f32 v185, v178, v179
v_cvt_pk_bf16_f32 v186, v180, v181
v_cvt_pk_bf16_f32 v187, v182, v183
buffer_load_dwordx4 v40, s[16:19], s26 offen offset:2048 lds
buffer_load_dwordx4 v41, s[16:19], s26 offen offset:3072 lds
buffer_store_dwordx4 v[184:187], v7, s[20:23], s40 offen offset:1024 sc1
v_accvgpr_read_b32 v164, a80
v_accvgpr_read_b32 v165, a81
v_accvgpr_read_b32 v166, a82
v_accvgpr_read_b32 v167, a83
v_accvgpr_read_b32 v168, a84
v_accvgpr_read_b32 v169, a85
v_accvgpr_read_b32 v170, a86
v_accvgpr_read_b32 v171, a87
v_pk_mul_f32 v[164:165], v[164:165], v[160:161]
v_pk_mul_f32 v[166:167], v[166:167], v[160:161]
v_pk_mul_f32 v[168:169], v[168:169], v[160:161]
v_pk_mul_f32 v[170:171], v[170:171], v[160:161]
v_cvt_pk_bf16_f32 v172, v164, v165
v_cvt_pk_bf16_f32 v173, v166, v167
v_cvt_pk_bf16_f32 v174, v168, v169
v_cvt_pk_bf16_f32 v175, v170, v171
s_add_u32 m0, s48, 102400
s_nop 0
buffer_load_dwordx4 v40, s[16:19], s47 offen offset:0 lds
buffer_load_dwordx4 v41, s[16:19], s47 offen offset:1024 lds
buffer_store_dwordx4 v[172:175], v7, s[20:23], s40 offen offset:2048 sc1
v_accvgpr_read_b32 v176, a88
v_accvgpr_read_b32 v177, a89
v_accvgpr_read_b32 v178, a90
v_accvgpr_read_b32 v179, a91
v_accvgpr_read_b32 v180, a92
v_accvgpr_read_b32 v181, a93
v_accvgpr_read_b32 v182, a94
v_accvgpr_read_b32 v183, a95
v_pk_mul_f32 v[176:177], v[176:177], v[162:163]
v_pk_mul_f32 v[178:179], v[178:179], v[162:163]
v_pk_mul_f32 v[180:181], v[180:181], v[162:163]
v_pk_mul_f32 v[182:183], v[182:183], v[162:163]
v_cvt_pk_bf16_f32 v184, v176, v177
v_cvt_pk_bf16_f32 v185, v178, v179
v_cvt_pk_bf16_f32 v186, v180, v181
v_cvt_pk_bf16_f32 v187, v182, v183
buffer_load_dwordx4 v42, s[12:15], s25 offen offset:2048 lds
s_add_u32 m0, s91, 0
s_nop 0
buffer_load_dwordx4 v36, s[12:15], s24 offen offset:0 lds
buffer_store_dwordx4 v[184:187], v7, s[20:23], s40 offen offset:3072 sc1
v_accvgpr_read_b32 v164, a112
v_accvgpr_read_b32 v165, a113
v_accvgpr_read_b32 v166, a114
v_accvgpr_read_b32 v167, a115
v_accvgpr_read_b32 v168, a116
v_accvgpr_read_b32 v169, a117
v_accvgpr_read_b32 v170, a118
v_accvgpr_read_b32 v171, a119
v_pk_mul_f32 v[164:165], v[164:165], v[160:161]
v_pk_mul_f32 v[166:167], v[166:167], v[160:161]
v_pk_mul_f32 v[168:169], v[168:169], v[160:161]
v_pk_mul_f32 v[170:171], v[170:171], v[160:161]
v_cvt_pk_bf16_f32 v172, v164, v165
v_cvt_pk_bf16_f32 v173, v166, v167
v_cvt_pk_bf16_f32 v174, v168, v169
v_cvt_pk_bf16_f32 v175, v170, v171
buffer_load_dwordx4 v37, s[12:15], s24 offen offset:1024 lds
buffer_load_dwordx4 v38, s[12:15], s24 offen offset:2048 lds
buffer_store_dwordx4 v[172:175], v51, s[20:23], s40 offen offset:0 sc1
v_accvgpr_read_b32 v176, a120
v_accvgpr_read_b32 v177, a121
v_accvgpr_read_b32 v178, a122
v_accvgpr_read_b32 v179, a123
v_accvgpr_read_b32 v180, a124
v_accvgpr_read_b32 v181, a125
v_accvgpr_read_b32 v182, a126
v_accvgpr_read_b32 v183, a127
v_pk_mul_f32 v[176:177], v[176:177], v[162:163]
v_pk_mul_f32 v[178:179], v[178:179], v[162:163]
v_pk_mul_f32 v[180:181], v[180:181], v[162:163]
v_pk_mul_f32 v[182:183], v[182:183], v[162:163]
v_cvt_pk_bf16_f32 v184, v176, v177
v_cvt_pk_bf16_f32 v185, v178, v179
v_cvt_pk_bf16_f32 v186, v180, v181
v_cvt_pk_bf16_f32 v187, v182, v183
buffer_load_dwordx4 v39, s[12:15], s24 offen offset:3072 lds
s_add_u32 s26, s26, 393216
s_add_u32 s47, s47, 393216
s_add_u32 s25, s25, 128
s_add_u32 s24, s24, 128
buffer_store_dwordx4 v[184:187], v51, s[20:23], s40 offen offset:1024 sc1
v_accvgpr_read_b32 v164, a144
v_accvgpr_read_b32 v165, a145
v_accvgpr_read_b32 v166, a146
v_accvgpr_read_b32 v167, a147
v_accvgpr_read_b32 v168, a148
v_accvgpr_read_b32 v169, a149
v_accvgpr_read_b32 v170, a150
v_accvgpr_read_b32 v171, a151
v_pk_mul_f32 v[164:165], v[164:165], v[160:161]
v_pk_mul_f32 v[166:167], v[166:167], v[160:161]
v_pk_mul_f32 v[168:169], v[168:169], v[160:161]
v_pk_mul_f32 v[170:171], v[170:171], v[160:161]
v_cvt_pk_bf16_f32 v172, v164, v165
v_cvt_pk_bf16_f32 v173, v166, v167
v_cvt_pk_bf16_f32 v174, v168, v169
v_cvt_pk_bf16_f32 v175, v170, v171
buffer_store_dwordx4 v[172:175], v51, s[20:23], s40 offen offset:2048 sc1
v_accvgpr_read_b32 v176, a152
v_accvgpr_read_b32 v177, a153
v_accvgpr_read_b32 v178, a154
v_accvgpr_read_b32 v179, a155
v_accvgpr_read_b32 v180, a156
v_accvgpr_read_b32 v181, a157
v_accvgpr_read_b32 v182, a158
v_accvgpr_read_b32 v183, a159
v_pk_mul_f32 v[176:177], v[176:177], v[162:163]
v_pk_mul_f32 v[178:179], v[178:179], v[162:163]
v_pk_mul_f32 v[180:181], v[180:181], v[162:163]
v_pk_mul_f32 v[182:183], v[182:183], v[162:163]
v_cvt_pk_bf16_f32 v184, v176, v177
v_cvt_pk_bf16_f32 v185, v178, v179
v_cvt_pk_bf16_f32 v186, v180, v181
v_cvt_pk_bf16_f32 v187, v182, v183
buffer_store_dwordx4 v[184:187], v51, s[20:23], s40 offen offset:3072 sc1
s_cmp_eq_u32 s41, -1
s_cbranch_scc1 .Lfb_ret2
s_getpc_b64 s[30:31]
.Lfbp:
s_add_u32 s30, s30, .Lfb_ret2-.Lfbp
s_addc_u32 s31, s31, 0
.Lfb_pad:
s_cbranch_execz .Ltch3
.Lfb:
v_rcp_f32 v160, v216
v_rcp_f32 v162, v220
v_mov_b32 v161, v160
v_mov_b32 v163, v162
v_accvgpr_read_b32 v164, a176
v_accvgpr_read_b32 v165, a177
v_accvgpr_read_b32 v166, a178
v_accvgpr_read_b32 v167, a179
v_accvgpr_read_b32 v168, a180
v_accvgpr_read_b32 v169, a181
v_accvgpr_read_b32 v170, a182
v_accvgpr_read_b32 v171, a183
v_pk_mul_f32 v[164:165], v[164:165], v[160:161]
v_pk_mul_f32 v[166:167], v[166:167], v[160:161]
v_pk_mul_f32 v[168:169], v[168:169], v[160:161]
v_pk_mul_f32 v[170:171], v[170:171], v[160:161]
v_cvt_pk_bf16_f32 v172, v164, v165
v_cvt_pk_bf16_f32 v173, v166, v167
v_cvt_pk_bf16_f32 v174, v168, v169
v_cvt_pk_bf16_f32 v175, v170, v171
buffer_store_dwordx4 v[172:175], v7, s[20:23], s41 offen offset:0 sc1
v_accvgpr_read_b32 v176, a184
v_accvgpr_read_b32 v177, a185
v_accvgpr_read_b32 v178, a186
v_accvgpr_read_b32 v179, a187
v_accvgpr_read_b32 v180, a188
v_accvgpr_read_b32 v181, a189
v_accvgpr_read_b32 v182, a190
v_accvgpr_read_b32 v183, a191
v_pk_mul_f32 v[176:177], v[176:177], v[162:163]
v_pk_mul_f32 v[178:179], v[178:179], v[162:163]
v_pk_mul_f32 v[180:181], v[180:181], v[162:163]
v_pk_mul_f32 v[182:183], v[182:183], v[162:163]
v_cvt_pk_bf16_f32 v184, v176, v177
v_cvt_pk_bf16_f32 v185, v178, v179
v_cvt_pk_bf16_f32 v186, v180, v181
v_cvt_pk_bf16_f32 v187, v182, v183
buffer_store_dwordx4 v[184:187], v7, s[20:23], s41 offen offset:1024 sc1
v_accvgpr_read_b32 v164, a208
v_accvgpr_read_b32 v165, a209
v_accvgpr_read_b32 v166, a210
v_accvgpr_read_b32 v167, a211
v_accvgpr_read_b32 v168, a212
v_accvgpr_read_b32 v169, a213
v_accvgpr_read_b32 v170, a214
v_accvgpr_read_b32 v171, a215
v_pk_mul_f32 v[164:165], v[164:165], v[160:161]
v_pk_mul_f32 v[166:167], v[166:167], v[160:161]
v_pk_mul_f32 v[168:169], v[168:169], v[160:161]
v_pk_mul_f32 v[170:171], v[170:171], v[160:161]
v_cvt_pk_bf16_f32 v172, v164, v165
v_cvt_pk_bf16_f32 v173, v166, v167
v_cvt_pk_bf16_f32 v174, v168, v169
v_cvt_pk_bf16_f32 v175, v170, v171
buffer_store_dwordx4 v[172:175], v7, s[20:23], s41 offen offset:2048 sc1
v_accvgpr_read_b32 v176, a216
v_accvgpr_read_b32 v177, a217
v_accvgpr_read_b32 v178, a218
v_accvgpr_read_b32 v179, a219
v_accvgpr_read_b32 v180, a220
v_accvgpr_read_b32 v181, a221
v_accvgpr_read_b32 v182, a222
v_accvgpr_read_b32 v183, a223
v_pk_mul_f32 v[176:177], v[176:177], v[162:163]
v_pk_mul_f32 v[178:179], v[178:179], v[162:163]
v_pk_mul_f32 v[180:181], v[180:181], v[162:163]
v_pk_mul_f32 v[182:183], v[182:183], v[162:163]
v_cvt_pk_bf16_f32 v184, v176, v177
v_cvt_pk_bf16_f32 v185, v178, v179
v_cvt_pk_bf16_f32 v186, v180, v181
v_cvt_pk_bf16_f32 v187, v182, v183
buffer_store_dwordx4 v[184:187], v7, s[20:23], s41 offen offset:3072 sc1
v_accvgpr_read_b32 v164, a240
v_accvgpr_read_b32 v165, a241
v_accvgpr_read_b32 v166, a242
v_accvgpr_read_b32 v167, a243
v_accvgpr_read_b32 v168, a244
v_accvgpr_read_b32 v169, a245
v_accvgpr_read_b32 v170, a246
v_accvgpr_read_b32 v171, a247
v_pk_mul_f32 v[164:165], v[164:165], v[160:161]
v_pk_mul_f32 v[166:167], v[166:167], v[160:161]
v_pk_mul_f32 v[168:169], v[168:169], v[160:161]
v_pk_mul_f32 v[170:171], v[170:171], v[160:161]
v_cvt_pk_bf16_f32 v172, v164, v165
v_cvt_pk_bf16_f32 v173, v166, v167
v_cvt_pk_bf16_f32 v174, v168, v169
v_cvt_pk_bf16_f32 v175, v170, v171
buffer_store_dwordx4 v[172:175], v51, s[20:23], s41 offen offset:0 sc1
v_accvgpr_read_b32 v176, a248
v_accvgpr_read_b32 v177, a249
v_accvgpr_read_b32 v178, a250
v_accvgpr_read_b32 v179, a251
v_accvgpr_read_b32 v180, a252
v_accvgpr_read_b32 v181, a253
v_accvgpr_read_b32 v182, a254
v_accvgpr_read_b32 v183, a255
v_pk_mul_f32 v[176:177], v[176:177], v[162:163]
v_pk_mul_f32 v[178:179], v[178:179], v[162:163]
v_pk_mul_f32 v[180:181], v[180:181], v[162:163]
v_pk_mul_f32 v[182:183], v[182:183], v[162:163]
v_cvt_pk_bf16_f32 v184, v176, v177
v_cvt_pk_bf16_f32 v185, v178, v179
v_cvt_pk_bf16_f32 v186, v180, v181
v_cvt_pk_bf16_f32 v187, v182, v183
buffer_store_dwordx4 v[184:187], v51, s[20:23], s41 offen offset:1024 sc1
v_accvgpr_read_b32 v164, a96
v_accvgpr_read_b32 v165, a97
v_accvgpr_read_b32 v166, a98
v_accvgpr_read_b32 v167, a99
v_accvgpr_read_b32 v168, a100
v_accvgpr_read_b32 v169, a101
v_accvgpr_read_b32 v170, a102
v_accvgpr_read_b32 v171, a103
v_pk_mul_f32 v[164:165], v[164:165], v[160:161]
v_pk_mul_f32 v[166:167], v[166:167], v[160:161]
v_pk_mul_f32 v[168:169], v[168:169], v[160:161]
v_pk_mul_f32 v[170:171], v[170:171], v[160:161]
v_cvt_pk_bf16_f32 v172, v164, v165
v_cvt_pk_bf16_f32 v173, v166, v167
v_cvt_pk_bf16_f32 v174, v168, v169
v_cvt_pk_bf16_f32 v175, v170, v171
buffer_store_dwordx4 v[172:175], v51, s[20:23], s41 offen offset:2048 sc1
v_accvgpr_read_b32 v176, a104
v_accvgpr_read_b32 v177, a105
v_accvgpr_read_b32 v178, a106
v_accvgpr_read_b32 v179, a107
v_accvgpr_read_b32 v180, a108
v_accvgpr_read_b32 v181, a109
v_accvgpr_read_b32 v182, a110
v_accvgpr_read_b32 v183, a111
v_pk_mul_f32 v[176:177], v[176:177], v[162:163]
v_pk_mul_f32 v[178:179], v[178:179], v[162:163]
v_pk_mul_f32 v[180:181], v[180:181], v[162:163]
v_pk_mul_f32 v[182:183], v[182:183], v[162:163]
v_cvt_pk_bf16_f32 v184, v176, v177
v_cvt_pk_bf16_f32 v185, v178, v179
v_cvt_pk_bf16_f32 v186, v180, v181
v_cvt_pk_bf16_f32 v187, v182, v183
buffer_store_dwordx4 v[184:187], v51, s[20:23], s41 offen offset:3072 sc1
s_mov_b32 s41, -1
s_setpc_b64 s[30:31]
.Lfb_ret2:
s_cmp_eq_u32 s3, 0
s_cbranch_scc0 .LEend1
s_mov_b64 exec, 0
s_branch .Lpad14
.Ltch5:
s_branch .Lpad15
.Ltch6:
s_branch .Lpad16
.Ltch7:
s_branch .Lpad17
.Ltch8:
s_mov_b64 exec, -1
s_branch .Lfb_ret
.LEend1:
s_cmp_eq_u32 s3, 1
s_cbranch_scc0 .Lfb_ret
s_mov_b64 exec, 0
s_branch .Lpad18
.Ltch9:
s_branch .Lpad19
.Ltch10:
s_mov_b64 exec, -1
.Lfb_ret:
s_cmp_lt_u32 s3, 2
s_waitcnt vmcnt(23)
s_barrier
ds_read_b128 a[128:131], v45
ds_read_b128 a[132:135], v43 offset:0
ds_read_b64_tr_b16 a[164:165], v47 offset:0
ds_read_b64_tr_b16 a[166:167], v47 offset:512
ds_read_b64_tr_b16 a[168:169], v48 offset:0
ds_read_b64_tr_b16 a[170:171], v48 offset:512
ds_read_b128 a[136:139], v43 offset:2048
ds_read_b128 a[140:143], v43 offset:4096
ds_read_b128 a[160:163], v43 offset:6144
ds_read_b64_tr_b16 a[172:173], v47 offset:2048
ds_read_b64_tr_b16 a[174:175], v47 offset:2560
ds_read_b64_tr_b16 a[192:193], v48 offset:2048
ds_read_b64_tr_b16 a[194:195], v48 offset:2560
s_cselect_b32 s39, 1, 0
s_waitcnt lgkmcnt(8)
ds_read_b64_tr_b16 a[196:197], v47 offset:4096
ds_read_b64_tr_b16 a[198:199], v47 offset:4608
ds_read_b64_tr_b16 a[200:201], v48 offset:4096
ds_read_b64_tr_b16 a[202:203], v48 offset:4608
s_waitcnt lgkmcnt(0)
v_mfma_f32_16x16x32_bf16 v[160:163], a[172:175], a[128:131], 0
s_add_u32 m0, s48, 32768
v_mfma_f32_16x16x32_bf16 v[164:167], a[192:195], a[128:131], 0
buffer_load_dwordx4 v40, s[16:19], s26 offen offset:0 lds
v_mfma_f32_16x16x32_bf16 v[200:203], a[128:131], a[196:199], 0
ds_read_b64_tr_b16 a[20:21], v47 offset:14336
ds_read_b64_tr_b16 a[22:23], v47 offset:14848
v_mfma_f32_16x16x32_bf16 v[204:207], a[128:131], a[200:203], 0
ds_read_b128 a[0:3], v46
v_mfma_f32_16x16x32_bf16 v[128:131], a[164:167], a[132:135], 0
buffer_load_dwordx4 v41, s[16:19], s26 offen offset:1024 lds
v_mfma_f32_16x16x32_bf16 v[132:135], a[168:171], a[132:135], 0
ds_read_b64_tr_b16 a[24:25], v48 offset:14336
ds_read_b64_tr_b16 a[26:27], v48 offset:14848
v_mfma_f32_16x16x32_bf16 v[168:171], a[172:175], a[132:135], 0
ds_read_b128 a[4:7], v44 offset:0
.Lpad14:
s_cbranch_execz .Ltch5
v_mfma_f32_16x16x32_bf16 v[172:175], a[192:195], a[132:135], 0
buffer_load_dwordx4 v40, s[16:19], s26 offen offset:2048 lds
v_mfma_f32_16x16x32_bf16 v[208:211], a[132:135], a[196:199], 0
ds_read_b64_tr_b16 a[28:29], v47 offset:16384
ds_read_b64_tr_b16 a[30:31], v47 offset:16896
v_mfma_f32_16x16x32_bf16 v[212:215], a[132:135], a[200:203], 0
ds_read_b128 a[8:11], v44 offset:2048
v_mfma_f32_16x16x32_bf16 v[136:139], a[164:167], a[136:139], 0
buffer_load_dwordx4 v41, s[16:19], s26 offen offset:3072 lds
v_mfma_f32_16x16x32_bf16 v[140:143], a[168:171], a[136:139], 0
ds_read_b64_tr_b16 a[32:33], v48 offset:16384
ds_read_b64_tr_b16 a[34:35], v48 offset:16896
v_mfma_f32_16x16x32_bf16 v[176:179], a[172:175], a[136:139], 0
s_add_u32 m0, s48, 36864
ds_read_b128 a[12:15], v44 offset:4096
v_mfma_f32_16x16x32_bf16 v[180:183], a[192:195], a[136:139], 0
buffer_load_dwordx4 v40, s[16:19], s47 offen offset:0 lds
v_mfma_f32_16x16x32_bf16 v[216:219], a[136:139], a[196:199], 0
ds_read_b64_tr_b16 a[36:37], v47 offset:18432
ds_read_b64_tr_b16 a[38:39], v47 offset:18944
v_mfma_f32_16x16x32_bf16 v[220:223], a[136:139], a[200:203], 0
ds_read_b128 a[16:19], v44 offset:6144
v_mfma_f32_16x16x32_bf16 v[144:147], a[164:167], a[140:143], 0
buffer_load_dwordx4 v41, s[16:19], s47 offen offset:1024 lds
v_mfma_f32_16x16x32_bf16 v[148:151], a[168:171], a[140:143], 0
ds_read_b64_tr_b16 a[40:41], v48 offset:18432
ds_read_b64_tr_b16 a[42:43], v48 offset:18944
v_mfma_f32_16x16x32_bf16 v[184:187], a[172:175], a[140:143], 0
s_cmp_eq_u32 s39, 1
s_mov_b32 s28, 16384
s_cselect_b32 s28, 4294934528, s28
v_mfma_f32_16x16x32_bf16 v[188:191], a[192:195], a[140:143], 0
buffer_load_dwordx4 v42, s[12:15], s25 offen offset:2048 lds
v_mfma_f32_16x16x32_bf16 v[224:227], a[140:143], a[196:199], 0
s_add_u32 s26, s26, 393216
s_add_u32 s47, s47, 393216
s_add_u32 s25, s25, 128
v_mfma_f32_16x16x32_bf16 v[228:231], a[140:143], a[200:203], 0
s_add_u32 m0, s91, 16384
v_add_u32 v43, 16384, v43
v_add_u32 v44, 16384, v44
v_mfma_f32_16x16x32_bf16 v[152:155], a[164:167], a[160:163], 0
buffer_load_dwordx4 v36, s[12:15], s24 offen offset:0 lds
v_mfma_f32_16x16x32_bf16 v[156:159], a[168:171], a[160:163], 0
v_add_u32 v47, 4294934528, v47
v_add_u32 v48, 4294934528, v48
v_mfma_f32_16x16x32_bf16 v[192:195], a[172:175], a[160:163], 0
buffer_load_dwordx4 v37, s[12:15], s24 offen offset:1024 lds
v_mfma_f32_16x16x32_bf16 v[196:199], a[192:195], a[160:163], 0
v_add_u32 v45, s28, v45
v_add_u32 v46, s28, v46
v_mfma_f32_16x16x32_bf16 v[232:235], a[160:163], a[196:199], 0
buffer_load_dwordx4 v38, s[12:15], s24 offen offset:2048 lds
v_mfma_f32_16x16x32_bf16 v[236:239], a[160:163], a[200:203], 0
s_waitcnt vmcnt(19) lgkmcnt(0)
s_barrier
v_mfma_f32_16x16x32_bf16 v[160:163], a[28:31], a[0:3], v[160:163]
buffer_load_dwordx4 v39, s[12:15], s24 offen offset:3072 lds
v_mfma_f32_16x16x32_bf16 v[164:167], a[32:35], a[0:3], v[164:167]
s_add_u32 s24, s24, 128
v_mfma_f32_16x16x32_bf16 v[200:203], a[0:3], a[36:39], v[200:203]
s_add_u32 m0, s48, 131072
v_mfma_f32_16x16x32_bf16 v[204:207], a[0:3], a[40:43], v[204:207]
buffer_load_dwordx4 v40, s[16:19], s26 offen offset:0 lds
v_mfma_f32_16x16x32_bf16 v[128:131], a[20:23], a[4:7], v[128:131]
ds_read_b64_tr_b16 a[164:165], v47 offset:0
ds_read_b64_tr_b16 a[166:167], v47 offset:512
v_mfma_f32_16x16x32_bf16 v[132:135], a[24:27], a[4:7], v[132:135]
ds_read_b128 a[128:131], v45
v_mfma_f32_16x16x32_bf16 v[168:171], a[28:31], a[4:7], v[168:171]
ds_read_b64_tr_b16 a[168:169], v48 offset:0
ds_read_b64_tr_b16 a[170:171], v48 offset:512
v_mfma_f32_16x16x32_bf16 v[172:175], a[32:35], a[4:7], v[172:175]
buffer_load_dwordx4 v41, s[16:19], s26 offen offset:1024 lds
v_mfma_f32_16x16x32_bf16 v[208:211], a[4:7], a[36:39], v[208:211]
ds_read_b128 a[132:135], v43 offset:0
v_mfma_f32_16x16x32_bf16 v[212:215], a[4:7], a[40:43], v[212:215]
ds_read_b64_tr_b16 a[172:173], v47 offset:2048
ds_read_b64_tr_b16 a[174:175], v47 offset:2560
v_mfma_f32_16x16x32_bf16 v[136:139], a[20:23], a[8:11], v[136:139]
ds_read_b128 a[136:139], v43 offset:2048
v_mfma_f32_16x16x32_bf16 v[140:143], a[24:27], a[8:11], v[140:143]
ds_read_b64_tr_b16 a[192:193], v48 offset:2048
ds_read_b64_tr_b16 a[194:195], v48 offset:2560
v_mfma_f32_16x16x32_bf16 v[176:179], a[28:31], a[8:11], v[176:179]
buffer_load_dwordx4 v40, s[16:19], s26 offen offset:2048 lds
v_mfma_f32_16x16x32_bf16 v[180:183], a[32:35], a[8:11], v[180:183]
ds_read_b128 a[140:143], v43 offset:4096
v_mfma_f32_16x16x32_bf16 v[216:219], a[8:11], a[36:39], v[216:219]
ds_read_b64_tr_b16 a[196:197], v47 offset:4096
ds_read_b64_tr_b16 a[198:199], v47 offset:4608
v_mfma_f32_16x16x32_bf16 v[220:223], a[8:11], a[40:43], v[220:223]
ds_read_b128 a[160:163], v43 offset:6144
v_mfma_f32_16x16x32_bf16 v[144:147], a[20:23], a[12:15], v[144:147]
ds_read_b64_tr_b16 a[200:201], v48 offset:4096
ds_read_b64_tr_b16 a[202:203], v48 offset:4608
v_mfma_f32_16x16x32_bf16 v[148:151], a[24:27], a[12:15], v[148:151]
buffer_load_dwordx4 v41, s[16:19], s26 offen offset:3072 lds
v_mfma_f32_16x16x32_bf16 v[184:187], a[28:31], a[12:15], v[184:187]
v_mfma_f32_16x16x32_bf16 v[188:191], a[32:35], a[12:15], v[188:191]
v_mfma_f32_16x16x32_bf16 v[224:227], a[12:15], a[36:39], v[224:227]
v_mfma_f32_16x16x32_bf16 v[228:231], a[12:15], a[40:43], v[228:231]
s_add_u32 m0, s48, 135168
v_mfma_f32_16x16x32_bf16 v[152:155], a[20:23], a[16:19], v[152:155]
buffer_load_dwordx4 v40, s[16:19], s47 offen offset:0 lds
v_mfma_f32_16x16x32_bf16 v[156:159], a[24:27], a[16:19], v[156:159]
v_mfma_f32_16x16x32_bf16 v[192:195], a[28:31], a[16:19], v[192:195]
v_mfma_f32_16x16x32_bf16 v[196:199], a[32:35], a[16:19], v[196:199]
v_mfma_f32_16x16x32_bf16 v[232:235], a[16:19], a[36:39], v[232:235]
buffer_load_dwordx4 v41, s[16:19], s47 offen offset:1024 lds
v_mfma_f32_16x16x32_bf16 v[236:239], a[16:19], a[40:43], v[236:239]
s_waitcnt lgkmcnt(0)
v_mfma_f32_16x16x32_bf16 v[160:163], a[172:175], a[128:131], v[160:163]
ds_read_b64_tr_b16 a[20:21], v47 offset:14336
ds_read_b64_tr_b16 a[22:23], v47 offset:14848
v_mfma_f32_16x16x32_bf16 v[164:167], a[192:195], a[128:131], v[164:167]
ds_read_b128 a[0:3], v46
v_mfma_f32_16x16x32_bf16 v[200:203], a[128:131], a[196:199], v[200:203]
ds_read_b64_tr_b16 a[24:25], v48 offset:14336
ds_read_b64_tr_b16 a[26:27], v48 offset:14848
v_mfma_f32_16x16x32_bf16 v[204:207], a[128:131], a[200:203], v[204:207]
buffer_load_dwordx4 v42, s[12:15], s25 offen offset:2048 lds
v_mfma_f32_16x16x32_bf16 v[128:131], a[164:167], a[132:135], v[128:131]
s_add_u32 s26, s26, 393216
s_add_u32 s47, s47, 393216
v_mfma_f32_16x16x32_bf16 v[132:135], a[168:171], a[132:135], v[132:135]
s_add_u32 s25, s25, 128
ds_read_b128 a[4:7], v44 offset:0
v_mfma_f32_16x16x32_bf16 v[168:171], a[172:175], a[132:135], v[168:171]
ds_read_b64_tr_b16 a[28:29], v47 offset:16384
ds_read_b64_tr_b16 a[30:31], v47 offset:16896
v_mfma_f32_16x16x32_bf16 v[172:175], a[192:195], a[132:135], v[172:175]
ds_read_b128 a[8:11], v44 offset:2048
v_mfma_f32_16x16x32_bf16 v[208:211], a[132:135], a[196:199], v[208:211]
s_add_u32 m0, s91, 65536
v_mfma_f32_16x16x32_bf16 v[212:215], a[132:135], a[200:203], v[212:215]
buffer_load_dwordx4 v36, s[12:15], s24 offen offset:0 lds
v_mfma_f32_16x16x32_bf16 v[136:139], a[164:167], a[136:139], v[136:139]
ds_read_b64_tr_b16 a[32:33], v48 offset:16384
ds_read_b64_tr_b16 a[34:35], v48 offset:16896
v_mfma_f32_16x16x32_bf16 v[140:143], a[168:171], a[136:139], v[140:143]
ds_read_b128 a[12:15], v44 offset:4096
v_mfma_f32_16x16x32_bf16 v[176:179], a[172:175], a[136:139], v[176:179]
ds_read_b64_tr_b16 a[36:37], v47 offset:18432
ds_read_b64_tr_b16 a[38:39], v47 offset:18944
v_mfma_f32_16x16x32_bf16 v[180:183], a[192:195], a[136:139], v[180:183]
ds_read_b128 a[16:19], v44 offset:6144
v_mfma_f32_16x16x32_bf16 v[216:219], a[136:139], a[196:199], v[216:219]
buffer_load_dwordx4 v37, s[12:15], s24 offen offset:1024 lds
v_mfma_f32_16x16x32_bf16 v[220:223], a[136:139], a[200:203], v[220:223]
ds_read_b64_tr_b16 a[40:41], v48 offset:18432
ds_read_b64_tr_b16 a[42:43], v48 offset:18944
v_mfma_f32_16x16x32_bf16 v[144:147], a[164:167], a[140:143], v[144:147]
s_cmp_eq_u32 s39, 1
s_mov_b32 s28, 4294885376
s_cselect_b32 s28, 4294901760, s28
v_mfma_f32_16x16x32_bf16 v[148:151], a[168:171], a[140:143], v[148:151]
v_add_u32 v43, 4294885376, v43
v_add_u32 v44, 4294885376, v44
v_mfma_f32_16x16x32_bf16 v[184:187], a[172:175], a[140:143], v[184:187]
v_add_u32 v47, 4294901760, v47
v_add_u32 v48, 4294901760, v48
v_mfma_f32_16x16x32_bf16 v[188:191], a[192:195], a[140:143], v[188:191]
buffer_load_dwordx4 v38, s[12:15], s24 offen offset:2048 lds
v_mfma_f32_16x16x32_bf16 v[224:227], a[140:143], a[196:199], v[224:227]
v_add_u32 v45, s28, v45
v_add_u32 v46, s28, v46
v_mfma_f32_16x16x32_bf16 v[228:231], a[140:143], a[200:203], v[228:231]
v_mfma_f32_16x16x32_bf16 v[152:155], a[164:167], a[160:163], v[152:155]
v_mfma_f32_16x16x32_bf16 v[156:159], a[168:171], a[160:163], v[156:159]
v_mfma_f32_16x16x32_bf16 v[192:195], a[172:175], a[160:163], v[192:195]
buffer_load_dwordx4 v39, s[12:15], s24 offen offset:3072 lds
v_mfma_f32_16x16x32_bf16 v[196:199], a[192:195], a[160:163], v[196:199]
s_add_u32 s24, s24, 128
v_mfma_f32_16x16x32_bf16 v[232:235], a[160:163], a[196:199], v[232:235]
v_mfma_f32_16x16x32_bf16 v[236:239], a[160:163], a[200:203], v[236:239]
s_waitcnt vmcnt(15) lgkmcnt(0)
s_barrier
v_mfma_f32_16x16x32_bf16 v[160:163], a[28:31], a[0:3], v[160:163]
ds_read_b64_tr_b16 a[164:165], v47 offset:0
ds_read_b64_tr_b16 a[166:167], v47 offset:512
v_mfma_f32_16x16x32_bf16 v[164:167], a[32:35], a[0:3], v[164:167]
s_add_u32 m0, s48, 98304
ds_read_b128 a[128:131], v45
v_mfma_f32_16x16x32_bf16 v[200:203], a[0:3], a[36:39], v[200:203]
buffer_load_dwordx4 v40, s[16:19], s26 offen offset:0 lds
v_mfma_f32_16x16x32_bf16 v[204:207], a[0:3], a[40:43], v[204:207]
ds_read_b64_tr_b16 a[168:169], v48 offset:0
ds_read_b64_tr_b16 a[170:171], v48 offset:512
v_mfma_f32_16x16x32_bf16 v[128:131], a[20:23], a[4:7], v[128:131]
ds_read_b128 a[132:135], v43 offset:0
v_mfma_f32_16x16x32_bf16 v[132:135], a[24:27], a[4:7], v[132:135]
ds_read_b64_tr_b16 a[172:173], v47 offset:2048
ds_read_b64_tr_b16 a[174:175], v47 offset:2560
v_mfma_f32_16x16x32_bf16 v[168:171], a[28:31], a[4:7], v[168:171]
ds_read_b128 a[136:139], v43 offset:2048
v_mfma_f32_16x16x32_bf16 v[172:175], a[32:35], a[4:7], v[172:175]
buffer_load_dwordx4 v41, s[16:19], s26 offen offset:1024 lds
v_mfma_f32_16x16x32_bf16 v[208:211], a[4:7], a[36:39], v[208:211]
ds_read_b64_tr_b16 a[192:193], v48 offset:2048
ds_read_b64_tr_b16 a[194:195], v48 offset:2560
v_mfma_f32_16x16x32_bf16 v[212:215], a[4:7], a[40:43], v[212:215]
ds_read_b128 a[140:143], v43 offset:4096
v_mfma_f32_16x16x32_bf16 v[136:139], a[20:23], a[8:11], v[136:139]
ds_read_b64_tr_b16 a[196:197], v47 offset:4096
ds_read_b64_tr_b16 a[198:199], v47 offset:4608
v_mfma_f32_16x16x32_bf16 v[140:143], a[24:27], a[8:11], v[140:143]
ds_read_b128 a[160:163], v43 offset:6144
v_mfma_f32_16x16x32_bf16 v[176:179], a[28:31], a[8:11], v[176:179]
buffer_load_dwordx4 v40, s[16:19], s26 offen offset:2048 lds
v_mfma_f32_16x16x32_bf16 v[180:183], a[32:35], a[8:11], v[180:183]
ds_read_b64_tr_b16 a[200:201], v48 offset:4096
ds_read_b64_tr_b16 a[202:203], v48 offset:4608
v_mfma_f32_16x16x32_bf16 v[216:219], a[8:11], a[36:39], v[216:219]
v_mfma_f32_16x16x32_bf16 v[220:223], a[8:11], a[40:43], v[220:223]
v_mfma_f32_16x16x32_bf16 v[144:147], a[20:23], a[12:15], v[144:147]
v_mfma_f32_16x16x32_bf16 v[148:151], a[24:27], a[12:15], v[148:151]
buffer_load_dwordx4 v41, s[16:19], s26 offen offset:3072 lds
v_mfma_f32_16x16x32_bf16 v[184:187], a[28:31], a[12:15], v[184:187]
v_mfma_f32_16x16x32_bf16 v[188:191], a[32:35], a[12:15], v[188:191]
v_mfma_f32_16x16x32_bf16 v[224:227], a[12:15], a[36:39], v[224:227]
v_mfma_f32_16x16x32_bf16 v[228:231], a[12:15], a[40:43], v[228:231]
s_add_u32 m0, s48, 102400
v_mfma_f32_16x16x32_bf16 v[152:155], a[20:23], a[16:19], v[152:155]
buffer_load_dwordx4 v40, s[16:19], s47 offen offset:0 lds
v_mfma_f32_16x16x32_bf16 v[156:159], a[24:27], a[16:19], v[156:159]
v_mfma_f32_16x16x32_bf16 v[192:195], a[28:31], a[16:19], v[192:195]
v_mfma_f32_16x16x32_bf16 v[196:199], a[32:35], a[16:19], v[196:199]
v_mfma_f32_16x16x32_bf16 v[232:235], a[16:19], a[36:39], v[232:235]
buffer_load_dwordx4 v41, s[16:19], s47 offen offset:1024 lds
v_mfma_f32_16x16x32_bf16 v[236:239], a[16:19], a[40:43], v[236:239]
s_waitcnt lgkmcnt(0)
v_mfma_f32_16x16x32_bf16 v[160:163], a[172:175], a[128:131], v[160:163]
ds_read_b64_tr_b16 a[20:21], v47 offset:14336
ds_read_b64_tr_b16 a[22:23], v47 offset:14848
v_mfma_f32_16x16x32_bf16 v[164:167], a[192:195], a[128:131], v[164:167]
ds_read_b128 a[0:3], v46
v_mfma_f32_16x16x32_bf16 v[200:203], a[128:131], a[196:199], v[200:203]
ds_read_b64_tr_b16 a[24:25], v48 offset:14336
ds_read_b64_tr_b16 a[26:27], v48 offset:14848
v_mfma_f32_16x16x32_bf16 v[204:207], a[128:131], a[200:203], v[204:207]
buffer_load_dwordx4 v42, s[12:15], s25 offen offset:2048 lds
v_mfma_f32_16x16x32_bf16 v[128:131], a[164:167], a[132:135], v[128:131]
s_add_u32 s26, s26, 393216
s_add_u32 s47, s47, 393216
v_mfma_f32_16x16x32_bf16 v[132:135], a[168:171], a[132:135], v[132:135]
s_add_u32 s25, s25, 128
ds_read_b128 a[4:7], v44 offset:0
v_mfma_f32_16x16x32_bf16 v[168:171], a[172:175], a[132:135], v[168:171]
ds_read_b64_tr_b16 a[28:29], v47 offset:16384
ds_read_b64_tr_b16 a[30:31], v47 offset:16896
v_mfma_f32_16x16x32_bf16 v[172:175], a[192:195], a[132:135], v[172:175]
ds_read_b128 a[8:11], v44 offset:2048
v_mfma_f32_16x16x32_bf16 v[208:211], a[132:135], a[196:199], v[208:211]
s_add_u32 m0, s91, 81920
v_mfma_f32_16x16x32_bf16 v[212:215], a[132:135], a[200:203], v[212:215]
buffer_load_dwordx4 v36, s[12:15], s24 offen offset:0 lds
v_mfma_f32_16x16x32_bf16 v[136:139], a[164:167], a[136:139], v[136:139]
ds_read_b64_tr_b16 a[32:33], v48 offset:16384
ds_read_b64_tr_b16 a[34:35], v48 offset:16896
v_mfma_f32_16x16x32_bf16 v[140:143], a[168:171], a[136:139], v[140:143]
ds_read_b128 a[12:15], v44 offset:4096
v_mfma_f32_16x16x32_bf16 v[176:179], a[172:175], a[136:139], v[176:179]
ds_read_b64_tr_b16 a[36:37], v47 offset:18432
ds_read_b64_tr_b16 a[38:39], v47 offset:18944
v_mfma_f32_16x16x32_bf16 v[180:183], a[192:195], a[136:139], v[180:183]
ds_read_b128 a[16:19], v44 offset:6144
v_mfma_f32_16x16x32_bf16 v[216:219], a[136:139], a[196:199], v[216:219]
buffer_load_dwordx4 v37, s[12:15], s24 offen offset:1024 lds
v_mfma_f32_16x16x32_bf16 v[220:223], a[136:139], a[200:203], v[220:223]
ds_read_b64_tr_b16 a[40:41], v48 offset:18432
ds_read_b64_tr_b16 a[42:43], v48 offset:18944
v_mfma_f32_16x16x32_bf16 v[144:147], a[164:167], a[140:143], v[144:147]
s_cmp_eq_u32 s39, 1
s_mov_b32 s28, 16384
s_cselect_b32 s28, 98304, s28
v_mfma_f32_16x16x32_bf16 v[148:151], a[168:171], a[140:143], v[148:151]
v_add_u32 v43, 16384, v43
v_add_u32 v44, 16384, v44
v_mfma_f32_16x16x32_bf16 v[184:187], a[172:175], a[140:143], v[184:187]
v_add_u32 v47, 98304, v47
v_add_u32 v48, 98304, v48
v_mfma_f32_16x16x32_bf16 v[188:191], a[192:195], a[140:143], v[188:191]
buffer_load_dwordx4 v38, s[12:15], s24 offen offset:2048 lds
v_mfma_f32_16x16x32_bf16 v[224:227], a[140:143], a[196:199], v[224:227]
v_add_u32 v45, s28, v45
v_add_u32 v46, s28, v46
v_mfma_f32_16x16x32_bf16 v[228:231], a[140:143], a[200:203], v[228:231]
v_mfma_f32_16x16x32_bf16 v[152:155], a[164:167], a[160:163], v[152:155]
v_mfma_f32_16x16x32_bf16 v[156:159], a[168:171], a[160:163], v[156:159]
v_mfma_f32_16x16x32_bf16 v[192:195], a[172:175], a[160:163], v[192:195]
buffer_load_dwordx4 v39, s[12:15], s24 offen offset:3072 lds
v_mfma_f32_16x16x32_bf16 v[196:199], a[192:195], a[160:163], v[196:199]
s_add_u32 s24, s24, 128
v_mfma_f32_16x16x32_bf16 v[232:235], a[160:163], a[196:199], v[232:235]
v_or_b32 v0, 3, v11
v_mfma_f32_16x16x32_bf16 v[236:239], a[160:163], a[200:203], v[236:239]
s_waitcnt vmcnt(33)
s_waitcnt vmcnt(15) lgkmcnt(0)
s_barrier
v_mfma_f32_16x16x32_bf16 v[160:163], a[28:31], a[0:3], v[160:163]
buffer_store_dword v0, v6, s[60:63], 0 offen sc1
ds_read_b64_tr_b16 a[164:165], v47 offset:0
ds_read_b64_tr_b16 a[166:167], v47 offset:512
v_mfma_f32_16x16x32_bf16 v[164:167], a[32:35], a[0:3], v[164:167]
s_add_u32 m0, s48, 32768
ds_read_b128 a[128:131], v45
v_mfma_f32_16x16x32_bf16 v[200:203], a[0:3], a[36:39], v[200:203]
buffer_load_dwordx4 v40, s[16:19], s26 offen offset:0 lds
v_mfma_f32_16x16x32_bf16 v[204:207], a[0:3], a[40:43], v[204:207]
ds_read_b64_tr_b16 a[168:169], v48 offset:0
ds_read_b64_tr_b16 a[170:171], v48 offset:512
v_mfma_f32_16x16x32_bf16 v[128:131], a[20:23], a[4:7], v[128:131]
ds_read_b128 a[132:135], v43 offset:0
v_mfma_f32_16x16x32_bf16 v[132:135], a[24:27], a[4:7], v[132:135]
ds_read_b64_tr_b16 a[172:173], v47 offset:2048
ds_read_b64_tr_b16 a[174:175], v47 offset:2560
v_mfma_f32_16x16x32_bf16 v[168:171], a[28:31], a[4:7], v[168:171]
ds_read_b128 a[136:139], v43 offset:2048
v_mfma_f32_16x16x32_bf16 v[172:175], a[32:35], a[4:7], v[172:175]
buffer_load_dwordx4 v41, s[16:19], s26 offen offset:1024 lds
v_mfma_f32_16x16x32_bf16 v[208:211], a[4:7], a[36:39], v[208:211]
ds_read_b64_tr_b16 a[192:193], v48 offset:2048
ds_read_b64_tr_b16 a[194:195], v48 offset:2560
v_mfma_f32_16x16x32_bf16 v[212:215], a[4:7], a[40:43], v[212:215]
ds_read_b128 a[140:143], v43 offset:4096
v_mfma_f32_16x16x32_bf16 v[136:139], a[20:23], a[8:11], v[136:139]
ds_read_b64_tr_b16 a[196:197], v47 offset:4096
ds_read_b64_tr_b16 a[198:199], v47 offset:4608
v_mfma_f32_16x16x32_bf16 v[140:143], a[24:27], a[8:11], v[140:143]
ds_read_b128 a[160:163], v43 offset:6144
v_mfma_f32_16x16x32_bf16 v[176:179], a[28:31], a[8:11], v[176:179]
buffer_load_dwordx4 v40, s[16:19], s26 offen offset:2048 lds
v_mfma_f32_16x16x32_bf16 v[180:183], a[32:35], a[8:11], v[180:183]
ds_read_b64_tr_b16 a[200:201], v48 offset:4096
ds_read_b64_tr_b16 a[202:203], v48 offset:4608
v_mfma_f32_16x16x32_bf16 v[216:219], a[8:11], a[36:39], v[216:219]
v_mfma_f32_16x16x32_bf16 v[220:223], a[8:11], a[40:43], v[220:223]
v_mfma_f32_16x16x32_bf16 v[144:147], a[20:23], a[12:15], v[144:147]
v_mfma_f32_16x16x32_bf16 v[148:151], a[24:27], a[12:15], v[148:151]
buffer_load_dwordx4 v41, s[16:19], s26 offen offset:3072 lds
v_mfma_f32_16x16x32_bf16 v[184:187], a[28:31], a[12:15], v[184:187]
v_mfma_f32_16x16x32_bf16 v[188:191], a[32:35], a[12:15], v[188:191]
v_mfma_f32_16x16x32_bf16 v[224:227], a[12:15], a[36:39], v[224:227]
v_mfma_f32_16x16x32_bf16 v[228:231], a[12:15], a[40:43], v[228:231]
s_add_u32 m0, s48, 36864
v_mfma_f32_16x16x32_bf16 v[152:155], a[20:23], a[16:19], v[152:155]
buffer_load_dwordx4 v40, s[16:19], s47 offen offset:0 lds
v_mfma_f32_16x16x32_bf16 v[156:159], a[24:27], a[16:19], v[156:159]
v_mfma_f32_16x16x32_bf16 v[192:195], a[28:31], a[16:19], v[192:195]
v_mfma_f32_16x16x32_bf16 v[196:199], a[32:35], a[16:19], v[196:199]
v_mfma_f32_16x16x32_bf16 v[232:235], a[16:19], a[36:39], v[232:235]
buffer_load_dwordx4 v41, s[16:19], s47 offen offset:1024 lds
v_mfma_f32_16x16x32_bf16 v[236:239], a[16:19], a[40:43], v[236:239]
s_waitcnt lgkmcnt(0)
v_mfma_f32_16x16x32_bf16 v[160:163], a[172:175], a[128:131], v[160:163]
ds_read_b64_tr_b16 a[20:21], v47 offset:14336
ds_read_b64_tr_b16 a[22:23], v47 offset:14848
v_mfma_f32_16x16x32_bf16 v[164:167], a[192:195], a[128:131], v[164:167]
ds_read_b128 a[0:3], v46
v_mfma_f32_16x16x32_bf16 v[200:203], a[128:131], a[196:199], v[200:203]
ds_read_b64_tr_b16 a[24:25], v48 offset:14336
ds_read_b64_tr_b16 a[26:27], v48 offset:14848
v_mfma_f32_16x16x32_bf16 v[204:207], a[128:131], a[200:203], v[204:207]
buffer_load_dwordx4 v42, s[12:15], s25 offen offset:2048 lds
v_mfma_f32_16x16x32_bf16 v[128:131], a[164:167], a[132:135], v[128:131]
s_add_u32 s26, s26, 393216
s_add_u32 s47, s47, 393216
v_mfma_f32_16x16x32_bf16 v[132:135], a[168:171], a[132:135], v[132:135]
s_add_u32 s25, s25, 128
ds_read_b128 a[4:7], v44 offset:0
v_mfma_f32_16x16x32_bf16 v[168:171], a[172:175], a[132:135], v[168:171]
ds_read_b64_tr_b16 a[28:29], v47 offset:16384
ds_read_b64_tr_b16 a[30:31], v47 offset:16896
v_mfma_f32_16x16x32_bf16 v[172:175], a[192:195], a[132:135], v[172:175]
ds_read_b128 a[8:11], v44 offset:2048
v_mfma_f32_16x16x32_bf16 v[208:211], a[132:135], a[196:199], v[208:211]
s_add_u32 m0, s91, 0
v_mfma_f32_16x16x32_bf16 v[212:215], a[132:135], a[200:203], v[212:215]
buffer_load_dwordx4 v36, s[12:15], s24 offen offset:0 lds
v_mfma_f32_16x16x32_bf16 v[136:139], a[164:167], a[136:139], v[136:139]
ds_read_b64_tr_b16 a[32:33], v48 offset:16384
ds_read_b64_tr_b16 a[34:35], v48 offset:16896
v_mfma_f32_16x16x32_bf16 v[140:143], a[168:171], a[136:139], v[140:143]
ds_read_b128 a[12:15], v44 offset:4096
v_mfma_f32_16x16x32_bf16 v[176:179], a[172:175], a[136:139], v[176:179]
ds_read_b64_tr_b16 a[36:37], v47 offset:18432
ds_read_b64_tr_b16 a[38:39], v47 offset:18944
v_mfma_f32_16x16x32_bf16 v[180:183], a[192:195], a[136:139], v[180:183]
ds_read_b128 a[16:19], v44 offset:6144
v_mfma_f32_16x16x32_bf16 v[216:219], a[136:139], a[196:199], v[216:219]
buffer_load_dwordx4 v37, s[12:15], s24 offen offset:1024 lds
v_mfma_f32_16x16x32_bf16 v[220:223], a[136:139], a[200:203], v[220:223]
ds_read_b64_tr_b16 a[40:41], v48 offset:18432
ds_read_b64_tr_b16 a[42:43], v48 offset:18944
v_mfma_f32_16x16x32_bf16 v[144:147], a[164:167], a[140:143], v[144:147]
s_cmp_eq_u32 s39, 1
s_mov_b32 s28, 49152
s_cselect_b32 s28, 4294934528, s28
v_mfma_f32_16x16x32_bf16 v[148:151], a[168:171], a[140:143], v[148:151]
v_add_u32 v43, 49152, v43
v_add_u32 v44, 49152, v44
v_mfma_f32_16x16x32_bf16 v[184:187], a[172:175], a[140:143], v[184:187]
v_add_u32 v47, 4294934528, v47
v_add_u32 v48, 4294934528, v48
v_mfma_f32_16x16x32_bf16 v[188:191], a[192:195], a[140:143], v[188:191]
buffer_load_dwordx4 v38, s[12:15], s24 offen offset:2048 lds
v_mfma_f32_16x16x32_bf16 v[224:227], a[140:143], a[196:199], v[224:227]
v_add_u32 v45, s28, v45
v_add_u32 v46, s28, v46
v_mfma_f32_16x16x32_bf16 v[228:231], a[140:143], a[200:203], v[228:231]
v_mfma_f32_16x16x32_bf16 v[152:155], a[164:167], a[160:163], v[152:155]
v_mfma_f32_16x16x32_bf16 v[156:159], a[168:171], a[160:163], v[156:159]
v_mfma_f32_16x16x32_bf16 v[192:195], a[172:175], a[160:163], v[192:195]
buffer_load_dwordx4 v39, s[12:15], s24 offen offset:3072 lds
v_mfma_f32_16x16x32_bf16 v[196:199], a[192:195], a[160:163], v[196:199]
s_add_u32 s24, s24, 128
v_mfma_f32_16x16x32_bf16 v[232:235], a[160:163], a[196:199], v[232:235]
v_mfma_f32_16x16x32_bf16 v[236:239], a[160:163], a[200:203], v[236:239]
s_waitcnt vmcnt(16) lgkmcnt(0)
s_barrier
v_mfma_f32_16x16x32_bf16 v[160:163], a[28:31], a[0:3], v[160:163]
ds_read_b64_tr_b16 a[164:165], v47 offset:0
ds_read_b64_tr_b16 a[166:167], v47 offset:512
v_mfma_f32_16x16x32_bf16 v[164:167], a[32:35], a[0:3], v[164:167]
s_add_u32 m0, s48, 131072
ds_read_b128 a[128:131], v45
v_mfma_f32_16x16x32_bf16 v[200:203], a[0:3], a[36:39], v[200:203]
buffer_load_dwordx4 v40, s[16:19], s26 offen offset:0 lds
v_mfma_f32_16x16x32_bf16 v[204:207], a[0:3], a[40:43], v[204:207]
ds_read_b64_tr_b16 a[168:169], v48 offset:0
ds_read_b64_tr_b16 a[170:171], v48 offset:512
v_mfma_f32_16x16x32_bf16 v[128:131], a[20:23], a[4:7], v[128:131]
ds_read_b128 a[132:135], v43 offset:0
v_mfma_f32_16x16x32_bf16 v[132:135], a[24:27], a[4:7], v[132:135]
ds_read_b64_tr_b16 a[172:173], v47 offset:2048
ds_read_b64_tr_b16 a[174:175], v47 offset:2560
v_mfma_f32_16x16x32_bf16 v[168:171], a[28:31], a[4:7], v[168:171]
ds_read_b128 a[136:139], v43 offset:2048
v_mfma_f32_16x16x32_bf16 v[172:175], a[32:35], a[4:7], v[172:175]
buffer_load_dwordx4 v41, s[16:19], s26 offen offset:1024 lds
v_mfma_f32_16x16x32_bf16 v[208:211], a[4:7], a[36:39], v[208:211]
ds_read_b64_tr_b16 a[192:193], v48 offset:2048
ds_read_b64_tr_b16 a[194:195], v48 offset:2560
v_mfma_f32_16x16x32_bf16 v[212:215], a[4:7], a[40:43], v[212:215]
ds_read_b128 a[140:143], v43 offset:4096
v_mfma_f32_16x16x32_bf16 v[136:139], a[20:23], a[8:11], v[136:139]
ds_read_b64_tr_b16 a[196:197], v47 offset:4096
ds_read_b64_tr_b16 a[198:199], v47 offset:4608
v_mfma_f32_16x16x32_bf16 v[140:143], a[24:27], a[8:11], v[140:143]
ds_read_b128 a[160:163], v43 offset:6144
v_mfma_f32_16x16x32_bf16 v[176:179], a[28:31], a[8:11], v[176:179]
buffer_load_dwordx4 v40, s[16:19], s26 offen offset:2048 lds
v_mfma_f32_16x16x32_bf16 v[180:183], a[32:35], a[8:11], v[180:183]
ds_read_b64_tr_b16 a[200:201], v48 offset:4096
ds_read_b64_tr_b16 a[202:203], v48 offset:4608
v_mfma_f32_16x16x32_bf16 v[216:219], a[8:11], a[36:39], v[216:219]
v_mfma_f32_16x16x32_bf16 v[220:223], a[8:11], a[40:43], v[220:223]
v_mfma_f32_16x16x32_bf16 v[144:147], a[20:23], a[12:15], v[144:147]
v_mfma_f32_16x16x32_bf16 v[148:151], a[24:27], a[12:15], v[148:151]
buffer_load_dwordx4 v41, s[16:19], s26 offen offset:3072 lds
v_mfma_f32_16x16x32_bf16 v[184:187], a[28:31], a[12:15], v[184:187]
v_mfma_f32_16x16x32_bf16 v[188:191], a[32:35], a[12:15], v[188:191]
v_mfma_f32_16x16x32_bf16 v[224:227], a[12:15], a[36:39], v[224:227]
v_mfma_f32_16x16x32_bf16 v[228:231], a[12:15], a[40:43], v[228:231]
s_add_u32 m0, s48, 135168
v_mfma_f32_16x16x32_bf16 v[152:155], a[20:23], a[16:19], v[152:155]
buffer_load_dwordx4 v40, s[16:19], s47 offen offset:0 lds
v_mfma_f32_16x16x32_bf16 v[156:159], a[24:27], a[16:19], v[156:159]
v_mfma_f32_16x16x32_bf16 v[192:195], a[28:31], a[16:19], v[192:195]
v_mfma_f32_16x16x32_bf16 v[196:199], a[32:35], a[16:19], v[196:199]
v_mfma_f32_16x16x32_bf16 v[232:235], a[16:19], a[36:39], v[232:235]
buffer_load_dwordx4 v41, s[16:19], s47 offen offset:1024 lds
v_mfma_f32_16x16x32_bf16 v[236:239], a[16:19], a[40:43], v[236:239]
s_waitcnt lgkmcnt(0)
v_mfma_f32_16x16x32_bf16 v[160:163], a[172:175], a[128:131], v[160:163]
ds_read_b64_tr_b16 a[20:21], v47 offset:14336
ds_read_b64_tr_b16 a[22:23], v47 offset:14848
v_mfma_f32_16x16x32_bf16 v[164:167], a[192:195], a[128:131], v[164:167]
s_lshl_b32 s54, s86, 7
ds_read_b128 a[0:3], v46
v_mfma_f32_16x16x32_bf16 v[200:203], a[128:131], a[196:199], v[200:203]
ds_read_b64_tr_b16 a[24:25], v48 offset:14336
ds_read_b64_tr_b16 a[26:27], v48 offset:14848
v_mfma_f32_16x16x32_bf16 v[204:207], a[128:131], a[200:203], v[204:207]
s_add_u32 s54, s54, 0x14000
buffer_load_dwordx4 v42, s[12:15], s25 offen offset:2048 lds
v_mfma_f32_16x16x32_bf16 v[128:131], a[164:167], a[132:135], v[128:131]
s_add_u32 s26, s26, 393216
s_add_u32 s47, s47, 393216
v_and_b32 v249, 7, v1
v_mfma_f32_16x16x32_bf16 v[132:135], a[168:171], a[132:135], v[132:135]
s_add_u32 s25, s25, 128
ds_read_b128 a[4:7], v44 offset:0
v_mfma_f32_16x16x32_bf16 v[168:171], a[172:175], a[132:135], v[168:171]
ds_read_b64_tr_b16 a[28:29], v47 offset:16384
ds_read_b64_tr_b16 a[30:31], v47 offset:16896
v_mfma_f32_16x16x32_bf16 v[172:175], a[192:195], a[132:135], v[172:175]
ds_read_b128 a[8:11], v44 offset:2048
v_lshl_add_u32 v249, v249, 2, s54
v_mfma_f32_16x16x32_bf16 v[208:211], a[132:135], a[196:199], v[208:211]
s_add_u32 m0, s91, 16384
v_mfma_f32_16x16x32_bf16 v[212:215], a[132:135], a[200:203], v[212:215]
buffer_load_dwordx4 v36, s[12:15], s24 offen offset:0 lds
v_mfma_f32_16x16x32_bf16 v[136:139], a[164:167], a[136:139], v[136:139]
ds_read_b64_tr_b16 a[32:33], v48 offset:16384
ds_read_b64_tr_b16 a[34:35], v48 offset:16896
v_mfma_f32_16x16x32_bf16 v[140:143], a[168:171], a[136:139], v[140:143]
ds_read_b128 a[12:15], v44 offset:4096
v_mfma_f32_16x16x32_bf16 v[176:179], a[172:175], a[136:139], v[176:179]
ds_read_b64_tr_b16 a[36:37], v47 offset:18432
ds_read_b64_tr_b16 a[38:39], v47 offset:18944
v_mfma_f32_16x16x32_bf16 v[180:183], a[192:195], a[136:139], v[180:183]
ds_read_b128 a[16:19], v44 offset:6144
v_mfma_f32_16x16x32_bf16 v[216:219], a[136:139], a[196:199], v[216:219]
buffer_load_dwordx4 v37, s[12:15], s24 offen offset:1024 lds
v_mfma_f32_16x16x32_bf16 v[220:223], a[136:139], a[200:203], v[220:223]
ds_read_b64_tr_b16 a[40:41], v48 offset:18432
ds_read_b64_tr_b16 a[42:43], v48 offset:18944
v_mfma_f32_16x16x32_bf16 v[144:147], a[164:167], a[140:143], v[144:147]
s_cmp_eq_u32 s39, 1
s_mov_b32 s28, 16384
s_cselect_b32 s28, 4294901760, s28
v_mfma_f32_16x16x32_bf16 v[148:151], a[168:171], a[140:143], v[148:151]
v_add_u32 v43, 16384, v43
v_add_u32 v44, 16384, v44
v_mfma_f32_16x16x32_bf16 v[184:187], a[172:175], a[140:143], v[184:187]
v_add_u32 v47, 4294901760, v47
v_add_u32 v48, 4294901760, v48
v_mfma_f32_16x16x32_bf16 v[188:191], a[192:195], a[140:143], v[188:191]
.Lpad15:
s_cbranch_execz .Ltch6
buffer_load_dwordx4 v38, s[12:15], s24 offen offset:2048 lds
v_mfma_f32_16x16x32_bf16 v[224:227], a[140:143], a[196:199], v[224:227]
v_add_u32 v45, s28, v45
v_add_u32 v46, s28, v46
v_mfma_f32_16x16x32_bf16 v[228:231], a[140:143], a[200:203], v[228:231]
v_mfma_f32_16x16x32_bf16 v[152:155], a[164:167], a[160:163], v[152:155]
v_mfma_f32_16x16x32_bf16 v[156:159], a[168:171], a[160:163], v[156:159]
v_mfma_f32_16x16x32_bf16 v[192:195], a[172:175], a[160:163], v[192:195]
buffer_load_dwordx4 v39, s[12:15], s24 offen offset:3072 lds
v_mfma_f32_16x16x32_bf16 v[196:199], a[192:195], a[160:163], v[196:199]
s_add_u32 s24, s24, 128
v_mfma_f32_16x16x32_bf16 v[232:235], a[160:163], a[196:199], v[232:235]
v_mfma_f32_16x16x32_bf16 v[236:239], a[160:163], a[200:203], v[236:239]
s_waitcnt vmcnt(15) lgkmcnt(0)
s_barrier
v_mfma_f32_16x16x32_bf16 v[160:163], a[28:31], a[0:3], v[160:163]
ds_read_b64_tr_b16 a[164:165], v47 offset:0
ds_read_b64_tr_b16 a[166:167], v47 offset:512
v_mfma_f32_16x16x32_bf16 v[164:167], a[32:35], a[0:3], v[164:167]
s_add_u32 m0, s48, 98304
ds_read_b128 a[128:131], v45
v_mfma_f32_16x16x32_bf16 v[200:203], a[0:3], a[36:39], v[200:203]
buffer_load_dwordx4 v40, s[16:19], s26 offen offset:0 lds
v_mfma_f32_16x16x32_bf16 v[204:207], a[0:3], a[40:43], v[204:207]
ds_read_b64_tr_b16 a[168:169], v48 offset:0
ds_read_b64_tr_b16 a[170:171], v48 offset:512
v_mfma_f32_16x16x32_bf16 v[128:131], a[20:23], a[4:7], v[128:131]
ds_read_b128 a[132:135], v43 offset:0
v_mfma_f32_16x16x32_bf16 v[132:135], a[24:27], a[4:7], v[132:135]
ds_read_b64_tr_b16 a[172:173], v47 offset:2048
ds_read_b64_tr_b16 a[174:175], v47 offset:2560
v_mfma_f32_16x16x32_bf16 v[168:171], a[28:31], a[4:7], v[168:171]
ds_read_b128 a[136:139], v43 offset:2048
v_mfma_f32_16x16x32_bf16 v[172:175], a[32:35], a[4:7], v[172:175]
buffer_load_dwordx4 v41, s[16:19], s26 offen offset:1024 lds
v_mfma_f32_16x16x32_bf16 v[208:211], a[4:7], a[36:39], v[208:211]
ds_read_b64_tr_b16 a[192:193], v48 offset:2048
ds_read_b64_tr_b16 a[194:195], v48 offset:2560
v_mfma_f32_16x16x32_bf16 v[212:215], a[4:7], a[40:43], v[212:215]
ds_read_b128 a[140:143], v43 offset:4096
v_mfma_f32_16x16x32_bf16 v[136:139], a[20:23], a[8:11], v[136:139]
ds_read_b64_tr_b16 a[196:197], v47 offset:4096
ds_read_b64_tr_b16 a[198:199], v47 offset:4608
v_mfma_f32_16x16x32_bf16 v[140:143], a[24:27], a[8:11], v[140:143]
ds_read_b128 a[160:163], v43 offset:6144
v_mfma_f32_16x16x32_bf16 v[176:179], a[28:31], a[8:11], v[176:179]
buffer_load_dwordx4 v40, s[16:19], s26 offen offset:2048 lds
v_mfma_f32_16x16x32_bf16 v[180:183], a[32:35], a[8:11], v[180:183]
ds_read_b64_tr_b16 a[200:201], v48 offset:4096
ds_read_b64_tr_b16 a[202:203], v48 offset:4608
v_mfma_f32_16x16x32_bf16 v[216:219], a[8:11], a[36:39], v[216:219]
v_mfma_f32_16x16x32_bf16 v[220:223], a[8:11], a[40:43], v[220:223]
v_mfma_f32_16x16x32_bf16 v[144:147], a[20:23], a[12:15], v[144:147]
v_mfma_f32_16x16x32_bf16 v[148:151], a[24:27], a[12:15], v[148:151]
buffer_load_dwordx4 v41, s[16:19], s26 offen offset:3072 lds
v_mfma_f32_16x16x32_bf16 v[184:187], a[28:31], a[12:15], v[184:187]
v_mfma_f32_16x16x32_bf16 v[188:191], a[32:35], a[12:15], v[188:191]
v_mfma_f32_16x16x32_bf16 v[224:227], a[12:15], a[36:39], v[224:227]
v_mfma_f32_16x16x32_bf16 v[228:231], a[12:15], a[40:43], v[228:231]
s_add_u32 m0, s48, 102400
v_mfma_f32_16x16x32_bf16 v[152:155], a[20:23], a[16:19], v[152:155]
buffer_load_dwordx4 v40, s[16:19], s47 offen offset:0 lds
v_mfma_f32_16x16x32_bf16 v[156:159], a[24:27], a[16:19], v[156:159]
v_mfma_f32_16x16x32_bf16 v[192:195], a[28:31], a[16:19], v[192:195]
buffer_load_dword v35, v4, s[96:99], 0 offen
v_mfma_f32_16x16x32_bf16 v[196:199], a[32:35], a[16:19], v[196:199]
v_mfma_f32_16x16x32_bf16 v[232:235], a[16:19], a[36:39], v[232:235]
buffer_load_dwordx4 v41, s[16:19], s47 offen offset:1024 lds
v_mfma_f32_16x16x32_bf16 v[236:239], a[16:19], a[40:43], v[236:239]
s_waitcnt lgkmcnt(0)
v_mfma_f32_16x16x32_bf16 v[160:163], a[172:175], a[128:131], v[160:163]
ds_read_b64_tr_b16 a[20:21], v47 offset:14336
ds_read_b64_tr_b16 a[22:23], v47 offset:14848
v_mfma_f32_16x16x32_bf16 v[164:167], a[192:195], a[128:131], v[164:167]
ds_read_b128 a[0:3], v46
v_mfma_f32_16x16x32_bf16 v[200:203], a[128:131], a[196:199], v[200:203]
ds_read_b64_tr_b16 a[24:25], v48 offset:14336
ds_read_b64_tr_b16 a[26:27], v48 offset:14848
v_mfma_f32_16x16x32_bf16 v[204:207], a[128:131], a[200:203], v[204:207]
buffer_load_dwordx4 v42, s[12:15], s25 offen offset:2048 lds
v_mfma_f32_16x16x32_bf16 v[128:131], a[164:167], a[132:135], v[128:131]
s_add_u32 s26, s26, 393216
s_add_u32 s47, s47, 393216
v_mfma_f32_16x16x32_bf16 v[132:135], a[168:171], a[132:135], v[132:135]
s_add_u32 s25, s25, 128
ds_read_b128 a[4:7], v44 offset:0
v_mfma_f32_16x16x32_bf16 v[168:171], a[172:175], a[132:135], v[168:171]
buffer_load_dword v250, v249, s[60:63], 0 offen sc1
ds_read_b64_tr_b16 a[28:29], v47 offset:16384
ds_read_b64_tr_b16 a[30:31], v47 offset:16896
v_mfma_f32_16x16x32_bf16 v[172:175], a[192:195], a[132:135], v[172:175]
ds_read_b128 a[8:11], v44 offset:2048
v_mfma_f32_16x16x32_bf16 v[208:211], a[132:135], a[196:199], v[208:211]
s_add_u32 m0, s91, 65536
v_mfma_f32_16x16x32_bf16 v[212:215], a[132:135], a[200:203], v[212:215]
buffer_load_dwordx4 v36, s[12:15], s24 offen offset:0 lds
v_mfma_f32_16x16x32_bf16 v[136:139], a[164:167], a[136:139], v[136:139]
ds_read_b64_tr_b16 a[32:33], v48 offset:16384
ds_read_b64_tr_b16 a[34:35], v48 offset:16896
v_mfma_f32_16x16x32_bf16 v[140:143], a[168:171], a[136:139], v[140:143]
ds_read_b128 a[12:15], v44 offset:4096
v_mfma_f32_16x16x32_bf16 v[176:179], a[172:175], a[136:139], v[176:179]
ds_read_b64_tr_b16 a[36:37], v47 offset:18432
ds_read_b64_tr_b16 a[38:39], v47 offset:18944
v_mfma_f32_16x16x32_bf16 v[180:183], a[192:195], a[136:139], v[180:183]
ds_read_b128 a[16:19], v44 offset:6144
v_mfma_f32_16x16x32_bf16 v[216:219], a[136:139], a[196:199], v[216:219]
buffer_load_dwordx4 v37, s[12:15], s24 offen offset:1024 lds
v_mfma_f32_16x16x32_bf16 v[220:223], a[136:139], a[200:203], v[220:223]
ds_read_b64_tr_b16 a[40:41], v48 offset:18432
ds_read_b64_tr_b16 a[42:43], v48 offset:18944
v_mfma_f32_16x16x32_bf16 v[144:147], a[164:167], a[140:143], v[144:147]
s_cmp_eq_u32 s39, 1
s_mov_b32 s28, 4294885376
s_cselect_b32 s28, 98304, s28
v_mfma_f32_16x16x32_bf16 v[148:151], a[168:171], a[140:143], v[148:151]
v_add_u32 v43, 4294885376, v43
v_add_u32 v44, 4294885376, v44
v_mfma_f32_16x16x32_bf16 v[184:187], a[172:175], a[140:143], v[184:187]
v_add_u32 v47, 98304, v47
v_add_u32 v48, 98304, v48
v_mfma_f32_16x16x32_bf16 v[188:191], a[192:195], a[140:143], v[188:191]
buffer_load_dwordx4 v38, s[12:15], s24 offen offset:2048 lds
v_mfma_f32_16x16x32_bf16 v[224:227], a[140:143], a[196:199], v[224:227]
v_add_u32 v45, s28, v45
v_add_u32 v46, s28, v46
v_mfma_f32_16x16x32_bf16 v[228:231], a[140:143], a[200:203], v[228:231]
v_mfma_f32_16x16x32_bf16 v[152:155], a[164:167], a[160:163], v[152:155]
v_mfma_f32_16x16x32_bf16 v[156:159], a[168:171], a[160:163], v[156:159]
v_mfma_f32_16x16x32_bf16 v[192:195], a[172:175], a[160:163], v[192:195]
buffer_load_dwordx4 v39, s[12:15], s24 offen offset:3072 lds
v_mfma_f32_16x16x32_bf16 v[196:199], a[192:195], a[160:163], v[196:199]
s_add_u32 s24, s24, 128
v_mfma_f32_16x16x32_bf16 v[232:235], a[160:163], a[196:199], v[232:235]
v_mfma_f32_16x16x32_bf16 v[236:239], a[160:163], a[200:203], v[236:239]
s_waitcnt vmcnt(17) lgkmcnt(0)
s_barrier
v_mfma_f32_16x16x32_bf16 v[160:163], a[28:31], a[0:3], v[160:163]
ds_read_b64_tr_b16 a[164:165], v47 offset:0
ds_read_b64_tr_b16 a[166:167], v47 offset:512
v_mfma_f32_16x16x32_bf16 v[164:167], a[32:35], a[0:3], v[164:167]
s_add_u32 m0, s48, 32768
ds_read_b128 a[128:131], v45
v_mfma_f32_16x16x32_bf16 v[200:203], a[0:3], a[36:39], v[200:203]
buffer_load_dwordx4 v40, s[16:19], s26 offen offset:0 lds
v_mfma_f32_16x16x32_bf16 v[204:207], a[0:3], a[40:43], v[204:207]
ds_read_b64_tr_b16 a[168:169], v48 offset:0
ds_read_b64_tr_b16 a[170:171], v48 offset:512
v_mfma_f32_16x16x32_bf16 v[128:131], a[20:23], a[4:7], v[128:131]
ds_read_b128 a[132:135], v43 offset:0
v_mfma_f32_16x16x32_bf16 v[132:135], a[24:27], a[4:7], v[132:135]
ds_read_b64_tr_b16 a[172:173], v47 offset:2048
ds_read_b64_tr_b16 a[174:175], v47 offset:2560
v_mfma_f32_16x16x32_bf16 v[168:171], a[28:31], a[4:7], v[168:171]
ds_read_b128 a[136:139], v43 offset:2048
v_mfma_f32_16x16x32_bf16 v[172:175], a[32:35], a[4:7], v[172:175]
buffer_load_dwordx4 v41, s[16:19], s26 offen offset:1024 lds
v_mfma_f32_16x16x32_bf16 v[208:211], a[4:7], a[36:39], v[208:211]
ds_read_b64_tr_b16 a[192:193], v48 offset:2048
ds_read_b64_tr_b16 a[194:195], v48 offset:2560
v_mfma_f32_16x16x32_bf16 v[212:215], a[4:7], a[40:43], v[212:215]
ds_read_b128 a[140:143], v43 offset:4096
v_mfma_f32_16x16x32_bf16 v[136:139], a[20:23], a[8:11], v[136:139]
ds_read_b64_tr_b16 a[196:197], v47 offset:4096
ds_read_b64_tr_b16 a[198:199], v47 offset:4608
v_mfma_f32_16x16x32_bf16 v[140:143], a[24:27], a[8:11], v[140:143]
ds_read_b128 a[160:163], v43 offset:6144
v_mfma_f32_16x16x32_bf16 v[176:179], a[28:31], a[8:11], v[176:179]
buffer_load_dwordx4 v40, s[16:19], s26 offen offset:2048 lds
v_mfma_f32_16x16x32_bf16 v[180:183], a[32:35], a[8:11], v[180:183]
ds_read_b64_tr_b16 a[200:201], v48 offset:4096
ds_read_b64_tr_b16 a[202:203], v48 offset:4608
v_mfma_f32_16x16x32_bf16 v[216:219], a[8:11], a[36:39], v[216:219]
v_mfma_f32_16x16x32_bf16 v[220:223], a[8:11], a[40:43], v[220:223]
v_mfma_f32_16x16x32_bf16 v[144:147], a[20:23], a[12:15], v[144:147]
v_mfma_f32_16x16x32_bf16 v[148:151], a[24:27], a[12:15], v[148:151]
buffer_load_dwordx4 v41, s[16:19], s26 offen offset:3072 lds
v_mfma_f32_16x16x32_bf16 v[184:187], a[28:31], a[12:15], v[184:187]
v_mfma_f32_16x16x32_bf16 v[188:191], a[32:35], a[12:15], v[188:191]
v_mfma_f32_16x16x32_bf16 v[224:227], a[12:15], a[36:39], v[224:227]
v_mfma_f32_16x16x32_bf16 v[228:231], a[12:15], a[40:43], v[228:231]
s_add_u32 m0, s48, 36864
v_mfma_f32_16x16x32_bf16 v[152:155], a[20:23], a[16:19], v[152:155]
buffer_load_dwordx4 v40, s[16:19], s47 offen offset:0 lds
v_mfma_f32_16x16x32_bf16 v[156:159], a[24:27], a[16:19], v[156:159]
v_mfma_f32_16x16x32_bf16 v[192:195], a[28:31], a[16:19], v[192:195]
v_mfma_f32_16x16x32_bf16 v[196:199], a[32:35], a[16:19], v[196:199]
v_mfma_f32_16x16x32_bf16 v[232:235], a[16:19], a[36:39], v[232:235]
buffer_load_dwordx4 v41, s[16:19], s47 offen offset:1024 lds
v_mfma_f32_16x16x32_bf16 v[236:239], a[16:19], a[40:43], v[236:239]
s_waitcnt lgkmcnt(0)
v_mfma_f32_16x16x32_bf16 v[160:163], a[172:175], a[128:131], v[160:163]
ds_read_b64_tr_b16 a[20:21], v47 offset:14336
ds_read_b64_tr_b16 a[22:23], v47 offset:14848
v_mfma_f32_16x16x32_bf16 v[164:167], a[192:195], a[128:131], v[164:167]
ds_read_b128 a[0:3], v46
v_mfma_f32_16x16x32_bf16 v[200:203], a[128:131], a[196:199], v[200:203]
ds_read_b64_tr_b16 a[24:25], v48 offset:14336
ds_read_b64_tr_b16 a[26:27], v48 offset:14848
v_mfma_f32_16x16x32_bf16 v[204:207], a[128:131], a[200:203], v[204:207]
buffer_load_dwordx4 v42, s[12:15], s25 offen offset:2048 lds
v_mfma_f32_16x16x32_bf16 v[128:131], a[164:167], a[132:135], v[128:131]
s_add_u32 s26, s26, 393216
s_add_u32 s47, s47, 393216
v_mfma_f32_16x16x32_bf16 v[132:135], a[168:171], a[132:135], v[132:135]
s_add_u32 s25, s25, 128
ds_read_b128 a[4:7], v44 offset:0
v_mfma_f32_16x16x32_bf16 v[168:171], a[172:175], a[132:135], v[168:171]
ds_read_b64_tr_b16 a[28:29], v47 offset:16384
ds_read_b64_tr_b16 a[30:31], v47 offset:16896
v_mfma_f32_16x16x32_bf16 v[172:175], a[192:195], a[132:135], v[172:175]
ds_read_b128 a[8:11], v44 offset:2048
v_mfma_f32_16x16x32_bf16 v[208:211], a[132:135], a[196:199], v[208:211]
s_add_u32 m0, s91, 81920
v_mfma_f32_16x16x32_bf16 v[212:215], a[132:135], a[200:203], v[212:215]
buffer_load_dwordx4 v36, s[12:15], s24 offen offset:0 lds
v_mfma_f32_16x16x32_bf16 v[136:139], a[164:167], a[136:139], v[136:139]
ds_read_b64_tr_b16 a[32:33], v48 offset:16384
ds_read_b64_tr_b16 a[34:35], v48 offset:16896
v_mfma_f32_16x16x32_bf16 v[140:143], a[168:171], a[136:139], v[140:143]
ds_read_b128 a[12:15], v44 offset:4096
v_mfma_f32_16x16x32_bf16 v[176:179], a[172:175], a[136:139], v[176:179]
ds_read_b64_tr_b16 a[36:37], v47 offset:18432
ds_read_b64_tr_b16 a[38:39], v47 offset:18944
v_mfma_f32_16x16x32_bf16 v[180:183], a[192:195], a[136:139], v[180:183]
ds_read_b128 a[16:19], v44 offset:6144
v_mfma_f32_16x16x32_bf16 v[216:219], a[136:139], a[196:199], v[216:219]
buffer_load_dwordx4 v37, s[12:15], s24 offen offset:1024 lds
v_mfma_f32_16x16x32_bf16 v[220:223], a[136:139], a[200:203], v[220:223]
ds_read_b64_tr_b16 a[40:41], v48 offset:18432
ds_read_b64_tr_b16 a[42:43], v48 offset:18944
v_mfma_f32_16x16x32_bf16 v[144:147], a[164:167], a[140:143], v[144:147]
s_cmp_eq_u32 s39, 1
s_mov_b32 s28, 16384
s_cselect_b32 s28, 4294934528, s28
v_mfma_f32_16x16x32_bf16 v[148:151], a[168:171], a[140:143], v[148:151]
v_add_u32 v43, 16384, v43
v_add_u32 v44, 16384, v44
v_mfma_f32_16x16x32_bf16 v[184:187], a[172:175], a[140:143], v[184:187]
v_add_u32 v47, 4294934528, v47
v_add_u32 v48, 4294934528, v48
v_mfma_f32_16x16x32_bf16 v[188:191], a[192:195], a[140:143], v[188:191]
buffer_load_dwordx4 v38, s[12:15], s24 offen offset:2048 lds
v_mfma_f32_16x16x32_bf16 v[224:227], a[140:143], a[196:199], v[224:227]
v_add_u32 v45, s28, v45
v_add_u32 v46, s28, v46
v_mfma_f32_16x16x32_bf16 v[228:231], a[140:143], a[200:203], v[228:231]
v_mfma_f32_16x16x32_bf16 v[152:155], a[164:167], a[160:163], v[152:155]
v_mfma_f32_16x16x32_bf16 v[156:159], a[168:171], a[160:163], v[156:159]
v_mfma_f32_16x16x32_bf16 v[192:195], a[172:175], a[160:163], v[192:195]
buffer_load_dwordx4 v39, s[12:15], s24 offen offset:3072 lds
v_mfma_f32_16x16x32_bf16 v[196:199], a[192:195], a[160:163], v[196:199]
s_add_u32 s24, s24, 128
v_mfma_f32_16x16x32_bf16 v[232:235], a[160:163], a[196:199], v[232:235]
v_mfma_f32_16x16x32_bf16 v[236:239], a[160:163], a[200:203], v[236:239]
s_waitcnt vmcnt(16) lgkmcnt(0)
s_barrier
v_mfma_f32_16x16x32_bf16 v[160:163], a[28:31], a[0:3], v[160:163]
ds_read_b64_tr_b16 a[164:165], v47 offset:0
ds_read_b64_tr_b16 a[166:167], v47 offset:512
v_mfma_f32_16x16x32_bf16 v[164:167], a[32:35], a[0:3], v[164:167]
s_add_u32 m0, s48, 131072
ds_read_b128 a[128:131], v45
v_mfma_f32_16x16x32_bf16 v[200:203], a[0:3], a[36:39], v[200:203]
buffer_load_dwordx4 v40, s[16:19], s26 offen offset:0 lds
v_mfma_f32_16x16x32_bf16 v[204:207], a[0:3], a[40:43], v[204:207]
ds_read_b64_tr_b16 a[168:169], v48 offset:0
ds_read_b64_tr_b16 a[170:171], v48 offset:512
v_mfma_f32_16x16x32_bf16 v[128:131], a[20:23], a[4:7], v[128:131]
ds_read_b128 a[132:135], v43 offset:0
v_mfma_f32_16x16x32_bf16 v[132:135], a[24:27], a[4:7], v[132:135]
ds_read_b64_tr_b16 a[172:173], v47 offset:2048
ds_read_b64_tr_b16 a[174:175], v47 offset:2560
v_mfma_f32_16x16x32_bf16 v[168:171], a[28:31], a[4:7], v[168:171]
ds_read_b128 a[136:139], v43 offset:2048
v_mfma_f32_16x16x32_bf16 v[172:175], a[32:35], a[4:7], v[172:175]
buffer_load_dwordx4 v41, s[16:19], s26 offen offset:1024 lds
v_mfma_f32_16x16x32_bf16 v[208:211], a[4:7], a[36:39], v[208:211]
ds_read_b64_tr_b16 a[192:193], v48 offset:2048
ds_read_b64_tr_b16 a[194:195], v48 offset:2560
v_mfma_f32_16x16x32_bf16 v[212:215], a[4:7], a[40:43], v[212:215]
ds_read_b128 a[140:143], v43 offset:4096
v_mfma_f32_16x16x32_bf16 v[136:139], a[20:23], a[8:11], v[136:139]
ds_read_b64_tr_b16 a[196:197], v47 offset:4096
ds_read_b64_tr_b16 a[198:199], v47 offset:4608
v_mfma_f32_16x16x32_bf16 v[140:143], a[24:27], a[8:11], v[140:143]
ds_read_b128 a[160:163], v43 offset:6144
v_mfma_f32_16x16x32_bf16 v[176:179], a[28:31], a[8:11], v[176:179]
buffer_load_dwordx4 v40, s[16:19], s26 offen offset:2048 lds
v_mfma_f32_16x16x32_bf16 v[180:183], a[32:35], a[8:11], v[180:183]
ds_read_b64_tr_b16 a[200:201], v48 offset:4096
ds_read_b64_tr_b16 a[202:203], v48 offset:4608
v_mfma_f32_16x16x32_bf16 v[216:219], a[8:11], a[36:39], v[216:219]
v_mfma_f32_16x16x32_bf16 v[220:223], a[8:11], a[40:43], v[220:223]
v_mfma_f32_16x16x32_bf16 v[144:147], a[20:23], a[12:15], v[144:147]
v_mfma_f32_16x16x32_bf16 v[148:151], a[24:27], a[12:15], v[148:151]
buffer_load_dwordx4 v41, s[16:19], s26 offen offset:3072 lds
v_mfma_f32_16x16x32_bf16 v[184:187], a[28:31], a[12:15], v[184:187]
v_mfma_f32_16x16x32_bf16 v[188:191], a[32:35], a[12:15], v[188:191]
v_mfma_f32_16x16x32_bf16 v[224:227], a[12:15], a[36:39], v[224:227]
v_mfma_f32_16x16x32_bf16 v[228:231], a[12:15], a[40:43], v[228:231]
s_add_u32 m0, s48, 135168
v_mfma_f32_16x16x32_bf16 v[152:155], a[20:23], a[16:19], v[152:155]
buffer_load_dwordx4 v40, s[16:19], s47 offen offset:0 lds
v_mfma_f32_16x16x32_bf16 v[156:159], a[24:27], a[16:19], v[156:159]
v_mfma_f32_16x16x32_bf16 v[192:195], a[28:31], a[16:19], v[192:195]
v_mfma_f32_16x16x32_bf16 v[196:199], a[32:35], a[16:19], v[196:199]
v_mfma_f32_16x16x32_bf16 v[232:235], a[16:19], a[36:39], v[232:235]
buffer_load_dwordx4 v41, s[16:19], s47 offen offset:1024 lds
v_mfma_f32_16x16x32_bf16 v[236:239], a[16:19], a[40:43], v[236:239]
s_waitcnt lgkmcnt(0)
v_mfma_f32_16x16x32_bf16 v[160:163], a[172:175], a[128:131], v[160:163]
ds_read_b64_tr_b16 a[20:21], v47 offset:14336
ds_read_b64_tr_b16 a[22:23], v47 offset:14848
v_mfma_f32_16x16x32_bf16 v[164:167], a[192:195], a[128:131], v[164:167]
s_waitcnt vmcnt(21)
ds_read_b128 a[0:3], v46
v_cmp_eq_u32 vcc, 0, v250
v_mfma_f32_16x16x32_bf16 v[200:203], a[128:131], a[196:199], v[200:203]
ds_read_b64_tr_b16 a[24:25], v48 offset:14336
ds_read_b64_tr_b16 a[26:27], v48 offset:14848
v_mfma_f32_16x16x32_bf16 v[204:207], a[128:131], a[200:203], v[204:207]
buffer_load_dwordx4 v42, s[12:15], s25 offen offset:2048 lds
v_mfma_f32_16x16x32_bf16 v[128:131], a[164:167], a[132:135], v[128:131]
s_add_u32 s26, s26, 393216
s_add_u32 s47, s47, 393216
v_mfma_f32_16x16x32_bf16 v[132:135], a[168:171], a[132:135], v[132:135]
s_add_u32 s25, s25, 128
s_cmp_lg_u64 vcc, 0
s_cbranch_scc1 .Lspin6
.Lspin6_ret:
ds_read_b128 a[4:7], v44 offset:0
v_mfma_f32_16x16x32_bf16 v[168:171], a[172:175], a[132:135], v[168:171]
ds_read_b64_tr_b16 a[28:29], v47 offset:16384
ds_read_b64_tr_b16 a[30:31], v47 offset:16896
v_mfma_f32_16x16x32_bf16 v[172:175], a[192:195], a[132:135], v[172:175]
ds_read_b128 a[8:11], v44 offset:2048
v_mov_b32 v251, 0
v_mfma_f32_16x16x32_bf16 v[208:211], a[132:135], a[196:199], v[208:211]
s_add_u32 m0, s91, 0
v_readlane_b32 s54, v250, 0
v_nop
v_mfma_f32_16x16x32_bf16 v[212:215], a[132:135], a[200:203], v[212:215]
buffer_load_dwordx4 v36, s[12:15], s24 offen offset:0 lds
v_nop
v_mfma_f32_16x16x32_bf16 v[136:139], a[164:167], a[136:139], v[136:139]
ds_read_b64_tr_b16 a[32:33], v48 offset:16384
ds_read_b64_tr_b16 a[34:35], v48 offset:16896
v_mfma_f32_16x16x32_bf16 v[140:143], a[168:171], a[136:139], v[140:143]
ds_read_b128 a[12:15], v44 offset:4096
v_subrev_u32 v252, s54, v250
v_mfma_f32_16x16x32_bf16 v[176:179], a[172:175], a[136:139], v[176:179]
ds_read_b64_tr_b16 a[36:37], v47 offset:18432
ds_read_b64_tr_b16 a[38:39], v47 offset:18944
v_mfma_f32_16x16x32_bf16 v[180:183], a[192:195], a[136:139], v[180:183]
ds_read_b128 a[16:19], v44 offset:6144
v_cmp_lt_i32 vcc, 0, v252
v_mfma_f32_16x16x32_bf16 v[216:219], a[136:139], a[196:199], v[216:219]
buffer_load_dwordx4 v37, s[12:15], s24 offen offset:1024 lds
v_nop
v_mfma_f32_16x16x32_bf16 v[220:223], a[136:139], a[200:203], v[220:223]
ds_read_b64_tr_b16 a[40:41], v48 offset:18432
ds_read_b64_tr_b16 a[42:43], v48 offset:18944
v_mfma_f32_16x16x32_bf16 v[144:147], a[164:167], a[140:143], v[144:147]
s_cmp_eq_u32 s39, 1
s_mov_b32 s28, 49152
s_cselect_b32 s28, 4294901760, s28
v_mfma_f32_16x16x32_bf16 v[148:151], a[168:171], a[140:143], v[148:151]
v_add_u32 v43, 49152, v43
v_add_u32 v44, 49152, v44
v_mfma_f32_16x16x32_bf16 v[184:187], a[172:175], a[140:143], v[184:187]
v_add_u32 v47, 4294901760, v47
v_add_u32 v48, 4294901760, v48
v_mfma_f32_16x16x32_bf16 v[188:191], a[192:195], a[140:143], v[188:191]
buffer_load_dwordx4 v38, s[12:15], s24 offen offset:2048 lds
v_nop
v_mfma_f32_16x16x32_bf16 v[224:227], a[140:143], a[196:199], v[224:227]
v_add_u32 v45, s28, v45
v_add_u32 v46, s28, v46
v_mfma_f32_16x16x32_bf16 v[228:231], a[140:143], a[200:203], v[228:231]
v_addc_co_u32 v251, vcc, 0, v251, vcc
v_readlane_b32 s55, v250, 1
v_mfma_f32_16x16x32_bf16 v[152:155], a[164:167], a[160:163], v[152:155]
v_nop
v_nop
v_mfma_f32_16x16x32_bf16 v[156:159], a[168:171], a[160:163], v[156:159]
v_subrev_u32 v252, s55, v250
v_cmp_lt_i32 vcc, 0, v252
v_mfma_f32_16x16x32_bf16 v[192:195], a[172:175], a[160:163], v[192:195]
buffer_load_dwordx4 v39, s[12:15], s24 offen offset:3072 lds
v_nop
v_mfma_f32_16x16x32_bf16 v[196:199], a[192:195], a[160:163], v[196:199]
s_add_u32 s24, s24, 128
v_nop
v_addc_co_u32 v251, vcc, 0, v251, vcc
v_mfma_f32_16x16x32_bf16 v[232:235], a[160:163], a[196:199], v[232:235]
v_readlane_b32 s54, v250, 2
v_nop
v_mfma_f32_16x16x32_bf16 v[236:239], a[160:163], a[200:203], v[236:239]
s_waitcnt vmcnt(15) lgkmcnt(0)
s_barrier
v_nop
v_mfma_f32_16x16x32_bf16 v[160:163], a[28:31], a[0:3], v[160:163]
ds_read_b64_tr_b16 a[164:165], v47 offset:0
ds_read_b64_tr_b16 a[166:167], v47 offset:512
v_mfma_f32_16x16x32_bf16 v[164:167], a[32:35], a[0:3], v[164:167]
s_add_u32 m0, s48, 98304
ds_read_b128 a[128:131], v45
v_mfma_f32_16x16x32_bf16 v[200:203], a[0:3], a[36:39], v[200:203]
buffer_load_dwordx4 v40, s[16:19], s26 offen offset:0 lds
v_subrev_u32 v252, s54, v250
v_mfma_f32_16x16x32_bf16 v[204:207], a[0:3], a[40:43], v[204:207]
ds_read_b64_tr_b16 a[168:169], v48 offset:0
ds_read_b64_tr_b16 a[170:171], v48 offset:512
v_mfma_f32_16x16x32_bf16 v[128:131], a[20:23], a[4:7], v[128:131]
ds_read_b128 a[132:135], v43 offset:0
v_cmp_lt_i32 vcc, 0, v252
v_mfma_f32_16x16x32_bf16 v[132:135], a[24:27], a[4:7], v[132:135]
ds_read_b64_tr_b16 a[172:173], v47 offset:2048
ds_read_b64_tr_b16 a[174:175], v47 offset:2560
v_mfma_f32_16x16x32_bf16 v[168:171], a[28:31], a[4:7], v[168:171]
ds_read_b128 a[136:139], v43 offset:2048
v_nop
v_mfma_f32_16x16x32_bf16 v[172:175], a[32:35], a[4:7], v[172:175]
buffer_load_dwordx4 v41, s[16:19], s26 offen offset:1024 lds
v_nop
v_mfma_f32_16x16x32_bf16 v[208:211], a[4:7], a[36:39], v[208:211]
ds_read_b64_tr_b16 a[192:193], v48 offset:2048
ds_read_b64_tr_b16 a[194:195], v48 offset:2560
v_mfma_f32_16x16x32_bf16 v[212:215], a[4:7], a[40:43], v[212:215]
ds_read_b128 a[140:143], v43 offset:4096
v_addc_co_u32 v251, vcc, 0, v251, vcc
v_mfma_f32_16x16x32_bf16 v[136:139], a[20:23], a[8:11], v[136:139]
ds_read_b64_tr_b16 a[196:197], v47 offset:4096
ds_read_b64_tr_b16 a[198:199], v47 offset:4608
v_mfma_f32_16x16x32_bf16 v[140:143], a[24:27], a[8:11], v[140:143]
ds_read_b128 a[160:163], v43 offset:6144
v_readlane_b32 s55, v250, 3
v_mfma_f32_16x16x32_bf16 v[176:179], a[28:31], a[8:11], v[176:179]
buffer_load_dwordx4 v40, s[16:19], s26 offen offset:2048 lds
v_nop
v_mfma_f32_16x16x32_bf16 v[180:183], a[32:35], a[8:11], v[180:183]
ds_read_b64_tr_b16 a[200:201], v48 offset:4096
ds_read_b64_tr_b16 a[202:203], v48 offset:4608
v_mfma_f32_16x16x32_bf16 v[216:219], a[8:11], a[36:39], v[216:219]
v_nop
v_subrev_u32 v252, s55, v250
v_mfma_f32_16x16x32_bf16 v[220:223], a[8:11], a[40:43], v[220:223]
v_cmp_lt_i32 vcc, 0, v252
v_nop
v_mfma_f32_16x16x32_bf16 v[144:147], a[20:23], a[12:15], v[144:147]
v_nop
v_addc_co_u32 v251, vcc, 0, v251, vcc
v_mfma_f32_16x16x32_bf16 v[148:151], a[24:27], a[12:15], v[148:151]
buffer_load_dwordx4 v41, s[16:19], s26 offen offset:3072 lds
v_readlane_b32 s54, v250, 4
v_mfma_f32_16x16x32_bf16 v[184:187], a[28:31], a[12:15], v[184:187]
v_nop
v_nop
v_mfma_f32_16x16x32_bf16 v[188:191], a[32:35], a[12:15], v[188:191]
v_subrev_u32 v252, s54, v250
v_cmp_lt_i32 vcc, 0, v252
v_mfma_f32_16x16x32_bf16 v[224:227], a[12:15], a[36:39], v[224:227]
v_nop
v_nop
v_mfma_f32_16x16x32_bf16 v[228:231], a[12:15], a[40:43], v[228:231]
s_add_u32 m0, s48, 102400
v_addc_co_u32 v251, vcc, 0, v251, vcc
v_readlane_b32 s55, v250, 5
v_mfma_f32_16x16x32_bf16 v[152:155], a[20:23], a[16:19], v[152:155]
buffer_load_dwordx4 v40, s[16:19], s47 offen offset:0 lds
v_nop
v_mfma_f32_16x16x32_bf16 v[156:159], a[24:27], a[16:19], v[156:159]
v_nop
v_subrev_u32 v252, s55, v250
v_mfma_f32_16x16x32_bf16 v[192:195], a[28:31], a[16:19], v[192:195]
v_cmp_lt_i32 vcc, 0, v252
v_nop
v_mfma_f32_16x16x32_bf16 v[196:199], a[32:35], a[16:19], v[196:199]
v_nop
v_addc_co_u32 v251, vcc, 0, v251, vcc
v_mfma_f32_16x16x32_bf16 v[232:235], a[16:19], a[36:39], v[232:235]
buffer_load_dwordx4 v41, s[16:19], s47 offen offset:1024 lds
v_readlane_b32 s54, v250, 6
v_mfma_f32_16x16x32_bf16 v[236:239], a[16:19], a[40:43], v[236:239]
s_waitcnt lgkmcnt(0)
v_nop
v_nop
v_mfma_f32_16x16x32_bf16 v[160:163], a[172:175], a[128:131], v[160:163]
ds_read_b64_tr_b16 a[20:21], v47 offset:14336
ds_read_b64_tr_b16 a[22:23], v47 offset:14848
v_mfma_f32_16x16x32_bf16 v[164:167], a[192:195], a[128:131], v[164:167]
ds_read_b128 a[0:3], v46
v_subrev_u32 v252, s54, v250
v_mfma_f32_16x16x32_bf16 v[200:203], a[128:131], a[196:199], v[200:203]
ds_read_b64_tr_b16 a[24:25], v48 offset:14336
ds_read_b64_tr_b16 a[26:27], v48 offset:14848
v_mfma_f32_16x16x32_bf16 v[204:207], a[128:131], a[200:203], v[204:207]
buffer_load_dwordx4 v42, s[12:15], s25 offen offset:2048 lds
v_cmp_lt_i32 vcc, 0, v252
v_mfma_f32_16x16x32_bf16 v[128:131], a[164:167], a[132:135], v[128:131]
s_add_u32 s26, s26, 393216
s_add_u32 s47, s47, 393216
v_nop
v_mfma_f32_16x16x32_bf16 v[132:135], a[168:171], a[132:135], v[132:135]
s_add_u32 s25, s25, 128
ds_read_b128 a[4:7], v44 offset:0
v_mfma_f32_16x16x32_bf16 v[168:171], a[172:175], a[132:135], v[168:171]
ds_read_b64_tr_b16 a[28:29], v47 offset:16384
ds_read_b64_tr_b16 a[30:31], v47 offset:16896
v_mfma_f32_16x16x32_bf16 v[172:175], a[192:195], a[132:135], v[172:175]
ds_read_b128 a[8:11], v44 offset:2048
v_nop
v_mfma_f32_16x16x32_bf16 v[208:211], a[132:135], a[196:199], v[208:211]
s_add_u32 m0, s91, 16384
v_addc_co_u32 v251, vcc, 0, v251, vcc
v_readlane_b32 s55, v250, 7
v_mfma_f32_16x16x32_bf16 v[212:215], a[132:135], a[200:203], v[212:215]
buffer_load_dwordx4 v36, s[12:15], s24 offen offset:0 lds
v_nop
v_mfma_f32_16x16x32_bf16 v[136:139], a[164:167], a[136:139], v[136:139]
ds_read_b64_tr_b16 a[32:33], v48 offset:16384
ds_read_b64_tr_b16 a[34:35], v48 offset:16896
v_mfma_f32_16x16x32_bf16 v[140:143], a[168:171], a[136:139], v[140:143]
ds_read_b128 a[12:15], v44 offset:4096
v_nop
v_mfma_f32_16x16x32_bf16 v[176:179], a[172:175], a[136:139], v[176:179]
ds_read_b64_tr_b16 a[36:37], v47 offset:18432
ds_read_b64_tr_b16 a[38:39], v47 offset:18944
v_mfma_f32_16x16x32_bf16 v[180:183], a[192:195], a[136:139], v[180:183]
ds_read_b128 a[16:19], v44 offset:6144
v_subrev_u32 v252, s55, v250
v_mfma_f32_16x16x32_bf16 v[216:219], a[136:139], a[196:199], v[216:219]
buffer_load_dwordx4 v37, s[12:15], s24 offen offset:1024 lds
v_cmp_lt_i32 vcc, 0, v252
v_mfma_f32_16x16x32_bf16 v[220:223], a[136:139], a[200:203], v[220:223]
ds_read_b64_tr_b16 a[40:41], v48 offset:18432
ds_read_b64_tr_b16 a[42:43], v48 offset:18944
v_mfma_f32_16x16x32_bf16 v[144:147], a[164:167], a[140:143], v[144:147]
s_cmp_eq_u32 s39, 1
s_mov_b32 s28, 16384
s_cselect_b32 s28, 98304, s28
v_mfma_f32_16x16x32_bf16 v[148:151], a[168:171], a[140:143], v[148:151]
v_add_u32 v43, 16384, v43
v_add_u32 v44, 16384, v44
v_mfma_f32_16x16x32_bf16 v[184:187], a[172:175], a[140:143], v[184:187]
v_add_u32 v47, 98304, v47
v_add_u32 v48, 98304, v48
v_mfma_f32_16x16x32_bf16 v[188:191], a[192:195], a[140:143], v[188:191]
buffer_load_dwordx4 v38, s[12:15], s24 offen offset:2048 lds
v_nop
v_mfma_f32_16x16x32_bf16 v[224:227], a[140:143], a[196:199], v[224:227]
v_add_u32 v45, s28, v45
v_add_u32 v46, s28, v46
v_mfma_f32_16x16x32_bf16 v[228:231], a[140:143], a[200:203], v[228:231]
v_nop
.Lpad16:
s_cbranch_execz .Ltch7
v_addc_co_u32 v251, vcc, 0, v251, vcc
v_mfma_f32_16x16x32_bf16 v[152:155], a[164:167], a[160:163], v[152:155]
v_nop
v_readlane_b32 s54, v251, s86
v_mfma_f32_16x16x32_bf16 v[156:159], a[168:171], a[160:163], v[156:159]
v_mfma_f32_16x16x32_bf16 v[192:195], a[172:175], a[160:163], v[192:195]
s_sub_u32 s55, 7, s54
buffer_load_dwordx4 v39, s[12:15], s24 offen offset:3072 lds
v_mfma_f32_16x16x32_bf16 v[196:199], a[192:195], a[160:163], v[196:199]
s_add_u32 s24, s24, 128
v_cmp_eq_u32 vcc, s55, v251
v_mfma_f32_16x16x32_bf16 v[232:235], a[160:163], a[196:199], v[232:235]
v_mfma_f32_16x16x32_bf16 v[236:239], a[160:163], a[200:203], v[236:239]
s_waitcnt vmcnt(15) lgkmcnt(0)
s_barrier
s_ff1_i32_b64 s38, vcc
v_mfma_f32_16x16x32_bf16 v[160:163], a[28:31], a[0:3], v[160:163]
ds_read_b64_tr_b16 a[164:165], v47 offset:0
ds_read_b64_tr_b16 a[166:167], v47 offset:512
v_mfma_f32_16x16x32_bf16 v[164:167], a[32:35], a[0:3], v[164:167]
s_add_u32 m0, s48, 32768
ds_read_b128 a[128:131], v45
v_mfma_f32_16x16x32_bf16 v[200:203], a[0:3], a[36:39], v[200:203]
s_and_b32 s56, s2, -8
buffer_load_dwordx4 v40, s[16:19], s26 offen offset:0 lds
v_mfma_f32_16x16x32_bf16 v[204:207], a[0:3], a[40:43], v[204:207]
ds_read_b64_tr_b16 a[168:169], v48 offset:0
ds_read_b64_tr_b16 a[170:171], v48 offset:512
v_mfma_f32_16x16x32_bf16 v[128:131], a[20:23], a[4:7], v[128:131]
s_or_b32 s38, s38, s56
ds_read_b128 a[132:135], v43 offset:0
v_mfma_f32_16x16x32_bf16 v[132:135], a[24:27], a[4:7], v[132:135]
ds_read_b64_tr_b16 a[172:173], v47 offset:2048
ds_read_b64_tr_b16 a[174:175], v47 offset:2560
v_mfma_f32_16x16x32_bf16 v[168:171], a[28:31], a[4:7], v[168:171]
s_min_u32 s55, s55, s54
ds_read_b128 a[136:139], v43 offset:2048
v_mfma_f32_16x16x32_bf16 v[172:175], a[32:35], a[4:7], v[172:175]
s_lshr_b32 s54, s54, 2
buffer_load_dwordx4 v41, s[16:19], s26 offen offset:1024 lds
v_mfma_f32_16x16x32_bf16 v[208:211], a[4:7], a[36:39], v[208:211]
ds_read_b64_tr_b16 a[192:193], v48 offset:2048
ds_read_b64_tr_b16 a[194:195], v48 offset:2560
v_mfma_f32_16x16x32_bf16 v[212:215], a[4:7], a[40:43], v[212:215]
s_lshl_b32 s42, s55, 1
ds_read_b128 a[140:143], v43 offset:4096
v_mfma_f32_16x16x32_bf16 v[136:139], a[20:23], a[8:11], v[136:139]
ds_read_b64_tr_b16 a[196:197], v47 offset:4096
ds_read_b64_tr_b16 a[198:199], v47 offset:4608
v_mfma_f32_16x16x32_bf16 v[140:143], a[24:27], a[8:11], v[140:143]
s_or_b32 s42, s42, s54
ds_read_b128 a[160:163], v43 offset:6144
v_mfma_f32_16x16x32_bf16 v[176:179], a[28:31], a[8:11], v[176:179]
s_lshl_b32 s55, s55, 3
buffer_load_dwordx4 v40, s[16:19], s26 offen offset:2048 lds
v_mfma_f32_16x16x32_bf16 v[180:183], a[32:35], a[8:11], v[180:183]
ds_read_b64_tr_b16 a[200:201], v48 offset:4096
ds_read_b64_tr_b16 a[202:203], v48 offset:4608
v_mfma_f32_16x16x32_bf16 v[216:219], a[8:11], a[36:39], v[216:219]
s_lshr_b32 s43, 707472430, s55
s_and_b32 s43, s43, 0xff
v_mfma_f32_16x16x32_bf16 v[220:223], a[8:11], a[40:43], v[220:223]
v_mfma_f32_16x16x32_bf16 v[144:147], a[20:23], a[12:15], v[144:147]
v_mfma_f32_16x16x32_bf16 v[148:151], a[24:27], a[12:15], v[148:151]
buffer_load_dwordx4 v41, s[16:19], s26 offen offset:3072 lds
v_mfma_f32_16x16x32_bf16 v[184:187], a[28:31], a[12:15], v[184:187]
v_mfma_f32_16x16x32_bf16 v[188:191], a[32:35], a[12:15], v[188:191]
v_mfma_f32_16x16x32_bf16 v[224:227], a[12:15], a[36:39], v[224:227]
v_mfma_f32_16x16x32_bf16 v[228:231], a[12:15], a[40:43], v[228:231]
s_add_u32 m0, s48, 36864
v_mfma_f32_16x16x32_bf16 v[152:155], a[20:23], a[16:19], v[152:155]
buffer_load_dwordx4 v40, s[16:19], s47 offen offset:0 lds
v_mfma_f32_16x16x32_bf16 v[156:159], a[24:27], a[16:19], v[156:159]
v_mfma_f32_16x16x32_bf16 v[192:195], a[28:31], a[16:19], v[192:195]
v_mfma_f32_16x16x32_bf16 v[196:199], a[32:35], a[16:19], v[196:199]
v_mfma_f32_16x16x32_bf16 v[232:235], a[16:19], a[36:39], v[232:235]
buffer_load_dwordx4 v41, s[16:19], s47 offen offset:1024 lds
v_mfma_f32_16x16x32_bf16 v[236:239], a[16:19], a[40:43], v[236:239]
s_waitcnt lgkmcnt(0)
v_mfma_f32_16x16x32_bf16 v[160:163], a[172:175], a[128:131], v[160:163]
ds_read_b64_tr_b16 a[20:21], v47 offset:14336
ds_read_b64_tr_b16 a[22:23], v47 offset:14848
v_mfma_f32_16x16x32_bf16 v[164:167], a[192:195], a[128:131], v[164:167]
ds_read_b128 a[0:3], v46
v_mfma_f32_16x16x32_bf16 v[200:203], a[128:131], a[196:199], v[200:203]
ds_read_b64_tr_b16 a[24:25], v48 offset:14336
ds_read_b64_tr_b16 a[26:27], v48 offset:14848
v_mfma_f32_16x16x32_bf16 v[204:207], a[128:131], a[200:203], v[204:207]
buffer_load_dwordx4 v42, s[12:15], s25 offen offset:2048 lds
v_mfma_f32_16x16x32_bf16 v[128:131], a[164:167], a[132:135], v[128:131]
s_add_u32 s26, s26, 393216
s_add_u32 s47, s47, 393216
v_mfma_f32_16x16x32_bf16 v[132:135], a[168:171], a[132:135], v[132:135]
s_add_u32 s25, s25, 128
ds_read_b128 a[4:7], v44 offset:0
v_mfma_f32_16x16x32_bf16 v[168:171], a[172:175], a[132:135], v[168:171]
ds_read_b64_tr_b16 a[28:29], v47 offset:16384
ds_read_b64_tr_b16 a[30:31], v47 offset:16896
v_mfma_f32_16x16x32_bf16 v[172:175], a[192:195], a[132:135], v[172:175]
ds_read_b128 a[8:11], v44 offset:2048
v_mfma_f32_16x16x32_bf16 v[208:211], a[132:135], a[196:199], v[208:211]
s_add_u32 m0, s91, 65536
v_mfma_f32_16x16x32_bf16 v[212:215], a[132:135], a[200:203], v[212:215]
buffer_load_dwordx4 v36, s[12:15], s24 offen offset:0 lds
v_mfma_f32_16x16x32_bf16 v[136:139], a[164:167], a[136:139], v[136:139]
ds_read_b64_tr_b16 a[32:33], v48 offset:16384
ds_read_b64_tr_b16 a[34:35], v48 offset:16896
v_mfma_f32_16x16x32_bf16 v[140:143], a[168:171], a[136:139], v[140:143]
ds_read_b128 a[12:15], v44 offset:4096
v_mfma_f32_16x16x32_bf16 v[176:179], a[172:175], a[136:139], v[176:179]
ds_read_b64_tr_b16 a[36:37], v47 offset:18432
ds_read_b64_tr_b16 a[38:39], v47 offset:18944
v_mfma_f32_16x16x32_bf16 v[180:183], a[192:195], a[136:139], v[180:183]
ds_read_b128 a[16:19], v44 offset:6144
v_mfma_f32_16x16x32_bf16 v[216:219], a[136:139], a[196:199], v[216:219]
buffer_load_dwordx4 v37, s[12:15], s24 offen offset:1024 lds
v_mfma_f32_16x16x32_bf16 v[220:223], a[136:139], a[200:203], v[220:223]
ds_read_b64_tr_b16 a[40:41], v48 offset:18432
ds_read_b64_tr_b16 a[42:43], v48 offset:18944
v_mfma_f32_16x16x32_bf16 v[144:147], a[164:167], a[140:143], v[144:147]
s_cmp_eq_u32 s39, 1
s_mov_b32 s28, 4294885376
s_cselect_b32 s28, 4294934528, s28
v_mfma_f32_16x16x32_bf16 v[148:151], a[168:171], a[140:143], v[148:151]
v_add_u32 v43, 4294885376, v43
v_add_u32 v44, 4294885376, v44
v_mfma_f32_16x16x32_bf16 v[184:187], a[172:175], a[140:143], v[184:187]
v_add_u32 v47, 4294934528, v47
v_add_u32 v48, 4294934528, v48
v_mfma_f32_16x16x32_bf16 v[188:191], a[192:195], a[140:143], v[188:191]
buffer_load_dwordx4 v38, s[12:15], s24 offen offset:2048 lds
v_mfma_f32_16x16x32_bf16 v[224:227], a[140:143], a[196:199], v[224:227]
v_add_u32 v45, s28, v45
v_add_u32 v46, s28, v46
v_mfma_f32_16x16x32_bf16 v[228:231], a[140:143], a[200:203], v[228:231]
v_mfma_f32_16x16x32_bf16 v[152:155], a[164:167], a[160:163], v[152:155]
s_bfe_u32 s51, s42, 0x10001
s_lshl_b32 s51, s51, 3
s_and_b32 s54, s87, 7
v_mfma_f32_16x16x32_bf16 v[156:159], a[168:171], a[160:163], v[156:159]
s_or_b32 s51, s51, s54
s_lshr_b32 s52, s42, 2
s_lshl_b32 s52, s52, 2
v_mfma_f32_16x16x32_bf16 v[192:195], a[172:175], a[160:163], v[192:195]
s_lshr_b32 s54, s87, 3
buffer_load_dwordx4 v39, s[12:15], s24 offen offset:3072 lds
v_mfma_f32_16x16x32_bf16 v[196:199], a[192:195], a[160:163], v[196:199]
s_add_u32 s24, s24, 128
s_or_b32 s52, s52, s54
s_lshl_b32 s52, s52, 8
v_mfma_f32_16x16x32_bf16 v[232:235], a[160:163], a[196:199], v[232:235]
s_and_b32 s55, s42, 1
s_lshl_b32 s54, s51, 2
s_add_u32 s54, s54, s3
v_mfma_f32_16x16x32_bf16 v[236:239], a[160:163], a[200:203], v[236:239]
s_waitcnt vmcnt(15) lgkmcnt(0)
s_barrier
s_lshl_b32 s54, s54, 18
v_mfma_f32_16x16x32_bf16 v[160:163], a[28:31], a[0:3], v[160:163]
ds_read_b64_tr_b16 a[164:165], v47 offset:0
ds_read_b64_tr_b16 a[166:167], v47 offset:512
v_mfma_f32_16x16x32_bf16 v[164:167], a[32:35], a[0:3], v[164:167]
s_add_u32 m0, s48, 131072
ds_read_b128 a[128:131], v45
v_mfma_f32_16x16x32_bf16 v[200:203], a[0:3], a[36:39], v[200:203]
s_add_u32 s54, s54, 33554432
buffer_load_dwordx4 v40, s[16:19], s26 offen offset:0 lds
v_mfma_f32_16x16x32_bf16 v[204:207], a[0:3], a[40:43], v[204:207]
ds_read_b64_tr_b16 a[168:169], v48 offset:0
ds_read_b64_tr_b16 a[170:171], v48 offset:512
v_mfma_f32_16x16x32_bf16 v[128:131], a[20:23], a[4:7], v[128:131]
ds_read_b128 a[132:135], v43 offset:0
v_mfma_f32_16x16x32_bf16 v[132:135], a[24:27], a[4:7], v[132:135]
ds_read_b64_tr_b16 a[172:173], v47 offset:2048
ds_read_b64_tr_b16 a[174:175], v47 offset:2560
v_mfma_f32_16x16x32_bf16 v[168:171], a[28:31], a[4:7], v[168:171]
ds_read_b128 a[136:139], v43 offset:2048
v_mfma_f32_16x16x32_bf16 v[172:175], a[32:35], a[4:7], v[172:175]
buffer_load_dwordx4 v41, s[16:19], s26 offen offset:1024 lds
v_mfma_f32_16x16x32_bf16 v[208:211], a[4:7], a[36:39], v[208:211]
ds_read_b64_tr_b16 a[192:193], v48 offset:2048
ds_read_b64_tr_b16 a[194:195], v48 offset:2560
v_mfma_f32_16x16x32_bf16 v[212:215], a[4:7], a[40:43], v[212:215]
ds_read_b128 a[140:143], v43 offset:4096
v_mfma_f32_16x16x32_bf16 v[136:139], a[20:23], a[8:11], v[136:139]
ds_read_b64_tr_b16 a[196:197], v47 offset:4096
ds_read_b64_tr_b16 a[198:199], v47 offset:4608
v_mfma_f32_16x16x32_bf16 v[140:143], a[24:27], a[8:11], v[140:143]
ds_read_b128 a[160:163], v43 offset:6144
v_mfma_f32_16x16x32_bf16 v[176:179], a[28:31], a[8:11], v[176:179]
buffer_load_dwordx4 v40, s[16:19], s26 offen offset:2048 lds
v_mfma_f32_16x16x32_bf16 v[180:183], a[32:35], a[8:11], v[180:183]
ds_read_b64_tr_b16 a[200:201], v48 offset:4096
ds_read_b64_tr_b16 a[202:203], v48 offset:4608
v_mfma_f32_16x16x32_bf16 v[216:219], a[8:11], a[36:39], v[216:219]
s_add_u32 s4, s84, s54
s_addc_u32 s5, s85, 0
s_and_b32 s5, s5, 0xffff
v_mfma_f32_16x16x32_bf16 v[220:223], a[8:11], a[40:43], v[220:223]
s_lshl_b32 s54, s3, 15
s_add_u32 s54, s54, s52
v_mfma_f32_16x16x32_bf16 v[144:147], a[20:23], a[12:15], v[144:147]
s_add_u32 s8, s80, s54
s_addc_u32 s9, s81, 0
s_and_b32 s9, s9, 0xffff
v_mfma_f32_16x16x32_bf16 v[148:151], a[24:27], a[12:15], v[148:151]
s_lshl_b32 s56, s43, 12
buffer_load_dwordx4 v41, s[16:19], s26 offen offset:3072 lds
v_mfma_f32_16x16x32_bf16 v[184:187], a[28:31], a[12:15], v[184:187]
s_lshl_b32 s57, s43, 17
s_mul_i32 s44, s55, s56
s_mul_i32 s45, s55, s57
v_mfma_f32_16x16x32_bf16 v[188:191], a[32:35], a[12:15], v[188:191]
s_cmp_eq_u32 s55, 0
s_cselect_b32 s6, s56, 0x40000
s_cselect_b32 s57, s57, 0x800000
v_mfma_f32_16x16x32_bf16 v[224:227], a[12:15], a[36:39], v[224:227]
s_sub_u32 s10, s57, s54
s_and_b32 s54, s3, 1
s_lshl_b32 s54, s54, 13
v_mfma_f32_16x16x32_bf16 v[228:231], a[12:15], a[40:43], v[228:231]
s_add_u32 m0, s48, 135168
s_add_u32 s54, s54, 98304
s_lshr_b32 s56, s3, 1
v_mfma_f32_16x16x32_bf16 v[152:155], a[20:23], a[16:19], v[152:155]
s_lshl_b32 s56, s56, 2
buffer_load_dwordx4 v40, s[16:19], s47 offen offset:0 lds
v_mfma_f32_16x16x32_bf16 v[156:159], a[24:27], a[16:19], v[156:159]
s_sub_u32 s57, 15, s51
s_min_u32 s57, s57, s51
s_lshl_b32 s57, s57, 10
v_mfma_f32_16x16x32_bf16 v[192:195], a[28:31], a[16:19], v[192:195]
v_lshrrev_b32 v252, 4, v1
v_and_b32 v253, 15, v1
v_mfma_f32_16x16x32_bf16 v[196:199], a[32:35], a[16:19], v[196:199]
v_lshlrev_b32 v254, 1, v252
v_xor_b32 v254, v254, v253
v_mfma_f32_16x16x32_bf16 v[232:235], a[16:19], a[36:39], v[232:235]
buffer_load_dwordx4 v41, s[16:19], s47 offen offset:1024 lds
v_lshlrev_b32 v254, 4, v254
v_mfma_f32_16x16x32_bf16 v[236:239], a[16:19], a[40:43], v[236:239]
s_waitcnt lgkmcnt(0)
v_lshl_add_u32 v240, v252, 11, v254
v_xor_b32 v241, 0x80, v240
v_mfma_f32_16x16x32_bf16 v[160:163], a[172:175], a[128:131], v[160:163]
ds_read_b64_tr_b16 a[20:21], v47 offset:14336
ds_read_b64_tr_b16 a[22:23], v47 offset:14848
v_mfma_f32_16x16x32_bf16 v[164:167], a[192:195], a[128:131], v[164:167]
ds_read_b128 a[0:3], v46
v_add_u32 v241, 7168, v241
v_mfma_f32_16x16x32_bf16 v[200:203], a[128:131], a[196:199], v[200:203]
ds_read_b64_tr_b16 a[24:25], v48 offset:14336
ds_read_b64_tr_b16 a[26:27], v48 offset:14848
v_mfma_f32_16x16x32_bf16 v[204:207], a[128:131], a[200:203], v[204:207]
buffer_load_dwordx4 v42, s[12:15], s25 offen offset:2048 lds
v_add_u32 v242, 14336, v240
v_mfma_f32_16x16x32_bf16 v[128:131], a[164:167], a[132:135], v[128:131]
s_add_u32 s26, s26, 393216
s_add_u32 s47, s47, 393216
v_add_u32 v243, 14336, v241
v_mfma_f32_16x16x32_bf16 v[132:135], a[168:171], a[132:135], v[132:135]
s_add_u32 s25, s25, 128
ds_read_b128 a[4:7], v44 offset:0
v_mfma_f32_16x16x32_bf16 v[168:171], a[172:175], a[132:135], v[168:171]
ds_read_b64_tr_b16 a[28:29], v47 offset:16384
ds_read_b64_tr_b16 a[30:31], v47 offset:16896
v_mfma_f32_16x16x32_bf16 v[172:175], a[192:195], a[132:135], v[172:175]
ds_read_b128 a[8:11], v44 offset:2048
v_add_u32 v244, s54, v7
v_mfma_f32_16x16x32_bf16 v[208:211], a[132:135], a[196:199], v[208:211]
s_add_u32 m0, s91, 81920
v_bfe_u32 v254, v1, 2, 3
v_lshrrev_b32 v255, 2, v1
v_mfma_f32_16x16x32_bf16 v[212:215], a[132:135], a[200:203], v[212:215]
buffer_load_dwordx4 v36, s[12:15], s24 offen offset:0 lds
v_lshlrev_b32 v255, 8, v255
v_mfma_f32_16x16x32_bf16 v[136:139], a[164:167], a[136:139], v[136:139]
ds_read_b64_tr_b16 a[32:33], v48 offset:16384
ds_read_b64_tr_b16 a[34:35], v48 offset:16896
v_mfma_f32_16x16x32_bf16 v[140:143], a[168:171], a[136:139], v[140:143]
ds_read_b128 a[12:15], v44 offset:4096
v_and_b32 v253, 3, v1
v_mfma_f32_16x16x32_bf16 v[176:179], a[172:175], a[136:139], v[176:179]
ds_read_b64_tr_b16 a[36:37], v47 offset:18432
ds_read_b64_tr_b16 a[38:39], v47 offset:18944
v_mfma_f32_16x16x32_bf16 v[180:183], a[192:195], a[136:139], v[180:183]
ds_read_b128 a[16:19], v44 offset:6144
v_lshl_add_u32 v255, v253, 3, v255
v_mfma_f32_16x16x32_bf16 v[216:219], a[136:139], a[196:199], v[216:219]
buffer_load_dwordx4 v37, s[12:15], s24 offen offset:1024 lds
v_add_u32 v255, 114688, v255
v_mfma_f32_16x16x32_bf16 v[220:223], a[136:139], a[200:203], v[220:223]
ds_read_b64_tr_b16 a[40:41], v48 offset:18432
ds_read_b64_tr_b16 a[42:43], v48 offset:18944
v_mfma_f32_16x16x32_bf16 v[144:147], a[164:167], a[140:143], v[144:147]
s_cmp_eq_u32 s39, 1
s_mov_b32 s28, 16384
s_cselect_b32 s28, 4294901760, s28
v_mfma_f32_16x16x32_bf16 v[148:151], a[168:171], a[140:143], v[148:151]
v_add_u32 v43, 16384, v43
v_add_u32 v44, 16384, v44
v_mfma_f32_16x16x32_bf16 v[184:187], a[172:175], a[140:143], v[184:187]
v_add_u32 v47, 4294901760, v47
v_add_u32 v48, 4294901760, v48
v_mfma_f32_16x16x32_bf16 v[188:191], a[192:195], a[140:143], v[188:191]
buffer_load_dwordx4 v38, s[12:15], s24 offen offset:2048 lds
v_xor_b32 v254, s56, v254
v_mfma_f32_16x16x32_bf16 v[224:227], a[140:143], a[196:199], v[224:227]
v_add_u32 v45, s28, v45
v_add_u32 v46, s28, v46
v_mfma_f32_16x16x32_bf16 v[228:231], a[140:143], a[200:203], v[228:231]
v_xor_b32 v253, 0, v254
v_lshl_add_u32 v245, v253, 5, v255
v_mfma_f32_16x16x32_bf16 v[152:155], a[164:167], a[160:163], v[152:155]
v_xor_b32 v253, 1, v254
v_lshl_add_u32 v246, v253, 5, v255
v_mfma_f32_16x16x32_bf16 v[156:159], a[168:171], a[160:163], v[156:159]
v_xor_b32 v253, 2, v254
v_lshl_add_u32 v247, v253, 5, v255
v_mfma_f32_16x16x32_bf16 v[192:195], a[172:175], a[160:163], v[192:195]
buffer_load_dwordx4 v39, s[12:15], s24 offen offset:3072 lds
v_xor_b32 v253, 3, v254
v_mfma_f32_16x16x32_bf16 v[196:199], a[192:195], a[160:163], v[196:199]
s_add_u32 s24, s24, 128
v_lshl_add_u32 v248, v253, 5, v255
v_and_b32 v253, 31, v1
v_mfma_f32_16x16x32_bf16 v[232:235], a[160:163], a[196:199], v[232:235]
v_lshl_add_u32 v249, v253, 5, s57
v_mfma_f32_16x16x32_bf16 v[236:239], a[160:163], a[200:203], v[236:239]
s_waitcnt vmcnt(15) lgkmcnt(0)
s_barrier
v_mfma_f32_16x16x32_bf16 v[160:163], a[28:31], a[0:3], v[160:163]
ds_read_b64_tr_b16 a[164:165], v47 offset:0
ds_read_b64_tr_b16 a[166:167], v47 offset:512
v_mfma_f32_16x16x32_bf16 v[164:167], a[32:35], a[0:3], v[164:167]
s_add_u32 m0, s48, 98304
ds_read_b128 a[128:131], v45
v_mfma_f32_16x16x32_bf16 v[200:203], a[0:3], a[36:39], v[200:203]
buffer_load_dwordx4 v40, s[16:19], s26 offen offset:0 lds
v_mfma_f32_16x16x32_bf16 v[204:207], a[0:3], a[40:43], v[204:207]
ds_read_b64_tr_b16 a[168:169], v48 offset:0
ds_read_b64_tr_b16 a[170:171], v48 offset:512
v_mfma_f32_16x16x32_bf16 v[128:131], a[20:23], a[4:7], v[128:131]
ds_read_b128 a[132:135], v43 offset:0
v_mfma_f32_16x16x32_bf16 v[132:135], a[24:27], a[4:7], v[132:135]
ds_read_b64_tr_b16 a[172:173], v47 offset:2048
ds_read_b64_tr_b16 a[174:175], v47 offset:2560
v_mfma_f32_16x16x32_bf16 v[168:171], a[28:31], a[4:7], v[168:171]
ds_read_b128 a[136:139], v43 offset:2048
v_mfma_f32_16x16x32_bf16 v[172:175], a[32:35], a[4:7], v[172:175]
buffer_load_dwordx4 v41, s[16:19], s26 offen offset:1024 lds
v_mfma_f32_16x16x32_bf16 v[208:211], a[4:7], a[36:39], v[208:211]
ds_read_b64_tr_b16 a[192:193], v48 offset:2048
ds_read_b64_tr_b16 a[194:195], v48 offset:2560
v_mfma_f32_16x16x32_bf16 v[212:215], a[4:7], a[40:43], v[212:215]
ds_read_b128 a[140:143], v43 offset:4096
v_mfma_f32_16x16x32_bf16 v[136:139], a[20:23], a[8:11], v[136:139]
ds_read_b64_tr_b16 a[196:197], v47 offset:4096
ds_read_b64_tr_b16 a[198:199], v47 offset:4608
v_mfma_f32_16x16x32_bf16 v[140:143], a[24:27], a[8:11], v[140:143]
ds_read_b128 a[160:163], v43 offset:6144
v_mfma_f32_16x16x32_bf16 v[176:179], a[28:31], a[8:11], v[176:179]
buffer_load_dwordx4 v40, s[16:19], s26 offen offset:2048 lds
v_mfma_f32_16x16x32_bf16 v[180:183], a[32:35], a[8:11], v[180:183]
ds_read_b64_tr_b16 a[200:201], v48 offset:4096
ds_read_b64_tr_b16 a[202:203], v48 offset:4608
v_mfma_f32_16x16x32_bf16 v[216:219], a[8:11], a[36:39], v[216:219]
v_mfma_f32_16x16x32_bf16 v[220:223], a[8:11], a[40:43], v[220:223]
v_mfma_f32_16x16x32_bf16 v[144:147], a[20:23], a[12:15], v[144:147]
v_mfma_f32_16x16x32_bf16 v[148:151], a[24:27], a[12:15], v[148:151]
buffer_load_dwordx4 v41, s[16:19], s26 offen offset:3072 lds
v_mfma_f32_16x16x32_bf16 v[184:187], a[28:31], a[12:15], v[184:187]
v_mfma_f32_16x16x32_bf16 v[188:191], a[32:35], a[12:15], v[188:191]
v_mfma_f32_16x16x32_bf16 v[224:227], a[12:15], a[36:39], v[224:227]
v_mfma_f32_16x16x32_bf16 v[228:231], a[12:15], a[40:43], v[228:231]
s_add_u32 m0, s48, 102400
v_mfma_f32_16x16x32_bf16 v[152:155], a[20:23], a[16:19], v[152:155]
buffer_load_dwordx4 v40, s[16:19], s47 offen offset:0 lds
v_mfma_f32_16x16x32_bf16 v[156:159], a[24:27], a[16:19], v[156:159]
v_mfma_f32_16x16x32_bf16 v[192:195], a[28:31], a[16:19], v[192:195]
buffer_load_dword v35, v4, s[96:99], 0 offen
v_mfma_f32_16x16x32_bf16 v[196:199], a[32:35], a[16:19], v[196:199]
v_mfma_f32_16x16x32_bf16 v[232:235], a[16:19], a[36:39], v[232:235]
buffer_load_dwordx4 v41, s[16:19], s47 offen offset:1024 lds
v_mfma_f32_16x16x32_bf16 v[236:239], a[16:19], a[40:43], v[236:239]
s_waitcnt lgkmcnt(0)
v_mfma_f32_16x16x32_bf16 v[160:163], a[172:175], a[128:131], v[160:163]
ds_read_b64_tr_b16 a[20:21], v47 offset:14336
ds_read_b64_tr_b16 a[22:23], v47 offset:14848
v_mfma_f32_16x16x32_bf16 v[164:167], a[192:195], a[128:131], v[164:167]
ds_read_b128 a[0:3], v46
v_mfma_f32_16x16x32_bf16 v[200:203], a[128:131], a[196:199], v[200:203]
ds_read_b64_tr_b16 a[24:25], v48 offset:14336
ds_read_b64_tr_b16 a[26:27], v48 offset:14848
v_mfma_f32_16x16x32_bf16 v[204:207], a[128:131], a[200:203], v[204:207]
buffer_load_dwordx4 v42, s[12:15], s25 offen offset:2048 lds
v_mfma_f32_16x16x32_bf16 v[128:131], a[164:167], a[132:135], v[128:131]
s_add_u32 s26, s26, 393216
s_add_u32 s47, s47, 393216
v_mfma_f32_16x16x32_bf16 v[132:135], a[168:171], a[132:135], v[132:135]
s_add_u32 s25, s25, 128
ds_read_b128 a[4:7], v44 offset:0
v_mfma_f32_16x16x32_bf16 v[168:171], a[172:175], a[132:135], v[168:171]
ds_read_b64_tr_b16 a[28:29], v47 offset:16384
ds_read_b64_tr_b16 a[30:31], v47 offset:16896
v_mfma_f32_16x16x32_bf16 v[172:175], a[192:195], a[132:135], v[172:175]
ds_read_b128 a[8:11], v44 offset:2048
v_mfma_f32_16x16x32_bf16 v[208:211], a[132:135], a[196:199], v[208:211]
s_add_u32 m0, s91, 0
v_mfma_f32_16x16x32_bf16 v[212:215], a[132:135], a[200:203], v[212:215]
buffer_load_dwordx4 v36, s[12:15], s24 offen offset:0 lds
v_mfma_f32_16x16x32_bf16 v[136:139], a[164:167], a[136:139], v[136:139]
ds_read_b64_tr_b16 a[32:33], v48 offset:16384
ds_read_b64_tr_b16 a[34:35], v48 offset:16896
v_mfma_f32_16x16x32_bf16 v[140:143], a[168:171], a[136:139], v[140:143]
ds_read_b128 a[12:15], v44 offset:4096
v_mfma_f32_16x16x32_bf16 v[176:179], a[172:175], a[136:139], v[176:179]
ds_read_b64_tr_b16 a[36:37], v47 offset:18432
ds_read_b64_tr_b16 a[38:39], v47 offset:18944
v_mfma_f32_16x16x32_bf16 v[180:183], a[192:195], a[136:139], v[180:183]
ds_read_b128 a[16:19], v44 offset:6144
v_mfma_f32_16x16x32_bf16 v[216:219], a[136:139], a[196:199], v[216:219]
buffer_load_dwordx4 v37, s[12:15], s24 offen offset:1024 lds
v_mfma_f32_16x16x32_bf16 v[220:223], a[136:139], a[200:203], v[220:223]
ds_read_b64_tr_b16 a[40:41], v48 offset:18432
ds_read_b64_tr_b16 a[42:43], v48 offset:18944
v_mfma_f32_16x16x32_bf16 v[144:147], a[164:167], a[140:143], v[144:147]
s_cmp_eq_u32 s39, 1
s_mov_b32 s28, 49152
s_cselect_b32 s28, 98304, s28
v_mfma_f32_16x16x32_bf16 v[148:151], a[168:171], a[140:143], v[148:151]
v_add_u32 v43, 49152, v43
v_add_u32 v44, 49152, v44
v_mfma_f32_16x16x32_bf16 v[184:187], a[172:175], a[140:143], v[184:187]
v_add_u32 v47, 98304, v47
v_add_u32 v48, 98304, v48
v_mfma_f32_16x16x32_bf16 v[188:191], a[192:195], a[140:143], v[188:191]
buffer_load_dwordx4 v38, s[12:15], s24 offen offset:2048 lds
v_mfma_f32_16x16x32_bf16 v[224:227], a[140:143], a[196:199], v[224:227]
v_add_u32 v45, s28, v45
v_add_u32 v46, s28, v46
v_mfma_f32_16x16x32_bf16 v[228:231], a[140:143], a[200:203], v[228:231]
v_mfma_f32_16x16x32_bf16 v[152:155], a[164:167], a[160:163], v[152:155]
v_mfma_f32_16x16x32_bf16 v[156:159], a[168:171], a[160:163], v[156:159]
v_mfma_f32_16x16x32_bf16 v[192:195], a[172:175], a[160:163], v[192:195]
buffer_load_dwordx4 v39, s[12:15], s24 offen offset:3072 lds
v_mfma_f32_16x16x32_bf16 v[196:199], a[192:195], a[160:163], v[196:199]
s_add_u32 s24, s24, 128
v_mfma_f32_16x16x32_bf16 v[232:235], a[160:163], a[196:199], v[232:235]
v_mfma_f32_16x16x32_bf16 v[236:239], a[160:163], a[200:203], v[236:239]
s_waitcnt vmcnt(16) lgkmcnt(0)
s_barrier
v_mfma_f32_16x16x32_bf16 v[160:163], a[28:31], a[0:3], v[160:163]
ds_read_b64_tr_b16 a[164:165], v47 offset:0
ds_read_b64_tr_b16 a[166:167], v47 offset:512
v_mfma_f32_16x16x32_bf16 v[164:167], a[32:35], a[0:3], v[164:167]
s_add_u32 m0, s48, 32768
ds_read_b128 a[128:131], v45
v_mfma_f32_16x16x32_bf16 v[200:203], a[0:3], a[36:39], v[200:203]
buffer_load_dwordx4 v40, s[16:19], s26 offen offset:0 lds
v_mfma_f32_16x16x32_bf16 v[204:207], a[0:3], a[40:43], v[204:207]
ds_read_b64_tr_b16 a[168:169], v48 offset:0
ds_read_b64_tr_b16 a[170:171], v48 offset:512
v_mfma_f32_16x16x32_bf16 v[128:131], a[20:23], a[4:7], v[128:131]
ds_read_b128 a[132:135], v43 offset:0
v_mfma_f32_16x16x32_bf16 v[132:135], a[24:27], a[4:7], v[132:135]
ds_read_b64_tr_b16 a[172:173], v47 offset:2048
ds_read_b64_tr_b16 a[174:175], v47 offset:2560
v_mfma_f32_16x16x32_bf16 v[168:171], a[28:31], a[4:7], v[168:171]
ds_read_b128 a[136:139], v43 offset:2048
v_mfma_f32_16x16x32_bf16 v[172:175], a[32:35], a[4:7], v[172:175]
buffer_load_dwordx4 v41, s[16:19], s26 offen offset:1024 lds
v_mfma_f32_16x16x32_bf16 v[208:211], a[4:7], a[36:39], v[208:211]
ds_read_b64_tr_b16 a[192:193], v48 offset:2048
ds_read_b64_tr_b16 a[194:195], v48 offset:2560
v_mfma_f32_16x16x32_bf16 v[212:215], a[4:7], a[40:43], v[212:215]
ds_read_b128 a[140:143], v43 offset:4096
v_mfma_f32_16x16x32_bf16 v[136:139], a[20:23], a[8:11], v[136:139]
ds_read_b64_tr_b16 a[196:197], v47 offset:4096
ds_read_b64_tr_b16 a[198:199], v47 offset:4608
v_mfma_f32_16x16x32_bf16 v[140:143], a[24:27], a[8:11], v[140:143]
ds_read_b128 a[160:163], v43 offset:6144
v_mfma_f32_16x16x32_bf16 v[176:179], a[28:31], a[8:11], v[176:179]
buffer_load_dwordx4 v40, s[16:19], s26 offen offset:2048 lds
v_mfma_f32_16x16x32_bf16 v[180:183], a[32:35], a[8:11], v[180:183]
ds_read_b64_tr_b16 a[200:201], v48 offset:4096
ds_read_b64_tr_b16 a[202:203], v48 offset:4608
v_mfma_f32_16x16x32_bf16 v[216:219], a[8:11], a[36:39], v[216:219]
v_mfma_f32_16x16x32_bf16 v[220:223], a[8:11], a[40:43], v[220:223]
v_mfma_f32_16x16x32_bf16 v[144:147], a[20:23], a[12:15], v[144:147]
v_mfma_f32_16x16x32_bf16 v[148:151], a[24:27], a[12:15], v[148:151]
buffer_load_dwordx4 v41, s[16:19], s26 offen offset:3072 lds
v_mfma_f32_16x16x32_bf16 v[184:187], a[28:31], a[12:15], v[184:187]
v_mfma_f32_16x16x32_bf16 v[188:191], a[32:35], a[12:15], v[188:191]
v_mfma_f32_16x16x32_bf16 v[224:227], a[12:15], a[36:39], v[224:227]
v_mfma_f32_16x16x32_bf16 v[228:231], a[12:15], a[40:43], v[228:231]
s_add_u32 m0, s48, 36864
v_mfma_f32_16x16x32_bf16 v[152:155], a[20:23], a[16:19], v[152:155]
buffer_load_dwordx4 v40, s[16:19], s47 offen offset:0 lds
v_mfma_f32_16x16x32_bf16 v[156:159], a[24:27], a[16:19], v[156:159]
v_mfma_f32_16x16x32_bf16 v[192:195], a[28:31], a[16:19], v[192:195]
v_mfma_f32_16x16x32_bf16 v[196:199], a[32:35], a[16:19], v[196:199]
v_mfma_f32_16x16x32_bf16 v[232:235], a[16:19], a[36:39], v[232:235]
buffer_load_dwordx4 v41, s[16:19], s47 offen offset:1024 lds
v_mfma_f32_16x16x32_bf16 v[236:239], a[16:19], a[40:43], v[236:239]
s_waitcnt lgkmcnt(0)
v_mfma_f32_16x16x32_bf16 v[160:163], a[172:175], a[128:131], v[160:163]
ds_read_b64_tr_b16 a[20:21], v47 offset:14336
ds_read_b64_tr_b16 a[22:23], v47 offset:14848
v_mfma_f32_16x16x32_bf16 v[164:167], a[192:195], a[128:131], v[164:167]
ds_read_b128 a[0:3], v46
v_and_b32 v28, 15, v1
v_mfma_f32_16x16x32_bf16 v[200:203], a[128:131], a[196:199], v[200:203]
ds_read_b64_tr_b16 a[24:25], v48 offset:14336
ds_read_b64_tr_b16 a[26:27], v48 offset:14848
v_mfma_f32_16x16x32_bf16 v[204:207], a[128:131], a[200:203], v[204:207]
buffer_load_dwordx4 v42, s[12:15], s25 offen offset:2048 lds
v_lshrrev_b32 v29, 4, v1
v_mfma_f32_16x16x32_bf16 v[128:131], a[164:167], a[132:135], v[128:131]
s_add_u32 s26, s26, 393216
s_add_u32 s47, s47, 393216
v_lshlrev_b32 v29, 2, v29
v_mfma_f32_16x16x32_bf16 v[132:135], a[168:171], a[132:135], v[132:135]
s_add_u32 s25, s25, 128
ds_read_b128 a[4:7], v44 offset:0
v_mfma_f32_16x16x32_bf16 v[168:171], a[172:175], a[132:135], v[168:171]
ds_read_b64_tr_b16 a[28:29], v47 offset:16384
ds_read_b64_tr_b16 a[30:31], v47 offset:16896
v_mfma_f32_16x16x32_bf16 v[172:175], a[192:195], a[132:135], v[172:175]
ds_read_b128 a[8:11], v44 offset:2048
v_sub_u32 v28, v28, v29
v_mfma_f32_16x16x32_bf16 v[208:211], a[132:135], a[196:199], v[208:211]
s_add_u32 m0, s91, 16384
v_mov_b32 v30, 0
v_add_u32 v29, 16, v28
v_mfma_f32_16x16x32_bf16 v[212:215], a[132:135], a[200:203], v[212:215]
buffer_load_dwordx4 v36, s[12:15], s24 offen offset:0 lds
v_and_b32 v29, 0xfffffff1, v29
v_mfma_f32_16x16x32_bf16 v[136:139], a[164:167], a[136:139], v[136:139]
ds_read_b64_tr_b16 a[32:33], v48 offset:16384
ds_read_b64_tr_b16 a[34:35], v48 offset:16896
v_mfma_f32_16x16x32_bf16 v[140:143], a[168:171], a[136:139], v[140:143]
ds_read_b128 a[12:15], v44 offset:4096
v_cmp_eq_u32 vcc, 0, v29
v_mfma_f32_16x16x32_bf16 v[176:179], a[172:175], a[136:139], v[176:179]
ds_read_b64_tr_b16 a[36:37], v47 offset:18432
ds_read_b64_tr_b16 a[38:39], v47 offset:18944
v_mfma_f32_16x16x32_bf16 v[180:183], a[192:195], a[136:139], v[180:183]
ds_read_b128 a[16:19], v44 offset:6144
v_nop
v_mfma_f32_16x16x32_bf16 v[216:219], a[136:139], a[196:199], v[216:219]
buffer_load_dwordx4 v37, s[12:15], s24 offen offset:1024 lds
v_nop
v_mfma_f32_16x16x32_bf16 v[220:223], a[136:139], a[200:203], v[220:223]
ds_read_b64_tr_b16 a[40:41], v48 offset:18432
ds_read_b64_tr_b16 a[42:43], v48 offset:18944
.Lpad17:
s_cbranch_execz .Ltch8
v_mfma_f32_16x16x32_bf16 v[144:147], a[164:167], a[140:143], v[144:147]
s_cmp_eq_u32 s39, 1
s_mov_b32 s28, 16384
s_cselect_b32 s28, 4294934528, s28
v_mfma_f32_16x16x32_bf16 v[148:151], a[168:171], a[140:143], v[148:151]
v_add_u32 v43, 16384, v43
v_add_u32 v44, 16384, v44
v_mfma_f32_16x16x32_bf16 v[184:187], a[172:175], a[140:143], v[184:187]
v_add_u32 v47, 4294934528, v47
v_add_u32 v48, 4294934528, v48
v_mfma_f32_16x16x32_bf16 v[188:191], a[192:195], a[140:143], v[188:191]
buffer_load_dwordx4 v38, s[12:15], s24 offen offset:2048 lds
v_cndmask_b32 v16, v34, v30, vcc
v_mfma_f32_16x16x32_bf16 v[224:227], a[140:143], a[196:199], v[224:227]
v_add_u32 v45, s28, v45
v_add_u32 v46, s28, v46
v_mfma_f32_16x16x32_bf16 v[228:231], a[140:143], a[200:203], v[228:231]
v_add_u32 v29, 15, v28
v_and_b32 v29, 0xfffffff1, v29
v_mfma_f32_16x16x32_bf16 v[152:155], a[164:167], a[160:163], v[152:155]
v_cmp_eq_u32 vcc, 0, v29
v_nop
v_mfma_f32_16x16x32_bf16 v[156:159], a[168:171], a[160:163], v[156:159]
v_nop
v_cndmask_b32 v17, v34, v30, vcc
v_mfma_f32_16x16x32_bf16 v[192:195], a[172:175], a[160:163], v[192:195]
buffer_load_dwordx4 v39, s[12:15], s24 offen offset:3072 lds
v_add_u32 v29, 14, v28
v_mfma_f32_16x16x32_bf16 v[196:199], a[192:195], a[160:163], v[196:199]
s_add_u32 s24, s24, 128
v_and_b32 v29, 0xfffffff1, v29
v_cmp_eq_u32 vcc, 0, v29
v_mfma_f32_16x16x32_bf16 v[232:235], a[160:163], a[196:199], v[232:235]
buffer_load_dword v250, v249, s[60:63], s90 offen sc1
v_nop
v_nop
v_mfma_f32_16x16x32_bf16 v[236:239], a[160:163], a[200:203], v[236:239]
s_waitcnt vmcnt(16) lgkmcnt(0)
s_barrier
v_cndmask_b32 v18, v34, v30, vcc
v_mfma_f32_16x16x32_bf16 v[160:163], a[28:31], a[0:3], v[160:163]
ds_read_b64_tr_b16 a[164:165], v47 offset:0
ds_read_b64_tr_b16 a[166:167], v47 offset:512
v_mfma_f32_16x16x32_bf16 v[164:167], a[32:35], a[0:3], v[164:167]
s_add_u32 m0, s48, 131072
ds_read_b128 a[128:131], v45
v_mfma_f32_16x16x32_bf16 v[200:203], a[0:3], a[36:39], v[200:203]
buffer_load_dwordx4 v40, s[16:19], s26 offen offset:0 lds
v_add_u32 v29, 13, v28
v_mfma_f32_16x16x32_bf16 v[204:207], a[0:3], a[40:43], v[204:207]
ds_read_b64_tr_b16 a[168:169], v48 offset:0
ds_read_b64_tr_b16 a[170:171], v48 offset:512
v_mfma_f32_16x16x32_bf16 v[128:131], a[20:23], a[4:7], v[128:131]
ds_read_b128 a[132:135], v43 offset:0
v_and_b32 v29, 0xfffffff1, v29
v_mfma_f32_16x16x32_bf16 v[132:135], a[24:27], a[4:7], v[132:135]
ds_read_b64_tr_b16 a[172:173], v47 offset:2048
ds_read_b64_tr_b16 a[174:175], v47 offset:2560
v_mfma_f32_16x16x32_bf16 v[168:171], a[28:31], a[4:7], v[168:171]
ds_read_b128 a[136:139], v43 offset:2048
v_cmp_eq_u32 vcc, 0, v29
v_mfma_f32_16x16x32_bf16 v[172:175], a[32:35], a[4:7], v[172:175]
buffer_load_dwordx4 v41, s[16:19], s26 offen offset:1024 lds
v_nop
v_mfma_f32_16x16x32_bf16 v[208:211], a[4:7], a[36:39], v[208:211]
ds_read_b64_tr_b16 a[192:193], v48 offset:2048
ds_read_b64_tr_b16 a[194:195], v48 offset:2560
v_mfma_f32_16x16x32_bf16 v[212:215], a[4:7], a[40:43], v[212:215]
ds_read_b128 a[140:143], v43 offset:4096
v_nop
v_mfma_f32_16x16x32_bf16 v[136:139], a[20:23], a[8:11], v[136:139]
ds_read_b64_tr_b16 a[196:197], v47 offset:4096
ds_read_b64_tr_b16 a[198:199], v47 offset:4608
v_mfma_f32_16x16x32_bf16 v[140:143], a[24:27], a[8:11], v[140:143]
ds_read_b128 a[160:163], v43 offset:6144
v_cndmask_b32 v19, v34, v30, vcc
v_mfma_f32_16x16x32_bf16 v[176:179], a[28:31], a[8:11], v[176:179]
buffer_load_dwordx4 v40, s[16:19], s26 offen offset:2048 lds
v_add_u32 v29, 0, v28
v_mfma_f32_16x16x32_bf16 v[180:183], a[32:35], a[8:11], v[180:183]
ds_read_b64_tr_b16 a[200:201], v48 offset:4096
ds_read_b64_tr_b16 a[202:203], v48 offset:4608
v_mfma_f32_16x16x32_bf16 v[216:219], a[8:11], a[36:39], v[216:219]
v_and_b32 v29, 0xfffffff1, v29
v_cmp_eq_u32 vcc, 0, v29
v_mfma_f32_16x16x32_bf16 v[220:223], a[8:11], a[40:43], v[220:223]
v_nop
v_nop
v_mfma_f32_16x16x32_bf16 v[144:147], a[20:23], a[12:15], v[144:147]
v_cndmask_b32 v20, v34, v30, vcc
v_add_u32 v29, -1, v28
v_mfma_f32_16x16x32_bf16 v[148:151], a[24:27], a[12:15], v[148:151]
buffer_load_dwordx4 v41, s[16:19], s26 offen offset:3072 lds
v_and_b32 v29, 0xfffffff1, v29
v_mfma_f32_16x16x32_bf16 v[184:187], a[28:31], a[12:15], v[184:187]
v_cmp_eq_u32 vcc, 0, v29
v_nop
v_mfma_f32_16x16x32_bf16 v[188:191], a[32:35], a[12:15], v[188:191]
v_nop
v_cndmask_b32 v21, v34, v30, vcc
v_mfma_f32_16x16x32_bf16 v[224:227], a[12:15], a[36:39], v[224:227]
v_add_u32 v29, -2, v28
v_and_b32 v29, 0xfffffff1, v29
v_mfma_f32_16x16x32_bf16 v[228:231], a[12:15], a[40:43], v[228:231]
s_add_u32 m0, s48, 135168
v_cmp_eq_u32 vcc, 0, v29
v_nop
v_mfma_f32_16x16x32_bf16 v[152:155], a[20:23], a[16:19], v[152:155]
buffer_load_dwordx4 v40, s[16:19], s47 offen offset:0 lds
v_nop
v_mfma_f32_16x16x32_bf16 v[156:159], a[24:27], a[16:19], v[156:159]
v_cndmask_b32 v22, v34, v30, vcc
v_add_u32 v29, -3, v28
v_mfma_f32_16x16x32_bf16 v[192:195], a[28:31], a[16:19], v[192:195]
v_and_b32 v29, 0xfffffff1, v29
v_cmp_eq_u32 vcc, 0, v29
v_mfma_f32_16x16x32_bf16 v[196:199], a[32:35], a[16:19], v[196:199]
v_nop
v_nop
v_mfma_f32_16x16x32_bf16 v[232:235], a[16:19], a[36:39], v[232:235]
buffer_load_dwordx4 v41, s[16:19], s47 offen offset:1024 lds
v_cndmask_b32 v23, v34, v30, vcc
v_mfma_f32_16x16x32_bf16 v[236:239], a[16:19], a[40:43], v[236:239]
s_waitcnt lgkmcnt(0)
s_lshr_b32 s29, s3, 1
s_or_b32 s29, s29, s36
v_mfma_f32_16x16x32_bf16 v[160:163], a[172:175], a[128:131], v[160:163]
ds_read_b64_tr_b16 a[20:21], v47 offset:14336
ds_read_b64_tr_b16 a[22:23], v47 offset:14848
v_mfma_f32_16x16x32_bf16 v[164:167], a[192:195], a[128:131], v[164:167]
ds_read_b128 a[0:3], v46
v_mfma_f32_16x16x32_bf16 v[200:203], a[128:131], a[196:199], v[200:203]
ds_read_b64_tr_b16 a[24:25], v48 offset:14336
ds_read_b64_tr_b16 a[26:27], v48 offset:14848
v_mfma_f32_16x16x32_bf16 v[204:207], a[128:131], a[200:203], v[204:207]
buffer_load_dwordx4 v42, s[12:15], s25 offen offset:2048 lds
v_mfma_f32_16x16x32_bf16 v[128:131], a[164:167], a[132:135], v[128:131]
s_add_u32 s26, s26, 393216
s_add_u32 s47, s47, 393216
v_mfma_f32_16x16x32_bf16 v[132:135], a[168:171], a[132:135], v[132:135]
s_add_u32 s25, s25, 128
ds_read_b128 a[4:7], v44 offset:0
v_mfma_f32_16x16x32_bf16 v[168:171], a[172:175], a[132:135], v[168:171]
ds_read_b64_tr_b16 a[28:29], v47 offset:16384
ds_read_b64_tr_b16 a[30:31], v47 offset:16896
v_mfma_f32_16x16x32_bf16 v[172:175], a[192:195], a[132:135], v[172:175]
ds_read_b128 a[8:11], v44 offset:2048
v_mfma_f32_16x16x32_bf16 v[208:211], a[132:135], a[196:199], v[208:211]
ds_read_b64_tr_b16 a[32:33], v48 offset:16384
ds_read_b64_tr_b16 a[34:35], v48 offset:16896
v_mfma_f32_16x16x32_bf16 v[212:215], a[132:135], a[200:203], v[212:215]
ds_read_b128 a[12:15], v44 offset:4096
v_mfma_f32_16x16x32_bf16 v[136:139], a[164:167], a[136:139], v[136:139]
ds_read_b64_tr_b16 a[36:37], v47 offset:18432
ds_read_b64_tr_b16 a[38:39], v47 offset:18944
v_mfma_f32_16x16x32_bf16 v[140:143], a[168:171], a[136:139], v[140:143]
ds_read_b128 a[16:19], v44 offset:6144
v_mfma_f32_16x16x32_bf16 v[176:179], a[172:175], a[136:139], v[176:179]
ds_read_b64_tr_b16 a[40:41], v48 offset:18432
ds_read_b64_tr_b16 a[42:43], v48 offset:18944
v_mfma_f32_16x16x32_bf16 v[180:183], a[192:195], a[136:139], v[180:183]
s_cmp_eq_u32 s39, 1
s_mov_b32 s28, 4294885376
s_cselect_b32 s28, 4294901760, s28
v_mfma_f32_16x16x32_bf16 v[216:219], a[136:139], a[196:199], v[216:219]
v_add_u32 v43, 4294885376, v43
v_add_u32 v44, 4294885376, v44
v_mfma_f32_16x16x32_bf16 v[220:223], a[136:139], a[200:203], v[220:223]
v_add_u32 v47, 4294901760, v47
v_add_u32 v48, 4294901760, v48
v_mfma_f32_16x16x32_bf16 v[144:147], a[164:167], a[140:143], v[144:147]
v_add_u32 v45, s28, v45
v_add_u32 v46, s28, v46
v_mfma_f32_16x16x32_bf16 v[148:151], a[168:171], a[140:143], v[148:151]
s_cmp_eq_u32 s29, 0
s_cselect_b64 s[32:33], -1, 0
v_mfma_f32_16x16x32_bf16 v[184:187], a[172:175], a[140:143], v[184:187]
v_cndmask_b32 v24, v16, v34, s[32:33]
v_cndmask_b32 v25, v17, v34, s[32:33]
v_mfma_f32_16x16x32_bf16 v[188:191], a[192:195], a[140:143], v[188:191]
v_cndmask_b32 v26, v18, v34, s[32:33]
v_cndmask_b32 v27, v19, v34, s[32:33]
v_mfma_f32_16x16x32_bf16 v[224:227], a[140:143], a[196:199], v[224:227]
s_waitcnt vmcnt(7)
v_and_b32 v250, 7, v250
v_mfma_f32_16x16x32_bf16 v[228:231], a[140:143], a[200:203], v[228:231]
v_mfma_f32_16x16x32_bf16 v[152:155], a[164:167], a[160:163], v[152:155]
v_cmp_gt_u32 vcc, 3, v250
v_mfma_f32_16x16x32_bf16 v[156:159], a[168:171], a[160:163], v[156:159]
v_mfma_f32_16x16x32_bf16 v[192:195], a[172:175], a[160:163], v[192:195]
s_cmp_lg_u64 vcc, 0
s_cbranch_scc1 .Lspin3
.Lspin3_ret:
v_mfma_f32_16x16x32_bf16 v[196:199], a[192:195], a[160:163], v[196:199]
v_mfma_f32_16x16x32_bf16 v[232:235], a[160:163], a[196:199], v[232:235]
s_lshl_b32 s54, s51, 19
s_and_b32 s56, s3, 1
s_lshl_b32 s56, s56, 18
v_mfma_f32_16x16x32_bf16 v[236:239], a[160:163], a[200:203], v[236:239]
s_waitcnt vmcnt(12) lgkmcnt(0)
s_barrier
s_add_u32 s54, s54, s56
v_mfma_f32_16x16x32_bf16 v[160:163], a[28:31], a[0:3], v[160:163]
ds_read_b64_tr_b16 a[164:165], v47 offset:0
ds_read_b64_tr_b16 a[166:167], v47 offset:512
v_mfma_f32_16x16x32_bf16 v[164:167], a[32:35], a[0:3], v[164:167]
s_add_u32 m0, s91, 114688
ds_read_b128 a[128:131], v45
v_mfma_f32_16x16x32_bf16 v[200:203], a[0:3], a[36:39], v[200:203]
buffer_load_dwordx4 v240, s[8:11], s45 offen offset:0 lds
ds_read_b64_tr_b16 a[168:169], v48 offset:0
ds_read_b64_tr_b16 a[170:171], v48 offset:512
v_mfma_f32_16x16x32_bf16 v[204:207], a[0:3], a[40:43], v[204:207]
s_add_u32 s54, s54, s52
ds_read_b128 a[132:135], v43 offset:0
v_mfma_f32_16x16x32_bf16 v[128:131], a[20:23], a[4:7], v[128:131]
ds_read_b64_tr_b16 a[172:173], v47 offset:2048
ds_read_b64_tr_b16 a[174:175], v47 offset:2560
v_mfma_f32_16x16x32_bf16 v[132:135], a[24:27], a[4:7], v[132:135]
buffer_load_dwordx4 v241, s[8:11], s45 offen offset:1024 lds
ds_read_b128 a[136:139], v43 offset:2048
v_mfma_f32_16x16x32_bf16 v[168:171], a[28:31], a[4:7], v[168:171]
ds_read_b64_tr_b16 a[192:193], v48 offset:2048
ds_read_b64_tr_b16 a[194:195], v48 offset:2560
v_mfma_f32_16x16x32_bf16 v[172:175], a[32:35], a[4:7], v[172:175]
s_lshr_b32 s56, s3, 1
ds_read_b128 a[140:143], v43 offset:4096
v_mfma_f32_16x16x32_bf16 v[208:211], a[4:7], a[36:39], v[208:211]
buffer_load_dwordx4 v242, s[8:11], s45 offen offset:2048 lds
ds_read_b64_tr_b16 a[196:197], v47 offset:4096
ds_read_b64_tr_b16 a[198:199], v47 offset:4608
v_mfma_f32_16x16x32_bf16 v[212:215], a[4:7], a[40:43], v[212:215]
s_lshl_b32 s56, s56, 7
ds_read_b128 a[160:163], v43 offset:6144
v_mfma_f32_16x16x32_bf16 v[136:139], a[20:23], a[8:11], v[136:139]
ds_read_b64_tr_b16 a[200:201], v48 offset:4096
ds_read_b64_tr_b16 a[202:203], v48 offset:4608
v_mfma_f32_16x16x32_bf16 v[140:143], a[24:27], a[8:11], v[140:143]
s_add_u32 s54, s54, s56
buffer_load_dwordx4 v243, s[8:11], s45 offen offset:3072 lds
v_mfma_f32_16x16x32_bf16 v[176:179], a[28:31], a[8:11], v[176:179]
s_add_u32 s45, s45, 131072
s_xor_b32 s56, s55, 1
s_lshl_b32 s56, s56, 11
v_mfma_f32_16x16x32_bf16 v[180:183], a[32:35], a[8:11], v[180:183]
s_add_u32 m0, s91, 98304
s_add_u32 s54, s54, s56
s_sub_u32 s18, 0x800000, s54
v_mfma_f32_16x16x32_bf16 v[216:219], a[8:11], a[36:39], v[216:219]
buffer_load_dwordx4 v7, s[4:7], s44 offen offset:0 sc1 lds
v_mfma_f32_16x16x32_bf16 v[220:223], a[8:11], a[40:43], v[220:223]
s_add_u32 s16, s82, s54
s_addc_u32 s17, s83, 0
s_and_b32 s17, s17, 0xffff
v_mfma_f32_16x16x32_bf16 v[144:147], a[20:23], a[12:15], v[144:147]
s_lshr_b32 s54, s42, 1
s_lshl_b32 s54, s54, 5
s_add_u32 s54, s54, s87
v_mfma_f32_16x16x32_bf16 v[148:151], a[24:27], a[12:15], v[148:151]
s_lshl_b32 s54, s54, 16
buffer_load_dwordx4 v7, s[4:7], s44 offen offset:1024 sc1 lds
v_mfma_f32_16x16x32_bf16 v[184:187], a[28:31], a[12:15], v[184:187]
s_lshl_b32 s56, s3, 14
s_add_u32 s54, s54, s56
s_add_u32 s54, s54, 54525952
v_mfma_f32_16x16x32_bf16 v[188:191], a[32:35], a[12:15], v[188:191]
s_add_u32 s12, s84, s54
s_addc_u32 s13, s85, 0
s_and_b32 s13, s13, 0xffff
v_mfma_f32_16x16x32_bf16 v[224:227], a[12:15], a[36:39], v[224:227]
s_mov_b32 s14, 16384
buffer_load_dwordx4 v7, s[4:7], s44 offen offset:2048 sc1 lds
v_mfma_f32_16x16x32_bf16 v[228:231], a[12:15], a[40:43], v[228:231]
s_mov_b32 s56, 131072
s_mov_b32 s57, -32768
s_mov_b32 s46, 98304
v_mfma_f32_16x16x32_bf16 v[152:155], a[20:23], a[16:19], v[152:155]
s_lshr_b32 s30, s51, 3
s_and_b32 s31, s51, 7
s_lshl_b32 s31, s31, 3
v_mfma_f32_16x16x32_bf16 v[156:159], a[24:27], a[16:19], v[156:159]
s_or_b32 s30, s30, s31
buffer_load_dwordx4 v7, s[4:7], s44 offen offset:3072 sc1 lds
v_mfma_f32_16x16x32_bf16 v[192:195], a[28:31], a[16:19], v[192:195]
s_add_u32 s44, s44, 4096
s_mov_b32 s31, s38
s_sub_u32 s50, s43, 14
v_mfma_f32_16x16x32_bf16 v[196:199], a[32:35], a[16:19], v[196:199]
s_sub_u32 s54, 48, s43
v_mfma_f32_16x16x32_bf16 v[232:235], a[16:19], a[36:39], v[232:235]
s_add_u32 m0, s91, 81920
v_mfma_f32_16x16x32_bf16 v[236:239], a[16:19], a[40:43], v[236:239]
s_waitcnt lgkmcnt(0)
buffer_load_dwordx4 v240, s[8:11], s45 offen offset:0 lds
v_mfma_f32_16x16x32_bf16 v[160:163], a[172:175], a[128:131], v[160:163]
ds_read_b64_tr_b16 a[20:21], v47 offset:14336
ds_read_b64_tr_b16 a[22:23], v47 offset:14848
v_mfma_f32_16x16x32_bf16 v[164:167], a[192:195], a[128:131], v[164:167]
s_lshl_b32 s34, s36, 18
ds_read_b128 a[0:3], v46
v_mfma_f32_16x16x32_bf16 v[200:203], a[128:131], a[196:199], v[200:203]
buffer_load_dwordx4 v241, s[8:11], s45 offen offset:1024 lds
ds_read_b64_tr_b16 a[24:25], v48 offset:14336
ds_read_b64_tr_b16 a[26:27], v48 offset:14848
v_mfma_f32_16x16x32_bf16 v[204:207], a[128:131], a[200:203], v[204:207]
s_lshr_b32 s35, s3, 1
ds_read_b128 a[4:7], v44 offset:0
v_mfma_f32_16x16x32_bf16 v[128:131], a[164:167], a[132:135], v[128:131]
ds_read_b64_tr_b16 a[28:29], v47 offset:16384
ds_read_b64_tr_b16 a[30:31], v47 offset:16896
v_mfma_f32_16x16x32_bf16 v[132:135], a[168:171], a[132:135], v[132:135]
s_lshl_b32 s35, s35, 17
buffer_load_dwordx4 v242, s[8:11], s45 offen offset:2048 lds
ds_read_b128 a[8:11], v44 offset:2048
v_mfma_f32_16x16x32_bf16 v[168:171], a[172:175], a[132:135], v[168:171]
ds_read_b64_tr_b16 a[32:33], v48 offset:16384
ds_read_b64_tr_b16 a[34:35], v48 offset:16896
v_mfma_f32_16x16x32_bf16 v[172:175], a[192:195], a[132:135], v[172:175]
s_add_u32 s34, s34, s35
ds_read_b128 a[12:15], v44 offset:4096
v_mfma_f32_16x16x32_bf16 v[208:211], a[132:135], a[196:199], v[208:211]
buffer_load_dwordx4 v243, s[8:11], s45 offen offset:3072 lds
ds_read_b64_tr_b16 a[36:37], v47 offset:18432
ds_read_b64_tr_b16 a[38:39], v47 offset:18944
v_mfma_f32_16x16x32_bf16 v[212:215], a[132:135], a[200:203], v[212:215]
s_lshl_b32 s35, s37, 12
s_add_u32 s45, s45, 131072
ds_read_b128 a[16:19], v44 offset:6144
v_mfma_f32_16x16x32_bf16 v[136:139], a[164:167], a[136:139], v[136:139]
s_add_u32 m0, s91, 65536
ds_read_b64_tr_b16 a[40:41], v48 offset:18432
ds_read_b64_tr_b16 a[42:43], v48 offset:18944
v_mfma_f32_16x16x32_bf16 v[140:143], a[168:171], a[136:139], v[140:143]
s_cmp_eq_u32 s39, 1
s_mov_b32 s28, 16384
s_cselect_b32 s28, 98304, s28
buffer_load_dwordx4 v7, s[4:7], s44 offen offset:0 sc1 lds
v_mfma_f32_16x16x32_bf16 v[176:179], a[172:175], a[136:139], v[176:179]
v_add_u32 v43, 16384, v43
v_add_u32 v44, 16384, v44
v_mfma_f32_16x16x32_bf16 v[180:183], a[192:195], a[136:139], v[180:183]
v_add_u32 v47, 98304, v47
v_add_u32 v48, 98304, v48
v_mfma_f32_16x16x32_bf16 v[216:219], a[136:139], a[196:199], v[216:219]
buffer_load_dwordx4 v7, s[4:7], s44 offen offset:1024 sc1 lds
v_add_u32 v45, s28, v45
v_add_u32 v46, s28, v46
v_mfma_f32_16x16x32_bf16 v[220:223], a[136:139], a[200:203], v[220:223]
s_add_u32 s34, s34, s35
s_and_b32 s35, s3, 1
s_lshl_b32 s35, s35, 11
v_mfma_f32_16x16x32_bf16 v[144:147], a[164:167], a[140:143], v[144:147]
s_add_u32 s34, s34, s35
s_add_u32 s34, s34, 50331648
v_mfma_f32_16x16x32_bf16 v[148:151], a[168:171], a[140:143], v[148:151]
s_add_u32 s20, s84, s34
s_addc_u32 s21, s85, 0
s_and_b32 s21, s21, 0xffff
buffer_load_dwordx4 v7, s[4:7], s44 offen offset:2048 sc1 lds
v_mfma_f32_16x16x32_bf16 v[184:187], a[172:175], a[140:143], v[184:187]
s_mov_b32 s22, 131072
s_mov_b32 s40, 0
s_mov_b32 s41, 65536
v_mfma_f32_16x16x32_bf16 v[188:191], a[192:195], a[140:143], v[188:191]
s_cmp_eq_u32 s55, 0
s_cselect_b64 s[58:59], -1, 0
s_cselect_b32 s50, s50, s54
v_mfma_f32_16x16x32_bf16 v[224:227], a[140:143], a[196:199], v[224:227]
buffer_load_dwordx4 v7, s[4:7], s44 offen offset:3072 sc1 lds
v_and_b32 v252, 7, v1
v_mfma_f32_16x16x32_bf16 v[228:231], a[140:143], a[200:203], v[228:231]
s_add_u32 s44, s44, 4096
v_bfe_u32 v253, v1, 3, 1
v_bfe_u32 v254, v1, 4, 1
v_mfma_f32_16x16x32_bf16 v[152:155], a[164:167], a[160:163], v[152:155]
v_lshrrev_b32 v255, 5, v1
v_lshlrev_b32 v252, 12, v252
v_mfma_f32_16x16x32_bf16 v[156:159], a[168:171], a[160:163], v[156:159]
v_lshl_add_u32 v252, v253, 6, v252
v_lshl_add_u32 v252, v254, 5, v252
v_mfma_f32_16x16x32_bf16 v[192:195], a[172:175], a[160:163], v[192:195]
v_lshl_add_u32 v251, v255, 4, v252
v_bfe_u32 v252, v1, 2, 2
v_mfma_f32_16x16x32_bf16 v[196:199], a[192:195], a[160:163], v[196:199]
v_and_b32 v253, 3, v1
v_lshlrev_b32 v252, 1, v252
v_mfma_f32_16x16x32_bf16 v[232:235], a[160:163], a[196:199], v[232:235]
v_lshl_or_b32 v252, v253, 6, v252
v_or_b32 v252, s30, v252
v_mfma_f32_16x16x32_bf16 v[236:239], a[160:163], a[200:203], v[236:239]
s_waitcnt vmcnt(16) lgkmcnt(0)
s_barrier
v_mov_b32 v253, s31
v_mfma_f32_16x16x32_bf16 v[160:163], a[28:31], a[0:3], v[160:163]
ds_read_b64_tr_b16 a[164:165], v47 offset:0
ds_read_b64_tr_b16 a[166:167], v47 offset:512
v_mfma_f32_16x16x32_bf16 v[164:167], a[32:35], a[0:3], v[164:167]
s_add_u32 m0, s91, 49152
ds_read_b128 a[128:131], v45
v_mfma_f32_16x16x32_bf16 v[200:203], a[0:3], a[36:39], v[200:203]
buffer_load_dwordx4 v240, s[8:11], s45 offen offset:0 lds
ds_read_b64_tr_b16 a[168:169], v48 offset:0
ds_read_b64_tr_b16 a[170:171], v48 offset:512
v_mfma_f32_16x16x32_bf16 v[204:207], a[0:3], a[40:43], v[204:207]
ds_read_b128 a[132:135], v43 offset:0
v_cndmask_b32 v252, v252, v253, s[58:59]
v_mfma_f32_16x16x32_bf16 v[128:131], a[20:23], a[4:7], v[128:131]
ds_read_b64_tr_b16 a[172:173], v47 offset:2048
ds_read_b64_tr_b16 a[174:175], v47 offset:2560
v_mfma_f32_16x16x32_bf16 v[132:135], a[24:27], a[4:7], v[132:135]
ds_read_b128 a[136:139], v43 offset:2048
v_lshlrev_b32 v249, 5, v252
v_mfma_f32_16x16x32_bf16 v[168:171], a[28:31], a[4:7], v[168:171]
ds_read_b64_tr_b16 a[192:193], v48 offset:2048
ds_read_b64_tr_b16 a[194:195], v48 offset:2560
v_mfma_f32_16x16x32_bf16 v[172:175], a[32:35], a[4:7], v[172:175]
buffer_load_dwordx4 v241, s[8:11], s45 offen offset:1024 lds
ds_read_b128 a[140:143], v43 offset:4096
v_mfma_f32_16x16x32_bf16 v[208:211], a[4:7], a[36:39], v[208:211]
ds_read_b64_tr_b16 a[196:197], v47 offset:4096
ds_read_b64_tr_b16 a[198:199], v47 offset:4608
v_mfma_f32_16x16x32_bf16 v[212:215], a[4:7], a[40:43], v[212:215]
ds_read_b128 a[160:163], v43 offset:6144
v_mfma_f32_16x16x32_bf16 v[136:139], a[20:23], a[8:11], v[136:139]
ds_read_b64_tr_b16 a[200:201], v48 offset:4096
ds_read_b64_tr_b16 a[202:203], v48 offset:4608
v_mfma_f32_16x16x32_bf16 v[140:143], a[24:27], a[8:11], v[140:143]
v_mfma_f32_16x16x32_bf16 v[176:179], a[28:31], a[8:11], v[176:179]
buffer_load_dwordx4 v242, s[8:11], s45 offen offset:2048 lds
v_mfma_f32_16x16x32_bf16 v[180:183], a[32:35], a[8:11], v[180:183]
v_mfma_f32_16x16x32_bf16 v[216:219], a[8:11], a[36:39], v[216:219]
v_mfma_f32_16x16x32_bf16 v[220:223], a[8:11], a[40:43], v[220:223]
v_mfma_f32_16x16x32_bf16 v[144:147], a[20:23], a[12:15], v[144:147]
v_mfma_f32_16x16x32_bf16 v[148:151], a[24:27], a[12:15], v[148:151]
buffer_load_dwordx4 v243, s[8:11], s45 offen offset:3072 lds
v_mfma_f32_16x16x32_bf16 v[184:187], a[28:31], a[12:15], v[184:187]
s_add_u32 s45, s45, 131072
v_mfma_f32_16x16x32_bf16 v[188:191], a[32:35], a[12:15], v[188:191]
v_mfma_f32_16x16x32_bf16 v[224:227], a[12:15], a[36:39], v[224:227]
v_mfma_f32_16x16x32_bf16 v[228:231], a[12:15], a[40:43], v[228:231]
s_add_u32 m0, s91, 32768
v_mfma_f32_16x16x32_bf16 v[152:155], a[20:23], a[16:19], v[152:155]
buffer_load_dwordx4 v7, s[4:7], s44 offen offset:0 sc1 lds
v_mfma_f32_16x16x32_bf16 v[156:159], a[24:27], a[16:19], v[156:159]
v_mfma_f32_16x16x32_bf16 v[192:195], a[28:31], a[16:19], v[192:195]
v_mfma_f32_16x16x32_bf16 v[196:199], a[32:35], a[16:19], v[196:199]
v_mfma_f32_16x16x32_bf16 v[232:235], a[16:19], a[36:39], v[232:235]
v_mfma_f32_16x16x32_bf16 v[236:239], a[16:19], a[40:43], v[236:239]
s_waitcnt lgkmcnt(0)
buffer_load_dwordx4 v7, s[4:7], s44 offen offset:1024 sc1 lds
v_mfma_f32_16x16x32_bf16 v[160:163], a[172:175], a[128:131], v[160:163]
ds_read_b64_tr_b16 a[20:21], v47 offset:14336
ds_read_b64_tr_b16 a[22:23], v47 offset:14848
v_mfma_f32_16x16x32_bf16 v[164:167], a[192:195], a[128:131], v[164:167]
ds_read_b128 a[0:3], v46
v_mfma_f32_16x16x32_bf16 v[200:203], a[128:131], a[196:199], v[200:203]
ds_read_b64_tr_b16 a[24:25], v48 offset:14336
ds_read_b64_tr_b16 a[26:27], v48 offset:14848
v_mfma_f32_16x16x32_bf16 v[204:207], a[128:131], a[200:203], v[204:207]
ds_read_b128 a[4:7], v44 offset:0
v_mfma_f32_16x16x32_bf16 v[128:131], a[164:167], a[132:135], v[128:131]
buffer_load_dwordx4 v7, s[4:7], s44 offen offset:2048 sc1 lds
ds_read_b64_tr_b16 a[28:29], v47 offset:16384
ds_read_b64_tr_b16 a[30:31], v47 offset:16896
v_mfma_f32_16x16x32_bf16 v[132:135], a[168:171], a[132:135], v[132:135]
ds_read_b128 a[8:11], v44 offset:2048
v_mfma_f32_16x16x32_bf16 v[168:171], a[172:175], a[132:135], v[168:171]
ds_read_b64_tr_b16 a[32:33], v48 offset:16384
ds_read_b64_tr_b16 a[34:35], v48 offset:16896
v_mfma_f32_16x16x32_bf16 v[172:175], a[192:195], a[132:135], v[172:175]
ds_read_b128 a[12:15], v44 offset:4096
v_mfma_f32_16x16x32_bf16 v[208:211], a[132:135], a[196:199], v[208:211]
ds_read_b64_tr_b16 a[36:37], v47 offset:18432
ds_read_b64_tr_b16 a[38:39], v47 offset:18944
v_mfma_f32_16x16x32_bf16 v[212:215], a[132:135], a[200:203], v[212:215]
buffer_load_dwordx4 v7, s[4:7], s44 offen offset:3072 sc1 lds
ds_read_b128 a[16:19], v44 offset:6144
v_mfma_f32_16x16x32_bf16 v[136:139], a[164:167], a[136:139], v[136:139]
s_add_u32 s44, s44, 4096
ds_read_b64_tr_b16 a[40:41], v48 offset:18432
ds_read_b64_tr_b16 a[42:43], v48 offset:18944
v_mfma_f32_16x16x32_bf16 v[140:143], a[168:171], a[136:139], v[140:143]
v_mfma_f32_16x16x32_bf16 v[176:179], a[172:175], a[136:139], v[176:179]
v_mfma_f32_16x16x32_bf16 v[180:183], a[192:195], a[136:139], v[180:183]
v_mfma_f32_16x16x32_bf16 v[216:219], a[136:139], a[196:199], v[216:219]
v_mfma_f32_16x16x32_bf16 v[220:223], a[136:139], a[200:203], v[220:223]
v_mfma_f32_16x16x32_bf16 v[144:147], a[164:167], a[140:143], v[144:147]
v_mfma_f32_16x16x32_bf16 v[148:151], a[168:171], a[140:143], v[148:151]
v_mfma_f32_16x16x32_bf16 v[184:187], a[172:175], a[140:143], v[184:187]
v_mfma_f32_16x16x32_bf16 v[188:191], a[192:195], a[140:143], v[188:191]
v_mfma_f32_16x16x32_bf16 v[224:227], a[140:143], a[196:199], v[224:227]
v_mfma_f32_16x16x32_bf16 v[228:231], a[140:143], a[200:203], v[228:231]
v_mfma_f32_16x16x32_bf16 v[152:155], a[164:167], a[160:163], v[152:155]
v_mfma_f32_16x16x32_bf16 v[156:159], a[168:171], a[160:163], v[156:159]
v_mfma_f32_16x16x32_bf16 v[192:195], a[172:175], a[160:163], v[192:195]
v_mfma_f32_16x16x32_bf16 v[196:199], a[192:195], a[160:163], v[196:199]
v_mfma_f32_16x16x32_bf16 v[232:235], a[160:163], a[196:199], v[232:235]
v_mfma_f32_16x16x32_bf16 v[236:239], a[160:163], a[200:203], v[236:239]
s_waitcnt vmcnt(16)
s_waitcnt lgkmcnt(0)
s_barrier
v_mfma_f32_16x16x32_bf16 v[160:163], a[28:31], a[0:3], v[160:163]
v_mfma_f32_16x16x32_bf16 v[164:167], a[32:35], a[0:3], v[164:167]
s_add_u32 m0, s91, 16384
ds_read_b64_tr_b16 a[80:81], v245 offset:0
ds_read_b64_tr_b16 a[82:83], v245 offset:4096
v_mfma_f32_16x16x32_bf16 v[200:203], a[0:3], a[36:39], v[200:203]
buffer_load_dwordx4 v240, s[8:11], s45 offen offset:0 lds
v_mfma_f32_16x16x32_bf16 v[204:207], a[0:3], a[40:43], v[204:207]
v_mfma_f32_16x16x32_bf16 v[128:131], a[20:23], a[4:7], v[128:131]
ds_read_b128 a[64:67], v244 offset:0
ds_read_b128 a[68:71], v244 offset:1024
v_mfma_f32_16x16x32_bf16 v[132:135], a[24:27], a[4:7], v[132:135]
buffer_load_dwordx4 v241, s[8:11], s45 offen offset:1024 lds
v_mfma_f32_16x16x32_bf16 v[168:171], a[28:31], a[4:7], v[168:171]
v_mfma_f32_16x16x32_bf16 v[172:175], a[32:35], a[4:7], v[172:175]
ds_read_b64_tr_b16 a[84:85], v246 offset:0
ds_read_b64_tr_b16 a[86:87], v246 offset:4096
v_mfma_f32_16x16x32_bf16 v[208:211], a[4:7], a[36:39], v[208:211]
buffer_load_dwordx4 v242, s[8:11], s45 offen offset:2048 lds
v_mfma_f32_16x16x32_bf16 v[212:215], a[4:7], a[40:43], v[212:215]
v_mfma_f32_16x16x32_bf16 v[136:139], a[20:23], a[8:11], v[136:139]
ds_read_b64_tr_b16 a[88:89], v247 offset:0
ds_read_b64_tr_b16 a[90:91], v247 offset:4096
v_mfma_f32_16x16x32_bf16 v[140:143], a[24:27], a[8:11], v[140:143]
buffer_load_dwordx4 v243, s[8:11], s45 offen offset:3072 lds
v_mfma_f32_16x16x32_bf16 v[176:179], a[28:31], a[8:11], v[176:179]
s_add_u32 s45, s45, 131072
v_mfma_f32_16x16x32_bf16 v[180:183], a[32:35], a[8:11], v[180:183]
s_add_u32 m0, s91, 0
ds_read_b128 a[72:75], v244 offset:4096
ds_read_b128 a[76:79], v244 offset:5120
v_mfma_f32_16x16x32_bf16 v[216:219], a[8:11], a[36:39], v[216:219]
buffer_load_dwordx4 v7, s[4:7], s44 offen offset:0 sc1 lds
v_mfma_f32_16x16x32_bf16 v[220:223], a[8:11], a[40:43], v[220:223]
v_mfma_f32_16x16x32_bf16 v[144:147], a[20:23], a[12:15], v[144:147]
ds_read_b64_tr_b16 a[92:93], v248 offset:0
ds_read_b64_tr_b16 a[94:95], v248 offset:4096
v_mfma_f32_16x16x32_bf16 v[148:151], a[24:27], a[12:15], v[148:151]
buffer_load_dwordx4 v7, s[4:7], s44 offen offset:1024 sc1 lds
v_mfma_f32_16x16x32_bf16 v[184:187], a[28:31], a[12:15], v[184:187]
v_mfma_f32_16x16x32_bf16 v[188:191], a[32:35], a[12:15], v[188:191]
v_mfma_f32_16x16x32_bf16 v[224:227], a[12:15], a[36:39], v[224:227]
buffer_load_dwordx4 v7, s[4:7], s44 offen offset:2048 sc1 lds
v_mfma_f32_16x16x32_bf16 v[228:231], a[12:15], a[40:43], v[228:231]
v_mfma_f32_16x16x32_bf16 v[152:155], a[20:23], a[16:19], v[152:155]
v_mfma_f32_16x16x32_bf16 v[156:159], a[24:27], a[16:19], v[156:159]
buffer_load_dwordx4 v7, s[4:7], s44 offen offset:3072 sc1 lds
v_mfma_f32_16x16x32_bf16 v[192:195], a[28:31], a[16:19], v[192:195]
s_add_u32 s44, s44, 4096
v_mfma_f32_16x16x32_bf16 v[196:199], a[32:35], a[16:19], v[196:199]
v_mfma_f32_16x16x32_bf16 v[232:235], a[16:19], a[36:39], v[232:235]
v_mfma_f32_16x16x32_bf16 v[236:239], a[16:19], a[40:43], v[236:239]
s_mov_b32 s39, 1048744942
s_waitcnt lgkmcnt(0)
v_mfma_f32_16x16x32_bf16 v[64:67], a[80:83], a[64:67], 0
s_add_u32 m0, s91, 147456
ds_read_b64_tr_b16 a[112:113], v245 offset:8192
ds_read_b64_tr_b16 a[114:115], v245 offset:12288
v_mfma_f32_16x16x32_bf16 v[68:71], a[84:87], a[64:67], 0
buffer_load_dwordx4 v240, s[8:11], s45 offen offset:0 lds
v_mfma_f32_16x16x32_bf16 v[72:75], a[88:91], a[64:67], 0
ds_read_b128 a[96:99], v244 offset:2048
ds_read_b128 a[100:103], v244 offset:3072
v_mfma_f32_16x16x32_bf16 v[76:79], a[92:95], a[64:67], 0
buffer_load_dwordx4 v241, s[8:11], s45 offen offset:1024 lds
ds_read_b64_tr_b16 a[116:117], v246 offset:8192
ds_read_b64_tr_b16 a[118:119], v246 offset:12288
v_mfma_f32_16x16x32_bf16 v[80:83], a[80:83], a[68:71], 0
v_cvt_pk_bf16_f32 v160, v160, v161
v_cvt_pk_bf16_f32 v161, v162, v163
v_cvt_pk_bf16_f32 v162, v164, v165
v_cvt_pk_bf16_f32 v163, v166, v167
v_mfma_f32_16x16x32_bf16 v[84:87], a[84:87], a[68:71], 0
buffer_load_dwordx4 v242, s[8:11], s45 offen offset:2048 lds
ds_read_b64_tr_b16 a[120:121], v247 offset:8192
.Lpad18:
s_cbranch_execz .Ltch9
ds_read_b64_tr_b16 a[122:123], v247 offset:12288
v_mfma_f32_16x16x32_bf16 v[88:91], a[88:91], a[68:71], 0
ds_read_b128 a[104:107], v244 offset:6144
ds_read_b128 a[108:111], v244 offset:7168
v_cvt_pk_bf16_f32 v36, v200, v201
v_cvt_pk_bf16_f32 v37, v202, v203
v_mfma_f32_16x16x32_bf16 v[92:95], a[92:95], a[68:71], 0
buffer_load_dwordx4 v243, s[8:11], s45 offen offset:3072 lds
v_cvt_pk_bf16_f32 v46, v204, v205
v_cvt_pk_bf16_f32 v47, v206, v207
v_cvt_pk_bf16_f32 v168, v168, v169
v_mfma_f32_16x16x32_bf16 v[96:99], a[80:83], a[72:75], 0
s_cmp_eq_u32 s46, 0
s_cselect_b32 s53, s56, s57
s_add_u32 s45, s45, 131072
s_add_u32 m0, s91, 131072
ds_read_b64_tr_b16 a[124:125], v248 offset:8192
ds_read_b64_tr_b16 a[126:127], v248 offset:12288
v_mfma_f32_16x16x32_bf16 v[100:103], a[84:87], a[72:75], 0
buffer_load_dwordx4 v7, s[4:7], s44 offen offset:0 sc1 lds
v_cvt_pk_bf16_f32 v169, v170, v171
v_cvt_pk_bf16_f32 v170, v172, v173
v_cvt_pk_bf16_f32 v171, v174, v175
v_mfma_f32_16x16x32_bf16 v[104:107], a[88:91], a[72:75], 0
s_add_u32 s49, s46, s91
s_add_u32 s46, s46, s53
v_add_u32 v244, s53, v244
v_cvt_pk_bf16_f32 v128, v128, v129
v_cvt_pk_bf16_f32 v129, v130, v131
v_mfma_f32_16x16x32_bf16 v[108:111], a[92:95], a[72:75], 0
buffer_load_dwordx4 v7, s[4:7], s44 offen offset:1024 sc1 lds
v_cvt_pk_bf16_f32 v130, v132, v133
v_cvt_pk_bf16_f32 v131, v134, v135
v_cvt_pk_bf16_f32 v38, v208, v209
v_mfma_f32_16x16x32_bf16 v[112:115], a[80:83], a[76:79], 0
v_add_u32 v245, s53, v245
v_add_u32 v246, s53, v246
v_cvt_pk_bf16_f32 v39, v210, v211
v_cvt_pk_bf16_f32 v48, v212, v213
v_mfma_f32_16x16x32_bf16 v[116:119], a[84:87], a[76:79], 0
buffer_load_dwordx4 v7, s[4:7], s44 offen offset:2048 sc1 lds
v_cvt_pk_bf16_f32 v49, v214, v215
v_cvt_pk_bf16_f32 v176, v176, v177
v_cvt_pk_bf16_f32 v177, v178, v179
v_mfma_f32_16x16x32_bf16 v[120:123], a[88:91], a[76:79], 0
buffer_load_dwordx4 v7, s[4:7], s44 offen offset:3072 sc1 lds
v_add_u32 v247, s53, v247
v_add_u32 v248, s53, v248
v_cvt_pk_bf16_f32 v178, v180, v181
v_mfma_f32_16x16x32_bf16 v[124:127], a[92:95], a[76:79], 0
s_waitcnt vmcnt(24) lgkmcnt(0)
s_barrier
v_cvt_pk_bf16_f32 v179, v182, v183
v_cvt_pk_bf16_f32 v136, v136, v137
v_cvt_pk_bf16_f32 v137, v138, v139
v_mfma_f32_16x16x32_bf16 v[64:67], a[112:115], a[96:99], v[64:67]
ds_read_b64_tr_b16 a[80:81], v245 offset:0
ds_read_b64_tr_b16 a[82:83], v245 offset:4096
v_cvt_pk_bf16_f32 v138, v140, v141
v_cvt_pk_bf16_f32 v139, v142, v143
v_mfma_f32_16x16x32_bf16 v[68:71], a[116:119], a[96:99], v[68:71]
s_add_u32 m0, s49, 16384
ds_read_b128 a[64:67], v244 offset:0
v_cvt_pk_bf16_f32 v40, v216, v217
v_cvt_pk_bf16_f32 v41, v218, v219
v_mfma_f32_16x16x32_bf16 v[72:75], a[120:123], a[96:99], v[72:75]
buffer_load_dwordx4 v240, s[8:11], s45 offen offset:0 lds
v_cvt_pk_bf16_f32 v50, v220, v221
v_cvt_pk_bf16_f32 v51, v222, v223
v_cvt_pk_bf16_f32 v184, v184, v185
v_mfma_f32_16x16x32_bf16 v[76:79], a[124:127], a[96:99], v[76:79]
ds_read_b64_tr_b16 a[84:85], v246 offset:0
ds_read_b64_tr_b16 a[86:87], v246 offset:4096
v_cvt_pk_bf16_f32 v185, v186, v187
v_cvt_pk_bf16_f32 v186, v188, v189
v_mfma_f32_16x16x32_bf16 v[80:83], a[112:115], a[100:103], v[80:83]
s_add_u32 s44, s44, 4096
ds_read_b128 a[68:71], v244 offset:1024
v_cvt_pk_bf16_f32 v187, v190, v191
v_cvt_pk_bf16_f32 v144, v144, v145
v_mfma_f32_16x16x32_bf16 v[84:87], a[116:119], a[100:103], v[84:87]
ds_read_b64_tr_b16 a[88:89], v247 offset:0
ds_read_b64_tr_b16 a[90:91], v247 offset:4096
v_cvt_pk_bf16_f32 v145, v146, v147
v_cvt_pk_bf16_f32 v146, v148, v149
v_mfma_f32_16x16x32_bf16 v[88:91], a[120:123], a[100:103], v[88:91]
buffer_load_dwordx4 v241, s[8:11], s45 offen offset:1024 lds
v_cvt_pk_bf16_f32 v147, v150, v151
v_cvt_pk_bf16_f32 v42, v224, v225
v_cvt_pk_bf16_f32 v43, v226, v227
v_mfma_f32_16x16x32_bf16 v[92:95], a[124:127], a[100:103], v[92:95]
ds_read_b128 a[72:75], v244 offset:4096
v_cvt_pk_bf16_f32 v52, v228, v229
v_cvt_pk_bf16_f32 v53, v230, v231
v_cvt_pk_bf16_f32 v192, v192, v193
v_mfma_f32_16x16x32_bf16 v[96:99], a[112:115], a[104:107], v[96:99]
ds_read_b64_tr_b16 a[92:93], v248 offset:0
ds_read_b64_tr_b16 a[94:95], v248 offset:4096
v_cvt_pk_bf16_f32 v193, v194, v195
v_cvt_pk_bf16_f32 v194, v196, v197
v_mfma_f32_16x16x32_bf16 v[100:103], a[116:119], a[104:107], v[100:103]
ds_read_b128 a[76:79], v244 offset:5120
v_cvt_pk_bf16_f32 v195, v198, v199
v_cvt_pk_bf16_f32 v152, v152, v153
v_cvt_pk_bf16_f32 v153, v154, v155
v_mfma_f32_16x16x32_bf16 v[104:107], a[120:123], a[104:107], v[104:107]
buffer_load_dwordx4 v242, s[8:11], s45 offen offset:2048 lds
v_cvt_pk_bf16_f32 v154, v156, v157
v_cvt_pk_bf16_f32 v155, v158, v159
v_cvt_pk_bf16_f32 v44, v232, v233
v_mfma_f32_16x16x32_bf16 v[108:111], a[124:127], a[104:107], v[108:111]
s_cmp_eq_u32 s46, 0
s_cselect_b32 s53, s56, s57
v_cvt_pk_bf16_f32 v45, v234, v235
v_cvt_pk_bf16_f32 v54, v236, v237
v_cvt_pk_bf16_f32 v55, v238, v239
v_mfma_f32_16x16x32_bf16 v[200:203], v[160:163], v[128:131], v[24:27]
v_mfma_f32_16x16x32_bf16 v[112:115], a[112:115], a[108:111], v[112:115]
v_mfma_f32_16x16x32_bf16 v[204:207], v[168:171], v[128:131], v[20:23]
v_mfma_f32_16x16x32_bf16 v[116:119], a[116:119], a[108:111], v[116:119]
v_mfma_f32_16x16x32_bf16 v[208:211], v[168:171], v[136:139], v[16:19]
v_mfma_f32_16x16x32_bf16 v[120:123], a[120:123], a[108:111], v[120:123]
buffer_load_dwordx4 v243, s[8:11], s45 offen offset:3072 lds
v_mfma_f32_16x16x32_bf16 v[212:215], v[176:179], v[136:139], v[20:23]
v_mfma_f32_16x16x32_bf16 v[124:127], a[124:127], a[108:111], v[124:127]
s_waitcnt lgkmcnt(0)
s_mov_b32 m0, s49
s_add_u32 s45, s45, 131072
v_mfma_f32_16x16x32_bf16 v[216:219], v[176:179], v[144:147], v[16:19]
v_mfma_f32_16x16x32_bf16 v[64:67], a[80:83], a[64:67], v[64:67]
ds_read_b64_tr_b16 a[112:113], v245 offset:8192
ds_read_b64_tr_b16 a[114:115], v245 offset:12288
v_mfma_f32_16x16x32_bf16 v[220:223], v[184:187], v[144:147], v[20:23]
v_mfma_f32_16x16x32_bf16 v[68:71], a[84:87], a[64:67], v[68:71]
ds_read_b128 a[96:99], v244 offset:2048
v_add_u32 v245, s53, v245
v_mfma_f32_16x16x32_bf16 v[224:227], v[184:187], v[152:155], v[16:19]
v_mfma_f32_16x16x32_bf16 v[72:75], a[88:91], a[64:67], v[72:75]
buffer_load_dwordx4 v7, s[4:7], s44 offen offset:0 sc1 lds
v_mfma_f32_16x16x32_bf16 v[228:231], v[192:195], v[152:155], v[20:23]
v_mfma_f32_16x16x32_bf16 v[76:79], a[92:95], a[64:67], v[76:79]
ds_read_b64_tr_b16 a[116:117], v246 offset:8192
ds_read_b64_tr_b16 a[118:119], v246 offset:12288
v_max3_f32 v56, v200, v201, v202
v_max3_f32 v58, v208, v209, v210
v_mfma_f32_16x16x32_bf16 v[80:83], a[80:83], a[68:71], v[80:83]
ds_read_b128 a[100:103], v244 offset:3072
v_add_u32 v246, s53, v246
v_max3_f32 v60, v216, v217, v218
v_max3_f32 v62, v224, v225, v226
v_mfma_f32_16x16x32_bf16 v[84:87], a[84:87], a[68:71], v[84:87]
ds_read_b64_tr_b16 a[120:121], v247 offset:8192
ds_read_b64_tr_b16 a[122:123], v247 offset:12288
v_max3_f32 v57, v203, v204, v205
v_max3_f32 v59, v211, v212, v213
v_mfma_f32_16x16x32_bf16 v[88:91], a[88:91], a[68:71], v[88:91]
buffer_load_dwordx4 v7, s[4:7], s44 offen offset:1024 sc1 lds
v_max3_f32 v61, v219, v220, v221
v_max3_f32 v63, v227, v228, v229
v_max3_f32 v56, v56, v206, v207
v_mfma_f32_16x16x32_bf16 v[92:95], a[92:95], a[68:71], v[92:95]
ds_read_b128 a[104:107], v244 offset:6144
v_add_u32 v247, s53, v247
v_max3_f32 v58, v58, v214, v215
v_max3_f32 v60, v60, v222, v223
v_mfma_f32_16x16x32_bf16 v[96:99], a[80:83], a[72:75], v[96:99]
ds_read_b64_tr_b16 a[124:125], v248 offset:8192
ds_read_b64_tr_b16 a[126:127], v248 offset:12288
v_max3_f32 v62, v62, v230, v231
v_max_f32 v56, v56, v57
v_mfma_f32_16x16x32_bf16 v[100:103], a[84:87], a[72:75], v[100:103]
ds_read_b128 a[108:111], v244 offset:7168
v_add_u32 v248, s53, v248
v_max_f32 v58, v58, v59
v_max_f32 v60, v60, v61
v_mfma_f32_16x16x32_bf16 v[104:107], a[88:91], a[72:75], v[104:107]
buffer_load_dwordx4 v7, s[4:7], s44 offen offset:2048 sc1 lds
v_max_f32 v62, v62, v63
v_mov_b32 v57, v56
v_mov_b32 v59, v58
v_mfma_f32_16x16x32_bf16 v[108:111], a[92:95], a[72:75], v[108:111]
s_add_u32 s49, s46, s91
v_add_u32 v244, s53, v244
v_mov_b32 v61, v60
v_mov_b32 v63, v62
v_mfma_f32_16x16x32_bf16 v[112:115], a[80:83], a[76:79], v[112:115]
s_add_u32 s46, s46, s53
v_permlane16_swap_b32 v56, v57
v_permlane16_swap_b32 v58, v59
v_mfma_f32_16x16x32_bf16 v[116:119], a[84:87], a[76:79], v[116:119]
v_permlane16_swap_b32 v60, v61
v_permlane16_swap_b32 v62, v63
v_mfma_f32_16x16x32_bf16 v[120:123], a[88:91], a[76:79], v[120:123]
buffer_load_dwordx4 v7, s[4:7], s44 offen offset:3072 sc1 lds
v_max_f32 v56, v56, v57
v_max_f32 v58, v58, v59
v_max_f32 v60, v60, v61
v_mfma_f32_16x16x32_bf16 v[124:127], a[92:95], a[76:79], v[124:127]
s_waitcnt vmcnt(24) lgkmcnt(0)
s_barrier
v_max_f32 v62, v62, v63
v_mov_b32 v57, v56
v_mov_b32 v59, v58
v_mfma_f32_16x16x32_bf16 v[64:67], a[112:115], a[96:99], v[64:67]
ds_read_b64_tr_b16 a[80:81], v245 offset:0
ds_read_b64_tr_b16 a[82:83], v245 offset:4096
v_mov_b32 v61, v60
v_mov_b32 v63, v62
v_mfma_f32_16x16x32_bf16 v[68:71], a[116:119], a[96:99], v[68:71]
s_add_u32 m0, s49, 16384
ds_read_b128 a[64:67], v244 offset:0
v_permlane32_swap_b32 v56, v57
v_mfma_f32_16x16x32_bf16 v[72:75], a[120:123], a[96:99], v[72:75]
buffer_load_dwordx4 v240, s[8:11], s45 offen offset:0 lds
v_permlane32_swap_b32 v58, v59
v_mfma_f32_16x16x32_bf16 v[76:79], a[124:127], a[96:99], v[76:79]
ds_read_b64_tr_b16 a[84:85], v246 offset:0
ds_read_b64_tr_b16 a[86:87], v246 offset:4096
v_permlane32_swap_b32 v60, v61
v_mfma_f32_16x16x32_bf16 v[80:83], a[112:115], a[100:103], v[80:83]
s_add_u32 s44, s44, 4096
ds_read_b128 a[68:71], v244 offset:1024
v_permlane32_swap_b32 v62, v63
v_mfma_f32_16x16x32_bf16 v[84:87], a[116:119], a[100:103], v[84:87]
ds_read_b64_tr_b16 a[88:89], v247 offset:0
ds_read_b64_tr_b16 a[90:91], v247 offset:4096
v_max_f32 v56, v56, v57
v_max_f32 v58, v58, v59
v_mfma_f32_16x16x32_bf16 v[88:91], a[120:123], a[100:103], v[88:91]
buffer_load_dwordx4 v241, s[8:11], s45 offen offset:1024 lds
v_max_f32 v60, v60, v61
v_max_f32 v62, v62, v63
v_mul_f32 v56, s39, v56
v_mfma_f32_16x16x32_bf16 v[92:95], a[124:127], a[100:103], v[92:95]
ds_read_b128 a[72:75], v244 offset:4096
v_mul_f32 v58, s39, v58
v_mul_f32 v60, s39, v60
v_mul_f32 v62, s39, v62
v_mfma_f32_16x16x32_bf16 v[96:99], a[112:115], a[104:107], v[96:99]
ds_read_b64_tr_b16 a[92:93], v248 offset:0
ds_read_b64_tr_b16 a[94:95], v248 offset:4096
v_fma_f32 v200, v200, s39, -v56
v_fma_f32 v208, v208, s39, -v58
v_mfma_f32_16x16x32_bf16 v[100:103], a[116:119], a[104:107], v[100:103]
ds_read_b128 a[76:79], v244 offset:5120
v_fma_f32 v216, v216, s39, -v60
v_fma_f32 v224, v224, s39, -v62
v_fma_f32 v201, v201, s39, -v56
v_mfma_f32_16x16x32_bf16 v[104:107], a[120:123], a[104:107], v[104:107]
buffer_load_dwordx4 v242, s[8:11], s45 offen offset:2048 lds
v_fma_f32 v209, v209, s39, -v58
v_fma_f32 v217, v217, s39, -v60
v_fma_f32 v225, v225, s39, -v62
v_mfma_f32_16x16x32_bf16 v[108:111], a[124:127], a[104:107], v[108:111]
s_cmp_eq_u32 s46, 0
s_cselect_b32 s53, s56, s57
v_fma_f32 v202, v202, s39, -v56
v_fma_f32 v210, v210, s39, -v58
v_fma_f32 v218, v218, s39, -v60
v_mfma_f32_16x16x32_bf16 v[112:115], a[112:115], a[108:111], v[112:115]
v_fma_f32 v226, v226, s39, -v62
v_fma_f32 v203, v203, s39, -v56
v_fma_f32 v211, v211, s39, -v58
v_fma_f32 v219, v219, s39, -v60
v_mfma_f32_16x16x32_bf16 v[116:119], a[116:119], a[108:111], v[116:119]
v_fma_f32 v227, v227, s39, -v62
v_fma_f32 v204, v204, s39, -v56
v_fma_f32 v212, v212, s39, -v58
v_fma_f32 v220, v220, s39, -v60
v_mfma_f32_16x16x32_bf16 v[120:123], a[120:123], a[108:111], v[120:123]
buffer_load_dwordx4 v243, s[8:11], s45 offen offset:3072 lds
v_fma_f32 v228, v228, s39, -v62
v_fma_f32 v205, v205, s39, -v56
v_fma_f32 v213, v213, s39, -v58
v_mfma_f32_16x16x32_bf16 v[124:127], a[124:127], a[108:111], v[124:127]
s_waitcnt lgkmcnt(0)
s_mov_b32 m0, s49
s_add_u32 s45, s45, 131072
v_fma_f32 v221, v221, s39, -v60
v_fma_f32 v229, v229, s39, -v62
v_mfma_f32_16x16x32_bf16 v[64:67], a[80:83], a[64:67], v[64:67]
ds_read_b64_tr_b16 a[112:113], v245 offset:8192
ds_read_b64_tr_b16 a[114:115], v245 offset:12288
v_fma_f32 v206, v206, s39, -v56
v_fma_f32 v214, v214, s39, -v58
v_mfma_f32_16x16x32_bf16 v[68:71], a[84:87], a[64:67], v[68:71]
ds_read_b128 a[96:99], v244 offset:2048
v_add_u32 v245, s53, v245
v_fma_f32 v222, v222, s39, -v60
v_fma_f32 v230, v230, s39, -v62
v_mfma_f32_16x16x32_bf16 v[72:75], a[88:91], a[64:67], v[72:75]
buffer_load_dwordx4 v7, s[4:7], s44 offen offset:0 sc1 lds
v_fma_f32 v207, v207, s39, -v56
v_fma_f32 v215, v215, s39, -v58
v_fma_f32 v223, v223, s39, -v60
v_mfma_f32_16x16x32_bf16 v[76:79], a[92:95], a[64:67], v[76:79]
ds_read_b64_tr_b16 a[116:117], v246 offset:8192
ds_read_b64_tr_b16 a[118:119], v246 offset:12288
v_fma_f32 v231, v231, s39, -v62
v_mfma_f32_16x16x32_bf16 v[80:83], a[80:83], a[68:71], v[80:83]
ds_read_b128 a[100:103], v244 offset:3072
v_add_u32 v246, s53, v246
v_exp_f32 v200, v200
v_mfma_f32_16x16x32_bf16 v[84:87], a[84:87], a[68:71], v[84:87]
ds_read_b64_tr_b16 a[120:121], v247 offset:8192
ds_read_b64_tr_b16 a[122:123], v247 offset:12288
v_exp_f32 v208, v208
v_mfma_f32_16x16x32_bf16 v[88:91], a[88:91], a[68:71], v[88:91]
buffer_load_dwordx4 v7, s[4:7], s44 offen offset:1024 sc1 lds
v_exp_f32 v216, v216
v_mfma_f32_16x16x32_bf16 v[92:95], a[92:95], a[68:71], v[92:95]
ds_read_b128 a[104:107], v244 offset:6144
v_add_u32 v247, s53, v247
v_exp_f32 v224, v224
v_mfma_f32_16x16x32_bf16 v[96:99], a[80:83], a[72:75], v[96:99]
ds_read_b64_tr_b16 a[124:125], v248 offset:8192
ds_read_b64_tr_b16 a[126:127], v248 offset:12288
v_exp_f32 v201, v201
v_mfma_f32_16x16x32_bf16 v[100:103], a[84:87], a[72:75], v[100:103]
ds_read_b128 a[108:111], v244 offset:7168
v_add_u32 v248, s53, v248
v_exp_f32 v209, v209
v_mfma_f32_16x16x32_bf16 v[104:107], a[88:91], a[72:75], v[104:107]
buffer_load_dwordx4 v7, s[4:7], s44 offen offset:2048 sc1 lds
v_exp_f32 v217, v217
v_mfma_f32_16x16x32_bf16 v[108:111], a[92:95], a[72:75], v[108:111]
s_add_u32 s49, s46, s91
v_add_u32 v244, s53, v244
v_exp_f32 v225, v225
v_mfma_f32_16x16x32_bf16 v[112:115], a[80:83], a[76:79], v[112:115]
s_add_u32 s46, s46, s53
v_exp_f32 v202, v202
v_exp_f32 v210, v210
v_mfma_f32_16x16x32_bf16 v[116:119], a[84:87], a[76:79], v[116:119]
v_exp_f32 v218, v218
v_exp_f32 v226, v226
v_mfma_f32_16x16x32_bf16 v[120:123], a[88:91], a[76:79], v[120:123]
buffer_load_dwordx4 v7, s[4:7], s44 offen offset:3072 sc1 lds
v_exp_f32 v203, v203
v_mfma_f32_16x16x32_bf16 v[124:127], a[92:95], a[76:79], v[124:127]
s_waitcnt vmcnt(24) lgkmcnt(0)
s_barrier
v_exp_f32 v211, v211
v_mfma_f32_16x16x32_bf16 v[64:67], a[112:115], a[96:99], v[64:67]
ds_read_b64_tr_b16 a[80:81], v245 offset:0
ds_read_b64_tr_b16 a[82:83], v245 offset:4096
v_exp_f32 v219, v219
v_mfma_f32_16x16x32_bf16 v[68:71], a[116:119], a[96:99], v[68:71]
s_add_u32 m0, s49, 16384
ds_read_b128 a[64:67], v244 offset:0
v_exp_f32 v227, v227
v_mfma_f32_16x16x32_bf16 v[72:75], a[120:123], a[96:99], v[72:75]
buffer_load_dwordx4 v240, s[8:11], s45 offen offset:0 lds
v_exp_f32 v204, v204
v_mfma_f32_16x16x32_bf16 v[76:79], a[124:127], a[96:99], v[76:79]
ds_read_b64_tr_b16 a[84:85], v246 offset:0
ds_read_b64_tr_b16 a[86:87], v246 offset:4096
v_exp_f32 v212, v212
v_mfma_f32_16x16x32_bf16 v[80:83], a[112:115], a[100:103], v[80:83]
s_add_u32 s44, s44, 4096
ds_read_b128 a[68:71], v244 offset:1024
v_exp_f32 v220, v220
v_mfma_f32_16x16x32_bf16 v[84:87], a[116:119], a[100:103], v[84:87]
ds_read_b64_tr_b16 a[88:89], v247 offset:0
ds_read_b64_tr_b16 a[90:91], v247 offset:4096
v_exp_f32 v228, v228
v_mfma_f32_16x16x32_bf16 v[88:91], a[120:123], a[100:103], v[88:91]
buffer_load_dwordx4 v241, s[8:11], s45 offen offset:1024 lds
v_exp_f32 v205, v205
v_mfma_f32_16x16x32_bf16 v[92:95], a[124:127], a[100:103], v[92:95]
ds_read_b128 a[72:75], v244 offset:4096
v_exp_f32 v213, v213
v_mfma_f32_16x16x32_bf16 v[96:99], a[112:115], a[104:107], v[96:99]
ds_read_b64_tr_b16 a[92:93], v248 offset:0
ds_read_b64_tr_b16 a[94:95], v248 offset:4096
v_exp_f32 v221, v221
v_mfma_f32_16x16x32_bf16 v[100:103], a[116:119], a[104:107], v[100:103]
ds_read_b128 a[76:79], v244 offset:5120
v_exp_f32 v229, v229
v_mfma_f32_16x16x32_bf16 v[104:107], a[120:123], a[104:107], v[104:107]
buffer_load_dwordx4 v242, s[8:11], s45 offen offset:2048 lds
v_exp_f32 v206, v206
v_mfma_f32_16x16x32_bf16 v[108:111], a[124:127], a[104:107], v[108:111]
s_cmp_eq_u32 s46, 0
s_cselect_b32 s53, s56, s57
v_exp_f32 v214, v214
v_mfma_f32_16x16x32_bf16 v[112:115], a[112:115], a[108:111], v[112:115]
v_exp_f32 v222, v222
v_exp_f32 v230, v230
v_mfma_f32_16x16x32_bf16 v[116:119], a[116:119], a[108:111], v[116:119]
v_exp_f32 v207, v207
v_exp_f32 v215, v215
v_mfma_f32_16x16x32_bf16 v[120:123], a[120:123], a[108:111], v[120:123]
buffer_load_dwordx4 v243, s[8:11], s45 offen offset:3072 lds
v_exp_f32 v223, v223
v_mfma_f32_16x16x32_bf16 v[124:127], a[124:127], a[108:111], v[124:127]
s_waitcnt lgkmcnt(0)
s_mov_b32 m0, s49
s_add_u32 s45, s45, 131072
v_exp_f32 v231, v231
v_mfma_f32_16x16x32_bf16 v[64:67], a[80:83], a[64:67], v[64:67]
ds_read_b64_tr_b16 a[112:113], v245 offset:8192
ds_read_b64_tr_b16 a[114:115], v245 offset:12288
v_cvt_pk_bf16_f32 v132, v200, v201
v_cvt_pk_bf16_f32 v140, v208, v209
v_mfma_f32_16x16x32_bf16 v[68:71], a[84:87], a[64:67], v[68:71]
ds_read_b128 a[96:99], v244 offset:2048
v_add_u32 v245, s53, v245
v_cvt_pk_bf16_f32 v148, v216, v217
v_cvt_pk_bf16_f32 v156, v224, v225
v_mfma_f32_16x16x32_bf16 v[72:75], a[88:91], a[64:67], v[72:75]
buffer_load_dwordx4 v7, s[4:7], s44 offen offset:0 sc1 lds
v_cvt_pk_bf16_f32 v133, v202, v203
v_cvt_pk_bf16_f32 v141, v210, v211
v_cvt_pk_bf16_f32 v149, v218, v219
v_mfma_f32_16x16x32_bf16 v[76:79], a[92:95], a[64:67], v[76:79]
ds_read_b64_tr_b16 a[116:117], v246 offset:8192
ds_read_b64_tr_b16 a[118:119], v246 offset:12288
v_cvt_pk_bf16_f32 v157, v226, v227
v_cvt_pk_bf16_f32 v134, v204, v205
v_mfma_f32_16x16x32_bf16 v[80:83], a[80:83], a[68:71], v[80:83]
ds_read_b128 a[100:103], v244 offset:3072
v_add_u32 v246, s53, v246
v_cvt_pk_bf16_f32 v142, v212, v213
v_cvt_pk_bf16_f32 v150, v220, v221
v_mfma_f32_16x16x32_bf16 v[84:87], a[84:87], a[68:71], v[84:87]
ds_read_b64_tr_b16 a[120:121], v247 offset:8192
ds_read_b64_tr_b16 a[122:123], v247 offset:12288
v_cvt_pk_bf16_f32 v158, v228, v229
v_cvt_pk_bf16_f32 v135, v206, v207
v_mfma_f32_16x16x32_bf16 v[88:91], a[88:91], a[68:71], v[88:91]
buffer_load_dwordx4 v7, s[4:7], s44 offen offset:1024 sc1 lds
v_cvt_pk_bf16_f32 v143, v214, v215
v_cvt_pk_bf16_f32 v151, v222, v223
v_cvt_pk_bf16_f32 v159, v230, v231
v_mfma_f32_16x16x32_bf16 v[92:95], a[92:95], a[68:71], v[92:95]
ds_read_b128 a[104:107], v244 offset:6144
v_add_u32 v247, s53, v247
v_add_f32 v56, v200, v201
v_add_f32 v58, v208, v209
v_mfma_f32_16x16x32_bf16 v[96:99], a[80:83], a[72:75], v[96:99]
ds_read_b64_tr_b16 a[124:125], v248 offset:8192
ds_read_b64_tr_b16 a[126:127], v248 offset:12288
v_add_f32 v60, v216, v217
v_add_f32 v62, v224, v225
v_mfma_f32_16x16x32_bf16 v[160:163], v[36:39], v[132:135], 0
v_mfma_f32_16x16x32_bf16 v[100:103], a[84:87], a[72:75], v[100:103]
ds_read_b128 a[108:111], v244 offset:7168
v_add_u32 v248, s53, v248
v_mfma_f32_16x16x32_bf16 v[164:167], v[46:49], v[132:135], 0
v_mfma_f32_16x16x32_bf16 v[104:107], a[88:91], a[72:75], v[104:107]
buffer_load_dwordx4 v7, s[4:7], s44 offen offset:2048 sc1 lds
v_mfma_f32_16x16x32_bf16 v[168:171], v[38:41], v[140:143], 0
v_mfma_f32_16x16x32_bf16 v[108:111], a[92:95], a[72:75], v[108:111]
s_add_u32 s49, s46, s91
v_add_u32 v244, s53, v244
v_mfma_f32_16x16x32_bf16 v[172:175], v[48:51], v[140:143], 0
v_mfma_f32_16x16x32_bf16 v[112:115], a[80:83], a[76:79], v[112:115]
s_add_u32 s46, s46, s53
v_mfma_f32_16x16x32_bf16 v[176:179], v[40:43], v[148:151], 0
v_mfma_f32_16x16x32_bf16 v[116:119], a[84:87], a[76:79], v[116:119]
v_mfma_f32_16x16x32_bf16 v[180:183], v[50:53], v[148:151], 0
v_mfma_f32_16x16x32_bf16 v[120:123], a[88:91], a[76:79], v[120:123]
buffer_load_dwordx4 v7, s[4:7], s44 offen offset:3072 sc1 lds
v_mfma_f32_16x16x32_bf16 v[184:187], v[42:45], v[156:159], 0
v_mfma_f32_16x16x32_bf16 v[124:127], a[92:95], a[76:79], v[124:127]
s_waitcnt vmcnt(24) lgkmcnt(0)
s_barrier
v_mfma_f32_16x16x32_bf16 v[188:191], v[52:55], v[156:159], 0
v_mfma_f32_16x16x32_bf16 v[64:67], a[112:115], a[96:99], v[64:67]
ds_read_b64_tr_b16 a[80:81], v245 offset:0
ds_read_b64_tr_b16 a[82:83], v245 offset:4096
v_add_f32 v57, v202, v203
v_add_f32 v59, v210, v211
v_mfma_f32_16x16x32_bf16 v[68:71], a[116:119], a[96:99], v[68:71]
s_add_u32 m0, s49, 16384
ds_read_b128 a[64:67], v244 offset:0
v_add_f32 v61, v218, v219
v_add_f32 v63, v226, v227
v_mfma_f32_16x16x32_bf16 v[72:75], a[120:123], a[96:99], v[72:75]
buffer_load_dwordx4 v240, s[8:11], s45 offen offset:0 lds
v_add_f32 v56, v56, v204
v_add_f32 v58, v58, v212
v_add_f32 v60, v60, v220
v_mfma_f32_16x16x32_bf16 v[76:79], a[124:127], a[96:99], v[76:79]
ds_read_b64_tr_b16 a[84:85], v246 offset:0
ds_read_b64_tr_b16 a[86:87], v246 offset:4096
v_add_f32 v62, v62, v228
v_add_f32 v57, v57, v205
v_mfma_f32_16x16x32_bf16 v[80:83], a[112:115], a[100:103], v[80:83]
s_add_u32 s44, s44, 4096
ds_read_b128 a[68:71], v244 offset:1024
v_add_f32 v59, v59, v213
v_add_f32 v61, v61, v221
v_mfma_f32_16x16x32_bf16 v[84:87], a[116:119], a[100:103], v[84:87]
ds_read_b64_tr_b16 a[88:89], v247 offset:0
ds_read_b64_tr_b16 a[90:91], v247 offset:4096
v_add_f32 v63, v63, v229
v_add_f32 v56, v56, v206
v_mfma_f32_16x16x32_bf16 v[88:91], a[120:123], a[100:103], v[88:91]
buffer_load_dwordx4 v241, s[8:11], s45 offen offset:1024 lds
v_add_f32 v58, v58, v214
v_add_f32 v60, v60, v222
v_add_f32 v62, v62, v230
v_mfma_f32_16x16x32_bf16 v[92:95], a[124:127], a[100:103], v[92:95]
ds_read_b128 a[72:75], v244 offset:4096
v_add_f32 v57, v57, v207
v_add_f32 v59, v59, v215
v_add_f32 v61, v61, v223
v_mfma_f32_16x16x32_bf16 v[96:99], a[112:115], a[104:107], v[96:99]
ds_read_b64_tr_b16 a[92:93], v248 offset:0
ds_read_b64_tr_b16 a[94:95], v248 offset:4096
v_add_f32 v63, v63, v231
v_add_f32 v56, v56, v57
v_mfma_f32_16x16x32_bf16 v[100:103], a[116:119], a[104:107], v[100:103]
ds_read_b128 a[76:79], v244 offset:5120
v_add_f32 v58, v58, v59
v_add_f32 v60, v60, v61
v_add_f32 v62, v62, v63
v_mfma_f32_16x16x32_bf16 v[104:107], a[120:123], a[104:107], v[104:107]
buffer_load_dwordx4 v242, s[8:11], s45 offen offset:2048 lds
v_mov_b32 v57, v56
v_mov_b32 v59, v58
v_mov_b32 v61, v60
v_mfma_f32_16x16x32_bf16 v[108:111], a[124:127], a[104:107], v[108:111]
s_cmp_eq_u32 s46, 0
s_cselect_b32 s53, s56, s57
v_mov_b32 v63, v62
v_permlane16_swap_b32 v56, v57
v_mfma_f32_16x16x32_bf16 v[112:115], a[112:115], a[108:111], v[112:115]
v_permlane16_swap_b32 v58, v59
v_permlane16_swap_b32 v60, v61
v_mfma_f32_16x16x32_bf16 v[116:119], a[116:119], a[108:111], v[116:119]
v_permlane16_swap_b32 v62, v63
v_add_f32 v56, v56, v57
v_add_f32 v58, v58, v59
v_mfma_f32_16x16x32_bf16 v[120:123], a[120:123], a[108:111], v[120:123]
buffer_load_dwordx4 v243, s[8:11], s45 offen offset:3072 lds
v_add_f32 v60, v60, v61
v_add_f32 v62, v62, v63
v_mov_b32 v57, v56
v_mfma_f32_16x16x32_bf16 v[124:127], a[124:127], a[108:111], v[124:127]
s_waitcnt lgkmcnt(0)
s_mov_b32 m0, s49
s_add_u32 s45, s45, 131072
v_mov_b32 v59, v58
v_mov_b32 v61, v60
v_mfma_f32_16x16x32_bf16 v[64:67], a[80:83], a[64:67], v[64:67]
ds_read_b64_tr_b16 a[112:113], v245 offset:8192
ds_read_b64_tr_b16 a[114:115], v245 offset:12288
v_mov_b32 v63, v62
v_mfma_f32_16x16x32_bf16 v[68:71], a[84:87], a[64:67], v[68:71]
ds_read_b128 a[96:99], v244 offset:2048
v_add_u32 v245, s53, v245
v_permlane32_swap_b32 v56, v57
v_mfma_f32_16x16x32_bf16 v[72:75], a[88:91], a[64:67], v[72:75]
buffer_load_dwordx4 v7, s[4:7], s44 offen offset:0 sc1 lds
v_permlane32_swap_b32 v58, v59
v_mfma_f32_16x16x32_bf16 v[76:79], a[92:95], a[64:67], v[76:79]
ds_read_b64_tr_b16 a[116:117], v246 offset:8192
ds_read_b64_tr_b16 a[118:119], v246 offset:12288
v_permlane32_swap_b32 v60, v61
v_mfma_f32_16x16x32_bf16 v[80:83], a[80:83], a[68:71], v[80:83]
ds_read_b128 a[100:103], v244 offset:3072
v_add_u32 v246, s53, v246
v_permlane32_swap_b32 v62, v63
v_mfma_f32_16x16x32_bf16 v[84:87], a[84:87], a[68:71], v[84:87]
ds_read_b64_tr_b16 a[120:121], v247 offset:8192
ds_read_b64_tr_b16 a[122:123], v247 offset:12288
v_add_f32 v56, v56, v57
v_add_f32 v58, v58, v59
v_mfma_f32_16x16x32_bf16 v[88:91], a[88:91], a[68:71], v[88:91]
buffer_load_dwordx4 v7, s[4:7], s44 offen offset:1024 sc1 lds
v_add_f32 v60, v60, v61
v_add_f32 v62, v62, v63
v_mfma_f32_16x16x32_bf16 v[92:95], a[92:95], a[68:71], v[92:95]
ds_read_b128 a[104:107], v244 offset:6144
v_add_u32 v247, s53, v247
v_rcp_f32 v56, v56
v_mfma_f32_16x16x32_bf16 v[96:99], a[80:83], a[72:75], v[96:99]
ds_read_b64_tr_b16 a[124:125], v248 offset:8192
ds_read_b64_tr_b16 a[126:127], v248 offset:12288
v_rcp_f32 v58, v58
v_mfma_f32_16x16x32_bf16 v[100:103], a[84:87], a[72:75], v[100:103]
ds_read_b128 a[108:111], v244 offset:7168
v_add_u32 v248, s53, v248
v_rcp_f32 v60, v60
v_mfma_f32_16x16x32_bf16 v[104:107], a[88:91], a[72:75], v[104:107]
buffer_load_dwordx4 v7, s[4:7], s44 offen offset:2048 sc1 lds
v_rcp_f32 v62, v62
v_mul_f32 v160, v56, v160
v_mfma_f32_16x16x32_bf16 v[108:111], a[92:95], a[72:75], v[108:111]
s_add_u32 s49, s46, s91
v_add_u32 v244, s53, v244
v_mul_f32 v161, v56, v161
v_mul_f32 v162, v56, v162
v_mul_f32 v163, v56, v163
v_mfma_f32_16x16x32_bf16 v[112:115], a[80:83], a[76:79], v[112:115]
s_add_u32 s46, s46, s53
v_mul_f32 v164, v56, v164
v_mul_f32 v165, v56, v165
v_mul_f32 v166, v56, v166
v_mul_f32 v167, v56, v167
v_mfma_f32_16x16x32_bf16 v[116:119], a[84:87], a[76:79], v[116:119]
v_cvt_pk_bf16_f32 v128, v160, v161
v_cvt_pk_bf16_f32 v129, v162, v163
.Lpad19:
s_cbranch_execz .Ltch10
v_cvt_pk_bf16_f32 v130, v164, v165
v_cvt_pk_bf16_f32 v131, v166, v167
v_mfma_f32_16x16x32_bf16 v[120:123], a[88:91], a[76:79], v[120:123]
buffer_load_dwordx4 v7, s[4:7], s44 offen offset:3072 sc1 lds
buffer_store_dwordx4 v[128:131], v7, s[20:23], s40 offen offset:0 sc1
v_mul_f32 v168, v58, v168
v_mfma_f32_16x16x32_bf16 v[124:127], a[92:95], a[76:79], v[124:127]
s_waitcnt vmcnt(25) lgkmcnt(0)
s_barrier
v_mul_f32 v169, v58, v169
v_mul_f32 v170, v58, v170
v_mul_f32 v171, v58, v171
v_mfma_f32_16x16x32_bf16 v[64:67], a[112:115], a[96:99], v[64:67]
ds_read_b64_tr_b16 a[80:81], v245 offset:0
ds_read_b64_tr_b16 a[82:83], v245 offset:4096
v_mul_f32 v172, v58, v172
v_mul_f32 v173, v58, v173
v_mfma_f32_16x16x32_bf16 v[68:71], a[116:119], a[96:99], v[68:71]
s_add_u32 m0, s49, 16384
ds_read_b128 a[64:67], v244 offset:0
v_mul_f32 v174, v58, v174
v_mul_f32 v175, v58, v175
v_mfma_f32_16x16x32_bf16 v[72:75], a[120:123], a[96:99], v[72:75]
buffer_load_dwordx4 v240, s[8:11], s45 offen offset:0 lds
v_cvt_pk_bf16_f32 v136, v168, v169
v_cvt_pk_bf16_f32 v137, v170, v171
v_cvt_pk_bf16_f32 v138, v172, v173
v_mfma_f32_16x16x32_bf16 v[76:79], a[124:127], a[96:99], v[76:79]
ds_read_b64_tr_b16 a[84:85], v246 offset:0
ds_read_b64_tr_b16 a[86:87], v246 offset:4096
v_cvt_pk_bf16_f32 v139, v174, v175
v_mfma_f32_16x16x32_bf16 v[80:83], a[112:115], a[100:103], v[80:83]
s_add_u32 s44, s44, 4096
buffer_store_dwordx4 v[136:139], v7, s[20:23], s40 offen offset:1024 sc1
ds_read_b128 a[68:71], v244 offset:1024
v_mfma_f32_16x16x32_bf16 v[84:87], a[116:119], a[100:103], v[84:87]
ds_read_b64_tr_b16 a[88:89], v247 offset:0
ds_read_b64_tr_b16 a[90:91], v247 offset:4096
v_mul_f32 v176, v60, v176
v_mul_f32 v177, v60, v177
v_mfma_f32_16x16x32_bf16 v[88:91], a[120:123], a[100:103], v[88:91]
buffer_load_dwordx4 v241, s[8:11], s45 offen offset:1024 lds
v_mul_f32 v178, v60, v178
v_mul_f32 v179, v60, v179
v_mul_f32 v180, v60, v180
v_mfma_f32_16x16x32_bf16 v[92:95], a[124:127], a[100:103], v[92:95]
ds_read_b128 a[72:75], v244 offset:4096
v_mul_f32 v181, v60, v181
v_mul_f32 v182, v60, v182
v_mul_f32 v183, v60, v183
v_mfma_f32_16x16x32_bf16 v[96:99], a[112:115], a[104:107], v[96:99]
ds_read_b64_tr_b16 a[92:93], v248 offset:0
ds_read_b64_tr_b16 a[94:95], v248 offset:4096
v_cvt_pk_bf16_f32 v144, v176, v177
v_cvt_pk_bf16_f32 v145, v178, v179
v_mfma_f32_16x16x32_bf16 v[100:103], a[116:119], a[104:107], v[100:103]
ds_read_b128 a[76:79], v244 offset:5120
v_cvt_pk_bf16_f32 v146, v180, v181
v_cvt_pk_bf16_f32 v147, v182, v183
v_mfma_f32_16x16x32_bf16 v[104:107], a[120:123], a[104:107], v[104:107]
buffer_load_dwordx4 v242, s[8:11], s45 offen offset:2048 lds
buffer_store_dwordx4 v[144:147], v7, s[20:23], s41 offen offset:0 sc1
v_mul_f32 v184, v62, v184
v_mfma_f32_16x16x32_bf16 v[108:111], a[124:127], a[104:107], v[108:111]
s_cmp_eq_u32 s46, 0
s_cselect_b32 s53, s56, s57
v_mul_f32 v185, v62, v185
v_mul_f32 v186, v62, v186
v_mul_f32 v187, v62, v187
v_mfma_f32_16x16x32_bf16 v[112:115], a[112:115], a[108:111], v[112:115]
v_mul_f32 v188, v62, v188
v_mul_f32 v189, v62, v189
v_mul_f32 v190, v62, v190
v_mul_f32 v191, v62, v191
v_mfma_f32_16x16x32_bf16 v[116:119], a[116:119], a[108:111], v[116:119]
v_cvt_pk_bf16_f32 v152, v184, v185
v_cvt_pk_bf16_f32 v153, v186, v187
v_cvt_pk_bf16_f32 v154, v188, v189
v_cvt_pk_bf16_f32 v155, v190, v191
v_mfma_f32_16x16x32_bf16 v[120:123], a[120:123], a[108:111], v[120:123]
buffer_load_dwordx4 v243, s[8:11], s45 offen offset:3072 lds
buffer_store_dwordx4 v[152:155], v7, s[20:23], s41 offen offset:1024 sc1
v_mfma_f32_16x16x32_bf16 v[124:127], a[124:127], a[108:111], v[124:127]
s_waitcnt lgkmcnt(0)
s_mov_b32 m0, s49
s_add_u32 s45, s45, 131072
v_mfma_f32_16x16x32_bf16 v[64:67], a[80:83], a[64:67], v[64:67]
ds_read_b64_tr_b16 a[112:113], v245 offset:8192
ds_read_b64_tr_b16 a[114:115], v245 offset:12288
v_mfma_f32_16x16x32_bf16 v[68:71], a[84:87], a[64:67], v[68:71]
ds_read_b128 a[96:99], v244 offset:2048
v_add_u32 v245, s53, v245
v_mfma_f32_16x16x32_bf16 v[72:75], a[88:91], a[64:67], v[72:75]
buffer_load_dwordx4 v7, s[4:7], s44 offen offset:0 sc1 lds
v_mfma_f32_16x16x32_bf16 v[76:79], a[92:95], a[64:67], v[76:79]
ds_read_b64_tr_b16 a[116:117], v246 offset:8192
ds_read_b64_tr_b16 a[118:119], v246 offset:12288
v_mfma_f32_16x16x32_bf16 v[80:83], a[80:83], a[68:71], v[80:83]
ds_read_b128 a[100:103], v244 offset:3072
v_add_u32 v246, s53, v246
v_mfma_f32_16x16x32_bf16 v[84:87], a[84:87], a[68:71], v[84:87]
ds_read_b64_tr_b16 a[120:121], v247 offset:8192
ds_read_b64_tr_b16 a[122:123], v247 offset:12288
v_mfma_f32_16x16x32_bf16 v[88:91], a[88:91], a[68:71], v[88:91]
buffer_load_dwordx4 v7, s[4:7], s44 offen offset:1024 sc1 lds
v_mfma_f32_16x16x32_bf16 v[92:95], a[92:95], a[68:71], v[92:95]
ds_read_b128 a[104:107], v244 offset:6144
v_add_u32 v247, s53, v247
v_mfma_f32_16x16x32_bf16 v[96:99], a[80:83], a[72:75], v[96:99]
ds_read_b64_tr_b16 a[124:125], v248 offset:8192
ds_read_b64_tr_b16 a[126:127], v248 offset:12288
v_mfma_f32_16x16x32_bf16 v[100:103], a[84:87], a[72:75], v[100:103]
ds_read_b128 a[108:111], v244 offset:7168
v_add_u32 v248, s53, v248
v_mfma_f32_16x16x32_bf16 v[104:107], a[88:91], a[72:75], v[104:107]
buffer_load_dwordx4 v7, s[4:7], s44 offen offset:2048 sc1 lds
v_mfma_f32_16x16x32_bf16 v[108:111], a[92:95], a[72:75], v[108:111]
s_add_u32 s49, s46, s91
v_add_u32 v244, s53, v244
v_mfma_f32_16x16x32_bf16 v[112:115], a[80:83], a[76:79], v[112:115]
s_add_u32 s46, s46, s53
v_mfma_f32_16x16x32_bf16 v[116:119], a[84:87], a[76:79], v[116:119]
v_mfma_f32_16x16x32_bf16 v[120:123], a[88:91], a[76:79], v[120:123]
buffer_load_dwordx4 v7, s[4:7], s44 offen offset:3072 sc1 lds
v_mfma_f32_16x16x32_bf16 v[124:127], a[92:95], a[76:79], v[124:127]
s_waitcnt vmcnt(28) lgkmcnt(0)
s_barrier
v_mfma_f32_16x16x32_bf16 v[64:67], a[112:115], a[96:99], v[64:67]
ds_read_b64_tr_b16 a[80:81], v245 offset:0
ds_read_b64_tr_b16 a[82:83], v245 offset:4096
v_mfma_f32_16x16x32_bf16 v[68:71], a[116:119], a[96:99], v[68:71]
s_add_u32 m0, s49, 16384
ds_read_b128 a[64:67], v244 offset:0
v_mfma_f32_16x16x32_bf16 v[72:75], a[120:123], a[96:99], v[72:75]
buffer_load_dwordx4 v240, s[8:11], s45 offen offset:0 lds
v_mfma_f32_16x16x32_bf16 v[76:79], a[124:127], a[96:99], v[76:79]
ds_read_b64_tr_b16 a[84:85], v246 offset:0
ds_read_b64_tr_b16 a[86:87], v246 offset:4096
v_mfma_f32_16x16x32_bf16 v[80:83], a[112:115], a[100:103], v[80:83]
s_add_u32 s44, s44, 4096
ds_read_b128 a[68:71], v244 offset:1024
v_mfma_f32_16x16x32_bf16 v[84:87], a[116:119], a[100:103], v[84:87]
ds_read_b64_tr_b16 a[88:89], v247 offset:0
ds_read_b64_tr_b16 a[90:91], v247 offset:4096
v_mfma_f32_16x16x32_bf16 v[88:91], a[120:123], a[100:103], v[88:91]
buffer_load_dwordx4 v241, s[8:11], s45 offen offset:1024 lds
v_mfma_f32_16x16x32_bf16 v[92:95], a[124:127], a[100:103], v[92:95]
ds_read_b128 a[72:75], v244 offset:4096
v_mfma_f32_16x16x32_bf16 v[96:99], a[112:115], a[104:107], v[96:99]
ds_read_b64_tr_b16 a[92:93], v248 offset:0
ds_read_b64_tr_b16 a[94:95], v248 offset:4096
v_mfma_f32_16x16x32_bf16 v[100:103], a[116:119], a[104:107], v[100:103]
ds_read_b128 a[76:79], v244 offset:5120
v_mfma_f32_16x16x32_bf16 v[104:107], a[120:123], a[104:107], v[104:107]
buffer_load_dwordx4 v242, s[8:11], s45 offen offset:2048 lds
v_mfma_f32_16x16x32_bf16 v[108:111], a[124:127], a[104:107], v[108:111]
s_cmp_eq_u32 s46, 0
s_cselect_b32 s53, s56, s57
v_mfma_f32_16x16x32_bf16 v[112:115], a[112:115], a[108:111], v[112:115]
v_mfma_f32_16x16x32_bf16 v[116:119], a[116:119], a[108:111], v[116:119]
v_mfma_f32_16x16x32_bf16 v[120:123], a[120:123], a[108:111], v[120:123]
buffer_load_dwordx4 v243, s[8:11], s45 offen offset:3072 lds
v_mfma_f32_16x16x32_bf16 v[124:127], a[124:127], a[108:111], v[124:127]
s_waitcnt lgkmcnt(0)
s_mov_b32 m0, s49
s_add_u32 s45, s45, 131072
v_mfma_f32_16x16x32_bf16 v[64:67], a[80:83], a[64:67], v[64:67]
ds_read_b64_tr_b16 a[112:113], v245 offset:8192
ds_read_b64_tr_b16 a[114:115], v245 offset:12288
v_mfma_f32_16x16x32_bf16 v[68:71], a[84:87], a[64:67], v[68:71]
ds_read_b128 a[96:99], v244 offset:2048
v_add_u32 v245, s53, v245
v_mfma_f32_16x16x32_bf16 v[72:75], a[88:91], a[64:67], v[72:75]
buffer_load_dwordx4 v7, s[4:7], s44 offen offset:0 sc1 lds
v_mfma_f32_16x16x32_bf16 v[76:79], a[92:95], a[64:67], v[76:79]
ds_read_b64_tr_b16 a[116:117], v246 offset:8192
ds_read_b64_tr_b16 a[118:119], v246 offset:12288
v_mfma_f32_16x16x32_bf16 v[80:83], a[80:83], a[68:71], v[80:83]
ds_read_b128 a[100:103], v244 offset:3072
v_add_u32 v246, s53, v246
v_mfma_f32_16x16x32_bf16 v[84:87], a[84:87], a[68:71], v[84:87]
ds_read_b64_tr_b16 a[120:121], v247 offset:8192
ds_read_b64_tr_b16 a[122:123], v247 offset:12288
v_mfma_f32_16x16x32_bf16 v[88:91], a[88:91], a[68:71], v[88:91]
buffer_load_dwordx4 v7, s[4:7], s44 offen offset:1024 sc1 lds
v_mfma_f32_16x16x32_bf16 v[92:95], a[92:95], a[68:71], v[92:95]
ds_read_b128 a[104:107], v244 offset:6144
v_add_u32 v247, s53, v247
v_mfma_f32_16x16x32_bf16 v[96:99], a[80:83], a[72:75], v[96:99]
ds_read_b64_tr_b16 a[124:125], v248 offset:8192
ds_read_b64_tr_b16 a[126:127], v248 offset:12288
v_mfma_f32_16x16x32_bf16 v[100:103], a[84:87], a[72:75], v[100:103]
ds_read_b128 a[108:111], v244 offset:7168
v_add_u32 v248, s53, v248
v_mfma_f32_16x16x32_bf16 v[104:107], a[88:91], a[72:75], v[104:107]
buffer_load_dwordx4 v7, s[4:7], s44 offen offset:2048 sc1 lds
v_mfma_f32_16x16x32_bf16 v[108:111], a[92:95], a[72:75], v[108:111]
s_add_u32 s49, s46, s91
v_add_u32 v244, s53, v244
v_mfma_f32_16x16x32_bf16 v[112:115], a[80:83], a[76:79], v[112:115]
s_add_u32 s46, s46, s53
v_mfma_f32_16x16x32_bf16 v[116:119], a[84:87], a[76:79], v[116:119]
v_mfma_f32_16x16x32_bf16 v[120:123], a[88:91], a[76:79], v[120:123]
buffer_load_dwordx4 v7, s[4:7], s44 offen offset:3072 sc1 lds
v_mfma_f32_16x16x32_bf16 v[124:127], a[92:95], a[76:79], v[124:127]
s_waitcnt vmcnt(28) lgkmcnt(0)
s_barrier
v_mfma_f32_16x16x32_bf16 v[64:67], a[112:115], a[96:99], v[64:67]
ds_read_b64_tr_b16 a[80:81], v245 offset:0
ds_read_b64_tr_b16 a[82:83], v245 offset:4096
v_mfma_f32_16x16x32_bf16 v[68:71], a[116:119], a[96:99], v[68:71]
s_add_u32 m0, s49, 16384
ds_read_b128 a[64:67], v244 offset:0
v_mfma_f32_16x16x32_bf16 v[72:75], a[120:123], a[96:99], v[72:75]
buffer_load_dwordx4 v240, s[8:11], s45 offen offset:0 lds
v_mfma_f32_16x16x32_bf16 v[76:79], a[124:127], a[96:99], v[76:79]
ds_read_b64_tr_b16 a[84:85], v246 offset:0
ds_read_b64_tr_b16 a[86:87], v246 offset:4096
v_mfma_f32_16x16x32_bf16 v[80:83], a[112:115], a[100:103], v[80:83]
s_add_u32 s44, s44, 4096
ds_read_b128 a[68:71], v244 offset:1024
v_mfma_f32_16x16x32_bf16 v[84:87], a[116:119], a[100:103], v[84:87]
ds_read_b64_tr_b16 a[88:89], v247 offset:0
ds_read_b64_tr_b16 a[90:91], v247 offset:4096
v_mfma_f32_16x16x32_bf16 v[88:91], a[120:123], a[100:103], v[88:91]
buffer_load_dwordx4 v241, s[8:11], s45 offen offset:1024 lds
v_mfma_f32_16x16x32_bf16 v[92:95], a[124:127], a[100:103], v[92:95]
ds_read_b128 a[72:75], v244 offset:4096
v_mfma_f32_16x16x32_bf16 v[96:99], a[112:115], a[104:107], v[96:99]
ds_read_b64_tr_b16 a[92:93], v248 offset:0
ds_read_b64_tr_b16 a[94:95], v248 offset:4096
v_mfma_f32_16x16x32_bf16 v[100:103], a[116:119], a[104:107], v[100:103]
ds_read_b128 a[76:79], v244 offset:5120
v_mfma_f32_16x16x32_bf16 v[104:107], a[120:123], a[104:107], v[104:107]
buffer_load_dwordx4 v242, s[8:11], s45 offen offset:2048 lds
v_mfma_f32_16x16x32_bf16 v[108:111], a[124:127], a[104:107], v[108:111]
s_cmp_eq_u32 s46, 0
s_cselect_b32 s53, s56, s57
v_mfma_f32_16x16x32_bf16 v[112:115], a[112:115], a[108:111], v[112:115]
v_mfma_f32_16x16x32_bf16 v[116:119], a[116:119], a[108:111], v[116:119]
v_mfma_f32_16x16x32_bf16 v[120:123], a[120:123], a[108:111], v[120:123]
buffer_load_dwordx4 v243, s[8:11], s45 offen offset:3072 lds
v_mfma_f32_16x16x32_bf16 v[124:127], a[124:127], a[108:111], v[124:127]
s_waitcnt lgkmcnt(0)
s_mov_b32 m0, s49
s_add_u32 s45, s45, 131072
v_mfma_f32_16x16x32_bf16 v[64:67], a[80:83], a[64:67], v[64:67]
ds_read_b64_tr_b16 a[112:113], v245 offset:8192
ds_read_b64_tr_b16 a[114:115], v245 offset:12288
v_mfma_f32_16x16x32_bf16 v[68:71], a[84:87], a[64:67], v[68:71]
ds_read_b128 a[96:99], v244 offset:2048
v_add_u32 v245, s53, v245
v_mfma_f32_16x16x32_bf16 v[72:75], a[88:91], a[64:67], v[72:75]
buffer_load_dwordx4 v7, s[4:7], s44 offen offset:0 sc1 lds
v_mfma_f32_16x16x32_bf16 v[76:79], a[92:95], a[64:67], v[76:79]
ds_read_b64_tr_b16 a[116:117], v246 offset:8192
ds_read_b64_tr_b16 a[118:119], v246 offset:12288
v_mfma_f32_16x16x32_bf16 v[80:83], a[80:83], a[68:71], v[80:83]
ds_read_b128 a[100:103], v244 offset:3072
v_add_u32 v246, s53, v246
v_mfma_f32_16x16x32_bf16 v[84:87], a[84:87], a[68:71], v[84:87]
ds_read_b64_tr_b16 a[120:121], v247 offset:8192
ds_read_b64_tr_b16 a[122:123], v247 offset:12288
v_mfma_f32_16x16x32_bf16 v[88:91], a[88:91], a[68:71], v[88:91]
buffer_load_dwordx4 v7, s[4:7], s44 offen offset:1024 sc1 lds
v_mfma_f32_16x16x32_bf16 v[92:95], a[92:95], a[68:71], v[92:95]
ds_read_b128 a[104:107], v244 offset:6144
v_add_u32 v247, s53, v247
v_mfma_f32_16x16x32_bf16 v[96:99], a[80:83], a[72:75], v[96:99]
ds_read_b64_tr_b16 a[124:125], v248 offset:8192
ds_read_b64_tr_b16 a[126:127], v248 offset:12288
v_mfma_f32_16x16x32_bf16 v[100:103], a[84:87], a[72:75], v[100:103]
ds_read_b128 a[108:111], v244 offset:7168
v_add_u32 v248, s53, v248
v_mfma_f32_16x16x32_bf16 v[104:107], a[88:91], a[72:75], v[104:107]
buffer_load_dwordx4 v7, s[4:7], s44 offen offset:2048 sc1 lds
v_mfma_f32_16x16x32_bf16 v[108:111], a[92:95], a[72:75], v[108:111]
s_add_u32 s49, s46, s91
v_add_u32 v244, s53, v244
v_mfma_f32_16x16x32_bf16 v[112:115], a[80:83], a[76:79], v[112:115]
s_add_u32 s46, s46, s53
v_mfma_f32_16x16x32_bf16 v[116:119], a[84:87], a[76:79], v[116:119]
v_mfma_f32_16x16x32_bf16 v[120:123], a[88:91], a[76:79], v[120:123]
buffer_load_dwordx4 v7, s[4:7], s44 offen offset:3072 sc1 lds
v_mfma_f32_16x16x32_bf16 v[124:127], a[92:95], a[76:79], v[124:127]
s_waitcnt vmcnt(20)
s_waitcnt vmcnt(28) lgkmcnt(0)
s_barrier
v_mfma_f32_16x16x32_bf16 v[64:67], a[112:115], a[96:99], v[64:67]
ds_read_b64_tr_b16 a[80:81], v245 offset:0
ds_read_b64_tr_b16 a[82:83], v245 offset:4096
v_mfma_f32_16x16x32_bf16 v[68:71], a[116:119], a[96:99], v[68:71]
s_add_u32 m0, s49, 16384
ds_read_b128 a[64:67], v244 offset:0
v_mfma_f32_16x16x32_bf16 v[72:75], a[120:123], a[96:99], v[72:75]
buffer_load_dwordx4 v240, s[8:11], s45 offen offset:0 lds
v_mfma_f32_16x16x32_bf16 v[76:79], a[124:127], a[96:99], v[76:79]
ds_read_b64_tr_b16 a[84:85], v246 offset:0
ds_read_b64_tr_b16 a[86:87], v246 offset:4096
v_mfma_f32_16x16x32_bf16 v[80:83], a[112:115], a[100:103], v[80:83]
s_add_u32 s44, s44, 4096
ds_read_b128 a[68:71], v244 offset:1024
v_mfma_f32_16x16x32_bf16 v[84:87], a[116:119], a[100:103], v[84:87]
ds_read_b64_tr_b16 a[88:89], v247 offset:0
ds_read_b64_tr_b16 a[90:91], v247 offset:4096
v_mfma_f32_16x16x32_bf16 v[88:91], a[120:123], a[100:103], v[88:91]
buffer_load_dwordx4 v241, s[8:11], s45 offen offset:1024 lds
v_mfma_f32_16x16x32_bf16 v[92:95], a[124:127], a[100:103], v[92:95]
ds_read_b128 a[72:75], v244 offset:4096
v_mfma_f32_16x16x32_bf16 v[96:99], a[112:115], a[104:107], v[96:99]
ds_read_b64_tr_b16 a[92:93], v248 offset:0
ds_read_b64_tr_b16 a[94:95], v248 offset:4096
v_mfma_f32_16x16x32_bf16 v[100:103], a[116:119], a[104:107], v[100:103]
ds_read_b128 a[76:79], v244 offset:5120
v_mfma_f32_16x16x32_bf16 v[104:107], a[120:123], a[104:107], v[104:107]
s_nop 0
buffer_load_dwordx4 v242, s[8:11], s45 offen offset:2048 lds
v_or_b32 v0, 4, v11
v_mfma_f32_16x16x32_bf16 v[108:111], a[124:127], a[104:107], v[108:111]
s_cmp_eq_u32 s46, 0
s_cselect_b32 s53, s56, s57
v_mfma_f32_16x16x32_bf16 v[112:115], a[112:115], a[108:111], v[112:115]
v_mfma_f32_16x16x32_bf16 v[116:119], a[116:119], a[108:111], v[116:119]
v_mfma_f32_16x16x32_bf16 v[120:123], a[120:123], a[108:111], v[120:123]
buffer_load_dwordx4 v243, s[8:11], s45 offen offset:3072 lds
buffer_store_dword v0, v6, s[60:63], 0 offen sc1
v_mfma_f32_16x16x32_bf16 v[124:127], a[124:127], a[108:111], v[124:127]
s_waitcnt lgkmcnt(0)
s_mov_b32 m0, s49
s_add_u32 s45, s45, 131072
v_mfma_f32_16x16x32_bf16 v[64:67], a[80:83], a[64:67], v[64:67]
ds_read_b64_tr_b16 a[112:113], v245 offset:8192
ds_read_b64_tr_b16 a[114:115], v245 offset:12288
v_mfma_f32_16x16x32_bf16 v[68:71], a[84:87], a[64:67], v[68:71]
ds_read_b128 a[96:99], v244 offset:2048
v_add_u32 v245, s53, v245
v_mfma_f32_16x16x32_bf16 v[72:75], a[88:91], a[64:67], v[72:75]
buffer_load_dwordx4 v7, s[4:7], s44 offen offset:0 sc1 lds
v_mfma_f32_16x16x32_bf16 v[76:79], a[92:95], a[64:67], v[76:79]
ds_read_b64_tr_b16 a[116:117], v246 offset:8192
ds_read_b64_tr_b16 a[118:119], v246 offset:12288
v_mfma_f32_16x16x32_bf16 v[80:83], a[80:83], a[68:71], v[80:83]
ds_read_b128 a[100:103], v244 offset:3072
v_add_u32 v246, s53, v246
v_mfma_f32_16x16x32_bf16 v[84:87], a[84:87], a[68:71], v[84:87]
ds_read_b64_tr_b16 a[120:121], v247 offset:8192
ds_read_b64_tr_b16 a[122:123], v247 offset:12288
v_mfma_f32_16x16x32_bf16 v[88:91], a[88:91], a[68:71], v[88:91]
buffer_load_dwordx4 v7, s[4:7], s44 offen offset:1024 sc1 lds
v_mfma_f32_16x16x32_bf16 v[92:95], a[92:95], a[68:71], v[92:95]
ds_read_b128 a[104:107], v244 offset:6144
v_add_u32 v247, s53, v247
v_mfma_f32_16x16x32_bf16 v[96:99], a[80:83], a[72:75], v[96:99]
buffer_load_dword v35, v4, s[96:99], 0 offen
ds_read_b64_tr_b16 a[124:125], v248 offset:8192
ds_read_b64_tr_b16 a[126:127], v248 offset:12288
v_mfma_f32_16x16x32_bf16 v[100:103], a[84:87], a[72:75], v[100:103]
ds_read_b128 a[108:111], v244 offset:7168
v_add_u32 v248, s53, v248
v_mfma_f32_16x16x32_bf16 v[104:107], a[88:91], a[72:75], v[104:107]
buffer_load_dwordx4 v7, s[4:7], s44 offen offset:2048 sc1 lds
v_mfma_f32_16x16x32_bf16 v[108:111], a[92:95], a[72:75], v[108:111]
s_add_u32 s49, s46, s91
v_add_u32 v244, s53, v244
v_mfma_f32_16x16x32_bf16 v[112:115], a[80:83], a[76:79], v[112:115]
s_add_u32 s46, s46, s53
v_mfma_f32_16x16x32_bf16 v[116:119], a[84:87], a[76:79], v[116:119]
v_mfma_f32_16x16x32_bf16 v[120:123], a[88:91], a[76:79], v[120:123]
buffer_load_dwordx4 v7, s[4:7], s44 offen offset:3072 sc1 lds
v_mfma_f32_16x16x32_bf16 v[124:127], a[92:95], a[76:79], v[124:127]
s_waitcnt vmcnt(26) lgkmcnt(0)
s_barrier
s_mov_b64 exec, 0
s_cmp_eq_u32 s3, 0
s_cbranch_scc1 .Lpad20
s_cmp_eq_u32 s3, 1
s_cbranch_scc1 .L5odd_pad
s_cmp_eq_u32 s3, 2
s_cbranch_scc1 .Lpad21
s_branch .Lpad22
.Ltch11:
s_mov_b64 exec, -1
s_nop 0
s_nop 0
s_nop 0
s_nop 0
s_nop 0
.L5b:
v_mfma_f32_16x16x32_bf16 v[64:67], a[112:115], a[96:99], v[64:67]
ds_read_b64_tr_b16 a[80:81], v245 offset:0
ds_read_b64_tr_b16 a[82:83], v245 offset:4096
v_mfma_f32_16x16x32_bf16 v[68:71], a[116:119], a[96:99], v[68:71]
s_add_u32 m0, s49, 16384
ds_read_b128 a[64:67], v244 offset:0
v_mfma_f32_16x16x32_bf16 v[72:75], a[120:123], a[96:99], v[72:75]
buffer_load_dwordx4 v240, s[8:11], s45 offen offset:0 lds
v_mfma_f32_16x16x32_bf16 v[76:79], a[124:127], a[96:99], v[76:79]
ds_read_b64_tr_b16 a[84:85], v246 offset:0
ds_read_b64_tr_b16 a[86:87], v246 offset:4096
v_mfma_f32_16x16x32_bf16 v[80:83], a[112:115], a[100:103], v[80:83]
s_add_u32 s44, s44, 4096
ds_read_b128 a[68:71], v244 offset:1024
v_mfma_f32_16x16x32_bf16 v[84:87], a[116:119], a[100:103], v[84:87]
ds_read_b64_tr_b16 a[88:89], v247 offset:0
ds_read_b64_tr_b16 a[90:91], v247 offset:4096
v_mfma_f32_16x16x32_bf16 v[88:91], a[120:123], a[100:103], v[88:91]
buffer_load_dwordx4 v241, s[8:11], s45 offen offset:1024 lds
v_mfma_f32_16x16x32_bf16 v[92:95], a[124:127], a[100:103], v[92:95]
ds_read_b128 a[72:75], v244 offset:4096
v_mfma_f32_16x16x32_bf16 v[96:99], a[112:115], a[104:107], v[96:99]
ds_read_b64_tr_b16 a[92:93], v248 offset:0
ds_read_b64_tr_b16 a[94:95], v248 offset:4096
v_mfma_f32_16x16x32_bf16 v[100:103], a[116:119], a[104:107], v[100:103]
ds_read_b128 a[76:79], v244 offset:5120
v_mfma_f32_16x16x32_bf16 v[104:107], a[120:123], a[104:107], v[104:107]
buffer_load_dwordx4 v242, s[8:11], s45 offen offset:2048 lds
v_mfma_f32_16x16x32_bf16 v[108:111], a[124:127], a[104:107], v[108:111]
s_cmp_eq_u32 s46, 0
s_cselect_b32 s53, s56, s57
v_mfma_f32_16x16x32_bf16 v[112:115], a[112:115], a[108:111], v[112:115]
v_mfma_f32_16x16x32_bf16 v[116:119], a[116:119], a[108:111], v[116:119]
v_mfma_f32_16x16x32_bf16 v[120:123], a[120:123], a[108:111], v[120:123]
buffer_load_dwordx4 v243, s[8:11], s45 offen offset:3072 lds
v_mfma_f32_16x16x32_bf16 v[124:127], a[124:127], a[108:111], v[124:127]
s_waitcnt lgkmcnt(0)
s_mov_b32 m0, s49
s_add_u32 s45, s45, 131072
v_mfma_f32_16x16x32_bf16 v[64:67], a[80:83], a[64:67], v[64:67]
ds_read_b64_tr_b16 a[112:113], v245 offset:8192
ds_read_b64_tr_b16 a[114:115], v245 offset:12288
v_mfma_f32_16x16x32_bf16 v[68:71], a[84:87], a[64:67], v[68:71]
ds_read_b128 a[96:99], v244 offset:2048
v_add_u32 v245, s53, v245
v_mfma_f32_16x16x32_bf16 v[72:75], a[88:91], a[64:67], v[72:75]
buffer_load_dwordx4 v7, s[4:7], s44 offen offset:0 sc1 lds
v_mfma_f32_16x16x32_bf16 v[76:79], a[92:95], a[64:67], v[76:79]
ds_read_b64_tr_b16 a[116:117], v246 offset:8192
ds_read_b64_tr_b16 a[118:119], v246 offset:12288
v_mfma_f32_16x16x32_bf16 v[80:83], a[80:83], a[68:71], v[80:83]
ds_read_b128 a[100:103], v244 offset:3072
v_add_u32 v246, s53, v246
v_mfma_f32_16x16x32_bf16 v[84:87], a[84:87], a[68:71], v[84:87]
ds_read_b64_tr_b16 a[120:121], v247 offset:8192
ds_read_b64_tr_b16 a[122:123], v247 offset:12288
v_mfma_f32_16x16x32_bf16 v[88:91], a[88:91], a[68:71], v[88:91]
buffer_load_dwordx4 v7, s[4:7], s44 offen offset:1024 sc1 lds
v_mfma_f32_16x16x32_bf16 v[92:95], a[92:95], a[68:71], v[92:95]
ds_read_b128 a[104:107], v244 offset:6144
v_add_u32 v247, s53, v247
v_mfma_f32_16x16x32_bf16 v[96:99], a[80:83], a[72:75], v[96:99]
ds_read_b64_tr_b16 a[124:125], v248 offset:8192
ds_read_b64_tr_b16 a[126:127], v248 offset:12288
v_mfma_f32_16x16x32_bf16 v[100:103], a[84:87], a[72:75], v[100:103]
ds_read_b128 a[108:111], v244 offset:7168
v_add_u32 v248, s53, v248
v_mfma_f32_16x16x32_bf16 v[104:107], a[88:91], a[72:75], v[104:107]
buffer_load_dwordx4 v7, s[4:7], s44 offen offset:2048 sc1 lds
v_mfma_f32_16x16x32_bf16 v[108:111], a[92:95], a[72:75], v[108:111]
s_add_u32 s49, s46, s91
v_add_u32 v244, s53, v244
v_mfma_f32_16x16x32_bf16 v[112:115], a[80:83], a[76:79], v[112:115]
s_add_u32 s46, s46, s53
v_mfma_f32_16x16x32_bf16 v[116:119], a[84:87], a[76:79], v[116:119]
s_sub_u32 s50, s50, 1
v_mfma_f32_16x16x32_bf16 v[120:123], a[88:91], a[76:79], v[120:123]
buffer_load_dwordx4 v7, s[4:7], s44 offen offset:3072 sc1 lds
v_mfma_f32_16x16x32_bf16 v[124:127], a[92:95], a[76:79], v[124:127]
s_waitcnt vmcnt(24) lgkmcnt(0)
s_barrier
s_cbranch_scc0 .L5b
s_cmp_eq_u32 s55, 0
s_cbranch_scc0 .L5odd
v_mfma_f32_16x16x32_bf16 v[64:67], a[112:115], a[96:99], v[64:67]
ds_read_b64_tr_b16 a[80:81], v245 offset:0
ds_read_b64_tr_b16 a[82:83], v245 offset:4096
v_mfma_f32_16x16x32_bf16 v[68:71], a[116:119], a[96:99], v[68:71]
buffer_load_dword v250, v249, s[60:63], s90 offen sc1
ds_read_b128 a[64:67], v244 offset:0
v_mfma_f32_16x16x32_bf16 v[72:75], a[120:123], a[96:99], v[72:75]
v_and_b32 v168, 15, v1
v_lshrrev_b32 v169, 4, v1
v_mfma_f32_16x16x32_bf16 v[76:79], a[124:127], a[96:99], v[76:79]
ds_read_b64_tr_b16 a[84:85], v246 offset:0
ds_read_b64_tr_b16 a[86:87], v246 offset:4096
v_mfma_f32_16x16x32_bf16 v[80:83], a[112:115], a[100:103], v[80:83]
s_mov_b32 s32, 4096
ds_read_b128 a[68:71], v244 offset:1024
v_lshlrev_b32 v169, 2, v169
v_mfma_f32_16x16x32_bf16 v[84:87], a[116:119], a[100:103], v[84:87]
ds_read_b64_tr_b16 a[88:89], v247 offset:0
ds_read_b64_tr_b16 a[90:91], v247 offset:4096
v_mfma_f32_16x16x32_bf16 v[88:91], a[120:123], a[100:103], v[88:91]
v_sub_u32 v168, v168, v169
v_mov_b32 v169, 0x3f80
v_mfma_f32_16x16x32_bf16 v[92:95], a[124:127], a[100:103], v[92:95]
ds_read_b128 a[72:75], v244 offset:4096
v_mov_b32 v170, 0x3f800000
v_mfma_f32_16x16x32_bf16 v[96:99], a[112:115], a[104:107], v[96:99]
ds_read_b64_tr_b16 a[92:93], v248 offset:0
ds_read_b64_tr_b16 a[94:95], v248 offset:4096
v_mfma_f32_16x16x32_bf16 v[100:103], a[116:119], a[104:107], v[100:103]
ds_read_b128 a[76:79], v244 offset:5120
v_cmp_eq_u32 s[24:25], 0, v168
v_mfma_f32_16x16x32_bf16 v[104:107], a[120:123], a[104:107], v[104:107]
v_cmp_eq_u32 s[26:27], 1, v168
v_cmp_eq_u32 s[28:29], 2, v168
v_mfma_f32_16x16x32_bf16 v[108:111], a[124:127], a[104:107], v[108:111]
s_cmp_eq_u32 s46, 0
s_cselect_b32 s53, s56, s57
v_cmp_eq_u32 s[30:31], 3, v168
v_mfma_f32_16x16x32_bf16 v[112:115], a[112:115], a[108:111], v[112:115]
v_cndmask_b32 v160, 0, v169, s[24:25]
v_cndmask_b32 v161, 0, v169, s[28:29]
v_mfma_f32_16x16x32_bf16 v[116:119], a[116:119], a[108:111], v[116:119]
v_cndmask_b32 v160, v160, v170, s[26:27]
v_cndmask_b32 v161, v161, v170, s[30:31]
v_mfma_f32_16x16x32_bf16 v[120:123], a[120:123], a[108:111], v[120:123]
v_mov_b32 v162, 0
v_mov_b32 v163, 0
v_mfma_f32_16x16x32_bf16 v[124:127], a[124:127], a[108:111], v[124:127]
s_waitcnt lgkmcnt(0)
v_mov_b32 v164, 0
v_mov_b32 v165, 0
v_mfma_f32_16x16x32_bf16 v[64:67], a[80:83], a[64:67], v[64:67]
ds_read_b64_tr_b16 a[112:113], v245 offset:8192
ds_read_b64_tr_b16 a[114:115], v245 offset:12288
v_mfma_f32_16x16x32_bf16 v[68:71], a[84:87], a[64:67], v[68:71]
ds_read_b128 a[96:99], v244 offset:2048
v_add_u32 v245, s53, v245
v_mfma_f32_16x16x32_bf16 v[72:75], a[88:91], a[64:67], v[72:75]
v_mov_b32 v166, v160
v_mov_b32 v167, v161
v_mfma_f32_16x16x32_bf16 v[76:79], a[92:95], a[64:67], v[76:79]
ds_read_b64_tr_b16 a[116:117], v246 offset:8192
ds_read_b64_tr_b16 a[118:119], v246 offset:12288
v_mfma_f32_16x16x32_bf16 v[80:83], a[80:83], a[68:71], v[80:83]
ds_read_b128 a[100:103], v244 offset:3072
v_add_u32 v246, s53, v246
v_mfma_f32_16x16x32_bf16 v[84:87], a[84:87], a[68:71], v[84:87]
ds_read_b64_tr_b16 a[120:121], v247 offset:8192
ds_read_b64_tr_b16 a[122:123], v247 offset:12288
v_mfma_f32_16x16x32_bf16 v[88:91], a[88:91], a[68:71], v[88:91]
v_mfma_f32_16x16x32_bf16 v[92:95], a[92:95], a[68:71], v[92:95]
ds_read_b128 a[104:107], v244 offset:6144
v_add_u32 v247, s53, v247
v_mfma_f32_16x16x32_bf16 v[96:99], a[80:83], a[72:75], v[96:99]
ds_read_b64_tr_b16 a[124:125], v248 offset:8192
ds_read_b64_tr_b16 a[126:127], v248 offset:12288
v_mfma_f32_16x16x32_bf16 v[100:103], a[84:87], a[72:75], v[100:103]
ds_read_b128 a[108:111], v244 offset:7168
v_add_u32 v248, s53, v248
v_mfma_f32_16x16x32_bf16 v[104:107], a[88:91], a[72:75], v[104:107]
v_mfma_f32_16x16x32_bf16 v[108:111], a[92:95], a[72:75], v[108:111]
s_add_u32 s49, s46, s91
v_add_u32 v244, s53, v244
v_mfma_f32_16x16x32_bf16 v[112:115], a[80:83], a[76:79], v[112:115]
s_add_u32 s46, s46, s53
v_mfma_f32_16x16x32_bf16 v[116:119], a[84:87], a[76:79], v[116:119]
v_mfma_f32_16x16x32_bf16 v[120:123], a[88:91], a[76:79], v[120:123]
v_mfma_f32_16x16x32_bf16 v[124:127], a[92:95], a[76:79], v[124:127]
s_waitcnt vmcnt(17) lgkmcnt(0)
s_barrier
v_mfma_f32_16x16x32_bf16 v[64:67], a[112:115], a[96:99], v[64:67]
ds_read_b64_tr_b16 a[80:81], v245 offset:0
ds_read_b64_tr_b16 a[82:83], v245 offset:4096
v_mfma_f32_16x16x32_bf16 v[68:71], a[116:119], a[96:99], v[68:71]
ds_read_b128 a[64:67], v244 offset:0
v_mfma_f32_16x16x32_bf16 v[72:75], a[120:123], a[96:99], v[72:75]
v_mfma_f32_16x16x32_bf16 v[76:79], a[124:127], a[96:99], v[76:79]
ds_read_b64_tr_b16 a[84:85], v246 offset:0
ds_read_b64_tr_b16 a[86:87], v246 offset:4096
v_mfma_f32_16x16x32_bf16 v[80:83], a[112:115], a[100:103], v[80:83]
ds_read_b128 a[68:71], v244 offset:1024
v_mfma_f32_16x16x32_bf16 v[84:87], a[116:119], a[100:103], v[84:87]
ds_read_b64_tr_b16 a[88:89], v247 offset:0
ds_read_b64_tr_b16 a[90:91], v247 offset:4096
.Lpad20:
s_cbranch_execz .Ltch11
v_mfma_f32_16x16x32_bf16 v[88:91], a[120:123], a[100:103], v[88:91]
v_mfma_f32_16x16x32_bf16 v[92:95], a[124:127], a[100:103], v[92:95]
ds_read_b128 a[72:75], v244 offset:4096
v_mfma_f32_16x16x32_bf16 v[96:99], a[112:115], a[104:107], v[96:99]
ds_read_b64_tr_b16 a[92:93], v248 offset:0
ds_read_b64_tr_b16 a[94:95], v248 offset:4096
v_mfma_f32_16x16x32_bf16 v[100:103], a[116:119], a[104:107], v[100:103]
ds_read_b128 a[76:79], v244 offset:5120
v_mfma_f32_16x16x32_bf16 v[104:107], a[120:123], a[104:107], v[104:107]
s_waitcnt vmcnt(0)
v_and_b32 v250, 7, v250
v_mfma_f32_16x16x32_bf16 v[108:111], a[124:127], a[104:107], v[108:111]
s_cmp_eq_u32 s46, 0
s_cselect_b32 s53, s56, s57
v_cmp_gt_u32 vcc, 5, v250
v_mfma_f32_16x16x32_bf16 v[112:115], a[112:115], a[108:111], v[112:115]
v_mfma_f32_16x16x32_bf16 v[116:119], a[116:119], a[108:111], v[116:119]
s_cmp_lg_u64 vcc, 0
s_cbranch_scc1 .Lspin5
.Lspin5_ret:
v_mfma_f32_16x16x32_bf16 v[120:123], a[120:123], a[108:111], v[120:123]
v_mfma_f32_16x16x32_bf16 v[124:127], a[124:127], a[108:111], v[124:127]
s_waitcnt lgkmcnt(0)
v_mfma_f32_16x16x32_bf16 v[64:67], a[80:83], a[64:67], v[64:67]
ds_read_b64_tr_b16 a[112:113], v245 offset:8192
ds_read_b64_tr_b16 a[114:115], v245 offset:12288
v_mfma_f32_16x16x32_bf16 v[68:71], a[84:87], a[64:67], v[68:71]
buffer_load_dwordx4 v[128:131], v7, s[12:15], 0 offen offset:0 sc1
ds_read_b128 a[96:99], v244 offset:2048
v_add_u32 v245, s53, v245
v_mfma_f32_16x16x32_bf16 v[72:75], a[88:91], a[64:67], v[72:75]
v_mfma_f32_16x16x32_bf16 v[76:79], a[92:95], a[64:67], v[76:79]
ds_read_b64_tr_b16 a[116:117], v246 offset:8192
ds_read_b64_tr_b16 a[118:119], v246 offset:12288
v_mfma_f32_16x16x32_bf16 v[80:83], a[80:83], a[68:71], v[80:83]
buffer_load_dwordx4 v[132:135], v7, s[12:15], 0 offen offset:1024 sc1
ds_read_b128 a[100:103], v244 offset:3072
v_add_u32 v246, s53, v246
v_mfma_f32_16x16x32_bf16 v[84:87], a[84:87], a[68:71], v[84:87]
ds_read_b64_tr_b16 a[120:121], v247 offset:8192
ds_read_b64_tr_b16 a[122:123], v247 offset:12288
v_mfma_f32_16x16x32_bf16 v[88:91], a[88:91], a[68:71], v[88:91]
v_mfma_f32_16x16x32_bf16 v[92:95], a[92:95], a[68:71], v[92:95]
buffer_load_dwordx4 v[136:139], v7, s[12:15], 0 offen offset:2048 sc1
ds_read_b128 a[104:107], v244 offset:6144
v_add_u32 v247, s53, v247
v_mfma_f32_16x16x32_bf16 v[96:99], a[80:83], a[72:75], v[96:99]
ds_read_b64_tr_b16 a[124:125], v248 offset:8192
ds_read_b64_tr_b16 a[126:127], v248 offset:12288
v_mfma_f32_16x16x32_bf16 v[100:103], a[84:87], a[72:75], v[100:103]
ds_read_b128 a[108:111], v244 offset:7168
v_add_u32 v248, s53, v248
v_mfma_f32_16x16x32_bf16 v[104:107], a[88:91], a[72:75], v[104:107]
buffer_load_dwordx4 v[140:143], v7, s[12:15], 0 offen offset:3072 sc1
v_mfma_f32_16x16x32_bf16 v[108:111], a[92:95], a[72:75], v[108:111]
s_add_u32 s49, s46, s91
v_add_u32 v244, s53, v244
v_mfma_f32_16x16x32_bf16 v[112:115], a[80:83], a[76:79], v[112:115]
s_add_u32 s46, s46, s53
v_mfma_f32_16x16x32_bf16 v[116:119], a[84:87], a[76:79], v[116:119]
v_mfma_f32_16x16x32_bf16 v[120:123], a[88:91], a[76:79], v[120:123]
v_mfma_f32_16x16x32_bf16 v[124:127], a[92:95], a[76:79], v[124:127]
s_waitcnt vmcnt(13) lgkmcnt(0)
s_barrier
v_mfma_f32_16x16x32_bf16 v[64:67], a[112:115], a[96:99], v[64:67]
ds_read_b64_tr_b16 a[80:81], v245 offset:0
ds_read_b64_tr_b16 a[82:83], v245 offset:4096
v_mfma_f32_16x16x32_bf16 v[68:71], a[116:119], a[96:99], v[68:71]
buffer_load_dwordx4 v[144:147], v7, s[12:15], s32 offen offset:0 sc1
ds_read_b128 a[64:67], v244 offset:0
v_mfma_f32_16x16x32_bf16 v[72:75], a[120:123], a[96:99], v[72:75]
v_mfma_f32_16x16x32_bf16 v[76:79], a[124:127], a[96:99], v[76:79]
ds_read_b64_tr_b16 a[84:85], v246 offset:0
ds_read_b64_tr_b16 a[86:87], v246 offset:4096
v_mfma_f32_16x16x32_bf16 v[80:83], a[112:115], a[100:103], v[80:83]
buffer_load_dwordx4 v[148:151], v7, s[12:15], s32 offen offset:1024 sc1
ds_read_b128 a[68:71], v244 offset:1024
v_mfma_f32_16x16x32_bf16 v[84:87], a[116:119], a[100:103], v[84:87]
ds_read_b64_tr_b16 a[88:89], v247 offset:0
ds_read_b64_tr_b16 a[90:91], v247 offset:4096
v_mfma_f32_16x16x32_bf16 v[88:91], a[120:123], a[100:103], v[88:91]
v_mfma_f32_16x16x32_bf16 v[92:95], a[124:127], a[100:103], v[92:95]
ds_read_b128 a[72:75], v244 offset:4096
v_mfma_f32_16x16x32_bf16 v[96:99], a[112:115], a[104:107], v[96:99]
ds_read_b64_tr_b16 a[92:93], v248 offset:0
ds_read_b64_tr_b16 a[94:95], v248 offset:4096
v_mfma_f32_16x16x32_bf16 v[100:103], a[116:119], a[104:107], v[100:103]
buffer_load_dwordx4 v[152:155], v7, s[12:15], s32 offen offset:2048 sc1
ds_read_b128 a[76:79], v244 offset:5120
v_mfma_f32_16x16x32_bf16 v[104:107], a[120:123], a[104:107], v[104:107]
v_mfma_f32_16x16x32_bf16 v[108:111], a[124:127], a[104:107], v[108:111]
s_cmp_eq_u32 s46, 0
s_cselect_b32 s53, s56, s57
v_mfma_f32_16x16x32_bf16 v[112:115], a[112:115], a[108:111], v[112:115]
buffer_load_dwordx4 v[156:159], v7, s[12:15], s32 offen offset:3072 sc1
v_mfma_f32_16x16x32_bf16 v[116:119], a[116:119], a[108:111], v[116:119]
v_mfma_f32_16x16x32_bf16 v[120:123], a[120:123], a[108:111], v[120:123]
v_mfma_f32_16x16x32_bf16 v[124:127], a[124:127], a[108:111], v[124:127]
s_waitcnt lgkmcnt(0)
v_mfma_f32_16x16x32_bf16 v[64:67], a[80:83], a[64:67], v[64:67]
ds_read_b64_tr_b16 a[112:113], v245 offset:8192
ds_read_b64_tr_b16 a[114:115], v245 offset:12288
v_mfma_f32_16x16x32_bf16 v[68:71], a[84:87], a[64:67], v[68:71]
ds_read_b128 a[96:99], v244 offset:2048
v_add_u32 v245, s53, v245
v_mfma_f32_16x16x32_bf16 v[72:75], a[88:91], a[64:67], v[72:75]
v_mfma_f32_16x16x32_bf16 v[76:79], a[92:95], a[64:67], v[76:79]
ds_read_b64_tr_b16 a[116:117], v246 offset:8192
ds_read_b64_tr_b16 a[118:119], v246 offset:12288
v_mfma_f32_16x16x32_bf16 v[80:83], a[80:83], a[68:71], v[80:83]
ds_read_b128 a[100:103], v244 offset:3072
v_add_u32 v246, s53, v246
v_mfma_f32_16x16x32_bf16 v[84:87], a[84:87], a[68:71], v[84:87]
ds_read_b64_tr_b16 a[120:121], v247 offset:8192
ds_read_b64_tr_b16 a[122:123], v247 offset:12288
v_mfma_f32_16x16x32_bf16 v[88:91], a[88:91], a[68:71], v[88:91]
v_mfma_f32_16x16x32_bf16 v[92:95], a[92:95], a[68:71], v[92:95]
ds_read_b128 a[104:107], v244 offset:6144
v_add_u32 v247, s53, v247
v_mfma_f32_16x16x32_bf16 v[96:99], a[80:83], a[72:75], v[96:99]
ds_read_b64_tr_b16 a[124:125], v248 offset:8192
ds_read_b64_tr_b16 a[126:127], v248 offset:12288
v_mfma_f32_16x16x32_bf16 v[100:103], a[84:87], a[72:75], v[100:103]
ds_read_b128 a[108:111], v244 offset:7168
v_add_u32 v248, s53, v248
v_mfma_f32_16x16x32_bf16 v[104:107], a[88:91], a[72:75], v[104:107]
v_mfma_f32_16x16x32_bf16 v[108:111], a[92:95], a[72:75], v[108:111]
s_add_u32 s49, s46, s91
v_add_u32 v244, s53, v244
v_mfma_f32_16x16x32_bf16 v[112:115], a[80:83], a[76:79], v[112:115]
s_add_u32 s46, s46, s53
v_mfma_f32_16x16x32_bf16 v[116:119], a[84:87], a[76:79], v[116:119]
v_mfma_f32_16x16x32_bf16 v[120:123], a[88:91], a[76:79], v[120:123]
v_mfma_f32_16x16x32_bf16 v[124:127], a[92:95], a[76:79], v[124:127]
s_waitcnt vmcnt(9) lgkmcnt(0)
s_barrier
v_mfma_f32_16x16x32_bf16 v[64:67], a[112:115], a[96:99], v[64:67]
ds_read_b64_tr_b16 a[80:81], v245 offset:0
ds_read_b64_tr_b16 a[82:83], v245 offset:4096
v_mfma_f32_16x16x32_bf16 v[68:71], a[116:119], a[96:99], v[68:71]
ds_read_b128 a[64:67], v244 offset:0
ds_read_b128 a[68:71], v244 offset:1024
v_mfma_f32_16x16x32_bf16 v[72:75], a[120:123], a[96:99], v[72:75]
ds_read_b64_tr_b16 a[84:85], v246 offset:0
ds_read_b64_tr_b16 a[86:87], v246 offset:4096
v_mfma_f32_16x16x32_bf16 v[76:79], a[124:127], a[96:99], v[76:79]
ds_read_b64_tr_b16 a[88:89], v247 offset:0
ds_read_b64_tr_b16 a[90:91], v247 offset:4096
v_mfma_f32_16x16x32_bf16 v[80:83], a[112:115], a[100:103], v[80:83]
ds_read_b64_tr_b16 a[92:93], v248 offset:0
ds_read_b64_tr_b16 a[94:95], v248 offset:4096
v_mfma_f32_16x16x32_bf16 v[84:87], a[116:119], a[100:103], v[84:87]
ds_read_b64_tr_b16 a[144:145], v245 offset:8192
ds_read_b64_tr_b16 a[146:147], v245 offset:12288
v_mfma_f32_16x16x32_bf16 v[88:91], a[120:123], a[100:103], v[88:91]
ds_read_b128 a[128:131], v244 offset:2048
ds_read_b128 a[132:135], v244 offset:3072
v_mfma_f32_16x16x32_bf16 v[92:95], a[124:127], a[100:103], v[92:95]
ds_read_b64_tr_b16 a[148:149], v246 offset:8192
ds_read_b64_tr_b16 a[150:151], v246 offset:12288
v_mfma_f32_16x16x32_bf16 v[96:99], a[112:115], a[104:107], v[96:99]
ds_read_b64_tr_b16 a[152:153], v247 offset:8192
ds_read_b64_tr_b16 a[154:155], v247 offset:12288
v_mfma_f32_16x16x32_bf16 v[100:103], a[116:119], a[104:107], v[100:103]
ds_read_b64_tr_b16 a[156:157], v248 offset:8192
ds_read_b64_tr_b16 a[158:159], v248 offset:12288
v_mfma_f32_16x16x32_bf16 v[104:107], a[120:123], a[104:107], v[104:107]
ds_read_b128 a[72:75], v244 offset:4096
ds_read_b128 a[76:79], v244 offset:5120
v_mfma_f32_16x16x32_bf16 v[108:111], a[124:127], a[104:107], v[108:111]
ds_read_b128 a[136:139], v244 offset:6144
ds_read_b128 a[140:143], v244 offset:7168
v_mfma_f32_16x16x32_bf16 v[112:115], a[112:115], a[108:111], v[112:115]
s_mov_b32 s24, 0
s_mov_b32 s25, 32768
v_mfma_f32_16x16x32_bf16 v[116:119], a[116:119], a[108:111], v[116:119]
s_mov_b32 s26, 65536
s_mov_b32 s27, 98304
v_mfma_f32_16x16x32_bf16 v[120:123], a[120:123], a[108:111], v[120:123]
s_mov_b32 s28, 131072
s_mov_b32 s29, 163840
v_mfma_f32_16x16x32_bf16 v[124:127], a[124:127], a[108:111], v[124:127]
s_mov_b32 s30, 196608
s_mov_b32 s31, 229376
s_waitcnt vmcnt(0) lgkmcnt(0)
v_mfma_f32_16x16x32_bf16 v[64:67], v[160:163], v[128:131], v[64:67]
v_mfma_f32_16x16x32_bf16 v[68:71], v[164:167], v[128:131], v[68:71]
v_mfma_f32_16x16x32_bf16 v[72:75], v[160:163], v[132:135], v[72:75]
v_mfma_f32_16x16x32_bf16 v[76:79], v[164:167], v[132:135], v[76:79]
v_mfma_f32_16x16x32_bf16 v[64:67], a[80:83], a[64:67], v[64:67]
v_mfma_f32_16x16x32_bf16 v[68:71], a[84:87], a[64:67], v[68:71]
v_mfma_f32_16x16x32_bf16 v[72:75], a[88:91], a[64:67], v[72:75]
v_mfma_f32_16x16x32_bf16 v[76:79], a[92:95], a[64:67], v[76:79]
v_mfma_f32_16x16x32_bf16 v[64:67], a[144:147], a[128:131], v[64:67]
v_mfma_f32_16x16x32_bf16 v[68:71], a[148:151], a[128:131], v[68:71]
v_mfma_f32_16x16x32_bf16 v[72:75], a[152:155], a[128:131], v[72:75]
v_mfma_f32_16x16x32_bf16 v[76:79], a[156:159], a[128:131], v[76:79]
v_mfma_f32_16x16x32_bf16 v[80:83], v[160:163], v[136:139], v[80:83]
v_mfma_f32_16x16x32_bf16 v[84:87], v[164:167], v[136:139], v[84:87]
v_mfma_f32_16x16x32_bf16 v[88:91], v[160:163], v[140:143], v[88:91]
s_nop 1
v_cvt_pk_bf16_f32 v192, v64, v65
v_cvt_pk_bf16_f32 v194, v68, v69
v_cvt_pk_bf16_f32 v193, v66, v67
v_mfma_f32_16x16x32_bf16 v[92:95], v[164:167], v[140:143], v[92:95]
v_cvt_pk_bf16_f32 v195, v70, v71
v_cvt_pk_bf16_f32 v196, v72, v73
v_cvt_pk_bf16_f32 v198, v76, v77
v_mfma_f32_16x16x32_bf16 v[80:83], a[80:83], a[68:71], v[80:83]
v_cvt_pk_bf16_f32 v197, v74, v75
v_cvt_pk_bf16_f32 v199, v78, v79
v_mfma_f32_16x16x32_bf16 v[84:87], a[84:87], a[68:71], v[84:87]
v_permlane16_swap_b32 v192, v194
v_mfma_f32_16x16x32_bf16 v[88:91], a[88:91], a[68:71], v[88:91]
v_permlane16_swap_b32 v193, v195
v_mfma_f32_16x16x32_bf16 v[92:95], a[92:95], a[68:71], v[92:95]
v_permlane16_swap_b32 v196, v198
v_mfma_f32_16x16x32_bf16 v[80:83], a[144:147], a[132:135], v[80:83]
v_permlane16_swap_b32 v197, v199
v_mov_b32 v200, v192
v_mfma_f32_16x16x32_bf16 v[84:87], a[148:151], a[132:135], v[84:87]
v_mov_b32 v201, v193
v_mov_b32 v202, v194
v_mov_b32 v203, v195
v_mfma_f32_16x16x32_bf16 v[88:91], a[152:155], a[132:135], v[88:91]
v_mov_b32_dpp v192, v196 row_ror:8 row_mask:0xf bank_mask:0xc
v_mov_b32_dpp v193, v197 row_ror:8 row_mask:0xf bank_mask:0xc
v_mov_b32_dpp v194, v198 row_ror:8 row_mask:0xf bank_mask:0xc
v_mfma_f32_16x16x32_bf16 v[92:95], a[156:159], a[132:135], v[92:95]
v_mov_b32_dpp v195, v199 row_ror:8 row_mask:0xf bank_mask:0xc
v_mfma_f32_16x16x32_bf16 v[96:99], v[160:163], v[144:147], v[96:99]
buffer_store_dwordx4 v[192:195], v251, s[16:19], s24 offen nt
v_mov_b32_dpp v196, v200 row_ror:8 row_mask:0xf bank_mask:0x3
v_mov_b32_dpp v197, v201 row_ror:8 row_mask:0xf bank_mask:0x3
v_mfma_f32_16x16x32_bf16 v[100:103], v[164:167], v[144:147], v[100:103]
v_mov_b32_dpp v198, v202 row_ror:8 row_mask:0xf bank_mask:0x3
v_mov_b32_dpp v199, v203 row_ror:8 row_mask:0xf bank_mask:0x3
v_mfma_f32_16x16x32_bf16 v[104:107], v[160:163], v[148:151], v[104:107]
buffer_store_dwordx4 v[196:199], v251, s[16:19], s25 offen nt
v_cvt_pk_bf16_f32 v204, v80, v81
v_cvt_pk_bf16_f32 v206, v84, v85
v_mfma_f32_16x16x32_bf16 v[108:111], v[164:167], v[148:151], v[108:111]
v_cvt_pk_bf16_f32 v205, v82, v83
v_cvt_pk_bf16_f32 v207, v86, v87
v_cvt_pk_bf16_f32 v208, v88, v89
v_mfma_f32_16x16x32_bf16 v[96:99], a[80:83], a[72:75], v[96:99]
v_cvt_pk_bf16_f32 v210, v92, v93
v_cvt_pk_bf16_f32 v209, v90, v91
v_cvt_pk_bf16_f32 v211, v94, v95
v_mfma_f32_16x16x32_bf16 v[100:103], a[84:87], a[72:75], v[100:103]
v_permlane16_swap_b32 v204, v206
v_mfma_f32_16x16x32_bf16 v[104:107], a[88:91], a[72:75], v[104:107]
v_permlane16_swap_b32 v205, v207
v_mfma_f32_16x16x32_bf16 v[108:111], a[92:95], a[72:75], v[108:111]
v_permlane16_swap_b32 v208, v210
v_mfma_f32_16x16x32_bf16 v[96:99], a[144:147], a[136:139], v[96:99]
v_permlane16_swap_b32 v209, v211
v_mov_b32 v212, v204
v_mfma_f32_16x16x32_bf16 v[100:103], a[148:151], a[136:139], v[100:103]
v_mov_b32 v213, v205
v_mov_b32 v214, v206
v_mov_b32 v215, v207
v_mfma_f32_16x16x32_bf16 v[104:107], a[152:155], a[136:139], v[104:107]
v_mov_b32_dpp v204, v208 row_ror:8 row_mask:0xf bank_mask:0xc
v_mov_b32_dpp v205, v209 row_ror:8 row_mask:0xf bank_mask:0xc
v_mov_b32_dpp v206, v210 row_ror:8 row_mask:0xf bank_mask:0xc
v_mfma_f32_16x16x32_bf16 v[108:111], a[156:159], a[136:139], v[108:111]
v_mov_b32_dpp v207, v211 row_ror:8 row_mask:0xf bank_mask:0xc
v_mfma_f32_16x16x32_bf16 v[112:115], v[160:163], v[152:155], v[112:115]
buffer_store_dwordx4 v[204:207], v251, s[16:19], s26 offen nt
v_mov_b32_dpp v208, v212 row_ror:8 row_mask:0xf bank_mask:0x3
v_mov_b32_dpp v209, v213 row_ror:8 row_mask:0xf bank_mask:0x3
v_mfma_f32_16x16x32_bf16 v[116:119], v[164:167], v[152:155], v[116:119]
v_mov_b32_dpp v210, v214 row_ror:8 row_mask:0xf bank_mask:0x3
v_mov_b32_dpp v211, v215 row_ror:8 row_mask:0xf bank_mask:0x3
v_mfma_f32_16x16x32_bf16 v[120:123], v[160:163], v[156:159], v[120:123]
buffer_store_dwordx4 v[208:211], v251, s[16:19], s27 offen nt
v_cvt_pk_bf16_f32 v192, v96, v97
v_cvt_pk_bf16_f32 v194, v100, v101
v_mfma_f32_16x16x32_bf16 v[124:127], v[164:167], v[156:159], v[124:127]
v_cvt_pk_bf16_f32 v193, v98, v99
v_cvt_pk_bf16_f32 v195, v102, v103
v_cvt_pk_bf16_f32 v196, v104, v105
v_mfma_f32_16x16x32_bf16 v[112:115], a[80:83], a[76:79], v[112:115]
v_cvt_pk_bf16_f32 v198, v108, v109
v_cvt_pk_bf16_f32 v197, v106, v107
v_cvt_pk_bf16_f32 v199, v110, v111
v_mfma_f32_16x16x32_bf16 v[116:119], a[84:87], a[76:79], v[116:119]
v_permlane16_swap_b32 v192, v194
v_mfma_f32_16x16x32_bf16 v[120:123], a[88:91], a[76:79], v[120:123]
v_permlane16_swap_b32 v193, v195
v_mfma_f32_16x16x32_bf16 v[124:127], a[92:95], a[76:79], v[124:127]
v_permlane16_swap_b32 v196, v198
v_mfma_f32_16x16x32_bf16 v[112:115], a[144:147], a[140:143], v[112:115]
v_permlane16_swap_b32 v197, v199
v_mov_b32 v200, v192
v_mfma_f32_16x16x32_bf16 v[116:119], a[148:151], a[140:143], v[116:119]
v_mov_b32 v201, v193
v_mov_b32 v202, v194
v_mov_b32 v203, v195
v_mfma_f32_16x16x32_bf16 v[120:123], a[152:155], a[140:143], v[120:123]
v_mov_b32_dpp v192, v196 row_ror:8 row_mask:0xf bank_mask:0xc
v_mov_b32_dpp v193, v197 row_ror:8 row_mask:0xf bank_mask:0xc
v_mov_b32_dpp v194, v198 row_ror:8 row_mask:0xf bank_mask:0xc
v_mfma_f32_16x16x32_bf16 v[124:127], a[156:159], a[140:143], v[124:127]
v_mov_b32_dpp v195, v199 row_ror:8 row_mask:0xf bank_mask:0xc
buffer_store_dwordx4 v[192:195], v251, s[16:19], s28 offen nt
v_mov_b32_dpp v196, v200 row_ror:8 row_mask:0xf bank_mask:0x3
v_mov_b32_dpp v197, v201 row_ror:8 row_mask:0xf bank_mask:0x3
v_mov_b32_dpp v198, v202 row_ror:8 row_mask:0xf bank_mask:0x3
v_mov_b32_dpp v199, v203 row_ror:8 row_mask:0xf bank_mask:0x3
buffer_store_dwordx4 v[196:199], v251, s[16:19], s29 offen nt
v_cvt_pk_bf16_f32 v204, v112, v113
v_cvt_pk_bf16_f32 v206, v116, v117
v_cvt_pk_bf16_f32 v205, v114, v115
v_cvt_pk_bf16_f32 v207, v118, v119
v_cvt_pk_bf16_f32 v208, v120, v121
v_cvt_pk_bf16_f32 v210, v124, v125
v_cvt_pk_bf16_f32 v209, v122, v123
v_cvt_pk_bf16_f32 v211, v126, v127
v_permlane16_swap_b32 v204, v206
v_permlane16_swap_b32 v205, v207
v_permlane16_swap_b32 v208, v210
v_permlane16_swap_b32 v209, v211
v_mov_b32 v212, v204
v_mov_b32 v213, v205
v_mov_b32 v214, v206
v_mov_b32 v215, v207
v_mov_b32_dpp v204, v208 row_ror:8 row_mask:0xf bank_mask:0xc
v_mov_b32_dpp v205, v209 row_ror:8 row_mask:0xf bank_mask:0xc
v_mov_b32_dpp v206, v210 row_ror:8 row_mask:0xf bank_mask:0xc
v_mov_b32_dpp v207, v211 row_ror:8 row_mask:0xf bank_mask:0xc
buffer_store_dwordx4 v[204:207], v251, s[16:19], s30 offen nt
v_mov_b32_dpp v208, v212 row_ror:8 row_mask:0xf bank_mask:0x3
v_mov_b32_dpp v209, v213 row_ror:8 row_mask:0xf bank_mask:0x3
v_mov_b32_dpp v210, v214 row_ror:8 row_mask:0xf bank_mask:0x3
v_mov_b32_dpp v211, v215 row_ror:8 row_mask:0xf bank_mask:0x3
buffer_store_dwordx4 v[208:211], v251, s[16:19], s31 offen nt
s_branch .Lend
.L5odd_pad:
s_cbranch_execz .Ltch11
.L5odd:
v_mfma_f32_16x16x32_bf16 v[64:67], a[112:115], a[96:99], v[64:67]
ds_read_b64_tr_b16 a[80:81], v245 offset:0
ds_read_b64_tr_b16 a[82:83], v245 offset:4096
v_mfma_f32_16x16x32_bf16 v[68:71], a[116:119], a[96:99], v[68:71]
s_add_u32 m0, s49, 16384
ds_read_b128 a[64:67], v244 offset:0
v_mfma_f32_16x16x32_bf16 v[72:75], a[120:123], a[96:99], v[72:75]
buffer_load_dwordx4 v240, s[8:11], s45 offen offset:0 lds
v_mfma_f32_16x16x32_bf16 v[76:79], a[124:127], a[96:99], v[76:79]
ds_read_b64_tr_b16 a[84:85], v246 offset:0
ds_read_b64_tr_b16 a[86:87], v246 offset:4096
v_mfma_f32_16x16x32_bf16 v[80:83], a[112:115], a[100:103], v[80:83]
s_add_u32 s44, s44, 4096
ds_read_b128 a[68:71], v244 offset:1024
v_mfma_f32_16x16x32_bf16 v[84:87], a[116:119], a[100:103], v[84:87]
ds_read_b64_tr_b16 a[88:89], v247 offset:0
ds_read_b64_tr_b16 a[90:91], v247 offset:4096
v_mfma_f32_16x16x32_bf16 v[88:91], a[120:123], a[100:103], v[88:91]
buffer_load_dwordx4 v241, s[8:11], s45 offen offset:1024 lds
v_mfma_f32_16x16x32_bf16 v[92:95], a[124:127], a[100:103], v[92:95]
buffer_load_dword v250, v249, s[60:63], s90 offen sc1
ds_read_b128 a[72:75], v244 offset:4096
v_mfma_f32_16x16x32_bf16 v[96:99], a[112:115], a[104:107], v[96:99]
ds_read_b64_tr_b16 a[92:93], v248 offset:0
ds_read_b64_tr_b16 a[94:95], v248 offset:4096
v_mfma_f32_16x16x32_bf16 v[100:103], a[116:119], a[104:107], v[100:103]
ds_read_b128 a[76:79], v244 offset:5120
v_mfma_f32_16x16x32_bf16 v[104:107], a[120:123], a[104:107], v[104:107]
buffer_load_dwordx4 v242, s[8:11], s45 offen offset:2048 lds
v_mfma_f32_16x16x32_bf16 v[108:111], a[124:127], a[104:107], v[108:111]
s_cmp_eq_u32 s46, 0
s_cselect_b32 s53, s56, s57
v_mfma_f32_16x16x32_bf16 v[112:115], a[112:115], a[108:111], v[112:115]
v_mfma_f32_16x16x32_bf16 v[116:119], a[116:119], a[108:111], v[116:119]
v_mfma_f32_16x16x32_bf16 v[120:123], a[120:123], a[108:111], v[120:123]
buffer_load_dwordx4 v243, s[8:11], s45 offen offset:3072 lds
v_mfma_f32_16x16x32_bf16 v[124:127], a[124:127], a[108:111], v[124:127]
s_waitcnt lgkmcnt(0)
s_mov_b32 m0, s49
s_add_u32 s45, s45, 131072
v_mfma_f32_16x16x32_bf16 v[64:67], a[80:83], a[64:67], v[64:67]
ds_read_b64_tr_b16 a[112:113], v245 offset:8192
ds_read_b64_tr_b16 a[114:115], v245 offset:12288
v_mfma_f32_16x16x32_bf16 v[68:71], a[84:87], a[64:67], v[68:71]
ds_read_b128 a[96:99], v244 offset:2048
v_add_u32 v245, s53, v245
v_mfma_f32_16x16x32_bf16 v[72:75], a[88:91], a[64:67], v[72:75]
buffer_load_dwordx4 v7, s[4:7], s44 offen offset:0 sc1 lds
v_mfma_f32_16x16x32_bf16 v[76:79], a[92:95], a[64:67], v[76:79]
ds_read_b64_tr_b16 a[116:117], v246 offset:8192
ds_read_b64_tr_b16 a[118:119], v246 offset:12288
v_mfma_f32_16x16x32_bf16 v[80:83], a[80:83], a[68:71], v[80:83]
ds_read_b128 a[100:103], v244 offset:3072
v_add_u32 v246, s53, v246
v_mfma_f32_16x16x32_bf16 v[84:87], a[84:87], a[68:71], v[84:87]
ds_read_b64_tr_b16 a[120:121], v247 offset:8192
ds_read_b64_tr_b16 a[122:123], v247 offset:12288
v_mfma_f32_16x16x32_bf16 v[88:91], a[88:91], a[68:71], v[88:91]
buffer_load_dwordx4 v7, s[4:7], s44 offen offset:1024 sc1 lds
v_mfma_f32_16x16x32_bf16 v[92:95], a[92:95], a[68:71], v[92:95]
ds_read_b128 a[104:107], v244 offset:6144
v_add_u32 v247, s53, v247
v_mfma_f32_16x16x32_bf16 v[96:99], a[80:83], a[72:75], v[96:99]
ds_read_b64_tr_b16 a[124:125], v248 offset:8192
ds_read_b64_tr_b16 a[126:127], v248 offset:12288
v_mfma_f32_16x16x32_bf16 v[100:103], a[84:87], a[72:75], v[100:103]
ds_read_b128 a[108:111], v244 offset:7168
v_add_u32 v248, s53, v248
v_mfma_f32_16x16x32_bf16 v[104:107], a[88:91], a[72:75], v[104:107]
buffer_load_dwordx4 v7, s[4:7], s44 offen offset:2048 sc1 lds
v_mfma_f32_16x16x32_bf16 v[108:111], a[92:95], a[72:75], v[108:111]
s_add_u32 s49, s46, s91
v_add_u32 v244, s53, v244
v_mfma_f32_16x16x32_bf16 v[112:115], a[80:83], a[76:79], v[112:115]
s_add_u32 s46, s46, s53
v_mfma_f32_16x16x32_bf16 v[116:119], a[84:87], a[76:79], v[116:119]
v_mfma_f32_16x16x32_bf16 v[120:123], a[88:91], a[76:79], v[120:123]
buffer_load_dwordx4 v7, s[4:7], s44 offen offset:3072 sc1 lds
v_mfma_f32_16x16x32_bf16 v[124:127], a[92:95], a[76:79], v[124:127]
s_waitcnt vmcnt(25) lgkmcnt(0)
s_barrier
v_mfma_f32_16x16x32_bf16 v[64:67], a[112:115], a[96:99], v[64:67]
ds_read_b64_tr_b16 a[80:81], v245 offset:0
ds_read_b64_tr_b16 a[82:83], v245 offset:4096
v_mfma_f32_16x16x32_bf16 v[68:71], a[116:119], a[96:99], v[68:71]
s_add_u32 m0, s49, 16384
ds_read_b128 a[64:67], v244 offset:0
v_mfma_f32_16x16x32_bf16 v[72:75], a[120:123], a[96:99], v[72:75]
buffer_load_dwordx4 v240, s[8:11], s45 offen offset:0 lds
v_mfma_f32_16x16x32_bf16 v[76:79], a[124:127], a[96:99], v[76:79]
ds_read_b64_tr_b16 a[84:85], v246 offset:0
ds_read_b64_tr_b16 a[86:87], v246 offset:4096
v_mfma_f32_16x16x32_bf16 v[80:83], a[112:115], a[100:103], v[80:83]
s_add_u32 s44, s44, 4096
ds_read_b128 a[68:71], v244 offset:1024
v_mfma_f32_16x16x32_bf16 v[84:87], a[116:119], a[100:103], v[84:87]
ds_read_b64_tr_b16 a[88:89], v247 offset:0
ds_read_b64_tr_b16 a[90:91], v247 offset:4096
v_mfma_f32_16x16x32_bf16 v[88:91], a[120:123], a[100:103], v[88:91]
buffer_load_dwordx4 v241, s[8:11], s45 offen offset:1024 lds
v_mfma_f32_16x16x32_bf16 v[92:95], a[124:127], a[100:103], v[92:95]
ds_read_b128 a[72:75], v244 offset:4096
v_mfma_f32_16x16x32_bf16 v[96:99], a[112:115], a[104:107], v[96:99]
ds_read_b64_tr_b16 a[92:93], v248 offset:0
ds_read_b64_tr_b16 a[94:95], v248 offset:4096
v_mfma_f32_16x16x32_bf16 v[100:103], a[116:119], a[104:107], v[100:103]
ds_read_b128 a[76:79], v244 offset:5120
v_mfma_f32_16x16x32_bf16 v[104:107], a[120:123], a[104:107], v[104:107]
buffer_load_dwordx4 v242, s[8:11], s45 offen offset:2048 lds
v_mfma_f32_16x16x32_bf16 v[108:111], a[124:127], a[104:107], v[108:111]
s_cmp_eq_u32 s46, 0
s_cselect_b32 s53, s56, s57
v_mfma_f32_16x16x32_bf16 v[112:115], a[112:115], a[108:111], v[112:115]
v_mfma_f32_16x16x32_bf16 v[116:119], a[116:119], a[108:111], v[116:119]
v_mfma_f32_16x16x32_bf16 v[120:123], a[120:123], a[108:111], v[120:123]
buffer_load_dwordx4 v243, s[8:11], s45 offen offset:3072 lds
v_mfma_f32_16x16x32_bf16 v[124:127], a[124:127], a[108:111], v[124:127]
s_waitcnt lgkmcnt(0)
s_mov_b32 m0, s49
s_add_u32 s45, s45, 131072
v_mfma_f32_16x16x32_bf16 v[64:67], a[80:83], a[64:67], v[64:67]
ds_read_b64_tr_b16 a[112:113], v245 offset:8192
ds_read_b64_tr_b16 a[114:115], v245 offset:12288
v_mfma_f32_16x16x32_bf16 v[68:71], a[84:87], a[64:67], v[68:71]
ds_read_b128 a[96:99], v244 offset:2048
v_add_u32 v245, s53, v245
v_mfma_f32_16x16x32_bf16 v[72:75], a[88:91], a[64:67], v[72:75]
buffer_load_dwordx4 v7, s[4:7], s44 offen offset:0 sc1 lds
v_mfma_f32_16x16x32_bf16 v[76:79], a[92:95], a[64:67], v[76:79]
ds_read_b64_tr_b16 a[116:117], v246 offset:8192
ds_read_b64_tr_b16 a[118:119], v246 offset:12288
v_mfma_f32_16x16x32_bf16 v[80:83], a[80:83], a[68:71], v[80:83]
ds_read_b128 a[100:103], v244 offset:3072
v_add_u32 v246, s53, v246
v_mfma_f32_16x16x32_bf16 v[84:87], a[84:87], a[68:71], v[84:87]
ds_read_b64_tr_b16 a[120:121], v247 offset:8192
ds_read_b64_tr_b16 a[122:123], v247 offset:12288
v_mfma_f32_16x16x32_bf16 v[88:91], a[88:91], a[68:71], v[88:91]
buffer_load_dwordx4 v7, s[4:7], s44 offen offset:1024 sc1 lds
v_mfma_f32_16x16x32_bf16 v[92:95], a[92:95], a[68:71], v[92:95]
ds_read_b128 a[104:107], v244 offset:6144
v_add_u32 v247, s53, v247
v_mfma_f32_16x16x32_bf16 v[96:99], a[80:83], a[72:75], v[96:99]
ds_read_b64_tr_b16 a[124:125], v248 offset:8192
ds_read_b64_tr_b16 a[126:127], v248 offset:12288
v_mfma_f32_16x16x32_bf16 v[100:103], a[84:87], a[72:75], v[100:103]
ds_read_b128 a[108:111], v244 offset:7168
v_add_u32 v248, s53, v248
v_mfma_f32_16x16x32_bf16 v[104:107], a[88:91], a[72:75], v[104:107]
buffer_load_dwordx4 v7, s[4:7], s44 offen offset:2048 sc1 lds
v_mfma_f32_16x16x32_bf16 v[108:111], a[92:95], a[72:75], v[108:111]
s_add_u32 s49, s46, s91
v_add_u32 v244, s53, v244
v_mfma_f32_16x16x32_bf16 v[112:115], a[80:83], a[76:79], v[112:115]
s_add_u32 s46, s46, s53
v_mfma_f32_16x16x32_bf16 v[116:119], a[84:87], a[76:79], v[116:119]
v_mfma_f32_16x16x32_bf16 v[120:123], a[88:91], a[76:79], v[120:123]
buffer_load_dwordx4 v7, s[4:7], s44 offen offset:3072 sc1 lds
v_mfma_f32_16x16x32_bf16 v[124:127], a[92:95], a[76:79], v[124:127]
s_waitcnt vmcnt(25) lgkmcnt(0)
s_barrier
s_lshl_b32 s54, s51, 2
s_add_u32 s54, s54, s3
s_lshl_b32 s54, s54, 16
s_add_u32 s54, s54, 50331648
s_add_u32 s4, s84, s54
s_addc_u32 s5, s85, 0
s_and_b32 s5, s5, 0xffff
s_mov_b32 s6, 65536
s_lshl_b32 s54, s3, 15
s_add_u32 s54, s54, s52
s_sub_u32 s28, 0x200000, s54
s_add_u32 s8, s68, s54
s_addc_u32 s9, s69, 0
s_and_b32 s9, s9, 0xffff
s_mov_b32 s10, s28
s_mov_b32 s44, -4096
s_mov_b32 s45, 0
v_mfma_f32_16x16x32_bf16 v[64:67], a[112:115], a[96:99], v[64:67]
ds_read_b64_tr_b16 a[80:81], v245 offset:0
ds_read_b64_tr_b16 a[82:83], v245 offset:4096
v_mfma_f32_16x16x32_bf16 v[68:71], a[116:119], a[96:99], v[68:71]
s_add_u32 m0, s49, 16384
ds_read_b128 a[64:67], v244 offset:0
v_mfma_f32_16x16x32_bf16 v[72:75], a[120:123], a[96:99], v[72:75]
buffer_load_dwordx4 v240, s[8:11], s45 offen offset:0 lds
v_mfma_f32_16x16x32_bf16 v[76:79], a[124:127], a[96:99], v[76:79]
ds_read_b64_tr_b16 a[84:85], v246 offset:0
ds_read_b64_tr_b16 a[86:87], v246 offset:4096
v_mfma_f32_16x16x32_bf16 v[80:83], a[112:115], a[100:103], v[80:83]
s_add_u32 s44, s44, 4096
ds_read_b128 a[68:71], v244 offset:1024
v_mfma_f32_16x16x32_bf16 v[84:87], a[116:119], a[100:103], v[84:87]
ds_read_b64_tr_b16 a[88:89], v247 offset:0
ds_read_b64_tr_b16 a[90:91], v247 offset:4096
v_mfma_f32_16x16x32_bf16 v[88:91], a[120:123], a[100:103], v[88:91]
buffer_load_dwordx4 v241, s[8:11], s45 offen offset:1024 lds
v_mfma_f32_16x16x32_bf16 v[92:95], a[124:127], a[100:103], v[92:95]
ds_read_b128 a[72:75], v244 offset:4096
v_mfma_f32_16x16x32_bf16 v[96:99], a[112:115], a[104:107], v[96:99]
ds_read_b64_tr_b16 a[92:93], v248 offset:0
ds_read_b64_tr_b16 a[94:95], v248 offset:4096
v_mfma_f32_16x16x32_bf16 v[100:103], a[116:119], a[104:107], v[100:103]
ds_read_b128 a[76:79], v244 offset:5120
v_mfma_f32_16x16x32_bf16 v[104:107], a[120:123], a[104:107], v[104:107]
s_waitcnt vmcnt(16)
buffer_load_dwordx4 v242, s[8:11], s45 offen offset:2048 lds
v_and_b32 v250, 7, v250
v_mfma_f32_16x16x32_bf16 v[108:111], a[124:127], a[104:107], v[108:111]
s_cmp_eq_u32 s46, 0
s_cselect_b32 s53, s56, s57
v_cmp_gt_u32 vcc, 4, v250
v_mfma_f32_16x16x32_bf16 v[112:115], a[112:115], a[108:111], v[112:115]
v_mfma_f32_16x16x32_bf16 v[116:119], a[116:119], a[108:111], v[116:119]
s_cmp_lg_u64 vcc, 0
s_cbranch_scc1 .Lspin4
.Lspin4_ret:
v_mfma_f32_16x16x32_bf16 v[120:123], a[120:123], a[108:111], v[120:123]
buffer_load_dwordx4 v243, s[8:11], s45 offen offset:3072 lds
v_mfma_f32_16x16x32_bf16 v[124:127], a[124:127], a[108:111], v[124:127]
s_waitcnt lgkmcnt(0)
s_mov_b32 m0, s49
s_add_u32 s45, s45, 131072
v_mfma_f32_16x16x32_bf16 v[64:67], a[80:83], a[64:67], v[64:67]
ds_read_b64_tr_b16 a[112:113], v245 offset:8192
ds_read_b64_tr_b16 a[114:115], v245 offset:12288
v_mfma_f32_16x16x32_bf16 v[68:71], a[84:87], a[64:67], v[68:71]
ds_read_b128 a[96:99], v244 offset:2048
v_add_u32 v245, s53, v245
v_mfma_f32_16x16x32_bf16 v[72:75], a[88:91], a[64:67], v[72:75]
buffer_load_dwordx4 v7, s[4:7], s44 offen offset:0 sc1 lds
v_mfma_f32_16x16x32_bf16 v[76:79], a[92:95], a[64:67], v[76:79]
ds_read_b64_tr_b16 a[116:117], v246 offset:8192
ds_read_b64_tr_b16 a[118:119], v246 offset:12288
.Lpad21:
s_cbranch_execz .Ltch11
v_mfma_f32_16x16x32_bf16 v[80:83], a[80:83], a[68:71], v[80:83]
ds_read_b128 a[100:103], v244 offset:3072
v_add_u32 v246, s53, v246
v_mfma_f32_16x16x32_bf16 v[84:87], a[84:87], a[68:71], v[84:87]
ds_read_b64_tr_b16 a[120:121], v247 offset:8192
ds_read_b64_tr_b16 a[122:123], v247 offset:12288
v_mfma_f32_16x16x32_bf16 v[88:91], a[88:91], a[68:71], v[88:91]
buffer_load_dwordx4 v7, s[4:7], s44 offen offset:1024 sc1 lds
v_mfma_f32_16x16x32_bf16 v[92:95], a[92:95], a[68:71], v[92:95]
ds_read_b128 a[104:107], v244 offset:6144
v_add_u32 v247, s53, v247
v_mfma_f32_16x16x32_bf16 v[96:99], a[80:83], a[72:75], v[96:99]
ds_read_b64_tr_b16 a[124:125], v248 offset:8192
ds_read_b64_tr_b16 a[126:127], v248 offset:12288
v_mfma_f32_16x16x32_bf16 v[100:103], a[84:87], a[72:75], v[100:103]
ds_read_b128 a[108:111], v244 offset:7168
v_add_u32 v248, s53, v248
v_mfma_f32_16x16x32_bf16 v[104:107], a[88:91], a[72:75], v[104:107]
buffer_load_dwordx4 v7, s[4:7], s44 offen offset:2048 sc1 lds
v_mfma_f32_16x16x32_bf16 v[108:111], a[92:95], a[72:75], v[108:111]
s_add_u32 s49, s46, s91
v_add_u32 v244, s53, v244
v_mfma_f32_16x16x32_bf16 v[112:115], a[80:83], a[76:79], v[112:115]
s_add_u32 s46, s46, s53
v_mfma_f32_16x16x32_bf16 v[116:119], a[84:87], a[76:79], v[116:119]
v_mfma_f32_16x16x32_bf16 v[120:123], a[88:91], a[76:79], v[120:123]
buffer_load_dwordx4 v7, s[4:7], s44 offen offset:3072 sc1 lds
v_mfma_f32_16x16x32_bf16 v[124:127], a[92:95], a[76:79], v[124:127]
s_waitcnt vmcnt(25) lgkmcnt(0)
s_barrier
v_mfma_f32_16x16x32_bf16 v[64:67], a[112:115], a[96:99], v[64:67]
ds_read_b64_tr_b16 a[80:81], v245 offset:0
ds_read_b64_tr_b16 a[82:83], v245 offset:4096
v_mfma_f32_16x16x32_bf16 v[68:71], a[116:119], a[96:99], v[68:71]
s_add_u32 m0, s49, 16384
ds_read_b128 a[64:67], v244 offset:0
v_mfma_f32_16x16x32_bf16 v[72:75], a[120:123], a[96:99], v[72:75]
buffer_load_dwordx4 v240, s[8:11], s45 offen offset:0 lds
v_mfma_f32_16x16x32_bf16 v[76:79], a[124:127], a[96:99], v[76:79]
ds_read_b64_tr_b16 a[84:85], v246 offset:0
ds_read_b64_tr_b16 a[86:87], v246 offset:4096
v_mfma_f32_16x16x32_bf16 v[80:83], a[112:115], a[100:103], v[80:83]
s_add_u32 s44, s44, 4096
ds_read_b128 a[68:71], v244 offset:1024
v_mfma_f32_16x16x32_bf16 v[84:87], a[116:119], a[100:103], v[84:87]
ds_read_b64_tr_b16 a[88:89], v247 offset:0
ds_read_b64_tr_b16 a[90:91], v247 offset:4096
v_mfma_f32_16x16x32_bf16 v[88:91], a[120:123], a[100:103], v[88:91]
buffer_load_dwordx4 v241, s[8:11], s45 offen offset:1024 lds
v_mfma_f32_16x16x32_bf16 v[92:95], a[124:127], a[100:103], v[92:95]
ds_read_b128 a[72:75], v244 offset:4096
v_mfma_f32_16x16x32_bf16 v[96:99], a[112:115], a[104:107], v[96:99]
ds_read_b64_tr_b16 a[92:93], v248 offset:0
ds_read_b64_tr_b16 a[94:95], v248 offset:4096
v_mfma_f32_16x16x32_bf16 v[100:103], a[116:119], a[104:107], v[100:103]
ds_read_b128 a[76:79], v244 offset:5120
v_mfma_f32_16x16x32_bf16 v[104:107], a[120:123], a[104:107], v[104:107]
buffer_load_dwordx4 v242, s[8:11], s45 offen offset:2048 lds
v_mfma_f32_16x16x32_bf16 v[108:111], a[124:127], a[104:107], v[108:111]
s_cmp_eq_u32 s46, 0
s_cselect_b32 s53, s56, s57
v_mfma_f32_16x16x32_bf16 v[112:115], a[112:115], a[108:111], v[112:115]
v_mfma_f32_16x16x32_bf16 v[116:119], a[116:119], a[108:111], v[116:119]
v_mfma_f32_16x16x32_bf16 v[120:123], a[120:123], a[108:111], v[120:123]
buffer_load_dwordx4 v243, s[8:11], s45 offen offset:3072 lds
v_mfma_f32_16x16x32_bf16 v[124:127], a[124:127], a[108:111], v[124:127]
s_waitcnt lgkmcnt(0)
s_mov_b32 m0, s49
s_add_u32 s45, s45, 131072
v_mfma_f32_16x16x32_bf16 v[64:67], a[80:83], a[64:67], v[64:67]
ds_read_b64_tr_b16 a[112:113], v245 offset:8192
ds_read_b64_tr_b16 a[114:115], v245 offset:12288
v_mfma_f32_16x16x32_bf16 v[68:71], a[84:87], a[64:67], v[68:71]
ds_read_b128 a[96:99], v244 offset:2048
v_add_u32 v245, s53, v245
v_mfma_f32_16x16x32_bf16 v[72:75], a[88:91], a[64:67], v[72:75]
buffer_load_dwordx4 v7, s[4:7], s44 offen offset:0 sc1 lds
v_mfma_f32_16x16x32_bf16 v[76:79], a[92:95], a[64:67], v[76:79]
ds_read_b64_tr_b16 a[116:117], v246 offset:8192
ds_read_b64_tr_b16 a[118:119], v246 offset:12288
v_mfma_f32_16x16x32_bf16 v[80:83], a[80:83], a[68:71], v[80:83]
ds_read_b128 a[100:103], v244 offset:3072
v_add_u32 v246, s53, v246
v_mfma_f32_16x16x32_bf16 v[84:87], a[84:87], a[68:71], v[84:87]
ds_read_b64_tr_b16 a[120:121], v247 offset:8192
ds_read_b64_tr_b16 a[122:123], v247 offset:12288
v_mfma_f32_16x16x32_bf16 v[88:91], a[88:91], a[68:71], v[88:91]
buffer_load_dwordx4 v7, s[4:7], s44 offen offset:1024 sc1 lds
v_mfma_f32_16x16x32_bf16 v[92:95], a[92:95], a[68:71], v[92:95]
ds_read_b128 a[104:107], v244 offset:6144
v_add_u32 v247, s53, v247
v_mfma_f32_16x16x32_bf16 v[96:99], a[80:83], a[72:75], v[96:99]
ds_read_b64_tr_b16 a[124:125], v248 offset:8192
ds_read_b64_tr_b16 a[126:127], v248 offset:12288
v_mfma_f32_16x16x32_bf16 v[100:103], a[84:87], a[72:75], v[100:103]
ds_read_b128 a[108:111], v244 offset:7168
v_add_u32 v248, s53, v248
v_mfma_f32_16x16x32_bf16 v[104:107], a[88:91], a[72:75], v[104:107]
buffer_load_dwordx4 v7, s[4:7], s44 offen offset:2048 sc1 lds
v_mfma_f32_16x16x32_bf16 v[108:111], a[92:95], a[72:75], v[108:111]
s_add_u32 s49, s46, s91
v_add_u32 v244, s53, v244
v_mfma_f32_16x16x32_bf16 v[112:115], a[80:83], a[76:79], v[112:115]
s_add_u32 s46, s46, s53
v_mfma_f32_16x16x32_bf16 v[116:119], a[84:87], a[76:79], v[116:119]
v_mfma_f32_16x16x32_bf16 v[120:123], a[88:91], a[76:79], v[120:123]
buffer_load_dwordx4 v7, s[4:7], s44 offen offset:3072 sc1 lds
v_mfma_f32_16x16x32_bf16 v[124:127], a[92:95], a[76:79], v[124:127]
s_waitcnt vmcnt(24) lgkmcnt(0)
s_barrier
v_mfma_f32_16x16x32_bf16 v[64:67], a[112:115], a[96:99], v[64:67]
ds_read_b64_tr_b16 a[80:81], v245 offset:0
ds_read_b64_tr_b16 a[82:83], v245 offset:4096
v_mfma_f32_16x16x32_bf16 v[68:71], a[116:119], a[96:99], v[68:71]
s_add_u32 m0, s49, 16384
ds_read_b128 a[64:67], v244 offset:0
v_mfma_f32_16x16x32_bf16 v[72:75], a[120:123], a[96:99], v[72:75]
buffer_load_dwordx4 v240, s[8:11], s45 offen offset:0 lds
v_mfma_f32_16x16x32_bf16 v[76:79], a[124:127], a[96:99], v[76:79]
ds_read_b64_tr_b16 a[84:85], v246 offset:0
ds_read_b64_tr_b16 a[86:87], v246 offset:4096
v_mfma_f32_16x16x32_bf16 v[80:83], a[112:115], a[100:103], v[80:83]
s_add_u32 s44, s44, 4096
ds_read_b128 a[68:71], v244 offset:1024
v_mfma_f32_16x16x32_bf16 v[84:87], a[116:119], a[100:103], v[84:87]
ds_read_b64_tr_b16 a[88:89], v247 offset:0
ds_read_b64_tr_b16 a[90:91], v247 offset:4096
v_mfma_f32_16x16x32_bf16 v[88:91], a[120:123], a[100:103], v[88:91]
buffer_load_dwordx4 v241, s[8:11], s45 offen offset:1024 lds
v_mfma_f32_16x16x32_bf16 v[92:95], a[124:127], a[100:103], v[92:95]
ds_read_b128 a[72:75], v244 offset:4096
v_mfma_f32_16x16x32_bf16 v[96:99], a[112:115], a[104:107], v[96:99]
ds_read_b64_tr_b16 a[92:93], v248 offset:0
ds_read_b64_tr_b16 a[94:95], v248 offset:4096
v_mfma_f32_16x16x32_bf16 v[100:103], a[116:119], a[104:107], v[100:103]
ds_read_b128 a[76:79], v244 offset:5120
v_mfma_f32_16x16x32_bf16 v[104:107], a[120:123], a[104:107], v[104:107]
buffer_load_dwordx4 v242, s[8:11], s45 offen offset:2048 lds
v_mfma_f32_16x16x32_bf16 v[108:111], a[124:127], a[104:107], v[108:111]
s_cmp_eq_u32 s46, 0
s_cselect_b32 s53, s56, s57
v_mfma_f32_16x16x32_bf16 v[112:115], a[112:115], a[108:111], v[112:115]
v_mfma_f32_16x16x32_bf16 v[116:119], a[116:119], a[108:111], v[116:119]
v_mfma_f32_16x16x32_bf16 v[120:123], a[120:123], a[108:111], v[120:123]
buffer_load_dwordx4 v243, s[8:11], s45 offen offset:3072 lds
v_mfma_f32_16x16x32_bf16 v[124:127], a[124:127], a[108:111], v[124:127]
s_waitcnt lgkmcnt(0)
s_mov_b32 m0, s49
s_add_u32 s45, s45, 131072
v_mfma_f32_16x16x32_bf16 v[64:67], a[80:83], a[64:67], v[64:67]
ds_read_b64_tr_b16 a[112:113], v245 offset:8192
ds_read_b64_tr_b16 a[114:115], v245 offset:12288
v_mfma_f32_16x16x32_bf16 v[68:71], a[84:87], a[64:67], v[68:71]
ds_read_b128 a[96:99], v244 offset:2048
v_add_u32 v245, s53, v245
v_mfma_f32_16x16x32_bf16 v[72:75], a[88:91], a[64:67], v[72:75]
buffer_load_dwordx4 v7, s[4:7], s44 offen offset:0 sc1 lds
v_mfma_f32_16x16x32_bf16 v[76:79], a[92:95], a[64:67], v[76:79]
ds_read_b64_tr_b16 a[116:117], v246 offset:8192
ds_read_b64_tr_b16 a[118:119], v246 offset:12288
v_mfma_f32_16x16x32_bf16 v[80:83], a[80:83], a[68:71], v[80:83]
ds_read_b128 a[100:103], v244 offset:3072
v_add_u32 v246, s53, v246
v_mfma_f32_16x16x32_bf16 v[84:87], a[84:87], a[68:71], v[84:87]
ds_read_b64_tr_b16 a[120:121], v247 offset:8192
ds_read_b64_tr_b16 a[122:123], v247 offset:12288
v_mfma_f32_16x16x32_bf16 v[88:91], a[88:91], a[68:71], v[88:91]
buffer_load_dwordx4 v7, s[4:7], s44 offen offset:1024 sc1 lds
v_mfma_f32_16x16x32_bf16 v[92:95], a[92:95], a[68:71], v[92:95]
ds_read_b128 a[104:107], v244 offset:6144
v_add_u32 v247, s53, v247
v_mfma_f32_16x16x32_bf16 v[96:99], a[80:83], a[72:75], v[96:99]
ds_read_b64_tr_b16 a[124:125], v248 offset:8192
ds_read_b64_tr_b16 a[126:127], v248 offset:12288
v_mfma_f32_16x16x32_bf16 v[100:103], a[84:87], a[72:75], v[100:103]
ds_read_b128 a[108:111], v244 offset:7168
v_add_u32 v248, s53, v248
v_mfma_f32_16x16x32_bf16 v[104:107], a[88:91], a[72:75], v[104:107]
buffer_load_dwordx4 v7, s[4:7], s44 offen offset:2048 sc1 lds
v_mfma_f32_16x16x32_bf16 v[108:111], a[92:95], a[72:75], v[108:111]
s_add_u32 s49, s46, s91
v_add_u32 v244, s53, v244
v_mfma_f32_16x16x32_bf16 v[112:115], a[80:83], a[76:79], v[112:115]
s_add_u32 s46, s46, s53
v_mfma_f32_16x16x32_bf16 v[116:119], a[84:87], a[76:79], v[116:119]
v_mfma_f32_16x16x32_bf16 v[120:123], a[88:91], a[76:79], v[120:123]
buffer_load_dwordx4 v7, s[4:7], s44 offen offset:3072 sc1 lds
v_mfma_f32_16x16x32_bf16 v[124:127], a[92:95], a[76:79], v[124:127]
s_waitcnt vmcnt(24) lgkmcnt(0)
s_barrier
v_mfma_f32_16x16x32_bf16 v[64:67], a[112:115], a[96:99], v[64:67]
ds_read_b64_tr_b16 a[80:81], v245 offset:0
ds_read_b64_tr_b16 a[82:83], v245 offset:4096
v_mfma_f32_16x16x32_bf16 v[68:71], a[116:119], a[96:99], v[68:71]
s_add_u32 m0, s49, 16384
ds_read_b128 a[64:67], v244 offset:0
v_mfma_f32_16x16x32_bf16 v[72:75], a[120:123], a[96:99], v[72:75]
buffer_load_dwordx4 v240, s[8:11], s45 offen offset:0 lds
v_mfma_f32_16x16x32_bf16 v[76:79], a[124:127], a[96:99], v[76:79]
ds_read_b64_tr_b16 a[84:85], v246 offset:0
ds_read_b64_tr_b16 a[86:87], v246 offset:4096
v_mfma_f32_16x16x32_bf16 v[80:83], a[112:115], a[100:103], v[80:83]
s_add_u32 s44, s44, 4096
ds_read_b128 a[68:71], v244 offset:1024
v_mfma_f32_16x16x32_bf16 v[84:87], a[116:119], a[100:103], v[84:87]
ds_read_b64_tr_b16 a[88:89], v247 offset:0
ds_read_b64_tr_b16 a[90:91], v247 offset:4096
v_mfma_f32_16x16x32_bf16 v[88:91], a[120:123], a[100:103], v[88:91]
buffer_load_dwordx4 v241, s[8:11], s45 offen offset:1024 lds
v_mfma_f32_16x16x32_bf16 v[92:95], a[124:127], a[100:103], v[92:95]
ds_read_b128 a[72:75], v244 offset:4096
v_mfma_f32_16x16x32_bf16 v[96:99], a[112:115], a[104:107], v[96:99]
ds_read_b64_tr_b16 a[92:93], v248 offset:0
ds_read_b64_tr_b16 a[94:95], v248 offset:4096
v_mfma_f32_16x16x32_bf16 v[100:103], a[116:119], a[104:107], v[100:103]
ds_read_b128 a[76:79], v244 offset:5120
v_mfma_f32_16x16x32_bf16 v[104:107], a[120:123], a[104:107], v[104:107]
buffer_load_dwordx4 v242, s[8:11], s45 offen offset:2048 lds
v_mfma_f32_16x16x32_bf16 v[108:111], a[124:127], a[104:107], v[108:111]
s_cmp_eq_u32 s46, 0
s_cselect_b32 s53, s56, s57
v_mfma_f32_16x16x32_bf16 v[112:115], a[112:115], a[108:111], v[112:115]
v_mfma_f32_16x16x32_bf16 v[116:119], a[116:119], a[108:111], v[116:119]
v_mfma_f32_16x16x32_bf16 v[120:123], a[120:123], a[108:111], v[120:123]
buffer_load_dwordx4 v243, s[8:11], s45 offen offset:3072 lds
v_mfma_f32_16x16x32_bf16 v[124:127], a[124:127], a[108:111], v[124:127]
s_waitcnt lgkmcnt(0)
s_mov_b32 m0, s49
s_add_u32 s45, s45, 131072
v_mfma_f32_16x16x32_bf16 v[64:67], a[80:83], a[64:67], v[64:67]
ds_read_b64_tr_b16 a[112:113], v245 offset:8192
ds_read_b64_tr_b16 a[114:115], v245 offset:12288
v_mfma_f32_16x16x32_bf16 v[68:71], a[84:87], a[64:67], v[68:71]
ds_read_b128 a[96:99], v244 offset:2048
v_add_u32 v245, s53, v245
v_mfma_f32_16x16x32_bf16 v[72:75], a[88:91], a[64:67], v[72:75]
buffer_load_dwordx4 v7, s[4:7], s44 offen offset:0 sc1 lds
v_mfma_f32_16x16x32_bf16 v[76:79], a[92:95], a[64:67], v[76:79]
ds_read_b64_tr_b16 a[116:117], v246 offset:8192
ds_read_b64_tr_b16 a[118:119], v246 offset:12288
v_mfma_f32_16x16x32_bf16 v[80:83], a[80:83], a[68:71], v[80:83]
ds_read_b128 a[100:103], v244 offset:3072
v_add_u32 v246, s53, v246
v_mfma_f32_16x16x32_bf16 v[84:87], a[84:87], a[68:71], v[84:87]
ds_read_b64_tr_b16 a[120:121], v247 offset:8192
ds_read_b64_tr_b16 a[122:123], v247 offset:12288
v_mfma_f32_16x16x32_bf16 v[88:91], a[88:91], a[68:71], v[88:91]
buffer_load_dwordx4 v7, s[4:7], s44 offen offset:1024 sc1 lds
v_mfma_f32_16x16x32_bf16 v[92:95], a[92:95], a[68:71], v[92:95]
ds_read_b128 a[104:107], v244 offset:6144
v_add_u32 v247, s53, v247
v_mfma_f32_16x16x32_bf16 v[96:99], a[80:83], a[72:75], v[96:99]
ds_read_b64_tr_b16 a[124:125], v248 offset:8192
ds_read_b64_tr_b16 a[126:127], v248 offset:12288
v_mfma_f32_16x16x32_bf16 v[100:103], a[84:87], a[72:75], v[100:103]
ds_read_b128 a[108:111], v244 offset:7168
v_add_u32 v248, s53, v248
v_mfma_f32_16x16x32_bf16 v[104:107], a[88:91], a[72:75], v[104:107]
buffer_load_dwordx4 v7, s[4:7], s44 offen offset:2048 sc1 lds
v_mfma_f32_16x16x32_bf16 v[108:111], a[92:95], a[72:75], v[108:111]
s_add_u32 s49, s46, s91
v_add_u32 v244, s53, v244
v_mfma_f32_16x16x32_bf16 v[112:115], a[80:83], a[76:79], v[112:115]
s_add_u32 s46, s46, s53
v_mfma_f32_16x16x32_bf16 v[116:119], a[84:87], a[76:79], v[116:119]
v_mfma_f32_16x16x32_bf16 v[120:123], a[88:91], a[76:79], v[120:123]
buffer_load_dwordx4 v7, s[4:7], s44 offen offset:3072 sc1 lds
v_mfma_f32_16x16x32_bf16 v[124:127], a[92:95], a[76:79], v[124:127]
s_waitcnt vmcnt(24) lgkmcnt(0)
s_barrier
v_mfma_f32_16x16x32_bf16 v[64:67], a[112:115], a[96:99], v[64:67]
ds_read_b64_tr_b16 a[80:81], v245 offset:0
ds_read_b64_tr_b16 a[82:83], v245 offset:4096
v_mfma_f32_16x16x32_bf16 v[68:71], a[116:119], a[96:99], v[68:71]
s_add_u32 m0, s49, 16384
ds_read_b128 a[64:67], v244 offset:0
v_mfma_f32_16x16x32_bf16 v[72:75], a[120:123], a[96:99], v[72:75]
buffer_load_dwordx4 v240, s[8:11], s45 offen offset:0 lds
v_mfma_f32_16x16x32_bf16 v[76:79], a[124:127], a[96:99], v[76:79]
ds_read_b64_tr_b16 a[84:85], v246 offset:0
ds_read_b64_tr_b16 a[86:87], v246 offset:4096
v_mfma_f32_16x16x32_bf16 v[80:83], a[112:115], a[100:103], v[80:83]
s_add_u32 s44, s44, 4096
ds_read_b128 a[68:71], v244 offset:1024
v_mfma_f32_16x16x32_bf16 v[84:87], a[116:119], a[100:103], v[84:87]
ds_read_b64_tr_b16 a[88:89], v247 offset:0
ds_read_b64_tr_b16 a[90:91], v247 offset:4096
v_mfma_f32_16x16x32_bf16 v[88:91], a[120:123], a[100:103], v[88:91]
buffer_load_dwordx4 v241, s[8:11], s45 offen offset:1024 lds
v_mfma_f32_16x16x32_bf16 v[92:95], a[124:127], a[100:103], v[92:95]
ds_read_b128 a[72:75], v244 offset:4096
v_mfma_f32_16x16x32_bf16 v[96:99], a[112:115], a[104:107], v[96:99]
ds_read_b64_tr_b16 a[92:93], v248 offset:0
ds_read_b64_tr_b16 a[94:95], v248 offset:4096
v_mfma_f32_16x16x32_bf16 v[100:103], a[116:119], a[104:107], v[100:103]
ds_read_b128 a[76:79], v244 offset:5120
v_mfma_f32_16x16x32_bf16 v[104:107], a[120:123], a[104:107], v[104:107]
buffer_load_dwordx4 v242, s[8:11], s45 offen offset:2048 lds
v_mfma_f32_16x16x32_bf16 v[108:111], a[124:127], a[104:107], v[108:111]
s_cmp_eq_u32 s46, 0
s_cselect_b32 s53, s56, s57
v_mfma_f32_16x16x32_bf16 v[112:115], a[112:115], a[108:111], v[112:115]
v_mfma_f32_16x16x32_bf16 v[116:119], a[116:119], a[108:111], v[116:119]
v_mfma_f32_16x16x32_bf16 v[120:123], a[120:123], a[108:111], v[120:123]
buffer_load_dwordx4 v243, s[8:11], s45 offen offset:3072 lds
v_mfma_f32_16x16x32_bf16 v[124:127], a[124:127], a[108:111], v[124:127]
s_waitcnt lgkmcnt(0)
s_mov_b32 m0, s49
s_add_u32 s45, s45, 131072
v_mfma_f32_16x16x32_bf16 v[128:131], a[80:83], a[64:67], 0
ds_read_b64_tr_b16 a[112:113], v245 offset:8192
ds_read_b64_tr_b16 a[114:115], v245 offset:12288
v_mfma_f32_16x16x32_bf16 v[132:135], a[84:87], a[64:67], 0
ds_read_b128 a[96:99], v244 offset:2048
v_add_u32 v245, s53, v245
v_mfma_f32_16x16x32_bf16 v[136:139], a[88:91], a[64:67], 0
buffer_load_dwordx4 v7, s[4:7], s44 offen offset:0 sc1 lds
v_mfma_f32_16x16x32_bf16 v[140:143], a[92:95], a[64:67], 0
ds_read_b64_tr_b16 a[116:117], v246 offset:8192
ds_read_b64_tr_b16 a[118:119], v246 offset:12288
v_mfma_f32_16x16x32_bf16 v[144:147], a[80:83], a[68:71], 0
ds_read_b128 a[100:103], v244 offset:3072
v_add_u32 v246, s53, v246
v_mfma_f32_16x16x32_bf16 v[148:151], a[84:87], a[68:71], 0
ds_read_b64_tr_b16 a[120:121], v247 offset:8192
ds_read_b64_tr_b16 a[122:123], v247 offset:12288
v_mfma_f32_16x16x32_bf16 v[152:155], a[88:91], a[68:71], 0
buffer_load_dwordx4 v7, s[4:7], s44 offen offset:1024 sc1 lds
v_mfma_f32_16x16x32_bf16 v[156:159], a[92:95], a[68:71], 0
ds_read_b128 a[104:107], v244 offset:6144
v_add_u32 v247, s53, v247
v_mfma_f32_16x16x32_bf16 v[160:163], a[80:83], a[72:75], 0
ds_read_b64_tr_b16 a[124:125], v248 offset:8192
ds_read_b64_tr_b16 a[126:127], v248 offset:12288
v_mfma_f32_16x16x32_bf16 v[164:167], a[84:87], a[72:75], 0
ds_read_b128 a[108:111], v244 offset:7168
v_add_u32 v248, s53, v248
v_mfma_f32_16x16x32_bf16 v[168:171], a[88:91], a[72:75], 0
buffer_load_dwordx4 v7, s[4:7], s44 offen offset:2048 sc1 lds
v_mfma_f32_16x16x32_bf16 v[172:175], a[92:95], a[72:75], 0
s_add_u32 s49, s46, s91
v_add_u32 v244, s53, v244
v_mfma_f32_16x16x32_bf16 v[176:179], a[80:83], a[76:79], 0
s_add_u32 s46, s46, s53
v_mfma_f32_16x16x32_bf16 v[180:183], a[84:87], a[76:79], 0
v_mfma_f32_16x16x32_bf16 v[184:187], a[88:91], a[76:79], 0
buffer_load_dwordx4 v7, s[4:7], s44 offen offset:3072 sc1 lds
v_mfma_f32_16x16x32_bf16 v[188:191], a[92:95], a[76:79], 0
s_waitcnt vmcnt(24) lgkmcnt(0)
s_barrier
s_mov_b32 s32, 4096
v_mfma_f32_16x16x32_bf16 v[128:131], a[112:115], a[96:99], v[128:131]
ds_read_b64_tr_b16 a[80:81], v245 offset:0
ds_read_b64_tr_b16 a[82:83], v245 offset:4096
v_mfma_f32_16x16x32_bf16 v[132:135], a[116:119], a[96:99], v[132:135]
s_add_u32 m0, s49, 16384
ds_read_b128 a[64:67], v244 offset:0
v_cvt_pk_bf16_f32 v64, v64, v65
v_mfma_f32_16x16x32_bf16 v[136:139], a[120:123], a[96:99], v[136:139]
buffer_load_dwordx4 v240, s[8:11], s45 offen offset:0 lds
v_mfma_f32_16x16x32_bf16 v[140:143], a[124:127], a[96:99], v[140:143]
ds_read_b64_tr_b16 a[84:85], v246 offset:0
ds_read_b64_tr_b16 a[86:87], v246 offset:4096
v_mfma_f32_16x16x32_bf16 v[144:147], a[112:115], a[100:103], v[144:147]
s_add_u32 s44, s44, 4096
ds_read_b128 a[68:71], v244 offset:1024
v_cvt_pk_bf16_f32 v65, v66, v67
v_mfma_f32_16x16x32_bf16 v[148:151], a[116:119], a[100:103], v[148:151]
ds_read_b64_tr_b16 a[88:89], v247 offset:0
ds_read_b64_tr_b16 a[90:91], v247 offset:4096
v_cvt_pk_bf16_f32 v66, v68, v69
v_mfma_f32_16x16x32_bf16 v[152:155], a[120:123], a[100:103], v[152:155]
buffer_load_dwordx4 v241, s[8:11], s45 offen offset:1024 lds
v_cvt_pk_bf16_f32 v67, v70, v71
v_mfma_f32_16x16x32_bf16 v[156:159], a[124:127], a[100:103], v[156:159]
buffer_store_dwordx4 v[64:67], v7, s[12:15], 0 offen offset:0 sc1
ds_read_b128 a[72:75], v244 offset:4096
v_mfma_f32_16x16x32_bf16 v[160:163], a[112:115], a[104:107], v[160:163]
ds_read_b64_tr_b16 a[92:93], v248 offset:0
ds_read_b64_tr_b16 a[94:95], v248 offset:4096
v_mfma_f32_16x16x32_bf16 v[164:167], a[116:119], a[104:107], v[164:167]
ds_read_b128 a[76:79], v244 offset:5120
v_mfma_f32_16x16x32_bf16 v[168:171], a[120:123], a[104:107], v[168:171]
buffer_load_dwordx4 v242, s[8:11], s45 offen offset:2048 lds
v_mfma_f32_16x16x32_bf16 v[172:175], a[124:127], a[104:107], v[172:175]
s_cmp_eq_u32 s46, 0
s_cselect_b32 s53, s56, s57
v_mfma_f32_16x16x32_bf16 v[176:179], a[112:115], a[108:111], v[176:179]
v_mfma_f32_16x16x32_bf16 v[180:183], a[116:119], a[108:111], v[180:183]
v_mfma_f32_16x16x32_bf16 v[184:187], a[120:123], a[108:111], v[184:187]
buffer_load_dwordx4 v243, s[8:11], s45 offen offset:3072 lds
v_mfma_f32_16x16x32_bf16 v[188:191], a[124:127], a[108:111], v[188:191]
s_waitcnt lgkmcnt(0)
s_mov_b32 m0, s49
s_add_u32 s45, s45, 131072
v_mfma_f32_16x16x32_bf16 v[128:131], a[80:83], a[64:67], v[128:131]
ds_read_b64_tr_b16 a[112:113], v245 offset:8192
ds_read_b64_tr_b16 a[114:115], v245 offset:12288
v_mfma_f32_16x16x32_bf16 v[132:135], a[84:87], a[64:67], v[132:135]
ds_read_b128 a[96:99], v244 offset:2048
v_add_u32 v245, s53, v245
v_cvt_pk_bf16_f32 v72, v72, v73
v_mfma_f32_16x16x32_bf16 v[136:139], a[88:91], a[64:67], v[136:139]
buffer_load_dwordx4 v7, s[4:7], s44 offen offset:0 sc1 lds
v_mfma_f32_16x16x32_bf16 v[140:143], a[92:95], a[64:67], v[140:143]
ds_read_b64_tr_b16 a[116:117], v246 offset:8192
ds_read_b64_tr_b16 a[118:119], v246 offset:12288
v_mfma_f32_16x16x32_bf16 v[144:147], a[80:83], a[68:71], v[144:147]
ds_read_b128 a[100:103], v244 offset:3072
v_add_u32 v246, s53, v246
v_cvt_pk_bf16_f32 v73, v74, v75
v_mfma_f32_16x16x32_bf16 v[148:151], a[84:87], a[68:71], v[148:151]
ds_read_b64_tr_b16 a[120:121], v247 offset:8192
ds_read_b64_tr_b16 a[122:123], v247 offset:12288
v_cvt_pk_bf16_f32 v74, v76, v77
v_mfma_f32_16x16x32_bf16 v[152:155], a[88:91], a[68:71], v[152:155]
buffer_load_dwordx4 v7, s[4:7], s44 offen offset:1024 sc1 lds
v_cvt_pk_bf16_f32 v75, v78, v79
v_mfma_f32_16x16x32_bf16 v[156:159], a[92:95], a[68:71], v[156:159]
buffer_store_dwordx4 v[72:75], v7, s[12:15], 0 offen offset:1024 sc1
ds_read_b128 a[104:107], v244 offset:6144
v_add_u32 v247, s53, v247
v_mfma_f32_16x16x32_bf16 v[160:163], a[80:83], a[72:75], v[160:163]
ds_read_b64_tr_b16 a[124:125], v248 offset:8192
ds_read_b64_tr_b16 a[126:127], v248 offset:12288
v_mfma_f32_16x16x32_bf16 v[164:167], a[84:87], a[72:75], v[164:167]
ds_read_b128 a[108:111], v244 offset:7168
v_add_u32 v248, s53, v248
v_mfma_f32_16x16x32_bf16 v[168:171], a[88:91], a[72:75], v[168:171]
buffer_load_dwordx4 v7, s[4:7], s44 offen offset:2048 sc1 lds
v_mfma_f32_16x16x32_bf16 v[172:175], a[92:95], a[72:75], v[172:175]
s_add_u32 s49, s46, s91
v_add_u32 v244, s53, v244
v_mfma_f32_16x16x32_bf16 v[176:179], a[80:83], a[76:79], v[176:179]
s_add_u32 s46, s46, s53
v_mfma_f32_16x16x32_bf16 v[180:183], a[84:87], a[76:79], v[180:183]
v_mfma_f32_16x16x32_bf16 v[184:187], a[88:91], a[76:79], v[184:187]
buffer_load_dwordx4 v7, s[4:7], s44 offen offset:3072 sc1 lds
v_mfma_f32_16x16x32_bf16 v[188:191], a[92:95], a[76:79], v[188:191]
s_waitcnt vmcnt(26) lgkmcnt(0)
s_barrier
v_mfma_f32_16x16x32_bf16 v[128:131], a[112:115], a[96:99], v[128:131]
ds_read_b64_tr_b16 a[80:81], v245 offset:0
ds_read_b64_tr_b16 a[82:83], v245 offset:4096
v_mfma_f32_16x16x32_bf16 v[132:135], a[116:119], a[96:99], v[132:135]
s_add_u32 m0, s49, 16384
ds_read_b128 a[64:67], v244 offset:0
v_cvt_pk_bf16_f32 v80, v80, v81
v_mfma_f32_16x16x32_bf16 v[136:139], a[120:123], a[96:99], v[136:139]
buffer_load_dwordx4 v240, s[8:11], s45 offen offset:0 lds
v_mfma_f32_16x16x32_bf16 v[140:143], a[124:127], a[96:99], v[140:143]
ds_read_b64_tr_b16 a[84:85], v246 offset:0
ds_read_b64_tr_b16 a[86:87], v246 offset:4096
v_mfma_f32_16x16x32_bf16 v[144:147], a[112:115], a[100:103], v[144:147]
s_add_u32 s44, s44, 4096
ds_read_b128 a[68:71], v244 offset:1024
v_cvt_pk_bf16_f32 v81, v82, v83
v_mfma_f32_16x16x32_bf16 v[148:151], a[116:119], a[100:103], v[148:151]
ds_read_b64_tr_b16 a[88:89], v247 offset:0
ds_read_b64_tr_b16 a[90:91], v247 offset:4096
v_cvt_pk_bf16_f32 v82, v84, v85
v_mfma_f32_16x16x32_bf16 v[152:155], a[120:123], a[100:103], v[152:155]
buffer_load_dwordx4 v241, s[8:11], s45 offen offset:1024 lds
v_cvt_pk_bf16_f32 v83, v86, v87
v_mfma_f32_16x16x32_bf16 v[156:159], a[124:127], a[100:103], v[156:159]
buffer_store_dwordx4 v[80:83], v7, s[12:15], 0 offen offset:2048 sc1
ds_read_b128 a[72:75], v244 offset:4096
v_mfma_f32_16x16x32_bf16 v[160:163], a[112:115], a[104:107], v[160:163]
ds_read_b64_tr_b16 a[92:93], v248 offset:0
ds_read_b64_tr_b16 a[94:95], v248 offset:4096
v_mfma_f32_16x16x32_bf16 v[164:167], a[116:119], a[104:107], v[164:167]
ds_read_b128 a[76:79], v244 offset:5120
v_mfma_f32_16x16x32_bf16 v[168:171], a[120:123], a[104:107], v[168:171]
buffer_load_dwordx4 v242, s[8:11], s45 offen offset:2048 lds
v_mfma_f32_16x16x32_bf16 v[172:175], a[124:127], a[104:107], v[172:175]
s_cmp_eq_u32 s46, 0
s_cselect_b32 s53, s56, s57
v_mfma_f32_16x16x32_bf16 v[176:179], a[112:115], a[108:111], v[176:179]
v_mfma_f32_16x16x32_bf16 v[180:183], a[116:119], a[108:111], v[180:183]
v_mfma_f32_16x16x32_bf16 v[184:187], a[120:123], a[108:111], v[184:187]
buffer_load_dwordx4 v243, s[8:11], s45 offen offset:3072 lds
v_mfma_f32_16x16x32_bf16 v[188:191], a[124:127], a[108:111], v[188:191]
s_waitcnt lgkmcnt(0)
s_mov_b32 m0, s49
s_add_u32 s45, s45, 131072
v_mfma_f32_16x16x32_bf16 v[128:131], a[80:83], a[64:67], v[128:131]
ds_read_b64_tr_b16 a[112:113], v245 offset:8192
ds_read_b64_tr_b16 a[114:115], v245 offset:12288
v_mfma_f32_16x16x32_bf16 v[132:135], a[84:87], a[64:67], v[132:135]
ds_read_b128 a[96:99], v244 offset:2048
v_add_u32 v245, s53, v245
v_cvt_pk_bf16_f32 v88, v88, v89
v_mfma_f32_16x16x32_bf16 v[136:139], a[88:91], a[64:67], v[136:139]
buffer_load_dwordx4 v7, s[4:7], s44 offen offset:0 sc1 lds
v_mfma_f32_16x16x32_bf16 v[140:143], a[92:95], a[64:67], v[140:143]
ds_read_b64_tr_b16 a[116:117], v246 offset:8192
ds_read_b64_tr_b16 a[118:119], v246 offset:12288
v_mfma_f32_16x16x32_bf16 v[144:147], a[80:83], a[68:71], v[144:147]
ds_read_b128 a[100:103], v244 offset:3072
v_add_u32 v246, s53, v246
v_cvt_pk_bf16_f32 v89, v90, v91
v_mfma_f32_16x16x32_bf16 v[148:151], a[84:87], a[68:71], v[148:151]
ds_read_b64_tr_b16 a[120:121], v247 offset:8192
ds_read_b64_tr_b16 a[122:123], v247 offset:12288
v_cvt_pk_bf16_f32 v90, v92, v93
v_mfma_f32_16x16x32_bf16 v[152:155], a[88:91], a[68:71], v[152:155]
buffer_load_dwordx4 v7, s[4:7], s44 offen offset:1024 sc1 lds
v_cvt_pk_bf16_f32 v91, v94, v95
v_mfma_f32_16x16x32_bf16 v[156:159], a[92:95], a[68:71], v[156:159]
buffer_store_dwordx4 v[88:91], v7, s[12:15], 0 offen offset:3072 sc1
ds_read_b128 a[104:107], v244 offset:6144
v_add_u32 v247, s53, v247
v_mfma_f32_16x16x32_bf16 v[160:163], a[80:83], a[72:75], v[160:163]
ds_read_b64_tr_b16 a[124:125], v248 offset:8192
ds_read_b64_tr_b16 a[126:127], v248 offset:12288
v_mfma_f32_16x16x32_bf16 v[164:167], a[84:87], a[72:75], v[164:167]
ds_read_b128 a[108:111], v244 offset:7168
v_add_u32 v248, s53, v248
v_mfma_f32_16x16x32_bf16 v[168:171], a[88:91], a[72:75], v[168:171]
buffer_load_dwordx4 v7, s[4:7], s44 offen offset:2048 sc1 lds
v_mfma_f32_16x16x32_bf16 v[172:175], a[92:95], a[72:75], v[172:175]
s_add_u32 s49, s46, s91
v_add_u32 v244, s53, v244
v_mfma_f32_16x16x32_bf16 v[176:179], a[80:83], a[76:79], v[176:179]
s_add_u32 s46, s46, s53
v_mfma_f32_16x16x32_bf16 v[180:183], a[84:87], a[76:79], v[180:183]
v_mfma_f32_16x16x32_bf16 v[184:187], a[88:91], a[76:79], v[184:187]
buffer_load_dwordx4 v7, s[4:7], s44 offen offset:3072 sc1 lds
v_mfma_f32_16x16x32_bf16 v[188:191], a[92:95], a[76:79], v[188:191]
s_waitcnt vmcnt(28) lgkmcnt(0)
s_barrier
v_mfma_f32_16x16x32_bf16 v[128:131], a[112:115], a[96:99], v[128:131]
ds_read_b64_tr_b16 a[80:81], v245 offset:0
ds_read_b64_tr_b16 a[82:83], v245 offset:4096
v_mfma_f32_16x16x32_bf16 v[132:135], a[116:119], a[96:99], v[132:135]
s_add_u32 m0, s49, 16384
ds_read_b128 a[64:67], v244 offset:0
v_cvt_pk_bf16_f32 v96, v96, v97
v_mfma_f32_16x16x32_bf16 v[136:139], a[120:123], a[96:99], v[136:139]
buffer_load_dwordx4 v240, s[8:11], s45 offen offset:0 lds
v_mfma_f32_16x16x32_bf16 v[140:143], a[124:127], a[96:99], v[140:143]
ds_read_b64_tr_b16 a[84:85], v246 offset:0
ds_read_b64_tr_b16 a[86:87], v246 offset:4096
v_mfma_f32_16x16x32_bf16 v[144:147], a[112:115], a[100:103], v[144:147]
s_add_u32 s44, s44, 4096
ds_read_b128 a[68:71], v244 offset:1024
v_cvt_pk_bf16_f32 v97, v98, v99
v_mfma_f32_16x16x32_bf16 v[148:151], a[116:119], a[100:103], v[148:151]
ds_read_b64_tr_b16 a[88:89], v247 offset:0
ds_read_b64_tr_b16 a[90:91], v247 offset:4096
v_cvt_pk_bf16_f32 v98, v100, v101
v_mfma_f32_16x16x32_bf16 v[152:155], a[120:123], a[100:103], v[152:155]
buffer_load_dwordx4 v241, s[8:11], s45 offen offset:1024 lds
v_cvt_pk_bf16_f32 v99, v102, v103
v_mfma_f32_16x16x32_bf16 v[156:159], a[124:127], a[100:103], v[156:159]
buffer_store_dwordx4 v[96:99], v7, s[12:15], s32 offen offset:0 sc1
.Lpad22:
s_cbranch_execz .Ltch11
ds_read_b128 a[72:75], v244 offset:4096
v_mfma_f32_16x16x32_bf16 v[160:163], a[112:115], a[104:107], v[160:163]
ds_read_b64_tr_b16 a[92:93], v248 offset:0
ds_read_b64_tr_b16 a[94:95], v248 offset:4096
v_mfma_f32_16x16x32_bf16 v[164:167], a[116:119], a[104:107], v[164:167]
ds_read_b128 a[76:79], v244 offset:5120
v_mfma_f32_16x16x32_bf16 v[168:171], a[120:123], a[104:107], v[168:171]
buffer_load_dwordx4 v242, s[8:11], s45 offen offset:2048 lds
v_mfma_f32_16x16x32_bf16 v[172:175], a[124:127], a[104:107], v[172:175]
s_cmp_eq_u32 s46, 0
s_cselect_b32 s53, s56, s57
v_mfma_f32_16x16x32_bf16 v[176:179], a[112:115], a[108:111], v[176:179]
v_mfma_f32_16x16x32_bf16 v[180:183], a[116:119], a[108:111], v[180:183]
v_mfma_f32_16x16x32_bf16 v[184:187], a[120:123], a[108:111], v[184:187]
buffer_load_dwordx4 v243, s[8:11], s45 offen offset:3072 lds
v_mfma_f32_16x16x32_bf16 v[188:191], a[124:127], a[108:111], v[188:191]
s_waitcnt lgkmcnt(0)
s_mov_b32 m0, s49
s_add_u32 s45, s45, 131072
v_mfma_f32_16x16x32_bf16 v[128:131], a[80:83], a[64:67], v[128:131]
ds_read_b64_tr_b16 a[112:113], v245 offset:8192
ds_read_b64_tr_b16 a[114:115], v245 offset:12288
v_mfma_f32_16x16x32_bf16 v[132:135], a[84:87], a[64:67], v[132:135]
ds_read_b128 a[96:99], v244 offset:2048
v_add_u32 v245, s53, v245
v_cvt_pk_bf16_f32 v104, v104, v105
v_mfma_f32_16x16x32_bf16 v[136:139], a[88:91], a[64:67], v[136:139]
buffer_load_dwordx4 v7, s[4:7], s44 offen offset:0 sc1 lds
v_mfma_f32_16x16x32_bf16 v[140:143], a[92:95], a[64:67], v[140:143]
ds_read_b64_tr_b16 a[116:117], v246 offset:8192
ds_read_b64_tr_b16 a[118:119], v246 offset:12288
v_mfma_f32_16x16x32_bf16 v[144:147], a[80:83], a[68:71], v[144:147]
ds_read_b128 a[100:103], v244 offset:3072
v_add_u32 v246, s53, v246
v_cvt_pk_bf16_f32 v105, v106, v107
v_mfma_f32_16x16x32_bf16 v[148:151], a[84:87], a[68:71], v[148:151]
ds_read_b64_tr_b16 a[120:121], v247 offset:8192
ds_read_b64_tr_b16 a[122:123], v247 offset:12288
v_cvt_pk_bf16_f32 v106, v108, v109
v_mfma_f32_16x16x32_bf16 v[152:155], a[88:91], a[68:71], v[152:155]
buffer_load_dwordx4 v7, s[4:7], s44 offen offset:1024 sc1 lds
v_cvt_pk_bf16_f32 v107, v110, v111
v_mfma_f32_16x16x32_bf16 v[156:159], a[92:95], a[68:71], v[156:159]
buffer_store_dwordx4 v[104:107], v7, s[12:15], s32 offen offset:1024 sc1
ds_read_b128 a[104:107], v244 offset:6144
v_add_u32 v247, s53, v247
v_mfma_f32_16x16x32_bf16 v[160:163], a[80:83], a[72:75], v[160:163]
ds_read_b64_tr_b16 a[124:125], v248 offset:8192
ds_read_b64_tr_b16 a[126:127], v248 offset:12288
v_mfma_f32_16x16x32_bf16 v[164:167], a[84:87], a[72:75], v[164:167]
ds_read_b128 a[108:111], v244 offset:7168
v_add_u32 v248, s53, v248
v_mfma_f32_16x16x32_bf16 v[168:171], a[88:91], a[72:75], v[168:171]
buffer_load_dwordx4 v7, s[4:7], s44 offen offset:2048 sc1 lds
v_mfma_f32_16x16x32_bf16 v[172:175], a[92:95], a[72:75], v[172:175]
s_add_u32 s49, s46, s91
v_add_u32 v244, s53, v244
v_mfma_f32_16x16x32_bf16 v[176:179], a[80:83], a[76:79], v[176:179]
s_add_u32 s46, s46, s53
v_mfma_f32_16x16x32_bf16 v[180:183], a[84:87], a[76:79], v[180:183]
v_mfma_f32_16x16x32_bf16 v[184:187], a[88:91], a[76:79], v[184:187]
buffer_load_dwordx4 v7, s[4:7], s44 offen offset:3072 sc1 lds
v_mfma_f32_16x16x32_bf16 v[188:191], a[92:95], a[76:79], v[188:191]
s_waitcnt vmcnt(30) lgkmcnt(0)
s_barrier
v_mfma_f32_16x16x32_bf16 v[128:131], a[112:115], a[96:99], v[128:131]
ds_read_b64_tr_b16 a[80:81], v245 offset:0
ds_read_b64_tr_b16 a[82:83], v245 offset:4096
v_mfma_f32_16x16x32_bf16 v[132:135], a[116:119], a[96:99], v[132:135]
s_add_u32 m0, s49, 16384
ds_read_b128 a[64:67], v244 offset:0
v_cvt_pk_bf16_f32 v112, v112, v113
v_mfma_f32_16x16x32_bf16 v[136:139], a[120:123], a[96:99], v[136:139]
buffer_load_dwordx4 v240, s[8:11], s45 offen offset:0 lds
v_mfma_f32_16x16x32_bf16 v[140:143], a[124:127], a[96:99], v[140:143]
ds_read_b64_tr_b16 a[84:85], v246 offset:0
ds_read_b64_tr_b16 a[86:87], v246 offset:4096
v_mfma_f32_16x16x32_bf16 v[144:147], a[112:115], a[100:103], v[144:147]
s_add_u32 s44, s44, 4096
ds_read_b128 a[68:71], v244 offset:1024
v_cvt_pk_bf16_f32 v113, v114, v115
v_mfma_f32_16x16x32_bf16 v[148:151], a[116:119], a[100:103], v[148:151]
ds_read_b64_tr_b16 a[88:89], v247 offset:0
ds_read_b64_tr_b16 a[90:91], v247 offset:4096
v_cvt_pk_bf16_f32 v114, v116, v117
v_mfma_f32_16x16x32_bf16 v[152:155], a[120:123], a[100:103], v[152:155]
buffer_load_dwordx4 v241, s[8:11], s45 offen offset:1024 lds
v_cvt_pk_bf16_f32 v115, v118, v119
v_mfma_f32_16x16x32_bf16 v[156:159], a[124:127], a[100:103], v[156:159]
buffer_store_dwordx4 v[112:115], v7, s[12:15], s32 offen offset:2048 sc1
ds_read_b128 a[72:75], v244 offset:4096
v_mfma_f32_16x16x32_bf16 v[160:163], a[112:115], a[104:107], v[160:163]
ds_read_b64_tr_b16 a[92:93], v248 offset:0
ds_read_b64_tr_b16 a[94:95], v248 offset:4096
v_mfma_f32_16x16x32_bf16 v[164:167], a[116:119], a[104:107], v[164:167]
ds_read_b128 a[76:79], v244 offset:5120
v_mfma_f32_16x16x32_bf16 v[168:171], a[120:123], a[104:107], v[168:171]
buffer_load_dwordx4 v242, s[8:11], s45 offen offset:2048 lds
v_mfma_f32_16x16x32_bf16 v[172:175], a[124:127], a[104:107], v[172:175]
s_cmp_eq_u32 s46, 0
s_cselect_b32 s53, s56, s57
v_mfma_f32_16x16x32_bf16 v[176:179], a[112:115], a[108:111], v[176:179]
v_mfma_f32_16x16x32_bf16 v[180:183], a[116:119], a[108:111], v[180:183]
v_mfma_f32_16x16x32_bf16 v[184:187], a[120:123], a[108:111], v[184:187]
buffer_load_dwordx4 v243, s[8:11], s45 offen offset:3072 lds
v_mfma_f32_16x16x32_bf16 v[188:191], a[124:127], a[108:111], v[188:191]
s_waitcnt lgkmcnt(0)
s_mov_b32 m0, s49
s_add_u32 s45, s45, 131072
v_mfma_f32_16x16x32_bf16 v[128:131], a[80:83], a[64:67], v[128:131]
ds_read_b64_tr_b16 a[112:113], v245 offset:8192
ds_read_b64_tr_b16 a[114:115], v245 offset:12288
v_mfma_f32_16x16x32_bf16 v[132:135], a[84:87], a[64:67], v[132:135]
ds_read_b128 a[96:99], v244 offset:2048
v_add_u32 v245, s53, v245
v_cvt_pk_bf16_f32 v120, v120, v121
v_mfma_f32_16x16x32_bf16 v[136:139], a[88:91], a[64:67], v[136:139]
buffer_load_dwordx4 v7, s[4:7], s44 offen offset:0 sc1 lds
v_mfma_f32_16x16x32_bf16 v[140:143], a[92:95], a[64:67], v[140:143]
ds_read_b64_tr_b16 a[116:117], v246 offset:8192
ds_read_b64_tr_b16 a[118:119], v246 offset:12288
v_mfma_f32_16x16x32_bf16 v[144:147], a[80:83], a[68:71], v[144:147]
ds_read_b128 a[100:103], v244 offset:3072
v_add_u32 v246, s53, v246
v_cvt_pk_bf16_f32 v121, v122, v123
v_mfma_f32_16x16x32_bf16 v[148:151], a[84:87], a[68:71], v[148:151]
ds_read_b64_tr_b16 a[120:121], v247 offset:8192
ds_read_b64_tr_b16 a[122:123], v247 offset:12288
v_cvt_pk_bf16_f32 v122, v124, v125
v_mfma_f32_16x16x32_bf16 v[152:155], a[88:91], a[68:71], v[152:155]
buffer_load_dwordx4 v7, s[4:7], s44 offen offset:1024 sc1 lds
v_cvt_pk_bf16_f32 v123, v126, v127
v_mfma_f32_16x16x32_bf16 v[156:159], a[92:95], a[68:71], v[156:159]
buffer_store_dwordx4 v[120:123], v7, s[12:15], s32 offen offset:3072 sc1
ds_read_b128 a[104:107], v244 offset:6144
v_add_u32 v247, s53, v247
v_mfma_f32_16x16x32_bf16 v[160:163], a[80:83], a[72:75], v[160:163]
ds_read_b64_tr_b16 a[124:125], v248 offset:8192
ds_read_b64_tr_b16 a[126:127], v248 offset:12288
v_mfma_f32_16x16x32_bf16 v[164:167], a[84:87], a[72:75], v[164:167]
ds_read_b128 a[108:111], v244 offset:7168
v_add_u32 v248, s53, v248
v_mfma_f32_16x16x32_bf16 v[168:171], a[88:91], a[72:75], v[168:171]
buffer_load_dwordx4 v7, s[4:7], s44 offen offset:2048 sc1 lds
v_mfma_f32_16x16x32_bf16 v[172:175], a[92:95], a[72:75], v[172:175]
s_add_u32 s49, s46, s91
v_add_u32 v244, s53, v244
v_mfma_f32_16x16x32_bf16 v[176:179], a[80:83], a[76:79], v[176:179]
s_add_u32 s46, s46, s53
v_mfma_f32_16x16x32_bf16 v[180:183], a[84:87], a[76:79], v[180:183]
v_mfma_f32_16x16x32_bf16 v[184:187], a[88:91], a[76:79], v[184:187]
buffer_load_dwordx4 v7, s[4:7], s44 offen offset:3072 sc1 lds
v_mfma_f32_16x16x32_bf16 v[188:191], a[92:95], a[76:79], v[188:191]
s_waitcnt vmcnt(30) lgkmcnt(0)
s_barrier
v_mfma_f32_16x16x32_bf16 v[128:131], a[112:115], a[96:99], v[128:131]
ds_read_b64_tr_b16 a[80:81], v245 offset:0
ds_read_b64_tr_b16 a[82:83], v245 offset:4096
v_mfma_f32_16x16x32_bf16 v[132:135], a[116:119], a[96:99], v[132:135]
s_add_u32 m0, s49, 16384
ds_read_b128 a[64:67], v244 offset:0
v_mfma_f32_16x16x32_bf16 v[136:139], a[120:123], a[96:99], v[136:139]
buffer_load_dwordx4 v240, s[8:11], s45 offen offset:0 lds
v_mfma_f32_16x16x32_bf16 v[140:143], a[124:127], a[96:99], v[140:143]
ds_read_b64_tr_b16 a[84:85], v246 offset:0
ds_read_b64_tr_b16 a[86:87], v246 offset:4096
v_mfma_f32_16x16x32_bf16 v[144:147], a[112:115], a[100:103], v[144:147]
s_add_u32 s44, s44, 4096
ds_read_b128 a[68:71], v244 offset:1024
v_mfma_f32_16x16x32_bf16 v[148:151], a[116:119], a[100:103], v[148:151]
ds_read_b64_tr_b16 a[88:89], v247 offset:0
ds_read_b64_tr_b16 a[90:91], v247 offset:4096
v_mfma_f32_16x16x32_bf16 v[152:155], a[120:123], a[100:103], v[152:155]
buffer_load_dwordx4 v241, s[8:11], s45 offen offset:1024 lds
v_mfma_f32_16x16x32_bf16 v[156:159], a[124:127], a[100:103], v[156:159]
ds_read_b128 a[72:75], v244 offset:4096
v_mfma_f32_16x16x32_bf16 v[160:163], a[112:115], a[104:107], v[160:163]
ds_read_b64_tr_b16 a[92:93], v248 offset:0
ds_read_b64_tr_b16 a[94:95], v248 offset:4096
v_mfma_f32_16x16x32_bf16 v[164:167], a[116:119], a[104:107], v[164:167]
ds_read_b128 a[76:79], v244 offset:5120
v_mfma_f32_16x16x32_bf16 v[168:171], a[120:123], a[104:107], v[168:171]
buffer_load_dwordx4 v242, s[8:11], s45 offen offset:2048 lds
v_mfma_f32_16x16x32_bf16 v[172:175], a[124:127], a[104:107], v[172:175]
s_cmp_eq_u32 s46, 0
s_cselect_b32 s53, s56, s57
v_mfma_f32_16x16x32_bf16 v[176:179], a[112:115], a[108:111], v[176:179]
v_mfma_f32_16x16x32_bf16 v[180:183], a[116:119], a[108:111], v[180:183]
v_mfma_f32_16x16x32_bf16 v[184:187], a[120:123], a[108:111], v[184:187]
buffer_load_dwordx4 v243, s[8:11], s45 offen offset:3072 lds
v_mfma_f32_16x16x32_bf16 v[188:191], a[124:127], a[108:111], v[188:191]
s_waitcnt lgkmcnt(0)
s_mov_b32 m0, s49
s_add_u32 s45, s45, 131072
v_mfma_f32_16x16x32_bf16 v[128:131], a[80:83], a[64:67], v[128:131]
ds_read_b64_tr_b16 a[112:113], v245 offset:8192
ds_read_b64_tr_b16 a[114:115], v245 offset:12288
v_mfma_f32_16x16x32_bf16 v[132:135], a[84:87], a[64:67], v[132:135]
ds_read_b128 a[96:99], v244 offset:2048
v_add_u32 v245, s53, v245
v_mfma_f32_16x16x32_bf16 v[136:139], a[88:91], a[64:67], v[136:139]
buffer_load_dwordx4 v7, s[4:7], s44 offen offset:0 sc1 lds
v_mfma_f32_16x16x32_bf16 v[140:143], a[92:95], a[64:67], v[140:143]
ds_read_b64_tr_b16 a[116:117], v246 offset:8192
ds_read_b64_tr_b16 a[118:119], v246 offset:12288
v_mfma_f32_16x16x32_bf16 v[144:147], a[80:83], a[68:71], v[144:147]
ds_read_b128 a[100:103], v244 offset:3072
v_add_u32 v246, s53, v246
v_mfma_f32_16x16x32_bf16 v[148:151], a[84:87], a[68:71], v[148:151]
ds_read_b64_tr_b16 a[120:121], v247 offset:8192
ds_read_b64_tr_b16 a[122:123], v247 offset:12288
v_mfma_f32_16x16x32_bf16 v[152:155], a[88:91], a[68:71], v[152:155]
buffer_load_dwordx4 v7, s[4:7], s44 offen offset:1024 sc1 lds
v_mfma_f32_16x16x32_bf16 v[156:159], a[92:95], a[68:71], v[156:159]
ds_read_b128 a[104:107], v244 offset:6144
v_add_u32 v247, s53, v247
v_mfma_f32_16x16x32_bf16 v[160:163], a[80:83], a[72:75], v[160:163]
ds_read_b64_tr_b16 a[124:125], v248 offset:8192
ds_read_b64_tr_b16 a[126:127], v248 offset:12288
v_mfma_f32_16x16x32_bf16 v[164:167], a[84:87], a[72:75], v[164:167]
ds_read_b128 a[108:111], v244 offset:7168
v_add_u32 v248, s53, v248
v_mfma_f32_16x16x32_bf16 v[168:171], a[88:91], a[72:75], v[168:171]
buffer_load_dwordx4 v7, s[4:7], s44 offen offset:2048 sc1 lds
v_mfma_f32_16x16x32_bf16 v[172:175], a[92:95], a[72:75], v[172:175]
s_add_u32 s49, s46, s91
v_add_u32 v244, s53, v244
v_mfma_f32_16x16x32_bf16 v[176:179], a[80:83], a[76:79], v[176:179]
s_add_u32 s46, s46, s53
v_mfma_f32_16x16x32_bf16 v[180:183], a[84:87], a[76:79], v[180:183]
v_mfma_f32_16x16x32_bf16 v[184:187], a[88:91], a[76:79], v[184:187]
buffer_load_dwordx4 v7, s[4:7], s44 offen offset:3072 sc1 lds
v_mfma_f32_16x16x32_bf16 v[188:191], a[92:95], a[76:79], v[188:191]
s_waitcnt vmcnt(28) lgkmcnt(0)
s_barrier
v_mfma_f32_16x16x32_bf16 v[128:131], a[112:115], a[96:99], v[128:131]
ds_read_b64_tr_b16 a[80:81], v245 offset:0
ds_read_b64_tr_b16 a[82:83], v245 offset:4096
v_mfma_f32_16x16x32_bf16 v[132:135], a[116:119], a[96:99], v[132:135]
s_add_u32 m0, s49, 16384
ds_read_b128 a[64:67], v244 offset:0
v_mfma_f32_16x16x32_bf16 v[136:139], a[120:123], a[96:99], v[136:139]
buffer_load_dwordx4 v240, s[8:11], s45 offen offset:0 lds
v_mfma_f32_16x16x32_bf16 v[140:143], a[124:127], a[96:99], v[140:143]
ds_read_b64_tr_b16 a[84:85], v246 offset:0
ds_read_b64_tr_b16 a[86:87], v246 offset:4096
v_mfma_f32_16x16x32_bf16 v[144:147], a[112:115], a[100:103], v[144:147]
s_add_u32 s44, s44, 4096
ds_read_b128 a[68:71], v244 offset:1024
v_mfma_f32_16x16x32_bf16 v[148:151], a[116:119], a[100:103], v[148:151]
ds_read_b64_tr_b16 a[88:89], v247 offset:0
ds_read_b64_tr_b16 a[90:91], v247 offset:4096
v_mfma_f32_16x16x32_bf16 v[152:155], a[120:123], a[100:103], v[152:155]
buffer_load_dwordx4 v241, s[8:11], s45 offen offset:1024 lds
v_mfma_f32_16x16x32_bf16 v[156:159], a[124:127], a[100:103], v[156:159]
ds_read_b128 a[72:75], v244 offset:4096
v_mfma_f32_16x16x32_bf16 v[160:163], a[112:115], a[104:107], v[160:163]
ds_read_b64_tr_b16 a[92:93], v248 offset:0
ds_read_b64_tr_b16 a[94:95], v248 offset:4096
v_mfma_f32_16x16x32_bf16 v[164:167], a[116:119], a[104:107], v[164:167]
ds_read_b128 a[76:79], v244 offset:5120
v_mfma_f32_16x16x32_bf16 v[168:171], a[120:123], a[104:107], v[168:171]
buffer_load_dwordx4 v242, s[8:11], s45 offen offset:2048 lds
v_mfma_f32_16x16x32_bf16 v[172:175], a[124:127], a[104:107], v[172:175]
s_cmp_eq_u32 s46, 0
s_cselect_b32 s53, s56, s57
v_mfma_f32_16x16x32_bf16 v[176:179], a[112:115], a[108:111], v[176:179]
v_mfma_f32_16x16x32_bf16 v[180:183], a[116:119], a[108:111], v[180:183]
v_mfma_f32_16x16x32_bf16 v[184:187], a[120:123], a[108:111], v[184:187]
buffer_load_dwordx4 v243, s[8:11], s45 offen offset:3072 lds
v_mfma_f32_16x16x32_bf16 v[188:191], a[124:127], a[108:111], v[188:191]
s_waitcnt lgkmcnt(0)
s_mov_b32 m0, s49
s_add_u32 s45, s45, 131072
v_mfma_f32_16x16x32_bf16 v[128:131], a[80:83], a[64:67], v[128:131]
ds_read_b64_tr_b16 a[112:113], v245 offset:8192
ds_read_b64_tr_b16 a[114:115], v245 offset:12288
v_mfma_f32_16x16x32_bf16 v[132:135], a[84:87], a[64:67], v[132:135]
ds_read_b128 a[96:99], v244 offset:2048
v_add_u32 v245, s53, v245
v_mfma_f32_16x16x32_bf16 v[136:139], a[88:91], a[64:67], v[136:139]
buffer_load_dwordx4 v7, s[4:7], s44 offen offset:0 sc1 lds
v_mfma_f32_16x16x32_bf16 v[140:143], a[92:95], a[64:67], v[140:143]
ds_read_b64_tr_b16 a[116:117], v246 offset:8192
ds_read_b64_tr_b16 a[118:119], v246 offset:12288
v_mfma_f32_16x16x32_bf16 v[144:147], a[80:83], a[68:71], v[144:147]
ds_read_b128 a[100:103], v244 offset:3072
v_add_u32 v246, s53, v246
v_mfma_f32_16x16x32_bf16 v[148:151], a[84:87], a[68:71], v[148:151]
ds_read_b64_tr_b16 a[120:121], v247 offset:8192
ds_read_b64_tr_b16 a[122:123], v247 offset:12288
v_mfma_f32_16x16x32_bf16 v[152:155], a[88:91], a[68:71], v[152:155]
buffer_load_dwordx4 v7, s[4:7], s44 offen offset:1024 sc1 lds
v_mfma_f32_16x16x32_bf16 v[156:159], a[92:95], a[68:71], v[156:159]
ds_read_b128 a[104:107], v244 offset:6144
v_add_u32 v247, s53, v247
v_mfma_f32_16x16x32_bf16 v[160:163], a[80:83], a[72:75], v[160:163]
ds_read_b64_tr_b16 a[124:125], v248 offset:8192
ds_read_b64_tr_b16 a[126:127], v248 offset:12288
v_mfma_f32_16x16x32_bf16 v[164:167], a[84:87], a[72:75], v[164:167]
ds_read_b128 a[108:111], v244 offset:7168
v_add_u32 v248, s53, v248
v_mfma_f32_16x16x32_bf16 v[168:171], a[88:91], a[72:75], v[168:171]
buffer_load_dwordx4 v7, s[4:7], s44 offen offset:2048 sc1 lds
v_mfma_f32_16x16x32_bf16 v[172:175], a[92:95], a[72:75], v[172:175]
s_add_u32 s49, s46, s91
v_add_u32 v244, s53, v244
v_mfma_f32_16x16x32_bf16 v[176:179], a[80:83], a[76:79], v[176:179]
s_add_u32 s46, s46, s53
v_mfma_f32_16x16x32_bf16 v[180:183], a[84:87], a[76:79], v[180:183]
v_mfma_f32_16x16x32_bf16 v[184:187], a[88:91], a[76:79], v[184:187]
buffer_load_dwordx4 v7, s[4:7], s44 offen offset:3072 sc1 lds
v_mfma_f32_16x16x32_bf16 v[188:191], a[92:95], a[76:79], v[188:191]
s_waitcnt vmcnt(18)
s_waitcnt vmcnt(26) lgkmcnt(0)
s_barrier
v_mfma_f32_16x16x32_bf16 v[128:131], a[112:115], a[96:99], v[128:131]
ds_read_b64_tr_b16 a[80:81], v245 offset:0
ds_read_b64_tr_b16 a[82:83], v245 offset:4096
v_mfma_f32_16x16x32_bf16 v[132:135], a[116:119], a[96:99], v[132:135]
s_add_u32 m0, s49, 16384
ds_read_b128 a[64:67], v244 offset:0
v_mfma_f32_16x16x32_bf16 v[136:139], a[120:123], a[96:99], v[136:139]
buffer_load_dwordx4 v240, s[8:11], s45 offen offset:0 lds
v_mfma_f32_16x16x32_bf16 v[140:143], a[124:127], a[96:99], v[140:143]
ds_read_b64_tr_b16 a[84:85], v246 offset:0
ds_read_b64_tr_b16 a[86:87], v246 offset:4096
v_mfma_f32_16x16x32_bf16 v[144:147], a[112:115], a[100:103], v[144:147]
s_add_u32 s44, s44, 4096
ds_read_b128 a[68:71], v244 offset:1024
v_mfma_f32_16x16x32_bf16 v[148:151], a[116:119], a[100:103], v[148:151]
ds_read_b64_tr_b16 a[88:89], v247 offset:0
ds_read_b64_tr_b16 a[90:91], v247 offset:4096
v_mfma_f32_16x16x32_bf16 v[152:155], a[120:123], a[100:103], v[152:155]
buffer_load_dwordx4 v241, s[8:11], s45 offen offset:1024 lds
v_mfma_f32_16x16x32_bf16 v[156:159], a[124:127], a[100:103], v[156:159]
ds_read_b128 a[72:75], v244 offset:4096
v_mfma_f32_16x16x32_bf16 v[160:163], a[112:115], a[104:107], v[160:163]
ds_read_b64_tr_b16 a[92:93], v248 offset:0
ds_read_b64_tr_b16 a[94:95], v248 offset:4096
v_mfma_f32_16x16x32_bf16 v[164:167], a[116:119], a[104:107], v[164:167]
ds_read_b128 a[76:79], v244 offset:5120
v_mfma_f32_16x16x32_bf16 v[168:171], a[120:123], a[104:107], v[168:171]
s_nop 0
buffer_load_dwordx4 v242, s[8:11], s45 offen offset:2048 lds
v_or_b32 v0, 5, v11
v_mfma_f32_16x16x32_bf16 v[172:175], a[124:127], a[104:107], v[172:175]
s_cmp_eq_u32 s46, 0
s_cselect_b32 s53, s56, s57
v_mfma_f32_16x16x32_bf16 v[176:179], a[112:115], a[108:111], v[176:179]
v_mfma_f32_16x16x32_bf16 v[180:183], a[116:119], a[108:111], v[180:183]
v_mfma_f32_16x16x32_bf16 v[184:187], a[120:123], a[108:111], v[184:187]
buffer_load_dwordx4 v243, s[8:11], s45 offen offset:3072 lds
buffer_store_dword v0, v6, s[60:63], 0 offen sc1
v_mfma_f32_16x16x32_bf16 v[188:191], a[124:127], a[108:111], v[188:191]
s_waitcnt lgkmcnt(0)
s_mov_b32 m0, s49
s_add_u32 s45, s45, 131072
v_mfma_f32_16x16x32_bf16 v[128:131], a[80:83], a[64:67], v[128:131]
ds_read_b64_tr_b16 a[112:113], v245 offset:8192
ds_read_b64_tr_b16 a[114:115], v245 offset:12288
v_mfma_f32_16x16x32_bf16 v[132:135], a[84:87], a[64:67], v[132:135]
ds_read_b128 a[96:99], v244 offset:2048
v_add_u32 v245, s53, v245
v_mfma_f32_16x16x32_bf16 v[136:139], a[88:91], a[64:67], v[136:139]
buffer_load_dwordx4 v7, s[4:7], s44 offen offset:0 sc1 lds
v_mfma_f32_16x16x32_bf16 v[140:143], a[92:95], a[64:67], v[140:143]
ds_read_b64_tr_b16 a[116:117], v246 offset:8192
ds_read_b64_tr_b16 a[118:119], v246 offset:12288
v_mfma_f32_16x16x32_bf16 v[144:147], a[80:83], a[68:71], v[144:147]
ds_read_b128 a[100:103], v244 offset:3072
v_add_u32 v246, s53, v246
v_mfma_f32_16x16x32_bf16 v[148:151], a[84:87], a[68:71], v[148:151]
ds_read_b64_tr_b16 a[120:121], v247 offset:8192
ds_read_b64_tr_b16 a[122:123], v247 offset:12288
v_mfma_f32_16x16x32_bf16 v[152:155], a[88:91], a[68:71], v[152:155]
buffer_load_dwordx4 v7, s[4:7], s44 offen offset:1024 sc1 lds
v_mfma_f32_16x16x32_bf16 v[156:159], a[92:95], a[68:71], v[156:159]
ds_read_b128 a[104:107], v244 offset:6144
v_add_u32 v247, s53, v247
v_mfma_f32_16x16x32_bf16 v[160:163], a[80:83], a[72:75], v[160:163]
ds_read_b64_tr_b16 a[124:125], v248 offset:8192
ds_read_b64_tr_b16 a[126:127], v248 offset:12288
v_mfma_f32_16x16x32_bf16 v[164:167], a[84:87], a[72:75], v[164:167]
ds_read_b128 a[108:111], v244 offset:7168
v_add_u32 v248, s53, v248
v_mfma_f32_16x16x32_bf16 v[168:171], a[88:91], a[72:75], v[168:171]
buffer_load_dwordx4 v7, s[4:7], s44 offen offset:2048 sc1 lds
v_mfma_f32_16x16x32_bf16 v[172:175], a[92:95], a[72:75], v[172:175]
s_add_u32 s49, s46, s91
v_add_u32 v244, s53, v244
v_mfma_f32_16x16x32_bf16 v[176:179], a[80:83], a[76:79], v[176:179]
s_add_u32 s46, s46, s53
v_mfma_f32_16x16x32_bf16 v[180:183], a[84:87], a[76:79], v[180:183]
v_mfma_f32_16x16x32_bf16 v[184:187], a[88:91], a[76:79], v[184:187]
buffer_load_dwordx4 v7, s[4:7], s44 offen offset:3072 sc1 lds
v_mfma_f32_16x16x32_bf16 v[188:191], a[92:95], a[76:79], v[188:191]
s_waitcnt vmcnt(25) lgkmcnt(0)
s_barrier
v_mfma_f32_16x16x32_bf16 v[128:131], a[112:115], a[96:99], v[128:131]
ds_read_b64_tr_b16 a[80:81], v245 offset:0
ds_read_b64_tr_b16 a[82:83], v245 offset:4096
v_mfma_f32_16x16x32_bf16 v[132:135], a[116:119], a[96:99], v[132:135]
s_add_u32 m0, s49, 16384
ds_read_b128 a[64:67], v244 offset:0
v_mfma_f32_16x16x32_bf16 v[136:139], a[120:123], a[96:99], v[136:139]
buffer_load_dwordx4 v240, s[8:11], s45 offen offset:0 lds
v_mfma_f32_16x16x32_bf16 v[140:143], a[124:127], a[96:99], v[140:143]
ds_read_b64_tr_b16 a[84:85], v246 offset:0
ds_read_b64_tr_b16 a[86:87], v246 offset:4096
v_mfma_f32_16x16x32_bf16 v[144:147], a[112:115], a[100:103], v[144:147]
s_add_u32 s44, s44, 4096
ds_read_b128 a[68:71], v244 offset:1024
v_mfma_f32_16x16x32_bf16 v[148:151], a[116:119], a[100:103], v[148:151]
ds_read_b64_tr_b16 a[88:89], v247 offset:0
ds_read_b64_tr_b16 a[90:91], v247 offset:4096
v_mfma_f32_16x16x32_bf16 v[152:155], a[120:123], a[100:103], v[152:155]
buffer_load_dwordx4 v241, s[8:11], s45 offen offset:1024 lds
v_mfma_f32_16x16x32_bf16 v[156:159], a[124:127], a[100:103], v[156:159]
ds_read_b128 a[72:75], v244 offset:4096
v_mfma_f32_16x16x32_bf16 v[160:163], a[112:115], a[104:107], v[160:163]
ds_read_b64_tr_b16 a[92:93], v248 offset:0
ds_read_b64_tr_b16 a[94:95], v248 offset:4096
v_mfma_f32_16x16x32_bf16 v[164:167], a[116:119], a[104:107], v[164:167]
ds_read_b128 a[76:79], v244 offset:5120
v_mfma_f32_16x16x32_bf16 v[168:171], a[120:123], a[104:107], v[168:171]
buffer_load_dwordx4 v242, s[8:11], s45 offen offset:2048 lds
v_mfma_f32_16x16x32_bf16 v[172:175], a[124:127], a[104:107], v[172:175]
s_cmp_eq_u32 s46, 0
s_cselect_b32 s53, s56, s57
v_mfma_f32_16x16x32_bf16 v[176:179], a[112:115], a[108:111], v[176:179]
v_mfma_f32_16x16x32_bf16 v[180:183], a[116:119], a[108:111], v[180:183]
v_mfma_f32_16x16x32_bf16 v[184:187], a[120:123], a[108:111], v[184:187]
buffer_load_dwordx4 v243, s[8:11], s45 offen offset:3072 lds
v_mfma_f32_16x16x32_bf16 v[188:191], a[124:127], a[108:111], v[188:191]
s_waitcnt lgkmcnt(0)
s_mov_b32 m0, s49
s_add_u32 s45, s45, 131072
v_mfma_f32_16x16x32_bf16 v[128:131], a[80:83], a[64:67], v[128:131]
ds_read_b64_tr_b16 a[112:113], v245 offset:8192
ds_read_b64_tr_b16 a[114:115], v245 offset:12288
v_mfma_f32_16x16x32_bf16 v[132:135], a[84:87], a[64:67], v[132:135]
ds_read_b128 a[96:99], v244 offset:2048
v_add_u32 v245, s53, v245
v_mfma_f32_16x16x32_bf16 v[136:139], a[88:91], a[64:67], v[136:139]
buffer_load_dwordx4 v7, s[4:7], s44 offen offset:0 sc1 lds
v_mfma_f32_16x16x32_bf16 v[140:143], a[92:95], a[64:67], v[140:143]
ds_read_b64_tr_b16 a[116:117], v246 offset:8192
ds_read_b64_tr_b16 a[118:119], v246 offset:12288
v_mfma_f32_16x16x32_bf16 v[144:147], a[80:83], a[68:71], v[144:147]
ds_read_b128 a[100:103], v244 offset:3072
v_add_u32 v246, s53, v246
v_mfma_f32_16x16x32_bf16 v[148:151], a[84:87], a[68:71], v[148:151]
ds_read_b64_tr_b16 a[120:121], v247 offset:8192
ds_read_b64_tr_b16 a[122:123], v247 offset:12288
v_mfma_f32_16x16x32_bf16 v[152:155], a[88:91], a[68:71], v[152:155]
buffer_load_dwordx4 v7, s[4:7], s44 offen offset:1024 sc1 lds
v_mfma_f32_16x16x32_bf16 v[156:159], a[92:95], a[68:71], v[156:159]
ds_read_b128 a[104:107], v244 offset:6144
v_add_u32 v247, s53, v247
v_mfma_f32_16x16x32_bf16 v[160:163], a[80:83], a[72:75], v[160:163]
ds_read_b64_tr_b16 a[124:125], v248 offset:8192
ds_read_b64_tr_b16 a[126:127], v248 offset:12288
v_mfma_f32_16x16x32_bf16 v[164:167], a[84:87], a[72:75], v[164:167]
ds_read_b128 a[108:111], v244 offset:7168
v_add_u32 v248, s53, v248
v_mfma_f32_16x16x32_bf16 v[168:171], a[88:91], a[72:75], v[168:171]
buffer_load_dwordx4 v7, s[4:7], s44 offen offset:2048 sc1 lds
v_mfma_f32_16x16x32_bf16 v[172:175], a[92:95], a[72:75], v[172:175]
s_add_u32 s49, s46, s91
v_add_u32 v244, s53, v244
v_mfma_f32_16x16x32_bf16 v[176:179], a[80:83], a[76:79], v[176:179]
s_add_u32 s46, s46, s53
v_mfma_f32_16x16x32_bf16 v[180:183], a[84:87], a[76:79], v[180:183]
v_mfma_f32_16x16x32_bf16 v[184:187], a[88:91], a[76:79], v[184:187]
buffer_load_dwordx4 v7, s[4:7], s44 offen offset:3072 sc1 lds
v_mfma_f32_16x16x32_bf16 v[188:191], a[92:95], a[76:79], v[188:191]
s_waitcnt vmcnt(25) lgkmcnt(0)
s_barrier
v_mfma_f32_16x16x32_bf16 v[128:131], a[112:115], a[96:99], v[128:131]
ds_read_b64_tr_b16 a[80:81], v245 offset:0
ds_read_b64_tr_b16 a[82:83], v245 offset:4096
v_mfma_f32_16x16x32_bf16 v[132:135], a[116:119], a[96:99], v[132:135]
s_add_u32 m0, s49, 16384
ds_read_b128 a[64:67], v244 offset:0
v_mfma_f32_16x16x32_bf16 v[136:139], a[120:123], a[96:99], v[136:139]
buffer_load_dwordx4 v240, s[8:11], s45 offen offset:0 lds
v_mfma_f32_16x16x32_bf16 v[140:143], a[124:127], a[96:99], v[140:143]
ds_read_b64_tr_b16 a[84:85], v246 offset:0
ds_read_b64_tr_b16 a[86:87], v246 offset:4096
v_mfma_f32_16x16x32_bf16 v[144:147], a[112:115], a[100:103], v[144:147]
s_add_u32 s44, s44, 4096
ds_read_b128 a[68:71], v244 offset:1024
v_mfma_f32_16x16x32_bf16 v[148:151], a[116:119], a[100:103], v[148:151]
ds_read_b64_tr_b16 a[88:89], v247 offset:0
ds_read_b64_tr_b16 a[90:91], v247 offset:4096
v_mfma_f32_16x16x32_bf16 v[152:155], a[120:123], a[100:103], v[152:155]
buffer_load_dwordx4 v241, s[8:11], s45 offen offset:1024 lds
v_mfma_f32_16x16x32_bf16 v[156:159], a[124:127], a[100:103], v[156:159]
ds_read_b128 a[72:75], v244 offset:4096
v_mfma_f32_16x16x32_bf16 v[160:163], a[112:115], a[104:107], v[160:163]
ds_read_b64_tr_b16 a[92:93], v248 offset:0
ds_read_b64_tr_b16 a[94:95], v248 offset:4096
v_mfma_f32_16x16x32_bf16 v[164:167], a[116:119], a[104:107], v[164:167]
ds_read_b128 a[76:79], v244 offset:5120
v_mfma_f32_16x16x32_bf16 v[168:171], a[120:123], a[104:107], v[168:171]
buffer_load_dwordx4 v242, s[8:11], s45 offen offset:2048 lds
v_mfma_f32_16x16x32_bf16 v[172:175], a[124:127], a[104:107], v[172:175]
s_cmp_eq_u32 s46, 0
s_cselect_b32 s53, s56, s57
v_mfma_f32_16x16x32_bf16 v[176:179], a[112:115], a[108:111], v[176:179]
v_mfma_f32_16x16x32_bf16 v[180:183], a[116:119], a[108:111], v[180:183]
v_mfma_f32_16x16x32_bf16 v[184:187], a[120:123], a[108:111], v[184:187]
buffer_load_dwordx4 v243, s[8:11], s45 offen offset:3072 lds
v_mfma_f32_16x16x32_bf16 v[188:191], a[124:127], a[108:111], v[188:191]
s_waitcnt lgkmcnt(0)
s_mov_b32 m0, s49
s_add_u32 s45, s45, 131072
v_mfma_f32_16x16x32_bf16 v[128:131], a[80:83], a[64:67], v[128:131]
ds_read_b64_tr_b16 a[112:113], v245 offset:8192
ds_read_b64_tr_b16 a[114:115], v245 offset:12288
v_mfma_f32_16x16x32_bf16 v[132:135], a[84:87], a[64:67], v[132:135]
ds_read_b128 a[96:99], v244 offset:2048
v_add_u32 v245, s53, v245
v_mfma_f32_16x16x32_bf16 v[136:139], a[88:91], a[64:67], v[136:139]
buffer_load_dwordx4 v7, s[4:7], s44 offen offset:0 sc1 lds
v_mfma_f32_16x16x32_bf16 v[140:143], a[92:95], a[64:67], v[140:143]
ds_read_b64_tr_b16 a[116:117], v246 offset:8192
ds_read_b64_tr_b16 a[118:119], v246 offset:12288
v_mfma_f32_16x16x32_bf16 v[144:147], a[80:83], a[68:71], v[144:147]
ds_read_b128 a[100:103], v244 offset:3072
v_add_u32 v246, s53, v246
v_mfma_f32_16x16x32_bf16 v[148:151], a[84:87], a[68:71], v[148:151]
ds_read_b64_tr_b16 a[120:121], v247 offset:8192
ds_read_b64_tr_b16 a[122:123], v247 offset:12288
v_mfma_f32_16x16x32_bf16 v[152:155], a[88:91], a[68:71], v[152:155]
buffer_load_dwordx4 v7, s[4:7], s44 offen offset:1024 sc1 lds
v_mfma_f32_16x16x32_bf16 v[156:159], a[92:95], a[68:71], v[156:159]
ds_read_b128 a[104:107], v244 offset:6144
v_add_u32 v247, s53, v247
v_mfma_f32_16x16x32_bf16 v[160:163], a[80:83], a[72:75], v[160:163]
ds_read_b64_tr_b16 a[124:125], v248 offset:8192
ds_read_b64_tr_b16 a[126:127], v248 offset:12288
v_mfma_f32_16x16x32_bf16 v[164:167], a[84:87], a[72:75], v[164:167]
ds_read_b128 a[108:111], v244 offset:7168
v_add_u32 v248, s53, v248
v_mfma_f32_16x16x32_bf16 v[168:171], a[88:91], a[72:75], v[168:171]
buffer_load_dwordx4 v7, s[4:7], s44 offen offset:2048 sc1 lds
v_mfma_f32_16x16x32_bf16 v[172:175], a[92:95], a[72:75], v[172:175]
s_add_u32 s49, s46, s91
v_add_u32 v244, s53, v244
v_mfma_f32_16x16x32_bf16 v[176:179], a[80:83], a[76:79], v[176:179]
s_add_u32 s46, s46, s53
v_mfma_f32_16x16x32_bf16 v[180:183], a[84:87], a[76:79], v[180:183]
.Lpad23:
s_nop 0
v_mfma_f32_16x16x32_bf16 v[184:187], a[88:91], a[76:79], v[184:187]
buffer_load_dwordx4 v7, s[4:7], s44 offen offset:3072 sc1 lds
v_mfma_f32_16x16x32_bf16 v[188:191], a[92:95], a[76:79], v[188:191]
s_waitcnt vmcnt(25) lgkmcnt(0)
s_barrier
v_mfma_f32_16x16x32_bf16 v[128:131], a[112:115], a[96:99], v[128:131]
ds_read_b64_tr_b16 a[80:81], v245 offset:0
ds_read_b64_tr_b16 a[82:83], v245 offset:4096
v_mfma_f32_16x16x32_bf16 v[132:135], a[116:119], a[96:99], v[132:135]
s_add_u32 m0, s49, 16384
ds_read_b128 a[64:67], v244 offset:0
v_mfma_f32_16x16x32_bf16 v[136:139], a[120:123], a[96:99], v[136:139]
buffer_load_dwordx4 v240, s[8:11], s45 offen offset:0 lds
v_mfma_f32_16x16x32_bf16 v[140:143], a[124:127], a[96:99], v[140:143]
ds_read_b64_tr_b16 a[84:85], v246 offset:0
ds_read_b64_tr_b16 a[86:87], v246 offset:4096
v_mfma_f32_16x16x32_bf16 v[144:147], a[112:115], a[100:103], v[144:147]
s_add_u32 s44, s44, 4096
ds_read_b128 a[68:71], v244 offset:1024
v_mfma_f32_16x16x32_bf16 v[148:151], a[116:119], a[100:103], v[148:151]
ds_read_b64_tr_b16 a[88:89], v247 offset:0
ds_read_b64_tr_b16 a[90:91], v247 offset:4096
v_mfma_f32_16x16x32_bf16 v[152:155], a[120:123], a[100:103], v[152:155]
buffer_load_dwordx4 v241, s[8:11], s45 offen offset:1024 lds
v_mfma_f32_16x16x32_bf16 v[156:159], a[124:127], a[100:103], v[156:159]
ds_read_b128 a[72:75], v244 offset:4096
v_mfma_f32_16x16x32_bf16 v[160:163], a[112:115], a[104:107], v[160:163]
ds_read_b64_tr_b16 a[92:93], v248 offset:0
ds_read_b64_tr_b16 a[94:95], v248 offset:4096
v_mfma_f32_16x16x32_bf16 v[164:167], a[116:119], a[104:107], v[164:167]
ds_read_b128 a[76:79], v244 offset:5120
v_mfma_f32_16x16x32_bf16 v[168:171], a[120:123], a[104:107], v[168:171]
buffer_load_dwordx4 v242, s[8:11], s45 offen offset:2048 lds
v_mfma_f32_16x16x32_bf16 v[172:175], a[124:127], a[104:107], v[172:175]
s_cmp_eq_u32 s46, 0
s_cselect_b32 s53, s56, s57
v_mfma_f32_16x16x32_bf16 v[176:179], a[112:115], a[108:111], v[176:179]
v_mfma_f32_16x16x32_bf16 v[180:183], a[116:119], a[108:111], v[180:183]
v_mfma_f32_16x16x32_bf16 v[184:187], a[120:123], a[108:111], v[184:187]
buffer_load_dwordx4 v243, s[8:11], s45 offen offset:3072 lds
v_mfma_f32_16x16x32_bf16 v[188:191], a[124:127], a[108:111], v[188:191]
s_waitcnt lgkmcnt(0)
s_mov_b32 m0, s49
s_add_u32 s45, s45, 131072
v_mfma_f32_16x16x32_bf16 v[128:131], a[80:83], a[64:67], v[128:131]
ds_read_b64_tr_b16 a[112:113], v245 offset:8192
ds_read_b64_tr_b16 a[114:115], v245 offset:12288
v_mfma_f32_16x16x32_bf16 v[132:135], a[84:87], a[64:67], v[132:135]
ds_read_b128 a[96:99], v244 offset:2048
v_add_u32 v245, s53, v245
v_mfma_f32_16x16x32_bf16 v[136:139], a[88:91], a[64:67], v[136:139]
buffer_load_dwordx4 v7, s[4:7], s44 offen offset:0 sc1 lds
v_mfma_f32_16x16x32_bf16 v[140:143], a[92:95], a[64:67], v[140:143]
ds_read_b64_tr_b16 a[116:117], v246 offset:8192
ds_read_b64_tr_b16 a[118:119], v246 offset:12288
v_mfma_f32_16x16x32_bf16 v[144:147], a[80:83], a[68:71], v[144:147]
ds_read_b128 a[100:103], v244 offset:3072
v_add_u32 v246, s53, v246
v_mfma_f32_16x16x32_bf16 v[148:151], a[84:87], a[68:71], v[148:151]
ds_read_b64_tr_b16 a[120:121], v247 offset:8192
ds_read_b64_tr_b16 a[122:123], v247 offset:12288
v_mfma_f32_16x16x32_bf16 v[152:155], a[88:91], a[68:71], v[152:155]
buffer_load_dwordx4 v7, s[4:7], s44 offen offset:1024 sc1 lds
v_mfma_f32_16x16x32_bf16 v[156:159], a[92:95], a[68:71], v[156:159]
ds_read_b128 a[104:107], v244 offset:6144
v_add_u32 v247, s53, v247
v_mfma_f32_16x16x32_bf16 v[160:163], a[80:83], a[72:75], v[160:163]
ds_read_b64_tr_b16 a[124:125], v248 offset:8192
ds_read_b64_tr_b16 a[126:127], v248 offset:12288
v_mfma_f32_16x16x32_bf16 v[164:167], a[84:87], a[72:75], v[164:167]
ds_read_b128 a[108:111], v244 offset:7168
v_add_u32 v248, s53, v248
v_mfma_f32_16x16x32_bf16 v[168:171], a[88:91], a[72:75], v[168:171]
buffer_load_dwordx4 v7, s[4:7], s44 offen offset:2048 sc1 lds
v_mfma_f32_16x16x32_bf16 v[172:175], a[92:95], a[72:75], v[172:175]
s_add_u32 s49, s46, s91
v_add_u32 v244, s53, v244
v_mfma_f32_16x16x32_bf16 v[176:179], a[80:83], a[76:79], v[176:179]
s_add_u32 s46, s46, s53
v_mfma_f32_16x16x32_bf16 v[180:183], a[84:87], a[76:79], v[180:183]
v_mfma_f32_16x16x32_bf16 v[184:187], a[88:91], a[76:79], v[184:187]
buffer_load_dwordx4 v7, s[4:7], s44 offen offset:3072 sc1 lds
v_mfma_f32_16x16x32_bf16 v[188:191], a[92:95], a[76:79], v[188:191]
s_waitcnt vmcnt(24) lgkmcnt(0)
s_barrier
v_mfma_f32_16x16x32_bf16 v[128:131], a[112:115], a[96:99], v[128:131]
ds_read_b64_tr_b16 a[80:81], v245 offset:0
ds_read_b64_tr_b16 a[82:83], v245 offset:4096
v_mfma_f32_16x16x32_bf16 v[132:135], a[116:119], a[96:99], v[132:135]
s_add_u32 m0, s49, 16384
ds_read_b128 a[64:67], v244 offset:0
v_mfma_f32_16x16x32_bf16 v[136:139], a[120:123], a[96:99], v[136:139]
buffer_load_dwordx4 v240, s[8:11], s45 offen offset:0 lds
v_mfma_f32_16x16x32_bf16 v[140:143], a[124:127], a[96:99], v[140:143]
ds_read_b64_tr_b16 a[84:85], v246 offset:0
ds_read_b64_tr_b16 a[86:87], v246 offset:4096
v_mfma_f32_16x16x32_bf16 v[144:147], a[112:115], a[100:103], v[144:147]
s_add_u32 s44, s44, 4096
ds_read_b128 a[68:71], v244 offset:1024
v_mfma_f32_16x16x32_bf16 v[148:151], a[116:119], a[100:103], v[148:151]
ds_read_b64_tr_b16 a[88:89], v247 offset:0
ds_read_b64_tr_b16 a[90:91], v247 offset:4096
v_mfma_f32_16x16x32_bf16 v[152:155], a[120:123], a[100:103], v[152:155]
buffer_load_dwordx4 v241, s[8:11], s45 offen offset:1024 lds
v_mfma_f32_16x16x32_bf16 v[156:159], a[124:127], a[100:103], v[156:159]
ds_read_b128 a[72:75], v244 offset:4096
v_mfma_f32_16x16x32_bf16 v[160:163], a[112:115], a[104:107], v[160:163]
ds_read_b64_tr_b16 a[92:93], v248 offset:0
ds_read_b64_tr_b16 a[94:95], v248 offset:4096
v_mfma_f32_16x16x32_bf16 v[164:167], a[116:119], a[104:107], v[164:167]
ds_read_b128 a[76:79], v244 offset:5120
v_mfma_f32_16x16x32_bf16 v[168:171], a[120:123], a[104:107], v[168:171]
buffer_load_dwordx4 v242, s[8:11], s45 offen offset:2048 lds
v_mfma_f32_16x16x32_bf16 v[172:175], a[124:127], a[104:107], v[172:175]
s_cmp_eq_u32 s46, 0
s_cselect_b32 s53, s56, s57
v_mfma_f32_16x16x32_bf16 v[176:179], a[112:115], a[108:111], v[176:179]
v_mfma_f32_16x16x32_bf16 v[180:183], a[116:119], a[108:111], v[180:183]
v_mfma_f32_16x16x32_bf16 v[184:187], a[120:123], a[108:111], v[184:187]
buffer_load_dwordx4 v243, s[8:11], s45 offen offset:3072 lds
v_mfma_f32_16x16x32_bf16 v[188:191], a[124:127], a[108:111], v[188:191]
s_waitcnt lgkmcnt(0)
s_mov_b32 m0, s49
s_add_u32 s45, s45, 131072
v_mfma_f32_16x16x32_bf16 v[128:131], a[80:83], a[64:67], v[128:131]
ds_read_b64_tr_b16 a[112:113], v245 offset:8192
ds_read_b64_tr_b16 a[114:115], v245 offset:12288
v_mfma_f32_16x16x32_bf16 v[132:135], a[84:87], a[64:67], v[132:135]
ds_read_b128 a[96:99], v244 offset:2048
v_add_u32 v245, s53, v245
v_mfma_f32_16x16x32_bf16 v[136:139], a[88:91], a[64:67], v[136:139]
buffer_load_dwordx4 v7, s[4:7], s44 offen offset:0 sc1 lds
v_mfma_f32_16x16x32_bf16 v[140:143], a[92:95], a[64:67], v[140:143]
ds_read_b64_tr_b16 a[116:117], v246 offset:8192
ds_read_b64_tr_b16 a[118:119], v246 offset:12288
v_mfma_f32_16x16x32_bf16 v[144:147], a[80:83], a[68:71], v[144:147]
ds_read_b128 a[100:103], v244 offset:3072
v_add_u32 v246, s53, v246
v_mfma_f32_16x16x32_bf16 v[148:151], a[84:87], a[68:71], v[148:151]
ds_read_b64_tr_b16 a[120:121], v247 offset:8192
ds_read_b64_tr_b16 a[122:123], v247 offset:12288
v_mfma_f32_16x16x32_bf16 v[152:155], a[88:91], a[68:71], v[152:155]
buffer_load_dwordx4 v7, s[4:7], s44 offen offset:1024 sc1 lds
v_mfma_f32_16x16x32_bf16 v[156:159], a[92:95], a[68:71], v[156:159]
ds_read_b128 a[104:107], v244 offset:6144
v_add_u32 v247, s53, v247
v_mfma_f32_16x16x32_bf16 v[160:163], a[80:83], a[72:75], v[160:163]
ds_read_b64_tr_b16 a[124:125], v248 offset:8192
ds_read_b64_tr_b16 a[126:127], v248 offset:12288
v_mfma_f32_16x16x32_bf16 v[164:167], a[84:87], a[72:75], v[164:167]
ds_read_b128 a[108:111], v244 offset:7168
v_add_u32 v248, s53, v248
v_mfma_f32_16x16x32_bf16 v[168:171], a[88:91], a[72:75], v[168:171]
buffer_load_dwordx4 v7, s[4:7], s44 offen offset:2048 sc1 lds
v_mfma_f32_16x16x32_bf16 v[172:175], a[92:95], a[72:75], v[172:175]
s_add_u32 s49, s46, s91
v_add_u32 v244, s53, v244
v_mfma_f32_16x16x32_bf16 v[176:179], a[80:83], a[76:79], v[176:179]
s_add_u32 s46, s46, s53
v_mfma_f32_16x16x32_bf16 v[180:183], a[84:87], a[76:79], v[180:183]
v_mfma_f32_16x16x32_bf16 v[184:187], a[88:91], a[76:79], v[184:187]
buffer_load_dwordx4 v7, s[4:7], s44 offen offset:3072 sc1 lds
v_mfma_f32_16x16x32_bf16 v[188:191], a[92:95], a[76:79], v[188:191]
s_waitcnt vmcnt(24) lgkmcnt(0)
s_barrier
v_mfma_f32_16x16x32_bf16 v[128:131], a[112:115], a[96:99], v[128:131]
ds_read_b64_tr_b16 a[80:81], v245 offset:0
ds_read_b64_tr_b16 a[82:83], v245 offset:4096
v_mfma_f32_16x16x32_bf16 v[132:135], a[116:119], a[96:99], v[132:135]
ds_read_b128 a[64:67], v244 offset:0
v_mfma_f32_16x16x32_bf16 v[136:139], a[120:123], a[96:99], v[136:139]
v_mfma_f32_16x16x32_bf16 v[140:143], a[124:127], a[96:99], v[140:143]
ds_read_b64_tr_b16 a[84:85], v246 offset:0
ds_read_b64_tr_b16 a[86:87], v246 offset:4096
v_mfma_f32_16x16x32_bf16 v[144:147], a[112:115], a[100:103], v[144:147]
ds_read_b128 a[68:71], v244 offset:1024
v_mfma_f32_16x16x32_bf16 v[148:151], a[116:119], a[100:103], v[148:151]
ds_read_b64_tr_b16 a[88:89], v247 offset:0
ds_read_b64_tr_b16 a[90:91], v247 offset:4096
v_mfma_f32_16x16x32_bf16 v[152:155], a[120:123], a[100:103], v[152:155]
v_mfma_f32_16x16x32_bf16 v[156:159], a[124:127], a[100:103], v[156:159]
ds_read_b128 a[72:75], v244 offset:4096
v_mfma_f32_16x16x32_bf16 v[160:163], a[112:115], a[104:107], v[160:163]
ds_read_b64_tr_b16 a[92:93], v248 offset:0
ds_read_b64_tr_b16 a[94:95], v248 offset:4096
v_mfma_f32_16x16x32_bf16 v[164:167], a[116:119], a[104:107], v[164:167]
ds_read_b128 a[76:79], v244 offset:5120
v_mfma_f32_16x16x32_bf16 v[168:171], a[120:123], a[104:107], v[168:171]
v_mfma_f32_16x16x32_bf16 v[172:175], a[124:127], a[104:107], v[172:175]
s_cmp_eq_u32 s46, 0
s_cselect_b32 s53, s56, s57
v_mfma_f32_16x16x32_bf16 v[176:179], a[112:115], a[108:111], v[176:179]
v_mfma_f32_16x16x32_bf16 v[180:183], a[116:119], a[108:111], v[180:183]
v_mfma_f32_16x16x32_bf16 v[184:187], a[120:123], a[108:111], v[184:187]
v_mfma_f32_16x16x32_bf16 v[188:191], a[124:127], a[108:111], v[188:191]
s_waitcnt lgkmcnt(0)
v_mfma_f32_16x16x32_bf16 v[128:131], a[80:83], a[64:67], v[128:131]
ds_read_b64_tr_b16 a[112:113], v245 offset:8192
ds_read_b64_tr_b16 a[114:115], v245 offset:12288
v_mfma_f32_16x16x32_bf16 v[132:135], a[84:87], a[64:67], v[132:135]
ds_read_b128 a[96:99], v244 offset:2048
v_add_u32 v245, s53, v245
v_mfma_f32_16x16x32_bf16 v[136:139], a[88:91], a[64:67], v[136:139]
v_mfma_f32_16x16x32_bf16 v[140:143], a[92:95], a[64:67], v[140:143]
ds_read_b64_tr_b16 a[116:117], v246 offset:8192
ds_read_b64_tr_b16 a[118:119], v246 offset:12288
v_mfma_f32_16x16x32_bf16 v[144:147], a[80:83], a[68:71], v[144:147]
ds_read_b128 a[100:103], v244 offset:3072
v_add_u32 v246, s53, v246
v_mfma_f32_16x16x32_bf16 v[148:151], a[84:87], a[68:71], v[148:151]
ds_read_b64_tr_b16 a[120:121], v247 offset:8192
ds_read_b64_tr_b16 a[122:123], v247 offset:12288
v_mfma_f32_16x16x32_bf16 v[152:155], a[88:91], a[68:71], v[152:155]
v_mfma_f32_16x16x32_bf16 v[156:159], a[92:95], a[68:71], v[156:159]
ds_read_b128 a[104:107], v244 offset:6144
v_add_u32 v247, s53, v247
v_mfma_f32_16x16x32_bf16 v[160:163], a[80:83], a[72:75], v[160:163]
ds_read_b64_tr_b16 a[124:125], v248 offset:8192
ds_read_b64_tr_b16 a[126:127], v248 offset:12288
v_mfma_f32_16x16x32_bf16 v[164:167], a[84:87], a[72:75], v[164:167]
ds_read_b128 a[108:111], v244 offset:7168
v_add_u32 v248, s53, v248
v_mfma_f32_16x16x32_bf16 v[168:171], a[88:91], a[72:75], v[168:171]
v_mfma_f32_16x16x32_bf16 v[172:175], a[92:95], a[72:75], v[172:175]
s_add_u32 s49, s46, s91
v_add_u32 v244, s53, v244
v_mfma_f32_16x16x32_bf16 v[176:179], a[80:83], a[76:79], v[176:179]
s_add_u32 s46, s46, s53
v_mfma_f32_16x16x32_bf16 v[180:183], a[84:87], a[76:79], v[180:183]
v_mfma_f32_16x16x32_bf16 v[184:187], a[88:91], a[76:79], v[184:187]
v_mfma_f32_16x16x32_bf16 v[188:191], a[92:95], a[76:79], v[188:191]
s_waitcnt vmcnt(16) lgkmcnt(0)
s_barrier
v_mfma_f32_16x16x32_bf16 v[128:131], a[112:115], a[96:99], v[128:131]
ds_read_b64_tr_b16 a[80:81], v245 offset:0
ds_read_b64_tr_b16 a[82:83], v245 offset:4096
v_mfma_f32_16x16x32_bf16 v[132:135], a[116:119], a[96:99], v[132:135]
ds_read_b128 a[64:67], v244 offset:0
v_mfma_f32_16x16x32_bf16 v[136:139], a[120:123], a[96:99], v[136:139]
v_mfma_f32_16x16x32_bf16 v[140:143], a[124:127], a[96:99], v[140:143]
ds_read_b64_tr_b16 a[84:85], v246 offset:0
ds_read_b64_tr_b16 a[86:87], v246 offset:4096
v_mfma_f32_16x16x32_bf16 v[144:147], a[112:115], a[100:103], v[144:147]
ds_read_b128 a[68:71], v244 offset:1024
v_mfma_f32_16x16x32_bf16 v[148:151], a[116:119], a[100:103], v[148:151]
ds_read_b64_tr_b16 a[88:89], v247 offset:0
ds_read_b64_tr_b16 a[90:91], v247 offset:4096
v_mfma_f32_16x16x32_bf16 v[152:155], a[120:123], a[100:103], v[152:155]
v_mfma_f32_16x16x32_bf16 v[156:159], a[124:127], a[100:103], v[156:159]
ds_read_b128 a[72:75], v244 offset:4096
v_mfma_f32_16x16x32_bf16 v[160:163], a[112:115], a[104:107], v[160:163]
ds_read_b64_tr_b16 a[92:93], v248 offset:0
ds_read_b64_tr_b16 a[94:95], v248 offset:4096
v_mfma_f32_16x16x32_bf16 v[164:167], a[116:119], a[104:107], v[164:167]
ds_read_b128 a[76:79], v244 offset:5120
v_mfma_f32_16x16x32_bf16 v[168:171], a[120:123], a[104:107], v[168:171]
v_mfma_f32_16x16x32_bf16 v[172:175], a[124:127], a[104:107], v[172:175]
s_cmp_eq_u32 s46, 0
s_cselect_b32 s53, s56, s57
v_mfma_f32_16x16x32_bf16 v[176:179], a[112:115], a[108:111], v[176:179]
v_mfma_f32_16x16x32_bf16 v[180:183], a[116:119], a[108:111], v[180:183]
v_mfma_f32_16x16x32_bf16 v[184:187], a[120:123], a[108:111], v[184:187]
v_mfma_f32_16x16x32_bf16 v[188:191], a[124:127], a[108:111], v[188:191]
s_waitcnt lgkmcnt(0)
v_mfma_f32_16x16x32_bf16 v[128:131], a[80:83], a[64:67], v[128:131]
ds_read_b64_tr_b16 a[112:113], v245 offset:8192
ds_read_b64_tr_b16 a[114:115], v245 offset:12288
v_mfma_f32_16x16x32_bf16 v[132:135], a[84:87], a[64:67], v[132:135]
ds_read_b128 a[96:99], v244 offset:2048
v_add_u32 v245, s53, v245
v_mfma_f32_16x16x32_bf16 v[136:139], a[88:91], a[64:67], v[136:139]
v_mfma_f32_16x16x32_bf16 v[140:143], a[92:95], a[64:67], v[140:143]
ds_read_b64_tr_b16 a[116:117], v246 offset:8192
ds_read_b64_tr_b16 a[118:119], v246 offset:12288
v_mfma_f32_16x16x32_bf16 v[144:147], a[80:83], a[68:71], v[144:147]
ds_read_b128 a[100:103], v244 offset:3072
v_add_u32 v246, s53, v246
v_mfma_f32_16x16x32_bf16 v[148:151], a[84:87], a[68:71], v[148:151]
ds_read_b64_tr_b16 a[120:121], v247 offset:8192
ds_read_b64_tr_b16 a[122:123], v247 offset:12288
v_mfma_f32_16x16x32_bf16 v[152:155], a[88:91], a[68:71], v[152:155]
v_mfma_f32_16x16x32_bf16 v[156:159], a[92:95], a[68:71], v[156:159]
ds_read_b128 a[104:107], v244 offset:6144
v_add_u32 v247, s53, v247
v_mfma_f32_16x16x32_bf16 v[160:163], a[80:83], a[72:75], v[160:163]
ds_read_b64_tr_b16 a[124:125], v248 offset:8192
ds_read_b64_tr_b16 a[126:127], v248 offset:12288
v_mfma_f32_16x16x32_bf16 v[164:167], a[84:87], a[72:75], v[164:167]
ds_read_b128 a[108:111], v244 offset:7168
v_add_u32 v248, s53, v248
v_mfma_f32_16x16x32_bf16 v[168:171], a[88:91], a[72:75], v[168:171]
v_mfma_f32_16x16x32_bf16 v[172:175], a[92:95], a[72:75], v[172:175]
s_add_u32 s49, s46, s91
v_add_u32 v244, s53, v244
v_mfma_f32_16x16x32_bf16 v[176:179], a[80:83], a[76:79], v[176:179]
s_add_u32 s46, s46, s53
v_mfma_f32_16x16x32_bf16 v[180:183], a[84:87], a[76:79], v[180:183]
v_mfma_f32_16x16x32_bf16 v[184:187], a[88:91], a[76:79], v[184:187]
v_mfma_f32_16x16x32_bf16 v[188:191], a[92:95], a[76:79], v[188:191]
s_waitcnt vmcnt(8) lgkmcnt(0)
s_barrier
v_mfma_f32_16x16x32_bf16 v[128:131], a[112:115], a[96:99], v[128:131]
ds_read_b64_tr_b16 a[80:81], v245 offset:0
ds_read_b64_tr_b16 a[82:83], v245 offset:4096
v_mfma_f32_16x16x32_bf16 v[132:135], a[116:119], a[96:99], v[132:135]
ds_read_b128 a[64:67], v244 offset:0
v_mfma_f32_16x16x32_bf16 v[136:139], a[120:123], a[96:99], v[136:139]
v_mfma_f32_16x16x32_bf16 v[140:143], a[124:127], a[96:99], v[140:143]
ds_read_b64_tr_b16 a[84:85], v246 offset:0
ds_read_b64_tr_b16 a[86:87], v246 offset:4096
v_mfma_f32_16x16x32_bf16 v[144:147], a[112:115], a[100:103], v[144:147]
ds_read_b128 a[68:71], v244 offset:1024
v_mfma_f32_16x16x32_bf16 v[148:151], a[116:119], a[100:103], v[148:151]
ds_read_b64_tr_b16 a[88:89], v247 offset:0
ds_read_b64_tr_b16 a[90:91], v247 offset:4096
v_mfma_f32_16x16x32_bf16 v[152:155], a[120:123], a[100:103], v[152:155]
v_mfma_f32_16x16x32_bf16 v[156:159], a[124:127], a[100:103], v[156:159]
ds_read_b128 a[72:75], v244 offset:4096
v_mfma_f32_16x16x32_bf16 v[160:163], a[112:115], a[104:107], v[160:163]
ds_read_b64_tr_b16 a[92:93], v248 offset:0
ds_read_b64_tr_b16 a[94:95], v248 offset:4096
v_mfma_f32_16x16x32_bf16 v[164:167], a[116:119], a[104:107], v[164:167]
ds_read_b128 a[76:79], v244 offset:5120
v_mfma_f32_16x16x32_bf16 v[168:171], a[120:123], a[104:107], v[168:171]
v_mfma_f32_16x16x32_bf16 v[172:175], a[124:127], a[104:107], v[172:175]
s_cmp_eq_u32 s46, 0
s_cselect_b32 s53, s56, s57
v_mfma_f32_16x16x32_bf16 v[176:179], a[112:115], a[108:111], v[176:179]
v_mfma_f32_16x16x32_bf16 v[180:183], a[116:119], a[108:111], v[180:183]
v_mfma_f32_16x16x32_bf16 v[184:187], a[120:123], a[108:111], v[184:187]
v_mfma_f32_16x16x32_bf16 v[188:191], a[124:127], a[108:111], v[188:191]
s_waitcnt lgkmcnt(0)
v_mfma_f32_16x16x32_bf16 v[128:131], a[80:83], a[64:67], v[128:131]
ds_read_b64_tr_b16 a[112:113], v245 offset:8192
ds_read_b64_tr_b16 a[114:115], v245 offset:12288
v_mfma_f32_16x16x32_bf16 v[132:135], a[84:87], a[64:67], v[132:135]
ds_read_b128 a[96:99], v244 offset:2048
v_add_u32 v245, s53, v245
v_mfma_f32_16x16x32_bf16 v[136:139], a[88:91], a[64:67], v[136:139]
v_mfma_f32_16x16x32_bf16 v[140:143], a[92:95], a[64:67], v[140:143]
ds_read_b64_tr_b16 a[116:117], v246 offset:8192
ds_read_b64_tr_b16 a[118:119], v246 offset:12288
v_mfma_f32_16x16x32_bf16 v[144:147], a[80:83], a[68:71], v[144:147]
ds_read_b128 a[100:103], v244 offset:3072
v_add_u32 v246, s53, v246
v_mfma_f32_16x16x32_bf16 v[148:151], a[84:87], a[68:71], v[148:151]
ds_read_b64_tr_b16 a[120:121], v247 offset:8192
ds_read_b64_tr_b16 a[122:123], v247 offset:12288
v_mfma_f32_16x16x32_bf16 v[152:155], a[88:91], a[68:71], v[152:155]
v_mfma_f32_16x16x32_bf16 v[156:159], a[92:95], a[68:71], v[156:159]
ds_read_b128 a[104:107], v244 offset:6144
v_add_u32 v247, s53, v247
v_mfma_f32_16x16x32_bf16 v[160:163], a[80:83], a[72:75], v[160:163]
ds_read_b64_tr_b16 a[124:125], v248 offset:8192
ds_read_b64_tr_b16 a[126:127], v248 offset:12288
v_mfma_f32_16x16x32_bf16 v[164:167], a[84:87], a[72:75], v[164:167]
ds_read_b128 a[108:111], v244 offset:7168
v_add_u32 v248, s53, v248
v_mfma_f32_16x16x32_bf16 v[168:171], a[88:91], a[72:75], v[168:171]
v_mfma_f32_16x16x32_bf16 v[172:175], a[92:95], a[72:75], v[172:175]
s_add_u32 s49, s46, s91
v_add_u32 v244, s53, v244
v_mfma_f32_16x16x32_bf16 v[176:179], a[80:83], a[76:79], v[176:179]
s_add_u32 s46, s46, s53
v_mfma_f32_16x16x32_bf16 v[180:183], a[84:87], a[76:79], v[180:183]
v_mfma_f32_16x16x32_bf16 v[184:187], a[88:91], a[76:79], v[184:187]
v_mfma_f32_16x16x32_bf16 v[188:191], a[92:95], a[76:79], v[188:191]
s_waitcnt vmcnt(0) lgkmcnt(0)
s_barrier
v_mfma_f32_16x16x32_bf16 v[128:131], a[112:115], a[96:99], v[128:131]
ds_read_b64_tr_b16 a[80:81], v245 offset:0
ds_read_b64_tr_b16 a[82:83], v245 offset:4096
v_mfma_f32_16x16x32_bf16 v[132:135], a[116:119], a[96:99], v[132:135]
ds_read_b128 a[64:67], v244 offset:0
ds_read_b128 a[68:71], v244 offset:1024
v_mfma_f32_16x16x32_bf16 v[136:139], a[120:123], a[96:99], v[136:139]
ds_read_b64_tr_b16 a[84:85], v246 offset:0
ds_read_b64_tr_b16 a[86:87], v246 offset:4096
v_mfma_f32_16x16x32_bf16 v[140:143], a[124:127], a[96:99], v[140:143]
ds_read_b64_tr_b16 a[88:89], v247 offset:0
ds_read_b64_tr_b16 a[90:91], v247 offset:4096
v_mfma_f32_16x16x32_bf16 v[144:147], a[112:115], a[100:103], v[144:147]
ds_read_b64_tr_b16 a[92:93], v248 offset:0
ds_read_b64_tr_b16 a[94:95], v248 offset:4096
v_mfma_f32_16x16x32_bf16 v[148:151], a[116:119], a[100:103], v[148:151]
ds_read_b64_tr_b16 a[144:145], v245 offset:8192
ds_read_b64_tr_b16 a[146:147], v245 offset:12288
v_mfma_f32_16x16x32_bf16 v[152:155], a[120:123], a[100:103], v[152:155]
ds_read_b128 a[128:131], v244 offset:2048
ds_read_b128 a[132:135], v244 offset:3072
v_mfma_f32_16x16x32_bf16 v[156:159], a[124:127], a[100:103], v[156:159]
ds_read_b64_tr_b16 a[148:149], v246 offset:8192
ds_read_b64_tr_b16 a[150:151], v246 offset:12288
v_mfma_f32_16x16x32_bf16 v[160:163], a[112:115], a[104:107], v[160:163]
ds_read_b64_tr_b16 a[152:153], v247 offset:8192
ds_read_b64_tr_b16 a[154:155], v247 offset:12288
v_mfma_f32_16x16x32_bf16 v[164:167], a[116:119], a[104:107], v[164:167]
ds_read_b64_tr_b16 a[156:157], v248 offset:8192
ds_read_b64_tr_b16 a[158:159], v248 offset:12288
v_mfma_f32_16x16x32_bf16 v[168:171], a[120:123], a[104:107], v[168:171]
ds_read_b128 a[72:75], v244 offset:4096
ds_read_b128 a[76:79], v244 offset:5120
v_mfma_f32_16x16x32_bf16 v[172:175], a[124:127], a[104:107], v[172:175]
ds_read_b128 a[136:139], v244 offset:6144
ds_read_b128 a[140:143], v244 offset:7168
v_mfma_f32_16x16x32_bf16 v[176:179], a[112:115], a[108:111], v[176:179]
s_mov_b32 s24, 0
s_mov_b32 s25, 32768
v_mfma_f32_16x16x32_bf16 v[180:183], a[116:119], a[108:111], v[180:183]
s_mov_b32 s26, 65536
s_mov_b32 s27, 98304
v_mfma_f32_16x16x32_bf16 v[184:187], a[120:123], a[108:111], v[184:187]
s_mov_b32 s28, 131072
s_mov_b32 s29, 163840
v_mfma_f32_16x16x32_bf16 v[188:191], a[124:127], a[108:111], v[188:191]
s_mov_b32 s30, 196608
s_mov_b32 s31, 229376
s_waitcnt lgkmcnt(0)
v_mfma_f32_16x16x32_bf16 v[128:131], a[80:83], a[64:67], v[128:131]
v_mfma_f32_16x16x32_bf16 v[132:135], a[84:87], a[64:67], v[132:135]
v_mfma_f32_16x16x32_bf16 v[136:139], a[88:91], a[64:67], v[136:139]
v_mfma_f32_16x16x32_bf16 v[140:143], a[92:95], a[64:67], v[140:143]
v_mfma_f32_16x16x32_bf16 v[128:131], a[144:147], a[128:131], v[128:131]
v_mfma_f32_16x16x32_bf16 v[132:135], a[148:151], a[128:131], v[132:135]
v_mfma_f32_16x16x32_bf16 v[136:139], a[152:155], a[128:131], v[136:139]
v_mfma_f32_16x16x32_bf16 v[140:143], a[156:159], a[128:131], v[140:143]
v_mfma_f32_16x16x32_bf16 v[144:147], a[80:83], a[68:71], v[144:147]
v_mfma_f32_16x16x32_bf16 v[148:151], a[84:87], a[68:71], v[148:151]
v_mfma_f32_16x16x32_bf16 v[152:155], a[88:91], a[68:71], v[152:155]
s_nop 1
v_cvt_pk_bf16_f32 v192, v128, v129
v_cvt_pk_bf16_f32 v194, v132, v133
v_cvt_pk_bf16_f32 v193, v130, v131
v_mfma_f32_16x16x32_bf16 v[156:159], a[92:95], a[68:71], v[156:159]
v_cvt_pk_bf16_f32 v195, v134, v135
v_cvt_pk_bf16_f32 v196, v136, v137
v_cvt_pk_bf16_f32 v198, v140, v141
v_mfma_f32_16x16x32_bf16 v[144:147], a[144:147], a[132:135], v[144:147]
v_cvt_pk_bf16_f32 v197, v138, v139
v_cvt_pk_bf16_f32 v199, v142, v143
v_mfma_f32_16x16x32_bf16 v[148:151], a[148:151], a[132:135], v[148:151]
v_permlane16_swap_b32 v192, v194
v_mfma_f32_16x16x32_bf16 v[152:155], a[152:155], a[132:135], v[152:155]
v_permlane16_swap_b32 v193, v195
v_mfma_f32_16x16x32_bf16 v[156:159], a[156:159], a[132:135], v[156:159]
v_permlane16_swap_b32 v196, v198
v_mfma_f32_16x16x32_bf16 v[160:163], a[80:83], a[72:75], v[160:163]
v_permlane16_swap_b32 v197, v199
v_mov_b32 v200, v192
v_mfma_f32_16x16x32_bf16 v[164:167], a[84:87], a[72:75], v[164:167]
v_mov_b32 v201, v193
v_mov_b32 v202, v194
v_mov_b32 v203, v195
v_mfma_f32_16x16x32_bf16 v[168:171], a[88:91], a[72:75], v[168:171]
v_mov_b32_dpp v192, v196 row_ror:8 row_mask:0xf bank_mask:0xc
v_mov_b32_dpp v193, v197 row_ror:8 row_mask:0xf bank_mask:0xc
v_mov_b32_dpp v194, v198 row_ror:8 row_mask:0xf bank_mask:0xc
v_mfma_f32_16x16x32_bf16 v[172:175], a[92:95], a[72:75], v[172:175]
v_mov_b32_dpp v195, v199 row_ror:8 row_mask:0xf bank_mask:0xc
v_mfma_f32_16x16x32_bf16 v[160:163], a[144:147], a[136:139], v[160:163]
buffer_store_dwordx4 v[192:195], v251, s[16:19], s24 offen nt
v_mov_b32_dpp v196, v200 row_ror:8 row_mask:0xf bank_mask:0x3
v_mov_b32_dpp v197, v201 row_ror:8 row_mask:0xf bank_mask:0x3
v_mfma_f32_16x16x32_bf16 v[164:167], a[148:151], a[136:139], v[164:167]
v_mov_b32_dpp v198, v202 row_ror:8 row_mask:0xf bank_mask:0x3
v_mov_b32_dpp v199, v203 row_ror:8 row_mask:0xf bank_mask:0x3
v_mfma_f32_16x16x32_bf16 v[168:171], a[152:155], a[136:139], v[168:171]
buffer_store_dwordx4 v[196:199], v251, s[16:19], s25 offen nt
v_cvt_pk_bf16_f32 v204, v144, v145
v_cvt_pk_bf16_f32 v206, v148, v149
v_mfma_f32_16x16x32_bf16 v[172:175], a[156:159], a[136:139], v[172:175]
v_cvt_pk_bf16_f32 v205, v146, v147
v_cvt_pk_bf16_f32 v207, v150, v151
v_cvt_pk_bf16_f32 v208, v152, v153
v_mfma_f32_16x16x32_bf16 v[176:179], a[80:83], a[76:79], v[176:179]
v_cvt_pk_bf16_f32 v210, v156, v157
v_cvt_pk_bf16_f32 v209, v154, v155
v_cvt_pk_bf16_f32 v211, v158, v159
v_mfma_f32_16x16x32_bf16 v[180:183], a[84:87], a[76:79], v[180:183]
v_permlane16_swap_b32 v204, v206
v_mfma_f32_16x16x32_bf16 v[184:187], a[88:91], a[76:79], v[184:187]
v_permlane16_swap_b32 v205, v207
v_mfma_f32_16x16x32_bf16 v[188:191], a[92:95], a[76:79], v[188:191]
v_permlane16_swap_b32 v208, v210
v_mfma_f32_16x16x32_bf16 v[176:179], a[144:147], a[140:143], v[176:179]
v_permlane16_swap_b32 v209, v211
v_mov_b32 v212, v204
v_mfma_f32_16x16x32_bf16 v[180:183], a[148:151], a[140:143], v[180:183]
v_mov_b32 v213, v205
v_mov_b32 v214, v206
v_mov_b32 v215, v207
v_mfma_f32_16x16x32_bf16 v[184:187], a[152:155], a[140:143], v[184:187]
v_mov_b32_dpp v204, v208 row_ror:8 row_mask:0xf bank_mask:0xc
v_mov_b32_dpp v205, v209 row_ror:8 row_mask:0xf bank_mask:0xc
v_mov_b32_dpp v206, v210 row_ror:8 row_mask:0xf bank_mask:0xc
v_mfma_f32_16x16x32_bf16 v[188:191], a[156:159], a[140:143], v[188:191]
v_mov_b32_dpp v207, v211 row_ror:8 row_mask:0xf bank_mask:0xc
buffer_store_dwordx4 v[204:207], v251, s[16:19], s26 offen nt
v_mov_b32_dpp v208, v212 row_ror:8 row_mask:0xf bank_mask:0x3
v_mov_b32_dpp v209, v213 row_ror:8 row_mask:0xf bank_mask:0x3
v_mov_b32_dpp v210, v214 row_ror:8 row_mask:0xf bank_mask:0x3
v_mov_b32_dpp v211, v215 row_ror:8 row_mask:0xf bank_mask:0x3
buffer_store_dwordx4 v[208:211], v251, s[16:19], s27 offen nt
v_cvt_pk_bf16_f32 v192, v160, v161
v_cvt_pk_bf16_f32 v194, v164, v165
v_cvt_pk_bf16_f32 v193, v162, v163
v_cvt_pk_bf16_f32 v195, v166, v167
v_cvt_pk_bf16_f32 v196, v168, v169
v_cvt_pk_bf16_f32 v198, v172, v173
v_cvt_pk_bf16_f32 v197, v170, v171
v_cvt_pk_bf16_f32 v199, v174, v175
v_permlane16_swap_b32 v192, v194
v_permlane16_swap_b32 v193, v195
v_permlane16_swap_b32 v196, v198
v_permlane16_swap_b32 v197, v199
v_mov_b32 v200, v192
v_mov_b32 v201, v193
v_mov_b32 v202, v194
v_mov_b32 v203, v195
v_mov_b32_dpp v192, v196 row_ror:8 row_mask:0xf bank_mask:0xc
v_mov_b32_dpp v193, v197 row_ror:8 row_mask:0xf bank_mask:0xc
v_mov_b32_dpp v194, v198 row_ror:8 row_mask:0xf bank_mask:0xc
v_mov_b32_dpp v195, v199 row_ror:8 row_mask:0xf bank_mask:0xc
buffer_store_dwordx4 v[192:195], v251, s[16:19], s28 offen nt
v_mov_b32_dpp v196, v200 row_ror:8 row_mask:0xf bank_mask:0x3
v_mov_b32_dpp v197, v201 row_ror:8 row_mask:0xf bank_mask:0x3
v_mov_b32_dpp v198, v202 row_ror:8 row_mask:0xf bank_mask:0x3
v_mov_b32_dpp v199, v203 row_ror:8 row_mask:0xf bank_mask:0x3
buffer_store_dwordx4 v[196:199], v251, s[16:19], s29 offen nt
v_cvt_pk_bf16_f32 v204, v176, v177
v_cvt_pk_bf16_f32 v206, v180, v181
v_cvt_pk_bf16_f32 v205, v178, v179
v_cvt_pk_bf16_f32 v207, v182, v183
v_cvt_pk_bf16_f32 v208, v184, v185
v_cvt_pk_bf16_f32 v210, v188, v189
v_cvt_pk_bf16_f32 v209, v186, v187
v_cvt_pk_bf16_f32 v211, v190, v191
v_permlane16_swap_b32 v204, v206
v_permlane16_swap_b32 v205, v207
v_permlane16_swap_b32 v208, v210
v_permlane16_swap_b32 v209, v211
v_mov_b32 v212, v204
v_mov_b32 v213, v205
v_mov_b32 v214, v206
v_mov_b32 v215, v207
v_mov_b32_dpp v204, v208 row_ror:8 row_mask:0xf bank_mask:0xc
v_mov_b32_dpp v205, v209 row_ror:8 row_mask:0xf bank_mask:0xc
v_mov_b32_dpp v206, v210 row_ror:8 row_mask:0xf bank_mask:0xc
v_mov_b32_dpp v207, v211 row_ror:8 row_mask:0xf bank_mask:0xc
buffer_store_dwordx4 v[204:207], v251, s[16:19], s30 offen nt
v_mov_b32_dpp v208, v212 row_ror:8 row_mask:0xf bank_mask:0x3
v_mov_b32_dpp v209, v213 row_ror:8 row_mask:0xf bank_mask:0x3
v_mov_b32_dpp v210, v214 row_ror:8 row_mask:0xf bank_mask:0x3
v_mov_b32_dpp v211, v215 row_ror:8 row_mask:0xf bank_mask:0x3
buffer_store_dwordx4 v[208:211], v251, s[16:19], s31 offen nt
.Lend:
s_endpgm
.Lspin1:
s_mov_b32 s93, 0
.Lspin1_l:
s_sleep 4
buffer_load_dword v8, v3, s[60:63], s90 offen sc1
s_waitcnt vmcnt(0)
v_and_b32 v0, 7, v8
v_cmp_lt_u32 s[28:29], v0, v14
v_cmp_eq_u32 s[30:31], v8, v2
s_nop 0
s_or_b64 s[28:29], s[28:29], s[30:31]
s_cbranch_scc0 .Lspin1_ret
s_add_u32 s93, s93, 1
s_cmp_lt_u32 s93, 0x100000
s_cbranch_scc1 .Lspin1_l
.Lpad24:
s_nop 0
s_branch .Lspin1_ret
.Lj0_pad:
s_cbranch_execz .Ltch2
.Lj0a:
v_cmp_gt_i32 s[32:33], 0, v8
v_nop
v_cmp_gt_i32 s[34:35], 1, v8
v_cndmask_b32 v160, v160, v34, s[32:33]
v_cmp_gt_i32 s[32:33], 2, v8
v_cndmask_b32 v161, v161, v34, s[34:35]
v_cmp_gt_i32 s[34:35], 3, v8
v_cndmask_b32 v162, v162, v34, s[32:33]
v_cmp_gt_i32 s[32:33], 16, v8
v_cndmask_b32 v163, v163, v34, s[34:35]
v_cmp_gt_i32 s[34:35], 17, v8
v_cndmask_b32 v164, v164, v34, s[32:33]
v_cmp_gt_i32 s[32:33], 18, v8
v_cndmask_b32 v165, v165, v34, s[34:35]
v_cmp_gt_i32 s[34:35], 19, v8
v_cndmask_b32 v166, v166, v34, s[32:33]
v_cmp_gt_i32 s[32:33], -16, v8
v_cndmask_b32 v167, v167, v34, s[34:35]
v_cmp_gt_i32 s[34:35], -15, v8
v_cndmask_b32 v168, v168, v34, s[32:33]
v_cmp_gt_i32 s[32:33], -14, v8
v_cndmask_b32 v169, v169, v34, s[34:35]
v_cmp_gt_i32 s[34:35], -13, v8
v_cndmask_b32 v170, v170, v34, s[32:33]
v_cmp_gt_i32 s[32:33], 0, v8
v_cndmask_b32 v171, v171, v34, s[34:35]
v_cmp_gt_i32 s[34:35], 1, v8
v_cndmask_b32 v172, v172, v34, s[32:33]
v_cmp_gt_i32 s[32:33], 2, v8
v_cndmask_b32 v173, v173, v34, s[34:35]
v_cmp_gt_i32 s[34:35], 3, v8
v_cndmask_b32 v174, v174, v34, s[32:33]
v_nop
v_cndmask_b32 v175, v175, v34, s[34:35]
s_branch .Lj0a_ret
.Lj0b:
s_cmp_eq_u32 s51, 1
s_cbranch_scc1 .Lj0b1
v_mov_b32 v176, v34
v_mov_b32 v177, v34
v_mov_b32 v178, v34
v_mov_b32 v179, v34
v_mov_b32 v180, v34
v_mov_b32 v181, v34
v_mov_b32 v182, v34
v_mov_b32 v183, v34
v_mov_b32 v184, v34
v_mov_b32 v185, v34
v_mov_b32 v186, v34
v_mov_b32 v187, v34
v_mov_b32 v188, v34
v_mov_b32 v189, v34
v_mov_b32 v190, v34
v_mov_b32 v191, v34
s_branch .Lj0b_ret
.Lj0b1:
v_cmp_gt_i32 s[32:33], 0, v8
v_nop
v_cmp_gt_i32 s[34:35], 1, v8
v_cndmask_b32 v176, v176, v34, s[32:33]
v_cmp_gt_i32 s[32:33], 2, v8
v_cndmask_b32 v177, v177, v34, s[34:35]
v_cmp_gt_i32 s[34:35], 3, v8
v_cndmask_b32 v178, v178, v34, s[32:33]
v_cmp_gt_i32 s[32:33], 16, v8
v_cndmask_b32 v179, v179, v34, s[34:35]
v_cmp_gt_i32 s[34:35], 17, v8
v_cndmask_b32 v180, v180, v34, s[32:33]
v_cmp_gt_i32 s[32:33], 18, v8
v_cndmask_b32 v181, v181, v34, s[34:35]
v_cmp_gt_i32 s[34:35], 19, v8
v_cndmask_b32 v182, v182, v34, s[32:33]
v_cmp_gt_i32 s[32:33], -16, v8
v_cndmask_b32 v183, v183, v34, s[34:35]
v_cmp_gt_i32 s[34:35], -15, v8
v_cndmask_b32 v184, v184, v34, s[32:33]
v_cmp_gt_i32 s[32:33], -14, v8
v_cndmask_b32 v185, v185, v34, s[34:35]
v_cmp_gt_i32 s[34:35], -13, v8
v_cndmask_b32 v186, v186, v34, s[32:33]
v_cmp_gt_i32 s[32:33], 0, v8
v_cndmask_b32 v187, v187, v34, s[34:35]
v_cmp_gt_i32 s[34:35], 1, v8
v_cndmask_b32 v188, v188, v34, s[32:33]
v_cmp_gt_i32 s[32:33], 2, v8
v_cndmask_b32 v189, v189, v34, s[34:35]
v_cmp_gt_i32 s[34:35], 3, v8
v_cndmask_b32 v190, v190, v34, s[32:33]
v_nop
v_cndmask_b32 v191, v191, v34, s[34:35]
s_branch .Lj0b_ret
.Lj0c:
s_cmp_eq_u32 s51, 2
s_cbranch_scc1 .Lj0c1
v_mov_b32 v160, v34
v_mov_b32 v161, v34
v_mov_b32 v162, v34
v_mov_b32 v163, v34
v_mov_b32 v164, v34
v_mov_b32 v165, v34
v_mov_b32 v166, v34
v_mov_b32 v167, v34
v_mov_b32 v168, v34
v_mov_b32 v169, v34
v_mov_b32 v170, v34
v_mov_b32 v171, v34
v_mov_b32 v172, v34
v_mov_b32 v173, v34
v_mov_b32 v174, v34
v_mov_b32 v175, v34
s_branch .LT1
.Lj0c1:
v_cmp_gt_i32 s[32:33], 0, v8
v_nop
v_cmp_gt_i32 s[34:35], 1, v8
v_cndmask_b32 v160, v160, v34, s[32:33]
v_cmp_gt_i32 s[32:33], 2, v8
v_cndmask_b32 v161, v161, v34, s[34:35]
v_cmp_gt_i32 s[34:35], 3, v8
v_cndmask_b32 v162, v162, v34, s[32:33]
v_cmp_gt_i32 s[32:33], 16, v8
v_cndmask_b32 v163, v163, v34, s[34:35]
v_cmp_gt_i32 s[34:35], 17, v8
v_cndmask_b32 v164, v164, v34, s[32:33]
v_cmp_gt_i32 s[32:33], 18, v8
v_cndmask_b32 v165, v165, v34, s[34:35]
v_cmp_gt_i32 s[34:35], 19, v8
v_cndmask_b32 v166, v166, v34, s[32:33]
v_cmp_gt_i32 s[32:33], -16, v8
v_cndmask_b32 v167, v167, v34, s[34:35]
v_cmp_gt_i32 s[34:35], -15, v8
v_cndmask_b32 v168, v168, v34, s[32:33]
v_cmp_gt_i32 s[32:33], -14, v8
v_cndmask_b32 v169, v169, v34, s[34:35]
v_cmp_gt_i32 s[34:35], -13, v8
v_cndmask_b32 v170, v170, v34, s[32:33]
v_cmp_gt_i32 s[32:33], 0, v8
v_cndmask_b32 v171, v171, v34, s[34:35]
v_cmp_gt_i32 s[34:35], 1, v8
v_cndmask_b32 v172, v172, v34, s[32:33]
v_cmp_gt_i32 s[32:33], 2, v8
v_cndmask_b32 v173, v173, v34, s[34:35]
v_cmp_gt_i32 s[34:35], 3, v8
v_cndmask_b32 v174, v174, v34, s[32:33]
v_nop
v_cndmask_b32 v175, v175, v34, s[34:35]
s_branch .LT1
.Lool_pad:
s_cbranch_execz .Ltch4
.Lev_d1:
s_cmp_eq_u32 s43, s42
s_cbranch_scc1 .LT0
s_swappc_b64 s[30:31], s[58:59]
.Lev_d1_r:
s_branch .LD0
.Lev_s1:
s_cmp_eq_u32 s43, s42
s_cbranch_scc1 .LE0
s_swappc_b64 s[30:31], s[58:59]
.Lev_s1_r:
s_branch .LS0
.Lspin22:
s_mov_b32 s93, 0
.Lspin22_l:
s_sleep 4
buffer_load_dword v9, v10, s[60:63], s90 offen sc1
s_waitcnt vmcnt(0)
v_and_b32 v9, 7, v9
v_cmp_gt_u32 vcc, 2, v9
s_cmp_lg_u64 vcc, 0
s_cbranch_scc0 .Lspin22_ret
s_add_u32 s93, s93, 1
s_cmp_lt_u32 s93, 0x100000
s_cbranch_scc1 .Lspin22_l
s_branch .Lspin22_ret
.Lspin21:
s_mov_b32 s93, 0
.Lspin21_l:
s_sleep 4
buffer_load_dword v9, v10, s[60:63], 0 offen sc1
s_waitcnt vmcnt(0)
v_cmp_gt_u32 vcc, 1, v9
s_cmp_lg_u64 vcc, 0
s_cbranch_scc0 .Lspin21_ret
s_add_u32 s93, s93, 1
s_cmp_lt_u32 s93, 0x100000
s_cbranch_scc1 .Lspin21_l
s_branch .Lspin21_ret
.Ljob0:
s_waitcnt vmcnt(8)
v_mov_b32 v0, 1
buffer_store_dword v0, v5, s[60:63], 0 offen sc1
v_and_b32 v10, 7, v1
v_and_b32 v9, 8, v1
v_lshl_or_b32 v10, v9, 1, v10
s_and_b32 s93, s87, 2
s_lshl_b32 s93, s93, 2
v_or_b32 v10, s93, v10
v_lshlrev_b32 v10, 8, v10
s_lshl_b32 s93, s86, 5
v_add_u32 v10, s93, v10
buffer_load_dword v9, v10, s[60:63], s90 offen sc1
s_mov_b32 s57, 3
s_add_u32 s93, s43, 1
s_max_u32 s43, s57, s93
s_min_u32 s43, s43, s42
s_sub_u32 s55, s43, s93
s_swappc_b64 s[58:59], s[30:31]
.Ljob1:
s_waitcnt vmcnt(4)
v_and_b32 v9, 7, v9
v_cmp_gt_u32 vcc, 2, v9
s_cmp_lg_u64 vcc, 0
s_cbranch_scc1 .Lspin22
.Lspin22_ret:
s_mov_b32 s57, 12
s_add_u32 s93, s43, 1
s_max_u32 s43, s57, s93
s_min_u32 s43, s43, s42
s_sub_u32 s55, s43, s93
s_swappc_b64 s[58:59], s[30:31]
.Ljob2:
v_and_b32 v10, 7, v1
v_and_b32 v9, 8, v1
v_lshl_or_b32 v10, v9, 1, v10
s_and_b32 s93, s87, 2
s_lshl_b32 s93, s93, 2
v_or_b32 v10, s93, v10
v_lshlrev_b32 v10, 9, v10
v_bfe_u32 v9, v1, 4, 1
v_lshl_or_b32 v10, v9, 2, v10
s_lshl_b32 s93, s86, 6
v_add_u32 v10, s93, v10
s_and_b32 s93, s87, 1
s_lshl_b32 s93, s93, 3
s_add_u32 s93, s93, 0x10000
v_add_u32 v10, s93, v10
buffer_load_dword v9, v10, s[60:63], 0 offen sc1
s_mov_b32 s57, 14
s_add_u32 s93, s43, 1
s_max_u32 s43, s57, s93
s_min_u32 s43, s43, s42
s_sub_u32 s55, s43, s93
s_swappc_b64 s[58:59], s[30:31]
.Ljob3:
s_waitcnt vmcnt(4)
v_cmp_gt_u32 vcc, 1, v9
s_cmp_lg_u64 vcc, 0
s_cbranch_scc1 .Lspin21
.Lspin21_ret:
s_cmp_eq_u32 s87, 8
s_cselect_b32 s0, 44, 0x7fffffff
s_mov_b32 s57, s0
s_add_u32 s93, s43, 1
s_max_u32 s43, s57, s93
s_min_u32 s43, s43, s42
s_sub_u32 s55, s43, s93
s_swappc_b64 s[58:59], s[30:31]
.Ljob4:
s_memrealtime s[100:101]
s_lshl_b32 s93, s87, 2
s_add_u32 s93, s93, s3
s_xor_b32 s93, s93, 32
s_lshl_b32 s93, s93, 20
s_lshl_b32 s0, s86, 2
s_add_u32 s93, s93, s0
s_add_u32 s93, s93, 0x14000
v_lshlrev_b32 v9, 7, v1
v_add_u32 v10, s93, v9
s_waitcnt lgkmcnt(0)
s_and_b32 s100, s100, -16
s_or_b32 s100, s100, s86
s_or_b32 s100, s100, 8
v_mov_b32 v0, s100
buffer_store_dword v0, v10, s[60:63], 0 offen sc1
s_mov_b32 s57, 2147483647
s_add_u32 s93, s43, 1
s_max_u32 s43, s57, s93
s_min_u32 s43, s43, s42
s_sub_u32 s55, s43, s93
s_swappc_b64 s[58:59], s[30:31]
.Lx:
s_sub_u32 s4, s4, s91
s_subb_u32 s5, s5, 0
s_mov_b32 s6, 0x80000
s_sub_u32 s8, s8, s91
s_subb_u32 s9, s9, 0
s_mov_b32 s10, 0x84000
s_mov_b64 s[38:39], s[52:53]
s_cmp_eq_u64 s[30:31], 0
s_cbranch_scc1 .Lx_s0
v_mov_b32 v224, v64
v_mov_b32 v225, v65
v_mov_b32 v226, v66
v_mov_b32 v227, v67
v_mov_b32 v228, v68
v_mov_b32 v229, v69
v_mov_b32 v230, v70
v_mov_b32 v231, v71
v_mov_b32 v232, v72
v_mov_b32 v233, v73
v_mov_b32 v234, v74
v_mov_b32 v235, v75
v_mov_b32 v236, v76
v_mov_b32 v237, v77
v_mov_b32 v238, v78
v_mov_b32 v239, v79
v_mov_b32 v240, v80
v_mov_b32 v241, v81
v_mov_b32 v242, v82
v_mov_b32 v243, v83
v_mov_b32 v244, v84
v_mov_b32 v245, v85
v_mov_b32 v246, v86
v_mov_b32 v247, v87
v_mov_b32 v248, v88
v_mov_b32 v249, v89
v_mov_b32 v250, v90
v_mov_b32 v251, v91
v_mov_b32 v252, v92
v_mov_b32 v253, v93
v_mov_b32 v254, v94
v_mov_b32 v255, v95
s_sub_u32 s50, 15, s89
s_lshl_b32 s50, s50, 2
s_add_u32 s50, s50, s3
s_getpc_b64 s[42:43]
.Lx_p0:
s_add_u32 s42, s42, .Lx_r0-.Lx_p0
s_addc_u32 s43, s43, 0
s_branch .Lx_core
.Lx_r0:
v_accvgpr_write_b32 a64, v128
v_accvgpr_write_b32 a65, v129
v_accvgpr_write_b32 a66, v130
v_accvgpr_write_b32 a67, v131
v_accvgpr_write_b32 a68, v132
v_accvgpr_write_b32 a69, v133
v_accvgpr_write_b32 a70, v134
v_accvgpr_write_b32 a71, v135
v_accvgpr_write_b32 a72, v136
v_accvgpr_write_b32 a73, v137
v_accvgpr_write_b32 a74, v138
v_accvgpr_write_b32 a75, v139
v_accvgpr_write_b32 a76, v140
v_accvgpr_write_b32 a77, v141
v_accvgpr_write_b32 a78, v142
v_accvgpr_write_b32 a79, v143
v_accvgpr_write_b32 a80, v144
v_accvgpr_write_b32 a81, v145
v_accvgpr_write_b32 a82, v146
v_accvgpr_write_b32 a83, v147
v_accvgpr_write_b32 a84, v148
v_accvgpr_write_b32 a85, v149
v_accvgpr_write_b32 a86, v150
v_accvgpr_write_b32 a87, v151
v_accvgpr_write_b32 a88, v152
v_accvgpr_write_b32 a89, v153
v_accvgpr_write_b32 a90, v154
v_accvgpr_write_b32 a91, v155
v_accvgpr_write_b32 a92, v156
v_accvgpr_write_b32 a93, v157
v_accvgpr_write_b32 a94, v158
v_accvgpr_write_b32 a95, v159
v_accvgpr_write_b32 a112, v160
v_accvgpr_write_b32 a113, v161
v_accvgpr_write_b32 a114, v162
v_accvgpr_write_b32 a115, v163
v_accvgpr_write_b32 a116, v164
v_accvgpr_write_b32 a117, v165
v_accvgpr_write_b32 a118, v166
v_accvgpr_write_b32 a119, v167
v_accvgpr_write_b32 a120, v168
v_accvgpr_write_b32 a121, v169
v_accvgpr_write_b32 a122, v170
v_accvgpr_write_b32 a123, v171
v_accvgpr_write_b32 a124, v172
v_accvgpr_write_b32 a125, v173
v_accvgpr_write_b32 a126, v174
v_accvgpr_write_b32 a127, v175
v_accvgpr_write_b32 a144, v176
v_accvgpr_write_b32 a145, v177
v_accvgpr_write_b32 a146, v178
v_accvgpr_write_b32 a147, v179
v_accvgpr_write_b32 a148, v180
v_accvgpr_write_b32 a149, v181
v_accvgpr_write_b32 a150, v182
v_accvgpr_write_b32 a151, v183
v_accvgpr_write_b32 a152, v184
v_accvgpr_write_b32 a153, v185
v_accvgpr_write_b32 a154, v186
v_accvgpr_write_b32 a155, v187
v_accvgpr_write_b32 a156, v188
v_accvgpr_write_b32 a157, v189
v_accvgpr_write_b32 a158, v190
v_accvgpr_write_b32 a159, v191
v_mov_b32 v208, v56
v_mov_b32 v212, v57
.Lx_s0:
s_cmp_eq_u64 s[38:39], 0
s_cbranch_scc1 .Lx_s1
v_mov_b32 v224, v96
v_mov_b32 v225, v97
v_mov_b32 v226, v98
v_mov_b32 v227, v99
v_mov_b32 v228, v100
v_mov_b32 v229, v101
v_mov_b32 v230, v102
v_mov_b32 v231, v103
v_mov_b32 v232, v104
v_mov_b32 v233, v105
v_mov_b32 v234, v106
v_mov_b32 v235, v107
v_mov_b32 v236, v108
v_mov_b32 v237, v109
v_mov_b32 v238, v110
v_mov_b32 v239, v111
v_mov_b32 v240, v112
v_mov_b32 v241, v113
v_mov_b32 v242, v114
v_mov_b32 v243, v115
v_mov_b32 v244, v116
v_mov_b32 v245, v117
v_mov_b32 v246, v118
v_mov_b32 v247, v119
v_mov_b32 v248, v120
v_mov_b32 v249, v121
v_mov_b32 v250, v122
v_mov_b32 v251, v123
v_mov_b32 v252, v124
v_mov_b32 v253, v125
v_mov_b32 v254, v126
v_mov_b32 v255, v127
s_mov_b32 s50, s89
s_lshl_b32 s50, s50, 2
s_add_u32 s50, s50, s94
s_getpc_b64 s[42:43]
.Lx_p1:
s_add_u32 s42, s42, .Lx_r1-.Lx_p1
s_addc_u32 s43, s43, 0
s_branch .Lx_core
.Lx_r1:
v_accvgpr_write_b32 a176, v128
v_accvgpr_write_b32 a177, v129
v_accvgpr_write_b32 a178, v130
v_accvgpr_write_b32 a179, v131
v_accvgpr_write_b32 a180, v132
v_accvgpr_write_b32 a181, v133
v_accvgpr_write_b32 a182, v134
v_accvgpr_write_b32 a183, v135
v_accvgpr_write_b32 a184, v136
v_accvgpr_write_b32 a185, v137
v_accvgpr_write_b32 a186, v138
v_accvgpr_write_b32 a187, v139
v_accvgpr_write_b32 a188, v140
v_accvgpr_write_b32 a189, v141
v_accvgpr_write_b32 a190, v142
v_accvgpr_write_b32 a191, v143
v_accvgpr_write_b32 a208, v144
v_accvgpr_write_b32 a209, v145
v_accvgpr_write_b32 a210, v146
v_accvgpr_write_b32 a211, v147
v_accvgpr_write_b32 a212, v148
v_accvgpr_write_b32 a213, v149
v_accvgpr_write_b32 a214, v150
v_accvgpr_write_b32 a215, v151
v_accvgpr_write_b32 a216, v152
v_accvgpr_write_b32 a217, v153
v_accvgpr_write_b32 a218, v154
v_accvgpr_write_b32 a219, v155
v_accvgpr_write_b32 a220, v156
v_accvgpr_write_b32 a221, v157
v_accvgpr_write_b32 a222, v158
v_accvgpr_write_b32 a223, v159
v_accvgpr_write_b32 a240, v160
v_accvgpr_write_b32 a241, v161
v_accvgpr_write_b32 a242, v162
v_accvgpr_write_b32 a243, v163
v_accvgpr_write_b32 a244, v164
v_accvgpr_write_b32 a245, v165
v_accvgpr_write_b32 a246, v166
v_accvgpr_write_b32 a247, v167
v_accvgpr_write_b32 a248, v168
v_accvgpr_write_b32 a249, v169
v_accvgpr_write_b32 a250, v170
v_accvgpr_write_b32 a251, v171
v_accvgpr_write_b32 a252, v172
v_accvgpr_write_b32 a253, v173
v_accvgpr_write_b32 a254, v174
v_accvgpr_write_b32 a255, v175
v_accvgpr_write_b32 a96, v176
v_accvgpr_write_b32 a97, v177
v_accvgpr_write_b32 a98, v178
v_accvgpr_write_b32 a99, v179
v_accvgpr_write_b32 a100, v180
v_accvgpr_write_b32 a101, v181
v_accvgpr_write_b32 a102, v182
v_accvgpr_write_b32 a103, v183
v_accvgpr_write_b32 a104, v184
v_accvgpr_write_b32 a105, v185
v_accvgpr_write_b32 a106, v186
v_accvgpr_write_b32 a107, v187
v_accvgpr_write_b32 a108, v188
v_accvgpr_write_b32 a109, v189
v_accvgpr_write_b32 a110, v190
v_accvgpr_write_b32 a111, v191
v_mov_b32 v216, v56
v_mov_b32 v220, v57
.Lx_s1:
s_branch .Lx_ret
.Lx_core:
s_lshl_b32 s51, s50, 5
v_add_u32 v58, s51, v8
s_mov_b32 s44, 0
s_mov_b32 s45, 16384
s_mov_b32 s46, 0
v_mov_b32 v24, v34
v_mov_b32 v25, v34
v_mov_b32 v56, 0
v_mov_b32 v57, 0
v_mov_b32 v128, 0
v_mov_b32 v129, 0
v_mov_b32 v130, 0
v_mov_b32 v131, 0
v_mov_b32 v132, 0
v_mov_b32 v133, 0
v_mov_b32 v134, 0
v_mov_b32 v135, 0
v_mov_b32 v136, 0
v_mov_b32 v137, 0
v_mov_b32 v138, 0
v_mov_b32 v139, 0
v_mov_b32 v140, 0
v_mov_b32 v141, 0
v_mov_b32 v142, 0
v_mov_b32 v143, 0
v_mov_b32 v144, 0
v_mov_b32 v145, 0
v_mov_b32 v146, 0
v_mov_b32 v147, 0
v_mov_b32 v148, 0
v_mov_b32 v149, 0
v_mov_b32 v150, 0
v_mov_b32 v151, 0
v_mov_b32 v152, 0
v_mov_b32 v153, 0
v_mov_b32 v154, 0
v_mov_b32 v155, 0
v_mov_b32 v156, 0
v_mov_b32 v157, 0
v_mov_b32 v158, 0
v_mov_b32 v159, 0
v_mov_b32 v160, 0
v_mov_b32 v161, 0
v_mov_b32 v162, 0
v_mov_b32 v163, 0
v_mov_b32 v164, 0
v_mov_b32 v165, 0
v_mov_b32 v166, 0
v_mov_b32 v167, 0
v_mov_b32 v168, 0
v_mov_b32 v169, 0
v_mov_b32 v170, 0
v_mov_b32 v171, 0
v_mov_b32 v172, 0
v_mov_b32 v173, 0
v_mov_b32 v174, 0
v_mov_b32 v175, 0
v_mov_b32 v176, 0
v_mov_b32 v177, 0
v_mov_b32 v178, 0
v_mov_b32 v179, 0
v_mov_b32 v180, 0
v_mov_b32 v181, 0
v_mov_b32 v182, 0
v_mov_b32 v183, 0
v_mov_b32 v184, 0
v_mov_b32 v185, 0
v_mov_b32 v186, 0
v_mov_b32 v187, 0
v_mov_b32 v188, 0
v_mov_b32 v189, 0
v_mov_b32 v190, 0
v_mov_b32 v191, 0
.Lx_loop:
buffer_load_dwordx4 a[0:3], v7, s[4:7], s44 offen offset:0 sc1
buffer_load_dwordx4 a[4:7], v51, s[4:7], s44 offen offset:0 sc1
buffer_load_dwordx4 a[8:11], v7, s[4:7], s44 offen offset:1024 sc1
buffer_load_dwordx4 a[12:15], v51, s[4:7], s44 offen offset:1024 sc1
buffer_load_dwordx4 a[16:19], v7, s[4:7], s44 offen offset:2048 sc1
buffer_load_dwordx4 a[20:23], v51, s[4:7], s44 offen offset:2048 sc1
buffer_load_dwordx4 a[24:27], v7, s[4:7], s44 offen offset:3072 sc1
buffer_load_dwordx4 a[28:31], v51, s[4:7], s44 offen offset:3072 sc1
buffer_load_dwordx4 a[32:35], v7, s[8:11], s45 offen offset:0 sc1
buffer_load_dwordx4 a[36:39], v7, s[8:11], s45 offen offset:1024 sc1
buffer_load_dwordx4 a[40:43], v7, s[8:11], s45 offen offset:2048 sc1
buffer_load_dwordx4 a[44:47], v7, s[8:11], s45 offen offset:3072 sc1
buffer_load_dwordx4 a[48:51], v51, s[8:11], s45 offen offset:0 sc1
buffer_load_dwordx4 a[52:55], v51, s[8:11], s45 offen offset:1024 sc1
buffer_load_dwordx4 a[56:59], v51, s[8:11], s45 offen offset:2048 sc1
buffer_load_dwordx4 a[60:63], v51, s[8:11], s45 offen offset:3072 sc1
s_waitcnt vmcnt(8)
v_mfma_f32_16x16x32_bf16 v[192:195], a[0:3], v[224:227], 0
v_mfma_f32_16x16x32_bf16 v[200:203], a[0:3], v[240:243], 0
v_mfma_f32_16x16x32_bf16 v[196:199], a[4:7], v[224:227], 0
v_mfma_f32_16x16x32_bf16 v[204:207], a[4:7], v[240:243], 0
v_mfma_f32_16x16x32_bf16 v[192:195], a[8:11], v[228:231], v[192:195]
v_mfma_f32_16x16x32_bf16 v[200:203], a[8:11], v[244:247], v[200:203]
v_mfma_f32_16x16x32_bf16 v[196:199], a[12:15], v[228:231], v[196:199]
v_mfma_f32_16x16x32_bf16 v[204:207], a[12:15], v[244:247], v[204:207]
v_mfma_f32_16x16x32_bf16 v[192:195], a[16:19], v[232:235], v[192:195]
v_mfma_f32_16x16x32_bf16 v[200:203], a[16:19], v[248:251], v[200:203]
v_mfma_f32_16x16x32_bf16 v[196:199], a[20:23], v[232:235], v[196:199]
v_mfma_f32_16x16x32_bf16 v[204:207], a[20:23], v[248:251], v[204:207]
v_mfma_f32_16x16x32_bf16 v[192:195], a[24:27], v[236:239], v[192:195]
v_mfma_f32_16x16x32_bf16 v[200:203], a[24:27], v[252:255], v[200:203]
v_mfma_f32_16x16x32_bf16 v[196:199], a[28:31], v[236:239], v[196:199]
v_mfma_f32_16x16x32_bf16 v[204:207], a[28:31], v[252:255], v[204:207]
v_cmp_gt_i32 s[32:33], 0, v58
v_nop
v_cmp_gt_i32 s[34:35], 1, v58
s_nop 1
v_cndmask_b32 v192, v192, v34, s[32:33]
v_cmp_gt_i32 s[32:33], 2, v58
v_cndmask_b32 v193, v193, v34, s[34:35]
v_cmp_gt_i32 s[34:35], 3, v58
v_cndmask_b32 v194, v194, v34, s[32:33]
v_cmp_gt_i32 s[32:33], 16, v58
v_cndmask_b32 v195, v195, v34, s[34:35]
v_cmp_gt_i32 s[34:35], 17, v58
v_cndmask_b32 v196, v196, v34, s[32:33]
v_cmp_gt_i32 s[32:33], 18, v58
v_cndmask_b32 v197, v197, v34, s[34:35]
v_cmp_gt_i32 s[34:35], 19, v58
v_cndmask_b32 v198, v198, v34, s[32:33]
v_cmp_gt_i32 s[32:33], -16, v58
v_cndmask_b32 v199, v199, v34, s[34:35]
v_cmp_gt_i32 s[34:35], -15, v58
v_cndmask_b32 v200, v200, v34, s[32:33]
v_cmp_gt_i32 s[32:33], -14, v58
v_cndmask_b32 v201, v201, v34, s[34:35]
v_cmp_gt_i32 s[34:35], -13, v58
v_cndmask_b32 v202, v202, v34, s[32:33]
v_cmp_gt_i32 s[32:33], 0, v58
v_cndmask_b32 v203, v203, v34, s[34:35]
v_cmp_gt_i32 s[34:35], 1, v58
v_cndmask_b32 v204, v204, v34, s[32:33]
v_cmp_gt_i32 s[32:33], 2, v58
v_cndmask_b32 v205, v205, v34, s[34:35]
v_cmp_gt_i32 s[34:35], 3, v58
v_cndmask_b32 v206, v206, v34, s[32:33]
v_nop
v_cndmask_b32 v207, v207, v34, s[34:35]
v_max3_f32 v26, v192, v193, v194
v_max3_f32 v27, v200, v201, v202
v_max3_f32 v28, v195, v196, v197
v_max3_f32 v29, v203, v204, v205
v_max3_f32 v26, v26, v198, v199
v_max3_f32 v27, v27, v206, v207
v_max3_f32 v26, v26, v28, v24
v_max3_f32 v27, v27, v29, v25
v_mov_b32 v28, v26
v_mov_b32 v29, v27
v_nop
v_permlane16_swap_b32 v26, v28
v_permlane16_swap_b32 v27, v29
v_max_f32 v26, v26, v28
v_max_f32 v27, v27, v29
v_mov_b32 v28, v26
v_mov_b32 v29, v27
v_nop
v_permlane32_swap_b32 v26, v28
v_permlane32_swap_b32 v27, v29
v_max_f32 v26, v26, v28
v_max_f32 v27, v27, v29
v_sub_f32 v30, v24, v26
v_exp_f32 v30, v30
v_mov_b32 v24, v26
v_sub_f32 v31, v25, v27
v_exp_f32 v31, v31
v_mov_b32 v25, v27
v_sub_f32 v192, v192, v26
v_sub_f32 v193, v193, v26
v_sub_f32 v194, v194, v26
v_sub_f32 v195, v195, v26
v_sub_f32 v196, v196, v26
v_sub_f32 v197, v197, v26
v_sub_f32 v198, v198, v26
v_sub_f32 v199, v199, v26
v_sub_f32 v200, v200, v27
v_sub_f32 v201, v201, v27
v_sub_f32 v202, v202, v27
v_sub_f32 v203, v203, v27
v_sub_f32 v204, v204, v27
v_sub_f32 v205, v205, v27
v_sub_f32 v206, v206, v27
v_sub_f32 v207, v207, v27
v_exp_f32 v192, v192
v_exp_f32 v193, v193
v_exp_f32 v194, v194
v_exp_f32 v195, v195
v_exp_f32 v196, v196
v_exp_f32 v197, v197
v_exp_f32 v198, v198
v_exp_f32 v199, v199
v_exp_f32 v200, v200
v_exp_f32 v201, v201
v_exp_f32 v202, v202
v_exp_f32 v203, v203
v_exp_f32 v204, v204
v_exp_f32 v205, v205
v_exp_f32 v206, v206
v_exp_f32 v207, v207
v_mul_f32 v56, v56, v30
v_mul_f32 v57, v57, v31
v_add_f32 v56, v56, v192
v_add_f32 v56, v56, v193
v_add_f32 v56, v56, v194
v_add_f32 v56, v56, v195
v_add_f32 v56, v56, v196
v_add_f32 v56, v56, v197
v_add_f32 v56, v56, v198
v_add_f32 v56, v56, v199
v_add_f32 v57, v57, v200
v_add_f32 v57, v57, v201
v_add_f32 v57, v57, v202
v_add_f32 v57, v57, v203
v_add_f32 v57, v57, v204
v_add_f32 v57, v57, v205
v_add_f32 v57, v57, v206
v_add_f32 v57, v57, v207
v_cvt_pk_bf16_f32 v16, v192, v193
.Lpad25:
s_nop 0
v_cvt_pk_bf16_f32 v17, v194, v195
v_cvt_pk_bf16_f32 v18, v196, v197
v_cvt_pk_bf16_f32 v19, v198, v199
v_cvt_pk_bf16_f32 v20, v200, v201
v_cvt_pk_bf16_f32 v21, v202, v203
v_cvt_pk_bf16_f32 v22, v204, v205
v_cvt_pk_bf16_f32 v23, v206, v207
v_mul_f32 v128, v30, v128
v_mul_f32 v129, v30, v129
v_mul_f32 v130, v30, v130
v_mul_f32 v131, v30, v131
v_mul_f32 v132, v30, v132
v_mul_f32 v133, v30, v133
v_mul_f32 v134, v30, v134
v_mul_f32 v135, v30, v135
v_mul_f32 v136, v31, v136
v_mul_f32 v137, v31, v137
v_mul_f32 v138, v31, v138
v_mul_f32 v139, v31, v139
v_mul_f32 v140, v31, v140
v_mul_f32 v141, v31, v141
v_mul_f32 v142, v31, v142
v_mul_f32 v143, v31, v143
v_mul_f32 v144, v30, v144
v_mul_f32 v145, v30, v145
v_mul_f32 v146, v30, v146
v_mul_f32 v147, v30, v147
v_mul_f32 v148, v30, v148
v_mul_f32 v149, v30, v149
v_mul_f32 v150, v30, v150
v_mul_f32 v151, v30, v151
v_mul_f32 v152, v31, v152
v_mul_f32 v153, v31, v153
v_mul_f32 v154, v31, v154
v_mul_f32 v155, v31, v155
v_mul_f32 v156, v31, v156
v_mul_f32 v157, v31, v157
v_mul_f32 v158, v31, v158
v_mul_f32 v159, v31, v159
v_mul_f32 v160, v30, v160
v_mul_f32 v161, v30, v161
v_mul_f32 v162, v30, v162
v_mul_f32 v163, v30, v163
v_mul_f32 v164, v30, v164
v_mul_f32 v165, v30, v165
v_mul_f32 v166, v30, v166
v_mul_f32 v167, v30, v167
v_mul_f32 v168, v31, v168
v_mul_f32 v169, v31, v169
v_mul_f32 v170, v31, v170
v_mul_f32 v171, v31, v171
v_mul_f32 v172, v31, v172
v_mul_f32 v173, v31, v173
v_mul_f32 v174, v31, v174
v_mul_f32 v175, v31, v175
v_mul_f32 v176, v30, v176
v_mul_f32 v177, v30, v177
v_mul_f32 v178, v30, v178
v_mul_f32 v179, v30, v179
v_mul_f32 v180, v30, v180
v_mul_f32 v181, v30, v181
v_mul_f32 v182, v30, v182
v_mul_f32 v183, v30, v183
v_mul_f32 v184, v31, v184
v_mul_f32 v185, v31, v185
v_mul_f32 v186, v31, v186
v_mul_f32 v187, v31, v187
v_mul_f32 v188, v31, v188
v_mul_f32 v189, v31, v189
v_mul_f32 v190, v31, v190
v_mul_f32 v191, v31, v191
s_waitcnt vmcnt(0)
v_mfma_f32_16x16x32_bf16 v[128:131], a[32:35], v[16:19], v[128:131]
v_mfma_f32_16x16x32_bf16 v[136:139], a[32:35], v[20:23], v[136:139]
v_mfma_f32_16x16x32_bf16 v[132:135], a[36:39], v[16:19], v[132:135]
v_mfma_f32_16x16x32_bf16 v[140:143], a[36:39], v[20:23], v[140:143]
v_mfma_f32_16x16x32_bf16 v[144:147], a[40:43], v[16:19], v[144:147]
v_mfma_f32_16x16x32_bf16 v[152:155], a[40:43], v[20:23], v[152:155]
v_mfma_f32_16x16x32_bf16 v[148:151], a[44:47], v[16:19], v[148:151]
v_mfma_f32_16x16x32_bf16 v[156:159], a[44:47], v[20:23], v[156:159]
v_mfma_f32_16x16x32_bf16 v[160:163], a[48:51], v[16:19], v[160:163]
v_mfma_f32_16x16x32_bf16 v[168:171], a[48:51], v[20:23], v[168:171]
v_mfma_f32_16x16x32_bf16 v[164:167], a[52:55], v[16:19], v[164:167]
v_mfma_f32_16x16x32_bf16 v[172:175], a[52:55], v[20:23], v[172:175]
v_mfma_f32_16x16x32_bf16 v[176:179], a[56:59], v[16:19], v[176:179]
v_mfma_f32_16x16x32_bf16 v[184:187], a[56:59], v[20:23], v[184:187]
v_mfma_f32_16x16x32_bf16 v[180:183], a[60:63], v[16:19], v[180:183]
v_mfma_f32_16x16x32_bf16 v[188:191], a[60:63], v[20:23], v[188:191]
v_subrev_u32 v58, 32, v58
s_add_u32 s44, s44, 8192
s_add_u32 s45, s45, 8192
s_add_u32 s46, s46, 1
s_cmp_le_u32 s46, s50
s_cbranch_scc1 .Lx_loop
v_mov_b32 v28, v56
v_mov_b32 v29, v57
s_nop 0
v_permlane16_swap_b32 v56, v28
v_permlane16_swap_b32 v57, v29
v_add_f32 v56, v56, v28
v_add_f32 v57, v57, v29
v_mov_b32 v28, v56
v_mov_b32 v29, v57
s_nop 0
v_permlane32_swap_b32 v56, v28
v_permlane32_swap_b32 v57, v29
v_add_f32 v56, v56, v28
v_add_f32 v57, v57, v29
s_setpc_b64 s[42:43]
.Lspin6:
s_mov_b32 s93, 0
.Lspin6_l:
s_sleep 4
buffer_load_dword v250, v249, s[60:63], 0 offen sc1
s_waitcnt vmcnt(0)
v_cmp_eq_u32 vcc, 0, v250
s_cmp_lg_u64 vcc, 0
s_cbranch_scc0 .Lspin6_ret
s_add_u32 s93, s93, 1
s_cmp_lt_u32 s93, 0x100000
s_cbranch_scc1 .Lspin6_l
s_branch .Lspin6_ret
.Lspin3:
s_mov_b32 s93, 0
.Lspin3_l:
s_sleep 4
buffer_load_dword v250, v249, s[60:63], s90 offen sc1
s_waitcnt vmcnt(0)
v_and_b32 v250, 7, v250
v_cmp_gt_u32 vcc, 3, v250
s_cmp_lg_u64 vcc, 0
s_cbranch_scc0 .Lspin3_ret
s_add_u32 s93, s93, 1
s_cmp_lt_u32 s93, 0x100000
s_cbranch_scc1 .Lspin3_l
s_branch .Lspin3_ret
.Lspin5:
s_mov_b32 s93, 0
.Lspin5_l:
s_sleep 4
buffer_load_dword v250, v249, s[60:63], s90 offen sc1
s_waitcnt vmcnt(0)
v_and_b32 v250, 7, v250
v_cmp_gt_u32 vcc, 5, v250
s_cmp_lg_u64 vcc, 0
s_cbranch_scc0 .Lspin5_ret
s_add_u32 s93, s93, 1
s_cmp_lt_u32 s93, 0x100000
s_cbranch_scc1 .Lspin5_l
s_branch .Lspin5_ret
.Lspin4:
s_mov_b32 s93, 0
.Lspin4_l:
s_sleep 4
buffer_load_dword v250, v249, s[60:63], s90 offen sc1
s_waitcnt vmcnt(0)
v_and_b32 v250, 7, v250
v_cmp_gt_u32 vcc, 4, v250
s_cmp_lg_u64 vcc, 0
s_cbranch_scc0 .Lspin4_ret
s_add_u32 s93, s93, 1
s_cmp_lt_u32 s93, 0x100000
s_cbranch_scc1 .Lspin4_l
s_branch .Lspin4_ret
.Lpret:
.Lcode1:
)ASM"
    :: "{s[0:1]}"(__builtin_amdgcn_kernarg_segment_ptr()), "{s2}"(blockIdx.x), "{v0}"(threadIdx.x),
       "{s3}"((unsigned)(size_t)(__attribute__((address_space(3))) char*)smem)
    : "v0", "v1", "v2", "v3", "v4", "v5", "v6", "v7", "v8", "v9", "v10", "v11", "v12", "v13", "v14", "v15", "v16", "v17", "v18", "v19", "v20", "v21", "v22", "v23", "v24", "v25", "v26", "v27", "v28", "v29", "v30", "v31", "v32", "v33", "v34", "v35", "v36", "v37", "v38", "v39", "v40", "v41", "v42", "v43", "v44", "v45", "v46", "v47", "v48", "v49", "v50", "v51", "v52", "v53", "v54", "v55", "v56", "v57", "v58", "v59", "v60", "v61", "v62", "v63", "v64", "v65", "v66", "v67", "v68", "v69", "v70", "v71", "v72", "v73", "v74", "v75", "v76", "v77", "v78", "v79", "v80", "v81", "v82", "v83", "v84", "v85", "v86", "v87", "v88", "v89", "v90", "v91", "v92", "v93", "v94", "v95", "v96", "v97", "v98", "v99", "v100", "v101", "v102", "v103", "v104", "v105", "v106", "v107", "v108", "v109", "v110", "v111", "v112", "v113", "v114", "v115", "v116", "v117", "v118", "v119", "v120", "v121", "v122", "v123", "v124", "v125", "v126", "v127", "v128", "v129", "v130", "v131", "v132", "v133", "v134", "v135", "v136", "v137", "v138", "v139", "v140", "v141", "v142", "v143", "v144", "v145", "v146", "v147", "v148", "v149", "v150", "v151", "v152", "v153", "v154", "v155", "v156", "v157", "v158", "v159", "v160", "v161", "v162", "v163", "v164", "v165", "v166", "v167", "v168", "v169", "v170", "v171", "v172", "v173", "v174", "v175", "v176", "v177", "v178", "v179", "v180", "v181", "v182", "v183", "v184", "v185", "v186", "v187", "v188", "v189", "v190", "v191", "v192", "v193", "v194", "v195", "v196", "v197", "v198", "v199", "v200", "v201", "v202", "v203", "v204", "v205", "v206", "v207", "v208", "v209", "v210", "v211", "v212", "v213", "v214", "v215", "v216", "v217", "v218", "v219", "v220", "v221", "v222", "v223", "v224", "v225", "v226", "v227", "v228", "v229", "v230", "v231", "v232", "v233", "v234", "v235", "v236", "v237", "v238", "v239", "v240", "v241", "v242", "v243", "v244", "v245", "v246", "v247", "v248", "v249", "v250", "v251", "v252", "v253", "v254", "v255", "a0", "a1", "a2", "a3", "a4", "a5", "a6", "a7", "a8", "a9", "a10", "a11", "a12", "a13", "a14", "a15", "a16", "a17", "a18", "a19", "a20", "a21", "a22", "a23", "a24", "a25", "a26", "a27", "a28", "a29", "a30", "a31", "a32", "a33", "a34", "a35", "a36", "a37", "a38", "a39", "a40", "a41", "a42", "a43", "a44", "a45", "a46", "a47", "a48", "a49", "a50", "a51", "a52", "a53", "a54", "a55", "a56", "a57", "a58", "a59", "a60", "a61", "a62", "a63", "a64", "a65", "a66", "a67", "a68", "a69", "a70", "a71", "a72", "a73", "a74", "a75", "a76", "a77", "a78", "a79", "a80", "a81", "a82", "a83", "a84", "a85", "a86", "a87", "a88", "a89", "a90", "a91", "a92", "a93", "a94", "a95", "a96", "a97", "a98", "a99", "a100", "a101", "a102", "a103", "a104", "a105", "a106", "a107", "a108", "a109", "a110", "a111", "a112", "a113", "a114", "a115", "a116", "a117", "a118", "a119", "a120", "a121", "a122", "a123", "a124", "a125", "a126", "a127", "a128", "a129", "a130", "a131", "a132", "a133", "a134", "a135", "a136", "a137", "a138", "a139", "a140", "a141", "a142", "a143", "a144", "a145", "a146", "a147", "a148", "a149", "a150", "a151", "a152", "a153", "a154", "a155", "a156", "a157", "a158", "a159", "a160", "a161", "a162", "a163", "a164", "a165", "a166", "a167", "a168", "a169", "a170", "a171", "a172", "a173", "a174", "a175", "a176", "a177", "a178", "a179", "a180", "a181", "a182", "a183", "a184", "a185", "a186", "a187", "a188", "a189", "a190", "a191", "a192", "a193", "a194", "a195", "a196", "a197", "a198", "a199", "a200", "a201", "a202", "a203", "a204", "a205", "a206", "a207", "a208", "a209", "a210", "a211", "a212", "a213", "a214", "a215", "a216", "a217", "a218", "a219", "a220", "a221", "a222", "a223", "a224", "a225", "a226", "a227", "a228", "a229", "a230", "a231", "a232", "a233", "a234", "a235", "a236", "a237", "a238", "a239", "a240", "a241", "a242", "a243", "a244", "a245", "a246", "a247", "a248", "a249", "a250", "a251", "a252", "a253", "a254", "a255", "s3", "s4", "s5", "s6", "s7", "s8", "s9", "s10", "s11", "s12", "s13", "s14", "s15", "s16", "s17", "s18", "s19", "s20", "s21", "s22", "s23", "s24", "s25", "s26", "s27", "s28", "s29", "s30", "s31", "s32", "s33", "s34", "s35", "s36", "s37", "s38", "s39", "s40", "s41", "s42", "s43", "s44", "s45", "s46", "s47", "s48", "s49", "s50", "s51", "s52", "s53", "s54", "s55", "s56", "s57", "s58", "s59", "s60", "s61", "s62", "s63", "s64", "s65", "s66", "s67", "s68", "s69", "s70", "s71", "s72", "s73", "s74", "s75", "s76", "s77", "s78", "s79", "s80", "s81", "s82", "s83", "s84", "s85", "s86", "s87", "s88", "s89", "s90", "s91", "s92", "s93", "s94", "s95", "s96", "s97", "s98", "s99", "s100", "s101", "vcc", "m0", "scc", "memory");
}
extern "C" void kernel_launch(void* const* d_in, const int* in_sizes, int n_in, void* d_out, int out_size,
                              void* d_ws, size_t ws_size, hipStream_t stream) {
  Args a;
  for (int i = 0; i < 9; ++i) a.p[i] = d_in[i];
  a.out = d_out; a.ws = (char*)d_ws + (0 ? ((ws_size - (72u << 20)) & ~(size_t)0xfffff) : 0);
  mega_kernel<<<256, 256, 0, stream>>>(a);
}
```

```cpp
#include <hip/hip_runtime.h>
#include <stdint.h>
struct Args { const void* p[9]; void* out; void* ws; };
__global__ __launch_bounds__(256, 1) __attribute__((aligned(4096))) void mega_kernel(Args args) {
  __shared__ __attribute__((aligned(16))) char smem[163840];
  asm volatile(R"ASM(
s_getpc_b64 s[96:97]
.Lcode0:
s_load_dwordx16 s[64:79], s[0:1], 0
s_load_dwordx4 s[80:83], s[0:1], 64
s_load_dwordx2 s[84:85], s[0:1], 80
v_and_b32 v1, 63, v0
v_lshrrev_b32 v16, 6, v0
v_lshlrev_b32 v7, 4, v1
v_readfirstlane_b32 s3, v16
s_and_b32 s86, s2, 7
s_lshr_b32 s87, s2, 3
s_and_b32 s4, s87, 3
s_lshl_b32 s88, s86, 2
s_add_u32 s88, s88, s4
s_lshr_b32 s89, s2, 5
s_lshl_b32 s91, s3, 12
s_lshl_b32 s92, s3, 13
s_and_b32 s97, s97, 0xffff
s_mov_b32 s98, .Lcode1-.Lcode0
s_mov_b32 s99, 0x20000
s_lshl_b32 s33, s87, 2
s_add_u32 s33, s33, s3
s_lshl_b32 s33, s33, 10
v_and_b32 v4, 7, v1
v_lshlrev_b32 v4, 7, v4
v_add_u32 v4, s33, v4
buffer_load_dword v35, v4, s[96:99], 0 offen
s_and_b32 s27, s87, 7
s_bfe_u32 s28, s87, 0x10003
s_lshl_b32 s29, s86, 1
s_add_u32 s28, s28, s29
v_lshrrev_b32 v40, 3, v1
v_and_b32 v41, 7, v1
v_xor_b32 v41, v41, v40
v_lshlrev_b32 v41, 4, v41
v_lshl_add_u32 v202, v40, 10, v41
v_add_u32 v203, 7168, v202
v_add_u32 v204, 14336, v202
v_add_u32 v205, 21504, v202
v_lshl_add_u32 v20, v40, 11, v41
v_add_u32 v21, 15360, v20
v_add_u32 v22, 30720, v20
v_add_u32 v23, 46080, v20
v_lshrrev_b32 v40, 4, v1
v_and_b32 v41, 15, v1
v_lshlrev_b32 v42, 1, v40
v_xor_b32 v41, v41, v42
v_lshlrev_b32 v42, 4, v41
v_lshl_add_u32 v16, v40, 13, v42
v_add_u32 v17, 31744, v16
v_xor_b32 v18, 0x80, v16
v_add_u32 v18, 63488, v18
v_add_u32 v19, 31744, v18
s_and_b32 s30, s3, 1
s_lshr_b32 s31, s3, 1
s_lshl_b32 s32, s30, 20
s_lshl_b32 s33, s27, 16
s_add_u32 s32, s32, s33
s_lshl_b32 s33, s31, 15
s_add_u32 s32, s32, s33
s_lshl_b32 s34, s28, 9
s_lshl_b32 s33, s31, 8
s_add_u32 s34, s34, s33
s_lshl_b32 s33, s30, 18
s_add_u32 s34, s34, s33
s_sub_u32 s35, 0x200000, s32
s_sub_u32 s36, 0x400000, s34
s_cmp_lt_u32 s87, 16
s_cselect_b32 s46, 1, 0
s_waitcnt lgkmcnt(0)
s_cselect_b32 s38, s76, s78
s_cselect_b32 s39, s77, s79
s_add_u32 s8, s38, s34
s_addc_u32 s9, s39, 0
s_and_b32 s9, s9, 0xffff
s_mov_b32 s10, s36
s_mov_b32 s11, 0x20000
s_add_u32 s4, s72, s32
s_addc_u32 s5, s73, 0
s_and_b32 s5, s5, 0xffff
s_mov_b32 s6, s35
s_mov_b32 s7, 0x20000
s_add_u32 s37, s92, 0
s_mov_b32 s42, 0
s_mov_b32 s43, 131072
s_mov_b32 m0, s37
s_nop 0
buffer_load_dwordx4 v16, s[8:11], s42 offen offset:0 nt lds
buffer_load_dwordx4 v17, s[8:11], s42 offen offset:1024 nt lds
buffer_load_dwordx4 v18, s[8:11], s42 offen offset:2048 nt lds
buffer_load_dwordx4 v19, s[8:11], s42 offen offset:3072 nt lds
s_add_u32 m0, s37, 4096
s_nop 0
buffer_load_dwordx4 v16, s[8:11], s43 offen offset:0 nt lds
buffer_load_dwordx4 v17, s[8:11], s43 offen offset:1024 nt lds
buffer_load_dwordx4 v18, s[8:11], s43 offen offset:2048 nt lds
buffer_load_dwordx4 v19, s[8:11], s43 offen offset:3072 nt lds
s_add_u32 s60, s84, 62914560
s_addc_u32 s61, s85, 0
s_and_b32 s61, s61, 0xffff
s_mov_b32 s62, 82944
s_mov_b32 s63, 0x20000
s_lshl_b32 s90, s86, 13
v_lshlrev_b32 v3, 5, v0
s_lshl_b32 s93, s2, 4
s_add_u32 s93, s93, s3
s_lshl_b32 s93, s93, 2
s_add_u32 s93, s93, 0x10000
s_add_u32 s37, s92, 32768
s_mov_b32 s40, 0
s_mov_b32 s41, 524288
s_mov_b32 m0, s37
s_nop 0
buffer_load_dwordx4 v202, s[4:7], s40 offen offset:0 lds
v_lshlrev_b32 v45, 20, v1
v_add_u32 v44, s93, v45
s_lshl_b32 s93, s87, 2
s_add_u32 s93, s93, s3
s_xor_b32 s93, s93, 32
s_lshl_b32 s93, s93, 20
s_lshl_b32 s0, s86, 2
s_add_u32 s93, s93, s0
s_add_u32 s93, s93, 0x14000
v_lshlrev_b32 v45, 7, v1
v_add_u32 v46, s93, v45
buffer_load_dwordx4 v203, s[4:7], s40 offen offset:1024 lds
s_mov_b64 exec, 0
s_cmp_eq_u32 s3, 0
s_cbranch_scc1 .Lpad1
s_cmp_eq_u32 s3, 1
s_cbranch_scc1 .Lp1_v_pad
s_cmp_eq_u32 s3, 2
s_cbranch_scc1 .Lpad2
s_branch .Lpad3
.Ltch0:
s_mov_b64 exec, -1
v_mov_b32 v45, 0
s_lshl_b32 s33, s2, 5
v_lshlrev_b32 v41, 13, v0
v_lshrrev_b32 v42, 3, v0
v_lshl_add_u32 v41, v42, 20, v41
v_add_u32 v6, s33, v41
s_and_b32 s33, s87, 2
s_lshl_b32 s33, s33, 5
s_or_b32 s33, s33, s86
v_and_b32 v41, 0x47, v0
v_cmp_eq_u32 vcc, s33, v41
s_nop 1
v_cndmask_b32 v14, 0, 1, vcc
buffer_load_dwordx4 v204, s[4:7], s40 offen offset:2048 lds
s_lshl_b32 s33, s30, 14
s_add_u32 s33, s33, 32768
v_and_b32 v36, 15, v1
v_lshrrev_b32 v37, 4, v1
v_and_b32 v38, 7, v36
v_xor_b32 v38, v38, v37
v_lshlrev_b32 v38, 4, v38
v_lshl_add_u32 v192, v36, 7, v38
v_add_u32 v192, s33, v192
v_xor_b32 v193, 64, v192
s_lshl_b32 s33, s31, 14
buffer_load_dwordx4 v205, s[4:7], s40 offen offset:3072 lds
v_and_b32 v36, 15, v1
v_lshrrev_b32 v37, 4, v1
v_lshrrev_b32 v38, 2, v36
v_and_b32 v39, 3, v36
v_lshl_add_u32 v40, v37, 3, v38
v_and_b32 v41, 1, v37
v_lshl_or_b32 v41, v41, 2, v38
v_lshlrev_b32 v40, 8, v40
v_lshl_add_u32 v40, v39, 3, v40
v_add_u32 v40, s33, v40
v_xor_b32 v36, 0, v41
v_lshl_add_u32 v194, v36, 5, v40
s_add_u32 m0, s37, 4096
s_nop 0
buffer_load_dwordx4 v202, s[4:7], s41 offen offset:0 lds
s_mov_b64 exec, 0
s_cmp_eq_u32 s3, 0
s_cbranch_scc1 .Lp2_pad
s_cmp_eq_u32 s3, 1
s_cbranch_scc1 .Lpad5
s_cmp_eq_u32 s3, 2
s_cbranch_scc1 .Lpad6
s_branch .Lpad7
.Ltch1:
s_mov_b64 exec, -1
v_xor_b32 v36, 1, v41
v_lshl_add_u32 v195, v36, 5, v40
v_xor_b32 v36, 2, v41
v_lshl_add_u32 v196, v36, 5, v40
v_xor_b32 v36, 3, v41
v_lshl_add_u32 v197, v36, 5, v40
v_xor_b32 v36, 4, v41
v_lshl_add_u32 v198, v36, 5, v40
v_xor_b32 v36, 5, v41
v_lshl_add_u32 v199, v36, 5, v40
v_xor_b32 v36, 6, v41
buffer_load_dwordx4 v203, s[4:7], s41 offen offset:1024 lds
v_lshl_add_u32 v200, v36, 5, v40
v_xor_b32 v36, 7, v41
v_lshl_add_u32 v201, v36, 5, v40
s_and_b32 s95, s27, 3
s_add_u32 s34, s46, 5
s_lshl_b32 s33, s95, s34
v_xor_b32 v194, s33, v194
v_xor_b32 v195, s33, v195
v_xor_b32 v196, s33, v196
v_xor_b32 v197, s33, v197
v_xor_b32 v198, s33, v198
buffer_load_dwordx4 v204, s[4:7], s41 offen offset:2048 lds
v_xor_b32 v199, s33, v199
v_xor_b32 v200, s33, v200
v_xor_b32 v201, s33, v201
s_lshl_b32 s95, s95, 10
s_add_u32 s33, s92, 32768
v_and_b32 v36, 15, v1
v_lshrrev_b32 v37, 4, v1
v_and_b32 v38, 7, v36
v_xor_b32 v38, v38, v37
v_lshlrev_b32 v38, 4, v38
v_lshl_add_u32 v24, v36, 7, v38
v_add_u32 v24, s33, v24
buffer_load_dwordx4 v205, s[4:7], s41 offen offset:3072 lds
s_mov_b64 exec, 0
s_cmp_eq_u32 s3, 0
s_cbranch_scc1 .Lpad8
s_cmp_eq_u32 s3, 1
s_cbranch_scc1 .Lpad9
s_cmp_eq_u32 s3, 2
s_cbranch_scc1 .Lpad10
s_branch .Lj0_pad
.Ltch2:
s_mov_b64 exec, -1
v_xor_b32 v25, 64, v24
v_and_b32 v36, 15, v1
v_lshrrev_b32 v37, 4, v1
v_lshrrev_b32 v38, 2, v36
v_and_b32 v39, 3, v36
v_lshl_add_u32 v40, v37, 3, v38
v_and_b32 v41, 1, v37
v_lshl_or_b32 v41, v41, 2, v38
v_lshlrev_b32 v40, 8, v40
v_lshl_add_u32 v40, v39, 3, v40
v_add_u32 v40, 65536, v40
s_add_u32 s37, s92, 65536
s_mov_b32 s42, 524288
s_mov_b32 s43, 655360
s_mov_b32 m0, s37
s_nop 0
buffer_load_dwordx4 v16, s[8:11], s42 offen offset:0 nt lds
v_xor_b32 v36, 0, v41
v_lshl_add_u32 v26, v36, 5, v40
v_xor_b32 v36, 1, v41
v_lshl_add_u32 v27, v36, 5, v40
v_xor_b32 v36, 2, v41
v_lshl_add_u32 v28, v36, 5, v40
v_xor_b32 v36, 3, v41
v_lshl_add_u32 v29, v36, 5, v40
v_xor_b32 v36, 4, v41
v_lshl_add_u32 v30, v36, 5, v40
v_xor_b32 v36, 5, v41
v_lshl_add_u32 v31, v36, 5, v40
buffer_load_dwordx4 v17, s[8:11], s42 offen offset:1024 nt lds
v_xor_b32 v36, 6, v41
v_lshl_add_u32 v32, v36, 5, v40
v_xor_b32 v36, 7, v41
v_lshl_add_u32 v33, v36, 5, v40
s_add_u32 s12, s70, 0
s_addc_u32 s13, s71, 0
s_and_b32 s13, s13, 0xffff
s_mov_b32 s14, 4194304
s_mov_b32 s15, 0x20000
s_lshl_b32 s33, s88, 8
s_lshl_b32 s34, s3, 17
buffer_load_dwordx4 v18, s[8:11], s42 offen offset:2048 nt lds
s_mov_b64 exec, 0
s_cmp_eq_u32 s3, 0
s_cbranch_scc1 .Lpad11
s_cmp_eq_u32 s3, 1
s_cbranch_scc1 .Lpad12
s_cmp_eq_u32 s3, 2
s_cbranch_scc1 .Lpad13
s_branch .Lfb_pad
.Ltch3:
s_mov_b64 exec, -1
s_add_u32 s33, s33, s34
s_sub_u32 s34, 0x800000, s33
s_add_u32 s16, s74, s33
s_addc_u32 s17, s75, 0
s_and_b32 s17, s17, 0xffff
s_mov_b32 s18, s34
s_mov_b32 s19, 0x20000
s_sub_u32 s24, 15, s89
s_lshl_b32 s24, s24, 18
s_lshl_b32 s25, s89, 18
s_lshl_b32 s33, s3, 16
buffer_load_dwordx4 v19, s[8:11], s42 offen offset:3072 nt lds
s_add_u32 s94, s3, 1
s_and_b32 s94, s94, 3
s_add_u32 s24, s24, s33
s_lshl_b32 s33, s94, 16
s_add_u32 s25, s25, s33
s_mov_b32 s26, 0
s_lshl_b32 s33, s28, 1
s_add_u32 s33, s33, s31
s_lshl_b32 s33, s33, 19
s_lshl_b32 s34, s27, 1
s_add_u32 s34, s34, s30
s_lshl_b32 s34, s34, 13
s_add_u32 m0, s37, 4096
s_nop 0
buffer_load_dwordx4 v16, s[8:11], s43 offen offset:0 nt lds
s_add_u32 s33, s33, s34
s_xor_b32 s34, s46, 1
s_lshl_b32 s34, s34, 24
s_add_u32 s33, s33, s34
s_add_u32 s20, s84, s33
s_addc_u32 s21, s85, 0
s_and_b32 s21, s21, 0xffff
s_mov_b32 s22, 524288
s_mov_b32 s23, 0x20000
s_cmp_eq_u32 s46, 1
s_cselect_b32 s47, 1040356334, 1.0
buffer_load_dwordx4 v17, s[8:11], s43 offen offset:1024 nt lds
s_mov_b64 exec, 0
s_cmp_eq_u32 s3, 0
s_cbranch_scc1 .LT1_pad
s_cmp_eq_u32 s3, 1
s_cbranch_scc1 .LS1_pad
s_cmp_eq_u32 s3, 2
s_cbranch_scc1 .LE1_pad
s_branch .Lool_pad
.Ltch4:
s_mov_b64 exec, -1
s_lshl_b32 s33, s87, 2
s_add_u32 s33, s33, s3
s_lshl_b32 s33, s33, 13
s_lshl_b32 s34, s86, 10
s_add_u32 s33, s33, s34
v_and_b32 v36, 7, v1
v_lshlrev_b32 v36, 7, v36
v_add_u32 v206, s33, v36
s_lshl_b32 s33, s2, 2
s_add_u32 s33, s33, s3
s_lshl_b32 s33, s33, 12
v_bfe_u32 v37, v1, 1, 1
buffer_load_dwordx4 v18, s[8:11], s43 offen offset:2048 nt lds
v_and_b32 v38, 1, v1
v_lshlrev_b32 v37, 11, v37
v_lshl_add_u32 v37, v38, 7, v37
v_add_u32 v207, s33, v37
s_mov_b32 s56, s74
s_and_b32 s57, s75, 0xffff
s_mov_b32 s58, 0x800000
s_mov_b32 s59, 0x20000
s_mov_b32 s48, 0x100000
s_mov_b32 s49, 256
s_lshl_b32 s33, s2, 2
buffer_load_dwordx4 v19, s[8:11], s43 offen offset:3072 nt lds
s_add_u32 s33, s33, s3
s_mul_i32 s33, s33, 640
v_min_u32 v37, 4, v1
v_lshlrev_b32 v37, 7, v37
v_add_u32 v207, s33, v37
s_mov_b32 s49, 0
s_mov_b32 s35, s92
s_add_u32 s36, s92, 32768
s_add_u32 s39, s92, 163840
s_mov_b32 s54, 65536
s_mov_b32 s55, -98304
s_mov_b32 s38, 1
s_waitcnt vmcnt(8)
s_barrier
ds_read_b64_tr_b16 v[64:65], v194 offset:0
ds_read_b64_tr_b16 v[66:67], v194 offset:1024
ds_read_b64_tr_b16 v[68:69], v194 offset:8192
ds_read_b64_tr_b16 v[70:71], v194 offset:9216
ds_read_b128 v[128:131], v192 offset:0
ds_read_b128 v[132:135], v193 offset:0
ds_read_b64_tr_b16 v[72:73], v195 offset:0
ds_read_b64_tr_b16 v[74:75], v195 offset:1024
ds_read_b64_tr_b16 v[76:77], v195 offset:8192
ds_read_b64_tr_b16 v[78:79], v195 offset:9216
ds_read_b64_tr_b16 v[80:81], v196 offset:0
ds_read_b64_tr_b16 v[82:83], v196 offset:1024
s_waitcnt lgkmcnt(6)
ds_read_b64_tr_b16 v[84:85], v196 offset:8192
ds_read_b64_tr_b16 v[86:87], v196 offset:9216
ds_read_b64_tr_b16 v[88:89], v197 offset:0
ds_read_b64_tr_b16 v[90:91], v197 offset:1024
ds_read_b64_tr_b16 v[92:93], v197 offset:8192
ds_read_b64_tr_b16 v[94:95], v197 offset:9216
ds_read_b128 v[136:139], v192 offset:2048
ds_read_b128 v[140:143], v193 offset:2048
s_cmp_eq_u32 s46, 1
s_waitcnt lgkmcnt(0)
s_cbranch_scc0 .Lp1_v
v_mfma_f32_16x16x32_bf16 a[0:3], v[64:67], v[128:131], 0
ds_read_b128 v[144:147], v192 offset:4096
v_mfma_f32_16x16x32_bf16 a[0:3], v[68:71], v[132:135], a[0:3]
s_add_u32 s37, s92, 98304
s_mov_b32 s40, 128
s_mov_b32 s41, 524416
v_mfma_f32_16x16x32_bf16 a[4:7], v[72:75], v[128:131], 0
s_mov_b32 m0, s37
ds_read_b128 v[148:151], v193 offset:4096
v_mfma_f32_16x16x32_bf16 a[4:7], v[76:79], v[132:135], a[4:7]
buffer_load_dwordx4 v202, s[4:7], s40 offen offset:0 lds
v_mfma_f32_16x16x32_bf16 a[8:11], v[80:83], v[128:131], 0
ds_read_b128 v[152:155], v192 offset:6144
v_mfma_f32_16x16x32_bf16 a[8:11], v[84:87], v[132:135], a[8:11]
buffer_load_dwordx4 v203, s[4:7], s40 offen offset:1024 lds
v_mfma_f32_16x16x32_bf16 a[12:15], v[88:91], v[128:131], 0
ds_read_b128 v[156:159], v193 offset:6144
v_mfma_f32_16x16x32_bf16 a[12:15], v[92:95], v[132:135], a[12:15]
buffer_load_dwordx4 v204, s[4:7], s40 offen offset:2048 lds
v_mfma_f32_16x16x32_bf16 a[32:35], v[64:67], v[136:139], 0
ds_read_b128 v[160:163], v192 offset:8192
v_mfma_f32_16x16x32_bf16 a[32:35], v[68:71], v[140:143], a[32:35]
buffer_load_dwordx4 v205, s[4:7], s40 offen offset:3072 lds
v_mfma_f32_16x16x32_bf16 a[36:39], v[72:75], v[136:139], 0
s_add_u32 m0, s37, 4096
ds_read_b128 v[164:167], v193 offset:8192
v_mfma_f32_16x16x32_bf16 a[36:39], v[76:79], v[140:143], a[36:39]
buffer_load_dwordx4 v202, s[4:7], s41 offen offset:0 lds
v_mfma_f32_16x16x32_bf16 a[40:43], v[80:83], v[136:139], 0
ds_read_b128 v[168:171], v192 offset:10240
v_mfma_f32_16x16x32_bf16 a[40:43], v[84:87], v[140:143], a[40:43]
buffer_load_dwordx4 v203, s[4:7], s41 offen offset:1024 lds
v_mfma_f32_16x16x32_bf16 a[44:47], v[88:91], v[136:139], 0
ds_read_b128 v[172:175], v193 offset:10240
v_mfma_f32_16x16x32_bf16 a[44:47], v[92:95], v[140:143], a[44:47]
s_waitcnt lgkmcnt(4)
buffer_load_dwordx4 v204, s[4:7], s41 offen offset:2048 lds
v_mfma_f32_16x16x32_bf16 a[64:67], v[64:67], v[144:147], 0
ds_read_b128 v[176:179], v192 offset:12288
v_mfma_f32_16x16x32_bf16 a[64:67], v[68:71], v[148:151], a[64:67]
buffer_load_dwordx4 v205, s[4:7], s41 offen offset:3072 lds
v_mfma_f32_16x16x32_bf16 a[68:71], v[72:75], v[144:147], 0
ds_read_b128 v[180:183], v193 offset:12288
v_mfma_f32_16x16x32_bf16 a[68:71], v[76:79], v[148:151], a[68:71]
s_add_u32 s37, s92, 131072
s_mov_b32 s42, 1048576
s_mov_b32 s43, 1179648
v_mfma_f32_16x16x32_bf16 a[72:75], v[80:83], v[144:147], 0
s_mov_b32 m0, s37
ds_read_b128 v[184:187], v192 offset:14336
v_mfma_f32_16x16x32_bf16 a[72:75], v[84:87], v[148:151], a[72:75]
buffer_load_dwordx4 v16, s[8:11], s42 offen offset:0 nt lds
v_mfma_f32_16x16x32_bf16 a[76:79], v[88:91], v[144:147], 0
ds_read_b128 v[188:191], v193 offset:14336
v_mfma_f32_16x16x32_bf16 a[76:79], v[92:95], v[148:151], a[76:79]
buffer_load_dwordx4 v17, s[8:11], s42 offen offset:1024 nt lds
v_mfma_f32_16x16x32_bf16 a[96:99], v[64:67], v[152:155], 0
ds_read_b64_tr_b16 v[96:97], v198 offset:0
v_mfma_f32_16x16x32_bf16 a[96:99], v[68:71], v[156:159], a[96:99]
buffer_load_dwordx4 v18, s[8:11], s42 offen offset:2048 nt lds
ds_read_b64_tr_b16 v[98:99], v198 offset:1024
v_mfma_f32_16x16x32_bf16 a[100:103], v[72:75], v[152:155], 0
v_mfma_f32_16x16x32_bf16 a[100:103], v[76:79], v[156:159], a[100:103]
buffer_load_dwordx4 v19, s[8:11], s42 offen offset:3072 nt lds
ds_read_b64_tr_b16 v[100:101], v198 offset:8192
v_mfma_f32_16x16x32_bf16 a[104:107], v[80:83], v[152:155], 0
s_add_u32 m0, s37, 4096
ds_read_b64_tr_b16 v[102:103], v198 offset:9216
v_mfma_f32_16x16x32_bf16 a[104:107], v[84:87], v[156:159], a[104:107]
buffer_load_dwordx4 v16, s[8:11], s43 offen offset:0 nt lds
v_mfma_f32_16x16x32_bf16 a[108:111], v[88:91], v[152:155], 0
ds_read_b64_tr_b16 v[104:105], v199 offset:0
v_mfma_f32_16x16x32_bf16 a[108:111], v[92:95], v[156:159], a[108:111]
s_waitcnt lgkmcnt(9)
buffer_load_dwordx4 v17, s[8:11], s43 offen offset:1024 nt lds
ds_read_b64_tr_b16 v[106:107], v199 offset:1024
v_mfma_f32_16x16x32_bf16 a[128:131], v[64:67], v[160:163], 0
v_mfma_f32_16x16x32_bf16 a[128:131], v[68:71], v[164:167], a[128:131]
buffer_load_dwordx4 v18, s[8:11], s43 offen offset:2048 nt lds
ds_read_b64_tr_b16 v[108:109], v199 offset:8192
v_mfma_f32_16x16x32_bf16 a[132:135], v[72:75], v[160:163], 0
ds_read_b64_tr_b16 v[110:111], v199 offset:9216
v_mfma_f32_16x16x32_bf16 a[132:135], v[76:79], v[164:167], a[132:135]
buffer_load_dwordx4 v19, s[8:11], s43 offen offset:3072 nt lds
v_mfma_f32_16x16x32_bf16 a[136:139], v[80:83], v[160:163], 0
ds_read_b64_tr_b16 v[112:113], v200 offset:0
v_mfma_f32_16x16x32_bf16 a[136:139], v[84:87], v[164:167], a[136:139]
ds_read_b64_tr_b16 v[114:115], v200 offset:1024
v_mfma_f32_16x16x32_bf16 a[140:143], v[88:91], v[160:163], 0
v_mfma_f32_16x16x32_bf16 a[140:143], v[92:95], v[164:167], a[140:143]
ds_read_b64_tr_b16 v[116:117], v200 offset:8192
v_mfma_f32_16x16x32_bf16 a[160:163], v[64:67], v[168:171], 0
ds_read_b64_tr_b16 v[118:119], v200 offset:9216
v_mfma_f32_16x16x32_bf16 a[160:163], v[68:71], v[172:175], a[160:163]
v_mfma_f32_16x16x32_bf16 a[164:167], v[72:75], v[168:171], 0
ds_read_b64_tr_b16 v[120:121], v201 offset:0
v_mfma_f32_16x16x32_bf16 a[164:167], v[76:79], v[172:175], a[164:167]
ds_read_b64_tr_b16 v[122:123], v201 offset:1024
v_mfma_f32_16x16x32_bf16 a[168:171], v[80:83], v[168:171], 0
s_add_u32 s44, s35, 65536
v_mfma_f32_16x16x32_bf16 a[168:171], v[84:87], v[172:175], a[168:171]
s_cmp_ge_u32 s44, s39
s_cselect_b32 s45, s55, s54
ds_read_b64_tr_b16 v[124:125], v201 offset:8192
v_mfma_f32_16x16x32_bf16 a[172:175], v[88:91], v[168:171], 0
ds_read_b64_tr_b16 v[126:127], v201 offset:9216
v_mfma_f32_16x16x32_bf16 a[172:175], v[92:95], v[172:175], a[172:175]
s_waitcnt lgkmcnt(15)
s_add_u32 s44, s36, 65536
v_mfma_f32_16x16x32_bf16 a[192:195], v[64:67], v[176:179], 0
v_mfma_f32_16x16x32_bf16 a[192:195], v[68:71], v[180:183], a[192:195]
s_cmp_ge_u32 s44, s39
s_cselect_b32 s34, s55, s54
v_mfma_f32_16x16x32_bf16 a[196:199], v[72:75], v[176:179], 0
v_add_u32 v194, s45, v194
v_mfma_f32_16x16x32_bf16 a[196:199], v[76:79], v[180:183], a[196:199]
v_add_u32 v195, s45, v195
v_mfma_f32_16x16x32_bf16 a[200:203], v[80:83], v[176:179], 0
v_add_u32 v196, s45, v196
v_mfma_f32_16x16x32_bf16 a[200:203], v[84:87], v[180:183], a[200:203]
v_add_u32 v197, s45, v197
v_mfma_f32_16x16x32_bf16 a[204:207], v[88:91], v[176:179], 0
v_add_u32 v198, s45, v198
v_mfma_f32_16x16x32_bf16 a[204:207], v[92:95], v[180:183], a[204:207]
v_add_u32 v199, s45, v199
v_mfma_f32_16x16x32_bf16 a[224:227], v[64:67], v[184:187], 0
v_add_u32 v200, s45, v200
v_mfma_f32_16x16x32_bf16 a[224:227], v[68:71], v[188:191], a[224:227]
v_add_u32 v201, s45, v201
v_mfma_f32_16x16x32_bf16 a[228:231], v[72:75], v[184:187], 0
v_add_u32 v192, s34, v192
v_mfma_f32_16x16x32_bf16 a[228:231], v[76:79], v[188:191], a[228:231]
v_add_u32 v193, s34, v193
v_mfma_f32_16x16x32_bf16 a[232:235], v[80:83], v[184:187], 0
v_mfma_f32_16x16x32_bf16 a[232:235], v[84:87], v[188:191], a[232:235]
s_mov_b32 s40, 256
v_mfma_f32_16x16x32_bf16 a[236:239], v[88:91], v[184:187], 0
s_mov_b32 s41, 524544
v_mfma_f32_16x16x32_bf16 a[236:239], v[92:95], v[188:191], a[236:239]
s_waitcnt vmcnt(8) lgkmcnt(0)
s_barrier
v_mfma_f32_16x16x32_bf16 a[16:19], v[96:99], v[128:131], 0
s_mov_b32 m0, s35
ds_read_b64_tr_b16 v[64:65], v194 offset:0
v_mfma_f32_16x16x32_bf16 a[16:19], v[100:103], v[132:135], a[16:19]
buffer_load_dwordx4 v202, s[4:7], s40 offen offset:0 lds
v_mfma_f32_16x16x32_bf16 a[20:23], v[104:107], v[128:131], 0
ds_read_b64_tr_b16 v[66:67], v194 offset:1024
v_mfma_f32_16x16x32_bf16 a[20:23], v[108:111], v[132:135], a[20:23]
v_mfma_f32_16x16x32_bf16 a[24:27], v[112:115], v[128:131], 0
ds_read_b64_tr_b16 v[68:69], v194 offset:8192
v_mfma_f32_16x16x32_bf16 a[24:27], v[116:119], v[132:135], a[24:27]
buffer_load_dwordx4 v203, s[4:7], s40 offen offset:1024 lds
v_mfma_f32_16x16x32_bf16 a[28:31], v[120:123], v[128:131], 0
ds_read_b64_tr_b16 v[70:71], v194 offset:9216
v_mfma_f32_16x16x32_bf16 a[28:31], v[124:127], v[132:135], a[28:31]
ds_read_b128 v[128:131], v192 offset:0
ds_read_b128 v[132:135], v193 offset:0
v_mfma_f32_16x16x32_bf16 a[48:51], v[96:99], v[136:139], 0
ds_read_b64_tr_b16 v[72:73], v195 offset:0
v_mfma_f32_16x16x32_bf16 a[48:51], v[100:103], v[140:143], a[48:51]
buffer_load_dwordx4 v204, s[4:7], s40 offen offset:2048 lds
v_mfma_f32_16x16x32_bf16 a[52:55], v[104:107], v[136:139], 0
ds_read_b64_tr_b16 v[74:75], v195 offset:1024
v_mfma_f32_16x16x32_bf16 a[52:55], v[108:111], v[140:143], a[52:55]
v_mfma_f32_16x16x32_bf16 a[56:59], v[112:115], v[136:139], 0
ds_read_b64_tr_b16 v[76:77], v195 offset:8192
v_mfma_f32_16x16x32_bf16 a[56:59], v[116:119], v[140:143], a[56:59]
buffer_load_dwordx4 v205, s[4:7], s40 offen offset:3072 lds
v_mfma_f32_16x16x32_bf16 a[60:63], v[120:123], v[136:139], 0
ds_read_b64_tr_b16 v[78:79], v195 offset:9216
v_mfma_f32_16x16x32_bf16 a[60:63], v[124:127], v[140:143], a[60:63]
ds_read_b128 v[136:139], v192 offset:2048
ds_read_b128 v[140:143], v193 offset:2048
v_mfma_f32_16x16x32_bf16 a[80:83], v[96:99], v[144:147], 0
s_add_u32 m0, s35, 4096
ds_read_b64_tr_b16 v[80:81], v196 offset:0
v_mfma_f32_16x16x32_bf16 a[80:83], v[100:103], v[148:151], a[80:83]
buffer_load_dwordx4 v202, s[4:7], s41 offen offset:0 lds
v_mfma_f32_16x16x32_bf16 a[84:87], v[104:107], v[144:147], 0
ds_read_b64_tr_b16 v[82:83], v196 offset:1024
v_mfma_f32_16x16x32_bf16 a[84:87], v[108:111], v[148:151], a[84:87]
v_mfma_f32_16x16x32_bf16 a[88:91], v[112:115], v[144:147], 0
ds_read_b64_tr_b16 v[84:85], v196 offset:8192
v_mfma_f32_16x16x32_bf16 a[88:91], v[116:119], v[148:151], a[88:91]
buffer_load_dwordx4 v203, s[4:7], s41 offen offset:1024 lds
v_mfma_f32_16x16x32_bf16 a[92:95], v[120:123], v[144:147], 0
ds_read_b64_tr_b16 v[86:87], v196 offset:9216
v_mfma_f32_16x16x32_bf16 a[92:95], v[124:127], v[148:151], a[92:95]
ds_read_b128 v[144:147], v192 offset:4096
ds_read_b128 v[148:151], v193 offset:4096
v_mfma_f32_16x16x32_bf16 a[112:115], v[96:99], v[152:155], 0
ds_read_b64_tr_b16 v[88:89], v197 offset:0
v_mfma_f32_16x16x32_bf16 a[112:115], v[100:103], v[156:159], a[112:115]
buffer_load_dwordx4 v204, s[4:7], s41 offen offset:2048 lds
v_mfma_f32_16x16x32_bf16 a[116:119], v[104:107], v[152:155], 0
ds_read_b64_tr_b16 v[90:91], v197 offset:1024
v_mfma_f32_16x16x32_bf16 a[116:119], v[108:111], v[156:159], a[116:119]
v_mfma_f32_16x16x32_bf16 a[120:123], v[112:115], v[152:155], 0
ds_read_b64_tr_b16 v[92:93], v197 offset:8192
v_mfma_f32_16x16x32_bf16 a[120:123], v[116:119], v[156:159], a[120:123]
buffer_load_dwordx4 v205, s[4:7], s41 offen offset:3072 lds
v_mfma_f32_16x16x32_bf16 a[124:127], v[120:123], v[152:155], 0
ds_read_b64_tr_b16 v[94:95], v197 offset:9216
v_mfma_f32_16x16x32_bf16 a[124:127], v[124:127], v[156:159], a[124:127]
ds_read_b128 v[152:155], v192 offset:6144
ds_read_b128 v[156:159], v193 offset:6144
v_mfma_f32_16x16x32_bf16 a[144:147], v[96:99], v[160:163], 0
v_mfma_f32_16x16x32_bf16 a[144:147], v[100:103], v[164:167], a[144:147]
buffer_load_dword v35, v206, s[56:59], s48 offen
v_mfma_f32_16x16x32_bf16 a[148:151], v[104:107], v[160:163], 0
v_mfma_f32_16x16x32_bf16 a[148:151], v[108:111], v[164:167], a[148:151]
buffer_load_dword v2, v3, s[60:63], s90 offen sc1
v_mfma_f32_16x16x32_bf16 a[152:155], v[112:115], v[160:163], 0
s_mov_b32 s42, 1572864
v_mfma_f32_16x16x32_bf16 a[152:155], v[116:119], v[164:167], a[152:155]
s_mov_b32 s43, 1703936
buffer_load_dword v35, v207, s[12:15], s49 offen
v_mfma_f32_16x16x32_bf16 a[156:159], v[120:123], v[160:163], 0
ds_read_b128 v[160:163], v192 offset:8192
v_mfma_f32_16x16x32_bf16 a[156:159], v[124:127], v[164:167], a[156:159]
buffer_load_dword v11, v6, s[60:63], 0 offen sc1
ds_read_b128 v[164:167], v193 offset:8192
v_mfma_f32_16x16x32_bf16 a[176:179], v[96:99], v[168:171], 0
s_add_u32 s40, s40, 128
v_mfma_f32_16x16x32_bf16 a[176:179], v[100:103], v[172:175], a[176:179]
s_add_u32 s41, s41, 128
v_mfma_f32_16x16x32_bf16 a[180:183], v[104:107], v[168:171], 0
v_mfma_f32_16x16x32_bf16 a[180:183], v[108:111], v[172:175], a[180:183]
s_add_u32 s48, s48, 0x100000
v_mfma_f32_16x16x32_bf16 a[184:187], v[112:115], v[168:171], 0
v_mfma_f32_16x16x32_bf16 a[184:187], v[116:119], v[172:175], a[184:187]
s_add_u32 s49, s49, 655360
v_mfma_f32_16x16x32_bf16 a[188:191], v[120:123], v[168:171], 0
ds_read_b128 v[168:171], v192 offset:10240
v_mfma_f32_16x16x32_bf16 a[188:191], v[124:127], v[172:175], a[188:191]
buffer_store_dword v45, v44, s[60:63], 0 offen sc1
ds_read_b128 v[172:175], v193 offset:10240
v_mfma_f32_16x16x32_bf16 a[208:211], v[96:99], v[176:179], 0
v_mfma_f32_16x16x32_bf16 a[208:211], v[100:103], v[180:183], a[208:211]
v_mfma_f32_16x16x32_bf16 a[212:215], v[104:107], v[176:179], 0
v_mfma_f32_16x16x32_bf16 a[212:215], v[108:111], v[180:183], a[212:215]
buffer_store_dword v45, v46, s[60:63], 0 offen sc1
v_mfma_f32_16x16x32_bf16 a[216:219], v[112:115], v[176:179], 0
v_mfma_f32_16x16x32_bf16 a[216:219], v[116:119], v[180:183], a[216:219]
v_mfma_f32_16x16x32_bf16 a[220:223], v[120:123], v[176:179], 0
ds_read_b128 v[176:179], v192 offset:12288
v_mfma_f32_16x16x32_bf16 a[220:223], v[124:127], v[180:183], a[220:223]
ds_read_b128 v[180:183], v193 offset:12288
v_mfma_f32_16x16x32_bf16 a[240:243], v[96:99], v[184:187], 0
v_mfma_f32_16x16x32_bf16 a[240:243], v[100:103], v[188:191], a[240:243]
s_mov_b32 s37, s36
v_mfma_f32_16x16x32_bf16 a[244:247], v[104:107], v[184:187], 0
s_add_u32 s35, s35, s45
v_mfma_f32_16x16x32_bf16 a[244:247], v[108:111], v[188:191], a[244:247]
s_add_u32 s36, s36, s34
v_mfma_f32_16x16x32_bf16 a[248:251], v[112:115], v[184:187], 0
v_mfma_f32_16x16x32_bf16 a[248:251], v[116:119], v[188:191], a[248:251]
s_waitcnt lgkmcnt(0)
v_mfma_f32_16x16x32_bf16 a[252:255], v[120:123], v[184:187], 0
ds_read_b128 v[184:187], v192 offset:14336
.Lpad1:
s_cbranch_execz .Ltch0
v_mfma_f32_16x16x32_bf16 a[252:255], v[124:127], v[188:191], a[252:255]
ds_read_b128 v[188:191], v193 offset:14336
s_nop 0
s_nop 0
s_nop 0
s_nop 0
s_nop 0
s_nop 0
s_nop 0
s_nop 0
.Lp1_kloop:
v_mfma_f32_16x16x32_bf16 a[0:3], v[64:67], v[128:131], a[0:3]
s_mov_b32 m0, s37
ds_read_b64_tr_b16 v[96:97], v198 offset:0
v_mfma_f32_16x16x32_bf16 a[0:3], v[68:71], v[132:135], a[0:3]
buffer_load_dwordx4 v16, s[8:11], s42 offen offset:0 nt lds
v_mfma_f32_16x16x32_bf16 a[4:7], v[72:75], v[128:131], a[4:7]
ds_read_b64_tr_b16 v[98:99], v198 offset:1024
v_mfma_f32_16x16x32_bf16 a[4:7], v[76:79], v[132:135], a[4:7]
v_mfma_f32_16x16x32_bf16 a[8:11], v[80:83], v[128:131], a[8:11]
ds_read_b64_tr_b16 v[100:101], v198 offset:8192
v_mfma_f32_16x16x32_bf16 a[8:11], v[84:87], v[132:135], a[8:11]
buffer_load_dwordx4 v17, s[8:11], s42 offen offset:1024 nt lds
v_mfma_f32_16x16x32_bf16 a[12:15], v[88:91], v[128:131], a[12:15]
ds_read_b64_tr_b16 v[102:103], v198 offset:9216
v_mfma_f32_16x16x32_bf16 a[12:15], v[92:95], v[132:135], a[12:15]
v_mfma_f32_16x16x32_bf16 a[32:35], v[64:67], v[136:139], a[32:35]
ds_read_b64_tr_b16 v[104:105], v199 offset:0
v_mfma_f32_16x16x32_bf16 a[32:35], v[68:71], v[140:143], a[32:35]
buffer_load_dwordx4 v18, s[8:11], s42 offen offset:2048 nt lds
v_mfma_f32_16x16x32_bf16 a[36:39], v[72:75], v[136:139], a[36:39]
ds_read_b64_tr_b16 v[106:107], v199 offset:1024
v_mfma_f32_16x16x32_bf16 a[36:39], v[76:79], v[140:143], a[36:39]
v_mfma_f32_16x16x32_bf16 a[40:43], v[80:83], v[136:139], a[40:43]
ds_read_b64_tr_b16 v[108:109], v199 offset:8192
v_mfma_f32_16x16x32_bf16 a[40:43], v[84:87], v[140:143], a[40:43]
buffer_load_dwordx4 v19, s[8:11], s42 offen offset:3072 nt lds
v_mfma_f32_16x16x32_bf16 a[44:47], v[88:91], v[136:139], a[44:47]
ds_read_b64_tr_b16 v[110:111], v199 offset:9216
v_mfma_f32_16x16x32_bf16 a[44:47], v[92:95], v[140:143], a[44:47]
v_mfma_f32_16x16x32_bf16 a[64:67], v[64:67], v[144:147], a[64:67]
s_add_u32 m0, s37, 4096
ds_read_b64_tr_b16 v[112:113], v200 offset:0
v_mfma_f32_16x16x32_bf16 a[64:67], v[68:71], v[148:151], a[64:67]
buffer_load_dwordx4 v16, s[8:11], s43 offen offset:0 nt lds
v_mfma_f32_16x16x32_bf16 a[68:71], v[72:75], v[144:147], a[68:71]
ds_read_b64_tr_b16 v[114:115], v200 offset:1024
v_mfma_f32_16x16x32_bf16 a[68:71], v[76:79], v[148:151], a[68:71]
v_mfma_f32_16x16x32_bf16 a[72:75], v[80:83], v[144:147], a[72:75]
ds_read_b64_tr_b16 v[116:117], v200 offset:8192
v_mfma_f32_16x16x32_bf16 a[72:75], v[84:87], v[148:151], a[72:75]
buffer_load_dwordx4 v17, s[8:11], s43 offen offset:1024 nt lds
v_mfma_f32_16x16x32_bf16 a[76:79], v[88:91], v[144:147], a[76:79]
ds_read_b64_tr_b16 v[118:119], v200 offset:9216
v_mfma_f32_16x16x32_bf16 a[76:79], v[92:95], v[148:151], a[76:79]
v_mfma_f32_16x16x32_bf16 a[96:99], v[64:67], v[152:155], a[96:99]
ds_read_b64_tr_b16 v[120:121], v201 offset:0
v_mfma_f32_16x16x32_bf16 a[96:99], v[68:71], v[156:159], a[96:99]
buffer_load_dwordx4 v18, s[8:11], s43 offen offset:2048 nt lds
v_mfma_f32_16x16x32_bf16 a[100:103], v[72:75], v[152:155], a[100:103]
ds_read_b64_tr_b16 v[122:123], v201 offset:1024
v_mfma_f32_16x16x32_bf16 a[100:103], v[76:79], v[156:159], a[100:103]
v_mfma_f32_16x16x32_bf16 a[104:107], v[80:83], v[152:155], a[104:107]
ds_read_b64_tr_b16 v[124:125], v201 offset:8192
v_mfma_f32_16x16x32_bf16 a[104:107], v[84:87], v[156:159], a[104:107]
buffer_load_dwordx4 v19, s[8:11], s43 offen offset:3072 nt lds
v_mfma_f32_16x16x32_bf16 a[108:111], v[88:91], v[152:155], a[108:111]
ds_read_b64_tr_b16 v[126:127], v201 offset:9216
v_mfma_f32_16x16x32_bf16 a[108:111], v[92:95], v[156:159], a[108:111]
v_mfma_f32_16x16x32_bf16 a[128:131], v[64:67], v[160:163], a[128:131]
v_mfma_f32_16x16x32_bf16 a[128:131], v[68:71], v[164:167], a[128:131]
v_mfma_f32_16x16x32_bf16 a[132:135], v[72:75], v[160:163], a[132:135]
v_mfma_f32_16x16x32_bf16 a[132:135], v[76:79], v[164:167], a[132:135]
v_mfma_f32_16x16x32_bf16 a[136:139], v[80:83], v[160:163], a[136:139]
v_mfma_f32_16x16x32_bf16 a[136:139], v[84:87], v[164:167], a[136:139]
v_mfma_f32_16x16x32_bf16 a[140:143], v[88:91], v[160:163], a[140:143]
v_mfma_f32_16x16x32_bf16 a[140:143], v[92:95], v[164:167], a[140:143]
v_mfma_f32_16x16x32_bf16 a[160:163], v[64:67], v[168:171], a[160:163]
s_add_u32 s42, s42, 524288
v_mfma_f32_16x16x32_bf16 a[160:163], v[68:71], v[172:175], a[160:163]
s_add_u32 s43, s43, 524288
v_mfma_f32_16x16x32_bf16 a[164:167], v[72:75], v[168:171], a[164:167]
v_mfma_f32_16x16x32_bf16 a[164:167], v[76:79], v[172:175], a[164:167]
s_waitcnt lgkmcnt(15)
v_mfma_f32_16x16x32_bf16 a[168:171], v[80:83], v[168:171], a[168:171]
s_add_u32 s44, s35, 65536
v_mfma_f32_16x16x32_bf16 a[168:171], v[84:87], v[172:175], a[168:171]
s_cmp_ge_u32 s44, s39
s_cselect_b32 s45, s55, s54
v_mfma_f32_16x16x32_bf16 a[172:175], v[88:91], v[168:171], a[172:175]
v_mfma_f32_16x16x32_bf16 a[172:175], v[92:95], v[172:175], a[172:175]
s_add_u32 s44, s36, 65536
v_mfma_f32_16x16x32_bf16 a[192:195], v[64:67], v[176:179], a[192:195]
v_mfma_f32_16x16x32_bf16 a[192:195], v[68:71], v[180:183], a[192:195]
s_cmp_ge_u32 s44, s39
s_cselect_b32 s34, s55, s54
v_mfma_f32_16x16x32_bf16 a[196:199], v[72:75], v[176:179], a[196:199]
v_add_u32 v194, s45, v194
v_mfma_f32_16x16x32_bf16 a[196:199], v[76:79], v[180:183], a[196:199]
v_add_u32 v195, s45, v195
v_mfma_f32_16x16x32_bf16 a[200:203], v[80:83], v[176:179], a[200:203]
v_add_u32 v196, s45, v196
v_mfma_f32_16x16x32_bf16 a[200:203], v[84:87], v[180:183], a[200:203]
v_add_u32 v197, s45, v197
v_mfma_f32_16x16x32_bf16 a[204:207], v[88:91], v[176:179], a[204:207]
v_add_u32 v198, s45, v198
v_mfma_f32_16x16x32_bf16 a[204:207], v[92:95], v[180:183], a[204:207]
v_add_u32 v199, s45, v199
v_mfma_f32_16x16x32_bf16 a[224:227], v[64:67], v[184:187], a[224:227]
v_add_u32 v200, s45, v200
v_mfma_f32_16x16x32_bf16 a[224:227], v[68:71], v[188:191], a[224:227]
v_add_u32 v201, s45, v201
v_mfma_f32_16x16x32_bf16 a[228:231], v[72:75], v[184:187], a[228:231]
v_add_u32 v192, s34, v192
v_mfma_f32_16x16x32_bf16 a[228:231], v[76:79], v[188:191], a[228:231]
v_add_u32 v193, s34, v193
v_mfma_f32_16x16x32_bf16 a[232:235], v[80:83], v[184:187], a[232:235]
v_mfma_f32_16x16x32_bf16 a[232:235], v[84:87], v[188:191], a[232:235]
v_mfma_f32_16x16x32_bf16 a[236:239], v[88:91], v[184:187], a[236:239]
v_mfma_f32_16x16x32_bf16 a[236:239], v[92:95], v[188:191], a[236:239]
s_waitcnt vmcnt(10) lgkmcnt(0)
s_barrier
v_mfma_f32_16x16x32_bf16 a[16:19], v[96:99], v[128:131], a[16:19]
s_mov_b32 m0, s35
ds_read_b64_tr_b16 v[64:65], v194 offset:0
v_mfma_f32_16x16x32_bf16 a[16:19], v[100:103], v[132:135], a[16:19]
buffer_load_dwordx4 v202, s[4:7], s40 offen offset:0 lds
v_mfma_f32_16x16x32_bf16 a[20:23], v[104:107], v[128:131], a[20:23]
ds_read_b64_tr_b16 v[66:67], v194 offset:1024
v_mfma_f32_16x16x32_bf16 a[20:23], v[108:111], v[132:135], a[20:23]
v_mfma_f32_16x16x32_bf16 a[24:27], v[112:115], v[128:131], a[24:27]
ds_read_b64_tr_b16 v[68:69], v194 offset:8192
v_mfma_f32_16x16x32_bf16 a[24:27], v[116:119], v[132:135], a[24:27]
buffer_load_dwordx4 v203, s[4:7], s40 offen offset:1024 lds
v_mfma_f32_16x16x32_bf16 a[28:31], v[120:123], v[128:131], a[28:31]
ds_read_b64_tr_b16 v[70:71], v194 offset:9216
v_mfma_f32_16x16x32_bf16 a[28:31], v[124:127], v[132:135], a[28:31]
ds_read_b128 v[128:131], v192 offset:0
ds_read_b128 v[132:135], v193 offset:0
v_mfma_f32_16x16x32_bf16 a[48:51], v[96:99], v[136:139], a[48:51]
ds_read_b64_tr_b16 v[72:73], v195 offset:0
v_mfma_f32_16x16x32_bf16 a[48:51], v[100:103], v[140:143], a[48:51]
buffer_load_dwordx4 v204, s[4:7], s40 offen offset:2048 lds
v_mfma_f32_16x16x32_bf16 a[52:55], v[104:107], v[136:139], a[52:55]
ds_read_b64_tr_b16 v[74:75], v195 offset:1024
v_mfma_f32_16x16x32_bf16 a[52:55], v[108:111], v[140:143], a[52:55]
v_mfma_f32_16x16x32_bf16 a[56:59], v[112:115], v[136:139], a[56:59]
ds_read_b64_tr_b16 v[76:77], v195 offset:8192
v_mfma_f32_16x16x32_bf16 a[56:59], v[116:119], v[140:143], a[56:59]
buffer_load_dwordx4 v205, s[4:7], s40 offen offset:3072 lds
v_mfma_f32_16x16x32_bf16 a[60:63], v[120:123], v[136:139], a[60:63]
ds_read_b64_tr_b16 v[78:79], v195 offset:9216
v_mfma_f32_16x16x32_bf16 a[60:63], v[124:127], v[140:143], a[60:63]
ds_read_b128 v[136:139], v192 offset:2048
ds_read_b128 v[140:143], v193 offset:2048
v_mfma_f32_16x16x32_bf16 a[80:83], v[96:99], v[144:147], a[80:83]
s_add_u32 m0, s35, 4096
ds_read_b64_tr_b16 v[80:81], v196 offset:0
v_mfma_f32_16x16x32_bf16 a[80:83], v[100:103], v[148:151], a[80:83]
buffer_load_dwordx4 v202, s[4:7], s41 offen offset:0 lds
v_mfma_f32_16x16x32_bf16 a[84:87], v[104:107], v[144:147], a[84:87]
ds_read_b64_tr_b16 v[82:83], v196 offset:1024
v_mfma_f32_16x16x32_bf16 a[84:87], v[108:111], v[148:151], a[84:87]
v_mfma_f32_16x16x32_bf16 a[88:91], v[112:115], v[144:147], a[88:91]
ds_read_b64_tr_b16 v[84:85], v196 offset:8192
v_mfma_f32_16x16x32_bf16 a[88:91], v[116:119], v[148:151], a[88:91]
buffer_load_dwordx4 v203, s[4:7], s41 offen offset:1024 lds
v_mfma_f32_16x16x32_bf16 a[92:95], v[120:123], v[144:147], a[92:95]
ds_read_b64_tr_b16 v[86:87], v196 offset:9216
v_mfma_f32_16x16x32_bf16 a[92:95], v[124:127], v[148:151], a[92:95]
ds_read_b128 v[144:147], v192 offset:4096
ds_read_b128 v[148:151], v193 offset:4096
v_mfma_f32_16x16x32_bf16 a[112:115], v[96:99], v[152:155], a[112:115]
ds_read_b64_tr_b16 v[88:89], v197 offset:0
v_mfma_f32_16x16x32_bf16 a[112:115], v[100:103], v[156:159], a[112:115]
buffer_load_dwordx4 v204, s[4:7], s41 offen offset:2048 lds
v_mfma_f32_16x16x32_bf16 a[116:119], v[104:107], v[152:155], a[116:119]
ds_read_b64_tr_b16 v[90:91], v197 offset:1024
v_mfma_f32_16x16x32_bf16 a[116:119], v[108:111], v[156:159], a[116:119]
v_mfma_f32_16x16x32_bf16 a[120:123], v[112:115], v[152:155], a[120:123]
ds_read_b64_tr_b16 v[92:93], v197 offset:8192
v_mfma_f32_16x16x32_bf16 a[120:123], v[116:119], v[156:159], a[120:123]
buffer_load_dwordx4 v205, s[4:7], s41 offen offset:3072 lds
v_mfma_f32_16x16x32_bf16 a[124:127], v[120:123], v[152:155], a[124:127]
ds_read_b64_tr_b16 v[94:95], v197 offset:9216
v_mfma_f32_16x16x32_bf16 a[124:127], v[124:127], v[156:159], a[124:127]
ds_read_b128 v[152:155], v192 offset:6144
ds_read_b128 v[156:159], v193 offset:6144
v_mfma_f32_16x16x32_bf16 a[144:147], v[96:99], v[160:163], a[144:147]
v_mfma_f32_16x16x32_bf16 a[144:147], v[100:103], v[164:167], a[144:147]
buffer_load_dword v35, v206, s[56:59], s48 offen
v_mfma_f32_16x16x32_bf16 a[148:151], v[104:107], v[160:163], a[148:151]
v_mfma_f32_16x16x32_bf16 a[148:151], v[108:111], v[164:167], a[148:151]
v_mfma_f32_16x16x32_bf16 a[152:155], v[112:115], v[160:163], a[152:155]
v_mfma_f32_16x16x32_bf16 a[152:155], v[116:119], v[164:167], a[152:155]
buffer_load_dword v35, v207, s[12:15], s49 offen
v_mfma_f32_16x16x32_bf16 a[156:159], v[120:123], v[160:163], a[156:159]
ds_read_b128 v[160:163], v192 offset:8192
v_mfma_f32_16x16x32_bf16 a[156:159], v[124:127], v[164:167], a[156:159]
ds_read_b128 v[164:167], v193 offset:8192
v_mfma_f32_16x16x32_bf16 a[176:179], v[96:99], v[168:171], a[176:179]
s_add_u32 s40, s40, 128
v_mfma_f32_16x16x32_bf16 a[176:179], v[100:103], v[172:175], a[176:179]
s_add_u32 s41, s41, 128
v_mfma_f32_16x16x32_bf16 a[180:183], v[104:107], v[168:171], a[180:183]
v_mfma_f32_16x16x32_bf16 a[180:183], v[108:111], v[172:175], a[180:183]
s_add_u32 s48, s48, 0x100000
v_mfma_f32_16x16x32_bf16 a[184:187], v[112:115], v[168:171], a[184:187]
v_mfma_f32_16x16x32_bf16 a[184:187], v[116:119], v[172:175], a[184:187]
s_add_u32 s49, s49, 655360
v_mfma_f32_16x16x32_bf16 a[188:191], v[120:123], v[168:171], a[188:191]
ds_read_b128 v[168:171], v192 offset:10240
v_mfma_f32_16x16x32_bf16 a[188:191], v[124:127], v[172:175], a[188:191]
ds_read_b128 v[172:175], v193 offset:10240
v_mfma_f32_16x16x32_bf16 a[208:211], v[96:99], v[176:179], a[208:211]
v_mfma_f32_16x16x32_bf16 a[208:211], v[100:103], v[180:183], a[208:211]
v_mfma_f32_16x16x32_bf16 a[212:215], v[104:107], v[176:179], a[212:215]
v_mfma_f32_16x16x32_bf16 a[212:215], v[108:111], v[180:183], a[212:215]
v_mfma_f32_16x16x32_bf16 a[216:219], v[112:115], v[176:179], a[216:219]
v_mfma_f32_16x16x32_bf16 a[216:219], v[116:119], v[180:183], a[216:219]
v_mfma_f32_16x16x32_bf16 a[220:223], v[120:123], v[176:179], a[220:223]
ds_read_b128 v[176:179], v192 offset:12288
v_mfma_f32_16x16x32_bf16 a[220:223], v[124:127], v[180:183], a[220:223]
ds_read_b128 v[180:183], v193 offset:12288
v_mfma_f32_16x16x32_bf16 a[240:243], v[96:99], v[184:187], a[240:243]
v_mfma_f32_16x16x32_bf16 a[240:243], v[100:103], v[188:191], a[240:243]
s_mov_b32 s37, s36
v_mfma_f32_16x16x32_bf16 a[244:247], v[104:107], v[184:187], a[244:247]
s_add_u32 s35, s35, s45
v_mfma_f32_16x16x32_bf16 a[244:247], v[108:111], v[188:191], a[244:247]
s_add_u32 s36, s36, s34
v_mfma_f32_16x16x32_bf16 a[248:251], v[112:115], v[184:187], a[248:251]
s_add_u32 s38, s38, 1
v_mfma_f32_16x16x32_bf16 a[248:251], v[116:119], v[188:191], a[248:251]
s_cmp_lt_u32 s38, 6
s_waitcnt lgkmcnt(0)
v_mfma_f32_16x16x32_bf16 a[252:255], v[120:123], v[184:187], a[252:255]
ds_read_b128 v[184:187], v192 offset:14336
v_mfma_f32_16x16x32_bf16 a[252:255], v[124:127], v[188:191], a[252:255]
ds_read_b128 v[188:191], v193 offset:14336
s_cbranch_scc1 .Lp1_kloop
v_mfma_f32_16x16x32_bf16 a[0:3], v[64:67], v[128:131], a[0:3]
s_add_u32 m0, s92, 32768
ds_read_b64_tr_b16 v[96:97], v198 offset:0
v_mfma_f32_16x16x32_bf16 a[0:3], v[68:71], v[132:135], a[0:3]
buffer_load_dwordx4 v20, s[12:15], s24 offen offset:0 lds
v_mfma_f32_16x16x32_bf16 a[4:7], v[72:75], v[128:131], a[4:7]
ds_read_b64_tr_b16 v[98:99], v198 offset:1024
v_mfma_f32_16x16x32_bf16 a[4:7], v[76:79], v[132:135], a[4:7]
v_mfma_f32_16x16x32_bf16 a[8:11], v[80:83], v[128:131], a[8:11]
ds_read_b64_tr_b16 v[100:101], v198 offset:8192
v_mfma_f32_16x16x32_bf16 a[8:11], v[84:87], v[132:135], a[8:11]
buffer_load_dwordx4 v21, s[12:15], s24 offen offset:1024 lds
v_mfma_f32_16x16x32_bf16 a[12:15], v[88:91], v[128:131], a[12:15]
ds_read_b64_tr_b16 v[102:103], v198 offset:9216
v_mfma_f32_16x16x32_bf16 a[12:15], v[92:95], v[132:135], a[12:15]
v_mfma_f32_16x16x32_bf16 a[32:35], v[64:67], v[136:139], a[32:35]
ds_read_b64_tr_b16 v[104:105], v199 offset:0
v_mfma_f32_16x16x32_bf16 a[32:35], v[68:71], v[140:143], a[32:35]
buffer_load_dwordx4 v22, s[12:15], s24 offen offset:2048 lds
v_mfma_f32_16x16x32_bf16 a[36:39], v[72:75], v[136:139], a[36:39]
ds_read_b64_tr_b16 v[106:107], v199 offset:1024
v_mfma_f32_16x16x32_bf16 a[36:39], v[76:79], v[140:143], a[36:39]
v_mfma_f32_16x16x32_bf16 a[40:43], v[80:83], v[136:139], a[40:43]
ds_read_b64_tr_b16 v[108:109], v199 offset:8192
v_mfma_f32_16x16x32_bf16 a[40:43], v[84:87], v[140:143], a[40:43]
buffer_load_dwordx4 v23, s[12:15], s24 offen offset:3072 lds
v_mfma_f32_16x16x32_bf16 a[44:47], v[88:91], v[136:139], a[44:47]
ds_read_b64_tr_b16 v[110:111], v199 offset:9216
v_mfma_f32_16x16x32_bf16 a[44:47], v[92:95], v[140:143], a[44:47]
v_mfma_f32_16x16x32_bf16 a[64:67], v[64:67], v[144:147], a[64:67]
s_add_u32 m0, s92, 36864
ds_read_b64_tr_b16 v[112:113], v200 offset:0
v_mfma_f32_16x16x32_bf16 a[64:67], v[68:71], v[148:151], a[64:67]
buffer_load_dwordx4 v20, s[12:15], s25 offen offset:0 lds
v_mfma_f32_16x16x32_bf16 a[68:71], v[72:75], v[144:147], a[68:71]
ds_read_b64_tr_b16 v[114:115], v200 offset:1024
v_mfma_f32_16x16x32_bf16 a[68:71], v[76:79], v[148:151], a[68:71]
v_mfma_f32_16x16x32_bf16 a[72:75], v[80:83], v[144:147], a[72:75]
ds_read_b64_tr_b16 v[116:117], v200 offset:8192
v_mfma_f32_16x16x32_bf16 a[72:75], v[84:87], v[148:151], a[72:75]
buffer_load_dwordx4 v21, s[12:15], s25 offen offset:1024 lds
v_mfma_f32_16x16x32_bf16 a[76:79], v[88:91], v[144:147], a[76:79]
ds_read_b64_tr_b16 v[118:119], v200 offset:9216
v_mfma_f32_16x16x32_bf16 a[76:79], v[92:95], v[148:151], a[76:79]
v_mfma_f32_16x16x32_bf16 a[96:99], v[64:67], v[152:155], a[96:99]
ds_read_b64_tr_b16 v[120:121], v201 offset:0
v_mfma_f32_16x16x32_bf16 a[96:99], v[68:71], v[156:159], a[96:99]
buffer_load_dwordx4 v22, s[12:15], s25 offen offset:2048 lds
v_mfma_f32_16x16x32_bf16 a[100:103], v[72:75], v[152:155], a[100:103]
ds_read_b64_tr_b16 v[122:123], v201 offset:1024
v_mfma_f32_16x16x32_bf16 a[100:103], v[76:79], v[156:159], a[100:103]
v_mfma_f32_16x16x32_bf16 a[104:107], v[80:83], v[152:155], a[104:107]
ds_read_b64_tr_b16 v[124:125], v201 offset:8192
v_mfma_f32_16x16x32_bf16 a[104:107], v[84:87], v[156:159], a[104:107]
buffer_load_dwordx4 v23, s[12:15], s25 offen offset:3072 lds
v_mfma_f32_16x16x32_bf16 a[108:111], v[88:91], v[152:155], a[108:111]
ds_read_b64_tr_b16 v[126:127], v201 offset:9216
v_mfma_f32_16x16x32_bf16 a[108:111], v[92:95], v[156:159], a[108:111]
v_mfma_f32_16x16x32_bf16 a[128:131], v[64:67], v[160:163], a[128:131]
v_mfma_f32_16x16x32_bf16 a[128:131], v[68:71], v[164:167], a[128:131]
v_mfma_f32_16x16x32_bf16 a[132:135], v[72:75], v[160:163], a[132:135]
v_mfma_f32_16x16x32_bf16 a[132:135], v[76:79], v[164:167], a[132:135]
v_mfma_f32_16x16x32_bf16 a[136:139], v[80:83], v[160:163], a[136:139]
v_mfma_f32_16x16x32_bf16 a[136:139], v[84:87], v[164:167], a[136:139]
v_mfma_f32_16x16x32_bf16 a[140:143], v[88:91], v[160:163], a[140:143]
v_mfma_f32_16x16x32_bf16 a[140:143], v[92:95], v[164:167], a[140:143]
v_mfma_f32_16x16x32_bf16 a[160:163], v[64:67], v[168:171], a[160:163]
s_add_u32 s24, s24, 128
s_add_u32 s25, s25, 128
v_mfma_f32_16x16x32_bf16 a[160:163], v[68:71], v[172:175], a[160:163]
v_mfma_f32_16x16x32_bf16 a[164:167], v[72:75], v[168:171], a[164:167]
v_mfma_f32_16x16x32_bf16 a[164:167], v[76:79], v[172:175], a[164:167]
s_waitcnt lgkmcnt(15)
v_mfma_f32_16x16x32_bf16 a[168:171], v[80:83], v[168:171], a[168:171]
s_add_u32 s44, s35, 65536
v_mfma_f32_16x16x32_bf16 a[168:171], v[84:87], v[172:175], a[168:171]
s_cmp_ge_u32 s44, s39
s_cselect_b32 s45, s55, s54
v_mfma_f32_16x16x32_bf16 a[172:175], v[88:91], v[168:171], a[172:175]
v_mfma_f32_16x16x32_bf16 a[172:175], v[92:95], v[172:175], a[172:175]
s_add_u32 s44, s36, 65536
v_mfma_f32_16x16x32_bf16 a[192:195], v[64:67], v[176:179], a[192:195]
v_mfma_f32_16x16x32_bf16 a[192:195], v[68:71], v[180:183], a[192:195]
s_cmp_ge_u32 s44, s39
s_cselect_b32 s34, s55, s54
v_mfma_f32_16x16x32_bf16 a[196:199], v[72:75], v[176:179], a[196:199]
v_add_u32 v194, s45, v194
v_mfma_f32_16x16x32_bf16 a[196:199], v[76:79], v[180:183], a[196:199]
v_add_u32 v195, s45, v195
v_mfma_f32_16x16x32_bf16 a[200:203], v[80:83], v[176:179], a[200:203]
v_add_u32 v196, s45, v196
v_mfma_f32_16x16x32_bf16 a[200:203], v[84:87], v[180:183], a[200:203]
v_add_u32 v197, s45, v197
v_mfma_f32_16x16x32_bf16 a[204:207], v[88:91], v[176:179], a[204:207]
v_add_u32 v198, s45, v198
v_mfma_f32_16x16x32_bf16 a[204:207], v[92:95], v[180:183], a[204:207]
v_add_u32 v199, s45, v199
v_mfma_f32_16x16x32_bf16 a[224:227], v[64:67], v[184:187], a[224:227]
v_add_u32 v200, s45, v200
v_mfma_f32_16x16x32_bf16 a[224:227], v[68:71], v[188:191], a[224:227]
v_add_u32 v201, s45, v201
v_mfma_f32_16x16x32_bf16 a[228:231], v[72:75], v[184:187], a[228:231]
v_add_u32 v192, s34, v192
v_mfma_f32_16x16x32_bf16 a[228:231], v[76:79], v[188:191], a[228:231]
v_add_u32 v193, s34, v193
v_mfma_f32_16x16x32_bf16 a[232:235], v[80:83], v[184:187], a[232:235]
v_mfma_f32_16x16x32_bf16 a[232:235], v[84:87], v[188:191], a[232:235]
v_mfma_f32_16x16x32_bf16 a[236:239], v[88:91], v[184:187], a[236:239]
v_mfma_f32_16x16x32_bf16 a[236:239], v[92:95], v[188:191], a[236:239]
s_waitcnt vmcnt(10) lgkmcnt(0)
s_barrier
v_mfma_f32_16x16x32_bf16 a[16:19], v[96:99], v[128:131], a[16:19]
s_add_u32 m0, s91, 65536
ds_read_b64_tr_b16 v[64:65], v194 offset:0
v_mfma_f32_16x16x32_bf16 a[16:19], v[100:103], v[132:135], a[16:19]
buffer_load_dwordx4 v16, s[16:19], s26 offen offset:0 lds
v_mfma_f32_16x16x32_bf16 a[20:23], v[104:107], v[128:131], a[20:23]
ds_read_b64_tr_b16 v[66:67], v194 offset:1024
v_mfma_f32_16x16x32_bf16 a[20:23], v[108:111], v[132:135], a[20:23]
v_mfma_f32_16x16x32_bf16 a[24:27], v[112:115], v[128:131], a[24:27]
ds_read_b64_tr_b16 v[68:69], v194 offset:8192
v_mfma_f32_16x16x32_bf16 a[24:27], v[116:119], v[132:135], a[24:27]
buffer_load_dwordx4 v17, s[16:19], s26 offen offset:1024 lds
v_mfma_f32_16x16x32_bf16 a[28:31], v[120:123], v[128:131], a[28:31]
ds_read_b64_tr_b16 v[70:71], v194 offset:9216
v_mfma_f32_16x16x32_bf16 a[28:31], v[124:127], v[132:135], a[28:31]
ds_read_b128 v[128:131], v192 offset:0
ds_read_b128 v[132:135], v193 offset:0
v_mfma_f32_16x16x32_bf16 a[48:51], v[96:99], v[136:139], a[48:51]
ds_read_b64_tr_b16 v[72:73], v195 offset:0
v_mfma_f32_16x16x32_bf16 a[48:51], v[100:103], v[140:143], a[48:51]
buffer_load_dwordx4 v18, s[16:19], s26 offen offset:2048 lds
v_mfma_f32_16x16x32_bf16 a[52:55], v[104:107], v[136:139], a[52:55]
ds_read_b64_tr_b16 v[74:75], v195 offset:1024
v_mfma_f32_16x16x32_bf16 a[52:55], v[108:111], v[140:143], a[52:55]
v_mfma_f32_16x16x32_bf16 a[56:59], v[112:115], v[136:139], a[56:59]
ds_read_b64_tr_b16 v[76:77], v195 offset:8192
v_mfma_f32_16x16x32_bf16 a[56:59], v[116:119], v[140:143], a[56:59]
buffer_load_dwordx4 v19, s[16:19], s26 offen offset:3072 lds
v_mfma_f32_16x16x32_bf16 a[60:63], v[120:123], v[136:139], a[60:63]
ds_read_b64_tr_b16 v[78:79], v195 offset:9216
v_mfma_f32_16x16x32_bf16 a[60:63], v[124:127], v[140:143], a[60:63]
s_add_u32 s26, s26, 524288
ds_read_b128 v[136:139], v192 offset:2048
ds_read_b128 v[140:143], v193 offset:2048
v_mfma_f32_16x16x32_bf16 a[80:83], v[96:99], v[144:147], a[80:83]
s_add_u32 m0, s91, 81920
ds_read_b64_tr_b16 v[80:81], v196 offset:0
v_mfma_f32_16x16x32_bf16 a[80:83], v[100:103], v[148:151], a[80:83]
buffer_load_dwordx4 v16, s[16:19], s26 offen offset:0 lds
v_mfma_f32_16x16x32_bf16 a[84:87], v[104:107], v[144:147], a[84:87]
ds_read_b64_tr_b16 v[82:83], v196 offset:1024
v_mfma_f32_16x16x32_bf16 a[84:87], v[108:111], v[148:151], a[84:87]
v_mfma_f32_16x16x32_bf16 a[88:91], v[112:115], v[144:147], a[88:91]
ds_read_b64_tr_b16 v[84:85], v196 offset:8192
v_mfma_f32_16x16x32_bf16 a[88:91], v[116:119], v[148:151], a[88:91]
buffer_load_dwordx4 v17, s[16:19], s26 offen offset:1024 lds
v_mfma_f32_16x16x32_bf16 a[92:95], v[120:123], v[144:147], a[92:95]
ds_read_b64_tr_b16 v[86:87], v196 offset:9216
v_mfma_f32_16x16x32_bf16 a[92:95], v[124:127], v[148:151], a[92:95]
ds_read_b128 v[144:147], v192 offset:4096
ds_read_b128 v[148:151], v193 offset:4096
v_mfma_f32_16x16x32_bf16 a[112:115], v[96:99], v[152:155], a[112:115]
ds_read_b64_tr_b16 v[88:89], v197 offset:0
v_mfma_f32_16x16x32_bf16 a[112:115], v[100:103], v[156:159], a[112:115]
buffer_load_dwordx4 v18, s[16:19], s26 offen offset:2048 lds
v_mfma_f32_16x16x32_bf16 a[116:119], v[104:107], v[152:155], a[116:119]
ds_read_b64_tr_b16 v[90:91], v197 offset:1024
v_mfma_f32_16x16x32_bf16 a[116:119], v[108:111], v[156:159], a[116:119]
v_mfma_f32_16x16x32_bf16 a[120:123], v[112:115], v[152:155], a[120:123]
ds_read_b64_tr_b16 v[92:93], v197 offset:8192
v_mfma_f32_16x16x32_bf16 a[120:123], v[116:119], v[156:159], a[120:123]
buffer_load_dwordx4 v19, s[16:19], s26 offen offset:3072 lds
v_mfma_f32_16x16x32_bf16 a[124:127], v[120:123], v[152:155], a[124:127]
ds_read_b64_tr_b16 v[94:95], v197 offset:9216
v_mfma_f32_16x16x32_bf16 a[124:127], v[124:127], v[156:159], a[124:127]
s_add_u32 s26, s26, 524288
ds_read_b128 v[152:155], v192 offset:6144
ds_read_b128 v[156:159], v193 offset:6144
v_mfma_f32_16x16x32_bf16 a[144:147], v[96:99], v[160:163], a[144:147]
v_mfma_f32_16x16x32_bf16 a[144:147], v[100:103], v[164:167], a[144:147]
buffer_load_dword v35, v206, s[56:59], s48 offen
v_mfma_f32_16x16x32_bf16 a[148:151], v[104:107], v[160:163], a[148:151]
v_mfma_f32_16x16x32_bf16 a[148:151], v[108:111], v[164:167], a[148:151]
v_mfma_f32_16x16x32_bf16 a[152:155], v[112:115], v[160:163], a[152:155]
v_mfma_f32_16x16x32_bf16 a[152:155], v[116:119], v[164:167], a[152:155]
buffer_load_dword v35, v207, s[12:15], s49 offen
v_add_u32 v11, 8, v11
v_and_b32 v11, -8, v11
v_mfma_f32_16x16x32_bf16 a[156:159], v[120:123], v[160:163], a[156:159]
ds_read_b128 v[160:163], v192 offset:8192
v_mfma_f32_16x16x32_bf16 a[156:159], v[124:127], v[164:167], a[156:159]
ds_read_b128 v[164:167], v193 offset:8192
v_mfma_f32_16x16x32_bf16 a[176:179], v[96:99], v[168:171], a[176:179]
v_mfma_f32_16x16x32_bf16 a[176:179], v[100:103], v[172:175], a[176:179]
s_nop 0
v_or_b32 v0, 0, v11
v_mfma_f32_16x16x32_bf16 a[180:183], v[104:107], v[168:171], a[180:183]
v_mfma_f32_16x16x32_bf16 a[180:183], v[108:111], v[172:175], a[180:183]
s_add_u32 s48, s48, 0x100000
v_mfma_f32_16x16x32_bf16 a[184:187], v[112:115], v[168:171], a[184:187]
v_mfma_f32_16x16x32_bf16 a[184:187], v[116:119], v[172:175], a[184:187]
s_add_u32 s49, s49, 655360
v_mfma_f32_16x16x32_bf16 a[188:191], v[120:123], v[168:171], a[188:191]
ds_read_b128 v[168:171], v192 offset:10240
v_mfma_f32_16x16x32_bf16 a[188:191], v[124:127], v[172:175], a[188:191]
buffer_store_dword v0, v6, s[60:63], 0 offen sc1
ds_read_b128 v[172:175], v193 offset:10240
v_mfma_f32_16x16x32_bf16 a[208:211], v[96:99], v[176:179], a[208:211]
v_mfma_f32_16x16x32_bf16 a[208:211], v[100:103], v[180:183], a[208:211]
v_mfma_f32_16x16x32_bf16 a[212:215], v[104:107], v[176:179], a[212:215]
v_mfma_f32_16x16x32_bf16 a[212:215], v[108:111], v[180:183], a[212:215]
buffer_load_dword v35, v4, s[96:99], 0 offen
v_mfma_f32_16x16x32_bf16 a[216:219], v[112:115], v[176:179], a[216:219]
v_mfma_f32_16x16x32_bf16 a[216:219], v[116:119], v[180:183], a[216:219]
v_mfma_f32_16x16x32_bf16 a[220:223], v[120:123], v[176:179], a[220:223]
ds_read_b128 v[176:179], v192 offset:12288
v_mfma_f32_16x16x32_bf16 a[220:223], v[124:127], v[180:183], a[220:223]
ds_read_b128 v[180:183], v193 offset:12288
v_mfma_f32_16x16x32_bf16 a[240:243], v[96:99], v[184:187], a[240:243]
v_mfma_f32_16x16x32_bf16 a[240:243], v[100:103], v[188:191], a[240:243]
s_mov_b32 s37, s36
v_mfma_f32_16x16x32_bf16 a[244:247], v[104:107], v[184:187], a[244:247]
s_add_u32 s35, s35, s45
v_mfma_f32_16x16x32_bf16 a[244:247], v[108:111], v[188:191], a[244:247]
s_add_u32 s36, s36, s34
v_mfma_f32_16x16x32_bf16 a[248:251], v[112:115], v[184:187], a[248:251]
v_mfma_f32_16x16x32_bf16 a[248:251], v[116:119], v[188:191], a[248:251]
s_waitcnt lgkmcnt(0)
v_mfma_f32_16x16x32_bf16 a[252:255], v[120:123], v[184:187], a[252:255]
ds_read_b128 v[184:187], v192 offset:14336
v_mfma_f32_16x16x32_bf16 a[252:255], v[124:127], v[188:191], a[252:255]
ds_read_b128 v[188:191], v193 offset:14336
v_mfma_f32_16x16x32_bf16 a[0:3], v[64:67], v[128:131], a[0:3]
s_add_u32 m0, s92, 98304
ds_read_b64_tr_b16 v[96:97], v198 offset:0
v_mfma_f32_16x16x32_bf16 a[0:3], v[68:71], v[132:135], a[0:3]
buffer_load_dwordx4 v20, s[12:15], s24 offen offset:0 lds
v_mfma_f32_16x16x32_bf16 a[4:7], v[72:75], v[128:131], a[4:7]
ds_read_b64_tr_b16 v[98:99], v198 offset:1024
v_mfma_f32_16x16x32_bf16 a[4:7], v[76:79], v[132:135], a[4:7]
v_mfma_f32_16x16x32_bf16 a[8:11], v[80:83], v[128:131], a[8:11]
ds_read_b64_tr_b16 v[100:101], v198 offset:8192
v_mfma_f32_16x16x32_bf16 a[8:11], v[84:87], v[132:135], a[8:11]
buffer_load_dwordx4 v21, s[12:15], s24 offen offset:1024 lds
v_mfma_f32_16x16x32_bf16 a[12:15], v[88:91], v[128:131], a[12:15]
ds_read_b64_tr_b16 v[102:103], v198 offset:9216
v_mfma_f32_16x16x32_bf16 a[12:15], v[92:95], v[132:135], a[12:15]
v_mfma_f32_16x16x32_bf16 a[32:35], v[64:67], v[136:139], a[32:35]
ds_read_b64_tr_b16 v[104:105], v199 offset:0
v_mfma_f32_16x16x32_bf16 a[32:35], v[68:71], v[140:143], a[32:35]
buffer_load_dwordx4 v22, s[12:15], s24 offen offset:2048 lds
v_mfma_f32_16x16x32_bf16 a[36:39], v[72:75], v[136:139], a[36:39]
ds_read_b64_tr_b16 v[106:107], v199 offset:1024
v_mfma_f32_16x16x32_bf16 a[36:39], v[76:79], v[140:143], a[36:39]
v_mfma_f32_16x16x32_bf16 a[40:43], v[80:83], v[136:139], a[40:43]
ds_read_b64_tr_b16 v[108:109], v199 offset:8192
v_mfma_f32_16x16x32_bf16 a[40:43], v[84:87], v[140:143], a[40:43]
buffer_load_dwordx4 v23, s[12:15], s24 offen offset:3072 lds
v_mfma_f32_16x16x32_bf16 a[44:47], v[88:91], v[136:139], a[44:47]
ds_read_b64_tr_b16 v[110:111], v199 offset:9216
v_mfma_f32_16x16x32_bf16 a[44:47], v[92:95], v[140:143], a[44:47]
v_mfma_f32_16x16x32_bf16 a[64:67], v[64:67], v[144:147], a[64:67]
s_add_u32 m0, s92, 102400
ds_read_b64_tr_b16 v[112:113], v200 offset:0
v_mfma_f32_16x16x32_bf16 a[64:67], v[68:71], v[148:151], a[64:67]
buffer_load_dwordx4 v20, s[12:15], s25 offen offset:0 lds
v_mfma_f32_16x16x32_bf16 a[68:71], v[72:75], v[144:147], a[68:71]
ds_read_b64_tr_b16 v[114:115], v200 offset:1024
v_mfma_f32_16x16x32_bf16 a[68:71], v[76:79], v[148:151], a[68:71]
v_mfma_f32_16x16x32_bf16 a[72:75], v[80:83], v[144:147], a[72:75]
ds_read_b64_tr_b16 v[116:117], v200 offset:8192
v_mfma_f32_16x16x32_bf16 a[72:75], v[84:87], v[148:151], a[72:75]
buffer_load_dwordx4 v21, s[12:15], s25 offen offset:1024 lds
v_mfma_f32_16x16x32_bf16 a[76:79], v[88:91], v[144:147], a[76:79]
ds_read_b64_tr_b16 v[118:119], v200 offset:9216
v_mfma_f32_16x16x32_bf16 a[76:79], v[92:95], v[148:151], a[76:79]
v_mfma_f32_16x16x32_bf16 a[96:99], v[64:67], v[152:155], a[96:99]
ds_read_b64_tr_b16 v[120:121], v201 offset:0
v_mfma_f32_16x16x32_bf16 a[96:99], v[68:71], v[156:159], a[96:99]
buffer_load_dwordx4 v22, s[12:15], s25 offen offset:2048 lds
v_mfma_f32_16x16x32_bf16 a[100:103], v[72:75], v[152:155], a[100:103]
ds_read_b64_tr_b16 v[122:123], v201 offset:1024
v_mfma_f32_16x16x32_bf16 a[100:103], v[76:79], v[156:159], a[100:103]
v_mfma_f32_16x16x32_bf16 a[104:107], v[80:83], v[152:155], a[104:107]
ds_read_b64_tr_b16 v[124:125], v201 offset:8192
v_mfma_f32_16x16x32_bf16 a[104:107], v[84:87], v[156:159], a[104:107]
buffer_load_dwordx4 v23, s[12:15], s25 offen offset:3072 lds
v_mfma_f32_16x16x32_bf16 a[108:111], v[88:91], v[152:155], a[108:111]
ds_read_b64_tr_b16 v[126:127], v201 offset:9216
v_mfma_f32_16x16x32_bf16 a[108:111], v[92:95], v[156:159], a[108:111]
v_mfma_f32_16x16x32_bf16 a[128:131], v[64:67], v[160:163], a[128:131]
v_mfma_f32_16x16x32_bf16 a[128:131], v[68:71], v[164:167], a[128:131]
v_mfma_f32_16x16x32_bf16 a[132:135], v[72:75], v[160:163], a[132:135]
v_mfma_f32_16x16x32_bf16 a[132:135], v[76:79], v[164:167], a[132:135]
v_mfma_f32_16x16x32_bf16 a[136:139], v[80:83], v[160:163], a[136:139]
v_mfma_f32_16x16x32_bf16 a[136:139], v[84:87], v[164:167], a[136:139]
v_mfma_f32_16x16x32_bf16 a[140:143], v[88:91], v[160:163], a[140:143]
v_mfma_f32_16x16x32_bf16 a[140:143], v[92:95], v[164:167], a[140:143]
v_mfma_f32_16x16x32_bf16 a[160:163], v[64:67], v[168:171], a[160:163]
s_add_u32 s24, s24, 128
s_add_u32 s25, s25, 128
v_mfma_f32_16x16x32_bf16 a[160:163], v[68:71], v[172:175], a[160:163]
.Lpad2:
s_cbranch_execz .Ltch0
v_mfma_f32_16x16x32_bf16 a[164:167], v[72:75], v[168:171], a[164:167]
v_mfma_f32_16x16x32_bf16 a[164:167], v[76:79], v[172:175], a[164:167]
s_waitcnt lgkmcnt(15)
v_mfma_f32_16x16x32_bf16 a[168:171], v[80:83], v[168:171], a[168:171]
v_mfma_f32_16x16x32_bf16 a[168:171], v[84:87], v[172:175], a[168:171]
v_mfma_f32_16x16x32_bf16 a[172:175], v[88:91], v[168:171], a[172:175]
v_mfma_f32_16x16x32_bf16 a[172:175], v[92:95], v[172:175], a[172:175]
v_mfma_f32_16x16x32_bf16 a[192:195], v[64:67], v[176:179], a[192:195]
v_mfma_f32_16x16x32_bf16 a[192:195], v[68:71], v[180:183], a[192:195]
v_mfma_f32_16x16x32_bf16 a[196:199], v[72:75], v[176:179], a[196:199]
v_mfma_f32_16x16x32_bf16 a[196:199], v[76:79], v[180:183], a[196:199]
v_mfma_f32_16x16x32_bf16 a[200:203], v[80:83], v[176:179], a[200:203]
v_mfma_f32_16x16x32_bf16 a[200:203], v[84:87], v[180:183], a[200:203]
v_mfma_f32_16x16x32_bf16 a[204:207], v[88:91], v[176:179], a[204:207]
v_mfma_f32_16x16x32_bf16 a[204:207], v[92:95], v[180:183], a[204:207]
v_mfma_f32_16x16x32_bf16 a[224:227], v[64:67], v[184:187], a[224:227]
v_mfma_f32_16x16x32_bf16 a[224:227], v[68:71], v[188:191], a[224:227]
v_mfma_f32_16x16x32_bf16 a[228:231], v[72:75], v[184:187], a[228:231]
v_mfma_f32_16x16x32_bf16 a[228:231], v[76:79], v[188:191], a[228:231]
v_mfma_f32_16x16x32_bf16 a[232:235], v[80:83], v[184:187], a[232:235]
v_mfma_f32_16x16x32_bf16 a[232:235], v[84:87], v[188:191], a[232:235]
v_mfma_f32_16x16x32_bf16 a[236:239], v[88:91], v[184:187], a[236:239]
v_mfma_f32_16x16x32_bf16 a[236:239], v[92:95], v[188:191], a[236:239]
s_waitcnt vmcnt(16) lgkmcnt(0)
s_barrier
v_mfma_f32_16x16x32_bf16 a[16:19], v[96:99], v[128:131], a[16:19]
s_add_u32 m0, s91, 0
ds_read_b64_tr_b16 v[64:65], v26 offset:0
v_mfma_f32_16x16x32_bf16 a[16:19], v[100:103], v[132:135], a[16:19]
buffer_load_dwordx4 v16, s[16:19], s26 offen offset:0 lds
v_mfma_f32_16x16x32_bf16 a[20:23], v[104:107], v[128:131], a[20:23]
ds_read_b64_tr_b16 v[66:67], v26 offset:1024
v_accvgpr_read_b32 v36, a0
v_mfma_f32_16x16x32_bf16 a[20:23], v[108:111], v[132:135], a[20:23]
v_accvgpr_read_b32 v37, a1
v_mul_f32 v36, s47, v36
v_mfma_f32_16x16x32_bf16 a[24:27], v[112:115], v[128:131], a[24:27]
ds_read_b64_tr_b16 v[68:69], v26 offset:8192
v_mul_f32 v37, s47, v37
v_mfma_f32_16x16x32_bf16 a[24:27], v[116:119], v[132:135], a[24:27]
buffer_load_dwordx4 v17, s[16:19], s26 offen offset:1024 lds
v_cvt_pk_bf16_f32 v40, v36, v37
v_mfma_f32_16x16x32_bf16 a[28:31], v[120:123], v[128:131], a[28:31]
ds_read_b64_tr_b16 v[70:71], v26 offset:9216
v_accvgpr_read_b32 v38, a2
v_mfma_f32_16x16x32_bf16 a[28:31], v[124:127], v[132:135], a[28:31]
v_accvgpr_read_b32 v39, a3
v_mul_f32 v38, s47, v38
v_mfma_f32_16x16x32_bf16 a[48:51], v[96:99], v[136:139], a[48:51]
ds_read_b64_tr_b16 v[72:73], v27 offset:0
v_mul_f32 v39, s47, v39
v_mfma_f32_16x16x32_bf16 a[48:51], v[100:103], v[140:143], a[48:51]
buffer_load_dwordx4 v18, s[16:19], s26 offen offset:2048 lds
v_cvt_pk_bf16_f32 v41, v38, v39
v_mfma_f32_16x16x32_bf16 a[52:55], v[104:107], v[136:139], a[52:55]
ds_read_b64_tr_b16 v[74:75], v27 offset:1024
v_accvgpr_read_b32 v36, a4
v_mfma_f32_16x16x32_bf16 a[52:55], v[108:111], v[140:143], a[52:55]
v_accvgpr_read_b32 v37, a5
v_mul_f32 v36, s47, v36
v_mfma_f32_16x16x32_bf16 a[56:59], v[112:115], v[136:139], a[56:59]
ds_read_b64_tr_b16 v[76:77], v27 offset:8192
v_mul_f32 v37, s47, v37
v_mfma_f32_16x16x32_bf16 a[56:59], v[116:119], v[140:143], a[56:59]
buffer_load_dwordx4 v19, s[16:19], s26 offen offset:3072 lds
v_cvt_pk_bf16_f32 v42, v36, v37
v_mfma_f32_16x16x32_bf16 a[60:63], v[120:123], v[136:139], a[60:63]
ds_read_b64_tr_b16 v[78:79], v27 offset:9216
v_accvgpr_read_b32 v38, a6
v_mfma_f32_16x16x32_bf16 a[60:63], v[124:127], v[140:143], a[60:63]
s_add_u32 s26, s26, 524288
v_accvgpr_read_b32 v39, a7
v_mul_f32 v38, s47, v38
v_mfma_f32_16x16x32_bf16 a[80:83], v[96:99], v[144:147], a[80:83]
s_add_u32 m0, s92, 131072
ds_read_b64_tr_b16 v[80:81], v28 offset:0
v_mfma_f32_16x16x32_bf16 a[80:83], v[100:103], v[148:151], a[80:83]
buffer_load_dwordx4 v20, s[12:15], s24 offen offset:0 lds
v_mul_f32 v39, s47, v39
v_mfma_f32_16x16x32_bf16 a[84:87], v[104:107], v[144:147], a[84:87]
ds_read_b64_tr_b16 v[82:83], v28 offset:1024
v_cvt_pk_bf16_f32 v43, v38, v39
v_mfma_f32_16x16x32_bf16 a[84:87], v[108:111], v[148:151], a[84:87]
v_accvgpr_read_b32 v36, a8
v_accvgpr_read_b32 v37, a9
v_mfma_f32_16x16x32_bf16 a[88:91], v[112:115], v[144:147], a[88:91]
ds_read_b64_tr_b16 v[84:85], v28 offset:8192
v_mul_f32 v36, s47, v36
v_mfma_f32_16x16x32_bf16 a[88:91], v[116:119], v[148:151], a[88:91]
buffer_load_dwordx4 v21, s[12:15], s24 offen offset:1024 lds
v_mul_f32 v37, s47, v37
v_mfma_f32_16x16x32_bf16 a[92:95], v[120:123], v[144:147], a[92:95]
ds_read_b64_tr_b16 v[86:87], v28 offset:9216
v_cvt_pk_bf16_f32 v44, v36, v37
v_mfma_f32_16x16x32_bf16 a[92:95], v[124:127], v[148:151], a[92:95]
v_accvgpr_read_b32 v38, a10
v_accvgpr_read_b32 v39, a11
v_mfma_f32_16x16x32_bf16 a[112:115], v[96:99], v[152:155], a[112:115]
ds_read_b64_tr_b16 v[88:89], v29 offset:0
v_mul_f32 v38, s47, v38
v_mfma_f32_16x16x32_bf16 a[112:115], v[100:103], v[156:159], a[112:115]
buffer_load_dwordx4 v22, s[12:15], s24 offen offset:2048 lds
v_mul_f32 v39, s47, v39
v_mfma_f32_16x16x32_bf16 a[116:119], v[104:107], v[152:155], a[116:119]
ds_read_b64_tr_b16 v[90:91], v29 offset:1024
v_cvt_pk_bf16_f32 v45, v38, v39
v_mfma_f32_16x16x32_bf16 a[116:119], v[108:111], v[156:159], a[116:119]
v_accvgpr_read_b32 v36, a12
v_accvgpr_read_b32 v37, a13
v_mfma_f32_16x16x32_bf16 a[120:123], v[112:115], v[152:155], a[120:123]
ds_read_b64_tr_b16 v[92:93], v29 offset:8192
v_mul_f32 v36, s47, v36
v_mfma_f32_16x16x32_bf16 a[120:123], v[116:119], v[156:159], a[120:123]
buffer_load_dwordx4 v23, s[12:15], s24 offen offset:3072 lds
v_mul_f32 v37, s47, v37
v_mfma_f32_16x16x32_bf16 a[124:127], v[120:123], v[152:155], a[124:127]
ds_read_b64_tr_b16 v[94:95], v29 offset:9216
v_cvt_pk_bf16_f32 v46, v36, v37
v_mfma_f32_16x16x32_bf16 a[124:127], v[124:127], v[156:159], a[124:127]
v_accvgpr_read_b32 v38, a14
v_accvgpr_read_b32 v39, a15
v_mfma_f32_16x16x32_bf16 a[144:147], v[96:99], v[160:163], a[144:147]
s_add_u32 m0, s92, 135168
v_mul_f32 v38, s47, v38
v_mul_f32 v39, s47, v39
v_mfma_f32_16x16x32_bf16 a[144:147], v[100:103], v[164:167], a[144:147]
buffer_load_dwordx4 v20, s[12:15], s25 offen offset:0 lds
v_cvt_pk_bf16_f32 v47, v38, v39
v_mfma_f32_16x16x32_bf16 a[148:151], v[104:107], v[160:163], a[148:151]
v_mfma_f32_16x16x32_bf16 a[148:151], v[108:111], v[164:167], a[148:151]
v_mfma_f32_16x16x32_bf16 a[152:155], v[112:115], v[160:163], a[152:155]
v_mfma_f32_16x16x32_bf16 a[152:155], v[116:119], v[164:167], a[152:155]
buffer_load_dwordx4 v21, s[12:15], s25 offen offset:1024 lds
v_mfma_f32_16x16x32_bf16 a[156:159], v[120:123], v[160:163], a[156:159]
v_mfma_f32_16x16x32_bf16 a[156:159], v[124:127], v[164:167], a[156:159]
v_mfma_f32_16x16x32_bf16 a[176:179], v[96:99], v[168:171], a[176:179]
ds_read_b128 v[48:51], v24 offset:0
ds_read_b128 a[0:3], v25 offset:0
v_mfma_f32_16x16x32_bf16 a[176:179], v[100:103], v[172:175], a[176:179]
buffer_load_dwordx4 v22, s[12:15], s25 offen offset:2048 lds
v_mfma_f32_16x16x32_bf16 a[180:183], v[104:107], v[168:171], a[180:183]
v_mfma_f32_16x16x32_bf16 a[180:183], v[108:111], v[172:175], a[180:183]
v_mfma_f32_16x16x32_bf16 a[184:187], v[112:115], v[168:171], a[184:187]
v_mfma_f32_16x16x32_bf16 a[184:187], v[116:119], v[172:175], a[184:187]
buffer_load_dwordx4 v23, s[12:15], s25 offen offset:3072 lds
v_mfma_f32_16x16x32_bf16 a[188:191], v[120:123], v[168:171], a[188:191]
ds_read_b128 v[52:55], v24 offset:2048
ds_read_b128 a[4:7], v25 offset:2048
v_mfma_f32_16x16x32_bf16 a[188:191], v[124:127], v[172:175], a[188:191]
s_xor_b32 s48, s95, 0
v_mfma_f32_16x16x32_bf16 a[208:211], v[96:99], v[176:179], a[208:211]
buffer_store_dwordx4 v[40:43], v7, s[20:23], s48 offen sc1
ds_read_b128 v[56:59], v24 offset:4096
ds_read_b128 a[8:11], v25 offset:4096
v_mfma_f32_16x16x32_bf16 a[208:211], v[100:103], v[180:183], a[208:211]
s_add_u32 s24, s24, 128
s_add_u32 s25, s25, 128
v_mfma_f32_16x16x32_bf16 a[212:215], v[104:107], v[176:179], a[212:215]
v_mfma_f32_16x16x32_bf16 a[212:215], v[108:111], v[180:183], a[212:215]
v_mfma_f32_16x16x32_bf16 a[216:219], v[112:115], v[176:179], a[216:219]
v_mfma_f32_16x16x32_bf16 a[216:219], v[116:119], v[180:183], a[216:219]
s_xor_b32 s48, s95, 1024
v_mfma_f32_16x16x32_bf16 a[220:223], v[120:123], v[176:179], a[220:223]
buffer_store_dwordx4 v[44:47], v7, s[20:23], s48 offen sc1
ds_read_b128 v[60:63], v24 offset:6144
ds_read_b128 a[12:15], v25 offset:6144
v_mfma_f32_16x16x32_bf16 a[220:223], v[124:127], v[180:183], a[220:223]
v_mfma_f32_16x16x32_bf16 a[240:243], v[96:99], v[184:187], a[240:243]
v_mfma_f32_16x16x32_bf16 a[240:243], v[100:103], v[188:191], a[240:243]
v_mfma_f32_16x16x32_bf16 a[244:247], v[104:107], v[184:187], a[244:247]
v_mfma_f32_16x16x32_bf16 a[244:247], v[108:111], v[188:191], a[244:247]
v_mfma_f32_16x16x32_bf16 a[248:251], v[112:115], v[184:187], a[248:251]
v_mfma_f32_16x16x32_bf16 a[248:251], v[116:119], v[188:191], a[248:251]
s_waitcnt lgkmcnt(0)
v_mfma_f32_16x16x32_bf16 a[252:255], v[120:123], v[184:187], a[252:255]
v_mfma_f32_16x16x32_bf16 a[252:255], v[124:127], v[188:191], a[252:255]
s_branch .Lp2
.Lp1_v_pad:
s_cbranch_execz .Ltch0
.Lp1_v:
v_mfma_f32_16x16x32_bf16 a[0:3], v[128:131], v[64:67], 0
ds_read_b128 v[144:147], v192 offset:4096
v_mfma_f32_16x16x32_bf16 a[0:3], v[132:135], v[68:71], a[0:3]
s_add_u32 s37, s92, 98304
s_mov_b32 s40, 128
s_mov_b32 s41, 524416
v_mfma_f32_16x16x32_bf16 a[8:11], v[128:131], v[72:75], 0
s_mov_b32 m0, s37
ds_read_b128 v[148:151], v193 offset:4096
v_mfma_f32_16x16x32_bf16 a[8:11], v[132:135], v[76:79], a[8:11]
buffer_load_dwordx4 v202, s[4:7], s40 offen offset:0 lds
v_mfma_f32_16x16x32_bf16 a[16:19], v[128:131], v[80:83], 0
ds_read_b128 v[152:155], v192 offset:6144
v_mfma_f32_16x16x32_bf16 a[16:19], v[132:135], v[84:87], a[16:19]
buffer_load_dwordx4 v203, s[4:7], s40 offen offset:1024 lds
v_mfma_f32_16x16x32_bf16 a[24:27], v[128:131], v[88:91], 0
ds_read_b128 v[156:159], v193 offset:6144
v_mfma_f32_16x16x32_bf16 a[24:27], v[132:135], v[92:95], a[24:27]
buffer_load_dwordx4 v204, s[4:7], s40 offen offset:2048 lds
v_mfma_f32_16x16x32_bf16 a[4:7], v[136:139], v[64:67], 0
ds_read_b128 v[160:163], v192 offset:8192
v_mfma_f32_16x16x32_bf16 a[4:7], v[140:143], v[68:71], a[4:7]
buffer_load_dwordx4 v205, s[4:7], s40 offen offset:3072 lds
v_mfma_f32_16x16x32_bf16 a[12:15], v[136:139], v[72:75], 0
s_add_u32 m0, s37, 4096
ds_read_b128 v[164:167], v193 offset:8192
v_mfma_f32_16x16x32_bf16 a[12:15], v[140:143], v[76:79], a[12:15]
buffer_load_dwordx4 v202, s[4:7], s41 offen offset:0 lds
v_mfma_f32_16x16x32_bf16 a[20:23], v[136:139], v[80:83], 0
ds_read_b128 v[168:171], v192 offset:10240
v_mfma_f32_16x16x32_bf16 a[20:23], v[140:143], v[84:87], a[20:23]
buffer_load_dwordx4 v203, s[4:7], s41 offen offset:1024 lds
v_mfma_f32_16x16x32_bf16 a[28:31], v[136:139], v[88:91], 0
ds_read_b128 v[172:175], v193 offset:10240
v_mfma_f32_16x16x32_bf16 a[28:31], v[140:143], v[92:95], a[28:31]
s_waitcnt lgkmcnt(4)
buffer_load_dwordx4 v204, s[4:7], s41 offen offset:2048 lds
v_mfma_f32_16x16x32_bf16 a[64:67], v[144:147], v[64:67], 0
ds_read_b128 v[176:179], v192 offset:12288
v_mfma_f32_16x16x32_bf16 a[64:67], v[148:151], v[68:71], a[64:67]
buffer_load_dwordx4 v205, s[4:7], s41 offen offset:3072 lds
v_mfma_f32_16x16x32_bf16 a[72:75], v[144:147], v[72:75], 0
ds_read_b128 v[180:183], v193 offset:12288
v_mfma_f32_16x16x32_bf16 a[72:75], v[148:151], v[76:79], a[72:75]
s_add_u32 s37, s92, 131072
s_mov_b32 s42, 1048576
s_mov_b32 s43, 1179648
v_mfma_f32_16x16x32_bf16 a[80:83], v[144:147], v[80:83], 0
s_mov_b32 m0, s37
ds_read_b128 v[184:187], v192 offset:14336
v_mfma_f32_16x16x32_bf16 a[80:83], v[148:151], v[84:87], a[80:83]
buffer_load_dwordx4 v16, s[8:11], s42 offen offset:0 nt lds
v_mfma_f32_16x16x32_bf16 a[88:91], v[144:147], v[88:91], 0
ds_read_b128 v[188:191], v193 offset:14336
v_mfma_f32_16x16x32_bf16 a[88:91], v[148:151], v[92:95], a[88:91]
buffer_load_dwordx4 v17, s[8:11], s42 offen offset:1024 nt lds
v_mfma_f32_16x16x32_bf16 a[68:71], v[152:155], v[64:67], 0
ds_read_b64_tr_b16 v[96:97], v198 offset:0
v_mfma_f32_16x16x32_bf16 a[68:71], v[156:159], v[68:71], a[68:71]
buffer_load_dwordx4 v18, s[8:11], s42 offen offset:2048 nt lds
ds_read_b64_tr_b16 v[98:99], v198 offset:1024
v_mfma_f32_16x16x32_bf16 a[76:79], v[152:155], v[72:75], 0
v_mfma_f32_16x16x32_bf16 a[76:79], v[156:159], v[76:79], a[76:79]
buffer_load_dwordx4 v19, s[8:11], s42 offen offset:3072 nt lds
ds_read_b64_tr_b16 v[100:101], v198 offset:8192
v_mfma_f32_16x16x32_bf16 a[84:87], v[152:155], v[80:83], 0
s_add_u32 m0, s37, 4096
ds_read_b64_tr_b16 v[102:103], v198 offset:9216
v_mfma_f32_16x16x32_bf16 a[84:87], v[156:159], v[84:87], a[84:87]
buffer_load_dwordx4 v16, s[8:11], s43 offen offset:0 nt lds
v_mfma_f32_16x16x32_bf16 a[92:95], v[152:155], v[88:91], 0
ds_read_b64_tr_b16 v[104:105], v199 offset:0
v_mfma_f32_16x16x32_bf16 a[92:95], v[156:159], v[92:95], a[92:95]
s_waitcnt lgkmcnt(9)
buffer_load_dwordx4 v17, s[8:11], s43 offen offset:1024 nt lds
ds_read_b64_tr_b16 v[106:107], v199 offset:1024
v_mfma_f32_16x16x32_bf16 a[128:131], v[160:163], v[64:67], 0
v_mfma_f32_16x16x32_bf16 a[128:131], v[164:167], v[68:71], a[128:131]
buffer_load_dwordx4 v18, s[8:11], s43 offen offset:2048 nt lds
ds_read_b64_tr_b16 v[108:109], v199 offset:8192
v_mfma_f32_16x16x32_bf16 a[136:139], v[160:163], v[72:75], 0
ds_read_b64_tr_b16 v[110:111], v199 offset:9216
v_mfma_f32_16x16x32_bf16 a[136:139], v[164:167], v[76:79], a[136:139]
buffer_load_dwordx4 v19, s[8:11], s43 offen offset:3072 nt lds
v_mfma_f32_16x16x32_bf16 a[144:147], v[160:163], v[80:83], 0
ds_read_b64_tr_b16 v[112:113], v200 offset:0
v_mfma_f32_16x16x32_bf16 a[144:147], v[164:167], v[84:87], a[144:147]
ds_read_b64_tr_b16 v[114:115], v200 offset:1024
v_mfma_f32_16x16x32_bf16 a[152:155], v[160:163], v[88:91], 0
v_mfma_f32_16x16x32_bf16 a[152:155], v[164:167], v[92:95], a[152:155]
ds_read_b64_tr_b16 v[116:117], v200 offset:8192
v_mfma_f32_16x16x32_bf16 a[132:135], v[168:171], v[64:67], 0
ds_read_b64_tr_b16 v[118:119], v200 offset:9216
v_mfma_f32_16x16x32_bf16 a[132:135], v[172:175], v[68:71], a[132:135]
v_mfma_f32_16x16x32_bf16 a[140:143], v[168:171], v[72:75], 0
ds_read_b64_tr_b16 v[120:121], v201 offset:0
v_mfma_f32_16x16x32_bf16 a[140:143], v[172:175], v[76:79], a[140:143]
ds_read_b64_tr_b16 v[122:123], v201 offset:1024
v_mfma_f32_16x16x32_bf16 a[148:151], v[168:171], v[80:83], 0
s_add_u32 s44, s35, 65536
v_mfma_f32_16x16x32_bf16 a[148:151], v[172:175], v[84:87], a[148:151]
s_cmp_ge_u32 s44, s39
s_cselect_b32 s45, s55, s54
ds_read_b64_tr_b16 v[124:125], v201 offset:8192
v_mfma_f32_16x16x32_bf16 a[156:159], v[168:171], v[88:91], 0
ds_read_b64_tr_b16 v[126:127], v201 offset:9216
v_mfma_f32_16x16x32_bf16 a[156:159], v[172:175], v[92:95], a[156:159]
s_waitcnt lgkmcnt(15)
s_add_u32 s44, s36, 65536
v_mfma_f32_16x16x32_bf16 a[192:195], v[176:179], v[64:67], 0
v_mfma_f32_16x16x32_bf16 a[192:195], v[180:183], v[68:71], a[192:195]
s_cmp_ge_u32 s44, s39
s_cselect_b32 s34, s55, s54
v_mfma_f32_16x16x32_bf16 a[200:203], v[176:179], v[72:75], 0
v_add_u32 v194, s45, v194
v_mfma_f32_16x16x32_bf16 a[200:203], v[180:183], v[76:79], a[200:203]
v_add_u32 v195, s45, v195
v_mfma_f32_16x16x32_bf16 a[208:211], v[176:179], v[80:83], 0
v_add_u32 v196, s45, v196
v_mfma_f32_16x16x32_bf16 a[208:211], v[180:183], v[84:87], a[208:211]
v_add_u32 v197, s45, v197
v_mfma_f32_16x16x32_bf16 a[216:219], v[176:179], v[88:91], 0
v_add_u32 v198, s45, v198
v_mfma_f32_16x16x32_bf16 a[216:219], v[180:183], v[92:95], a[216:219]
v_add_u32 v199, s45, v199
v_mfma_f32_16x16x32_bf16 a[196:199], v[184:187], v[64:67], 0
v_add_u32 v200, s45, v200
v_mfma_f32_16x16x32_bf16 a[196:199], v[188:191], v[68:71], a[196:199]
v_add_u32 v201, s45, v201
v_mfma_f32_16x16x32_bf16 a[204:207], v[184:187], v[72:75], 0
v_add_u32 v192, s34, v192
v_mfma_f32_16x16x32_bf16 a[204:207], v[188:191], v[76:79], a[204:207]
v_add_u32 v193, s34, v193
v_mfma_f32_16x16x32_bf16 a[212:215], v[184:187], v[80:83], 0
v_mfma_f32_16x16x32_bf16 a[212:215], v[188:191], v[84:87], a[212:215]
s_mov_b32 s40, 256
v_mfma_f32_16x16x32_bf16 a[220:223], v[184:187], v[88:91], 0
s_mov_b32 s41, 524544
v_mfma_f32_16x16x32_bf16 a[220:223], v[188:191], v[92:95], a[220:223]
s_waitcnt vmcnt(8) lgkmcnt(0)
s_barrier
v_mfma_f32_16x16x32_bf16 a[32:35], v[128:131], v[96:99], 0
s_mov_b32 m0, s35
ds_read_b64_tr_b16 v[64:65], v194 offset:0
v_mfma_f32_16x16x32_bf16 a[32:35], v[132:135], v[100:103], a[32:35]
buffer_load_dwordx4 v202, s[4:7], s40 offen offset:0 lds
v_mfma_f32_16x16x32_bf16 a[40:43], v[128:131], v[104:107], 0
ds_read_b64_tr_b16 v[66:67], v194 offset:1024
v_mfma_f32_16x16x32_bf16 a[40:43], v[132:135], v[108:111], a[40:43]
v_mfma_f32_16x16x32_bf16 a[48:51], v[128:131], v[112:115], 0
ds_read_b64_tr_b16 v[68:69], v194 offset:8192
v_mfma_f32_16x16x32_bf16 a[48:51], v[132:135], v[116:119], a[48:51]
buffer_load_dwordx4 v203, s[4:7], s40 offen offset:1024 lds
v_mfma_f32_16x16x32_bf16 a[56:59], v[128:131], v[120:123], 0
ds_read_b64_tr_b16 v[70:71], v194 offset:9216
v_mfma_f32_16x16x32_bf16 a[56:59], v[132:135], v[124:127], a[56:59]
ds_read_b128 v[128:131], v192 offset:0
ds_read_b128 v[132:135], v193 offset:0
v_mfma_f32_16x16x32_bf16 a[36:39], v[136:139], v[96:99], 0
ds_read_b64_tr_b16 v[72:73], v195 offset:0
v_mfma_f32_16x16x32_bf16 a[36:39], v[140:143], v[100:103], a[36:39]
buffer_load_dwordx4 v204, s[4:7], s40 offen offset:2048 lds
v_mfma_f32_16x16x32_bf16 a[44:47], v[136:139], v[104:107], 0
ds_read_b64_tr_b16 v[74:75], v195 offset:1024
v_mfma_f32_16x16x32_bf16 a[44:47], v[140:143], v[108:111], a[44:47]
v_mfma_f32_16x16x32_bf16 a[52:55], v[136:139], v[112:115], 0
ds_read_b64_tr_b16 v[76:77], v195 offset:8192
v_mfma_f32_16x16x32_bf16 a[52:55], v[140:143], v[116:119], a[52:55]
buffer_load_dwordx4 v205, s[4:7], s40 offen offset:3072 lds
v_mfma_f32_16x16x32_bf16 a[60:63], v[136:139], v[120:123], 0
ds_read_b64_tr_b16 v[78:79], v195 offset:9216
v_mfma_f32_16x16x32_bf16 a[60:63], v[140:143], v[124:127], a[60:63]
ds_read_b128 v[136:139], v192 offset:2048
ds_read_b128 v[140:143], v193 offset:2048
v_mfma_f32_16x16x32_bf16 a[96:99], v[144:147], v[96:99], 0
s_add_u32 m0, s35, 4096
ds_read_b64_tr_b16 v[80:81], v196 offset:0
v_mfma_f32_16x16x32_bf16 a[96:99], v[148:151], v[100:103], a[96:99]
buffer_load_dwordx4 v202, s[4:7], s41 offen offset:0 lds
v_mfma_f32_16x16x32_bf16 a[104:107], v[144:147], v[104:107], 0
ds_read_b64_tr_b16 v[82:83], v196 offset:1024
v_mfma_f32_16x16x32_bf16 a[104:107], v[148:151], v[108:111], a[104:107]
v_mfma_f32_16x16x32_bf16 a[112:115], v[144:147], v[112:115], 0
ds_read_b64_tr_b16 v[84:85], v196 offset:8192
v_mfma_f32_16x16x32_bf16 a[112:115], v[148:151], v[116:119], a[112:115]
buffer_load_dwordx4 v203, s[4:7], s41 offen offset:1024 lds
v_mfma_f32_16x16x32_bf16 a[120:123], v[144:147], v[120:123], 0
ds_read_b64_tr_b16 v[86:87], v196 offset:9216
v_mfma_f32_16x16x32_bf16 a[120:123], v[148:151], v[124:127], a[120:123]
ds_read_b128 v[144:147], v192 offset:4096
ds_read_b128 v[148:151], v193 offset:4096
v_mfma_f32_16x16x32_bf16 a[100:103], v[152:155], v[96:99], 0
ds_read_b64_tr_b16 v[88:89], v197 offset:0
v_mfma_f32_16x16x32_bf16 a[100:103], v[156:159], v[100:103], a[100:103]
buffer_load_dwordx4 v204, s[4:7], s41 offen offset:2048 lds
v_mfma_f32_16x16x32_bf16 a[108:111], v[152:155], v[104:107], 0
ds_read_b64_tr_b16 v[90:91], v197 offset:1024
v_mfma_f32_16x16x32_bf16 a[108:111], v[156:159], v[108:111], a[108:111]
v_mfma_f32_16x16x32_bf16 a[116:119], v[152:155], v[112:115], 0
ds_read_b64_tr_b16 v[92:93], v197 offset:8192
v_mfma_f32_16x16x32_bf16 a[116:119], v[156:159], v[116:119], a[116:119]
buffer_load_dwordx4 v205, s[4:7], s41 offen offset:3072 lds
v_mfma_f32_16x16x32_bf16 a[124:127], v[152:155], v[120:123], 0
ds_read_b64_tr_b16 v[94:95], v197 offset:9216
v_mfma_f32_16x16x32_bf16 a[124:127], v[156:159], v[124:127], a[124:127]
ds_read_b128 v[152:155], v192 offset:6144
ds_read_b128 v[156:159], v193 offset:6144
v_mfma_f32_16x16x32_bf16 a[160:163], v[160:163], v[96:99], 0
v_mfma_f32_16x16x32_bf16 a[160:163], v[164:167], v[100:103], a[160:163]
buffer_load_dword v35, v206, s[56:59], s48 offen
v_mfma_f32_16x16x32_bf16 a[168:171], v[160:163], v[104:107], 0
v_mfma_f32_16x16x32_bf16 a[168:171], v[164:167], v[108:111], a[168:171]
buffer_load_dword v2, v3, s[60:63], s90 offen sc1
v_mfma_f32_16x16x32_bf16 a[176:179], v[160:163], v[112:115], 0
s_mov_b32 s42, 1572864
v_mfma_f32_16x16x32_bf16 a[176:179], v[164:167], v[116:119], a[176:179]
s_mov_b32 s43, 1703936
buffer_load_dword v35, v207, s[12:15], s49 offen
v_mfma_f32_16x16x32_bf16 a[184:187], v[160:163], v[120:123], 0
ds_read_b128 v[160:163], v192 offset:8192
v_mfma_f32_16x16x32_bf16 a[184:187], v[164:167], v[124:127], a[184:187]
buffer_load_dword v11, v6, s[60:63], 0 offen sc1
ds_read_b128 v[164:167], v193 offset:8192
v_mfma_f32_16x16x32_bf16 a[164:167], v[168:171], v[96:99], 0
s_add_u32 s40, s40, 128
v_mfma_f32_16x16x32_bf16 a[164:167], v[172:175], v[100:103], a[164:167]
s_add_u32 s41, s41, 128
v_mfma_f32_16x16x32_bf16 a[172:175], v[168:171], v[104:107], 0
v_mfma_f32_16x16x32_bf16 a[172:175], v[172:175], v[108:111], a[172:175]
s_add_u32 s48, s48, 0x100000
v_mfma_f32_16x16x32_bf16 a[180:183], v[168:171], v[112:115], 0
v_mfma_f32_16x16x32_bf16 a[180:183], v[172:175], v[116:119], a[180:183]
s_add_u32 s49, s49, 655360
v_mfma_f32_16x16x32_bf16 a[188:191], v[168:171], v[120:123], 0
ds_read_b128 v[168:171], v192 offset:10240
v_mfma_f32_16x16x32_bf16 a[188:191], v[172:175], v[124:127], a[188:191]
buffer_store_dword v45, v44, s[60:63], 0 offen sc1
ds_read_b128 v[172:175], v193 offset:10240
v_mfma_f32_16x16x32_bf16 a[224:227], v[176:179], v[96:99], 0
v_mfma_f32_16x16x32_bf16 a[224:227], v[180:183], v[100:103], a[224:227]
v_mfma_f32_16x16x32_bf16 a[232:235], v[176:179], v[104:107], 0
v_mfma_f32_16x16x32_bf16 a[232:235], v[180:183], v[108:111], a[232:235]
buffer_store_dword v45, v46, s[60:63], 0 offen sc1
v_mfma_f32_16x16x32_bf16 a[240:243], v[176:179], v[112:115], 0
v_mfma_f32_16x16x32_bf16 a[240:243], v[180:183], v[116:119], a[240:243]
v_mfma_f32_16x16x32_bf16 a[248:251], v[176:179], v[120:123], 0
ds_read_b128 v[176:179], v192 offset:12288
v_mfma_f32_16x16x32_bf16 a[248:251], v[180:183], v[124:127], a[248:251]
ds_read_b128 v[180:183], v193 offset:12288
v_mfma_f32_16x16x32_bf16 a[228:231], v[184:187], v[96:99], 0
v_mfma_f32_16x16x32_bf16 a[228:231], v[188:191], v[100:103], a[228:231]
s_mov_b32 s37, s36
v_mfma_f32_16x16x32_bf16 a[236:239], v[184:187], v[104:107], 0
s_add_u32 s35, s35, s45
v_mfma_f32_16x16x32_bf16 a[236:239], v[188:191], v[108:111], a[236:239]
s_add_u32 s36, s36, s34
v_mfma_f32_16x16x32_bf16 a[244:247], v[184:187], v[112:115], 0
v_mfma_f32_16x16x32_bf16 a[244:247], v[188:191], v[116:119], a[244:247]
s_waitcnt lgkmcnt(0)
v_mfma_f32_16x16x32_bf16 a[252:255], v[184:187], v[120:123], 0
ds_read_b128 v[184:187], v192 offset:14336
v_mfma_f32_16x16x32_bf16 a[252:255], v[188:191], v[124:127], a[252:255]
ds_read_b128 v[188:191], v193 offset:14336
s_nop 0
s_nop 0
s_nop 0
s_nop 0
s_nop 0
s_nop 0
s_nop 0
s_nop 0
.Lp1_vloop:
v_mfma_f32_16x16x32_bf16 a[0:3], v[128:131], v[64:67], a[0:3]
s_mov_b32 m0, s37
ds_read_b64_tr_b16 v[96:97], v198 offset:0
v_mfma_f32_16x16x32_bf16 a[0:3], v[132:135], v[68:71], a[0:3]
buffer_load_dwordx4 v16, s[8:11], s42 offen offset:0 nt lds
v_mfma_f32_16x16x32_bf16 a[8:11], v[128:131], v[72:75], a[8:11]
ds_read_b64_tr_b16 v[98:99], v198 offset:1024
v_mfma_f32_16x16x32_bf16 a[8:11], v[132:135], v[76:79], a[8:11]
v_mfma_f32_16x16x32_bf16 a[16:19], v[128:131], v[80:83], a[16:19]
ds_read_b64_tr_b16 v[100:101], v198 offset:8192
v_mfma_f32_16x16x32_bf16 a[16:19], v[132:135], v[84:87], a[16:19]
buffer_load_dwordx4 v17, s[8:11], s42 offen offset:1024 nt lds
v_mfma_f32_16x16x32_bf16 a[24:27], v[128:131], v[88:91], a[24:27]
ds_read_b64_tr_b16 v[102:103], v198 offset:9216
v_mfma_f32_16x16x32_bf16 a[24:27], v[132:135], v[92:95], a[24:27]
v_mfma_f32_16x16x32_bf16 a[4:7], v[136:139], v[64:67], a[4:7]
ds_read_b64_tr_b16 v[104:105], v199 offset:0
v_mfma_f32_16x16x32_bf16 a[4:7], v[140:143], v[68:71], a[4:7]
buffer_load_dwordx4 v18, s[8:11], s42 offen offset:2048 nt lds
v_mfma_f32_16x16x32_bf16 a[12:15], v[136:139], v[72:75], a[12:15]
ds_read_b64_tr_b16 v[106:107], v199 offset:1024
v_mfma_f32_16x16x32_bf16 a[12:15], v[140:143], v[76:79], a[12:15]
v_mfma_f32_16x16x32_bf16 a[20:23], v[136:139], v[80:83], a[20:23]
ds_read_b64_tr_b16 v[108:109], v199 offset:8192
v_mfma_f32_16x16x32_bf16 a[20:23], v[140:143], v[84:87], a[20:23]
buffer_load_dwordx4 v19, s[8:11], s42 offen offset:3072 nt lds
v_mfma_f32_16x16x32_bf16 a[28:31], v[136:139], v[88:91], a[28:31]
ds_read_b64_tr_b16 v[110:111], v199 offset:9216
v_mfma_f32_16x16x32_bf16 a[28:31], v[140:143], v[92:95], a[28:31]
v_mfma_f32_16x16x32_bf16 a[64:67], v[144:147], v[64:67], a[64:67]
s_add_u32 m0, s37, 4096
ds_read_b64_tr_b16 v[112:113], v200 offset:0
v_mfma_f32_16x16x32_bf16 a[64:67], v[148:151], v[68:71], a[64:67]
buffer_load_dwordx4 v16, s[8:11], s43 offen offset:0 nt lds
v_mfma_f32_16x16x32_bf16 a[72:75], v[144:147], v[72:75], a[72:75]
ds_read_b64_tr_b16 v[114:115], v200 offset:1024
v_mfma_f32_16x16x32_bf16 a[72:75], v[148:151], v[76:79], a[72:75]
v_mfma_f32_16x16x32_bf16 a[80:83], v[144:147], v[80:83], a[80:83]
ds_read_b64_tr_b16 v[116:117], v200 offset:8192
v_mfma_f32_16x16x32_bf16 a[80:83], v[148:151], v[84:87], a[80:83]
buffer_load_dwordx4 v17, s[8:11], s43 offen offset:1024 nt lds
v_mfma_f32_16x16x32_bf16 a[88:91], v[144:147], v[88:91], a[88:91]
ds_read_b64_tr_b16 v[118:119], v200 offset:9216
v_mfma_f32_16x16x32_bf16 a[88:91], v[148:151], v[92:95], a[88:91]
v_mfma_f32_16x16x32_bf16 a[68:71], v[152:155], v[64:67], a[68:71]
ds_read_b64_tr_b16 v[120:121], v201 offset:0
v_mfma_f32_16x16x32_bf16 a[68:71], v[156:159], v[68:71], a[68:71]
buffer_load_dwordx4 v18, s[8:11], s43 offen offset:2048 nt lds
v_mfma_f32_16x16x32_bf16 a[76:79], v[152:155], v[72:75], a[76:79]
ds_read_b64_tr_b16 v[122:123], v201 offset:1024
v_mfma_f32_16x16x32_bf16 a[76:79], v[156:159], v[76:79], a[76:79]
v_mfma_f32_16x16x32_bf16 a[84:87], v[152:155], v[80:83], a[84:87]
ds_read_b64_tr_b16 v[124:125], v201 offset:8192
v_mfma_f32_16x16x32_bf16 a[84:87], v[156:159], v[84:87], a[84:87]
buffer_load_dwordx4 v19, s[8:11], s43 offen offset:3072 nt lds
v_mfma_f32_16x16x32_bf16 a[92:95], v[152:155], v[88:91], a[92:95]
ds_read_b64_tr_b16 v[126:127], v201 offset:9216
v_mfma_f32_16x16x32_bf16 a[92:95], v[156:159], v[92:95], a[92:95]
v_mfma_f32_16x16x32_bf16 a[128:131], v[160:163], v[64:67], a[128:131]
v_mfma_f32_16x16x32_bf16 a[128:131], v[164:167], v[68:71], a[128:131]
v_mfma_f32_16x16x32_bf16 a[136:139], v[160:163], v[72:75], a[136:139]
v_mfma_f32_16x16x32_bf16 a[136:139], v[164:167], v[76:79], a[136:139]
v_mfma_f32_16x16x32_bf16 a[144:147], v[160:163], v[80:83], a[144:147]
v_mfma_f32_16x16x32_bf16 a[144:147], v[164:167], v[84:87], a[144:147]
v_mfma_f32_16x16x32_bf16 a[152:155], v[160:163], v[88:91], a[152:155]
v_mfma_f32_16x16x32_bf16 a[152:155], v[164:167], v[92:95], a[152:155]
v_mfma_f32_16x16x32_bf16 a[132:135], v[168:171], v[64:67], a[132:135]
s_add_u32 s42, s42, 524288
v_mfma_f32_16x16x32_bf16 a[132:135], v[172:175], v[68:71], a[132:135]
s_add_u32 s43, s43, 524288
v_mfma_f32_16x16x32_bf16 a[140:143], v[168:171], v[72:75], a[140:143]
v_mfma_f32_16x16x32_bf16 a[140:143], v[172:175], v[76:79], a[140:143]
s_waitcnt lgkmcnt(15)
v_mfma_f32_16x16x32_bf16 a[148:151], v[168:171], v[80:83], a[148:151]
s_add_u32 s44, s35, 65536
v_mfma_f32_16x16x32_bf16 a[148:151], v[172:175], v[84:87], a[148:151]
s_cmp_ge_u32 s44, s39
s_cselect_b32 s45, s55, s54
v_mfma_f32_16x16x32_bf16 a[156:159], v[168:171], v[88:91], a[156:159]
v_mfma_f32_16x16x32_bf16 a[156:159], v[172:175], v[92:95], a[156:159]
s_add_u32 s44, s36, 65536
v_mfma_f32_16x16x32_bf16 a[192:195], v[176:179], v[64:67], a[192:195]
v_mfma_f32_16x16x32_bf16 a[192:195], v[180:183], v[68:71], a[192:195]
s_cmp_ge_u32 s44, s39
s_cselect_b32 s34, s55, s54
v_mfma_f32_16x16x32_bf16 a[200:203], v[176:179], v[72:75], a[200:203]
v_add_u32 v194, s45, v194
v_mfma_f32_16x16x32_bf16 a[200:203], v[180:183], v[76:79], a[200:203]
v_add_u32 v195, s45, v195
v_mfma_f32_16x16x32_bf16 a[208:211], v[176:179], v[80:83], a[208:211]
v_add_u32 v196, s45, v196
v_mfma_f32_16x16x32_bf16 a[208:211], v[180:183], v[84:87], a[208:211]
v_add_u32 v197, s45, v197
v_mfma_f32_16x16x32_bf16 a[216:219], v[176:179], v[88:91], a[216:219]
v_add_u32 v198, s45, v198
v_mfma_f32_16x16x32_bf16 a[216:219], v[180:183], v[92:95], a[216:219]
v_add_u32 v199, s45, v199
v_mfma_f32_16x16x32_bf16 a[196:199], v[184:187], v[64:67], a[196:199]
v_add_u32 v200, s45, v200
v_mfma_f32_16x16x32_bf16 a[196:199], v[188:191], v[68:71], a[196:199]
.Lpad3:
s_cbranch_execz .Ltch0
v_add_u32 v201, s45, v201
v_mfma_f32_16x16x32_bf16 a[204:207], v[184:187], v[72:75], a[204:207]
v_add_u32 v192, s34, v192
v_mfma_f32_16x16x32_bf16 a[204:207], v[188:191], v[76:79], a[204:207]
v_add_u32 v193, s34, v193
v_mfma_f32_16x16x32_bf16 a[212:215], v[184:187], v[80:83], a[212:215]
v_mfma_f32_16x16x32_bf16 a[212:215], v[188:191], v[84:87], a[212:215]
v_mfma_f32_16x16x32_bf16 a[220:223], v[184:187], v[88:91], a[220:223]
v_mfma_f32_16x16x32_bf16 a[220:223], v[188:191], v[92:95], a[220:223]
s_waitcnt vmcnt(10) lgkmcnt(0)
s_barrier
v_mfma_f32_16x16x32_bf16 a[32:35], v[128:131], v[96:99], a[32:35]
s_mov_b32 m0, s35
ds_read_b64_tr_b16 v[64:65], v194 offset:0
v_mfma_f32_16x16x32_bf16 a[32:35], v[132:135], v[100:103], a[32:35]
buffer_load_dwordx4 v202, s[4:7], s40 offen offset:0 lds
v_mfma_f32_16x16x32_bf16 a[40:43], v[128:131], v[104:107], a[40:43]
ds_read_b64_tr_b16 v[66:67], v194 offset:1024
v_mfma_f32_16x16x32_bf16 a[40:43], v[132:135], v[108:111], a[40:43]
v_mfma_f32_16x16x32_bf16 a[48:51], v[128:131], v[112:115], a[48:51]
ds_read_b64_tr_b16 v[68:69], v194 offset:8192
v_mfma_f32_16x16x32_bf16 a[48:51], v[132:135], v[116:119], a[48:51]
buffer_load_dwordx4 v203, s[4:7], s40 offen offset:1024 lds
v_mfma_f32_16x16x32_bf16 a[56:59], v[128:131], v[120:123], a[56:59]
ds_read_b64_tr_b16 v[70:71], v194 offset:9216
v_mfma_f32_16x16x32_bf16 a[56:59], v[132:135], v[124:127], a[56:59]
ds_read_b128 v[128:131], v192 offset:0
ds_read_b128 v[132:135], v193 offset:0
v_mfma_f32_16x16x32_bf16 a[36:39], v[136:139], v[96:99], a[36:39]
ds_read_b64_tr_b16 v[72:73], v195 offset:0
v_mfma_f32_16x16x32_bf16 a[36:39], v[140:143], v[100:103], a[36:39]
buffer_load_dwordx4 v204, s[4:7], s40 offen offset:2048 lds
v_mfma_f32_16x16x32_bf16 a[44:47], v[136:139], v[104:107], a[44:47]
ds_read_b64_tr_b16 v[74:75], v195 offset:1024
v_mfma_f32_16x16x32_bf16 a[44:47], v[140:143], v[108:111], a[44:47]
v_mfma_f32_16x16x32_bf16 a[52:55], v[136:139], v[112:115], a[52:55]
ds_read_b64_tr_b16 v[76:77], v195 offset:8192
v_mfma_f32_16x16x32_bf16 a[52:55], v[140:143], v[116:119], a[52:55]
buffer_load_dwordx4 v205, s[4:7], s40 offen offset:3072 lds
v_mfma_f32_16x16x32_bf16 a[60:63], v[136:139], v[120:123], a[60:63]
ds_read_b64_tr_b16 v[78:79], v195 offset:9216
v_mfma_f32_16x16x32_bf16 a[60:63], v[140:143], v[124:127], a[60:63]
ds_read_b128 v[136:139], v192 offset:2048
ds_read_b128 v[140:143], v193 offset:2048
v_mfma_f32_16x16x32_bf16 a[96:99], v[144:147], v[96:99], a[96:99]
s_add_u32 m0, s35, 4096
ds_read_b64_tr_b16 v[80:81], v196 offset:0
v_mfma_f32_16x16x32_bf16 a[96:99], v[148:151], v[100:103], a[96:99]
buffer_load_dwordx4 v202, s[4:7], s41 offen offset:0 lds
v_mfma_f32_16x16x32_bf16 a[104:107], v[144:147], v[104:107], a[104:107]
ds_read_b64_tr_b16 v[82:83], v196 offset:1024
v_mfma_f32_16x16x32_bf16 a[104:107], v[148:151], v[108:111], a[104:107]
v_mfma_f32_16x16x32_bf16 a[112:115], v[144:147], v[112:115], a[112:115]
ds_read_b64_tr_b16 v[84:85], v196 offset:8192
v_mfma_f32_16x16x32_bf16 a[112:115], v[148:151], v[116:119], a[112:115]
buffer_load_dwordx4 v203, s[4:7], s41 offen offset:1024 lds
v_mfma_f32_16x16x32_bf16 a[120:123], v[144:147], v[120:123], a[120:123]
ds_read_b64_tr_b16 v[86:87], v196 offset:9216
v_mfma_f32_16x16x32_bf16 a[120:123], v[148:151], v[124:127], a[120:123]
ds_read_b128 v[144:147], v192 offset:4096
ds_read_b128 v[148:151], v193 offset:4096
v_mfma_f32_16x16x32_bf16 a[100:103], v[152:155], v[96:99], a[100:103]
ds_read_b64_tr_b16 v[88:89], v197 offset:0
v_mfma_f32_16x16x32_bf16 a[100:103], v[156:159], v[100:103], a[100:103]
buffer_load_dwordx4 v204, s[4:7], s41 offen offset:2048 lds
v_mfma_f32_16x16x32_bf16 a[108:111], v[152:155], v[104:107], a[108:111]
ds_read_b64_tr_b16 v[90:91], v197 offset:1024
v_mfma_f32_16x16x32_bf16 a[108:111], v[156:159], v[108:111], a[108:111]
v_mfma_f32_16x16x32_bf16 a[116:119], v[152:155], v[112:115], a[116:119]
ds_read_b64_tr_b16 v[92:93], v197 offset:8192
v_mfma_f32_16x16x32_bf16 a[116:119], v[156:159], v[116:119], a[116:119]
buffer_load_dwordx4 v205, s[4:7], s41 offen offset:3072 lds
v_mfma_f32_16x16x32_bf16 a[124:127], v[152:155], v[120:123], a[124:127]
ds_read_b64_tr_b16 v[94:95], v197 offset:9216
v_mfma_f32_16x16x32_bf16 a[124:127], v[156:159], v[124:127], a[124:127]
ds_read_b128 v[152:155], v192 offset:6144
ds_read_b128 v[156:159], v193 offset:6144
v_mfma_f32_16x16x32_bf16 a[160:163], v[160:163], v[96:99], a[160:163]
v_mfma_f32_16x16x32_bf16 a[160:163], v[164:167], v[100:103], a[160:163]
buffer_load_dword v35, v206, s[56:59], s48 offen
v_mfma_f32_16x16x32_bf16 a[168:171], v[160:163], v[104:107], a[168:171]
v_mfma_f32_16x16x32_bf16 a[168:171], v[164:167], v[108:111], a[168:171]
v_mfma_f32_16x16x32_bf16 a[176:179], v[160:163], v[112:115], a[176:179]
v_mfma_f32_16x16x32_bf16 a[176:179], v[164:167], v[116:119], a[176:179]
buffer_load_dword v35, v207, s[12:15], s49 offen
v_mfma_f32_16x16x32_bf16 a[184:187], v[160:163], v[120:123], a[184:187]
ds_read_b128 v[160:163], v192 offset:8192
v_mfma_f32_16x16x32_bf16 a[184:187], v[164:167], v[124:127], a[184:187]
ds_read_b128 v[164:167], v193 offset:8192
v_mfma_f32_16x16x32_bf16 a[164:167], v[168:171], v[96:99], a[164:167]
s_add_u32 s40, s40, 128
v_mfma_f32_16x16x32_bf16 a[164:167], v[172:175], v[100:103], a[164:167]
s_add_u32 s41, s41, 128
v_mfma_f32_16x16x32_bf16 a[172:175], v[168:171], v[104:107], a[172:175]
v_mfma_f32_16x16x32_bf16 a[172:175], v[172:175], v[108:111], a[172:175]
s_add_u32 s48, s48, 0x100000
v_mfma_f32_16x16x32_bf16 a[180:183], v[168:171], v[112:115], a[180:183]
v_mfma_f32_16x16x32_bf16 a[180:183], v[172:175], v[116:119], a[180:183]
s_add_u32 s49, s49, 655360
v_mfma_f32_16x16x32_bf16 a[188:191], v[168:171], v[120:123], a[188:191]
ds_read_b128 v[168:171], v192 offset:10240
v_mfma_f32_16x16x32_bf16 a[188:191], v[172:175], v[124:127], a[188:191]
ds_read_b128 v[172:175], v193 offset:10240
v_mfma_f32_16x16x32_bf16 a[224:227], v[176:179], v[96:99], a[224:227]
v_mfma_f32_16x16x32_bf16 a[224:227], v[180:183], v[100:103], a[224:227]
v_mfma_f32_16x16x32_bf16 a[232:235], v[176:179], v[104:107], a[232:235]
v_mfma_f32_16x16x32_bf16 a[232:235], v[180:183], v[108:111], a[232:235]
v_mfma_f32_16x16x32_bf16 a[240:243], v[176:179], v[112:115], a[240:243]
v_mfma_f32_16x16x32_bf16 a[240:243], v[180:183], v[116:119], a[240:243]
v_mfma_f32_16x16x32_bf16 a[248:251], v[176:179], v[120:123], a[248:251]
ds_read_b128 v[176:179], v192 offset:12288
v_mfma_f32_16x16x32_bf16 a[248:251], v[180:183], v[124:127], a[248:251]
ds_read_b128 v[180:183], v193 offset:12288
v_mfma_f32_16x16x32_bf16 a[228:231], v[184:187], v[96:99], a[228:231]
v_mfma_f32_16x16x32_bf16 a[228:231], v[188:191], v[100:103], a[228:231]
s_mov_b32 s37, s36
v_mfma_f32_16x16x32_bf16 a[236:239], v[184:187], v[104:107], a[236:239]
s_add_u32 s35, s35, s45
v_mfma_f32_16x16x32_bf16 a[236:239], v[188:191], v[108:111], a[236:239]
s_add_u32 s36, s36, s34
v_mfma_f32_16x16x32_bf16 a[244:247], v[184:187], v[112:115], a[244:247]
s_add_u32 s38, s38, 1
v_mfma_f32_16x16x32_bf16 a[244:247], v[188:191], v[116:119], a[244:247]
s_cmp_lt_u32 s38, 6
s_waitcnt lgkmcnt(0)
v_mfma_f32_16x16x32_bf16 a[252:255], v[184:187], v[120:123], a[252:255]
ds_read_b128 v[184:187], v192 offset:14336
v_mfma_f32_16x16x32_bf16 a[252:255], v[188:191], v[124:127], a[252:255]
ds_read_b128 v[188:191], v193 offset:14336
s_cbranch_scc1 .Lp1_vloop
v_mfma_f32_16x16x32_bf16 a[0:3], v[128:131], v[64:67], a[0:3]
s_add_u32 m0, s92, 32768
ds_read_b64_tr_b16 v[96:97], v198 offset:0
v_mfma_f32_16x16x32_bf16 a[0:3], v[132:135], v[68:71], a[0:3]
buffer_load_dwordx4 v20, s[12:15], s24 offen offset:0 lds
v_mfma_f32_16x16x32_bf16 a[8:11], v[128:131], v[72:75], a[8:11]
ds_read_b64_tr_b16 v[98:99], v198 offset:1024
v_mfma_f32_16x16x32_bf16 a[8:11], v[132:135], v[76:79], a[8:11]
v_mfma_f32_16x16x32_bf16 a[16:19], v[128:131], v[80:83], a[16:19]
ds_read_b64_tr_b16 v[100:101], v198 offset:8192
v_mfma_f32_16x16x32_bf16 a[16:19], v[132:135], v[84:87], a[16:19]
buffer_load_dwordx4 v21, s[12:15], s24 offen offset:1024 lds
v_mfma_f32_16x16x32_bf16 a[24:27], v[128:131], v[88:91], a[24:27]
ds_read_b64_tr_b16 v[102:103], v198 offset:9216
v_mfma_f32_16x16x32_bf16 a[24:27], v[132:135], v[92:95], a[24:27]
v_mfma_f32_16x16x32_bf16 a[4:7], v[136:139], v[64:67], a[4:7]
ds_read_b64_tr_b16 v[104:105], v199 offset:0
v_mfma_f32_16x16x32_bf16 a[4:7], v[140:143], v[68:71], a[4:7]
buffer_load_dwordx4 v22, s[12:15], s24 offen offset:2048 lds
v_mfma_f32_16x16x32_bf16 a[12:15], v[136:139], v[72:75], a[12:15]
ds_read_b64_tr_b16 v[106:107], v199 offset:1024
v_mfma_f32_16x16x32_bf16 a[12:15], v[140:143], v[76:79], a[12:15]
v_mfma_f32_16x16x32_bf16 a[20:23], v[136:139], v[80:83], a[20:23]
ds_read_b64_tr_b16 v[108:109], v199 offset:8192
v_mfma_f32_16x16x32_bf16 a[20:23], v[140:143], v[84:87], a[20:23]
buffer_load_dwordx4 v23, s[12:15], s24 offen offset:3072 lds
v_mfma_f32_16x16x32_bf16 a[28:31], v[136:139], v[88:91], a[28:31]
ds_read_b64_tr_b16 v[110:111], v199 offset:9216
v_mfma_f32_16x16x32_bf16 a[28:31], v[140:143], v[92:95], a[28:31]
v_mfma_f32_16x16x32_bf16 a[64:67], v[144:147], v[64:67], a[64:67]
s_add_u32 m0, s92, 36864
ds_read_b64_tr_b16 v[112:113], v200 offset:0
v_mfma_f32_16x16x32_bf16 a[64:67], v[148:151], v[68:71], a[64:67]
buffer_load_dwordx4 v20, s[12:15], s25 offen offset:0 lds
v_mfma_f32_16x16x32_bf16 a[72:75], v[144:147], v[72:75], a[72:75]
ds_read_b64_tr_b16 v[114:115], v200 offset:1024
v_mfma_f32_16x16x32_bf16 a[72:75], v[148:151], v[76:79], a[72:75]
v_mfma_f32_16x16x32_bf16 a[80:83], v[144:147], v[80:83], a[80:83]
ds_read_b64_tr_b16 v[116:117], v200 offset:8192
v_mfma_f32_16x16x32_bf16 a[80:83], v[148:151], v[84:87], a[80:83]
buffer_load_dwordx4 v21, s[12:15], s25 offen offset:1024 lds
v_mfma_f32_16x16x32_bf16 a[88:91], v[144:147], v[88:91], a[88:91]
ds_read_b64_tr_b16 v[118:119], v200 offset:9216
v_mfma_f32_16x16x32_bf16 a[88:91], v[148:151], v[92:95], a[88:91]
v_mfma_f32_16x16x32_bf16 a[68:71], v[152:155], v[64:67], a[68:71]
ds_read_b64_tr_b16 v[120:121], v201 offset:0
v_mfma_f32_16x16x32_bf16 a[68:71], v[156:159], v[68:71], a[68:71]
buffer_load_dwordx4 v22, s[12:15], s25 offen offset:2048 lds
v_mfma_f32_16x16x32_bf16 a[76:79], v[152:155], v[72:75], a[76:79]
ds_read_b64_tr_b16 v[122:123], v201 offset:1024
v_mfma_f32_16x16x32_bf16 a[76:79], v[156:159], v[76:79], a[76:79]
v_mfma_f32_16x16x32_bf16 a[84:87], v[152:155], v[80:83], a[84:87]
ds_read_b64_tr_b16 v[124:125], v201 offset:8192
v_mfma_f32_16x16x32_bf16 a[84:87], v[156:159], v[84:87], a[84:87]
buffer_load_dwordx4 v23, s[12:15], s25 offen offset:3072 lds
v_mfma_f32_16x16x32_bf16 a[92:95], v[152:155], v[88:91], a[92:95]
ds_read_b64_tr_b16 v[126:127], v201 offset:9216
v_mfma_f32_16x16x32_bf16 a[92:95], v[156:159], v[92:95], a[92:95]
v_mfma_f32_16x16x32_bf16 a[128:131], v[160:163], v[64:67], a[128:131]
v_mfma_f32_16x16x32_bf16 a[128:131], v[164:167], v[68:71], a[128:131]
v_mfma_f32_16x16x32_bf16 a[136:139], v[160:163], v[72:75], a[136:139]
v_mfma_f32_16x16x32_bf16 a[136:139], v[164:167], v[76:79], a[136:139]
v_mfma_f32_16x16x32_bf16 a[144:147], v[160:163], v[80:83], a[144:147]
v_mfma_f32_16x16x32_bf16 a[144:147], v[164:167], v[84:87], a[144:147]
v_mfma_f32_16x16x32_bf16 a[152:155], v[160:163], v[88:91], a[152:155]
v_mfma_f32_16x16x32_bf16 a[152:155], v[164:167], v[92:95], a[152:155]
v_mfma_f32_16x16x32_bf16 a[132:135], v[168:171], v[64:67], a[132:135]
s_add_u32 s24, s24, 128
s_add_u32 s25, s25, 128
v_mfma_f32_16x16x32_bf16 a[132:135], v[172:175], v[68:71], a[132:135]
v_mfma_f32_16x16x32_bf16 a[140:143], v[168:171], v[72:75], a[140:143]
v_mfma_f32_16x16x32_bf16 a[140:143], v[172:175], v[76:79], a[140:143]
s_waitcnt lgkmcnt(15)
v_mfma_f32_16x16x32_bf16 a[148:151], v[168:171], v[80:83], a[148:151]
s_add_u32 s44, s35, 65536
v_mfma_f32_16x16x32_bf16 a[148:151], v[172:175], v[84:87], a[148:151]
s_cmp_ge_u32 s44, s39
s_cselect_b32 s45, s55, s54
v_mfma_f32_16x16x32_bf16 a[156:159], v[168:171], v[88:91], a[156:159]
v_mfma_f32_16x16x32_bf16 a[156:159], v[172:175], v[92:95], a[156:159]
s_add_u32 s44, s36, 65536
v_mfma_f32_16x16x32_bf16 a[192:195], v[176:179], v[64:67], a[192:195]
v_mfma_f32_16x16x32_bf16 a[192:195], v[180:183], v[68:71], a[192:195]
s_cmp_ge_u32 s44, s39
s_cselect_b32 s34, s55, s54
v_mfma_f32_16x16x32_bf16 a[200:203], v[176:179], v[72:75], a[200:203]
v_add_u32 v194, s45, v194
v_mfma_f32_16x16x32_bf16 a[200:203], v[180:183], v[76:79], a[200:203]
v_add_u32 v195, s45, v195
v_mfma_f32_16x16x32_bf16 a[208:211], v[176:179], v[80:83], a[208:211]
v_add_u32 v196, s45, v196
v_mfma_f32_16x16x32_bf16 a[208:211], v[180:183], v[84:87], a[208:211]
v_add_u32 v197, s45, v197
v_mfma_f32_16x16x32_bf16 a[216:219], v[176:179], v[88:91], a[216:219]
v_add_u32 v198, s45, v198
v_mfma_f32_16x16x32_bf16 a[216:219], v[180:183], v[92:95], a[216:219]
v_add_u32 v199, s45, v199
v_mfma_f32_16x16x32_bf16 a[196:199], v[184:187], v[64:67], a[196:199]
v_add_u32 v200, s45, v200
v_mfma_f32_16x16x32_bf16 a[196:199], v[188:191], v[68:71], a[196:199]
v_add_u32 v201, s45, v201
v_mfma_f32_16x16x32_bf16 a[204:207], v[184:187], v[72:75], a[204:207]
v_add_u32 v192, s34, v192
v_mfma_f32_16x16x32_bf16 a[204:207], v[188:191], v[76:79], a[204:207]
v_add_u32 v193, s34, v193
v_mfma_f32_16x16x32_bf16 a[212:215], v[184:187], v[80:83], a[212:215]
v_mfma_f32_16x16x32_bf16 a[212:215], v[188:191], v[84:87], a[212:215]
v_mfma_f32_16x16x32_bf16 a[220:223], v[184:187], v[88:91], a[220:223]
v_mfma_f32_16x16x32_bf16 a[220:223], v[188:191], v[92:95], a[220:223]
s_waitcnt vmcnt(10) lgkmcnt(0)
s_barrier
v_mfma_f32_16x16x32_bf16 a[32:35], v[128:131], v[96:99], a[32:35]
s_add_u32 m0, s91, 65536
ds_read_b64_tr_b16 v[64:65], v194 offset:0
v_mfma_f32_16x16x32_bf16 a[32:35], v[132:135], v[100:103], a[32:35]
buffer_load_dwordx4 v16, s[16:19], s26 offen offset:0 lds
v_mfma_f32_16x16x32_bf16 a[40:43], v[128:131], v[104:107], a[40:43]
ds_read_b64_tr_b16 v[66:67], v194 offset:1024
v_mfma_f32_16x16x32_bf16 a[40:43], v[132:135], v[108:111], a[40:43]
v_mfma_f32_16x16x32_bf16 a[48:51], v[128:131], v[112:115], a[48:51]
ds_read_b64_tr_b16 v[68:69], v194 offset:8192
v_mfma_f32_16x16x32_bf16 a[48:51], v[132:135], v[116:119], a[48:51]
buffer_load_dwordx4 v17, s[16:19], s26 offen offset:1024 lds
v_mfma_f32_16x16x32_bf16 a[56:59], v[128:131], v[120:123], a[56:59]
ds_read_b64_tr_b16 v[70:71], v194 offset:9216
v_mfma_f32_16x16x32_bf16 a[56:59], v[132:135], v[124:127], a[56:59]
ds_read_b128 v[128:131], v192 offset:0
ds_read_b128 v[132:135], v193 offset:0
v_mfma_f32_16x16x32_bf16 a[36:39], v[136:139], v[96:99], a[36:39]
ds_read_b64_tr_b16 v[72:73], v195 offset:0
v_mfma_f32_16x16x32_bf16 a[36:39], v[140:143], v[100:103], a[36:39]
buffer_load_dwordx4 v18, s[16:19], s26 offen offset:2048 lds
v_mfma_f32_16x16x32_bf16 a[44:47], v[136:139], v[104:107], a[44:47]
ds_read_b64_tr_b16 v[74:75], v195 offset:1024
v_mfma_f32_16x16x32_bf16 a[44:47], v[140:143], v[108:111], a[44:47]
v_mfma_f32_16x16x32_bf16 a[52:55], v[136:139], v[112:115], a[52:55]
ds_read_b64_tr_b16 v[76:77], v195 offset:8192
v_mfma_f32_16x16x32_bf16 a[52:55], v[140:143], v[116:119], a[52:55]
buffer_load_dwordx4 v19, s[16:19], s26 offen offset:3072 lds
v_mfma_f32_16x16x32_bf16 a[60:63], v[136:139], v[120:123], a[60:63]
ds_read_b64_tr_b16 v[78:79], v195 offset:9216
v_mfma_f32_16x16x32_bf16 a[60:63], v[140:143], v[124:127], a[60:63]
s_add_u32 s26, s26, 524288
ds_read_b128 v[136:139], v192 offset:2048
ds_read_b128 v[140:143], v193 offset:2048
v_mfma_f32_16x16x32_bf16 a[96:99], v[144:147], v[96:99], a[96:99]
s_add_u32 m0, s91, 81920
ds_read_b64_tr_b16 v[80:81], v196 offset:0
v_mfma_f32_16x16x32_bf16 a[96:99], v[148:151], v[100:103], a[96:99]
buffer_load_dwordx4 v16, s[16:19], s26 offen offset:0 lds
v_mfma_f32_16x16x32_bf16 a[104:107], v[144:147], v[104:107], a[104:107]
ds_read_b64_tr_b16 v[82:83], v196 offset:1024
v_mfma_f32_16x16x32_bf16 a[104:107], v[148:151], v[108:111], a[104:107]
v_mfma_f32_16x16x32_bf16 a[112:115], v[144:147], v[112:115], a[112:115]
ds_read_b64_tr_b16 v[84:85], v196 offset:8192
v_mfma_f32_16x16x32_bf16 a[112:115], v[148:151], v[116:119], a[112:115]
buffer_load_dwordx4 v17, s[16:19], s26 offen offset:1024 lds
v_mfma_f32_16x16x32_bf16 a[120:123], v[144:147], v[120:123], a[120:123]
ds_read_b64_tr_b16 v[86:87], v196 offset:9216
v_mfma_f32_16x16x32_bf16 a[120:123], v[148:151], v[124:127], a[120:123]
ds_read_b128 v[144:147], v192 offset:4096
ds_read_b128 v[148:151], v193 offset:4096
v_mfma_f32_16x16x32_bf16 a[100:103], v[152:155], v[96:99], a[100:103]
ds_read_b64_tr_b16 v[88:89], v197 offset:0
v_mfma_f32_16x16x32_bf16 a[100:103], v[156:159], v[100:103], a[100:103]
buffer_load_dwordx4 v18, s[16:19], s26 offen offset:2048 lds
v_mfma_f32_16x16x32_bf16 a[108:111], v[152:155], v[104:107], a[108:111]
ds_read_b64_tr_b16 v[90:91], v197 offset:1024
v_mfma_f32_16x16x32_bf16 a[108:111], v[156:159], v[108:111], a[108:111]
v_mfma_f32_16x16x32_bf16 a[116:119], v[152:155], v[112:115], a[116:119]
ds_read_b64_tr_b16 v[92:93], v197 offset:8192
v_mfma_f32_16x16x32_bf16 a[116:119], v[156:159], v[116:119], a[116:119]
buffer_load_dwordx4 v19, s[16:19], s26 offen offset:3072 lds
v_mfma_f32_16x16x32_bf16 a[124:127], v[152:155], v[120:123], a[124:127]
ds_read_b64_tr_b16 v[94:95], v197 offset:9216
v_mfma_f32_16x16x32_bf16 a[124:127], v[156:159], v[124:127], a[124:127]
s_add_u32 s26, s26, 524288
ds_read_b128 v[152:155], v192 offset:6144
ds_read_b128 v[156:159], v193 offset:6144
v_mfma_f32_16x16x32_bf16 a[160:163], v[160:163], v[96:99], a[160:163]
v_mfma_f32_16x16x32_bf16 a[160:163], v[164:167], v[100:103], a[160:163]
buffer_load_dword v35, v206, s[56:59], s48 offen
v_mfma_f32_16x16x32_bf16 a[168:171], v[160:163], v[104:107], a[168:171]
v_mfma_f32_16x16x32_bf16 a[168:171], v[164:167], v[108:111], a[168:171]
v_mfma_f32_16x16x32_bf16 a[176:179], v[160:163], v[112:115], a[176:179]
v_mfma_f32_16x16x32_bf16 a[176:179], v[164:167], v[116:119], a[176:179]
buffer_load_dword v35, v207, s[12:15], s49 offen
v_add_u32 v11, 8, v11
v_and_b32 v11, -8, v11
v_mfma_f32_16x16x32_bf16 a[184:187], v[160:163], v[120:123], a[184:187]
ds_read_b128 v[160:163], v192 offset:8192
v_mfma_f32_16x16x32_bf16 a[184:187], v[164:167], v[124:127], a[184:187]
ds_read_b128 v[164:167], v193 offset:8192
v_mfma_f32_16x16x32_bf16 a[164:167], v[168:171], v[96:99], a[164:167]
v_mfma_f32_16x16x32_bf16 a[164:167], v[172:175], v[100:103], a[164:167]
s_nop 0
v_or_b32 v0, 0, v11
v_mfma_f32_16x16x32_bf16 a[172:175], v[168:171], v[104:107], a[172:175]
v_mfma_f32_16x16x32_bf16 a[172:175], v[172:175], v[108:111], a[172:175]
s_add_u32 s48, s48, 0x100000
v_mfma_f32_16x16x32_bf16 a[180:183], v[168:171], v[112:115], a[180:183]
v_mfma_f32_16x16x32_bf16 a[180:183], v[172:175], v[116:119], a[180:183]
s_add_u32 s49, s49, 655360
v_mfma_f32_16x16x32_bf16 a[188:191], v[168:171], v[120:123], a[188:191]
ds_read_b128 v[168:171], v192 offset:10240
v_mfma_f32_16x16x32_bf16 a[188:191], v[172:175], v[124:127], a[188:191]
buffer_store_dword v0, v6, s[60:63], 0 offen sc1
ds_read_b128 v[172:175], v193 offset:10240
v_mfma_f32_16x16x32_bf16 a[224:227], v[176:179], v[96:99], a[224:227]
v_mfma_f32_16x16x32_bf16 a[224:227], v[180:183], v[100:103], a[224:227]
v_mfma_f32_16x16x32_bf16 a[232:235], v[176:179], v[104:107], a[232:235]
v_mfma_f32_16x16x32_bf16 a[232:235], v[180:183], v[108:111], a[232:235]
buffer_load_dword v35, v4, s[96:99], 0 offen
v_mfma_f32_16x16x32_bf16 a[240:243], v[176:179], v[112:115], a[240:243]
v_mfma_f32_16x16x32_bf16 a[240:243], v[180:183], v[116:119], a[240:243]
v_mfma_f32_16x16x32_bf16 a[248:251], v[176:179], v[120:123], a[248:251]
ds_read_b128 v[176:179], v192 offset:12288
v_mfma_f32_16x16x32_bf16 a[248:251], v[180:183], v[124:127], a[248:251]
ds_read_b128 v[180:183], v193 offset:12288
v_mfma_f32_16x16x32_bf16 a[228:231], v[184:187], v[96:99], a[228:231]
v_mfma_f32_16x16x32_bf16 a[228:231], v[188:191], v[100:103], a[228:231]
s_mov_b32 s37, s36
v_mfma_f32_16x16x32_bf16 a[236:239], v[184:187], v[104:107], a[236:239]
s_add_u32 s35, s35, s45
v_mfma_f32_16x16x32_bf16 a[236:239], v[188:191], v[108:111], a[236:239]
s_add_u32 s36, s36, s34
v_mfma_f32_16x16x32_bf16 a[244:247], v[184:187], v[112:115], a[244:247]
v_mfma_f32_16x16x32_bf16 a[244:247], v[188:191], v[116:119], a[244:247]
s_waitcnt lgkmcnt(0)
v_mfma_f32_16x16x32_bf16 a[252:255], v[184:187], v[120:123], a[252:255]
ds_read_b128 v[184:187], v192 offset:14336
v_mfma_f32_16x16x32_bf16 a[252:255], v[188:191], v[124:127], a[252:255]
ds_read_b128 v[188:191], v193 offset:14336
v_mfma_f32_16x16x32_bf16 a[0:3], v[128:131], v[64:67], a[0:3]
s_add_u32 m0, s92, 98304
ds_read_b64_tr_b16 v[96:97], v198 offset:0
v_mfma_f32_16x16x32_bf16 a[0:3], v[132:135], v[68:71], a[0:3]
buffer_load_dwordx4 v20, s[12:15], s24 offen offset:0 lds
v_mfma_f32_16x16x32_bf16 a[8:11], v[128:131], v[72:75], a[8:11]
ds_read_b64_tr_b16 v[98:99], v198 offset:1024
v_mfma_f32_16x16x32_bf16 a[8:11], v[132:135], v[76:79], a[8:11]
v_mfma_f32_16x16x32_bf16 a[16:19], v[128:131], v[80:83], a[16:19]
ds_read_b64_tr_b16 v[100:101], v198 offset:8192
v_mfma_f32_16x16x32_bf16 a[16:19], v[132:135], v[84:87], a[16:19]
buffer_load_dwordx4 v21, s[12:15], s24 offen offset:1024 lds
v_mfma_f32_16x16x32_bf16 a[24:27], v[128:131], v[88:91], a[24:27]
ds_read_b64_tr_b16 v[102:103], v198 offset:9216
v_mfma_f32_16x16x32_bf16 a[24:27], v[132:135], v[92:95], a[24:27]
v_mfma_f32_16x16x32_bf16 a[4:7], v[136:139], v[64:67], a[4:7]
ds_read_b64_tr_b16 v[104:105], v199 offset:0
v_mfma_f32_16x16x32_bf16 a[4:7], v[140:143], v[68:71], a[4:7]
buffer_load_dwordx4 v22, s[12:15], s24 offen offset:2048 lds
v_mfma_f32_16x16x32_bf16 a[12:15], v[136:139], v[72:75], a[12:15]
ds_read_b64_tr_b16 v[106:107], v199 offset:1024
v_mfma_f32_16x16x32_bf16 a[12:15], v[140:143], v[76:79], a[12:15]
v_mfma_f32_16x16x32_bf16 a[20:23], v[136:139], v[80:83], a[20:23]
ds_read_b64_tr_b16 v[108:109], v199 offset:8192
v_mfma_f32_16x16x32_bf16 a[20:23], v[140:143], v[84:87], a[20:23]
buffer_load_dwordx4 v23, s[12:15], s24 offen offset:3072 lds
v_mfma_f32_16x16x32_bf16 a[28:31], v[136:139], v[88:91], a[28:31]
ds_read_b64_tr_b16 v[110:111], v199 offset:9216
v_mfma_f32_16x16x32_bf16 a[28:31], v[140:143], v[92:95], a[28:31]
v_mfma_f32_16x16x32_bf16 a[64:67], v[144:147], v[64:67], a[64:67]
s_add_u32 m0, s92, 102400
ds_read_b64_tr_b16 v[112:113], v200 offset:0
v_mfma_f32_16x16x32_bf16 a[64:67], v[148:151], v[68:71], a[64:67]
buffer_load_dwordx4 v20, s[12:15], s25 offen offset:0 lds
v_mfma_f32_16x16x32_bf16 a[72:75], v[144:147], v[72:75], a[72:75]
ds_read_b64_tr_b16 v[114:115], v200 offset:1024
v_mfma_f32_16x16x32_bf16 a[72:75], v[148:151], v[76:79], a[72:75]
v_mfma_f32_16x16x32_bf16 a[80:83], v[144:147], v[80:83], a[80:83]
ds_read_b64_tr_b16 v[116:117], v200 offset:8192
v_mfma_f32_16x16x32_bf16 a[80:83], v[148:151], v[84:87], a[80:83]
buffer_load_dwordx4 v21, s[12:15], s25 offen offset:1024 lds
v_mfma_f32_16x16x32_bf16 a[88:91], v[144:147], v[88:91], a[88:91]
ds_read_b64_tr_b16 v[118:119], v200 offset:9216
v_mfma_f32_16x16x32_bf16 a[88:91], v[148:151], v[92:95], a[88:91]
v_mfma_f32_16x16x32_bf16 a[68:71], v[152:155], v[64:67], a[68:71]
ds_read_b64_tr_b16 v[120:121], v201 offset:0
v_mfma_f32_16x16x32_bf16 a[68:71], v[156:159], v[68:71], a[68:71]
buffer_load_dwordx4 v22, s[12:15], s25 offen offset:2048 lds
v_mfma_f32_16x16x32_bf16 a[76:79], v[152:155], v[72:75], a[76:79]
ds_read_b64_tr_b16 v[122:123], v201 offset:1024
v_mfma_f32_16x16x32_bf16 a[76:79], v[156:159], v[76:79], a[76:79]
v_mfma_f32_16x16x32_bf16 a[84:87], v[152:155], v[80:83], a[84:87]
ds_read_b64_tr_b16 v[124:125], v201 offset:8192
v_mfma_f32_16x16x32_bf16 a[84:87], v[156:159], v[84:87], a[84:87]
buffer_load_dwordx4 v23, s[12:15], s25 offen offset:3072 lds
v_mfma_f32_16x16x32_bf16 a[92:95], v[152:155], v[88:91], a[92:95]
ds_read_b64_tr_b16 v[126:127], v201 offset:9216
v_mfma_f32_16x16x32_bf16 a[92:95], v[156:159], v[92:95], a[92:95]
v_mfma_f32_16x16x32_bf16 a[128:131], v[160:163], v[64:67], a[128:131]
v_mfma_f32_16x16x32_bf16 a[128:131], v[164:167], v[68:71], a[128:131]
v_mfma_f32_16x16x32_bf16 a[136:139], v[160:163], v[72:75], a[136:139]
v_mfma_f32_16x16x32_bf16 a[136:139], v[164:167], v[76:79], a[136:139]
v_mfma_f32_16x16x32_bf16 a[144:147], v[160:163], v[80:83], a[144:147]
v_mfma_f32_16x16x32_bf16 a[144:147], v[164:167], v[84:87], a[144:147]
v_mfma_f32_16x16x32_bf16 a[152:155], v[160:163], v[88:91], a[152:155]
v_mfma_f32_16x16x32_bf16 a[152:155], v[164:167], v[92:95], a[152:155]
v_mfma_f32_16x16x32_bf16 a[132:135], v[168:171], v[64:67], a[132:135]
s_add_u32 s24, s24, 128
s_add_u32 s25, s25, 128
v_mfma_f32_16x16x32_bf16 a[132:135], v[172:175], v[68:71], a[132:135]
v_mfma_f32_16x16x32_bf16 a[140:143], v[168:171], v[72:75], a[140:143]
v_mfma_f32_16x16x32_bf16 a[140:143], v[172:175], v[76:79], a[140:143]
s_waitcnt lgkmcnt(15)
v_mfma_f32_16x16x32_bf16 a[148:151], v[168:171], v[80:83], a[148:151]
v_mfma_f32_16x16x32_bf16 a[148:151], v[172:175], v[84:87], a[148:151]
v_mfma_f32_16x16x32_bf16 a[156:159], v[168:171], v[88:91], a[156:159]
v_mfma_f32_16x16x32_bf16 a[156:159], v[172:175], v[92:95], a[156:159]
v_mfma_f32_16x16x32_bf16 a[192:195], v[176:179], v[64:67], a[192:195]
v_mfma_f32_16x16x32_bf16 a[192:195], v[180:183], v[68:71], a[192:195]
v_mfma_f32_16x16x32_bf16 a[200:203], v[176:179], v[72:75], a[200:203]
v_mfma_f32_16x16x32_bf16 a[200:203], v[180:183], v[76:79], a[200:203]
v_mfma_f32_16x16x32_bf16 a[208:211], v[176:179], v[80:83], a[208:211]
v_mfma_f32_16x16x32_bf16 a[208:211], v[180:183], v[84:87], a[208:211]
v_mfma_f32_16x16x32_bf16 a[216:219], v[176:179], v[88:91], a[216:219]
v_mfma_f32_16x16x32_bf16 a[216:219], v[180:183], v[92:95], a[216:219]
v_mfma_f32_16x16x32_bf16 a[196:199], v[184:187], v[64:67], a[196:199]
v_mfma_f32_16x16x32_bf16 a[196:199], v[188:191], v[68:71], a[196:199]
v_mfma_f32_16x16x32_bf16 a[204:207], v[184:187], v[72:75], a[204:207]
v_mfma_f32_16x16x32_bf16 a[204:207], v[188:191], v[76:79], a[204:207]
v_mfma_f32_16x16x32_bf16 a[212:215], v[184:187], v[80:83], a[212:215]
v_mfma_f32_16x16x32_bf16 a[212:215], v[188:191], v[84:87], a[212:215]
v_mfma_f32_16x16x32_bf16 a[220:223], v[184:187], v[88:91], a[220:223]
v_mfma_f32_16x16x32_bf16 a[220:223], v[188:191], v[92:95], a[220:223]
s_waitcnt vmcnt(16) lgkmcnt(0)
s_barrier
v_mfma_f32_16x16x32_bf16 a[32:35], v[128:131], v[96:99], a[32:35]
s_add_u32 m0, s91, 0
ds_read_b64_tr_b16 v[64:65], v26 offset:0
v_mfma_f32_16x16x32_bf16 a[32:35], v[132:135], v[100:103], a[32:35]
buffer_load_dwordx4 v16, s[16:19], s26 offen offset:0 lds
v_mfma_f32_16x16x32_bf16 a[40:43], v[128:131], v[104:107], a[40:43]
ds_read_b64_tr_b16 v[66:67], v26 offset:1024
v_accvgpr_read_b32 v36, a0
v_mfma_f32_16x16x32_bf16 a[40:43], v[132:135], v[108:111], a[40:43]
v_accvgpr_read_b32 v37, a1
v_mul_f32 v36, s47, v36
v_mfma_f32_16x16x32_bf16 a[48:51], v[128:131], v[112:115], a[48:51]
ds_read_b64_tr_b16 v[68:69], v26 offset:8192
v_mul_f32 v37, s47, v37
v_mfma_f32_16x16x32_bf16 a[48:51], v[132:135], v[116:119], a[48:51]
buffer_load_dwordx4 v17, s[16:19], s26 offen offset:1024 lds
v_cvt_pk_bf16_f32 v40, v36, v37
v_mfma_f32_16x16x32_bf16 a[56:59], v[128:131], v[120:123], a[56:59]
ds_read_b64_tr_b16 v[70:71], v26 offset:9216
v_accvgpr_read_b32 v38, a2
v_mfma_f32_16x16x32_bf16 a[56:59], v[132:135], v[124:127], a[56:59]
v_accvgpr_read_b32 v39, a3
v_mul_f32 v38, s47, v38
v_mfma_f32_16x16x32_bf16 a[36:39], v[136:139], v[96:99], a[36:39]
ds_read_b64_tr_b16 v[72:73], v27 offset:0
v_mul_f32 v39, s47, v39
v_mfma_f32_16x16x32_bf16 a[36:39], v[140:143], v[100:103], a[36:39]
buffer_load_dwordx4 v18, s[16:19], s26 offen offset:2048 lds
v_cvt_pk_bf16_f32 v41, v38, v39
v_mfma_f32_16x16x32_bf16 a[44:47], v[136:139], v[104:107], a[44:47]
ds_read_b64_tr_b16 v[74:75], v27 offset:1024
v_accvgpr_read_b32 v36, a4
v_mfma_f32_16x16x32_bf16 a[44:47], v[140:143], v[108:111], a[44:47]
v_accvgpr_read_b32 v37, a5
v_mul_f32 v36, s47, v36
v_mfma_f32_16x16x32_bf16 a[52:55], v[136:139], v[112:115], a[52:55]
ds_read_b64_tr_b16 v[76:77], v27 offset:8192
v_mul_f32 v37, s47, v37
v_mfma_f32_16x16x32_bf16 a[52:55], v[140:143], v[116:119], a[52:55]
buffer_load_dwordx4 v19, s[16:19], s26 offen offset:3072 lds
v_cvt_pk_bf16_f32 v42, v36, v37
v_mfma_f32_16x16x32_bf16 a[60:63], v[136:139], v[120:123], a[60:63]
ds_read_b64_tr_b16 v[78:79], v27 offset:9216
v_accvgpr_read_b32 v38, a6
v_mfma_f32_16x16x32_bf16 a[60:63], v[140:143], v[124:127], a[60:63]
s_add_u32 s26, s26, 524288
v_accvgpr_read_b32 v39, a7
v_mul_f32 v38, s47, v38
v_mfma_f32_16x16x32_bf16 a[96:99], v[144:147], v[96:99], a[96:99]
s_add_u32 m0, s92, 131072
ds_read_b64_tr_b16 v[80:81], v28 offset:0
v_mfma_f32_16x16x32_bf16 a[96:99], v[148:151], v[100:103], a[96:99]
buffer_load_dwordx4 v20, s[12:15], s24 offen offset:0 lds
v_mul_f32 v39, s47, v39
v_mfma_f32_16x16x32_bf16 a[104:107], v[144:147], v[104:107], a[104:107]
ds_read_b64_tr_b16 v[82:83], v28 offset:1024
v_cvt_pk_bf16_f32 v43, v38, v39
v_mfma_f32_16x16x32_bf16 a[104:107], v[148:151], v[108:111], a[104:107]
v_accvgpr_read_b32 v36, a8
v_accvgpr_read_b32 v37, a9
v_mfma_f32_16x16x32_bf16 a[112:115], v[144:147], v[112:115], a[112:115]
ds_read_b64_tr_b16 v[84:85], v28 offset:8192
v_mul_f32 v36, s47, v36
v_mfma_f32_16x16x32_bf16 a[112:115], v[148:151], v[116:119], a[112:115]
buffer_load_dwordx4 v21, s[12:15], s24 offen offset:1024 lds
v_mul_f32 v37, s47, v37
v_mfma_f32_16x16x32_bf16 a[120:123], v[144:147], v[120:123], a[120:123]
ds_read_b64_tr_b16 v[86:87], v28 offset:9216
v_cvt_pk_bf16_f32 v44, v36, v37
v_mfma_f32_16x16x32_bf16 a[120:123], v[148:151], v[124:127], a[120:123]
v_accvgpr_read_b32 v38, a10
v_accvgpr_read_b32 v39, a11
v_mfma_f32_16x16x32_bf16 a[100:103], v[152:155], v[96:99], a[100:103]
ds_read_b64_tr_b16 v[88:89], v29 offset:0
.Lpad4:
s_nop 0
v_mul_f32 v38, s47, v38
v_mfma_f32_16x16x32_bf16 a[100:103], v[156:159], v[100:103], a[100:103]
buffer_load_dwordx4 v22, s[12:15], s24 offen offset:2048 lds
v_mul_f32 v39, s47, v39
v_mfma_f32_16x16x32_bf16 a[108:111], v[152:155], v[104:107], a[108:111]
ds_read_b64_tr_b16 v[90:91], v29 offset:1024
v_cvt_pk_bf16_f32 v45, v38, v39
v_mfma_f32_16x16x32_bf16 a[108:111], v[156:159], v[108:111], a[108:111]
v_accvgpr_read_b32 v36, a12
v_accvgpr_read_b32 v37, a13
v_mfma_f32_16x16x32_bf16 a[116:119], v[152:155], v[112:115], a[116:119]
ds_read_b64_tr_b16 v[92:93], v29 offset:8192
v_mul_f32 v36, s47, v36
v_mfma_f32_16x16x32_bf16 a[116:119], v[156:159], v[116:119], a[116:119]
buffer_load_dwordx4 v23, s[12:15], s24 offen offset:3072 lds
v_mul_f32 v37, s47, v37
v_mfma_f32_16x16x32_bf16 a[124:127], v[152:155], v[120:123], a[124:127]
ds_read_b64_tr_b16 v[94:95], v29 offset:9216
v_cvt_pk_bf16_f32 v46, v36, v37
v_mfma_f32_16x16x32_bf16 a[124:127], v[156:159], v[124:127], a[124:127]
v_accvgpr_read_b32 v38, a14
v_accvgpr_read_b32 v39, a15
v_mfma_f32_16x16x32_bf16 a[160:163], v[160:163], v[96:99], a[160:163]
s_add_u32 m0, s92, 135168
v_mul_f32 v38, s47, v38
v_mul_f32 v39, s47, v39
v_mfma_f32_16x16x32_bf16 a[160:163], v[164:167], v[100:103], a[160:163]
buffer_load_dwordx4 v20, s[12:15], s25 offen offset:0 lds
v_cvt_pk_bf16_f32 v47, v38, v39
v_mfma_f32_16x16x32_bf16 a[168:171], v[160:163], v[104:107], a[168:171]
v_mfma_f32_16x16x32_bf16 a[168:171], v[164:167], v[108:111], a[168:171]
v_mfma_f32_16x16x32_bf16 a[176:179], v[160:163], v[112:115], a[176:179]
v_mfma_f32_16x16x32_bf16 a[176:179], v[164:167], v[116:119], a[176:179]
buffer_load_dwordx4 v21, s[12:15], s25 offen offset:1024 lds
v_mfma_f32_16x16x32_bf16 a[184:187], v[160:163], v[120:123], a[184:187]
v_mfma_f32_16x16x32_bf16 a[184:187], v[164:167], v[124:127], a[184:187]
v_mfma_f32_16x16x32_bf16 a[164:167], v[168:171], v[96:99], a[164:167]
ds_read_b128 v[48:51], v24 offset:0
ds_read_b128 a[0:3], v25 offset:0
v_mfma_f32_16x16x32_bf16 a[164:167], v[172:175], v[100:103], a[164:167]
buffer_load_dwordx4 v22, s[12:15], s25 offen offset:2048 lds
v_mfma_f32_16x16x32_bf16 a[172:175], v[168:171], v[104:107], a[172:175]
v_mfma_f32_16x16x32_bf16 a[172:175], v[172:175], v[108:111], a[172:175]
v_mfma_f32_16x16x32_bf16 a[180:183], v[168:171], v[112:115], a[180:183]
v_mfma_f32_16x16x32_bf16 a[180:183], v[172:175], v[116:119], a[180:183]
buffer_load_dwordx4 v23, s[12:15], s25 offen offset:3072 lds
v_mfma_f32_16x16x32_bf16 a[188:191], v[168:171], v[120:123], a[188:191]
ds_read_b128 v[52:55], v24 offset:2048
ds_read_b128 a[4:7], v25 offset:2048
v_mfma_f32_16x16x32_bf16 a[188:191], v[172:175], v[124:127], a[188:191]
s_xor_b32 s48, s95, 0
v_mfma_f32_16x16x32_bf16 a[224:227], v[176:179], v[96:99], a[224:227]
buffer_store_dwordx4 v[40:43], v7, s[20:23], s48 offen sc1
ds_read_b128 v[56:59], v24 offset:4096
ds_read_b128 a[8:11], v25 offset:4096
v_mfma_f32_16x16x32_bf16 a[224:227], v[180:183], v[100:103], a[224:227]
s_add_u32 s24, s24, 128
s_add_u32 s25, s25, 128
v_mfma_f32_16x16x32_bf16 a[232:235], v[176:179], v[104:107], a[232:235]
v_mfma_f32_16x16x32_bf16 a[232:235], v[180:183], v[108:111], a[232:235]
v_mfma_f32_16x16x32_bf16 a[240:243], v[176:179], v[112:115], a[240:243]
v_mfma_f32_16x16x32_bf16 a[240:243], v[180:183], v[116:119], a[240:243]
s_xor_b32 s48, s95, 1024
v_mfma_f32_16x16x32_bf16 a[248:251], v[176:179], v[120:123], a[248:251]
buffer_store_dwordx4 v[44:47], v7, s[20:23], s48 offen sc1
ds_read_b128 v[60:63], v24 offset:6144
ds_read_b128 a[12:15], v25 offset:6144
v_mfma_f32_16x16x32_bf16 a[248:251], v[180:183], v[124:127], a[248:251]
v_mfma_f32_16x16x32_bf16 a[228:231], v[184:187], v[96:99], a[228:231]
v_mfma_f32_16x16x32_bf16 a[228:231], v[188:191], v[100:103], a[228:231]
v_mfma_f32_16x16x32_bf16 a[236:239], v[184:187], v[104:107], a[236:239]
v_mfma_f32_16x16x32_bf16 a[236:239], v[188:191], v[108:111], a[236:239]
v_mfma_f32_16x16x32_bf16 a[244:247], v[184:187], v[112:115], a[244:247]
v_mfma_f32_16x16x32_bf16 a[244:247], v[188:191], v[116:119], a[244:247]
s_waitcnt lgkmcnt(0)
v_mfma_f32_16x16x32_bf16 a[252:255], v[184:187], v[120:123], a[252:255]
v_mfma_f32_16x16x32_bf16 a[252:255], v[188:191], v[124:127], a[252:255]
.Lp2_pad:
s_cbranch_execz .Ltch1
.Lp2:
v_mfma_f32_16x16x32_bf16 v[128:131], v[64:67], v[48:51], 0
ds_read_b64_tr_b16 v[96:97], v30 offset:0
ds_read_b64_tr_b16 v[98:99], v30 offset:1024
v_mfma_f32_16x16x32_bf16 v[128:131], v[68:71], a[0:3], v[128:131]
v_add_u32 v24, 65536, v24
v_add_u32 v25, 65536, v25
v_mfma_f32_16x16x32_bf16 v[132:135], v[72:75], v[48:51], 0
ds_read_b64_tr_b16 v[100:101], v30 offset:8192
ds_read_b64_tr_b16 v[102:103], v30 offset:9216
v_mfma_f32_16x16x32_bf16 v[132:135], v[76:79], a[0:3], v[132:135]
v_accvgpr_read_b32 v36, a16
v_accvgpr_read_b32 v37, a17
v_mfma_f32_16x16x32_bf16 v[136:139], v[80:83], v[48:51], 0
ds_read_b64_tr_b16 v[104:105], v31 offset:0
ds_read_b64_tr_b16 v[106:107], v31 offset:1024
v_mfma_f32_16x16x32_bf16 v[136:139], v[84:87], a[0:3], v[136:139]
v_mul_f32 v36, s47, v36
v_mul_f32 v37, s47, v37
v_mfma_f32_16x16x32_bf16 v[140:143], v[88:91], v[48:51], 0
ds_read_b64_tr_b16 v[108:109], v31 offset:8192
ds_read_b64_tr_b16 v[110:111], v31 offset:9216
v_mfma_f32_16x16x32_bf16 v[140:143], v[92:95], a[0:3], v[140:143]
v_cvt_pk_bf16_f32 v40, v36, v37
v_accvgpr_read_b32 v38, a18
v_mfma_f32_16x16x32_bf16 v[160:163], v[64:67], v[52:55], 0
v_accvgpr_read_b32 v39, a19
v_mul_f32 v38, s47, v38
v_mfma_f32_16x16x32_bf16 v[160:163], v[68:71], a[4:7], v[160:163]
v_mul_f32 v39, s47, v39
v_cvt_pk_bf16_f32 v41, v38, v39
v_mfma_f32_16x16x32_bf16 v[164:167], v[72:75], v[52:55], 0
ds_read_b64_tr_b16 v[112:113], v32 offset:0
ds_read_b64_tr_b16 v[114:115], v32 offset:1024
v_mfma_f32_16x16x32_bf16 v[164:167], v[76:79], a[4:7], v[164:167]
v_accvgpr_read_b32 v36, a20
v_accvgpr_read_b32 v37, a21
v_mfma_f32_16x16x32_bf16 v[168:171], v[80:83], v[52:55], 0
s_add_u32 m0, s91, 16384
ds_read_b64_tr_b16 v[116:117], v32 offset:8192
ds_read_b64_tr_b16 v[118:119], v32 offset:9216
v_mfma_f32_16x16x32_bf16 v[168:171], v[84:87], a[4:7], v[168:171]
buffer_load_dwordx4 v16, s[16:19], s26 offen offset:0 lds
v_mul_f32 v36, s47, v36
v_mfma_f32_16x16x32_bf16 v[172:175], v[88:91], v[52:55], 0
ds_read_b64_tr_b16 v[120:121], v33 offset:0
ds_read_b64_tr_b16 v[122:123], v33 offset:1024
v_mfma_f32_16x16x32_bf16 v[172:175], v[92:95], a[4:7], v[172:175]
v_mul_f32 v37, s47, v37
v_cvt_pk_bf16_f32 v42, v36, v37
v_mfma_f32_16x16x32_bf16 v[192:195], v[64:67], v[56:59], 0
v_accvgpr_read_b32 v38, a22
v_accvgpr_read_b32 v39, a23
v_mfma_f32_16x16x32_bf16 v[192:195], v[68:71], a[8:11], v[192:195]
buffer_load_dwordx4 v17, s[16:19], s26 offen offset:1024 lds
v_mul_f32 v38, s47, v38
v_mfma_f32_16x16x32_bf16 v[196:199], v[72:75], v[56:59], 0
ds_read_b64_tr_b16 v[124:125], v33 offset:8192
ds_read_b64_tr_b16 v[126:127], v33 offset:9216
v_mfma_f32_16x16x32_bf16 v[196:199], v[76:79], a[8:11], v[196:199]
v_mul_f32 v39, s47, v39
v_cvt_pk_bf16_f32 v43, v38, v39
v_mfma_f32_16x16x32_bf16 v[200:203], v[80:83], v[56:59], 0
s_xor_b32 s48, s95, 2048
buffer_store_dwordx4 v[40:43], v7, s[20:23], s48 offen sc1
v_mfma_f32_16x16x32_bf16 v[200:203], v[84:87], a[8:11], v[200:203]
buffer_load_dwordx4 v18, s[16:19], s26 offen offset:2048 lds
v_accvgpr_read_b32 v36, a128
v_mfma_f32_16x16x32_bf16 v[204:207], v[88:91], v[56:59], 0
s_waitcnt lgkmcnt(15)
v_accvgpr_read_b32 v37, a129
v_mul_f32 v36, s47, v36
v_mfma_f32_16x16x32_bf16 v[204:207], v[92:95], a[8:11], v[204:207]
v_mul_f32 v37, s47, v37
v_cvt_pk_bf16_f32 v44, v36, v37
v_mfma_f32_16x16x32_bf16 v[224:227], v[64:67], v[60:63], 0
v_accvgpr_read_b32 v38, a130
v_accvgpr_read_b32 v39, a131
v_mfma_f32_16x16x32_bf16 v[224:227], v[68:71], a[12:15], v[224:227]
buffer_load_dwordx4 v19, s[16:19], s26 offen offset:3072 lds
v_mul_f32 v38, s47, v38
v_mfma_f32_16x16x32_bf16 v[228:231], v[72:75], v[60:63], 0
v_mul_f32 v39, s47, v39
v_cvt_pk_bf16_f32 v45, v38, v39
v_mfma_f32_16x16x32_bf16 v[228:231], v[76:79], a[12:15], v[228:231]
s_add_u32 s26, s26, 524288
v_accvgpr_read_b32 v36, a132
v_accvgpr_read_b32 v37, a133
v_mfma_f32_16x16x32_bf16 v[232:235], v[80:83], v[60:63], 0
v_mul_f32 v36, s47, v36
v_mul_f32 v37, s47, v37
v_mfma_f32_16x16x32_bf16 v[232:235], v[84:87], a[12:15], v[232:235]
v_cvt_pk_bf16_f32 v46, v36, v37
v_accvgpr_read_b32 v38, a134
v_mfma_f32_16x16x32_bf16 v[236:239], v[88:91], v[60:63], 0
v_accvgpr_read_b32 v39, a135
v_mul_f32 v38, s47, v38
v_mfma_f32_16x16x32_bf16 v[236:239], v[92:95], a[12:15], v[236:239]
s_waitcnt vmcnt(19) lgkmcnt(0)
s_barrier
v_mul_f32 v39, s47, v39
v_mfma_f32_16x16x32_bf16 v[144:147], v[96:99], v[48:51], 0
ds_read_b64_tr_b16 v[64:65], v26 offset:16384
ds_read_b64_tr_b16 v[66:67], v26 offset:17408
v_mfma_f32_16x16x32_bf16 v[144:147], v[100:103], a[0:3], v[144:147]
v_cvt_pk_bf16_f32 v47, v38, v39
v_accvgpr_write_b32 a128, v44
v_mfma_f32_16x16x32_bf16 v[148:151], v[104:107], v[48:51], 0
s_add_u32 m0, s92, 32768
ds_read_b64_tr_b16 v[68:69], v26 offset:24576
ds_read_b64_tr_b16 v[70:71], v26 offset:25600
v_mfma_f32_16x16x32_bf16 v[148:151], v[108:111], a[0:3], v[148:151]
buffer_load_dwordx4 v20, s[12:15], s24 offen offset:0 lds
v_accvgpr_write_b32 a129, v45
v_mfma_f32_16x16x32_bf16 v[152:155], v[112:115], v[48:51], 0
ds_read_b64_tr_b16 v[72:73], v27 offset:16384
ds_read_b64_tr_b16 v[74:75], v27 offset:17408
v_mfma_f32_16x16x32_bf16 v[152:155], v[116:119], a[0:3], v[152:155]
v_accvgpr_write_b32 a130, v46
v_accvgpr_write_b32 a131, v47
v_mfma_f32_16x16x32_bf16 v[156:159], v[120:123], v[48:51], 0
ds_read_b64_tr_b16 v[76:77], v27 offset:24576
ds_read_b64_tr_b16 v[78:79], v27 offset:25600
v_mfma_f32_16x16x32_bf16 v[156:159], v[124:127], a[0:3], v[156:159]
buffer_load_dwordx4 v21, s[12:15], s24 offen offset:1024 lds
v_accvgpr_read_b32 v36, a136
v_mfma_f32_16x16x32_bf16 v[176:179], v[96:99], v[52:55], 0
ds_read_b128 v[48:51], v24 offset:0
v_accvgpr_read_b32 v37, a137
v_mfma_f32_16x16x32_bf16 v[176:179], v[100:103], a[4:7], v[176:179]
ds_read_b128 a[0:3], v25 offset:0
v_mul_f32 v36, s47, v36
v_mfma_f32_16x16x32_bf16 v[180:183], v[104:107], v[52:55], 0
ds_read_b64_tr_b16 v[80:81], v28 offset:16384
ds_read_b64_tr_b16 v[82:83], v28 offset:17408
v_mfma_f32_16x16x32_bf16 v[180:183], v[108:111], a[4:7], v[180:183]
v_mul_f32 v37, s47, v37
v_cvt_pk_bf16_f32 v40, v36, v37
v_mfma_f32_16x16x32_bf16 v[184:187], v[112:115], v[52:55], 0
ds_read_b64_tr_b16 v[84:85], v28 offset:24576
ds_read_b64_tr_b16 v[86:87], v28 offset:25600
v_mfma_f32_16x16x32_bf16 v[184:187], v[116:119], a[4:7], v[184:187]
buffer_load_dwordx4 v22, s[12:15], s24 offen offset:2048 lds
v_accvgpr_read_b32 v38, a138
v_mfma_f32_16x16x32_bf16 v[188:191], v[120:123], v[52:55], 0
ds_read_b64_tr_b16 v[88:89], v29 offset:16384
ds_read_b64_tr_b16 v[90:91], v29 offset:17408
v_mfma_f32_16x16x32_bf16 v[188:191], v[124:127], a[4:7], v[188:191]
v_accvgpr_read_b32 v39, a139
v_mul_f32 v38, s47, v38
v_mfma_f32_16x16x32_bf16 v[208:211], v[96:99], v[56:59], 0
ds_read_b128 v[52:55], v24 offset:2048
v_mul_f32 v39, s47, v39
v_mfma_f32_16x16x32_bf16 v[208:211], v[100:103], a[8:11], v[208:211]
ds_read_b128 a[4:7], v25 offset:2048
v_cvt_pk_bf16_f32 v41, v38, v39
v_mfma_f32_16x16x32_bf16 v[212:215], v[104:107], v[56:59], 0
ds_read_b64_tr_b16 v[92:93], v29 offset:24576
ds_read_b64_tr_b16 v[94:95], v29 offset:25600
v_mfma_f32_16x16x32_bf16 v[212:215], v[108:111], a[8:11], v[212:215]
buffer_load_dwordx4 v23, s[12:15], s24 offen offset:3072 lds
v_accvgpr_read_b32 v36, a140
v_mfma_f32_16x16x32_bf16 v[216:219], v[112:115], v[56:59], 0
v_accvgpr_read_b32 v37, a141
v_mul_f32 v36, s47, v36
v_mfma_f32_16x16x32_bf16 v[216:219], v[116:119], a[8:11], v[216:219]
v_mul_f32 v37, s47, v37
v_cvt_pk_bf16_f32 v42, v36, v37
v_mfma_f32_16x16x32_bf16 v[220:223], v[120:123], v[56:59], 0
s_add_u32 m0, s92, 36864
v_accvgpr_read_b32 v38, a142
v_accvgpr_read_b32 v39, a143
v_mfma_f32_16x16x32_bf16 v[220:223], v[124:127], a[8:11], v[220:223]
buffer_load_dwordx4 v20, s[12:15], s25 offen offset:0 lds
v_mul_f32 v38, s47, v38
v_mfma_f32_16x16x32_bf16 v[240:243], v[96:99], v[60:63], 0
ds_read_b128 v[56:59], v24 offset:4096
v_mul_f32 v39, s47, v39
v_mfma_f32_16x16x32_bf16 v[240:243], v[100:103], a[12:15], v[240:243]
ds_read_b128 a[8:11], v25 offset:4096
v_cvt_pk_bf16_f32 v43, v38, v39
v_mfma_f32_16x16x32_bf16 v[244:247], v[104:107], v[60:63], 0
v_accvgpr_write_b32 a132, v40
v_accvgpr_write_b32 a133, v41
v_mfma_f32_16x16x32_bf16 v[244:247], v[108:111], a[12:15], v[244:247]
buffer_load_dwordx4 v21, s[12:15], s25 offen offset:1024 lds
v_accvgpr_write_b32 a134, v42
v_mfma_f32_16x16x32_bf16 v[248:251], v[112:115], v[60:63], 0
v_accvgpr_write_b32 a135, v43
v_accvgpr_read_b32 v36, a24
v_mfma_f32_16x16x32_bf16 v[248:251], v[116:119], a[12:15], v[248:251]
s_waitcnt lgkmcnt(0)
v_accvgpr_read_b32 v37, a25
v_mul_f32 v36, s47, v36
v_mfma_f32_16x16x32_bf16 v[252:255], v[120:123], v[60:63], 0
ds_read_b128 v[60:63], v24 offset:6144
v_mul_f32 v37, s47, v37
v_mfma_f32_16x16x32_bf16 v[252:255], v[124:127], a[12:15], v[252:255]
ds_read_b128 a[12:15], v25 offset:6144
v_cvt_pk_bf16_f32 v44, v36, v37
v_mfma_f32_16x16x32_bf16 v[128:131], v[64:67], v[48:51], v[128:131]
ds_read_b64_tr_b16 v[96:97], v30 offset:16384
ds_read_b64_tr_b16 v[98:99], v30 offset:17408
v_mfma_f32_16x16x32_bf16 v[128:131], v[68:71], a[0:3], v[128:131]
v_add_u32 v24, 32768, v24
v_add_u32 v25, 32768, v25
v_mfma_f32_16x16x32_bf16 v[132:135], v[72:75], v[48:51], v[132:135]
ds_read_b64_tr_b16 v[100:101], v30 offset:24576
ds_read_b64_tr_b16 v[102:103], v30 offset:25600
v_mfma_f32_16x16x32_bf16 v[132:135], v[76:79], a[0:3], v[132:135]
buffer_load_dwordx4 v22, s[12:15], s25 offen offset:2048 lds
v_accvgpr_read_b32 v38, a26
v_mfma_f32_16x16x32_bf16 v[136:139], v[80:83], v[48:51], v[136:139]
ds_read_b64_tr_b16 v[104:105], v31 offset:16384
ds_read_b64_tr_b16 v[106:107], v31 offset:17408
v_mfma_f32_16x16x32_bf16 v[136:139], v[84:87], a[0:3], v[136:139]
v_accvgpr_read_b32 v39, a27
v_mul_f32 v38, s47, v38
v_mfma_f32_16x16x32_bf16 v[140:143], v[88:91], v[48:51], v[140:143]
ds_read_b64_tr_b16 v[108:109], v31 offset:24576
ds_read_b64_tr_b16 v[110:111], v31 offset:25600
v_mfma_f32_16x16x32_bf16 v[140:143], v[92:95], a[0:3], v[140:143]
buffer_load_dwordx4 v23, s[12:15], s25 offen offset:3072 lds
v_mul_f32 v39, s47, v39
v_mfma_f32_16x16x32_bf16 v[160:163], v[64:67], v[52:55], v[160:163]
v_xor_b32 v26, 0x10000, v26
v_xor_b32 v27, 0x10000, v27
v_mfma_f32_16x16x32_bf16 v[160:163], v[68:71], a[4:7], v[160:163]
s_add_u32 s24, s24, 128
s_add_u32 s25, s25, 128
v_cvt_pk_bf16_f32 v45, v38, v39
v_mfma_f32_16x16x32_bf16 v[164:167], v[72:75], v[52:55], v[164:167]
ds_read_b64_tr_b16 v[112:113], v32 offset:16384
ds_read_b64_tr_b16 v[114:115], v32 offset:17408
v_mfma_f32_16x16x32_bf16 v[164:167], v[76:79], a[4:7], v[164:167]
v_accvgpr_read_b32 v36, a28
v_accvgpr_read_b32 v37, a29
v_mfma_f32_16x16x32_bf16 v[168:171], v[80:83], v[52:55], v[168:171]
s_add_u32 m0, s91, 65536
ds_read_b64_tr_b16 v[116:117], v32 offset:24576
ds_read_b64_tr_b16 v[118:119], v32 offset:25600
v_mfma_f32_16x16x32_bf16 v[168:171], v[84:87], a[4:7], v[168:171]
buffer_load_dwordx4 v16, s[16:19], s26 offen offset:0 lds
v_mul_f32 v36, s47, v36
v_mfma_f32_16x16x32_bf16 v[172:175], v[88:91], v[52:55], v[172:175]
ds_read_b64_tr_b16 v[120:121], v33 offset:16384
ds_read_b64_tr_b16 v[122:123], v33 offset:17408
v_mfma_f32_16x16x32_bf16 v[172:175], v[92:95], a[4:7], v[172:175]
v_mul_f32 v37, s47, v37
v_cvt_pk_bf16_f32 v46, v36, v37
v_mfma_f32_16x16x32_bf16 v[192:195], v[64:67], v[56:59], v[192:195]
v_xor_b32 v28, 0x10000, v28
v_xor_b32 v29, 0x10000, v29
v_mfma_f32_16x16x32_bf16 v[192:195], v[68:71], a[8:11], v[192:195]
buffer_load_dwordx4 v17, s[16:19], s26 offen offset:1024 lds
v_accvgpr_read_b32 v38, a30
v_mfma_f32_16x16x32_bf16 v[196:199], v[72:75], v[56:59], v[196:199]
ds_read_b64_tr_b16 v[124:125], v33 offset:24576
ds_read_b64_tr_b16 v[126:127], v33 offset:25600
v_mfma_f32_16x16x32_bf16 v[196:199], v[76:79], a[8:11], v[196:199]
v_accvgpr_read_b32 v39, a31
v_mul_f32 v38, s47, v38
v_mfma_f32_16x16x32_bf16 v[200:203], v[80:83], v[56:59], v[200:203]
v_mul_f32 v39, s47, v39
v_cvt_pk_bf16_f32 v47, v38, v39
v_mfma_f32_16x16x32_bf16 v[200:203], v[84:87], a[8:11], v[200:203]
s_xor_b32 s48, s95, 3072
buffer_load_dwordx4 v18, s[16:19], s26 offen offset:2048 lds
v_mfma_f32_16x16x32_bf16 v[204:207], v[88:91], v[56:59], v[204:207]
s_waitcnt lgkmcnt(15)
buffer_store_dwordx4 v[44:47], v7, s[20:23], s48 offen sc1
v_mfma_f32_16x16x32_bf16 v[204:207], v[92:95], a[8:11], v[204:207]
v_xor_b32 v30, 0x10000, v30
v_xor_b32 v31, 0x10000, v31
v_mfma_f32_16x16x32_bf16 v[224:227], v[64:67], v[60:63], v[224:227]
v_accvgpr_read_b32 v36, a144
v_accvgpr_read_b32 v37, a145
v_mfma_f32_16x16x32_bf16 v[224:227], v[68:71], a[12:15], v[224:227]
buffer_load_dwordx4 v19, s[16:19], s26 offen offset:3072 lds
v_mul_f32 v36, s47, v36
v_mfma_f32_16x16x32_bf16 v[228:231], v[72:75], v[60:63], v[228:231]
v_mul_f32 v37, s47, v37
v_cvt_pk_bf16_f32 v40, v36, v37
v_mfma_f32_16x16x32_bf16 v[228:231], v[76:79], a[12:15], v[228:231]
s_add_u32 s26, s26, 524288
v_xor_b32 v32, 0x10000, v32
v_xor_b32 v33, 0x10000, v33
v_mfma_f32_16x16x32_bf16 v[232:235], v[80:83], v[60:63], v[232:235]
v_accvgpr_read_b32 v38, a146
v_accvgpr_read_b32 v39, a147
v_mfma_f32_16x16x32_bf16 v[232:235], v[84:87], a[12:15], v[232:235]
v_mul_f32 v38, s47, v38
v_mul_f32 v39, s47, v39
v_mfma_f32_16x16x32_bf16 v[236:239], v[88:91], v[60:63], v[236:239]
v_cvt_pk_bf16_f32 v41, v38, v39
v_accvgpr_read_b32 v36, a148
v_mfma_f32_16x16x32_bf16 v[236:239], v[92:95], a[12:15], v[236:239]
s_waitcnt vmcnt(20) lgkmcnt(0)
s_barrier
v_accvgpr_read_b32 v37, a149
v_mfma_f32_16x16x32_bf16 v[144:147], v[96:99], v[48:51], v[144:147]
ds_read_b64_tr_b16 v[64:65], v26 offset:0
ds_read_b64_tr_b16 v[66:67], v26 offset:1024
v_mfma_f32_16x16x32_bf16 v[144:147], v[100:103], a[0:3], v[144:147]
v_mul_f32 v36, s47, v36
v_mul_f32 v37, s47, v37
v_mfma_f32_16x16x32_bf16 v[148:151], v[104:107], v[48:51], v[148:151]
s_add_u32 m0, s92, 98304
ds_read_b64_tr_b16 v[68:69], v26 offset:8192
ds_read_b64_tr_b16 v[70:71], v26 offset:9216
v_mfma_f32_16x16x32_bf16 v[148:151], v[108:111], a[0:3], v[148:151]
buffer_load_dwordx4 v20, s[12:15], s24 offen offset:0 lds
v_cvt_pk_bf16_f32 v42, v36, v37
v_mfma_f32_16x16x32_bf16 v[152:155], v[112:115], v[48:51], v[152:155]
ds_read_b64_tr_b16 v[72:73], v27 offset:0
ds_read_b64_tr_b16 v[74:75], v27 offset:1024
v_mfma_f32_16x16x32_bf16 v[152:155], v[116:119], a[0:3], v[152:155]
v_accvgpr_read_b32 v38, a150
v_accvgpr_read_b32 v39, a151
v_mfma_f32_16x16x32_bf16 v[156:159], v[120:123], v[48:51], v[156:159]
ds_read_b64_tr_b16 v[76:77], v27 offset:8192
ds_read_b64_tr_b16 v[78:79], v27 offset:9216
v_mfma_f32_16x16x32_bf16 v[156:159], v[124:127], a[0:3], v[156:159]
buffer_load_dwordx4 v21, s[12:15], s24 offen offset:1024 lds
v_mul_f32 v38, s47, v38
v_mfma_f32_16x16x32_bf16 v[176:179], v[96:99], v[52:55], v[176:179]
ds_read_b128 v[48:51], v24 offset:0
v_mul_f32 v39, s47, v39
v_mfma_f32_16x16x32_bf16 v[176:179], v[100:103], a[4:7], v[176:179]
ds_read_b128 a[0:3], v25 offset:0
v_cvt_pk_bf16_f32 v43, v38, v39
v_mfma_f32_16x16x32_bf16 v[180:183], v[104:107], v[52:55], v[180:183]
ds_read_b64_tr_b16 v[80:81], v28 offset:0
ds_read_b64_tr_b16 v[82:83], v28 offset:1024
v_mfma_f32_16x16x32_bf16 v[180:183], v[108:111], a[4:7], v[180:183]
v_accvgpr_write_b32 a136, v40
v_accvgpr_write_b32 a137, v41
v_mfma_f32_16x16x32_bf16 v[184:187], v[112:115], v[52:55], v[184:187]
ds_read_b64_tr_b16 v[84:85], v28 offset:8192
ds_read_b64_tr_b16 v[86:87], v28 offset:9216
v_mfma_f32_16x16x32_bf16 v[184:187], v[116:119], a[4:7], v[184:187]
buffer_load_dwordx4 v22, s[12:15], s24 offen offset:2048 lds
v_accvgpr_write_b32 a138, v42
v_mfma_f32_16x16x32_bf16 v[188:191], v[120:123], v[52:55], v[188:191]
ds_read_b64_tr_b16 v[88:89], v29 offset:0
ds_read_b64_tr_b16 v[90:91], v29 offset:1024
v_mfma_f32_16x16x32_bf16 v[188:191], v[124:127], a[4:7], v[188:191]
v_accvgpr_write_b32 a139, v43
v_mfma_f32_16x16x32_bf16 v[208:211], v[96:99], v[56:59], v[208:211]
ds_read_b128 v[52:55], v24 offset:2048
v_mfma_f32_16x16x32_bf16 v[208:211], v[100:103], a[8:11], v[208:211]
ds_read_b128 a[4:7], v25 offset:2048
v_mfma_f32_16x16x32_bf16 v[212:215], v[104:107], v[56:59], v[212:215]
ds_read_b64_tr_b16 v[92:93], v29 offset:8192
ds_read_b64_tr_b16 v[94:95], v29 offset:9216
v_mfma_f32_16x16x32_bf16 v[212:215], v[108:111], a[8:11], v[212:215]
buffer_load_dwordx4 v23, s[12:15], s24 offen offset:3072 lds
v_accvgpr_read_b32 v36, a32
v_mfma_f32_16x16x32_bf16 v[216:219], v[112:115], v[56:59], v[216:219]
v_accvgpr_read_b32 v37, a33
v_mul_f32 v36, s47, v36
v_mfma_f32_16x16x32_bf16 v[216:219], v[116:119], a[8:11], v[216:219]
v_mul_f32 v37, s47, v37
v_cvt_pk_bf16_f32 v44, v36, v37
v_mfma_f32_16x16x32_bf16 v[220:223], v[120:123], v[56:59], v[220:223]
s_add_u32 m0, s92, 102400
v_accvgpr_read_b32 v38, a34
v_accvgpr_read_b32 v39, a35
v_mfma_f32_16x16x32_bf16 v[220:223], v[124:127], a[8:11], v[220:223]
buffer_load_dwordx4 v20, s[12:15], s25 offen offset:0 lds
v_mul_f32 v38, s47, v38
v_mfma_f32_16x16x32_bf16 v[240:243], v[96:99], v[60:63], v[240:243]
ds_read_b128 v[56:59], v24 offset:4096
v_mul_f32 v39, s47, v39
v_mfma_f32_16x16x32_bf16 v[240:243], v[100:103], a[12:15], v[240:243]
ds_read_b128 a[8:11], v25 offset:4096
v_cvt_pk_bf16_f32 v45, v38, v39
v_mfma_f32_16x16x32_bf16 v[244:247], v[104:107], v[60:63], v[244:247]
v_accvgpr_read_b32 v36, a36
v_accvgpr_read_b32 v37, a37
v_mfma_f32_16x16x32_bf16 v[244:247], v[108:111], a[12:15], v[244:247]
buffer_load_dwordx4 v21, s[12:15], s25 offen offset:1024 lds
v_mul_f32 v36, s47, v36
v_mfma_f32_16x16x32_bf16 v[248:251], v[112:115], v[60:63], v[248:251]
v_mul_f32 v37, s47, v37
v_cvt_pk_bf16_f32 v46, v36, v37
v_mfma_f32_16x16x32_bf16 v[248:251], v[116:119], a[12:15], v[248:251]
s_waitcnt lgkmcnt(0)
v_accvgpr_read_b32 v38, a38
v_accvgpr_read_b32 v39, a39
v_mfma_f32_16x16x32_bf16 v[252:255], v[120:123], v[60:63], v[252:255]
ds_read_b128 v[60:63], v24 offset:6144
v_mul_f32 v38, s47, v38
v_mfma_f32_16x16x32_bf16 v[252:255], v[124:127], a[12:15], v[252:255]
ds_read_b128 a[12:15], v25 offset:6144
v_mul_f32 v39, s47, v39
v_mfma_f32_16x16x32_bf16 v[128:131], v[64:67], v[48:51], v[128:131]
ds_read_b64_tr_b16 v[96:97], v30 offset:0
ds_read_b64_tr_b16 v[98:99], v30 offset:1024
v_mfma_f32_16x16x32_bf16 v[128:131], v[68:71], a[0:3], v[128:131]
v_add_u32 v24, 4294868992, v24
v_add_u32 v25, 4294868992, v25
v_mfma_f32_16x16x32_bf16 v[132:135], v[72:75], v[48:51], v[132:135]
ds_read_b64_tr_b16 v[100:101], v30 offset:8192
ds_read_b64_tr_b16 v[102:103], v30 offset:9216
v_mfma_f32_16x16x32_bf16 v[132:135], v[76:79], a[0:3], v[132:135]
buffer_load_dwordx4 v22, s[12:15], s25 offen offset:2048 lds
v_cvt_pk_bf16_f32 v47, v38, v39
v_mfma_f32_16x16x32_bf16 v[136:139], v[80:83], v[48:51], v[136:139]
ds_read_b64_tr_b16 v[104:105], v31 offset:0
ds_read_b64_tr_b16 v[106:107], v31 offset:1024
v_mfma_f32_16x16x32_bf16 v[136:139], v[84:87], a[0:3], v[136:139]
s_xor_b32 s48, s95, 4096
buffer_store_dwordx4 v[44:47], v7, s[20:23], s48 offen sc1
v_mfma_f32_16x16x32_bf16 v[140:143], v[88:91], v[48:51], v[140:143]
ds_read_b64_tr_b16 v[108:109], v31 offset:8192
ds_read_b64_tr_b16 v[110:111], v31 offset:9216
v_mfma_f32_16x16x32_bf16 v[140:143], v[92:95], a[0:3], v[140:143]
buffer_load_dwordx4 v23, s[12:15], s25 offen offset:3072 lds
v_accvgpr_read_b32 v36, a152
v_mfma_f32_16x16x32_bf16 v[160:163], v[64:67], v[52:55], v[160:163]
v_accvgpr_read_b32 v37, a153
v_mul_f32 v36, s47, v36
v_mfma_f32_16x16x32_bf16 v[160:163], v[68:71], a[4:7], v[160:163]
s_add_u32 s24, s24, 128
s_add_u32 s25, s25, 128
v_mul_f32 v37, s47, v37
v_mfma_f32_16x16x32_bf16 v[164:167], v[72:75], v[52:55], v[164:167]
ds_read_b64_tr_b16 v[112:113], v32 offset:0
ds_read_b64_tr_b16 v[114:115], v32 offset:1024
v_mfma_f32_16x16x32_bf16 v[164:167], v[76:79], a[4:7], v[164:167]
v_cvt_pk_bf16_f32 v40, v36, v37
v_accvgpr_read_b32 v38, a154
v_mfma_f32_16x16x32_bf16 v[168:171], v[80:83], v[52:55], v[168:171]
s_add_u32 m0, s91, 81920
ds_read_b64_tr_b16 v[116:117], v32 offset:8192
ds_read_b64_tr_b16 v[118:119], v32 offset:9216
v_mfma_f32_16x16x32_bf16 v[168:171], v[84:87], a[4:7], v[168:171]
buffer_load_dwordx4 v16, s[16:19], s26 offen offset:0 lds
v_accvgpr_read_b32 v39, a155
v_mfma_f32_16x16x32_bf16 v[172:175], v[88:91], v[52:55], v[172:175]
ds_read_b64_tr_b16 v[120:121], v33 offset:0
ds_read_b64_tr_b16 v[122:123], v33 offset:1024
v_mfma_f32_16x16x32_bf16 v[172:175], v[92:95], a[4:7], v[172:175]
v_mul_f32 v38, s47, v38
v_mul_f32 v39, s47, v39
v_mfma_f32_16x16x32_bf16 v[192:195], v[64:67], v[56:59], v[192:195]
v_cvt_pk_bf16_f32 v41, v38, v39
v_accvgpr_read_b32 v36, a156
v_mfma_f32_16x16x32_bf16 v[192:195], v[68:71], a[8:11], v[192:195]
buffer_load_dwordx4 v17, s[16:19], s26 offen offset:1024 lds
v_accvgpr_read_b32 v37, a157
v_mfma_f32_16x16x32_bf16 v[196:199], v[72:75], v[56:59], v[196:199]
ds_read_b64_tr_b16 v[124:125], v33 offset:8192
ds_read_b64_tr_b16 v[126:127], v33 offset:9216
v_mfma_f32_16x16x32_bf16 v[196:199], v[76:79], a[8:11], v[196:199]
v_mul_f32 v36, s47, v36
v_mul_f32 v37, s47, v37
v_mfma_f32_16x16x32_bf16 v[200:203], v[80:83], v[56:59], v[200:203]
v_cvt_pk_bf16_f32 v42, v36, v37
v_accvgpr_read_b32 v38, a158
v_mfma_f32_16x16x32_bf16 v[200:203], v[84:87], a[8:11], v[200:203]
buffer_load_dwordx4 v18, s[16:19], s26 offen offset:2048 lds
v_accvgpr_read_b32 v39, a159
v_mfma_f32_16x16x32_bf16 v[204:207], v[88:91], v[56:59], v[204:207]
s_waitcnt lgkmcnt(15)
v_mul_f32 v38, s47, v38
v_mul_f32 v39, s47, v39
v_mfma_f32_16x16x32_bf16 v[204:207], v[92:95], a[8:11], v[204:207]
v_cvt_pk_bf16_f32 v43, v38, v39
v_accvgpr_write_b32 a140, v40
v_mfma_f32_16x16x32_bf16 v[224:227], v[64:67], v[60:63], v[224:227]
v_accvgpr_write_b32 a141, v41
v_accvgpr_write_b32 a142, v42
v_mfma_f32_16x16x32_bf16 v[224:227], v[68:71], a[12:15], v[224:227]
buffer_load_dwordx4 v19, s[16:19], s26 offen offset:3072 lds
v_accvgpr_write_b32 a143, v43
.Lpad5:
s_cbranch_execz .Ltch1
v_mfma_f32_16x16x32_bf16 v[228:231], v[72:75], v[60:63], v[228:231]
v_accvgpr_read_b32 v36, a160
v_accvgpr_read_b32 v37, a161
v_mfma_f32_16x16x32_bf16 v[228:231], v[76:79], a[12:15], v[228:231]
s_add_u32 s26, s26, 524288
v_mul_f32 v36, s47, v36
v_mul_f32 v37, s47, v37
v_mfma_f32_16x16x32_bf16 v[232:235], v[80:83], v[60:63], v[232:235]
v_cvt_pk_bf16_f32 v44, v36, v37
v_accvgpr_read_b32 v38, a162
v_mfma_f32_16x16x32_bf16 v[232:235], v[84:87], a[12:15], v[232:235]
v_accvgpr_read_b32 v39, a163
v_mul_f32 v38, s47, v38
v_mfma_f32_16x16x32_bf16 v[236:239], v[88:91], v[60:63], v[236:239]
v_mul_f32 v39, s47, v39
v_cvt_pk_bf16_f32 v45, v38, v39
v_mfma_f32_16x16x32_bf16 v[236:239], v[92:95], a[12:15], v[236:239]
s_waitcnt vmcnt(18) lgkmcnt(0)
s_barrier
v_accvgpr_read_b32 v36, a164
v_mfma_f32_16x16x32_bf16 v[144:147], v[96:99], v[48:51], v[144:147]
ds_read_b64_tr_b16 v[64:65], v26 offset:16384
ds_read_b64_tr_b16 v[66:67], v26 offset:17408
v_mfma_f32_16x16x32_bf16 v[144:147], v[100:103], a[0:3], v[144:147]
v_accvgpr_read_b32 v37, a165
v_mul_f32 v36, s47, v36
v_mfma_f32_16x16x32_bf16 v[148:151], v[104:107], v[48:51], v[148:151]
s_add_u32 m0, s92, 131072
ds_read_b64_tr_b16 v[68:69], v26 offset:24576
ds_read_b64_tr_b16 v[70:71], v26 offset:25600
v_mfma_f32_16x16x32_bf16 v[148:151], v[108:111], a[0:3], v[148:151]
buffer_load_dwordx4 v20, s[12:15], s24 offen offset:0 lds
v_mul_f32 v37, s47, v37
v_mfma_f32_16x16x32_bf16 v[152:155], v[112:115], v[48:51], v[152:155]
ds_read_b64_tr_b16 v[72:73], v27 offset:16384
ds_read_b64_tr_b16 v[74:75], v27 offset:17408
v_mfma_f32_16x16x32_bf16 v[152:155], v[116:119], a[0:3], v[152:155]
v_cvt_pk_bf16_f32 v46, v36, v37
v_accvgpr_read_b32 v38, a166
v_mfma_f32_16x16x32_bf16 v[156:159], v[120:123], v[48:51], v[156:159]
ds_read_b64_tr_b16 v[76:77], v27 offset:24576
ds_read_b64_tr_b16 v[78:79], v27 offset:25600
v_mfma_f32_16x16x32_bf16 v[156:159], v[124:127], a[0:3], v[156:159]
buffer_load_dwordx4 v21, s[12:15], s24 offen offset:1024 lds
v_accvgpr_read_b32 v39, a167
v_mfma_f32_16x16x32_bf16 v[176:179], v[96:99], v[52:55], v[176:179]
ds_read_b128 v[48:51], v24 offset:0
v_mul_f32 v38, s47, v38
v_mfma_f32_16x16x32_bf16 v[176:179], v[100:103], a[4:7], v[176:179]
ds_read_b128 a[0:3], v25 offset:0
v_mul_f32 v39, s47, v39
v_mfma_f32_16x16x32_bf16 v[180:183], v[104:107], v[52:55], v[180:183]
ds_read_b64_tr_b16 v[80:81], v28 offset:16384
ds_read_b64_tr_b16 v[82:83], v28 offset:17408
v_mfma_f32_16x16x32_bf16 v[180:183], v[108:111], a[4:7], v[180:183]
v_cvt_pk_bf16_f32 v47, v38, v39
v_accvgpr_write_b32 a160, v44
v_mfma_f32_16x16x32_bf16 v[184:187], v[112:115], v[52:55], v[184:187]
ds_read_b64_tr_b16 v[84:85], v28 offset:24576
ds_read_b64_tr_b16 v[86:87], v28 offset:25600
v_mfma_f32_16x16x32_bf16 v[184:187], v[116:119], a[4:7], v[184:187]
buffer_load_dwordx4 v22, s[12:15], s24 offen offset:2048 lds
v_accvgpr_write_b32 a161, v45
v_mfma_f32_16x16x32_bf16 v[188:191], v[120:123], v[52:55], v[188:191]
ds_read_b64_tr_b16 v[88:89], v29 offset:16384
ds_read_b64_tr_b16 v[90:91], v29 offset:17408
v_mfma_f32_16x16x32_bf16 v[188:191], v[124:127], a[4:7], v[188:191]
v_accvgpr_write_b32 a162, v46
v_accvgpr_write_b32 a163, v47
v_mfma_f32_16x16x32_bf16 v[208:211], v[96:99], v[56:59], v[208:211]
ds_read_b128 v[52:55], v24 offset:2048
v_accvgpr_read_b32 v36, a40
v_mfma_f32_16x16x32_bf16 v[208:211], v[100:103], a[8:11], v[208:211]
ds_read_b128 a[4:7], v25 offset:2048
v_accvgpr_read_b32 v37, a41
v_mfma_f32_16x16x32_bf16 v[212:215], v[104:107], v[56:59], v[212:215]
ds_read_b64_tr_b16 v[92:93], v29 offset:24576
ds_read_b64_tr_b16 v[94:95], v29 offset:25600
v_mfma_f32_16x16x32_bf16 v[212:215], v[108:111], a[8:11], v[212:215]
buffer_load_dwordx4 v23, s[12:15], s24 offen offset:3072 lds
v_mul_f32 v36, s47, v36
v_mfma_f32_16x16x32_bf16 v[216:219], v[112:115], v[56:59], v[216:219]
v_mul_f32 v37, s47, v37
v_cvt_pk_bf16_f32 v40, v36, v37
v_mfma_f32_16x16x32_bf16 v[216:219], v[116:119], a[8:11], v[216:219]
v_accvgpr_read_b32 v38, a42
v_accvgpr_read_b32 v39, a43
v_mfma_f32_16x16x32_bf16 v[220:223], v[120:123], v[56:59], v[220:223]
s_add_u32 m0, s92, 135168
v_mul_f32 v38, s47, v38
v_mul_f32 v39, s47, v39
v_mfma_f32_16x16x32_bf16 v[220:223], v[124:127], a[8:11], v[220:223]
buffer_load_dwordx4 v20, s[12:15], s25 offen offset:0 lds
v_cvt_pk_bf16_f32 v41, v38, v39
v_mfma_f32_16x16x32_bf16 v[240:243], v[96:99], v[60:63], v[240:243]
ds_read_b128 v[56:59], v24 offset:4096
v_accvgpr_read_b32 v36, a44
v_mfma_f32_16x16x32_bf16 v[240:243], v[100:103], a[12:15], v[240:243]
ds_read_b128 a[8:11], v25 offset:4096
v_accvgpr_read_b32 v37, a45
v_mfma_f32_16x16x32_bf16 v[244:247], v[104:107], v[60:63], v[244:247]
v_mul_f32 v36, s47, v36
v_mul_f32 v37, s47, v37
v_mfma_f32_16x16x32_bf16 v[244:247], v[108:111], a[12:15], v[244:247]
buffer_load_dwordx4 v21, s[12:15], s25 offen offset:1024 lds
v_cvt_pk_bf16_f32 v42, v36, v37
v_mfma_f32_16x16x32_bf16 v[248:251], v[112:115], v[60:63], v[248:251]
v_accvgpr_read_b32 v38, a46
v_accvgpr_read_b32 v39, a47
v_mfma_f32_16x16x32_bf16 v[248:251], v[116:119], a[12:15], v[248:251]
s_waitcnt lgkmcnt(0)
v_mul_f32 v38, s47, v38
v_mul_f32 v39, s47, v39
v_mfma_f32_16x16x32_bf16 v[252:255], v[120:123], v[60:63], v[252:255]
ds_read_b128 v[60:63], v24 offset:6144
v_cvt_pk_bf16_f32 v43, v38, v39
v_mfma_f32_16x16x32_bf16 v[252:255], v[124:127], a[12:15], v[252:255]
s_xor_b32 s48, s95, 5120
ds_read_b128 a[12:15], v25 offset:6144
v_mfma_f32_16x16x32_bf16 v[128:131], v[64:67], v[48:51], v[128:131]
ds_read_b64_tr_b16 v[96:97], v30 offset:16384
ds_read_b64_tr_b16 v[98:99], v30 offset:17408
v_mfma_f32_16x16x32_bf16 v[128:131], v[68:71], a[0:3], v[128:131]
v_add_u32 v24, 65536, v24
v_add_u32 v25, 65536, v25
v_mfma_f32_16x16x32_bf16 v[132:135], v[72:75], v[48:51], v[132:135]
ds_read_b64_tr_b16 v[100:101], v30 offset:24576
ds_read_b64_tr_b16 v[102:103], v30 offset:25600
v_mfma_f32_16x16x32_bf16 v[132:135], v[76:79], a[0:3], v[132:135]
buffer_load_dwordx4 v22, s[12:15], s25 offen offset:2048 lds
v_accvgpr_read_b32 v36, a168
v_mfma_f32_16x16x32_bf16 v[136:139], v[80:83], v[48:51], v[136:139]
ds_read_b64_tr_b16 v[104:105], v31 offset:16384
ds_read_b64_tr_b16 v[106:107], v31 offset:17408
v_mfma_f32_16x16x32_bf16 v[136:139], v[84:87], a[0:3], v[136:139]
buffer_store_dwordx4 v[40:43], v7, s[20:23], s48 offen sc1
v_accvgpr_read_b32 v37, a169
v_mfma_f32_16x16x32_bf16 v[140:143], v[88:91], v[48:51], v[140:143]
ds_read_b64_tr_b16 v[108:109], v31 offset:24576
ds_read_b64_tr_b16 v[110:111], v31 offset:25600
v_mfma_f32_16x16x32_bf16 v[140:143], v[92:95], a[0:3], v[140:143]
buffer_load_dwordx4 v23, s[12:15], s25 offen offset:3072 lds
v_mul_f32 v36, s47, v36
v_mfma_f32_16x16x32_bf16 v[160:163], v[64:67], v[52:55], v[160:163]
v_xor_b32 v26, 0x10000, v26
v_xor_b32 v27, 0x10000, v27
v_mfma_f32_16x16x32_bf16 v[160:163], v[68:71], a[4:7], v[160:163]
s_add_u32 s24, s24, 128
s_add_u32 s25, s25, 128
v_mul_f32 v37, s47, v37
v_mfma_f32_16x16x32_bf16 v[164:167], v[72:75], v[52:55], v[164:167]
ds_read_b64_tr_b16 v[112:113], v32 offset:16384
ds_read_b64_tr_b16 v[114:115], v32 offset:17408
v_mfma_f32_16x16x32_bf16 v[164:167], v[76:79], a[4:7], v[164:167]
v_cvt_pk_bf16_f32 v44, v36, v37
v_accvgpr_read_b32 v38, a170
v_mfma_f32_16x16x32_bf16 v[168:171], v[80:83], v[52:55], v[168:171]
s_add_u32 m0, s91, 0
ds_read_b64_tr_b16 v[116:117], v32 offset:24576
ds_read_b64_tr_b16 v[118:119], v32 offset:25600
v_mfma_f32_16x16x32_bf16 v[168:171], v[84:87], a[4:7], v[168:171]
buffer_load_dwordx4 v16, s[16:19], s26 offen offset:0 lds
v_accvgpr_read_b32 v39, a171
v_mfma_f32_16x16x32_bf16 v[172:175], v[88:91], v[52:55], v[172:175]
ds_read_b64_tr_b16 v[120:121], v33 offset:16384
ds_read_b64_tr_b16 v[122:123], v33 offset:17408
v_mfma_f32_16x16x32_bf16 v[172:175], v[92:95], a[4:7], v[172:175]
v_mul_f32 v38, s47, v38
v_mul_f32 v39, s47, v39
v_mfma_f32_16x16x32_bf16 v[192:195], v[64:67], v[56:59], v[192:195]
v_xor_b32 v28, 0x10000, v28
v_xor_b32 v29, 0x10000, v29
v_mfma_f32_16x16x32_bf16 v[192:195], v[68:71], a[8:11], v[192:195]
buffer_load_dwordx4 v17, s[16:19], s26 offen offset:1024 lds
v_cvt_pk_bf16_f32 v45, v38, v39
v_mfma_f32_16x16x32_bf16 v[196:199], v[72:75], v[56:59], v[196:199]
ds_read_b64_tr_b16 v[124:125], v33 offset:24576
ds_read_b64_tr_b16 v[126:127], v33 offset:25600
v_mfma_f32_16x16x32_bf16 v[196:199], v[76:79], a[8:11], v[196:199]
v_accvgpr_read_b32 v36, a172
v_accvgpr_read_b32 v37, a173
v_mfma_f32_16x16x32_bf16 v[200:203], v[80:83], v[56:59], v[200:203]
v_mul_f32 v36, s47, v36
v_mul_f32 v37, s47, v37
v_mfma_f32_16x16x32_bf16 v[200:203], v[84:87], a[8:11], v[200:203]
buffer_load_dwordx4 v18, s[16:19], s26 offen offset:2048 lds
v_cvt_pk_bf16_f32 v46, v36, v37
v_mfma_f32_16x16x32_bf16 v[204:207], v[88:91], v[56:59], v[204:207]
s_waitcnt lgkmcnt(15)
v_accvgpr_read_b32 v38, a174
v_accvgpr_read_b32 v39, a175
v_mfma_f32_16x16x32_bf16 v[204:207], v[92:95], a[8:11], v[204:207]
v_xor_b32 v30, 0x10000, v30
v_xor_b32 v31, 0x10000, v31
v_mfma_f32_16x16x32_bf16 v[224:227], v[64:67], v[60:63], v[224:227]
v_mul_f32 v38, s47, v38
v_mul_f32 v39, s47, v39
v_mfma_f32_16x16x32_bf16 v[224:227], v[68:71], a[12:15], v[224:227]
buffer_load_dwordx4 v19, s[16:19], s26 offen offset:3072 lds
v_cvt_pk_bf16_f32 v47, v38, v39
v_mfma_f32_16x16x32_bf16 v[228:231], v[72:75], v[60:63], v[228:231]
v_accvgpr_write_b32 a164, v44
v_accvgpr_write_b32 a165, v45
v_mfma_f32_16x16x32_bf16 v[228:231], v[76:79], a[12:15], v[228:231]
s_add_u32 s26, s26, 524288
v_xor_b32 v32, 0x10000, v32
v_xor_b32 v33, 0x10000, v33
v_mfma_f32_16x16x32_bf16 v[232:235], v[80:83], v[60:63], v[232:235]
v_accvgpr_write_b32 a166, v46
v_accvgpr_write_b32 a167, v47
v_mfma_f32_16x16x32_bf16 v[232:235], v[84:87], a[12:15], v[232:235]
v_mfma_f32_16x16x32_bf16 v[236:239], v[88:91], v[60:63], v[236:239]
v_mfma_f32_16x16x32_bf16 v[236:239], v[92:95], a[12:15], v[236:239]
s_waitcnt vmcnt(17) lgkmcnt(0)
s_barrier
v_mfma_f32_16x16x32_bf16 v[144:147], v[96:99], v[48:51], v[144:147]
ds_read_b64_tr_b16 v[64:65], v26 offset:0
ds_read_b64_tr_b16 v[66:67], v26 offset:1024
v_mfma_f32_16x16x32_bf16 v[144:147], v[100:103], a[0:3], v[144:147]
v_mfma_f32_16x16x32_bf16 v[148:151], v[104:107], v[48:51], v[148:151]
s_add_u32 m0, s92, 32768
ds_read_b64_tr_b16 v[68:69], v26 offset:8192
ds_read_b64_tr_b16 v[70:71], v26 offset:9216
v_mfma_f32_16x16x32_bf16 v[148:151], v[108:111], a[0:3], v[148:151]
buffer_load_dwordx4 v20, s[12:15], s24 offen offset:0 lds
v_mfma_f32_16x16x32_bf16 v[152:155], v[112:115], v[48:51], v[152:155]
ds_read_b64_tr_b16 v[72:73], v27 offset:0
ds_read_b64_tr_b16 v[74:75], v27 offset:1024
v_mfma_f32_16x16x32_bf16 v[152:155], v[116:119], a[0:3], v[152:155]
v_accvgpr_read_b32 v36, a48
v_accvgpr_read_b32 v37, a49
v_mfma_f32_16x16x32_bf16 v[156:159], v[120:123], v[48:51], v[156:159]
ds_read_b64_tr_b16 v[76:77], v27 offset:8192
ds_read_b64_tr_b16 v[78:79], v27 offset:9216
v_mfma_f32_16x16x32_bf16 v[156:159], v[124:127], a[0:3], v[156:159]
buffer_load_dwordx4 v21, s[12:15], s24 offen offset:1024 lds
v_mul_f32 v36, s47, v36
v_mfma_f32_16x16x32_bf16 v[176:179], v[96:99], v[52:55], v[176:179]
ds_read_b128 v[48:51], v24 offset:0
v_mul_f32 v37, s47, v37
v_mfma_f32_16x16x32_bf16 v[176:179], v[100:103], a[4:7], v[176:179]
ds_read_b128 a[0:3], v25 offset:0
v_cvt_pk_bf16_f32 v40, v36, v37
v_mfma_f32_16x16x32_bf16 v[180:183], v[104:107], v[52:55], v[180:183]
ds_read_b64_tr_b16 v[80:81], v28 offset:0
ds_read_b64_tr_b16 v[82:83], v28 offset:1024
v_mfma_f32_16x16x32_bf16 v[180:183], v[108:111], a[4:7], v[180:183]
v_accvgpr_read_b32 v38, a50
v_accvgpr_read_b32 v39, a51
v_mfma_f32_16x16x32_bf16 v[184:187], v[112:115], v[52:55], v[184:187]
ds_read_b64_tr_b16 v[84:85], v28 offset:8192
ds_read_b64_tr_b16 v[86:87], v28 offset:9216
v_mfma_f32_16x16x32_bf16 v[184:187], v[116:119], a[4:7], v[184:187]
buffer_load_dwordx4 v22, s[12:15], s24 offen offset:2048 lds
v_mul_f32 v38, s47, v38
v_mfma_f32_16x16x32_bf16 v[188:191], v[120:123], v[52:55], v[188:191]
ds_read_b64_tr_b16 v[88:89], v29 offset:0
ds_read_b64_tr_b16 v[90:91], v29 offset:1024
v_mfma_f32_16x16x32_bf16 v[188:191], v[124:127], a[4:7], v[188:191]
v_mul_f32 v39, s47, v39
v_cvt_pk_bf16_f32 v41, v38, v39
v_mfma_f32_16x16x32_bf16 v[208:211], v[96:99], v[56:59], v[208:211]
ds_read_b128 v[52:55], v24 offset:2048
v_accvgpr_read_b32 v36, a52
v_mfma_f32_16x16x32_bf16 v[208:211], v[100:103], a[8:11], v[208:211]
ds_read_b128 a[4:7], v25 offset:2048
v_accvgpr_read_b32 v37, a53
v_mfma_f32_16x16x32_bf16 v[212:215], v[104:107], v[56:59], v[212:215]
ds_read_b64_tr_b16 v[92:93], v29 offset:8192
ds_read_b64_tr_b16 v[94:95], v29 offset:9216
v_mfma_f32_16x16x32_bf16 v[212:215], v[108:111], a[8:11], v[212:215]
buffer_load_dwordx4 v23, s[12:15], s24 offen offset:3072 lds
v_mul_f32 v36, s47, v36
v_mfma_f32_16x16x32_bf16 v[216:219], v[112:115], v[56:59], v[216:219]
v_mul_f32 v37, s47, v37
v_cvt_pk_bf16_f32 v42, v36, v37
v_mfma_f32_16x16x32_bf16 v[216:219], v[116:119], a[8:11], v[216:219]
v_accvgpr_read_b32 v38, a54
v_accvgpr_read_b32 v39, a55
v_mfma_f32_16x16x32_bf16 v[220:223], v[120:123], v[56:59], v[220:223]
s_add_u32 m0, s92, 36864
v_mul_f32 v38, s47, v38
v_mul_f32 v39, s47, v39
v_mfma_f32_16x16x32_bf16 v[220:223], v[124:127], a[8:11], v[220:223]
buffer_load_dwordx4 v20, s[12:15], s25 offen offset:0 lds
v_cvt_pk_bf16_f32 v43, v38, v39
v_mfma_f32_16x16x32_bf16 v[240:243], v[96:99], v[60:63], v[240:243]
s_xor_b32 s48, s95, 6144
ds_read_b128 v[56:59], v24 offset:4096
v_mfma_f32_16x16x32_bf16 v[240:243], v[100:103], a[12:15], v[240:243]
ds_read_b128 a[8:11], v25 offset:4096
v_accvgpr_read_b32 v36, a176
v_mfma_f32_16x16x32_bf16 v[244:247], v[104:107], v[60:63], v[244:247]
buffer_store_dwordx4 v[40:43], v7, s[20:23], s48 offen sc1
v_accvgpr_read_b32 v37, a177
v_mfma_f32_16x16x32_bf16 v[244:247], v[108:111], a[12:15], v[244:247]
buffer_load_dwordx4 v21, s[12:15], s25 offen offset:1024 lds
v_mul_f32 v36, s47, v36
v_mfma_f32_16x16x32_bf16 v[248:251], v[112:115], v[60:63], v[248:251]
v_mul_f32 v37, s47, v37
v_cvt_pk_bf16_f32 v44, v36, v37
v_mfma_f32_16x16x32_bf16 v[248:251], v[116:119], a[12:15], v[248:251]
s_waitcnt lgkmcnt(0)
v_accvgpr_read_b32 v38, a178
v_accvgpr_read_b32 v39, a179
v_mfma_f32_16x16x32_bf16 v[252:255], v[120:123], v[60:63], v[252:255]
ds_read_b128 v[60:63], v24 offset:6144
v_mul_f32 v38, s47, v38
v_mfma_f32_16x16x32_bf16 v[252:255], v[124:127], a[12:15], v[252:255]
ds_read_b128 a[12:15], v25 offset:6144
v_mul_f32 v39, s47, v39
v_mfma_f32_16x16x32_bf16 v[128:131], v[64:67], v[48:51], v[128:131]
ds_read_b64_tr_b16 v[96:97], v30 offset:0
ds_read_b64_tr_b16 v[98:99], v30 offset:1024
v_mfma_f32_16x16x32_bf16 v[128:131], v[68:71], a[0:3], v[128:131]
v_add_u32 v24, 32768, v24
v_add_u32 v25, 32768, v25
v_mfma_f32_16x16x32_bf16 v[132:135], v[72:75], v[48:51], v[132:135]
ds_read_b64_tr_b16 v[100:101], v30 offset:8192
ds_read_b64_tr_b16 v[102:103], v30 offset:9216
v_mfma_f32_16x16x32_bf16 v[132:135], v[76:79], a[0:3], v[132:135]
buffer_load_dwordx4 v22, s[12:15], s25 offen offset:2048 lds
v_cvt_pk_bf16_f32 v45, v38, v39
v_mfma_f32_16x16x32_bf16 v[136:139], v[80:83], v[48:51], v[136:139]
ds_read_b64_tr_b16 v[104:105], v31 offset:0
ds_read_b64_tr_b16 v[106:107], v31 offset:1024
v_mfma_f32_16x16x32_bf16 v[136:139], v[84:87], a[0:3], v[136:139]
v_accvgpr_read_b32 v36, a180
v_accvgpr_read_b32 v37, a181
v_mfma_f32_16x16x32_bf16 v[140:143], v[88:91], v[48:51], v[140:143]
ds_read_b64_tr_b16 v[108:109], v31 offset:8192
ds_read_b64_tr_b16 v[110:111], v31 offset:9216
v_mfma_f32_16x16x32_bf16 v[140:143], v[92:95], a[0:3], v[140:143]
buffer_load_dwordx4 v23, s[12:15], s25 offen offset:3072 lds
v_mul_f32 v36, s47, v36
v_mfma_f32_16x16x32_bf16 v[160:163], v[64:67], v[52:55], v[160:163]
v_mul_f32 v37, s47, v37
v_cvt_pk_bf16_f32 v46, v36, v37
v_mfma_f32_16x16x32_bf16 v[160:163], v[68:71], a[4:7], v[160:163]
s_add_u32 s24, s24, 128
s_add_u32 s25, s25, 128
buffer_load_dword v35, v4, s[96:99], 0 offen
v_mfma_f32_16x16x32_bf16 v[164:167], v[72:75], v[52:55], v[164:167]
ds_read_b64_tr_b16 v[112:113], v32 offset:0
ds_read_b64_tr_b16 v[114:115], v32 offset:1024
v_mfma_f32_16x16x32_bf16 v[164:167], v[76:79], a[4:7], v[164:167]
v_accvgpr_read_b32 v38, a182
v_accvgpr_read_b32 v39, a183
v_mfma_f32_16x16x32_bf16 v[168:171], v[80:83], v[52:55], v[168:171]
s_add_u32 m0, s91, 16384
ds_read_b64_tr_b16 v[116:117], v32 offset:8192
ds_read_b64_tr_b16 v[118:119], v32 offset:9216
v_mfma_f32_16x16x32_bf16 v[168:171], v[84:87], a[4:7], v[168:171]
buffer_load_dwordx4 v16, s[16:19], s26 offen offset:0 lds
v_mul_f32 v38, s47, v38
v_mfma_f32_16x16x32_bf16 v[172:175], v[88:91], v[52:55], v[172:175]
ds_read_b64_tr_b16 v[120:121], v33 offset:0
ds_read_b64_tr_b16 v[122:123], v33 offset:1024
v_mfma_f32_16x16x32_bf16 v[172:175], v[92:95], a[4:7], v[172:175]
v_mul_f32 v39, s47, v39
v_cvt_pk_bf16_f32 v47, v38, v39
v_mfma_f32_16x16x32_bf16 v[192:195], v[64:67], v[56:59], v[192:195]
v_accvgpr_write_b32 a168, v44
v_accvgpr_write_b32 a169, v45
v_mfma_f32_16x16x32_bf16 v[192:195], v[68:71], a[8:11], v[192:195]
buffer_load_dwordx4 v17, s[16:19], s26 offen offset:1024 lds
v_accvgpr_write_b32 a170, v46
v_mfma_f32_16x16x32_bf16 v[196:199], v[72:75], v[56:59], v[196:199]
ds_read_b64_tr_b16 v[124:125], v33 offset:8192
ds_read_b64_tr_b16 v[126:127], v33 offset:9216
v_mfma_f32_16x16x32_bf16 v[196:199], v[76:79], a[8:11], v[196:199]
v_accvgpr_write_b32 a171, v47
v_accvgpr_read_b32 v36, a184
v_mfma_f32_16x16x32_bf16 v[200:203], v[80:83], v[56:59], v[200:203]
v_accvgpr_read_b32 v37, a185
v_mul_f32 v36, s47, v36
v_mfma_f32_16x16x32_bf16 v[200:203], v[84:87], a[8:11], v[200:203]
buffer_load_dwordx4 v18, s[16:19], s26 offen offset:2048 lds
v_mul_f32 v37, s47, v37
v_mfma_f32_16x16x32_bf16 v[204:207], v[88:91], v[56:59], v[204:207]
s_waitcnt lgkmcnt(15)
v_cvt_pk_bf16_f32 v40, v36, v37
v_accvgpr_read_b32 v38, a186
v_mfma_f32_16x16x32_bf16 v[204:207], v[92:95], a[8:11], v[204:207]
v_accvgpr_read_b32 v39, a187
v_mul_f32 v38, s47, v38
v_mfma_f32_16x16x32_bf16 v[224:227], v[64:67], v[60:63], v[224:227]
v_mul_f32 v39, s47, v39
v_cvt_pk_bf16_f32 v41, v38, v39
v_mfma_f32_16x16x32_bf16 v[224:227], v[68:71], a[12:15], v[224:227]
buffer_load_dwordx4 v19, s[16:19], s26 offen offset:3072 lds
v_accvgpr_read_b32 v36, a188
v_mfma_f32_16x16x32_bf16 v[228:231], v[72:75], v[60:63], v[228:231]
v_accvgpr_read_b32 v37, a189
v_mul_f32 v36, s47, v36
v_mfma_f32_16x16x32_bf16 v[228:231], v[76:79], a[12:15], v[228:231]
s_add_u32 s26, s26, 524288
v_mul_f32 v37, s47, v37
v_cvt_pk_bf16_f32 v42, v36, v37
v_mfma_f32_16x16x32_bf16 v[232:235], v[80:83], v[60:63], v[232:235]
v_accvgpr_read_b32 v38, a190
v_accvgpr_read_b32 v39, a191
v_mfma_f32_16x16x32_bf16 v[232:235], v[84:87], a[12:15], v[232:235]
v_mul_f32 v38, s47, v38
v_mul_f32 v39, s47, v39
v_mfma_f32_16x16x32_bf16 v[236:239], v[88:91], v[60:63], v[236:239]
v_cvt_pk_bf16_f32 v43, v38, v39
v_accvgpr_write_b32 a172, v40
v_mfma_f32_16x16x32_bf16 v[236:239], v[92:95], a[12:15], v[236:239]
s_waitcnt vmcnt(18) lgkmcnt(0)
s_barrier
v_accvgpr_write_b32 a173, v41
v_mfma_f32_16x16x32_bf16 v[144:147], v[96:99], v[48:51], v[144:147]
ds_read_b64_tr_b16 v[64:65], v26 offset:16384
ds_read_b64_tr_b16 v[66:67], v26 offset:17408
v_mfma_f32_16x16x32_bf16 v[144:147], v[100:103], a[0:3], v[144:147]
v_accvgpr_write_b32 a174, v42
v_accvgpr_write_b32 a175, v43
v_mfma_f32_16x16x32_bf16 v[148:151], v[104:107], v[48:51], v[148:151]
s_add_u32 m0, s92, 98304
ds_read_b64_tr_b16 v[68:69], v26 offset:24576
ds_read_b64_tr_b16 v[70:71], v26 offset:25600
v_mfma_f32_16x16x32_bf16 v[148:151], v[108:111], a[0:3], v[148:151]
buffer_load_dwordx4 v20, s[12:15], s24 offen offset:0 lds
v_accvgpr_read_b32 v36, a56
v_mfma_f32_16x16x32_bf16 v[152:155], v[112:115], v[48:51], v[152:155]
ds_read_b64_tr_b16 v[72:73], v27 offset:16384
ds_read_b64_tr_b16 v[74:75], v27 offset:17408
v_mfma_f32_16x16x32_bf16 v[152:155], v[116:119], a[0:3], v[152:155]
v_accvgpr_read_b32 v37, a57
v_mul_f32 v36, s47, v36
v_mfma_f32_16x16x32_bf16 v[156:159], v[120:123], v[48:51], v[156:159]
ds_read_b64_tr_b16 v[76:77], v27 offset:24576
ds_read_b64_tr_b16 v[78:79], v27 offset:25600
v_mfma_f32_16x16x32_bf16 v[156:159], v[124:127], a[0:3], v[156:159]
buffer_load_dwordx4 v21, s[12:15], s24 offen offset:1024 lds
v_mul_f32 v37, s47, v37
v_mfma_f32_16x16x32_bf16 v[176:179], v[96:99], v[52:55], v[176:179]
ds_read_b128 v[48:51], v24 offset:0
v_cvt_pk_bf16_f32 v44, v36, v37
v_mfma_f32_16x16x32_bf16 v[176:179], v[100:103], a[4:7], v[176:179]
ds_read_b128 a[0:3], v25 offset:0
v_accvgpr_read_b32 v38, a58
v_mfma_f32_16x16x32_bf16 v[180:183], v[104:107], v[52:55], v[180:183]
ds_read_b64_tr_b16 v[80:81], v28 offset:16384
ds_read_b64_tr_b16 v[82:83], v28 offset:17408
v_mfma_f32_16x16x32_bf16 v[180:183], v[108:111], a[4:7], v[180:183]
v_accvgpr_read_b32 v39, a59
v_mul_f32 v38, s47, v38
v_mfma_f32_16x16x32_bf16 v[184:187], v[112:115], v[52:55], v[184:187]
ds_read_b64_tr_b16 v[84:85], v28 offset:24576
ds_read_b64_tr_b16 v[86:87], v28 offset:25600
v_mfma_f32_16x16x32_bf16 v[184:187], v[116:119], a[4:7], v[184:187]
buffer_load_dwordx4 v22, s[12:15], s24 offen offset:2048 lds
v_mul_f32 v39, s47, v39
v_mfma_f32_16x16x32_bf16 v[188:191], v[120:123], v[52:55], v[188:191]
ds_read_b64_tr_b16 v[88:89], v29 offset:16384
ds_read_b64_tr_b16 v[90:91], v29 offset:17408
v_mfma_f32_16x16x32_bf16 v[188:191], v[124:127], a[4:7], v[188:191]
v_cvt_pk_bf16_f32 v45, v38, v39
v_accvgpr_read_b32 v36, a60
v_mfma_f32_16x16x32_bf16 v[208:211], v[96:99], v[56:59], v[208:211]
ds_read_b128 v[52:55], v24 offset:2048
v_accvgpr_read_b32 v37, a61
v_mfma_f32_16x16x32_bf16 v[208:211], v[100:103], a[8:11], v[208:211]
ds_read_b128 a[4:7], v25 offset:2048
v_mul_f32 v36, s47, v36
v_mfma_f32_16x16x32_bf16 v[212:215], v[104:107], v[56:59], v[212:215]
ds_read_b64_tr_b16 v[92:93], v29 offset:24576
ds_read_b64_tr_b16 v[94:95], v29 offset:25600
v_mfma_f32_16x16x32_bf16 v[212:215], v[108:111], a[8:11], v[212:215]
buffer_load_dwordx4 v23, s[12:15], s24 offen offset:3072 lds
v_mul_f32 v37, s47, v37
v_mfma_f32_16x16x32_bf16 v[216:219], v[112:115], v[56:59], v[216:219]
v_cvt_pk_bf16_f32 v46, v36, v37
v_accvgpr_read_b32 v38, a62
v_mfma_f32_16x16x32_bf16 v[216:219], v[116:119], a[8:11], v[216:219]
v_accvgpr_read_b32 v39, a63
v_mul_f32 v38, s47, v38
v_mfma_f32_16x16x32_bf16 v[220:223], v[120:123], v[56:59], v[220:223]
s_add_u32 m0, s92, 102400
v_mul_f32 v39, s47, v39
v_cvt_pk_bf16_f32 v47, v38, v39
v_mfma_f32_16x16x32_bf16 v[220:223], v[124:127], a[8:11], v[220:223]
s_xor_b32 s48, s95, 7168
buffer_load_dwordx4 v20, s[12:15], s25 offen offset:0 lds
v_mfma_f32_16x16x32_bf16 v[240:243], v[96:99], v[60:63], v[240:243]
ds_read_b128 v[56:59], v24 offset:4096
v_accvgpr_read_b32 v36, a192
v_mfma_f32_16x16x32_bf16 v[240:243], v[100:103], a[12:15], v[240:243]
ds_read_b128 a[8:11], v25 offset:4096
v_accvgpr_read_b32 v37, a193
v_mfma_f32_16x16x32_bf16 v[244:247], v[104:107], v[60:63], v[244:247]
buffer_store_dwordx4 v[44:47], v7, s[20:23], s48 offen sc1
v_mul_f32 v36, s47, v36
v_mfma_f32_16x16x32_bf16 v[244:247], v[108:111], a[12:15], v[244:247]
buffer_load_dwordx4 v21, s[12:15], s25 offen offset:1024 lds
v_mul_f32 v37, s47, v37
v_mfma_f32_16x16x32_bf16 v[248:251], v[112:115], v[60:63], v[248:251]
v_cvt_pk_bf16_f32 v40, v36, v37
v_accvgpr_read_b32 v38, a194
v_mfma_f32_16x16x32_bf16 v[248:251], v[116:119], a[12:15], v[248:251]
s_waitcnt lgkmcnt(0)
v_accvgpr_read_b32 v39, a195
v_mul_f32 v38, s47, v38
v_mfma_f32_16x16x32_bf16 v[252:255], v[120:123], v[60:63], v[252:255]
ds_read_b128 v[60:63], v24 offset:6144
v_mul_f32 v39, s47, v39
v_mfma_f32_16x16x32_bf16 v[252:255], v[124:127], a[12:15], v[252:255]
ds_read_b128 a[12:15], v25 offset:6144
v_cvt_pk_bf16_f32 v41, v38, v39
v_mfma_f32_16x16x32_bf16 v[128:131], v[64:67], v[48:51], v[128:131]
ds_read_b64_tr_b16 v[96:97], v30 offset:16384
ds_read_b64_tr_b16 v[98:99], v30 offset:17408
v_mfma_f32_16x16x32_bf16 v[128:131], v[68:71], a[0:3], v[128:131]
v_add_u32 v24, 4294868992, v24
v_add_u32 v25, 4294868992, v25
v_mfma_f32_16x16x32_bf16 v[132:135], v[72:75], v[48:51], v[132:135]
ds_read_b64_tr_b16 v[100:101], v30 offset:24576
ds_read_b64_tr_b16 v[102:103], v30 offset:25600
v_mfma_f32_16x16x32_bf16 v[132:135], v[76:79], a[0:3], v[132:135]
buffer_load_dwordx4 v22, s[12:15], s25 offen offset:2048 lds
v_accvgpr_read_b32 v36, a196
v_mfma_f32_16x16x32_bf16 v[136:139], v[80:83], v[48:51], v[136:139]
ds_read_b64_tr_b16 v[104:105], v31 offset:16384
ds_read_b64_tr_b16 v[106:107], v31 offset:17408
v_mfma_f32_16x16x32_bf16 v[136:139], v[84:87], a[0:3], v[136:139]
v_accvgpr_read_b32 v37, a197
v_mul_f32 v36, s47, v36
v_mfma_f32_16x16x32_bf16 v[140:143], v[88:91], v[48:51], v[140:143]
ds_read_b64_tr_b16 v[108:109], v31 offset:24576
ds_read_b64_tr_b16 v[110:111], v31 offset:25600
v_mfma_f32_16x16x32_bf16 v[140:143], v[92:95], a[0:3], v[140:143]
buffer_load_dwordx4 v23, s[12:15], s25 offen offset:3072 lds
v_mul_f32 v37, s47, v37
v_mfma_f32_16x16x32_bf16 v[160:163], v[64:67], v[52:55], v[160:163]
v_xor_b32 v26, 0x10000, v26
v_xor_b32 v27, 0x10000, v27
v_mfma_f32_16x16x32_bf16 v[160:163], v[68:71], a[4:7], v[160:163]
s_add_u32 s24, s24, 128
s_add_u32 s25, s25, 128
v_cvt_pk_bf16_f32 v42, v36, v37
v_mfma_f32_16x16x32_bf16 v[164:167], v[72:75], v[52:55], v[164:167]
ds_read_b64_tr_b16 v[112:113], v32 offset:16384
ds_read_b64_tr_b16 v[114:115], v32 offset:17408
.Lpad6:
s_cbranch_execz .Ltch1
v_mfma_f32_16x16x32_bf16 v[164:167], v[76:79], a[4:7], v[164:167]
v_accvgpr_read_b32 v38, a198
v_accvgpr_read_b32 v39, a199
v_mfma_f32_16x16x32_bf16 v[168:171], v[80:83], v[52:55], v[168:171]
s_add_u32 m0, s91, 65536
ds_read_b64_tr_b16 v[116:117], v32 offset:24576
ds_read_b64_tr_b16 v[118:119], v32 offset:25600
v_mfma_f32_16x16x32_bf16 v[168:171], v[84:87], a[4:7], v[168:171]
buffer_load_dwordx4 v16, s[16:19], s26 offen offset:0 lds
v_mul_f32 v38, s47, v38
v_mfma_f32_16x16x32_bf16 v[172:175], v[88:91], v[52:55], v[172:175]
ds_read_b64_tr_b16 v[120:121], v33 offset:16384
ds_read_b64_tr_b16 v[122:123], v33 offset:17408
v_mfma_f32_16x16x32_bf16 v[172:175], v[92:95], a[4:7], v[172:175]
v_mul_f32 v39, s47, v39
v_cvt_pk_bf16_f32 v43, v38, v39
v_mfma_f32_16x16x32_bf16 v[192:195], v[64:67], v[56:59], v[192:195]
v_xor_b32 v28, 0x10000, v28
v_xor_b32 v29, 0x10000, v29
v_mfma_f32_16x16x32_bf16 v[192:195], v[68:71], a[8:11], v[192:195]
buffer_load_dwordx4 v17, s[16:19], s26 offen offset:1024 lds
v_accvgpr_write_b32 a192, v40
v_mfma_f32_16x16x32_bf16 v[196:199], v[72:75], v[56:59], v[196:199]
ds_read_b64_tr_b16 v[124:125], v33 offset:24576
ds_read_b64_tr_b16 v[126:127], v33 offset:25600
v_mfma_f32_16x16x32_bf16 v[196:199], v[76:79], a[8:11], v[196:199]
v_accvgpr_write_b32 a193, v41
v_accvgpr_write_b32 a194, v42
v_mfma_f32_16x16x32_bf16 v[200:203], v[80:83], v[56:59], v[200:203]
v_accvgpr_write_b32 a195, v43
v_mfma_f32_16x16x32_bf16 v[200:203], v[84:87], a[8:11], v[200:203]
buffer_load_dwordx4 v18, s[16:19], s26 offen offset:2048 lds
v_mfma_f32_16x16x32_bf16 v[204:207], v[88:91], v[56:59], v[204:207]
s_waitcnt lgkmcnt(15)
v_mfma_f32_16x16x32_bf16 v[204:207], v[92:95], a[8:11], v[204:207]
v_xor_b32 v30, 0x10000, v30
v_xor_b32 v31, 0x10000, v31
v_mfma_f32_16x16x32_bf16 v[224:227], v[64:67], v[60:63], v[224:227]
v_accvgpr_read_b32 v36, a64
v_accvgpr_read_b32 v37, a65
v_mfma_f32_16x16x32_bf16 v[224:227], v[68:71], a[12:15], v[224:227]
buffer_load_dwordx4 v19, s[16:19], s26 offen offset:3072 lds
v_mul_f32 v36, s47, v36
v_mfma_f32_16x16x32_bf16 v[228:231], v[72:75], v[60:63], v[228:231]
v_mul_f32 v37, s47, v37
v_cvt_pk_bf16_f32 v44, v36, v37
v_mfma_f32_16x16x32_bf16 v[228:231], v[76:79], a[12:15], v[228:231]
s_add_u32 s26, s26, 524288
v_xor_b32 v32, 0x10000, v32
v_xor_b32 v33, 0x10000, v33
v_mfma_f32_16x16x32_bf16 v[232:235], v[80:83], v[60:63], v[232:235]
v_accvgpr_read_b32 v38, a66
v_accvgpr_read_b32 v39, a67
v_mfma_f32_16x16x32_bf16 v[232:235], v[84:87], a[12:15], v[232:235]
v_mul_f32 v38, s47, v38
v_mul_f32 v39, s47, v39
v_mfma_f32_16x16x32_bf16 v[236:239], v[88:91], v[60:63], v[236:239]
v_cvt_pk_bf16_f32 v45, v38, v39
v_accvgpr_read_b32 v36, a68
v_mfma_f32_16x16x32_bf16 v[236:239], v[92:95], a[12:15], v[236:239]
s_waitcnt vmcnt(18) lgkmcnt(0)
s_barrier
v_accvgpr_read_b32 v37, a69
v_mfma_f32_16x16x32_bf16 v[144:147], v[96:99], v[48:51], v[144:147]
ds_read_b64_tr_b16 v[64:65], v26 offset:0
ds_read_b64_tr_b16 v[66:67], v26 offset:1024
v_mfma_f32_16x16x32_bf16 v[144:147], v[100:103], a[0:3], v[144:147]
v_mul_f32 v36, s47, v36
v_mul_f32 v37, s47, v37
v_mfma_f32_16x16x32_bf16 v[148:151], v[104:107], v[48:51], v[148:151]
s_add_u32 m0, s92, 131072
ds_read_b64_tr_b16 v[68:69], v26 offset:8192
ds_read_b64_tr_b16 v[70:71], v26 offset:9216
v_mfma_f32_16x16x32_bf16 v[148:151], v[108:111], a[0:3], v[148:151]
buffer_load_dwordx4 v20, s[12:15], s24 offen offset:0 lds
v_cvt_pk_bf16_f32 v46, v36, v37
v_mfma_f32_16x16x32_bf16 v[152:155], v[112:115], v[48:51], v[152:155]
ds_read_b64_tr_b16 v[72:73], v27 offset:0
ds_read_b64_tr_b16 v[74:75], v27 offset:1024
v_mfma_f32_16x16x32_bf16 v[152:155], v[116:119], a[0:3], v[152:155]
v_accvgpr_read_b32 v38, a70
v_accvgpr_read_b32 v39, a71
v_mfma_f32_16x16x32_bf16 v[156:159], v[120:123], v[48:51], v[156:159]
ds_read_b64_tr_b16 v[76:77], v27 offset:8192
ds_read_b64_tr_b16 v[78:79], v27 offset:9216
v_mfma_f32_16x16x32_bf16 v[156:159], v[124:127], a[0:3], v[156:159]
buffer_load_dwordx4 v21, s[12:15], s24 offen offset:1024 lds
v_mul_f32 v38, s47, v38
v_mfma_f32_16x16x32_bf16 v[176:179], v[96:99], v[52:55], v[176:179]
ds_read_b128 v[48:51], v24 offset:0
v_mul_f32 v39, s47, v39
v_mfma_f32_16x16x32_bf16 v[176:179], v[100:103], a[4:7], v[176:179]
ds_read_b128 a[0:3], v25 offset:0
v_cvt_pk_bf16_f32 v47, v38, v39
v_mfma_f32_16x16x32_bf16 v[180:183], v[104:107], v[52:55], v[180:183]
ds_read_b64_tr_b16 v[80:81], v28 offset:0
ds_read_b64_tr_b16 v[82:83], v28 offset:1024
v_mfma_f32_16x16x32_bf16 v[180:183], v[108:111], a[4:7], v[180:183]
s_xor_b32 s48, s95, 131072
buffer_store_dwordx4 v[44:47], v7, s[20:23], s48 offen sc1
v_mfma_f32_16x16x32_bf16 v[184:187], v[112:115], v[52:55], v[184:187]
ds_read_b64_tr_b16 v[84:85], v28 offset:8192
ds_read_b64_tr_b16 v[86:87], v28 offset:9216
v_mfma_f32_16x16x32_bf16 v[184:187], v[116:119], a[4:7], v[184:187]
buffer_load_dwordx4 v22, s[12:15], s24 offen offset:2048 lds
v_accvgpr_read_b32 v36, a200
v_mfma_f32_16x16x32_bf16 v[188:191], v[120:123], v[52:55], v[188:191]
ds_read_b64_tr_b16 v[88:89], v29 offset:0
ds_read_b64_tr_b16 v[90:91], v29 offset:1024
v_mfma_f32_16x16x32_bf16 v[188:191], v[124:127], a[4:7], v[188:191]
v_accvgpr_read_b32 v37, a201
v_mul_f32 v36, s47, v36
v_mfma_f32_16x16x32_bf16 v[208:211], v[96:99], v[56:59], v[208:211]
ds_read_b128 v[52:55], v24 offset:2048
v_mul_f32 v37, s47, v37
v_mfma_f32_16x16x32_bf16 v[208:211], v[100:103], a[8:11], v[208:211]
ds_read_b128 a[4:7], v25 offset:2048
v_cvt_pk_bf16_f32 v40, v36, v37
v_mfma_f32_16x16x32_bf16 v[212:215], v[104:107], v[56:59], v[212:215]
ds_read_b64_tr_b16 v[92:93], v29 offset:8192
ds_read_b64_tr_b16 v[94:95], v29 offset:9216
v_mfma_f32_16x16x32_bf16 v[212:215], v[108:111], a[8:11], v[212:215]
buffer_load_dwordx4 v23, s[12:15], s24 offen offset:3072 lds
v_accvgpr_read_b32 v38, a202
v_mfma_f32_16x16x32_bf16 v[216:219], v[112:115], v[56:59], v[216:219]
v_accvgpr_read_b32 v39, a203
v_mul_f32 v38, s47, v38
v_mfma_f32_16x16x32_bf16 v[216:219], v[116:119], a[8:11], v[216:219]
v_mul_f32 v39, s47, v39
v_cvt_pk_bf16_f32 v41, v38, v39
v_mfma_f32_16x16x32_bf16 v[220:223], v[120:123], v[56:59], v[220:223]
s_add_u32 m0, s92, 135168
v_accvgpr_read_b32 v36, a204
v_accvgpr_read_b32 v37, a205
v_mfma_f32_16x16x32_bf16 v[220:223], v[124:127], a[8:11], v[220:223]
buffer_load_dwordx4 v20, s[12:15], s25 offen offset:0 lds
v_mul_f32 v36, s47, v36
v_mfma_f32_16x16x32_bf16 v[240:243], v[96:99], v[60:63], v[240:243]
ds_read_b128 v[56:59], v24 offset:4096
v_mul_f32 v37, s47, v37
v_mfma_f32_16x16x32_bf16 v[240:243], v[100:103], a[12:15], v[240:243]
ds_read_b128 a[8:11], v25 offset:4096
v_cvt_pk_bf16_f32 v42, v36, v37
v_mfma_f32_16x16x32_bf16 v[244:247], v[104:107], v[60:63], v[244:247]
v_accvgpr_read_b32 v38, a206
v_accvgpr_read_b32 v39, a207
v_mfma_f32_16x16x32_bf16 v[244:247], v[108:111], a[12:15], v[244:247]
buffer_load_dwordx4 v21, s[12:15], s25 offen offset:1024 lds
v_mul_f32 v38, s47, v38
v_mfma_f32_16x16x32_bf16 v[248:251], v[112:115], v[60:63], v[248:251]
v_mul_f32 v39, s47, v39
v_cvt_pk_bf16_f32 v43, v38, v39
v_mfma_f32_16x16x32_bf16 v[248:251], v[116:119], a[12:15], v[248:251]
s_waitcnt lgkmcnt(0)
v_accvgpr_write_b32 a196, v40
v_accvgpr_write_b32 a197, v41
v_mfma_f32_16x16x32_bf16 v[252:255], v[120:123], v[60:63], v[252:255]
ds_read_b128 v[60:63], v24 offset:6144
v_accvgpr_write_b32 a198, v42
v_mfma_f32_16x16x32_bf16 v[252:255], v[124:127], a[12:15], v[252:255]
ds_read_b128 a[12:15], v25 offset:6144
v_accvgpr_write_b32 a199, v43
v_mfma_f32_16x16x32_bf16 v[128:131], v[64:67], v[48:51], v[128:131]
ds_read_b64_tr_b16 v[96:97], v30 offset:0
ds_read_b64_tr_b16 v[98:99], v30 offset:1024
v_mfma_f32_16x16x32_bf16 v[128:131], v[68:71], a[0:3], v[128:131]
v_add_u32 v24, 65536, v24
v_add_u32 v25, 65536, v25
v_mfma_f32_16x16x32_bf16 v[132:135], v[72:75], v[48:51], v[132:135]
ds_read_b64_tr_b16 v[100:101], v30 offset:8192
ds_read_b64_tr_b16 v[102:103], v30 offset:9216
v_mfma_f32_16x16x32_bf16 v[132:135], v[76:79], a[0:3], v[132:135]
buffer_load_dwordx4 v22, s[12:15], s25 offen offset:2048 lds
v_accvgpr_read_b32 v36, a208
v_mfma_f32_16x16x32_bf16 v[136:139], v[80:83], v[48:51], v[136:139]
ds_read_b64_tr_b16 v[104:105], v31 offset:0
ds_read_b64_tr_b16 v[106:107], v31 offset:1024
v_mfma_f32_16x16x32_bf16 v[136:139], v[84:87], a[0:3], v[136:139]
v_accvgpr_read_b32 v37, a209
v_mul_f32 v36, s47, v36
v_mfma_f32_16x16x32_bf16 v[140:143], v[88:91], v[48:51], v[140:143]
ds_read_b64_tr_b16 v[108:109], v31 offset:8192
ds_read_b64_tr_b16 v[110:111], v31 offset:9216
v_mfma_f32_16x16x32_bf16 v[140:143], v[92:95], a[0:3], v[140:143]
buffer_load_dwordx4 v23, s[12:15], s25 offen offset:3072 lds
v_mul_f32 v37, s47, v37
v_mfma_f32_16x16x32_bf16 v[160:163], v[64:67], v[52:55], v[160:163]
v_cvt_pk_bf16_f32 v44, v36, v37
v_accvgpr_read_b32 v38, a210
v_mfma_f32_16x16x32_bf16 v[160:163], v[68:71], a[4:7], v[160:163]
s_add_u32 s24, s24, 128
s_add_u32 s25, s25, 128
v_accvgpr_read_b32 v39, a211
v_mfma_f32_16x16x32_bf16 v[164:167], v[72:75], v[52:55], v[164:167]
ds_read_b64_tr_b16 v[112:113], v32 offset:0
ds_read_b64_tr_b16 v[114:115], v32 offset:1024
v_mfma_f32_16x16x32_bf16 v[164:167], v[76:79], a[4:7], v[164:167]
v_mul_f32 v38, s47, v38
v_mul_f32 v39, s47, v39
v_mfma_f32_16x16x32_bf16 v[168:171], v[80:83], v[52:55], v[168:171]
s_add_u32 m0, s91, 81920
ds_read_b64_tr_b16 v[116:117], v32 offset:8192
ds_read_b64_tr_b16 v[118:119], v32 offset:9216
v_mfma_f32_16x16x32_bf16 v[168:171], v[84:87], a[4:7], v[168:171]
buffer_load_dwordx4 v16, s[16:19], s26 offen offset:0 lds
v_cvt_pk_bf16_f32 v45, v38, v39
v_mfma_f32_16x16x32_bf16 v[172:175], v[88:91], v[52:55], v[172:175]
ds_read_b64_tr_b16 v[120:121], v33 offset:0
ds_read_b64_tr_b16 v[122:123], v33 offset:1024
v_mfma_f32_16x16x32_bf16 v[172:175], v[92:95], a[4:7], v[172:175]
v_accvgpr_read_b32 v36, a212
v_accvgpr_read_b32 v37, a213
v_mfma_f32_16x16x32_bf16 v[192:195], v[64:67], v[56:59], v[192:195]
v_mul_f32 v36, s47, v36
v_mul_f32 v37, s47, v37
v_mfma_f32_16x16x32_bf16 v[192:195], v[68:71], a[8:11], v[192:195]
buffer_load_dwordx4 v17, s[16:19], s26 offen offset:1024 lds
v_cvt_pk_bf16_f32 v46, v36, v37
v_mfma_f32_16x16x32_bf16 v[196:199], v[72:75], v[56:59], v[196:199]
ds_read_b64_tr_b16 v[124:125], v33 offset:8192
ds_read_b64_tr_b16 v[126:127], v33 offset:9216
v_mfma_f32_16x16x32_bf16 v[196:199], v[76:79], a[8:11], v[196:199]
v_accvgpr_read_b32 v38, a214
v_accvgpr_read_b32 v39, a215
v_mfma_f32_16x16x32_bf16 v[200:203], v[80:83], v[56:59], v[200:203]
v_mul_f32 v38, s47, v38
v_mul_f32 v39, s47, v39
v_mfma_f32_16x16x32_bf16 v[200:203], v[84:87], a[8:11], v[200:203]
buffer_load_dwordx4 v18, s[16:19], s26 offen offset:2048 lds
v_cvt_pk_bf16_f32 v47, v38, v39
v_mfma_f32_16x16x32_bf16 v[204:207], v[88:91], v[56:59], v[204:207]
s_waitcnt lgkmcnt(15)
v_accvgpr_write_b32 a200, v44
v_accvgpr_write_b32 a201, v45
v_mfma_f32_16x16x32_bf16 v[204:207], v[92:95], a[8:11], v[204:207]
v_accvgpr_write_b32 a202, v46
v_accvgpr_write_b32 a203, v47
v_mfma_f32_16x16x32_bf16 v[224:227], v[64:67], v[60:63], v[224:227]
v_accvgpr_read_b32 v36, a72
v_accvgpr_read_b32 v37, a73
v_mfma_f32_16x16x32_bf16 v[224:227], v[68:71], a[12:15], v[224:227]
buffer_load_dwordx4 v19, s[16:19], s26 offen offset:3072 lds
v_mul_f32 v36, s47, v36
v_mfma_f32_16x16x32_bf16 v[228:231], v[72:75], v[60:63], v[228:231]
v_mul_f32 v37, s47, v37
v_cvt_pk_bf16_f32 v40, v36, v37
v_mfma_f32_16x16x32_bf16 v[228:231], v[76:79], a[12:15], v[228:231]
s_add_u32 s26, s26, 524288
v_accvgpr_read_b32 v38, a74
v_accvgpr_read_b32 v39, a75
v_mfma_f32_16x16x32_bf16 v[232:235], v[80:83], v[60:63], v[232:235]
v_mul_f32 v38, s47, v38
v_mul_f32 v39, s47, v39
v_mfma_f32_16x16x32_bf16 v[232:235], v[84:87], a[12:15], v[232:235]
v_cvt_pk_bf16_f32 v41, v38, v39
v_accvgpr_read_b32 v36, a76
v_mfma_f32_16x16x32_bf16 v[236:239], v[88:91], v[60:63], v[236:239]
v_accvgpr_read_b32 v37, a77
v_mul_f32 v36, s47, v36
v_mfma_f32_16x16x32_bf16 v[236:239], v[92:95], a[12:15], v[236:239]
s_waitcnt vmcnt(17) lgkmcnt(0)
s_barrier
v_mul_f32 v37, s47, v37
v_mfma_f32_16x16x32_bf16 v[144:147], v[96:99], v[48:51], v[144:147]
ds_read_b64_tr_b16 v[64:65], v26 offset:16384
ds_read_b64_tr_b16 v[66:67], v26 offset:17408
v_mfma_f32_16x16x32_bf16 v[144:147], v[100:103], a[0:3], v[144:147]
v_cvt_pk_bf16_f32 v42, v36, v37
v_accvgpr_read_b32 v38, a78
v_mfma_f32_16x16x32_bf16 v[148:151], v[104:107], v[48:51], v[148:151]
s_add_u32 m0, s92, 32768
ds_read_b64_tr_b16 v[68:69], v26 offset:24576
ds_read_b64_tr_b16 v[70:71], v26 offset:25600
v_mfma_f32_16x16x32_bf16 v[148:151], v[108:111], a[0:3], v[148:151]
buffer_load_dwordx4 v20, s[12:15], s24 offen offset:0 lds
v_accvgpr_read_b32 v39, a79
v_mfma_f32_16x16x32_bf16 v[152:155], v[112:115], v[48:51], v[152:155]
ds_read_b64_tr_b16 v[72:73], v27 offset:16384
ds_read_b64_tr_b16 v[74:75], v27 offset:17408
v_mfma_f32_16x16x32_bf16 v[152:155], v[116:119], a[0:3], v[152:155]
v_mul_f32 v38, s47, v38
v_mul_f32 v39, s47, v39
v_mfma_f32_16x16x32_bf16 v[156:159], v[120:123], v[48:51], v[156:159]
ds_read_b64_tr_b16 v[76:77], v27 offset:24576
ds_read_b64_tr_b16 v[78:79], v27 offset:25600
v_mfma_f32_16x16x32_bf16 v[156:159], v[124:127], a[0:3], v[156:159]
buffer_load_dwordx4 v21, s[12:15], s24 offen offset:1024 lds
v_cvt_pk_bf16_f32 v43, v38, v39
v_mfma_f32_16x16x32_bf16 v[176:179], v[96:99], v[52:55], v[176:179]
s_xor_b32 s48, s95, 132096
ds_read_b128 v[48:51], v24 offset:0
v_mfma_f32_16x16x32_bf16 v[176:179], v[100:103], a[4:7], v[176:179]
ds_read_b128 a[0:3], v25 offset:0
v_accvgpr_read_b32 v36, a216
v_mfma_f32_16x16x32_bf16 v[180:183], v[104:107], v[52:55], v[180:183]
ds_read_b64_tr_b16 v[80:81], v28 offset:16384
ds_read_b64_tr_b16 v[82:83], v28 offset:17408
v_mfma_f32_16x16x32_bf16 v[180:183], v[108:111], a[4:7], v[180:183]
buffer_store_dwordx4 v[40:43], v7, s[20:23], s48 offen sc1
v_accvgpr_read_b32 v37, a217
v_mfma_f32_16x16x32_bf16 v[184:187], v[112:115], v[52:55], v[184:187]
ds_read_b64_tr_b16 v[84:85], v28 offset:24576
ds_read_b64_tr_b16 v[86:87], v28 offset:25600
v_mfma_f32_16x16x32_bf16 v[184:187], v[116:119], a[4:7], v[184:187]
buffer_load_dwordx4 v22, s[12:15], s24 offen offset:2048 lds
v_mul_f32 v36, s47, v36
v_mfma_f32_16x16x32_bf16 v[188:191], v[120:123], v[52:55], v[188:191]
ds_read_b64_tr_b16 v[88:89], v29 offset:16384
ds_read_b64_tr_b16 v[90:91], v29 offset:17408
v_mfma_f32_16x16x32_bf16 v[188:191], v[124:127], a[4:7], v[188:191]
v_mul_f32 v37, s47, v37
v_cvt_pk_bf16_f32 v44, v36, v37
v_mfma_f32_16x16x32_bf16 v[208:211], v[96:99], v[56:59], v[208:211]
ds_read_b128 v[52:55], v24 offset:2048
v_accvgpr_read_b32 v38, a218
v_mfma_f32_16x16x32_bf16 v[208:211], v[100:103], a[8:11], v[208:211]
ds_read_b128 a[4:7], v25 offset:2048
v_accvgpr_read_b32 v39, a219
v_mfma_f32_16x16x32_bf16 v[212:215], v[104:107], v[56:59], v[212:215]
ds_read_b64_tr_b16 v[92:93], v29 offset:24576
ds_read_b64_tr_b16 v[94:95], v29 offset:25600
v_mfma_f32_16x16x32_bf16 v[212:215], v[108:111], a[8:11], v[212:215]
buffer_load_dwordx4 v23, s[12:15], s24 offen offset:3072 lds
v_mul_f32 v38, s47, v38
v_mfma_f32_16x16x32_bf16 v[216:219], v[112:115], v[56:59], v[216:219]
v_mul_f32 v39, s47, v39
v_cvt_pk_bf16_f32 v45, v38, v39
v_mfma_f32_16x16x32_bf16 v[216:219], v[116:119], a[8:11], v[216:219]
v_accvgpr_read_b32 v36, a220
v_accvgpr_read_b32 v37, a221
v_mfma_f32_16x16x32_bf16 v[220:223], v[120:123], v[56:59], v[220:223]
s_add_u32 m0, s92, 36864
v_mul_f32 v36, s47, v36
v_mul_f32 v37, s47, v37
v_mfma_f32_16x16x32_bf16 v[220:223], v[124:127], a[8:11], v[220:223]
buffer_load_dwordx4 v20, s[12:15], s25 offen offset:0 lds
v_cvt_pk_bf16_f32 v46, v36, v37
v_mfma_f32_16x16x32_bf16 v[240:243], v[96:99], v[60:63], v[240:243]
ds_read_b128 v[56:59], v24 offset:4096
v_accvgpr_read_b32 v38, a222
v_mfma_f32_16x16x32_bf16 v[240:243], v[100:103], a[12:15], v[240:243]
ds_read_b128 a[8:11], v25 offset:4096
v_accvgpr_read_b32 v39, a223
v_mfma_f32_16x16x32_bf16 v[244:247], v[104:107], v[60:63], v[244:247]
v_mul_f32 v38, s47, v38
v_mul_f32 v39, s47, v39
v_mfma_f32_16x16x32_bf16 v[244:247], v[108:111], a[12:15], v[244:247]
buffer_load_dwordx4 v21, s[12:15], s25 offen offset:1024 lds
v_cvt_pk_bf16_f32 v47, v38, v39
v_mfma_f32_16x16x32_bf16 v[248:251], v[112:115], v[60:63], v[248:251]
v_accvgpr_write_b32 a204, v44
v_accvgpr_write_b32 a205, v45
v_mfma_f32_16x16x32_bf16 v[248:251], v[116:119], a[12:15], v[248:251]
s_waitcnt lgkmcnt(0)
v_accvgpr_write_b32 a206, v46
v_accvgpr_write_b32 a207, v47
v_mfma_f32_16x16x32_bf16 v[252:255], v[120:123], v[60:63], v[252:255]
ds_read_b128 v[60:63], v24 offset:6144
v_accvgpr_read_b32 v36, a224
v_mfma_f32_16x16x32_bf16 v[252:255], v[124:127], a[12:15], v[252:255]
ds_read_b128 a[12:15], v25 offset:6144
v_accvgpr_read_b32 v37, a225
v_mfma_f32_16x16x32_bf16 v[128:131], v[64:67], v[48:51], v[128:131]
ds_read_b64_tr_b16 v[96:97], v30 offset:16384
ds_read_b64_tr_b16 v[98:99], v30 offset:17408
v_mfma_f32_16x16x32_bf16 v[128:131], v[68:71], a[0:3], v[128:131]
v_add_u32 v24, 32768, v24
v_add_u32 v25, 32768, v25
v_mfma_f32_16x16x32_bf16 v[132:135], v[72:75], v[48:51], v[132:135]
ds_read_b64_tr_b16 v[100:101], v30 offset:24576
ds_read_b64_tr_b16 v[102:103], v30 offset:25600
v_mfma_f32_16x16x32_bf16 v[132:135], v[76:79], a[0:3], v[132:135]
buffer_load_dwordx4 v22, s[12:15], s25 offen offset:2048 lds
v_mul_f32 v36, s47, v36
v_mfma_f32_16x16x32_bf16 v[136:139], v[80:83], v[48:51], v[136:139]
ds_read_b64_tr_b16 v[104:105], v31 offset:16384
ds_read_b64_tr_b16 v[106:107], v31 offset:17408
v_mfma_f32_16x16x32_bf16 v[136:139], v[84:87], a[0:3], v[136:139]
v_mul_f32 v37, s47, v37
v_cvt_pk_bf16_f32 v40, v36, v37
v_mfma_f32_16x16x32_bf16 v[140:143], v[88:91], v[48:51], v[140:143]
ds_read_b64_tr_b16 v[108:109], v31 offset:24576
ds_read_b64_tr_b16 v[110:111], v31 offset:25600
v_mfma_f32_16x16x32_bf16 v[140:143], v[92:95], a[0:3], v[140:143]
buffer_load_dwordx4 v23, s[12:15], s25 offen offset:3072 lds
v_accvgpr_read_b32 v38, a226
v_mfma_f32_16x16x32_bf16 v[160:163], v[64:67], v[52:55], v[160:163]
v_xor_b32 v26, 0x10000, v26
v_xor_b32 v27, 0x10000, v27
v_mfma_f32_16x16x32_bf16 v[160:163], v[68:71], a[4:7], v[160:163]
s_add_u32 s24, s24, 128
s_add_u32 s25, s25, 128
v_accvgpr_read_b32 v39, a227
v_mfma_f32_16x16x32_bf16 v[164:167], v[72:75], v[52:55], v[164:167]
ds_read_b64_tr_b16 v[112:113], v32 offset:16384
ds_read_b64_tr_b16 v[114:115], v32 offset:17408
v_mfma_f32_16x16x32_bf16 v[164:167], v[76:79], a[4:7], v[164:167]
v_mul_f32 v38, s47, v38
v_mul_f32 v39, s47, v39
v_mfma_f32_16x16x32_bf16 v[168:171], v[80:83], v[52:55], v[168:171]
s_add_u32 m0, s91, 0
ds_read_b64_tr_b16 v[116:117], v32 offset:24576
ds_read_b64_tr_b16 v[118:119], v32 offset:25600
v_mfma_f32_16x16x32_bf16 v[168:171], v[84:87], a[4:7], v[168:171]
buffer_load_dwordx4 v16, s[16:19], s26 offen offset:0 lds
v_cvt_pk_bf16_f32 v41, v38, v39
v_mfma_f32_16x16x32_bf16 v[172:175], v[88:91], v[52:55], v[172:175]
ds_read_b64_tr_b16 v[120:121], v33 offset:16384
ds_read_b64_tr_b16 v[122:123], v33 offset:17408
v_mfma_f32_16x16x32_bf16 v[172:175], v[92:95], a[4:7], v[172:175]
v_accvgpr_read_b32 v36, a228
v_accvgpr_read_b32 v37, a229
v_mfma_f32_16x16x32_bf16 v[192:195], v[64:67], v[56:59], v[192:195]
v_xor_b32 v28, 0x10000, v28
v_xor_b32 v29, 0x10000, v29
v_mfma_f32_16x16x32_bf16 v[192:195], v[68:71], a[8:11], v[192:195]
buffer_load_dwordx4 v17, s[16:19], s26 offen offset:1024 lds
v_mul_f32 v36, s47, v36
v_mfma_f32_16x16x32_bf16 v[196:199], v[72:75], v[56:59], v[196:199]
ds_read_b64_tr_b16 v[124:125], v33 offset:24576
ds_read_b64_tr_b16 v[126:127], v33 offset:25600
v_mfma_f32_16x16x32_bf16 v[196:199], v[76:79], a[8:11], v[196:199]
v_mul_f32 v37, s47, v37
v_cvt_pk_bf16_f32 v42, v36, v37
v_mfma_f32_16x16x32_bf16 v[200:203], v[80:83], v[56:59], v[200:203]
v_accvgpr_read_b32 v38, a230
v_accvgpr_read_b32 v39, a231
v_mfma_f32_16x16x32_bf16 v[200:203], v[84:87], a[8:11], v[200:203]
buffer_load_dwordx4 v18, s[16:19], s26 offen offset:2048 lds
v_mul_f32 v38, s47, v38
v_mfma_f32_16x16x32_bf16 v[204:207], v[88:91], v[56:59], v[204:207]
s_waitcnt lgkmcnt(15)
v_mul_f32 v39, s47, v39
v_cvt_pk_bf16_f32 v43, v38, v39
v_mfma_f32_16x16x32_bf16 v[204:207], v[92:95], a[8:11], v[204:207]
v_xor_b32 v30, 0x10000, v30
v_xor_b32 v31, 0x10000, v31
v_mfma_f32_16x16x32_bf16 v[224:227], v[64:67], v[60:63], v[224:227]
v_accvgpr_write_b32 a224, v40
v_accvgpr_write_b32 a225, v41
v_mfma_f32_16x16x32_bf16 v[224:227], v[68:71], a[12:15], v[224:227]
buffer_load_dwordx4 v19, s[16:19], s26 offen offset:3072 lds
v_accvgpr_write_b32 a226, v42
v_mfma_f32_16x16x32_bf16 v[228:231], v[72:75], v[60:63], v[228:231]
v_accvgpr_write_b32 a227, v43
v_accvgpr_read_b32 v36, a80
v_mfma_f32_16x16x32_bf16 v[228:231], v[76:79], a[12:15], v[228:231]
s_add_u32 s26, s26, 524288
v_xor_b32 v32, 0x10000, v32
v_xor_b32 v33, 0x10000, v33
v_mfma_f32_16x16x32_bf16 v[232:235], v[80:83], v[60:63], v[232:235]
v_accvgpr_read_b32 v37, a81
v_mul_f32 v36, s47, v36
v_mfma_f32_16x16x32_bf16 v[232:235], v[84:87], a[12:15], v[232:235]
v_mul_f32 v37, s47, v37
v_cvt_pk_bf16_f32 v44, v36, v37
v_mfma_f32_16x16x32_bf16 v[236:239], v[88:91], v[60:63], v[236:239]
v_accvgpr_read_b32 v38, a82
v_accvgpr_read_b32 v39, a83
v_mfma_f32_16x16x32_bf16 v[236:239], v[92:95], a[12:15], v[236:239]
s_waitcnt vmcnt(33)
s_waitcnt vmcnt(17) lgkmcnt(0)
s_barrier
v_mul_f32 v38, s47, v38
v_mfma_f32_16x16x32_bf16 v[144:147], v[96:99], v[48:51], v[144:147]
ds_read_b64_tr_b16 v[64:65], v26 offset:0
ds_read_b64_tr_b16 v[66:67], v26 offset:1024
v_mfma_f32_16x16x32_bf16 v[144:147], v[100:103], a[0:3], v[144:147]
s_nop 0
v_mul_f32 v39, s47, v39
v_cvt_pk_bf16_f32 v45, v38, v39
v_or_b32 v0, 1, v11
v_mfma_f32_16x16x32_bf16 v[148:151], v[104:107], v[48:51], v[148:151]
s_add_u32 m0, s92, 98304
ds_read_b64_tr_b16 v[68:69], v26 offset:8192
ds_read_b64_tr_b16 v[70:71], v26 offset:9216
v_mfma_f32_16x16x32_bf16 v[148:151], v[108:111], a[0:3], v[148:151]
buffer_load_dwordx4 v20, s[12:15], s24 offen offset:0 lds
v_accvgpr_read_b32 v36, a84
v_mfma_f32_16x16x32_bf16 v[152:155], v[112:115], v[48:51], v[152:155]
ds_read_b64_tr_b16 v[72:73], v27 offset:0
ds_read_b64_tr_b16 v[74:75], v27 offset:1024
v_mfma_f32_16x16x32_bf16 v[152:155], v[116:119], a[0:3], v[152:155]
buffer_store_dword v0, v6, s[60:63], 0 offen sc1
v_accvgpr_read_b32 v37, a85
v_mul_f32 v36, s47, v36
v_mfma_f32_16x16x32_bf16 v[156:159], v[120:123], v[48:51], v[156:159]
ds_read_b64_tr_b16 v[76:77], v27 offset:8192
ds_read_b64_tr_b16 v[78:79], v27 offset:9216
v_mfma_f32_16x16x32_bf16 v[156:159], v[124:127], a[0:3], v[156:159]
buffer_load_dwordx4 v21, s[12:15], s24 offen offset:1024 lds
v_mul_f32 v37, s47, v37
v_mfma_f32_16x16x32_bf16 v[176:179], v[96:99], v[52:55], v[176:179]
ds_read_b128 v[48:51], v24 offset:0
v_cvt_pk_bf16_f32 v46, v36, v37
v_mfma_f32_16x16x32_bf16 v[176:179], v[100:103], a[4:7], v[176:179]
ds_read_b128 a[0:3], v25 offset:0
v_accvgpr_read_b32 v38, a86
v_mfma_f32_16x16x32_bf16 v[180:183], v[104:107], v[52:55], v[180:183]
ds_read_b64_tr_b16 v[80:81], v28 offset:0
ds_read_b64_tr_b16 v[82:83], v28 offset:1024
v_mfma_f32_16x16x32_bf16 v[180:183], v[108:111], a[4:7], v[180:183]
v_accvgpr_read_b32 v39, a87
v_mul_f32 v38, s47, v38
v_mfma_f32_16x16x32_bf16 v[184:187], v[112:115], v[52:55], v[184:187]
ds_read_b64_tr_b16 v[84:85], v28 offset:8192
ds_read_b64_tr_b16 v[86:87], v28 offset:9216
v_mfma_f32_16x16x32_bf16 v[184:187], v[116:119], a[4:7], v[184:187]
buffer_load_dwordx4 v22, s[12:15], s24 offen offset:2048 lds
v_mul_f32 v39, s47, v39
v_mfma_f32_16x16x32_bf16 v[188:191], v[120:123], v[52:55], v[188:191]
ds_read_b64_tr_b16 v[88:89], v29 offset:0
ds_read_b64_tr_b16 v[90:91], v29 offset:1024
v_mfma_f32_16x16x32_bf16 v[188:191], v[124:127], a[4:7], v[188:191]
v_cvt_pk_bf16_f32 v47, v38, v39
v_accvgpr_read_b32 v36, a232
v_mfma_f32_16x16x32_bf16 v[208:211], v[96:99], v[56:59], v[208:211]
s_xor_b32 s48, s95, 133120
ds_read_b128 v[52:55], v24 offset:2048
v_mfma_f32_16x16x32_bf16 v[208:211], v[100:103], a[8:11], v[208:211]
ds_read_b128 a[4:7], v25 offset:2048
v_accvgpr_read_b32 v37, a233
v_mfma_f32_16x16x32_bf16 v[212:215], v[104:107], v[56:59], v[212:215]
ds_read_b64_tr_b16 v[92:93], v29 offset:8192
ds_read_b64_tr_b16 v[94:95], v29 offset:9216
v_mfma_f32_16x16x32_bf16 v[212:215], v[108:111], a[8:11], v[212:215]
buffer_load_dwordx4 v23, s[12:15], s24 offen offset:3072 lds
v_mul_f32 v36, s47, v36
v_mfma_f32_16x16x32_bf16 v[216:219], v[112:115], v[56:59], v[216:219]
buffer_store_dwordx4 v[44:47], v7, s[20:23], s48 offen sc1
v_mul_f32 v37, s47, v37
v_mfma_f32_16x16x32_bf16 v[216:219], v[116:119], a[8:11], v[216:219]
v_cvt_pk_bf16_f32 v40, v36, v37
v_accvgpr_read_b32 v38, a234
v_mfma_f32_16x16x32_bf16 v[220:223], v[120:123], v[56:59], v[220:223]
s_add_u32 m0, s92, 102400
v_accvgpr_read_b32 v39, a235
v_mul_f32 v38, s47, v38
v_mfma_f32_16x16x32_bf16 v[220:223], v[124:127], a[8:11], v[220:223]
buffer_load_dwordx4 v20, s[12:15], s25 offen offset:0 lds
v_mul_f32 v39, s47, v39
v_mfma_f32_16x16x32_bf16 v[240:243], v[96:99], v[60:63], v[240:243]
ds_read_b128 v[56:59], v24 offset:4096
v_cvt_pk_bf16_f32 v41, v38, v39
v_mfma_f32_16x16x32_bf16 v[240:243], v[100:103], a[12:15], v[240:243]
ds_read_b128 a[8:11], v25 offset:4096
v_accvgpr_read_b32 v36, a236
v_mfma_f32_16x16x32_bf16 v[244:247], v[104:107], v[60:63], v[244:247]
v_accvgpr_read_b32 v37, a237
v_mul_f32 v36, s47, v36
v_mfma_f32_16x16x32_bf16 v[244:247], v[108:111], a[12:15], v[244:247]
buffer_load_dwordx4 v21, s[12:15], s25 offen offset:1024 lds
v_mul_f32 v37, s47, v37
v_mfma_f32_16x16x32_bf16 v[248:251], v[112:115], v[60:63], v[248:251]
.Lpad7:
s_cbranch_execz .Ltch1
v_cvt_pk_bf16_f32 v42, v36, v37
v_accvgpr_read_b32 v38, a238
v_mfma_f32_16x16x32_bf16 v[248:251], v[116:119], a[12:15], v[248:251]
s_waitcnt lgkmcnt(0)
v_accvgpr_read_b32 v39, a239
v_mul_f32 v38, s47, v38
v_mfma_f32_16x16x32_bf16 v[252:255], v[120:123], v[60:63], v[252:255]
ds_read_b128 v[60:63], v24 offset:6144
v_mul_f32 v39, s47, v39
v_mfma_f32_16x16x32_bf16 v[252:255], v[124:127], a[12:15], v[252:255]
ds_read_b128 a[12:15], v25 offset:6144
v_cvt_pk_bf16_f32 v43, v38, v39
v_mfma_f32_16x16x32_bf16 v[128:131], v[64:67], v[48:51], v[128:131]
ds_read_b64_tr_b16 v[96:97], v30 offset:0
ds_read_b64_tr_b16 v[98:99], v30 offset:1024
v_mfma_f32_16x16x32_bf16 v[128:131], v[68:71], a[0:3], v[128:131]
v_add_u32 v24, 4294868992, v24
v_add_u32 v25, 4294868992, v25
v_mfma_f32_16x16x32_bf16 v[132:135], v[72:75], v[48:51], v[132:135]
ds_read_b64_tr_b16 v[100:101], v30 offset:8192
ds_read_b64_tr_b16 v[102:103], v30 offset:9216
v_mfma_f32_16x16x32_bf16 v[132:135], v[76:79], a[0:3], v[132:135]
s_lshl_b32 s28, s88, 19
buffer_load_dwordx4 v22, s[12:15], s25 offen offset:2048 lds
v_mfma_f32_16x16x32_bf16 v[136:139], v[80:83], v[48:51], v[136:139]
ds_read_b64_tr_b16 v[104:105], v31 offset:0
ds_read_b64_tr_b16 v[106:107], v31 offset:1024
v_mfma_f32_16x16x32_bf16 v[136:139], v[84:87], a[0:3], v[136:139]
s_add_u32 s28, s28, s91
v_accvgpr_write_b32 a228, v40
v_accvgpr_write_b32 a229, v41
v_mfma_f32_16x16x32_bf16 v[140:143], v[88:91], v[48:51], v[140:143]
ds_read_b64_tr_b16 v[108:109], v31 offset:8192
ds_read_b64_tr_b16 v[110:111], v31 offset:9216
v_mfma_f32_16x16x32_bf16 v[140:143], v[92:95], a[0:3], v[140:143]
s_lshl_b32 s29, s89, 15
buffer_load_dwordx4 v23, s[12:15], s25 offen offset:3072 lds
v_mfma_f32_16x16x32_bf16 v[160:163], v[64:67], v[52:55], v[160:163]
s_sub_u32 s54, 0x80000, s29
v_accvgpr_write_b32 a230, v42
v_accvgpr_write_b32 a231, v43
v_mfma_f32_16x16x32_bf16 v[160:163], v[68:71], a[4:7], v[160:163]
s_add_u32 s24, s24, 128
s_add_u32 s25, s25, 128
v_accvgpr_read_b32 v36, a88
v_mfma_f32_16x16x32_bf16 v[164:167], v[72:75], v[52:55], v[164:167]
ds_read_b64_tr_b16 v[112:113], v32 offset:0
ds_read_b64_tr_b16 v[114:115], v32 offset:1024
v_mfma_f32_16x16x32_bf16 v[164:167], v[76:79], a[4:7], v[164:167]
s_sub_u32 s29, s54, s91
v_accvgpr_read_b32 v37, a89
v_mul_f32 v36, s47, v36
v_mfma_f32_16x16x32_bf16 v[168:171], v[80:83], v[52:55], v[168:171]
s_add_u32 m0, s91, 16384
ds_read_b64_tr_b16 v[116:117], v32 offset:8192
ds_read_b64_tr_b16 v[118:119], v32 offset:9216
v_mfma_f32_16x16x32_bf16 v[168:171], v[84:87], a[4:7], v[168:171]
buffer_load_dwordx4 v16, s[16:19], s26 offen offset:0 lds
v_mul_f32 v37, s47, v37
v_mfma_f32_16x16x32_bf16 v[172:175], v[88:91], v[52:55], v[172:175]
ds_read_b64_tr_b16 v[120:121], v33 offset:0
ds_read_b64_tr_b16 v[122:123], v33 offset:1024
v_mfma_f32_16x16x32_bf16 v[172:175], v[92:95], a[4:7], v[172:175]
s_add_u32 s4, s84, s28
s_addc_u32 s5, s85, 0
v_cvt_pk_bf16_f32 v44, v36, v37
v_mfma_f32_16x16x32_bf16 v[192:195], v[64:67], v[56:59], v[192:195]
s_and_b32 s5, s5, 0xffff
v_accvgpr_read_b32 v38, a90
v_accvgpr_read_b32 v39, a91
v_mfma_f32_16x16x32_bf16 v[192:195], v[68:71], a[8:11], v[192:195]
s_mov_b32 s6, s29
buffer_load_dwordx4 v17, s[16:19], s26 offen offset:1024 lds
v_mfma_f32_16x16x32_bf16 v[196:199], v[72:75], v[56:59], v[196:199]
ds_read_b64_tr_b16 v[124:125], v33 offset:8192
ds_read_b64_tr_b16 v[126:127], v33 offset:9216
v_mfma_f32_16x16x32_bf16 v[196:199], v[76:79], a[8:11], v[196:199]
s_mov_b32 s7, 0x20000
v_mul_f32 v38, s47, v38
v_mul_f32 v39, s47, v39
v_mfma_f32_16x16x32_bf16 v[200:203], v[80:83], v[56:59], v[200:203]
s_add_u32 s28, s28, 16760832
v_cvt_pk_bf16_f32 v45, v38, v39
v_accvgpr_read_b32 v36, a92
v_mfma_f32_16x16x32_bf16 v[200:203], v[84:87], a[8:11], v[200:203]
s_add_u32 s29, s29, 16384
buffer_load_dwordx4 v18, s[16:19], s26 offen offset:2048 lds
v_mfma_f32_16x16x32_bf16 v[204:207], v[88:91], v[56:59], v[204:207]
s_waitcnt lgkmcnt(15)
s_add_u32 s45, s54, 16384
v_accvgpr_read_b32 v37, a93
v_mfma_f32_16x16x32_bf16 v[204:207], v[92:95], a[8:11], v[204:207]
s_add_u32 s8, s84, s28
s_addc_u32 s9, s85, 0
v_mul_f32 v36, s47, v36
v_mfma_f32_16x16x32_bf16 v[224:227], v[64:67], v[60:63], v[224:227]
s_and_b32 s9, s9, 0xffff
v_mul_f32 v37, s47, v37
v_cvt_pk_bf16_f32 v46, v36, v37
v_mfma_f32_16x16x32_bf16 v[224:227], v[68:71], a[12:15], v[224:227]
s_mov_b32 s10, s29
buffer_load_dwordx4 v19, s[16:19], s26 offen offset:3072 lds
v_mfma_f32_16x16x32_bf16 v[228:231], v[72:75], v[60:63], v[228:231]
s_mov_b32 s11, 0x20000
v_accvgpr_read_b32 v38, a94
v_accvgpr_read_b32 v39, a95
v_mfma_f32_16x16x32_bf16 v[228:231], v[76:79], a[12:15], v[228:231]
s_add_u32 s26, s26, 524288
v_mul_f32 v38, s47, v38
v_mul_f32 v39, s47, v39
v_mfma_f32_16x16x32_bf16 v[232:235], v[80:83], v[60:63], v[232:235]
s_and_b32 s46, s89, 1
v_cvt_pk_bf16_f32 v47, v38, v39
v_accvgpr_read_b32 v36, a240
v_mfma_f32_16x16x32_bf16 v[232:235], v[84:87], a[12:15], v[232:235]
s_lshl_b32 s46, s46, 15
s_xor_b32 s48, s95, 134144
v_accvgpr_read_b32 v37, a241
v_mfma_f32_16x16x32_bf16 v[236:239], v[88:91], v[60:63], v[236:239]
s_add_u32 s46, s46, s91
buffer_store_dwordx4 v[44:47], v7, s[20:23], s48 offen sc1
v_mfma_f32_16x16x32_bf16 v[236:239], v[92:95], a[12:15], v[236:239]
s_waitcnt vmcnt(19) lgkmcnt(0)
s_barrier
s_mov_b32 s39, s46
v_mfma_f32_16x16x32_bf16 v[144:147], v[96:99], v[48:51], v[144:147]
ds_read_b64_tr_b16 v[64:65], v26 offset:16384
ds_read_b64_tr_b16 v[66:67], v26 offset:17408
v_mfma_f32_16x16x32_bf16 v[144:147], v[100:103], a[0:3], v[144:147]
s_mov_b32 s27, 0x30000
v_mul_f32 v36, s47, v36
v_mul_f32 v37, s47, v37
v_mfma_f32_16x16x32_bf16 v[148:151], v[104:107], v[48:51], v[148:151]
s_add_u32 m0, s92, 131072
ds_read_b64_tr_b16 v[68:69], v26 offset:24576
ds_read_b64_tr_b16 v[70:71], v26 offset:25600
v_mfma_f32_16x16x32_bf16 v[148:151], v[108:111], a[0:3], v[148:151]
buffer_load_dwordx4 v20, s[12:15], s24 offen offset:0 lds
v_cvt_pk_bf16_f32 v40, v36, v37
v_mfma_f32_16x16x32_bf16 v[152:155], v[112:115], v[48:51], v[152:155]
ds_read_b64_tr_b16 v[72:73], v27 offset:16384
ds_read_b64_tr_b16 v[74:75], v27 offset:17408
v_mfma_f32_16x16x32_bf16 v[152:155], v[116:119], a[0:3], v[152:155]
v_accvgpr_read_b32 v38, a242
v_accvgpr_read_b32 v39, a243
v_mfma_f32_16x16x32_bf16 v[156:159], v[120:123], v[48:51], v[156:159]
ds_read_b64_tr_b16 v[76:77], v27 offset:24576
ds_read_b64_tr_b16 v[78:79], v27 offset:25600
v_mfma_f32_16x16x32_bf16 v[156:159], v[124:127], a[0:3], v[156:159]
buffer_load_dwordx4 v21, s[12:15], s24 offen offset:1024 lds
v_mul_f32 v38, s47, v38
v_mfma_f32_16x16x32_bf16 v[176:179], v[96:99], v[52:55], v[176:179]
ds_read_b128 v[48:51], v24 offset:0
v_mul_f32 v39, s47, v39
v_mfma_f32_16x16x32_bf16 v[176:179], v[100:103], a[4:7], v[176:179]
ds_read_b128 a[0:3], v25 offset:0
v_cvt_pk_bf16_f32 v41, v38, v39
v_mfma_f32_16x16x32_bf16 v[180:183], v[104:107], v[52:55], v[180:183]
ds_read_b64_tr_b16 v[80:81], v28 offset:16384
ds_read_b64_tr_b16 v[82:83], v28 offset:17408
v_mfma_f32_16x16x32_bf16 v[180:183], v[108:111], a[4:7], v[180:183]
v_accvgpr_read_b32 v36, a244
v_accvgpr_read_b32 v37, a245
v_mfma_f32_16x16x32_bf16 v[184:187], v[112:115], v[52:55], v[184:187]
ds_read_b64_tr_b16 v[84:85], v28 offset:24576
ds_read_b64_tr_b16 v[86:87], v28 offset:25600
v_mfma_f32_16x16x32_bf16 v[184:187], v[116:119], a[4:7], v[184:187]
buffer_load_dwordx4 v22, s[12:15], s24 offen offset:2048 lds
v_mul_f32 v36, s47, v36
v_mfma_f32_16x16x32_bf16 v[188:191], v[120:123], v[52:55], v[188:191]
ds_read_b64_tr_b16 v[88:89], v29 offset:16384
ds_read_b64_tr_b16 v[90:91], v29 offset:17408
v_mfma_f32_16x16x32_bf16 v[188:191], v[124:127], a[4:7], v[188:191]
v_mul_f32 v37, s47, v37
v_cvt_pk_bf16_f32 v42, v36, v37
v_mfma_f32_16x16x32_bf16 v[208:211], v[96:99], v[56:59], v[208:211]
ds_read_b128 v[52:55], v24 offset:2048
v_accvgpr_read_b32 v38, a246
v_mfma_f32_16x16x32_bf16 v[208:211], v[100:103], a[8:11], v[208:211]
ds_read_b128 a[4:7], v25 offset:2048
v_accvgpr_read_b32 v39, a247
v_mfma_f32_16x16x32_bf16 v[212:215], v[104:107], v[56:59], v[212:215]
ds_read_b64_tr_b16 v[92:93], v29 offset:24576
ds_read_b64_tr_b16 v[94:95], v29 offset:25600
v_mfma_f32_16x16x32_bf16 v[212:215], v[108:111], a[8:11], v[212:215]
buffer_load_dwordx4 v23, s[12:15], s24 offen offset:3072 lds
v_mul_f32 v38, s47, v38
v_mfma_f32_16x16x32_bf16 v[216:219], v[112:115], v[56:59], v[216:219]
v_mul_f32 v39, s47, v39
v_cvt_pk_bf16_f32 v43, v38, v39
v_mfma_f32_16x16x32_bf16 v[216:219], v[116:119], a[8:11], v[216:219]
v_accvgpr_write_b32 a232, v40
v_accvgpr_write_b32 a233, v41
v_mfma_f32_16x16x32_bf16 v[220:223], v[120:123], v[56:59], v[220:223]
s_add_u32 m0, s92, 135168
v_accvgpr_write_b32 a234, v42
v_accvgpr_write_b32 a235, v43
v_mfma_f32_16x16x32_bf16 v[220:223], v[124:127], a[8:11], v[220:223]
buffer_load_dwordx4 v20, s[12:15], s25 offen offset:0 lds
v_mfma_f32_16x16x32_bf16 v[240:243], v[96:99], v[60:63], v[240:243]
ds_read_b128 v[56:59], v24 offset:4096
v_mfma_f32_16x16x32_bf16 v[240:243], v[100:103], a[12:15], v[240:243]
ds_read_b128 a[8:11], v25 offset:4096
v_mfma_f32_16x16x32_bf16 v[244:247], v[104:107], v[60:63], v[244:247]
v_mfma_f32_16x16x32_bf16 v[244:247], v[108:111], a[12:15], v[244:247]
buffer_load_dwordx4 v21, s[12:15], s25 offen offset:1024 lds
v_mfma_f32_16x16x32_bf16 v[248:251], v[112:115], v[60:63], v[248:251]
v_accvgpr_read_b32 v36, a96
v_accvgpr_read_b32 v37, a97
v_mfma_f32_16x16x32_bf16 v[248:251], v[116:119], a[12:15], v[248:251]
s_waitcnt lgkmcnt(0)
v_mul_f32 v36, s47, v36
v_mul_f32 v37, s47, v37
v_mfma_f32_16x16x32_bf16 v[252:255], v[120:123], v[60:63], v[252:255]
ds_read_b128 v[60:63], v24 offset:6144
v_cvt_pk_bf16_f32 v44, v36, v37
v_mfma_f32_16x16x32_bf16 v[252:255], v[124:127], a[12:15], v[252:255]
ds_read_b128 a[12:15], v25 offset:6144
v_accvgpr_read_b32 v38, a98
v_mfma_f32_16x16x32_bf16 v[128:131], v[64:67], v[48:51], v[128:131]
ds_read_b64_tr_b16 v[96:97], v30 offset:16384
ds_read_b64_tr_b16 v[98:99], v30 offset:17408
v_mfma_f32_16x16x32_bf16 v[128:131], v[68:71], a[0:3], v[128:131]
v_add_u32 v24, 65536, v24
v_add_u32 v25, 65536, v25
v_mfma_f32_16x16x32_bf16 v[132:135], v[72:75], v[48:51], v[132:135]
ds_read_b64_tr_b16 v[100:101], v30 offset:24576
ds_read_b64_tr_b16 v[102:103], v30 offset:25600
v_mfma_f32_16x16x32_bf16 v[132:135], v[76:79], a[0:3], v[132:135]
buffer_load_dwordx4 v22, s[12:15], s25 offen offset:2048 lds
v_accvgpr_read_b32 v39, a99
v_mfma_f32_16x16x32_bf16 v[136:139], v[80:83], v[48:51], v[136:139]
ds_read_b64_tr_b16 v[104:105], v31 offset:16384
ds_read_b64_tr_b16 v[106:107], v31 offset:17408
v_mfma_f32_16x16x32_bf16 v[136:139], v[84:87], a[0:3], v[136:139]
v_mul_f32 v38, s47, v38
v_mul_f32 v39, s47, v39
v_mfma_f32_16x16x32_bf16 v[140:143], v[88:91], v[48:51], v[140:143]
ds_read_b64_tr_b16 v[108:109], v31 offset:24576
ds_read_b64_tr_b16 v[110:111], v31 offset:25600
v_mfma_f32_16x16x32_bf16 v[140:143], v[92:95], a[0:3], v[140:143]
buffer_load_dwordx4 v23, s[12:15], s25 offen offset:3072 lds
v_cvt_pk_bf16_f32 v45, v38, v39
v_mfma_f32_16x16x32_bf16 v[160:163], v[64:67], v[52:55], v[160:163]
v_xor_b32 v26, 0x10000, v26
v_xor_b32 v27, 0x10000, v27
v_mfma_f32_16x16x32_bf16 v[160:163], v[68:71], a[4:7], v[160:163]
s_add_u32 s24, s24, 128
s_add_u32 s25, s25, 128
buffer_load_dword v35, v4, s[96:99], 0 offen
v_mfma_f32_16x16x32_bf16 v[164:167], v[72:75], v[52:55], v[164:167]
ds_read_b64_tr_b16 v[112:113], v32 offset:16384
ds_read_b64_tr_b16 v[114:115], v32 offset:17408
v_mfma_f32_16x16x32_bf16 v[164:167], v[76:79], a[4:7], v[164:167]
v_accvgpr_read_b32 v36, a100
v_accvgpr_read_b32 v37, a101
v_mfma_f32_16x16x32_bf16 v[168:171], v[80:83], v[52:55], v[168:171]
s_add_u32 m0, s91, 65536
ds_read_b64_tr_b16 v[116:117], v32 offset:24576
ds_read_b64_tr_b16 v[118:119], v32 offset:25600
v_mfma_f32_16x16x32_bf16 v[168:171], v[84:87], a[4:7], v[168:171]
buffer_load_dwordx4 v16, s[16:19], s26 offen offset:0 lds
v_mul_f32 v36, s47, v36
v_mfma_f32_16x16x32_bf16 v[172:175], v[88:91], v[52:55], v[172:175]
ds_read_b64_tr_b16 v[120:121], v33 offset:16384
ds_read_b64_tr_b16 v[122:123], v33 offset:17408
v_mfma_f32_16x16x32_bf16 v[172:175], v[92:95], a[4:7], v[172:175]
v_mul_f32 v37, s47, v37
v_cvt_pk_bf16_f32 v46, v36, v37
v_mfma_f32_16x16x32_bf16 v[192:195], v[64:67], v[56:59], v[192:195]
v_xor_b32 v28, 0x10000, v28
v_xor_b32 v29, 0x10000, v29
v_mfma_f32_16x16x32_bf16 v[192:195], v[68:71], a[8:11], v[192:195]
buffer_load_dwordx4 v17, s[16:19], s26 offen offset:1024 lds
v_accvgpr_read_b32 v38, a102
v_mfma_f32_16x16x32_bf16 v[196:199], v[72:75], v[56:59], v[196:199]
ds_read_b64_tr_b16 v[124:125], v33 offset:24576
ds_read_b64_tr_b16 v[126:127], v33 offset:25600
v_mfma_f32_16x16x32_bf16 v[196:199], v[76:79], a[8:11], v[196:199]
v_accvgpr_read_b32 v39, a103
v_mul_f32 v38, s47, v38
v_mfma_f32_16x16x32_bf16 v[200:203], v[80:83], v[56:59], v[200:203]
v_mul_f32 v39, s47, v39
v_cvt_pk_bf16_f32 v47, v38, v39
v_mfma_f32_16x16x32_bf16 v[200:203], v[84:87], a[8:11], v[200:203]
s_xor_b32 s48, s95, 135168
buffer_load_dwordx4 v18, s[16:19], s26 offen offset:2048 lds
v_mfma_f32_16x16x32_bf16 v[204:207], v[88:91], v[56:59], v[204:207]
s_waitcnt lgkmcnt(15)
buffer_store_dwordx4 v[44:47], v7, s[20:23], s48 offen sc1
v_mfma_f32_16x16x32_bf16 v[204:207], v[92:95], a[8:11], v[204:207]
v_xor_b32 v30, 0x10000, v30
v_xor_b32 v31, 0x10000, v31
v_mfma_f32_16x16x32_bf16 v[224:227], v[64:67], v[60:63], v[224:227]
v_accvgpr_read_b32 v36, a248
v_accvgpr_read_b32 v37, a249
v_mfma_f32_16x16x32_bf16 v[224:227], v[68:71], a[12:15], v[224:227]
buffer_load_dwordx4 v19, s[16:19], s26 offen offset:3072 lds
v_mul_f32 v36, s47, v36
v_mfma_f32_16x16x32_bf16 v[228:231], v[72:75], v[60:63], v[228:231]
v_mul_f32 v37, s47, v37
v_cvt_pk_bf16_f32 v40, v36, v37
v_mfma_f32_16x16x32_bf16 v[228:231], v[76:79], a[12:15], v[228:231]
s_add_u32 s26, s26, 524288
v_xor_b32 v32, 0x10000, v32
v_xor_b32 v33, 0x10000, v33
v_mfma_f32_16x16x32_bf16 v[232:235], v[80:83], v[60:63], v[232:235]
v_accvgpr_read_b32 v38, a250
v_accvgpr_read_b32 v39, a251
v_mfma_f32_16x16x32_bf16 v[232:235], v[84:87], a[12:15], v[232:235]
v_mul_f32 v38, s47, v38
v_mul_f32 v39, s47, v39
v_mfma_f32_16x16x32_bf16 v[236:239], v[88:91], v[60:63], v[236:239]
v_cvt_pk_bf16_f32 v41, v38, v39
v_accvgpr_read_b32 v36, a252
v_mfma_f32_16x16x32_bf16 v[236:239], v[92:95], a[12:15], v[236:239]
s_waitcnt vmcnt(19) lgkmcnt(0)
s_barrier
v_accvgpr_read_b32 v37, a253
v_mfma_f32_16x16x32_bf16 v[144:147], v[96:99], v[48:51], v[144:147]
ds_read_b64_tr_b16 v[64:65], v26 offset:0
ds_read_b64_tr_b16 v[66:67], v26 offset:1024
v_mfma_f32_16x16x32_bf16 v[144:147], v[100:103], a[0:3], v[144:147]
v_mul_f32 v36, s47, v36
v_mul_f32 v37, s47, v37
v_mfma_f32_16x16x32_bf16 v[148:151], v[104:107], v[48:51], v[148:151]
s_add_u32 m0, s92, 32768
ds_read_b64_tr_b16 v[68:69], v26 offset:8192
ds_read_b64_tr_b16 v[70:71], v26 offset:9216
v_mfma_f32_16x16x32_bf16 v[148:151], v[108:111], a[0:3], v[148:151]
buffer_load_dwordx4 v20, s[12:15], s24 offen offset:0 lds
v_cvt_pk_bf16_f32 v42, v36, v37
v_mfma_f32_16x16x32_bf16 v[152:155], v[112:115], v[48:51], v[152:155]
ds_read_b64_tr_b16 v[72:73], v27 offset:0
ds_read_b64_tr_b16 v[74:75], v27 offset:1024
v_mfma_f32_16x16x32_bf16 v[152:155], v[116:119], a[0:3], v[152:155]
v_accvgpr_read_b32 v38, a254
v_accvgpr_read_b32 v39, a255
v_mfma_f32_16x16x32_bf16 v[156:159], v[120:123], v[48:51], v[156:159]
ds_read_b64_tr_b16 v[76:77], v27 offset:8192
ds_read_b64_tr_b16 v[78:79], v27 offset:9216
v_mfma_f32_16x16x32_bf16 v[156:159], v[124:127], a[0:3], v[156:159]
buffer_load_dwordx4 v21, s[12:15], s24 offen offset:1024 lds
v_mul_f32 v38, s47, v38
v_mfma_f32_16x16x32_bf16 v[176:179], v[96:99], v[52:55], v[176:179]
ds_read_b128 v[48:51], v24 offset:0
v_mul_f32 v39, s47, v39
v_mfma_f32_16x16x32_bf16 v[176:179], v[100:103], a[4:7], v[176:179]
ds_read_b128 a[0:3], v25 offset:0
v_cvt_pk_bf16_f32 v43, v38, v39
v_mfma_f32_16x16x32_bf16 v[180:183], v[104:107], v[52:55], v[180:183]
ds_read_b64_tr_b16 v[80:81], v28 offset:0
ds_read_b64_tr_b16 v[82:83], v28 offset:1024
v_mfma_f32_16x16x32_bf16 v[180:183], v[108:111], a[4:7], v[180:183]
v_accvgpr_write_b32 a236, v40
v_accvgpr_write_b32 a237, v41
v_mfma_f32_16x16x32_bf16 v[184:187], v[112:115], v[52:55], v[184:187]
ds_read_b64_tr_b16 v[84:85], v28 offset:8192
ds_read_b64_tr_b16 v[86:87], v28 offset:9216
v_mfma_f32_16x16x32_bf16 v[184:187], v[116:119], a[4:7], v[184:187]
buffer_load_dwordx4 v22, s[12:15], s24 offen offset:2048 lds
v_accvgpr_write_b32 a238, v42
v_mfma_f32_16x16x32_bf16 v[188:191], v[120:123], v[52:55], v[188:191]
ds_read_b64_tr_b16 v[88:89], v29 offset:0
ds_read_b64_tr_b16 v[90:91], v29 offset:1024
v_mfma_f32_16x16x32_bf16 v[188:191], v[124:127], a[4:7], v[188:191]
v_accvgpr_write_b32 a239, v43
v_mfma_f32_16x16x32_bf16 v[208:211], v[96:99], v[56:59], v[208:211]
ds_read_b128 v[52:55], v24 offset:2048
v_mfma_f32_16x16x32_bf16 v[208:211], v[100:103], a[8:11], v[208:211]
ds_read_b128 a[4:7], v25 offset:2048
v_mfma_f32_16x16x32_bf16 v[212:215], v[104:107], v[56:59], v[212:215]
ds_read_b64_tr_b16 v[92:93], v29 offset:8192
ds_read_b64_tr_b16 v[94:95], v29 offset:9216
v_mfma_f32_16x16x32_bf16 v[212:215], v[108:111], a[8:11], v[212:215]
buffer_load_dwordx4 v23, s[12:15], s24 offen offset:3072 lds
v_mfma_f32_16x16x32_bf16 v[216:219], v[112:115], v[56:59], v[216:219]
v_mfma_f32_16x16x32_bf16 v[216:219], v[116:119], a[8:11], v[216:219]
v_accvgpr_read_b32 v36, a104
v_accvgpr_read_b32 v37, a105
v_mfma_f32_16x16x32_bf16 v[220:223], v[120:123], v[56:59], v[220:223]
s_add_u32 m0, s92, 36864
v_mul_f32 v36, s47, v36
v_mul_f32 v37, s47, v37
v_mfma_f32_16x16x32_bf16 v[220:223], v[124:127], a[8:11], v[220:223]
buffer_load_dwordx4 v20, s[12:15], s25 offen offset:0 lds
v_cvt_pk_bf16_f32 v44, v36, v37
v_mfma_f32_16x16x32_bf16 v[240:243], v[96:99], v[60:63], v[240:243]
ds_read_b128 v[56:59], v24 offset:4096
v_accvgpr_read_b32 v38, a106
v_mfma_f32_16x16x32_bf16 v[240:243], v[100:103], a[12:15], v[240:243]
ds_read_b128 a[8:11], v25 offset:4096
v_accvgpr_read_b32 v39, a107
v_mfma_f32_16x16x32_bf16 v[244:247], v[104:107], v[60:63], v[244:247]
v_mul_f32 v38, s47, v38
v_mul_f32 v39, s47, v39
v_mfma_f32_16x16x32_bf16 v[244:247], v[108:111], a[12:15], v[244:247]
buffer_load_dwordx4 v21, s[12:15], s25 offen offset:1024 lds
v_cvt_pk_bf16_f32 v45, v38, v39
v_mfma_f32_16x16x32_bf16 v[248:251], v[112:115], v[60:63], v[248:251]
v_accvgpr_read_b32 v36, a108
v_accvgpr_read_b32 v37, a109
v_mfma_f32_16x16x32_bf16 v[248:251], v[116:119], a[12:15], v[248:251]
s_waitcnt lgkmcnt(0)
v_mul_f32 v36, s47, v36
v_mul_f32 v37, s47, v37
v_mfma_f32_16x16x32_bf16 v[252:255], v[120:123], v[60:63], v[252:255]
ds_read_b128 v[60:63], v24 offset:6144
v_cvt_pk_bf16_f32 v46, v36, v37
v_mfma_f32_16x16x32_bf16 v[252:255], v[124:127], a[12:15], v[252:255]
ds_read_b128 a[12:15], v25 offset:6144
v_accvgpr_read_b32 v38, a110
v_mfma_f32_16x16x32_bf16 v[128:131], v[64:67], v[48:51], v[128:131]
ds_read_b64_tr_b16 v[96:97], v30 offset:0
ds_read_b64_tr_b16 v[98:99], v30 offset:1024
v_mfma_f32_16x16x32_bf16 v[128:131], v[68:71], a[0:3], v[128:131]
v_add_u32 v24, 32768, v24
v_add_u32 v25, 32768, v25
v_mfma_f32_16x16x32_bf16 v[132:135], v[72:75], v[48:51], v[132:135]
ds_read_b64_tr_b16 v[100:101], v30 offset:8192
ds_read_b64_tr_b16 v[102:103], v30 offset:9216
v_mfma_f32_16x16x32_bf16 v[132:135], v[76:79], a[0:3], v[132:135]
buffer_load_dwordx4 v22, s[12:15], s25 offen offset:2048 lds
v_accvgpr_read_b32 v39, a111
v_mfma_f32_16x16x32_bf16 v[136:139], v[80:83], v[48:51], v[136:139]
ds_read_b64_tr_b16 v[104:105], v31 offset:0
ds_read_b64_tr_b16 v[106:107], v31 offset:1024
v_mfma_f32_16x16x32_bf16 v[136:139], v[84:87], a[0:3], v[136:139]
v_mul_f32 v38, s47, v38
v_mul_f32 v39, s47, v39
v_mfma_f32_16x16x32_bf16 v[140:143], v[88:91], v[48:51], v[140:143]
ds_read_b64_tr_b16 v[108:109], v31 offset:8192
ds_read_b64_tr_b16 v[110:111], v31 offset:9216
v_mfma_f32_16x16x32_bf16 v[140:143], v[92:95], a[0:3], v[140:143]
buffer_load_dwordx4 v23, s[12:15], s25 offen offset:3072 lds
v_cvt_pk_bf16_f32 v47, v38, v39
v_mfma_f32_16x16x32_bf16 v[160:163], v[64:67], v[52:55], v[160:163]
s_xor_b32 s48, s95, 136192
buffer_store_dwordx4 v[44:47], v7, s[20:23], s48 offen sc1
v_mfma_f32_16x16x32_bf16 v[160:163], v[68:71], a[4:7], v[160:163]
s_add_u32 s24, s24, 128
s_add_u32 s25, s25, 128
v_mfma_f32_16x16x32_bf16 v[164:167], v[72:75], v[52:55], v[164:167]
ds_read_b64_tr_b16 v[112:113], v32 offset:0
ds_read_b64_tr_b16 v[114:115], v32 offset:1024
v_mfma_f32_16x16x32_bf16 v[164:167], v[76:79], a[4:7], v[164:167]
v_mfma_f32_16x16x32_bf16 v[168:171], v[80:83], v[52:55], v[168:171]
s_add_u32 m0, s91, 81920
ds_read_b64_tr_b16 v[116:117], v32 offset:8192
ds_read_b64_tr_b16 v[118:119], v32 offset:9216
v_mfma_f32_16x16x32_bf16 v[168:171], v[84:87], a[4:7], v[168:171]
buffer_load_dwordx4 v16, s[16:19], s26 offen offset:0 lds
v_mfma_f32_16x16x32_bf16 v[172:175], v[88:91], v[52:55], v[172:175]
ds_read_b64_tr_b16 v[120:121], v33 offset:0
ds_read_b64_tr_b16 v[122:123], v33 offset:1024
v_mfma_f32_16x16x32_bf16 v[172:175], v[92:95], a[4:7], v[172:175]
v_mfma_f32_16x16x32_bf16 v[192:195], v[64:67], v[56:59], v[192:195]
v_mfma_f32_16x16x32_bf16 v[192:195], v[68:71], a[8:11], v[192:195]
buffer_load_dwordx4 v17, s[16:19], s26 offen offset:1024 lds
v_mfma_f32_16x16x32_bf16 v[196:199], v[72:75], v[56:59], v[196:199]
ds_read_b64_tr_b16 v[124:125], v33 offset:8192
ds_read_b64_tr_b16 v[126:127], v33 offset:9216
v_mfma_f32_16x16x32_bf16 v[196:199], v[76:79], a[8:11], v[196:199]
v_mfma_f32_16x16x32_bf16 v[200:203], v[80:83], v[56:59], v[200:203]
v_mfma_f32_16x16x32_bf16 v[200:203], v[84:87], a[8:11], v[200:203]
buffer_load_dwordx4 v18, s[16:19], s26 offen offset:2048 lds
v_mfma_f32_16x16x32_bf16 v[204:207], v[88:91], v[56:59], v[204:207]
s_waitcnt lgkmcnt(15)
v_mfma_f32_16x16x32_bf16 v[204:207], v[92:95], a[8:11], v[204:207]
v_mfma_f32_16x16x32_bf16 v[224:227], v[64:67], v[60:63], v[224:227]
v_mfma_f32_16x16x32_bf16 v[224:227], v[68:71], a[12:15], v[224:227]
buffer_load_dwordx4 v19, s[16:19], s26 offen offset:3072 lds
v_mfma_f32_16x16x32_bf16 v[228:231], v[72:75], v[60:63], v[228:231]
v_mfma_f32_16x16x32_bf16 v[228:231], v[76:79], a[12:15], v[228:231]
s_add_u32 s26, s26, 524288
v_mfma_f32_16x16x32_bf16 v[232:235], v[80:83], v[60:63], v[232:235]
v_mfma_f32_16x16x32_bf16 v[232:235], v[84:87], a[12:15], v[232:235]
v_mfma_f32_16x16x32_bf16 v[236:239], v[88:91], v[60:63], v[236:239]
v_mfma_f32_16x16x32_bf16 v[236:239], v[92:95], a[12:15], v[236:239]
s_waitcnt vmcnt(19) lgkmcnt(0)
s_barrier
v_mfma_f32_16x16x32_bf16 v[144:147], v[96:99], v[48:51], v[144:147]
ds_read_b64_tr_b16 v[64:65], v26 offset:16384
ds_read_b64_tr_b16 v[66:67], v26 offset:17408
v_mfma_f32_16x16x32_bf16 v[144:147], v[100:103], a[0:3], v[144:147]
v_mfma_f32_16x16x32_bf16 v[148:151], v[104:107], v[48:51], v[148:151]
s_add_u32 m0, s92, 98304
ds_read_b64_tr_b16 v[68:69], v26 offset:24576
ds_read_b64_tr_b16 v[70:71], v26 offset:25600
v_mfma_f32_16x16x32_bf16 v[148:151], v[108:111], a[0:3], v[148:151]
buffer_load_dwordx4 v20, s[12:15], s24 offen offset:0 lds
v_mfma_f32_16x16x32_bf16 v[152:155], v[112:115], v[48:51], v[152:155]
ds_read_b64_tr_b16 v[72:73], v27 offset:16384
ds_read_b64_tr_b16 v[74:75], v27 offset:17408
v_mfma_f32_16x16x32_bf16 v[152:155], v[116:119], a[0:3], v[152:155]
v_mfma_f32_16x16x32_bf16 v[156:159], v[120:123], v[48:51], v[156:159]
ds_read_b64_tr_b16 v[76:77], v27 offset:24576
ds_read_b64_tr_b16 v[78:79], v27 offset:25600
v_mfma_f32_16x16x32_bf16 v[156:159], v[124:127], a[0:3], v[156:159]
buffer_load_dwordx4 v21, s[12:15], s24 offen offset:1024 lds
v_mfma_f32_16x16x32_bf16 v[176:179], v[96:99], v[52:55], v[176:179]
ds_read_b128 v[48:51], v24 offset:0
v_mfma_f32_16x16x32_bf16 v[176:179], v[100:103], a[4:7], v[176:179]
ds_read_b128 a[0:3], v25 offset:0
v_mfma_f32_16x16x32_bf16 v[180:183], v[104:107], v[52:55], v[180:183]
ds_read_b64_tr_b16 v[80:81], v28 offset:16384
ds_read_b64_tr_b16 v[82:83], v28 offset:17408
v_mfma_f32_16x16x32_bf16 v[180:183], v[108:111], a[4:7], v[180:183]
v_mfma_f32_16x16x32_bf16 v[184:187], v[112:115], v[52:55], v[184:187]
ds_read_b64_tr_b16 v[84:85], v28 offset:24576
ds_read_b64_tr_b16 v[86:87], v28 offset:25600
v_mfma_f32_16x16x32_bf16 v[184:187], v[116:119], a[4:7], v[184:187]
buffer_load_dwordx4 v22, s[12:15], s24 offen offset:2048 lds
v_mfma_f32_16x16x32_bf16 v[188:191], v[120:123], v[52:55], v[188:191]
ds_read_b64_tr_b16 v[88:89], v29 offset:16384
ds_read_b64_tr_b16 v[90:91], v29 offset:17408
v_mfma_f32_16x16x32_bf16 v[188:191], v[124:127], a[4:7], v[188:191]
v_accvgpr_read_b32 v36, a112
v_accvgpr_read_b32 v37, a113
v_mfma_f32_16x16x32_bf16 v[208:211], v[96:99], v[56:59], v[208:211]
ds_read_b128 v[52:55], v24 offset:2048
v_mul_f32 v36, s47, v36
v_mfma_f32_16x16x32_bf16 v[208:211], v[100:103], a[8:11], v[208:211]
ds_read_b128 a[4:7], v25 offset:2048
v_mul_f32 v37, s47, v37
v_mfma_f32_16x16x32_bf16 v[212:215], v[104:107], v[56:59], v[212:215]
ds_read_b64_tr_b16 v[92:93], v29 offset:24576
ds_read_b64_tr_b16 v[94:95], v29 offset:25600
v_mfma_f32_16x16x32_bf16 v[212:215], v[108:111], a[8:11], v[212:215]
buffer_load_dwordx4 v23, s[12:15], s24 offen offset:3072 lds
v_cvt_pk_bf16_f32 v40, v36, v37
v_mfma_f32_16x16x32_bf16 v[216:219], v[112:115], v[56:59], v[216:219]
v_accvgpr_read_b32 v38, a114
v_accvgpr_read_b32 v39, a115
v_mfma_f32_16x16x32_bf16 v[216:219], v[116:119], a[8:11], v[216:219]
v_mul_f32 v38, s47, v38
v_mul_f32 v39, s47, v39
v_mfma_f32_16x16x32_bf16 v[220:223], v[120:123], v[56:59], v[220:223]
s_add_u32 m0, s92, 102400
v_cvt_pk_bf16_f32 v41, v38, v39
v_accvgpr_read_b32 v36, a116
v_mfma_f32_16x16x32_bf16 v[220:223], v[124:127], a[8:11], v[220:223]
buffer_load_dwordx4 v20, s[12:15], s25 offen offset:0 lds
v_accvgpr_read_b32 v37, a117
v_mfma_f32_16x16x32_bf16 v[240:243], v[96:99], v[60:63], v[240:243]
ds_read_b128 v[56:59], v24 offset:4096
.Lpad8:
s_cbranch_execz .Ltch2
v_mul_f32 v36, s47, v36
v_mfma_f32_16x16x32_bf16 v[240:243], v[100:103], a[12:15], v[240:243]
ds_read_b128 a[8:11], v25 offset:4096
v_mul_f32 v37, s47, v37
v_mfma_f32_16x16x32_bf16 v[244:247], v[104:107], v[60:63], v[244:247]
v_cvt_pk_bf16_f32 v42, v36, v37
v_accvgpr_read_b32 v38, a118
v_mfma_f32_16x16x32_bf16 v[244:247], v[108:111], a[12:15], v[244:247]
buffer_load_dwordx4 v21, s[12:15], s25 offen offset:1024 lds
v_accvgpr_read_b32 v39, a119
v_mfma_f32_16x16x32_bf16 v[248:251], v[112:115], v[60:63], v[248:251]
v_mul_f32 v38, s47, v38
v_mul_f32 v39, s47, v39
v_mfma_f32_16x16x32_bf16 v[248:251], v[116:119], a[12:15], v[248:251]
s_waitcnt lgkmcnt(0)
v_cvt_pk_bf16_f32 v43, v38, v39
v_mfma_f32_16x16x32_bf16 v[252:255], v[120:123], v[60:63], v[252:255]
s_xor_b32 s48, s95, 137216
ds_read_b128 v[60:63], v24 offset:6144
v_mfma_f32_16x16x32_bf16 v[252:255], v[124:127], a[12:15], v[252:255]
ds_read_b128 a[12:15], v25 offset:6144
v_mfma_f32_16x16x32_bf16 v[128:131], v[64:67], v[48:51], v[128:131]
ds_read_b64_tr_b16 v[96:97], v30 offset:16384
ds_read_b64_tr_b16 v[98:99], v30 offset:17408
v_mfma_f32_16x16x32_bf16 v[128:131], v[68:71], a[0:3], v[128:131]
v_add_u32 v24, 4294868992, v24
v_add_u32 v25, 4294868992, v25
v_mfma_f32_16x16x32_bf16 v[132:135], v[72:75], v[48:51], v[132:135]
ds_read_b64_tr_b16 v[100:101], v30 offset:24576
ds_read_b64_tr_b16 v[102:103], v30 offset:25600
v_mfma_f32_16x16x32_bf16 v[132:135], v[76:79], a[0:3], v[132:135]
buffer_load_dwordx4 v22, s[12:15], s25 offen offset:2048 lds
v_mfma_f32_16x16x32_bf16 v[136:139], v[80:83], v[48:51], v[136:139]
ds_read_b64_tr_b16 v[104:105], v31 offset:16384
ds_read_b64_tr_b16 v[106:107], v31 offset:17408
v_mfma_f32_16x16x32_bf16 v[136:139], v[84:87], a[0:3], v[136:139]
buffer_store_dwordx4 v[40:43], v7, s[20:23], s48 offen sc1
v_mfma_f32_16x16x32_bf16 v[140:143], v[88:91], v[48:51], v[140:143]
ds_read_b64_tr_b16 v[108:109], v31 offset:24576
ds_read_b64_tr_b16 v[110:111], v31 offset:25600
v_mfma_f32_16x16x32_bf16 v[140:143], v[92:95], a[0:3], v[140:143]
buffer_load_dwordx4 v23, s[12:15], s25 offen offset:3072 lds
v_mfma_f32_16x16x32_bf16 v[160:163], v[64:67], v[52:55], v[160:163]
v_xor_b32 v26, 0x10000, v26
v_xor_b32 v27, 0x10000, v27
v_mfma_f32_16x16x32_bf16 v[160:163], v[68:71], a[4:7], v[160:163]
s_add_u32 s24, s24, 128
s_add_u32 s25, s25, 128
v_mfma_f32_16x16x32_bf16 v[164:167], v[72:75], v[52:55], v[164:167]
ds_read_b64_tr_b16 v[112:113], v32 offset:16384
ds_read_b64_tr_b16 v[114:115], v32 offset:17408
v_mfma_f32_16x16x32_bf16 v[164:167], v[76:79], a[4:7], v[164:167]
v_mfma_f32_16x16x32_bf16 v[168:171], v[80:83], v[52:55], v[168:171]
s_add_u32 m0, s91, 0
ds_read_b64_tr_b16 v[116:117], v32 offset:24576
ds_read_b64_tr_b16 v[118:119], v32 offset:25600
v_mfma_f32_16x16x32_bf16 v[168:171], v[84:87], a[4:7], v[168:171]
buffer_load_dwordx4 v16, s[16:19], s26 offen offset:0 lds
v_mfma_f32_16x16x32_bf16 v[172:175], v[88:91], v[52:55], v[172:175]
ds_read_b64_tr_b16 v[120:121], v33 offset:16384
ds_read_b64_tr_b16 v[122:123], v33 offset:17408
v_mfma_f32_16x16x32_bf16 v[172:175], v[92:95], a[4:7], v[172:175]
v_mfma_f32_16x16x32_bf16 v[192:195], v[64:67], v[56:59], v[192:195]
v_xor_b32 v28, 0x10000, v28
v_xor_b32 v29, 0x10000, v29
v_mfma_f32_16x16x32_bf16 v[192:195], v[68:71], a[8:11], v[192:195]
buffer_load_dwordx4 v17, s[16:19], s26 offen offset:1024 lds
v_mfma_f32_16x16x32_bf16 v[196:199], v[72:75], v[56:59], v[196:199]
ds_read_b64_tr_b16 v[124:125], v33 offset:24576
ds_read_b64_tr_b16 v[126:127], v33 offset:25600
v_mfma_f32_16x16x32_bf16 v[196:199], v[76:79], a[8:11], v[196:199]
v_mfma_f32_16x16x32_bf16 v[200:203], v[80:83], v[56:59], v[200:203]
v_mfma_f32_16x16x32_bf16 v[200:203], v[84:87], a[8:11], v[200:203]
buffer_load_dwordx4 v18, s[16:19], s26 offen offset:2048 lds
v_mfma_f32_16x16x32_bf16 v[204:207], v[88:91], v[56:59], v[204:207]
s_waitcnt lgkmcnt(15)
v_mfma_f32_16x16x32_bf16 v[204:207], v[92:95], a[8:11], v[204:207]
v_xor_b32 v30, 0x10000, v30
v_xor_b32 v31, 0x10000, v31
v_mfma_f32_16x16x32_bf16 v[224:227], v[64:67], v[60:63], v[224:227]
v_mfma_f32_16x16x32_bf16 v[224:227], v[68:71], a[12:15], v[224:227]
buffer_load_dwordx4 v19, s[16:19], s26 offen offset:3072 lds
v_mfma_f32_16x16x32_bf16 v[228:231], v[72:75], v[60:63], v[228:231]
v_mfma_f32_16x16x32_bf16 v[228:231], v[76:79], a[12:15], v[228:231]
s_add_u32 s26, s26, 524288
v_xor_b32 v32, 0x10000, v32
v_xor_b32 v33, 0x10000, v33
v_mfma_f32_16x16x32_bf16 v[232:235], v[80:83], v[60:63], v[232:235]
v_mfma_f32_16x16x32_bf16 v[232:235], v[84:87], a[12:15], v[232:235]
v_mfma_f32_16x16x32_bf16 v[236:239], v[88:91], v[60:63], v[236:239]
v_mfma_f32_16x16x32_bf16 v[236:239], v[92:95], a[12:15], v[236:239]
s_waitcnt vmcnt(18) lgkmcnt(0)
s_barrier
v_mfma_f32_16x16x32_bf16 v[144:147], v[96:99], v[48:51], v[144:147]
ds_read_b64_tr_b16 v[64:65], v26 offset:0
ds_read_b64_tr_b16 v[66:67], v26 offset:1024
v_mfma_f32_16x16x32_bf16 v[144:147], v[100:103], a[0:3], v[144:147]
v_mfma_f32_16x16x32_bf16 v[148:151], v[104:107], v[48:51], v[148:151]
s_add_u32 m0, s92, 131072
ds_read_b64_tr_b16 v[68:69], v26 offset:8192
ds_read_b64_tr_b16 v[70:71], v26 offset:9216
v_mfma_f32_16x16x32_bf16 v[148:151], v[108:111], a[0:3], v[148:151]
buffer_load_dwordx4 v20, s[12:15], s24 offen offset:0 lds
v_mfma_f32_16x16x32_bf16 v[152:155], v[112:115], v[48:51], v[152:155]
ds_read_b64_tr_b16 v[72:73], v27 offset:0
ds_read_b64_tr_b16 v[74:75], v27 offset:1024
v_mfma_f32_16x16x32_bf16 v[152:155], v[116:119], a[0:3], v[152:155]
v_mfma_f32_16x16x32_bf16 v[156:159], v[120:123], v[48:51], v[156:159]
ds_read_b64_tr_b16 v[76:77], v27 offset:8192
ds_read_b64_tr_b16 v[78:79], v27 offset:9216
v_mfma_f32_16x16x32_bf16 v[156:159], v[124:127], a[0:3], v[156:159]
buffer_load_dwordx4 v21, s[12:15], s24 offen offset:1024 lds
v_accvgpr_read_b32 v36, a120
v_mfma_f32_16x16x32_bf16 v[176:179], v[96:99], v[52:55], v[176:179]
ds_read_b128 v[48:51], v24 offset:0
v_accvgpr_read_b32 v37, a121
v_mfma_f32_16x16x32_bf16 v[176:179], v[100:103], a[4:7], v[176:179]
ds_read_b128 a[0:3], v25 offset:0
v_mul_f32 v36, s47, v36
v_mfma_f32_16x16x32_bf16 v[180:183], v[104:107], v[52:55], v[180:183]
ds_read_b64_tr_b16 v[80:81], v28 offset:0
ds_read_b64_tr_b16 v[82:83], v28 offset:1024
v_mfma_f32_16x16x32_bf16 v[180:183], v[108:111], a[4:7], v[180:183]
v_mul_f32 v37, s47, v37
v_cvt_pk_bf16_f32 v44, v36, v37
v_mfma_f32_16x16x32_bf16 v[184:187], v[112:115], v[52:55], v[184:187]
ds_read_b64_tr_b16 v[84:85], v28 offset:8192
ds_read_b64_tr_b16 v[86:87], v28 offset:9216
v_mfma_f32_16x16x32_bf16 v[184:187], v[116:119], a[4:7], v[184:187]
buffer_load_dwordx4 v22, s[12:15], s24 offen offset:2048 lds
v_accvgpr_read_b32 v38, a122
v_mfma_f32_16x16x32_bf16 v[188:191], v[120:123], v[52:55], v[188:191]
ds_read_b64_tr_b16 v[88:89], v29 offset:0
ds_read_b64_tr_b16 v[90:91], v29 offset:1024
v_mfma_f32_16x16x32_bf16 v[188:191], v[124:127], a[4:7], v[188:191]
v_accvgpr_read_b32 v39, a123
v_mul_f32 v38, s47, v38
v_mfma_f32_16x16x32_bf16 v[208:211], v[96:99], v[56:59], v[208:211]
ds_read_b128 v[52:55], v24 offset:2048
v_mul_f32 v39, s47, v39
v_mfma_f32_16x16x32_bf16 v[208:211], v[100:103], a[8:11], v[208:211]
ds_read_b128 a[4:7], v25 offset:2048
v_cvt_pk_bf16_f32 v45, v38, v39
v_mfma_f32_16x16x32_bf16 v[212:215], v[104:107], v[56:59], v[212:215]
ds_read_b64_tr_b16 v[92:93], v29 offset:8192
ds_read_b64_tr_b16 v[94:95], v29 offset:9216
v_mfma_f32_16x16x32_bf16 v[212:215], v[108:111], a[8:11], v[212:215]
buffer_load_dwordx4 v23, s[12:15], s24 offen offset:3072 lds
v_accvgpr_read_b32 v36, a124
v_mfma_f32_16x16x32_bf16 v[216:219], v[112:115], v[56:59], v[216:219]
v_accvgpr_read_b32 v37, a125
v_mul_f32 v36, s47, v36
v_mfma_f32_16x16x32_bf16 v[216:219], v[116:119], a[8:11], v[216:219]
v_mul_f32 v37, s47, v37
v_cvt_pk_bf16_f32 v46, v36, v37
v_mfma_f32_16x16x32_bf16 v[220:223], v[120:123], v[56:59], v[220:223]
s_add_u32 m0, s92, 135168
v_accvgpr_read_b32 v38, a126
v_accvgpr_read_b32 v39, a127
v_mfma_f32_16x16x32_bf16 v[220:223], v[124:127], a[8:11], v[220:223]
buffer_load_dwordx4 v20, s[12:15], s25 offen offset:0 lds
v_mul_f32 v38, s47, v38
v_mfma_f32_16x16x32_bf16 v[240:243], v[96:99], v[60:63], v[240:243]
ds_read_b128 v[56:59], v24 offset:4096
v_mul_f32 v39, s47, v39
v_mfma_f32_16x16x32_bf16 v[240:243], v[100:103], a[12:15], v[240:243]
ds_read_b128 a[8:11], v25 offset:4096
v_cvt_pk_bf16_f32 v47, v38, v39
v_mfma_f32_16x16x32_bf16 v[244:247], v[104:107], v[60:63], v[244:247]
s_xor_b32 s48, s95, 138240
buffer_store_dwordx4 v[44:47], v7, s[20:23], s48 offen sc1
v_mfma_f32_16x16x32_bf16 v[244:247], v[108:111], a[12:15], v[244:247]
buffer_load_dwordx4 v21, s[12:15], s25 offen offset:1024 lds
v_mfma_f32_16x16x32_bf16 v[248:251], v[112:115], v[60:63], v[248:251]
v_mfma_f32_16x16x32_bf16 v[248:251], v[116:119], a[12:15], v[248:251]
s_waitcnt lgkmcnt(0)
v_mfma_f32_16x16x32_bf16 v[252:255], v[120:123], v[60:63], v[252:255]
ds_read_b128 v[60:63], v24 offset:6144
v_mfma_f32_16x16x32_bf16 v[252:255], v[124:127], a[12:15], v[252:255]
ds_read_b128 a[12:15], v25 offset:6144
v_mfma_f32_16x16x32_bf16 v[128:131], v[64:67], v[48:51], v[128:131]
ds_read_b64_tr_b16 v[96:97], v30 offset:0
ds_read_b64_tr_b16 v[98:99], v30 offset:1024
v_mfma_f32_16x16x32_bf16 v[128:131], v[68:71], a[0:3], v[128:131]
v_add_u32 v24, 65536, v24
v_add_u32 v25, 65536, v25
v_mfma_f32_16x16x32_bf16 v[132:135], v[72:75], v[48:51], v[132:135]
ds_read_b64_tr_b16 v[100:101], v30 offset:8192
ds_read_b64_tr_b16 v[102:103], v30 offset:9216
v_mfma_f32_16x16x32_bf16 v[132:135], v[76:79], a[0:3], v[132:135]
buffer_load_dwordx4 v22, s[12:15], s25 offen offset:2048 lds
v_mfma_f32_16x16x32_bf16 v[136:139], v[80:83], v[48:51], v[136:139]
ds_read_b64_tr_b16 v[104:105], v31 offset:0
ds_read_b64_tr_b16 v[106:107], v31 offset:1024
v_mfma_f32_16x16x32_bf16 v[136:139], v[84:87], a[0:3], v[136:139]
v_mfma_f32_16x16x32_bf16 v[140:143], v[88:91], v[48:51], v[140:143]
ds_read_b64_tr_b16 v[108:109], v31 offset:8192
ds_read_b64_tr_b16 v[110:111], v31 offset:9216
v_mfma_f32_16x16x32_bf16 v[140:143], v[92:95], a[0:3], v[140:143]
buffer_load_dwordx4 v23, s[12:15], s25 offen offset:3072 lds
v_mfma_f32_16x16x32_bf16 v[160:163], v[64:67], v[52:55], v[160:163]
v_mfma_f32_16x16x32_bf16 v[160:163], v[68:71], a[4:7], v[160:163]
s_add_u32 s24, s24, 128
s_add_u32 s25, s25, 128
v_mfma_f32_16x16x32_bf16 v[164:167], v[72:75], v[52:55], v[164:167]
ds_read_b64_tr_b16 v[112:113], v32 offset:0
ds_read_b64_tr_b16 v[114:115], v32 offset:1024
v_mfma_f32_16x16x32_bf16 v[164:167], v[76:79], a[4:7], v[164:167]
v_mfma_f32_16x16x32_bf16 v[168:171], v[80:83], v[52:55], v[168:171]
s_add_u32 m0, s91, 16384
ds_read_b64_tr_b16 v[116:117], v32 offset:8192
ds_read_b64_tr_b16 v[118:119], v32 offset:9216
v_mfma_f32_16x16x32_bf16 v[168:171], v[84:87], a[4:7], v[168:171]
buffer_load_dwordx4 v16, s[16:19], s26 offen offset:0 lds
v_mfma_f32_16x16x32_bf16 v[172:175], v[88:91], v[52:55], v[172:175]
ds_read_b64_tr_b16 v[120:121], v33 offset:0
ds_read_b64_tr_b16 v[122:123], v33 offset:1024
v_mfma_f32_16x16x32_bf16 v[172:175], v[92:95], a[4:7], v[172:175]
v_mfma_f32_16x16x32_bf16 v[192:195], v[64:67], v[56:59], v[192:195]
v_mfma_f32_16x16x32_bf16 v[192:195], v[68:71], a[8:11], v[192:195]
buffer_load_dwordx4 v17, s[16:19], s26 offen offset:1024 lds
v_mfma_f32_16x16x32_bf16 v[196:199], v[72:75], v[56:59], v[196:199]
ds_read_b64_tr_b16 v[124:125], v33 offset:8192
ds_read_b64_tr_b16 v[126:127], v33 offset:9216
v_mfma_f32_16x16x32_bf16 v[196:199], v[76:79], a[8:11], v[196:199]
v_mfma_f32_16x16x32_bf16 v[200:203], v[80:83], v[56:59], v[200:203]
v_mfma_f32_16x16x32_bf16 v[200:203], v[84:87], a[8:11], v[200:203]
buffer_load_dwordx4 v18, s[16:19], s26 offen offset:2048 lds
v_mfma_f32_16x16x32_bf16 v[204:207], v[88:91], v[56:59], v[204:207]
s_waitcnt lgkmcnt(15)
v_mfma_f32_16x16x32_bf16 v[204:207], v[92:95], a[8:11], v[204:207]
v_mfma_f32_16x16x32_bf16 v[224:227], v[64:67], v[60:63], v[224:227]
v_mfma_f32_16x16x32_bf16 v[224:227], v[68:71], a[12:15], v[224:227]
buffer_load_dwordx4 v19, s[16:19], s26 offen offset:3072 lds
v_mfma_f32_16x16x32_bf16 v[228:231], v[72:75], v[60:63], v[228:231]
v_mfma_f32_16x16x32_bf16 v[228:231], v[76:79], a[12:15], v[228:231]
s_add_u32 s26, s26, 524288
v_mfma_f32_16x16x32_bf16 v[232:235], v[80:83], v[60:63], v[232:235]
v_mfma_f32_16x16x32_bf16 v[232:235], v[84:87], a[12:15], v[232:235]
buffer_load_dword v8, v3, s[60:63], s90 offen sc1
v_mfma_f32_16x16x32_bf16 v[236:239], v[88:91], v[60:63], v[236:239]
v_mfma_f32_16x16x32_bf16 v[236:239], v[92:95], a[12:15], v[236:239]
s_waitcnt vmcnt(18) lgkmcnt(0)
s_barrier
v_mfma_f32_16x16x32_bf16 v[144:147], v[96:99], v[48:51], v[144:147]
ds_read_b64_tr_b16 v[64:65], v26 offset:16384
ds_read_b64_tr_b16 v[66:67], v26 offset:17408
v_mfma_f32_16x16x32_bf16 v[144:147], v[100:103], a[0:3], v[144:147]
v_mfma_f32_16x16x32_bf16 v[148:151], v[104:107], v[48:51], v[148:151]
s_add_u32 m0, s92, 32768
ds_read_b64_tr_b16 v[68:69], v26 offset:24576
ds_read_b64_tr_b16 v[70:71], v26 offset:25600
v_mfma_f32_16x16x32_bf16 v[148:151], v[108:111], a[0:3], v[148:151]
buffer_load_dwordx4 v20, s[12:15], s24 offen offset:0 lds
v_mfma_f32_16x16x32_bf16 v[152:155], v[112:115], v[48:51], v[152:155]
ds_read_b64_tr_b16 v[72:73], v27 offset:16384
ds_read_b64_tr_b16 v[74:75], v27 offset:17408
v_mfma_f32_16x16x32_bf16 v[152:155], v[116:119], a[0:3], v[152:155]
v_mfma_f32_16x16x32_bf16 v[156:159], v[120:123], v[48:51], v[156:159]
ds_read_b64_tr_b16 v[76:77], v27 offset:24576
ds_read_b64_tr_b16 v[78:79], v27 offset:25600
v_mfma_f32_16x16x32_bf16 v[156:159], v[124:127], a[0:3], v[156:159]
buffer_load_dwordx4 v21, s[12:15], s24 offen offset:1024 lds
v_mfma_f32_16x16x32_bf16 v[176:179], v[96:99], v[52:55], v[176:179]
ds_read_b128 v[48:51], v24 offset:0
v_mfma_f32_16x16x32_bf16 v[176:179], v[100:103], a[4:7], v[176:179]
ds_read_b128 a[0:3], v25 offset:0
v_mfma_f32_16x16x32_bf16 v[180:183], v[104:107], v[52:55], v[180:183]
ds_read_b64_tr_b16 v[80:81], v28 offset:16384
ds_read_b64_tr_b16 v[82:83], v28 offset:17408
v_mfma_f32_16x16x32_bf16 v[180:183], v[108:111], a[4:7], v[180:183]
v_mfma_f32_16x16x32_bf16 v[184:187], v[112:115], v[52:55], v[184:187]
ds_read_b64_tr_b16 v[84:85], v28 offset:24576
ds_read_b64_tr_b16 v[86:87], v28 offset:25600
v_mfma_f32_16x16x32_bf16 v[184:187], v[116:119], a[4:7], v[184:187]
buffer_load_dwordx4 v22, s[12:15], s24 offen offset:2048 lds
v_mfma_f32_16x16x32_bf16 v[188:191], v[120:123], v[52:55], v[188:191]
ds_read_b64_tr_b16 v[88:89], v29 offset:16384
ds_read_b64_tr_b16 v[90:91], v29 offset:17408
v_mfma_f32_16x16x32_bf16 v[188:191], v[124:127], a[4:7], v[188:191]
v_mfma_f32_16x16x32_bf16 v[208:211], v[96:99], v[56:59], v[208:211]
ds_read_b128 v[52:55], v24 offset:2048
v_mfma_f32_16x16x32_bf16 v[208:211], v[100:103], a[8:11], v[208:211]
ds_read_b128 a[4:7], v25 offset:2048
v_mfma_f32_16x16x32_bf16 v[212:215], v[104:107], v[56:59], v[212:215]
ds_read_b64_tr_b16 v[92:93], v29 offset:24576
ds_read_b64_tr_b16 v[94:95], v29 offset:25600
v_mfma_f32_16x16x32_bf16 v[212:215], v[108:111], a[8:11], v[212:215]
buffer_load_dwordx4 v23, s[12:15], s24 offen offset:3072 lds
v_mfma_f32_16x16x32_bf16 v[216:219], v[112:115], v[56:59], v[216:219]
v_mfma_f32_16x16x32_bf16 v[216:219], v[116:119], a[8:11], v[216:219]
v_mfma_f32_16x16x32_bf16 v[220:223], v[120:123], v[56:59], v[220:223]
s_add_u32 m0, s92, 36864
v_mfma_f32_16x16x32_bf16 v[220:223], v[124:127], a[8:11], v[220:223]
buffer_load_dwordx4 v20, s[12:15], s25 offen offset:0 lds
v_mfma_f32_16x16x32_bf16 v[240:243], v[96:99], v[60:63], v[240:243]
ds_read_b128 v[56:59], v24 offset:4096
v_mfma_f32_16x16x32_bf16 v[240:243], v[100:103], a[12:15], v[240:243]
ds_read_b128 a[8:11], v25 offset:4096
v_mfma_f32_16x16x32_bf16 v[244:247], v[104:107], v[60:63], v[244:247]
v_mfma_f32_16x16x32_bf16 v[244:247], v[108:111], a[12:15], v[244:247]
buffer_load_dwordx4 v21, s[12:15], s25 offen offset:1024 lds
v_mfma_f32_16x16x32_bf16 v[248:251], v[112:115], v[60:63], v[248:251]
v_mfma_f32_16x16x32_bf16 v[248:251], v[116:119], a[12:15], v[248:251]
s_waitcnt lgkmcnt(0)
v_mfma_f32_16x16x32_bf16 v[252:255], v[120:123], v[60:63], v[252:255]
ds_read_b128 v[60:63], v24 offset:6144
v_mfma_f32_16x16x32_bf16 v[252:255], v[124:127], a[12:15], v[252:255]
ds_read_b128 a[12:15], v25 offset:6144
v_mfma_f32_16x16x32_bf16 v[128:131], v[64:67], v[48:51], v[128:131]
ds_read_b64_tr_b16 v[96:97], v30 offset:16384
ds_read_b64_tr_b16 v[98:99], v30 offset:17408
v_mfma_f32_16x16x32_bf16 v[128:131], v[68:71], a[0:3], v[128:131]
v_add_u32 v24, 32768, v24
v_add_u32 v25, 32768, v25
v_mfma_f32_16x16x32_bf16 v[132:135], v[72:75], v[48:51], v[132:135]
ds_read_b64_tr_b16 v[100:101], v30 offset:24576
ds_read_b64_tr_b16 v[102:103], v30 offset:25600
v_mfma_f32_16x16x32_bf16 v[132:135], v[76:79], a[0:3], v[132:135]
buffer_load_dwordx4 v22, s[12:15], s25 offen offset:2048 lds
v_mfma_f32_16x16x32_bf16 v[136:139], v[80:83], v[48:51], v[136:139]
ds_read_b64_tr_b16 v[104:105], v31 offset:16384
ds_read_b64_tr_b16 v[106:107], v31 offset:17408
v_mfma_f32_16x16x32_bf16 v[136:139], v[84:87], a[0:3], v[136:139]
v_mfma_f32_16x16x32_bf16 v[140:143], v[88:91], v[48:51], v[140:143]
ds_read_b64_tr_b16 v[108:109], v31 offset:24576
ds_read_b64_tr_b16 v[110:111], v31 offset:25600
v_mfma_f32_16x16x32_bf16 v[140:143], v[92:95], a[0:3], v[140:143]
buffer_load_dwordx4 v23, s[12:15], s25 offen offset:3072 lds
v_mfma_f32_16x16x32_bf16 v[160:163], v[64:67], v[52:55], v[160:163]
v_xor_b32 v26, 0x10000, v26
v_xor_b32 v27, 0x10000, v27
v_mfma_f32_16x16x32_bf16 v[160:163], v[68:71], a[4:7], v[160:163]
s_add_u32 s24, s24, 128
s_add_u32 s25, s25, 128
v_mfma_f32_16x16x32_bf16 v[164:167], v[72:75], v[52:55], v[164:167]
ds_read_b64_tr_b16 v[112:113], v32 offset:16384
ds_read_b64_tr_b16 v[114:115], v32 offset:17408
v_mfma_f32_16x16x32_bf16 v[164:167], v[76:79], a[4:7], v[164:167]
v_mfma_f32_16x16x32_bf16 v[168:171], v[80:83], v[52:55], v[168:171]
ds_read_b64_tr_b16 v[116:117], v32 offset:24576
ds_read_b64_tr_b16 v[118:119], v32 offset:25600
v_mfma_f32_16x16x32_bf16 v[168:171], v[84:87], a[4:7], v[168:171]
v_mfma_f32_16x16x32_bf16 v[172:175], v[88:91], v[52:55], v[172:175]
ds_read_b64_tr_b16 v[120:121], v33 offset:16384
ds_read_b64_tr_b16 v[122:123], v33 offset:17408
v_mfma_f32_16x16x32_bf16 v[172:175], v[92:95], a[4:7], v[172:175]
v_mfma_f32_16x16x32_bf16 v[192:195], v[64:67], v[56:59], v[192:195]
v_xor_b32 v28, 0x10000, v28
v_xor_b32 v29, 0x10000, v29
v_mfma_f32_16x16x32_bf16 v[192:195], v[68:71], a[8:11], v[192:195]
v_mfma_f32_16x16x32_bf16 v[196:199], v[72:75], v[56:59], v[196:199]
ds_read_b64_tr_b16 v[124:125], v33 offset:24576
ds_read_b64_tr_b16 v[126:127], v33 offset:25600
v_mfma_f32_16x16x32_bf16 v[196:199], v[76:79], a[8:11], v[196:199]
v_mfma_f32_16x16x32_bf16 v[200:203], v[80:83], v[56:59], v[200:203]
v_mfma_f32_16x16x32_bf16 v[200:203], v[84:87], a[8:11], v[200:203]
s_waitcnt vmcnt(8)
v_and_b32 v0, 7, v8
v_cmp_lt_u32 s[28:29], v0, v14
v_mfma_f32_16x16x32_bf16 v[204:207], v[88:91], v[56:59], v[204:207]
s_waitcnt lgkmcnt(15)
v_cmp_eq_u32 s[30:31], v8, v2
v_mfma_f32_16x16x32_bf16 v[204:207], v[92:95], a[8:11], v[204:207]
v_xor_b32 v30, 0x10000, v30
v_xor_b32 v31, 0x10000, v31
v_mfma_f32_16x16x32_bf16 v[224:227], v[64:67], v[60:63], v[224:227]
v_mfma_f32_16x16x32_bf16 v[224:227], v[68:71], a[12:15], v[224:227]
v_mfma_f32_16x16x32_bf16 v[228:231], v[72:75], v[60:63], v[228:231]
v_mfma_f32_16x16x32_bf16 v[228:231], v[76:79], a[12:15], v[228:231]
v_xor_b32 v32, 0x10000, v32
v_xor_b32 v33, 0x10000, v33
v_mfma_f32_16x16x32_bf16 v[232:235], v[80:83], v[60:63], v[232:235]
v_mfma_f32_16x16x32_bf16 v[232:235], v[84:87], a[12:15], v[232:235]
v_mfma_f32_16x16x32_bf16 v[236:239], v[88:91], v[60:63], v[236:239]
s_or_b64 s[28:29], s[28:29], s[30:31]
s_cbranch_scc1 .Lspin1
.Lspin1_ret:
s_add_u32 s28, s39, 0
s_and_b32 s28, s28, 0xffff
v_mfma_f32_16x16x32_bf16 v[236:239], v[92:95], a[12:15], v[236:239]
s_waitcnt vmcnt(13) lgkmcnt(0)
s_barrier
s_mov_b32 s40, 0
v_mfma_f32_16x16x32_bf16 v[144:147], v[96:99], v[48:51], v[144:147]
s_add_u32 m0, s28, 65536
ds_read_b64_tr_b16 v[64:65], v26 offset:0
ds_read_b64_tr_b16 v[66:67], v26 offset:1024
v_mfma_f32_16x16x32_bf16 v[144:147], v[100:103], a[0:3], v[144:147]
buffer_load_dwordx4 v7, s[4:7], s40 offen offset:0 sc1 lds
v_mfma_f32_16x16x32_bf16 v[148:151], v[104:107], v[48:51], v[148:151]
ds_read_b64_tr_b16 v[68:69], v26 offset:8192
ds_read_b64_tr_b16 v[70:71], v26 offset:9216
v_mfma_f32_16x16x32_bf16 v[148:151], v[108:111], a[0:3], v[148:151]
v_mfma_f32_16x16x32_bf16 v[152:155], v[112:115], v[48:51], v[152:155]
ds_read_b64_tr_b16 v[72:73], v27 offset:0
ds_read_b64_tr_b16 v[74:75], v27 offset:1024
v_mfma_f32_16x16x32_bf16 v[152:155], v[116:119], a[0:3], v[152:155]
buffer_load_dwordx4 v7, s[4:7], s40 offen offset:1024 sc1 lds
v_mfma_f32_16x16x32_bf16 v[156:159], v[120:123], v[48:51], v[156:159]
ds_read_b64_tr_b16 v[76:77], v27 offset:8192
ds_read_b64_tr_b16 v[78:79], v27 offset:9216
v_mfma_f32_16x16x32_bf16 v[156:159], v[124:127], a[0:3], v[156:159]
v_mfma_f32_16x16x32_bf16 v[176:179], v[96:99], v[52:55], v[176:179]
ds_read_b128 v[48:51], v24 offset:0
v_mfma_f32_16x16x32_bf16 v[176:179], v[100:103], a[4:7], v[176:179]
buffer_load_dwordx4 v7, s[4:7], s40 offen offset:2048 sc1 lds
ds_read_b128 a[0:3], v25 offset:0
v_mfma_f32_16x16x32_bf16 v[180:183], v[104:107], v[52:55], v[180:183]
ds_read_b64_tr_b16 v[80:81], v28 offset:0
ds_read_b64_tr_b16 v[82:83], v28 offset:1024
v_mfma_f32_16x16x32_bf16 v[180:183], v[108:111], a[4:7], v[180:183]
v_mfma_f32_16x16x32_bf16 v[184:187], v[112:115], v[52:55], v[184:187]
ds_read_b64_tr_b16 v[84:85], v28 offset:8192
ds_read_b64_tr_b16 v[86:87], v28 offset:9216
v_mfma_f32_16x16x32_bf16 v[184:187], v[116:119], a[4:7], v[184:187]
buffer_load_dwordx4 v7, s[4:7], s40 offen offset:3072 sc1 lds
v_mfma_f32_16x16x32_bf16 v[188:191], v[120:123], v[52:55], v[188:191]
ds_read_b64_tr_b16 v[88:89], v29 offset:0
ds_read_b64_tr_b16 v[90:91], v29 offset:1024
v_mfma_f32_16x16x32_bf16 v[188:191], v[124:127], a[4:7], v[188:191]
s_add_u32 s28, s39, 16384
s_and_b32 s28, s28, 0xffff
v_mfma_f32_16x16x32_bf16 v[208:211], v[96:99], v[56:59], v[208:211]
s_mov_b32 s41, 16384
ds_read_b128 v[52:55], v24 offset:2048
v_mfma_f32_16x16x32_bf16 v[208:211], v[100:103], a[8:11], v[208:211]
s_add_u32 m0, s28, 65536
ds_read_b128 a[4:7], v25 offset:2048
v_mfma_f32_16x16x32_bf16 v[212:215], v[104:107], v[56:59], v[212:215]
buffer_load_dwordx4 v7, s[4:7], s41 offen offset:0 sc1 lds
ds_read_b64_tr_b16 v[92:93], v29 offset:8192
ds_read_b64_tr_b16 v[94:95], v29 offset:9216
v_mfma_f32_16x16x32_bf16 v[212:215], v[108:111], a[8:11], v[212:215]
v_mfma_f32_16x16x32_bf16 v[216:219], v[112:115], v[56:59], v[216:219]
v_mfma_f32_16x16x32_bf16 v[216:219], v[116:119], a[8:11], v[216:219]
v_mfma_f32_16x16x32_bf16 v[220:223], v[120:123], v[56:59], v[220:223]
buffer_load_dwordx4 v7, s[4:7], s41 offen offset:1024 sc1 lds
v_mfma_f32_16x16x32_bf16 v[220:223], v[124:127], a[8:11], v[220:223]
v_mfma_f32_16x16x32_bf16 v[240:243], v[96:99], v[60:63], v[240:243]
ds_read_b128 v[56:59], v24 offset:4096
v_mfma_f32_16x16x32_bf16 v[240:243], v[100:103], a[12:15], v[240:243]
ds_read_b128 a[8:11], v25 offset:4096
v_mfma_f32_16x16x32_bf16 v[244:247], v[104:107], v[60:63], v[244:247]
buffer_load_dwordx4 v7, s[4:7], s41 offen offset:2048 sc1 lds
v_mfma_f32_16x16x32_bf16 v[244:247], v[108:111], a[12:15], v[244:247]
v_mfma_f32_16x16x32_bf16 v[248:251], v[112:115], v[60:63], v[248:251]
v_mfma_f32_16x16x32_bf16 v[248:251], v[116:119], a[12:15], v[248:251]
s_waitcnt lgkmcnt(0)
v_mfma_f32_16x16x32_bf16 v[252:255], v[120:123], v[60:63], v[252:255]
buffer_load_dwordx4 v7, s[4:7], s41 offen offset:3072 sc1 lds
ds_read_b128 v[60:63], v24 offset:6144
v_mfma_f32_16x16x32_bf16 v[252:255], v[124:127], a[12:15], v[252:255]
ds_read_b128 a[12:15], v25 offset:6144
v_mfma_f32_16x16x32_bf16 v[128:131], v[64:67], v[48:51], v[128:131]
s_add_u32 s28, s39, 32768
s_and_b32 s28, s28, 0xffff
ds_read_b64_tr_b16 v[96:97], v30 offset:0
ds_read_b64_tr_b16 v[98:99], v30 offset:1024
v_mfma_f32_16x16x32_bf16 v[128:131], v[68:71], a[0:3], v[128:131]
s_mov_b32 s40, 32768
v_add_u32 v24, 4294868992, v24
v_add_u32 v25, 4294868992, v25
v_mfma_f32_16x16x32_bf16 v[132:135], v[72:75], v[48:51], v[132:135]
s_add_u32 m0, s28, 65536
ds_read_b64_tr_b16 v[100:101], v30 offset:8192
ds_read_b64_tr_b16 v[102:103], v30 offset:9216
v_mfma_f32_16x16x32_bf16 v[132:135], v[76:79], a[0:3], v[132:135]
s_mov_b32 s14, 0
buffer_load_dwordx4 v7, s[4:7], s40 offen offset:0 sc1 lds
v_mfma_f32_16x16x32_bf16 v[136:139], v[80:83], v[48:51], v[136:139]
ds_read_b64_tr_b16 v[104:105], v31 offset:0
ds_read_b64_tr_b16 v[106:107], v31 offset:1024
v_mfma_f32_16x16x32_bf16 v[136:139], v[84:87], a[0:3], v[136:139]
s_mov_b32 s52, 2
s_mov_b32 s16, 0
s_mov_b32 s53, 0
v_mfma_f32_16x16x32_bf16 v[140:143], v[88:91], v[48:51], v[140:143]
ds_read_b64_tr_b16 v[108:109], v31 offset:8192
ds_read_b64_tr_b16 v[110:111], v31 offset:9216
v_mfma_f32_16x16x32_bf16 v[140:143], v[92:95], a[0:3], v[140:143]
s_and_b32 s38, s2, 31
buffer_load_dwordx4 v7, s[4:7], s40 offen offset:1024 sc1 lds
v_mfma_f32_16x16x32_bf16 v[160:163], v[64:67], v[52:55], v[160:163]
s_lshl_b32 s38, s38, 2
s_add_u32 s38, s38, s3
s_mul_i32 s38, s38, 6144
v_mfma_f32_16x16x32_bf16 v[160:163], v[68:71], a[4:7], v[160:163]
s_cmp_eq_u32 s89, 7
s_cselect_b64 s[12:13], s[80:81], s[12:13]
s_cselect_b32 s16, 8388608, s16
v_mfma_f32_16x16x32_bf16 v[164:167], v[72:75], v[52:55], v[164:167]
ds_read_b64_tr_b16 v[112:113], v32 offset:0
ds_read_b64_tr_b16 v[114:115], v32 offset:1024
v_mfma_f32_16x16x32_bf16 v[164:167], v[76:79], a[4:7], v[164:167]
buffer_load_dwordx4 v7, s[4:7], s40 offen offset:2048 sc1 lds
v_mfma_f32_16x16x32_bf16 v[168:171], v[80:83], v[52:55], v[168:171]
ds_read_b64_tr_b16 v[116:117], v32 offset:8192
ds_read_b64_tr_b16 v[118:119], v32 offset:9216
v_mfma_f32_16x16x32_bf16 v[168:171], v[84:87], a[4:7], v[168:171]
s_cmp_eq_u32 s89, 6
s_cselect_b64 s[12:13], s[66:67], s[12:13]
s_cselect_b32 s16, 6291456, s16
v_mfma_f32_16x16x32_bf16 v[172:175], v[88:91], v[52:55], v[172:175]
ds_read_b64_tr_b16 v[120:121], v33 offset:0
ds_read_b64_tr_b16 v[122:123], v33 offset:1024
v_mfma_f32_16x16x32_bf16 v[172:175], v[92:95], a[4:7], v[172:175]
buffer_load_dwordx4 v7, s[4:7], s40 offen offset:3072 sc1 lds
v_mfma_f32_16x16x32_bf16 v[192:195], v[64:67], v[56:59], v[192:195]
s_cmp_eq_u32 s89, 5
s_cselect_b64 s[12:13], s[64:65], s[12:13]
s_cselect_b32 s16, 4194304, s16
v_mfma_f32_16x16x32_bf16 v[192:195], v[68:71], a[8:11], v[192:195]
s_add_u32 s28, s39, 49152
s_and_b32 s28, s28, 0xffff
v_mfma_f32_16x16x32_bf16 v[196:199], v[72:75], v[56:59], v[196:199]
s_mov_b32 s41, 49152
ds_read_b64_tr_b16 v[124:125], v33 offset:8192
ds_read_b64_tr_b16 v[126:127], v33 offset:9216
v_mfma_f32_16x16x32_bf16 v[196:199], v[76:79], a[8:11], v[196:199]
s_add_u32 m0, s28, 65536
v_mfma_f32_16x16x32_bf16 v[200:203], v[80:83], v[56:59], v[200:203]
buffer_load_dwordx4 v7, s[4:7], s41 offen offset:0 sc1 lds
v_mfma_f32_16x16x32_bf16 v[200:203], v[84:87], a[8:11], v[200:203]
s_cmp_eq_u32 s89, 4
s_cselect_b64 s[12:13], s[68:69], s[12:13]
s_cselect_b32 s16, 2097152, s16
v_mfma_f32_16x16x32_bf16 v[204:207], v[88:91], v[56:59], v[204:207]
s_waitcnt lgkmcnt(15)
s_and_b32 s13, s13, 0xffff
v_mfma_f32_16x16x32_bf16 v[204:207], v[92:95], a[8:11], v[204:207]
v_min_u32 v9, 47, v1
v_lshlrev_b32 v9, 7, v9
v_mfma_f32_16x16x32_bf16 v[224:227], v[64:67], v[60:63], v[224:227]
buffer_load_dwordx4 v7, s[4:7], s41 offen offset:1024 sc1 lds
v_add_u32 v2, s38, v9
v_mfma_f32_16x16x32_bf16 v[224:227], v[68:71], a[12:15], v[224:227]
s_lshl_b32 s93, s2, 4
s_add_u32 s93, s93, s3
s_lshl_b32 s93, s93, 2
v_mfma_f32_16x16x32_bf16 v[228:231], v[72:75], v[60:63], v[228:231]
.Lpad9:
s_cbranch_execz .Ltch2
s_add_u32 s93, s93, 0x10000
v_mfma_f32_16x16x32_bf16 v[228:231], v[76:79], a[12:15], v[228:231]
v_lshlrev_b32 v9, 20, v1
v_add_u32 v5, s93, v9
v_mfma_f32_16x16x32_bf16 v[232:235], v[80:83], v[60:63], v[232:235]
buffer_load_dwordx4 v7, s[4:7], s41 offen offset:2048 sc1 lds
v_mfma_f32_16x16x32_bf16 v[232:235], v[84:87], a[12:15], v[232:235]
v_mfma_f32_16x16x32_bf16 v[236:239], v[88:91], v[60:63], v[236:239]
v_mfma_f32_16x16x32_bf16 v[236:239], v[92:95], a[12:15], v[236:239]
s_waitcnt vmcnt(31)
s_waitcnt vmcnt(15) lgkmcnt(0)
s_barrier
v_mfma_f32_16x16x32_bf16 v[144:147], v[96:99], v[48:51], v[144:147]
buffer_load_dwordx4 v7, s[4:7], s41 offen offset:3072 sc1 lds
ds_read_b64_tr_b16 v[64:65], v26 offset:16384
ds_read_b64_tr_b16 v[66:67], v26 offset:17408
v_mfma_f32_16x16x32_bf16 v[144:147], v[100:103], a[0:3], v[144:147]
v_or_b32 v0, 2, v11
v_mfma_f32_16x16x32_bf16 v[148:151], v[104:107], v[48:51], v[148:151]
ds_read_b64_tr_b16 v[68:69], v26 offset:24576
ds_read_b64_tr_b16 v[70:71], v26 offset:25600
v_mfma_f32_16x16x32_bf16 v[148:151], v[108:111], a[0:3], v[148:151]
buffer_store_dword v0, v6, s[60:63], 0 offen sc1
v_mfma_f32_16x16x32_bf16 v[152:155], v[112:115], v[48:51], v[152:155]
ds_read_b64_tr_b16 v[72:73], v27 offset:16384
ds_read_b64_tr_b16 v[74:75], v27 offset:17408
v_mfma_f32_16x16x32_bf16 v[152:155], v[116:119], a[0:3], v[152:155]
v_mfma_f32_16x16x32_bf16 v[156:159], v[120:123], v[48:51], v[156:159]
ds_read_b64_tr_b16 v[76:77], v27 offset:24576
ds_read_b64_tr_b16 v[78:79], v27 offset:25600
v_mfma_f32_16x16x32_bf16 v[156:159], v[124:127], a[0:3], v[156:159]
v_mfma_f32_16x16x32_bf16 v[176:179], v[96:99], v[52:55], v[176:179]
s_xor_b32 s48, s95, 262144
ds_read_b128 v[48:51], v24 offset:0
v_mfma_f32_16x16x32_bf16 v[176:179], v[100:103], a[4:7], v[176:179]
buffer_store_dwordx4 a[128:131], v7, s[20:23], s48 offen sc1
ds_read_b128 a[0:3], v25 offset:0
v_mfma_f32_16x16x32_bf16 v[180:183], v[104:107], v[52:55], v[180:183]
ds_read_b64_tr_b16 v[80:81], v28 offset:16384
ds_read_b64_tr_b16 v[82:83], v28 offset:17408
v_mfma_f32_16x16x32_bf16 v[180:183], v[108:111], a[4:7], v[180:183]
v_mfma_f32_16x16x32_bf16 v[184:187], v[112:115], v[52:55], v[184:187]
ds_read_b64_tr_b16 v[84:85], v28 offset:24576
ds_read_b64_tr_b16 v[86:87], v28 offset:25600
v_mfma_f32_16x16x32_bf16 v[184:187], v[116:119], a[4:7], v[184:187]
v_mfma_f32_16x16x32_bf16 v[188:191], v[120:123], v[52:55], v[188:191]
ds_read_b64_tr_b16 v[88:89], v29 offset:16384
ds_read_b64_tr_b16 v[90:91], v29 offset:17408
v_mfma_f32_16x16x32_bf16 v[188:191], v[124:127], a[4:7], v[188:191]
v_mfma_f32_16x16x32_bf16 v[208:211], v[96:99], v[56:59], v[208:211]
ds_read_b128 v[52:55], v24 offset:2048
v_mfma_f32_16x16x32_bf16 v[208:211], v[100:103], a[8:11], v[208:211]
ds_read_b128 a[4:7], v25 offset:2048
v_mfma_f32_16x16x32_bf16 v[212:215], v[104:107], v[56:59], v[212:215]
ds_read_b64_tr_b16 v[92:93], v29 offset:24576
ds_read_b64_tr_b16 v[94:95], v29 offset:25600
v_mfma_f32_16x16x32_bf16 v[212:215], v[108:111], a[8:11], v[212:215]
v_mfma_f32_16x16x32_bf16 v[216:219], v[112:115], v[56:59], v[216:219]
v_mfma_f32_16x16x32_bf16 v[216:219], v[116:119], a[8:11], v[216:219]
v_mfma_f32_16x16x32_bf16 v[220:223], v[120:123], v[56:59], v[220:223]
v_mfma_f32_16x16x32_bf16 v[220:223], v[124:127], a[8:11], v[220:223]
v_mfma_f32_16x16x32_bf16 v[240:243], v[96:99], v[60:63], v[240:243]
ds_read_b128 v[56:59], v24 offset:4096
v_mfma_f32_16x16x32_bf16 v[240:243], v[100:103], a[12:15], v[240:243]
s_xor_b32 s48, s95, 263168
ds_read_b128 a[8:11], v25 offset:4096
v_mfma_f32_16x16x32_bf16 v[244:247], v[104:107], v[60:63], v[244:247]
buffer_store_dwordx4 a[132:135], v7, s[20:23], s48 offen sc1
v_mfma_f32_16x16x32_bf16 v[244:247], v[108:111], a[12:15], v[244:247]
v_mfma_f32_16x16x32_bf16 v[248:251], v[112:115], v[60:63], v[248:251]
v_mfma_f32_16x16x32_bf16 v[248:251], v[116:119], a[12:15], v[248:251]
s_waitcnt lgkmcnt(0)
v_mfma_f32_16x16x32_bf16 v[252:255], v[120:123], v[60:63], v[252:255]
ds_read_b128 v[60:63], v24 offset:6144
v_mfma_f32_16x16x32_bf16 v[252:255], v[124:127], a[12:15], v[252:255]
ds_read_b128 a[12:15], v25 offset:6144
v_mfma_f32_16x16x32_bf16 v[128:131], v[64:67], v[48:51], v[128:131]
ds_read_b64_tr_b16 v[96:97], v30 offset:16384
ds_read_b64_tr_b16 v[98:99], v30 offset:17408
v_mfma_f32_16x16x32_bf16 v[128:131], v[68:71], a[0:3], v[128:131]
v_mfma_f32_16x16x32_bf16 v[132:135], v[72:75], v[48:51], v[132:135]
ds_read_b64_tr_b16 v[100:101], v30 offset:24576
ds_read_b64_tr_b16 v[102:103], v30 offset:25600
v_mfma_f32_16x16x32_bf16 v[132:135], v[76:79], a[0:3], v[132:135]
v_mfma_f32_16x16x32_bf16 v[136:139], v[80:83], v[48:51], v[136:139]
ds_read_b64_tr_b16 v[104:105], v31 offset:16384
ds_read_b64_tr_b16 v[106:107], v31 offset:17408
v_mfma_f32_16x16x32_bf16 v[136:139], v[84:87], a[0:3], v[136:139]
v_mfma_f32_16x16x32_bf16 v[140:143], v[88:91], v[48:51], v[140:143]
ds_read_b64_tr_b16 v[108:109], v31 offset:24576
ds_read_b64_tr_b16 v[110:111], v31 offset:25600
v_mfma_f32_16x16x32_bf16 v[140:143], v[92:95], a[0:3], v[140:143]
v_mfma_f32_16x16x32_bf16 v[160:163], v[64:67], v[52:55], v[160:163]
v_mfma_f32_16x16x32_bf16 v[160:163], v[68:71], a[4:7], v[160:163]
v_mfma_f32_16x16x32_bf16 v[164:167], v[72:75], v[52:55], v[164:167]
s_xor_b32 s48, s95, 264192
ds_read_b64_tr_b16 v[112:113], v32 offset:16384
ds_read_b64_tr_b16 v[114:115], v32 offset:17408
v_mfma_f32_16x16x32_bf16 v[164:167], v[76:79], a[4:7], v[164:167]
buffer_store_dwordx4 a[136:139], v7, s[20:23], s48 offen sc1
v_mfma_f32_16x16x32_bf16 v[168:171], v[80:83], v[52:55], v[168:171]
ds_read_b64_tr_b16 v[116:117], v32 offset:24576
ds_read_b64_tr_b16 v[118:119], v32 offset:25600
v_mfma_f32_16x16x32_bf16 v[168:171], v[84:87], a[4:7], v[168:171]
v_mfma_f32_16x16x32_bf16 v[172:175], v[88:91], v[52:55], v[172:175]
ds_read_b64_tr_b16 v[120:121], v33 offset:16384
ds_read_b64_tr_b16 v[122:123], v33 offset:17408
v_mfma_f32_16x16x32_bf16 v[172:175], v[92:95], a[4:7], v[172:175]
v_mfma_f32_16x16x32_bf16 v[192:195], v[64:67], v[56:59], v[192:195]
v_mfma_f32_16x16x32_bf16 v[192:195], v[68:71], a[8:11], v[192:195]
v_mfma_f32_16x16x32_bf16 v[196:199], v[72:75], v[56:59], v[196:199]
ds_read_b64_tr_b16 v[124:125], v33 offset:24576
ds_read_b64_tr_b16 v[126:127], v33 offset:25600
v_mfma_f32_16x16x32_bf16 v[196:199], v[76:79], a[8:11], v[196:199]
v_mfma_f32_16x16x32_bf16 v[200:203], v[80:83], v[56:59], v[200:203]
v_mfma_f32_16x16x32_bf16 v[200:203], v[84:87], a[8:11], v[200:203]
v_mfma_f32_16x16x32_bf16 v[204:207], v[88:91], v[56:59], v[204:207]
s_waitcnt lgkmcnt(15)
v_subrev_u32 v33, s91, v7
v_add_u32 v32, 0x10000, v33
v_mfma_f32_16x16x32_bf16 v[204:207], v[92:95], a[8:11], v[204:207]
v_add_u32 v12, s46, v32
v_and_b32 v8, 15, v1
v_mfma_f32_16x16x32_bf16 v[224:227], v[64:67], v[60:63], v[224:227]
v_lshrrev_b32 v9, 4, v1
v_lshlrev_b32 v9, 2, v9
v_mfma_f32_16x16x32_bf16 v[224:227], v[68:71], a[12:15], v[224:227]
v_sub_u32 v8, v8, v9
v_mov_b32 v34, 0xff800000
v_mfma_f32_16x16x32_bf16 v[228:231], v[72:75], v[60:63], v[228:231]
v_and_b32 v9, 3, v1
v_mov_b32 v10, 0x3f803f80
v_mfma_f32_16x16x32_bf16 v[228:231], v[76:79], a[12:15], v[228:231]
v_cmp_eq_u32 vcc, 0, v9
v_nop
v_mfma_f32_16x16x32_bf16 v[232:235], v[80:83], v[60:63], v[232:235]
v_nop
v_cndmask_b32 v14, 0, v10, vcc
v_mfma_f32_16x16x32_bf16 v[232:235], v[84:87], a[12:15], v[232:235]
s_add_u32 s49, s39, 16384
s_xor_b32 s46, s39, 0x8000
s_mov_b32 s44, 65536
v_mfma_f32_16x16x32_bf16 v[236:239], v[88:91], v[60:63], v[236:239]
s_add_u32 s28, s39, 0
s_and_b32 s28, s28, 0xffff
s_xor_b32 s50, s46, 0x18000
v_mfma_f32_16x16x32_bf16 v[236:239], v[92:95], a[12:15], v[236:239]
s_waitcnt lgkmcnt(0)
s_waitcnt vmcnt(4)
s_barrier
s_mov_b32 s40, 16384
v_mfma_f32_16x16x32_bf16 v[144:147], v[96:99], v[48:51], v[144:147]
s_add_u32 m0, s28, 0
s_lshl_b32 s30, s89, 2
s_add_u32 s51, s30, s94
v_mfma_f32_16x16x32_bf16 v[144:147], v[100:103], a[0:3], v[144:147]
s_max_u32 s29, s51, 3
buffer_load_dwordx4 v7, s[8:11], s40 offen offset:0 sc1 lds
v_mfma_f32_16x16x32_bf16 v[148:151], v[104:107], v[48:51], v[148:151]
s_sub_u32 s42, s29, 3
s_sub_u32 s56, 57, s30
s_add_u32 s56, s56, s3
v_mfma_f32_16x16x32_bf16 v[148:151], v[108:111], a[0:3], v[148:151]
s_mov_b32 s57, 2
s_min_u32 s43, s42, s57
s_sub_u32 s55, s43, 1
v_mfma_f32_16x16x32_bf16 v[152:155], v[112:115], v[48:51], v[152:155]
ds_read_b128 a[16:19], v12 offset:0
ds_read_b128 a[48:51], v12 offset:8192
v_mfma_f32_16x16x32_bf16 v[152:155], v[116:119], a[0:3], v[152:155]
s_sub_u32 s31, s51, s29
buffer_load_dwordx4 v7, s[8:11], s40 offen offset:1024 sc1 lds
v_mfma_f32_16x16x32_bf16 v[156:159], v[120:123], v[48:51], v[156:159]
ds_read_b128 a[20:23], v12 offset:4096
ds_read_b128 a[52:55], v12 offset:12288
v_mfma_f32_16x16x32_bf16 v[156:159], v[124:127], a[0:3], v[156:159]
s_lshl_b32 s31, s31, 5
v_cvt_pk_bf16_f32 v64, v128, v129
v_cvt_pk_bf16_f32 v65, v130, v131
v_mfma_f32_16x16x32_bf16 v[176:179], v[96:99], v[52:55], v[176:179]
ds_read_b128 a[24:27], v12 offset:1024
ds_read_b128 a[56:59], v12 offset:9216
v_mfma_f32_16x16x32_bf16 v[176:179], v[100:103], a[4:7], v[176:179]
buffer_load_dwordx4 v7, s[8:11], s40 offen offset:2048 sc1 lds
v_cvt_pk_bf16_f32 v66, v132, v133
v_mfma_f32_16x16x32_bf16 v[180:183], v[104:107], v[52:55], v[180:183]
ds_read_b128 a[28:31], v12 offset:5120
ds_read_b128 a[60:63], v12 offset:13312
v_mfma_f32_16x16x32_bf16 v[180:183], v[108:111], a[4:7], v[180:183]
v_cvt_pk_bf16_f32 v67, v134, v135
v_cvt_pk_bf16_f32 v72, v160, v161
v_mfma_f32_16x16x32_bf16 v[184:187], v[112:115], v[52:55], v[184:187]
ds_read_b128 a[32:35], v12 offset:2048
ds_read_b128 a[64:67], v12 offset:10240
v_mfma_f32_16x16x32_bf16 v[184:187], v[116:119], a[4:7], v[184:187]
buffer_load_dwordx4 v7, s[8:11], s40 offen offset:3072 sc1 lds
v_cvt_pk_bf16_f32 v73, v162, v163
v_mfma_f32_16x16x32_bf16 v[188:191], v[120:123], v[52:55], v[188:191]
ds_read_b128 a[36:39], v12 offset:6144
ds_read_b128 a[68:71], v12 offset:14336
v_mfma_f32_16x16x32_bf16 v[188:191], v[124:127], a[4:7], v[188:191]
v_cvt_pk_bf16_f32 v74, v164, v165
v_cvt_pk_bf16_f32 v75, v166, v167
v_mfma_f32_16x16x32_bf16 v[208:211], v[96:99], v[56:59], v[208:211]
ds_read_b128 a[40:43], v12 offset:3072
ds_read_b128 a[72:75], v12 offset:11264
v_mfma_f32_16x16x32_bf16 v[208:211], v[100:103], a[8:11], v[208:211]
v_cvt_pk_bf16_f32 v68, v136, v137
v_cvt_pk_bf16_f32 v69, v138, v139
v_mfma_f32_16x16x32_bf16 v[212:215], v[104:107], v[56:59], v[212:215]
ds_read_b128 a[44:47], v12 offset:7168
ds_read_b128 a[76:79], v12 offset:15360
v_mfma_f32_16x16x32_bf16 v[212:215], v[108:111], a[8:11], v[212:215]
v_cvt_pk_bf16_f32 v70, v140, v141
v_cvt_pk_bf16_f32 v71, v142, v143
v_mfma_f32_16x16x32_bf16 v[216:219], v[112:115], v[56:59], v[216:219]
v_cvt_pk_bf16_f32 v76, v168, v169
v_cvt_pk_bf16_f32 v77, v170, v171
v_mfma_f32_16x16x32_bf16 v[216:219], v[116:119], a[8:11], v[216:219]
v_cvt_pk_bf16_f32 v78, v172, v173
v_cvt_pk_bf16_f32 v79, v174, v175
v_mfma_f32_16x16x32_bf16 v[220:223], v[120:123], v[56:59], v[220:223]
v_cvt_pk_bf16_f32 v80, v192, v193
v_cvt_pk_bf16_f32 v81, v194, v195
v_mfma_f32_16x16x32_bf16 v[220:223], v[124:127], a[8:11], v[220:223]
v_cvt_pk_bf16_f32 v82, v196, v197
v_cvt_pk_bf16_f32 v83, v198, v199
v_mfma_f32_16x16x32_bf16 v[240:243], v[96:99], v[60:63], v[240:243]
v_cvt_pk_bf16_f32 v88, v224, v225
v_cvt_pk_bf16_f32 v89, v226, v227
v_mfma_f32_16x16x32_bf16 v[240:243], v[100:103], a[12:15], v[240:243]
v_cvt_pk_bf16_f32 v90, v228, v229
v_cvt_pk_bf16_f32 v91, v230, v231
v_mfma_f32_16x16x32_bf16 v[244:247], v[104:107], v[60:63], v[244:247]
v_cvt_pk_bf16_f32 v84, v200, v201
v_cvt_pk_bf16_f32 v85, v202, v203
v_mfma_f32_16x16x32_bf16 v[244:247], v[108:111], a[12:15], v[244:247]
v_cvt_pk_bf16_f32 v86, v204, v205
v_cvt_pk_bf16_f32 v87, v206, v207
v_mfma_f32_16x16x32_bf16 v[248:251], v[112:115], v[60:63], v[248:251]
v_cvt_pk_bf16_f32 v92, v232, v233
v_cvt_pk_bf16_f32 v93, v234, v235
v_mfma_f32_16x16x32_bf16 v[248:251], v[116:119], a[12:15], v[248:251]
v_cvt_pk_bf16_f32 v94, v236, v237
v_cvt_pk_bf16_f32 v95, v238, v239
v_mfma_f32_16x16x32_bf16 v[252:255], v[120:123], v[60:63], v[252:255]
s_getpc_b64 s[58:59]
.Lpc0:
s_add_u32 s58, s58, .Ljob0-.Lpc0
s_addc_u32 s59, s59, 0
v_mfma_f32_16x16x32_bf16 v[252:255], v[124:127], a[12:15], v[252:255]
s_waitcnt lgkmcnt(0)
v_cvt_pk_bf16_f32 v96, v144, v145
v_cvt_pk_bf16_f32 v97, v146, v147
v_cvt_pk_bf16_f32 v98, v148, v149
v_cvt_pk_bf16_f32 v99, v150, v151
v_cvt_pk_bf16_f32 v104, v176, v177
v_cvt_pk_bf16_f32 v105, v178, v179
v_cvt_pk_bf16_f32 v106, v180, v181
v_cvt_pk_bf16_f32 v107, v182, v183
v_mfma_f32_16x16x32_bf16 v[128:131], a[16:19], v[64:67], 0
s_add_u32 s28, s39, 16384
s_and_b32 s28, s28, 0xffff
s_mov_b32 s41, 32768
v_mfma_f32_16x16x32_bf16 v[136:139], a[16:19], v[72:75], 0
s_add_u32 m0, s28, 0
v_cvt_pk_bf16_f32 v100, v152, v153
v_cvt_pk_bf16_f32 v101, v154, v155
v_mfma_f32_16x16x32_bf16 v[132:135], a[20:23], v[64:67], 0
buffer_load_dwordx4 v7, s[8:11], s41 offen offset:0 sc1 lds
v_mfma_f32_16x16x32_bf16 v[140:143], a[20:23], v[72:75], 0
v_cvt_pk_bf16_f32 v102, v156, v157
v_mfma_f32_16x16x32_bf16 v[128:131], a[24:27], v[68:71], v[128:131]
v_cvt_pk_bf16_f32 v103, v158, v159
v_mfma_f32_16x16x32_bf16 v[136:139], a[24:27], v[76:79], v[136:139]
v_cvt_pk_bf16_f32 v108, v184, v185
v_add_u32 v62, s49, v32
v_mfma_f32_16x16x32_bf16 v[132:135], a[28:31], v[68:71], v[132:135]
buffer_load_dwordx4 v7, s[8:11], s41 offen offset:1024 sc1 lds
v_cvt_pk_bf16_f32 v112, v208, v209
v_mfma_f32_16x16x32_bf16 v[140:143], a[28:31], v[76:79], v[140:143]
v_cvt_pk_bf16_f32 v109, v186, v187
v_cvt_pk_bf16_f32 v113, v210, v211
v_cvt_pk_bf16_f32 v114, v212, v213
v_mfma_f32_16x16x32_bf16 v[128:131], a[32:35], v[96:99], v[128:131]
v_cvt_pk_bf16_f32 v110, v188, v189
v_cvt_pk_bf16_f32 v115, v214, v215
v_mfma_f32_16x16x32_bf16 v[136:139], a[32:35], v[104:107], v[136:139]
v_cvt_pk_bf16_f32 v111, v190, v191
v_cvt_pk_bf16_f32 v120, v240, v241
v_cvt_pk_bf16_f32 v121, v242, v243
v_mfma_f32_16x16x32_bf16 v[132:135], a[36:39], v[96:99], v[132:135]
buffer_load_dwordx4 v7, s[8:11], s41 offen offset:2048 sc1 lds
v_mfma_f32_16x16x32_bf16 v[140:143], a[36:39], v[104:107], v[140:143]
v_cvt_pk_bf16_f32 v122, v244, v245
v_cvt_pk_bf16_f32 v123, v246, v247
v_mfma_f32_16x16x32_bf16 v[128:131], a[40:43], v[100:103], v[128:131]
v_cvt_pk_bf16_f32 v116, v216, v217
v_cvt_pk_bf16_f32 v117, v218, v219
v_mfma_f32_16x16x32_bf16 v[136:139], a[40:43], v[108:111], v[136:139]
v_cvt_pk_bf16_f32 v118, v220, v221
v_cvt_pk_bf16_f32 v119, v222, v223
v_mfma_f32_16x16x32_bf16 v[132:135], a[44:47], v[100:103], v[132:135]
s_add_u32 s28, s39, 32768
s_and_b32 s28, s28, 0xffff
buffer_load_dwordx4 v7, s[8:11], s41 offen offset:3072 sc1 lds
v_mfma_f32_16x16x32_bf16 v[140:143], a[44:47], v[108:111], v[140:143]
s_mov_b32 s40, 49152
v_cvt_pk_bf16_f32 v124, v248, v249
v_cvt_pk_bf16_f32 v125, v250, v251
v_mfma_f32_16x16x32_bf16 v[160:163], a[16:19], v[80:83], 0
s_add_u32 m0, s28, 0
v_cvt_pk_bf16_f32 v126, v252, v253
v_cvt_pk_bf16_f32 v127, v254, v255
v_mfma_f32_16x16x32_bf16 v[168:171], a[16:19], v[88:91], 0
buffer_load_dwordx4 v7, s[8:11], s40 offen offset:0 sc1 lds
v_mfma_f32_16x16x32_bf16 v[164:167], a[20:23], v[80:83], 0
v_max3_f32 v56, v128, v129, v130
v_max3_f32 v57, v136, v137, v138
v_max3_f32 v58, v131, v132, v133
v_mfma_f32_16x16x32_bf16 v[172:175], a[20:23], v[88:91], 0
ds_read_b128 a[16:19], v62 offset:0
ds_read_b128 a[20:23], v62 offset:4096
v_max3_f32 v59, v139, v140, v141
v_mfma_f32_16x16x32_bf16 v[160:163], a[24:27], v[84:87], v[160:163]
v_max3_f32 v56, v56, v134, v135
v_max3_f32 v57, v57, v142, v143
v_max_f32 v56, v56, v58
v_mfma_f32_16x16x32_bf16 v[168:171], a[24:27], v[92:95], v[168:171]
buffer_load_dwordx4 v7, s[8:11], s40 offen offset:1024 sc1 lds
v_max_f32 v57, v57, v59
v_mov_b32 v58, v56
v_mfma_f32_16x16x32_bf16 v[164:167], a[28:31], v[84:87], v[164:167]
v_mov_b32 v59, v57
v_nop
v_permlane16_swap_b32 v56, v58
v_mfma_f32_16x16x32_bf16 v[172:175], a[28:31], v[92:95], v[172:175]
ds_read_b128 a[24:27], v62 offset:1024
ds_read_b128 a[28:31], v62 offset:5120
v_permlane16_swap_b32 v57, v59
v_mfma_f32_16x16x32_bf16 v[160:163], a[32:35], v[112:115], v[160:163]
v_max_f32 v56, v56, v58
v_max_f32 v57, v57, v59
v_mov_b32 v58, v56
v_mfma_f32_16x16x32_bf16 v[168:171], a[32:35], v[120:123], v[168:171]
buffer_load_dwordx4 v7, s[8:11], s40 offen offset:2048 sc1 lds
v_mov_b32 v59, v57
v_nop
v_mfma_f32_16x16x32_bf16 v[164:167], a[36:39], v[112:115], v[164:167]
v_permlane32_swap_b32 v56, v58
v_permlane32_swap_b32 v57, v59
v_max_f32 v56, v56, v58
v_mfma_f32_16x16x32_bf16 v[172:175], a[36:39], v[120:123], v[172:175]
ds_read_b128 a[32:35], v62 offset:2048
ds_read_b128 a[36:39], v62 offset:6144
v_max_f32 v57, v57, v59
v_sub_f32 v192, 0, v56
v_mfma_f32_16x16x32_bf16 v[160:163], a[40:43], v[116:119], v[160:163]
v_sub_f32 v193, 0, v56
v_sub_f32 v194, 0, v56
v_sub_f32 v195, 0, v56
v_mfma_f32_16x16x32_bf16 v[168:171], a[40:43], v[124:127], v[168:171]
buffer_load_dwordx4 v7, s[8:11], s40 offen offset:3072 sc1 lds
v_sub_f32 v196, 0, v57
v_mfma_f32_16x16x32_bf16 v[164:167], a[44:47], v[116:119], v[164:167]
v_sub_f32 v197, 0, v57
v_sub_f32 v198, 0, v57
v_sub_f32 v199, 0, v57
v_mfma_f32_16x16x32_bf16 v[172:175], a[44:47], v[124:127], v[172:175]
ds_read_b128 a[40:43], v62 offset:3072
ds_read_b128 a[44:47], v62 offset:7168
v_add_f32 v128, v128, v192
v_add_f32 v129, v129, v192
v_mfma_f32_16x16x32_bf16 v[144:147], a[48:51], v[64:67], v[192:195]
s_xor_b32 s48, s95, 265216
v_add_f32 v130, v130, v192
v_add_f32 v131, v131, v192
v_mfma_f32_16x16x32_bf16 v[152:155], a[48:51], v[72:75], v[196:199]
buffer_store_dwordx4 a[140:143], v7, s[20:23], s48 offen sc1
v_add_f32 v132, v132, v192
v_add_f32 v133, v133, v192
v_mfma_f32_16x16x32_bf16 v[148:151], a[52:55], v[64:67], v[192:195]
s_cmp_eq_u32 s51, 0
s_cbranch_scc1 .Lj0a
.Lj0a_ret:
v_max3_f32 v56, v160, v161, v162
v_max3_f32 v57, v168, v169, v170
v_mfma_f32_16x16x32_bf16 v[156:159], a[52:55], v[72:75], v[196:199]
v_max3_f32 v58, v163, v164, v165
v_max3_f32 v59, v171, v172, v173
v_max3_f32 v56, v56, v166, v167
v_mfma_f32_16x16x32_bf16 v[144:147], a[56:59], v[68:71], v[144:147]
v_max3_f32 v57, v57, v174, v175
v_max_f32 v56, v56, v58
v_mfma_f32_16x16x32_bf16 v[152:155], a[56:59], v[76:79], v[152:155]
s_xor_b32 s48, s95, 266240
v_max_f32 v57, v57, v59
v_mov_b32 v58, v56
v_mfma_f32_16x16x32_bf16 v[148:151], a[60:63], v[68:71], v[148:151]
buffer_store_dwordx4 a[160:163], v7, s[20:23], s48 offen sc1
v_mov_b32 v59, v57
v_nop
v_mfma_f32_16x16x32_bf16 v[156:159], a[60:63], v[76:79], v[156:159]
v_permlane16_swap_b32 v56, v58
v_permlane16_swap_b32 v57, v59
v_mfma_f32_16x16x32_bf16 v[144:147], a[64:67], v[96:99], v[144:147]
v_max_f32 v56, v56, v58
v_max_f32 v57, v57, v59
v_mov_b32 v58, v56
v_mfma_f32_16x16x32_bf16 v[152:155], a[64:67], v[104:107], v[152:155]
v_mov_b32 v59, v57
v_nop
v_permlane32_swap_b32 v56, v58
v_mfma_f32_16x16x32_bf16 v[148:151], a[68:71], v[96:99], v[148:151]
s_xor_b32 s48, s95, 267264
v_permlane32_swap_b32 v57, v59
v_max_f32 v56, v56, v58
v_mfma_f32_16x16x32_bf16 v[156:159], a[68:71], v[104:107], v[156:159]
buffer_store_dwordx4 a[164:167], v7, s[20:23], s48 offen sc1
v_max_f32 v57, v57, v59
v_sub_f32 v200, 0, v56
v_mfma_f32_16x16x32_bf16 v[144:147], a[72:75], v[100:103], v[144:147]
v_sub_f32 v201, 0, v56
v_sub_f32 v202, 0, v56
v_mfma_f32_16x16x32_bf16 v[152:155], a[72:75], v[108:111], v[152:155]
v_sub_f32 v203, 0, v56
v_sub_f32 v204, 0, v57
v_sub_f32 v205, 0, v57
v_mfma_f32_16x16x32_bf16 v[148:151], a[76:79], v[100:103], v[148:151]
v_sub_f32 v206, 0, v57
v_sub_f32 v207, 0, v57
v_mfma_f32_16x16x32_bf16 v[156:159], a[76:79], v[108:111], v[156:159]
s_xor_b32 s48, s95, 268288
v_add_f32 v134, v134, v192
v_add_f32 v135, v135, v192
v_mfma_f32_16x16x32_bf16 v[176:179], a[48:51], v[80:83], v[200:203]
buffer_store_dwordx4 a[168:171], v7, s[20:23], s48 offen sc1
v_mfma_f32_16x16x32_bf16 v[184:187], a[48:51], v[88:91], v[204:207]
v_add_f32 v136, v136, v196
v_add_f32 v137, v137, v196
v_mfma_f32_16x16x32_bf16 v[180:183], a[52:55], v[80:83], v[200:203]
v_add_f32 v138, v138, v196
v_add_f32 v139, v139, v196
v_mfma_f32_16x16x32_bf16 v[188:191], a[52:55], v[88:91], v[204:207]
v_add_f32 v140, v140, v196
v_mfma_f32_16x16x32_bf16 v[176:179], a[56:59], v[84:87], v[176:179]
s_xor_b32 s48, s95, 269312
v_add_f32 v141, v141, v196
v_add_f32 v142, v142, v196
v_mfma_f32_16x16x32_bf16 v[184:187], a[56:59], v[92:95], v[184:187]
buffer_store_dwordx4 a[172:175], v7, s[20:23], s48 offen sc1
v_mfma_f32_16x16x32_bf16 v[180:183], a[60:63], v[84:87], v[180:183]
v_add_f32 v143, v143, v196
v_add_u32 v63, s39, v33
v_mfma_f32_16x16x32_bf16 v[188:191], a[60:63], v[92:95], v[188:191]
v_add_u32 v13, s31, v8
v_mov_b32 v52, v14
v_mfma_f32_16x16x32_bf16 v[176:179], a[64:67], v[112:115], v[176:179]
v_mov_b32 v53, v14
v_mfma_f32_16x16x32_bf16 v[184:187], a[64:67], v[120:123], v[184:187]
s_xor_b32 s48, s95, 393216
v_mov_b32 v54, v14
v_mov_b32 v55, v14
v_mfma_f32_16x16x32_bf16 v[180:183], a[68:71], v[112:115], v[180:183]
buffer_store_dwordx4 a[192:195], v7, s[20:23], s48 offen sc1
v_mfma_f32_16x16x32_bf16 v[188:191], a[68:71], v[120:123], v[188:191]
v_add_f32 v160, v160, v200
v_add_f32 v161, v161, v200
v_mfma_f32_16x16x32_bf16 v[176:179], a[72:75], v[116:119], v[176:179]
v_add_f32 v162, v162, v200
v_add_f32 v163, v163, v200
v_mfma_f32_16x16x32_bf16 v[184:187], a[72:75], v[124:127], v[184:187]
v_add_f32 v164, v164, v200
v_mfma_f32_16x16x32_bf16 v[180:183], a[76:79], v[116:119], v[180:183]
v_add_f32 v165, v165, v200
v_add_f32 v166, v166, v200
v_mfma_f32_16x16x32_bf16 v[188:191], a[76:79], v[124:127], v[188:191]
v_add_f32 v167, v167, v200
v_add_f32 v168, v168, v204
v_add_f32 v169, v169, v204
v_add_f32 v170, v170, v204
v_add_f32 v171, v171, v204
v_add_f32 v172, v172, v204
v_add_f32 v173, v173, v204
v_add_f32 v174, v174, v204
v_add_f32 v175, v175, v204
v_exp_f32 v128, v128
v_exp_f32 v129, v129
v_exp_f32 v160, v160
v_cvt_pk_bf16_f32 v224, v128, v129
v_exp_f32 v161, v161
v_exp_f32 v130, v130
v_cvt_pk_bf16_f32 v240, v160, v161
v_exp_f32 v131, v131
v_exp_f32 v162, v162
v_cvt_pk_bf16_f32 v225, v130, v131
v_exp_f32 v163, v163
v_exp_f32 v132, v132
v_cvt_pk_bf16_f32 v241, v162, v163
v_exp_f32 v133, v133
v_exp_f32 v164, v164
v_cvt_pk_bf16_f32 v226, v132, v133
v_exp_f32 v165, v165
v_exp_f32 v134, v134
v_cvt_pk_bf16_f32 v242, v164, v165
v_exp_f32 v135, v135
v_exp_f32 v166, v166
v_cvt_pk_bf16_f32 v227, v134, v135
v_exp_f32 v167, v167
v_exp_f32 v136, v136
v_cvt_pk_bf16_f32 v243, v166, v167
v_exp_f32 v137, v137
v_exp_f32 v168, v168
v_cvt_pk_bf16_f32 v228, v136, v137
v_exp_f32 v169, v169
v_exp_f32 v138, v138
v_cvt_pk_bf16_f32 v244, v168, v169
v_exp_f32 v139, v139
v_exp_f32 v170, v170
v_cvt_pk_bf16_f32 v229, v138, v139
v_exp_f32 v171, v171
v_exp_f32 v140, v140
v_cvt_pk_bf16_f32 v245, v170, v171
v_exp_f32 v141, v141
v_exp_f32 v172, v172
v_cvt_pk_bf16_f32 v230, v140, v141
v_exp_f32 v173, v173
v_exp_f32 v142, v142
v_cvt_pk_bf16_f32 v246, v172, v173
v_exp_f32 v143, v143
v_exp_f32 v174, v174
v_cvt_pk_bf16_f32 v231, v142, v143
v_exp_f32 v175, v175
s_nop 0
v_cvt_pk_bf16_f32 v247, v174, v175
s_cmp_lt_u32 s51, 2
s_cbranch_scc1 .Lj0b
.Lj0b_ret:
s_waitcnt vmcnt(14) lgkmcnt(0)
s_barrier
v_mfma_f32_16x16x32_bf16 v[128:131], a[16:19], v[64:67], v[192:195]
v_exp_f32 v144, v144
v_mfma_f32_16x16x32_bf16 v[128:131], a[24:27], v[68:71], v[128:131]
ds_read_b128 a[48:51], v63 offset:0
ds_read_b128 a[52:55], v63 offset:1024
v_cmp_gt_i32 s[32:33], 0, v13
v_mfma_f32_16x16x32_bf16 v[136:139], a[16:19], v[72:75], v[196:199]
v_exp_f32 v145, v145
v_mfma_f32_16x16x32_bf16 v[136:139], a[24:27], v[76:79], v[136:139]
s_xor_b32 s48, s95, 394240
v_exp_f32 v176, v176
v_nop
v_mfma_f32_16x16x32_bf16 v[160:163], a[16:19], v[80:83], v[200:203]
buffer_store_dwordx4 a[196:199], v7, s[20:23], s48 offen sc1
v_mfma_f32_16x16x32_bf16 v[160:163], a[24:27], v[84:87], v[160:163]
ds_read_b128 a[56:59], v63 offset:2048
ds_read_b128 a[60:63], v63 offset:3072
v_cmp_gt_i32 s[34:35], 1, v13
v_mfma_f32_16x16x32_bf16 v[168:171], a[16:19], v[88:91], v[204:207]
s_sub_u32 s52, s52, 1
s_cselect_b32 s52, 0, s52
s_cselect_b32 s14, s16, 0
v_mfma_f32_16x16x32_bf16 v[168:171], a[24:27], v[92:95], v[168:171]
ds_read_b128 a[16:19], v62 offset:8192
v_cndmask_b32 v36, v200, v34, s[32:33]
v_mfma_f32_16x16x32_bf16 v[132:135], a[20:23], v[64:67], v[192:195]
s_cmp_lg_u32 s14, 0
s_cselect_b32 s17, 786432, 0
v_cmp_gt_i32 s[32:33], 2, v13
v_mfma_f32_16x16x32_bf16 v[132:135], a[28:31], v[68:71], v[132:135]
ds_read_b128 a[64:67], v63 offset:4096
ds_read_b128 a[68:71], v63 offset:5120
v_mfma_f32_16x16x32_bf16 v[140:143], a[20:23], v[72:75], v[196:199]
v_exp_f32 v177, v177
v_mfma_f32_16x16x32_bf16 v[140:143], a[28:31], v[76:79], v[140:143]
s_xor_b32 s48, s95, 395264
ds_read_b128 a[24:27], v62 offset:9216
v_cndmask_b32 v37, v200, v34, s[34:35]
v_mfma_f32_16x16x32_bf16 v[164:167], a[20:23], v[80:83], v[200:203]
buffer_store_dwordx4 a[200:203], v7, s[20:23], s48 offen sc1
v_mfma_f32_16x16x32_bf16 v[164:167], a[28:31], v[84:87], v[164:167]
ds_read_b128 a[72:75], v63 offset:6144
ds_read_b128 a[76:79], v63 offset:7168
v_cmp_gt_i32 s[34:35], 3, v13
v_mfma_f32_16x16x32_bf16 v[172:175], a[20:23], v[88:91], v[204:207]
v_exp_f32 v146, v146
v_mfma_f32_16x16x32_bf16 v[172:175], a[28:31], v[92:95], v[172:175]
ds_read_b128 a[20:23], v62 offset:12288
v_cndmask_b32 v38, v200, v34, s[32:33]
v_mfma_f32_16x16x32_bf16 v[128:131], a[32:35], v[96:99], v[128:131]
v_exp_f32 v147, v147
v_mfma_f32_16x16x32_bf16 v[128:131], a[40:43], v[100:103], v[128:131]
v_exp_f32 v178, v178
v_cmp_gt_i32 s[32:33], 16, v13
v_mfma_f32_16x16x32_bf16 v[136:139], a[32:35], v[104:107], v[136:139]
v_exp_f32 v179, v179
v_mfma_f32_16x16x32_bf16 v[136:139], a[40:43], v[108:111], v[136:139]
s_xor_b32 s48, s95, 396288
ds_read_b128 a[28:31], v62 offset:13312
.Lpad10:
s_cbranch_execz .Ltch2
v_cndmask_b32 v39, v200, v34, s[34:35]
v_mfma_f32_16x16x32_bf16 v[160:163], a[32:35], v[112:115], v[160:163]
buffer_store_dwordx4 a[204:207], v7, s[20:23], s48 offen sc1
v_mfma_f32_16x16x32_bf16 v[160:163], a[40:43], v[116:119], v[160:163]
v_exp_f32 v148, v148
v_cmp_gt_i32 s[34:35], 17, v13
v_mfma_f32_16x16x32_bf16 v[168:171], a[32:35], v[120:123], v[168:171]
v_exp_f32 v149, v149
v_mfma_f32_16x16x32_bf16 v[168:171], a[40:43], v[124:127], v[168:171]
ds_read_b128 a[32:35], v62 offset:10240
v_cndmask_b32 v40, v200, v34, s[32:33]
v_mfma_f32_16x16x32_bf16 v[132:135], a[36:39], v[96:99], v[132:135]
v_exp_f32 v180, v180
v_cmp_gt_i32 s[32:33], 18, v13
v_mfma_f32_16x16x32_bf16 v[132:135], a[44:47], v[100:103], v[132:135]
v_exp_f32 v181, v181
v_mfma_f32_16x16x32_bf16 v[140:143], a[36:39], v[104:107], v[140:143]
v_exp_f32 v150, v150
v_cndmask_b32 v41, v200, v34, s[34:35]
v_mfma_f32_16x16x32_bf16 v[140:143], a[44:47], v[108:111], v[140:143]
s_xor_b32 s48, s95, 397312
ds_read_b128 a[40:43], v62 offset:11264
v_mfma_f32_16x16x32_bf16 v[164:167], a[36:39], v[112:115], v[164:167]
buffer_store_dwordx4 a[224:227], v7, s[20:23], s48 offen sc1
v_cmp_gt_i32 s[34:35], 19, v13
v_mfma_f32_16x16x32_bf16 v[164:167], a[44:47], v[116:119], v[164:167]
v_exp_f32 v151, v151
v_mfma_f32_16x16x32_bf16 v[172:175], a[36:39], v[120:123], v[172:175]
v_exp_f32 v182, v182
v_cndmask_b32 v42, v200, v34, s[32:33]
v_mfma_f32_16x16x32_bf16 v[172:175], a[44:47], v[124:127], v[172:175]
ds_read_b128 a[36:39], v62 offset:14336
ds_read_b128 a[44:47], v62 offset:15360
v_mfma_f32_16x16x32_bf16 v[208:211], v[52:55], v[224:227], 0
v_add_u32 v62, s46, v32
v_cvt_pk_bf16_f32 v232, v144, v145
v_cmp_gt_i32 s[32:33], -16, v13
v_mfma_f32_16x16x32_bf16 v[212:215], v[52:55], v[228:231], 0
v_exp_f32 v183, v183
v_mfma_f32_16x16x32_bf16 v[216:219], v[52:55], v[240:243], 0
v_exp_f32 v152, v152
v_cndmask_b32 v43, v200, v34, s[34:35]
v_mfma_f32_16x16x32_bf16 v[220:223], v[52:55], v[244:247], 0
s_waitcnt lgkmcnt(6)
s_xor_b32 s48, s95, 398336
v_cvt_pk_bf16_f32 v248, v176, v177
v_mfma_f32_16x16x32_bf16 a[0:3], a[48:51], v[224:227], 0
buffer_store_dwordx4 a[228:231], v7, s[20:23], s48 offen sc1
v_cmp_gt_i32 s[34:35], -15, v13
v_mfma_f32_16x16x32_bf16 a[8:11], a[48:51], v[228:231], 0
v_exp_f32 v153, v153
v_mfma_f32_16x16x32_bf16 a[176:179], a[48:51], v[240:243], 0
v_exp_f32 v184, v184
v_cndmask_b32 v44, v204, v34, s[32:33]
v_mfma_f32_16x16x32_bf16 a[184:187], a[48:51], v[244:247], 0
ds_read_b128 a[48:51], v63 offset:8192
v_cvt_pk_bf16_f32 v233, v146, v147
v_mfma_f32_16x16x32_bf16 a[4:7], a[52:55], v[224:227], 0
v_exp_f32 v185, v185
v_cmp_gt_i32 s[32:33], -14, v13
v_mfma_f32_16x16x32_bf16 a[12:15], a[52:55], v[228:231], 0
v_cvt_pk_bf16_f32 v249, v178, v179
v_cvt_pk_bf16_f32 v234, v148, v149
v_mfma_f32_16x16x32_bf16 a[180:183], a[52:55], v[240:243], 0
v_exp_f32 v154, v154
v_cndmask_b32 v45, v204, v34, s[34:35]
v_mfma_f32_16x16x32_bf16 a[188:191], a[52:55], v[244:247], 0
s_xor_b32 s48, s95, 399360
ds_read_b128 a[52:55], v63 offset:9216
v_mfma_f32_16x16x32_bf16 a[80:83], a[56:59], v[224:227], 0
buffer_store_dwordx4 a[232:235], v7, s[20:23], s48 offen sc1
v_cmp_gt_i32 s[34:35], -13, v13
v_mfma_f32_16x16x32_bf16 a[88:91], a[56:59], v[228:231], 0
v_exp_f32 v155, v155
v_mfma_f32_16x16x32_bf16 a[208:211], a[56:59], v[240:243], 0
v_exp_f32 v186, v186
v_cndmask_b32 v46, v204, v34, s[32:33]
v_mfma_f32_16x16x32_bf16 a[216:219], a[56:59], v[244:247], 0
ds_read_b128 a[56:59], v63 offset:10240
v_cvt_pk_bf16_f32 v250, v180, v181
v_mfma_f32_16x16x32_bf16 a[84:87], a[60:63], v[224:227], 0
v_exp_f32 v187, v187
v_cmp_gt_i32 s[32:33], 0, v13
v_mfma_f32_16x16x32_bf16 a[92:95], a[60:63], v[228:231], 0
v_cvt_pk_bf16_f32 v235, v150, v151
v_cvt_pk_bf16_f32 v251, v182, v183
v_mfma_f32_16x16x32_bf16 a[212:215], a[60:63], v[240:243], 0
v_exp_f32 v156, v156
v_cndmask_b32 v47, v204, v34, s[34:35]
v_mfma_f32_16x16x32_bf16 a[220:223], a[60:63], v[244:247], 0
s_xor_b32 s48, s95, 400384
ds_read_b128 a[60:63], v63 offset:11264
v_mfma_f32_16x16x32_bf16 a[112:115], a[64:67], v[224:227], 0
buffer_store_dwordx4 a[236:239], v7, s[20:23], s48 offen sc1
v_cmp_gt_i32 s[34:35], 1, v13
v_mfma_f32_16x16x32_bf16 a[120:123], a[64:67], v[228:231], 0
v_exp_f32 v157, v157
v_mfma_f32_16x16x32_bf16 a[240:243], a[64:67], v[240:243], 0
buffer_load_dword v35, v2, s[12:15], s53 offen
v_cndmask_b32 v48, v204, v34, s[32:33]
v_mfma_f32_16x16x32_bf16 a[248:251], a[64:67], v[244:247], 0
s_add_u32 s53, s53, s17
ds_read_b128 a[64:67], v63 offset:12288
v_mfma_f32_16x16x32_bf16 a[116:119], a[68:71], v[224:227], 0
v_exp_f32 v188, v188
v_cmp_gt_i32 s[32:33], 2, v13
v_mfma_f32_16x16x32_bf16 a[124:127], a[68:71], v[228:231], 0
v_exp_f32 v189, v189
v_mfma_f32_16x16x32_bf16 a[244:247], a[68:71], v[240:243], 0
v_exp_f32 v158, v158
v_cndmask_b32 v49, v204, v34, s[34:35]
v_mfma_f32_16x16x32_bf16 a[252:255], a[68:71], v[244:247], 0
ds_read_b128 a[68:71], v63 offset:13312
v_cvt_pk_bf16_f32 v236, v152, v153
v_mfma_f32_16x16x32_bf16 a[144:147], a[72:75], v[224:227], 0
v_exp_f32 v159, v159
v_cmp_gt_i32 s[34:35], 3, v13
v_mfma_f32_16x16x32_bf16 a[152:155], a[72:75], v[228:231], 0
v_cvt_pk_bf16_f32 v252, v184, v185
v_cvt_pk_bf16_f32 v237, v154, v155
v_mfma_f32_16x16x32_bf16 a[96:99], a[72:75], v[240:243], 0
v_exp_f32 v190, v190
v_cndmask_b32 v50, v204, v34, s[32:33]
v_mfma_f32_16x16x32_bf16 a[104:107], a[72:75], v[244:247], 0
ds_read_b128 a[72:75], v63 offset:14336
v_cvt_pk_bf16_f32 v253, v186, v187
v_mfma_f32_16x16x32_bf16 a[148:151], a[76:79], v[224:227], 0
v_cvt_pk_bf16_f32 v238, v156, v157
v_cvt_pk_bf16_f32 v254, v188, v189
v_nop
v_mfma_f32_16x16x32_bf16 a[156:159], a[76:79], v[228:231], 0
s_waitcnt vmcnt(18) lgkmcnt(7)
v_exp_f32 v191, v191
v_mfma_f32_16x16x32_bf16 a[100:103], a[76:79], v[240:243], 0
s_barrier
v_cvt_pk_bf16_f32 v239, v158, v159
v_cvt_pk_bf16_f32 v255, v190, v191
v_cndmask_b32 v51, v204, v34, s[34:35]
v_mfma_f32_16x16x32_bf16 a[108:111], a[76:79], v[244:247], 0
ds_read_b128 a[76:79], v63 offset:15360
s_cmp_lt_u32 s51, 3
s_cbranch_scc1 .Lj0c
s_cmp_eq_u32 s29, 3
s_cbranch_scc1 .LT1
s_nop 0
.LD1:
v_mfma_f32_16x16x32_bf16 v[144:147], a[16:19], v[64:67], v[192:195]
s_cmp_lt_u32 s44, s54
v_exp_f32 v128, v128
v_mfma_f32_16x16x32_bf16 v[144:147], a[24:27], v[68:71], v[144:147]
s_cselect_b32 m0, s50, s27
v_exp_f32 v129, v129
v_mfma_f32_16x16x32_bf16 v[152:155], a[16:19], v[72:75], v[196:199]
v_add_u32 v63, s49, v33
v_mfma_f32_16x16x32_bf16 v[152:155], a[24:27], v[76:79], v[152:155]
v_mfma_f32_16x16x32_bf16 v[176:179], a[16:19], v[80:83], v[200:203]
v_exp_f32 v160, v160
v_mfma_f32_16x16x32_bf16 v[176:179], a[24:27], v[84:87], v[176:179]
v_mfma_f32_16x16x32_bf16 v[184:187], a[16:19], v[88:91], v[204:207]
v_exp_f32 v161, v161
v_mfma_f32_16x16x32_bf16 v[184:187], a[24:27], v[92:95], v[184:187]
ds_read_b128 a[16:19], v62 offset:0
v_mfma_f32_16x16x32_bf16 v[148:151], a[20:23], v[64:67], v[192:195]
buffer_load_dwordx4 v7, s[4:7], s44 offen offset:0 sc1 lds
v_mfma_f32_16x16x32_bf16 v[148:151], a[28:31], v[68:71], v[148:151]
v_exp_f32 v130, v130
v_mfma_f32_16x16x32_bf16 v[156:159], a[20:23], v[72:75], v[196:199]
v_exp_f32 v131, v131
v_mfma_f32_16x16x32_bf16 v[156:159], a[28:31], v[76:79], v[156:159]
ds_read_b128 a[24:27], v62 offset:1024
v_mfma_f32_16x16x32_bf16 v[180:183], a[20:23], v[80:83], v[200:203]
s_mov_b32 s49, s46
v_exp_f32 v162, v162
v_mfma_f32_16x16x32_bf16 v[180:183], a[28:31], v[84:87], v[180:183]
v_mfma_f32_16x16x32_bf16 v[188:191], a[20:23], v[88:91], v[204:207]
v_exp_f32 v163, v163
v_mfma_f32_16x16x32_bf16 v[188:191], a[28:31], v[92:95], v[188:191]
ds_read_b128 a[20:23], v62 offset:4096
v_mfma_f32_16x16x32_bf16 v[144:147], a[32:35], v[96:99], v[144:147]
v_exp_f32 v132, v132
v_mfma_f32_16x16x32_bf16 v[144:147], a[40:43], v[100:103], v[144:147]
v_mfma_f32_16x16x32_bf16 v[152:155], a[32:35], v[104:107], v[152:155]
s_add_u32 s46, s46, 16384
v_exp_f32 v133, v133
v_mfma_f32_16x16x32_bf16 v[152:155], a[40:43], v[108:111], v[152:155]
ds_read_b128 a[28:31], v62 offset:5120
v_mfma_f32_16x16x32_bf16 v[176:179], a[32:35], v[112:115], v[176:179]
buffer_load_dwordx4 v7, s[4:7], s44 offen offset:1024 sc1 lds
v_mfma_f32_16x16x32_bf16 v[176:179], a[40:43], v[116:119], v[176:179]
v_exp_f32 v164, v164
v_mfma_f32_16x16x32_bf16 v[184:187], a[32:35], v[120:123], v[184:187]
v_exp_f32 v165, v165
v_mfma_f32_16x16x32_bf16 v[184:187], a[40:43], v[124:127], v[184:187]
ds_read_b128 a[32:35], v62 offset:2048
v_mfma_f32_16x16x32_bf16 v[148:151], a[36:39], v[96:99], v[148:151]
v_mfma_f32_16x16x32_bf16 v[148:151], a[44:47], v[100:103], v[148:151]
v_exp_f32 v134, v134
v_mfma_f32_16x16x32_bf16 v[156:159], a[36:39], v[104:107], v[156:159]
s_and_b32 s46, s46, 0xffff
v_exp_f32 v135, v135
v_mfma_f32_16x16x32_bf16 v[156:159], a[44:47], v[108:111], v[156:159]
ds_read_b128 a[40:43], v62 offset:3072
v_mfma_f32_16x16x32_bf16 v[180:183], a[36:39], v[112:115], v[180:183]
v_mfma_f32_16x16x32_bf16 v[180:183], a[44:47], v[116:119], v[180:183]
v_exp_f32 v166, v166
v_mfma_f32_16x16x32_bf16 v[188:191], a[36:39], v[120:123], v[188:191]
v_mfma_f32_16x16x32_bf16 v[188:191], a[44:47], v[124:127], v[188:191]
ds_read_b128 a[36:39], v62 offset:6144
ds_read_b128 a[44:47], v62 offset:7168
v_mfma_f32_16x16x32_bf16 v[208:211], v[52:55], v[232:235], v[208:211]
v_exp_f32 v167, v167
v_mfma_f32_16x16x32_bf16 v[212:215], v[52:55], v[236:239], v[212:215]
v_exp_f32 v136, v136
v_mfma_f32_16x16x32_bf16 v[216:219], v[52:55], v[248:251], v[216:219]
v_mfma_f32_16x16x32_bf16 v[220:223], v[52:55], v[252:255], v[220:223]
s_waitcnt lgkmcnt(8)
v_exp_f32 v137, v137
v_mfma_f32_16x16x32_bf16 a[0:3], a[48:51], v[232:235], a[0:3]
buffer_load_dwordx4 v7, s[4:7], s44 offen offset:2048 sc1 lds
v_mfma_f32_16x16x32_bf16 a[8:11], a[48:51], v[236:239], a[8:11]
v_exp_f32 v168, v168
v_mfma_f32_16x16x32_bf16 a[176:179], a[48:51], v[248:251], a[176:179]
v_mfma_f32_16x16x32_bf16 a[184:187], a[48:51], v[252:255], a[184:187]
ds_read_b128 a[48:51], v63 offset:0
v_mfma_f32_16x16x32_bf16 a[4:7], a[52:55], v[232:235], a[4:7]
v_exp_f32 v169, v169
v_mfma_f32_16x16x32_bf16 a[12:15], a[52:55], v[236:239], a[12:15]
v_mfma_f32_16x16x32_bf16 a[180:183], a[52:55], v[248:251], a[180:183]
v_exp_f32 v138, v138
v_mfma_f32_16x16x32_bf16 a[188:191], a[52:55], v[252:255], a[188:191]
ds_read_b128 a[52:55], v63 offset:1024
v_mfma_f32_16x16x32_bf16 a[80:83], a[56:59], v[232:235], a[80:83]
v_exp_f32 v139, v139
v_mfma_f32_16x16x32_bf16 a[88:91], a[56:59], v[236:239], a[88:91]
v_cvt_pk_bf16_f32 v224, v128, v129
v_mfma_f32_16x16x32_bf16 a[208:211], a[56:59], v[248:251], a[208:211]
v_exp_f32 v170, v170
v_mfma_f32_16x16x32_bf16 a[216:219], a[56:59], v[252:255], a[216:219]
ds_read_b128 a[56:59], v63 offset:2048
v_cvt_pk_bf16_f32 v240, v160, v161
v_mfma_f32_16x16x32_bf16 a[84:87], a[60:63], v[232:235], a[84:87]
buffer_load_dwordx4 v7, s[4:7], s44 offen offset:3072 sc1 lds
v_mfma_f32_16x16x32_bf16 a[92:95], a[60:63], v[236:239], a[92:95]
v_exp_f32 v171, v171
v_mfma_f32_16x16x32_bf16 a[212:215], a[60:63], v[248:251], a[212:215]
v_exp_f32 v140, v140
v_mfma_f32_16x16x32_bf16 a[220:223], a[60:63], v[252:255], a[220:223]
ds_read_b128 a[60:63], v63 offset:3072
v_cvt_pk_bf16_f32 v225, v130, v131
v_mfma_f32_16x16x32_bf16 a[112:115], a[64:67], v[232:235], a[112:115]
v_exp_f32 v141, v141
v_mfma_f32_16x16x32_bf16 a[120:123], a[64:67], v[236:239], a[120:123]
v_cvt_pk_bf16_f32 v241, v162, v163
v_cvt_pk_bf16_f32 v226, v132, v133
v_mfma_f32_16x16x32_bf16 a[240:243], a[64:67], v[248:251], a[240:243]
v_exp_f32 v172, v172
v_mfma_f32_16x16x32_bf16 a[248:251], a[64:67], v[252:255], a[248:251]
ds_read_b128 a[64:67], v63 offset:4096
v_cvt_pk_bf16_f32 v242, v164, v165
v_mfma_f32_16x16x32_bf16 a[116:119], a[68:71], v[232:235], a[116:119]
s_sub_u32 s55, s55, 1
v_exp_f32 v173, v173
v_mfma_f32_16x16x32_bf16 a[124:127], a[68:71], v[236:239], a[124:127]
v_cvt_pk_bf16_f32 v227, v134, v135
v_cvt_pk_bf16_f32 v243, v166, v167
v_mfma_f32_16x16x32_bf16 a[244:247], a[68:71], v[248:251], a[244:247]
v_exp_f32 v142, v142
v_mfma_f32_16x16x32_bf16 a[252:255], a[68:71], v[252:255], a[252:255]
ds_read_b128 a[68:71], v63 offset:5120
v_cvt_pk_bf16_f32 v228, v136, v137
v_mfma_f32_16x16x32_bf16 a[144:147], a[72:75], v[232:235], a[144:147]
v_exp_f32 v143, v143
v_mfma_f32_16x16x32_bf16 a[152:155], a[72:75], v[236:239], a[152:155]
v_cvt_pk_bf16_f32 v244, v168, v169
v_cvt_pk_bf16_f32 v229, v138, v139
v_mfma_f32_16x16x32_bf16 a[96:99], a[72:75], v[248:251], a[96:99]
v_exp_f32 v174, v174
v_mfma_f32_16x16x32_bf16 a[104:107], a[72:75], v[252:255], a[104:107]
ds_read_b128 a[72:75], v63 offset:6144
v_cvt_pk_bf16_f32 v245, v170, v171
v_mfma_f32_16x16x32_bf16 a[148:151], a[76:79], v[232:235], a[148:151]
v_cvt_pk_bf16_f32 v230, v140, v141
v_cvt_pk_bf16_f32 v246, v172, v173
v_mfma_f32_16x16x32_bf16 a[156:159], a[76:79], v[236:239], a[156:159]
v_exp_f32 v175, v175
v_mfma_f32_16x16x32_bf16 a[100:103], a[76:79], v[248:251], a[100:103]
s_waitcnt  lgkmcnt(7)
v_cvt_pk_bf16_f32 v231, v142, v143
v_cvt_pk_bf16_f32 v247, v174, v175
v_mfma_f32_16x16x32_bf16 a[108:111], a[76:79], v[252:255], a[108:111]
ds_read_b128 a[76:79], v63 offset:7168
s_cbranch_scc1 .Lev_d1
.LD0:
v_mfma_f32_16x16x32_bf16 v[128:131], a[16:19], v[64:67], v[192:195]
s_cmp_lt_u32 s44, s45
v_exp_f32 v144, v144
v_mfma_f32_16x16x32_bf16 v[128:131], a[24:27], v[68:71], v[128:131]
s_cselect_b32 m0, s46, s27
v_mfma_f32_16x16x32_bf16 v[136:139], a[16:19], v[72:75], v[196:199]
v_exp_f32 v145, v145
v_mfma_f32_16x16x32_bf16 v[136:139], a[24:27], v[76:79], v[136:139]
v_exp_f32 v176, v176
v_mfma_f32_16x16x32_bf16 v[160:163], a[16:19], v[80:83], v[200:203]
s_sub_u32 s52, s52, 1
s_cselect_b32 s52, 0, s52
s_cselect_b32 s14, s16, 0
v_mfma_f32_16x16x32_bf16 v[160:163], a[24:27], v[84:87], v[160:163]
v_exp_f32 v177, v177
v_mfma_f32_16x16x32_bf16 v[168:171], a[16:19], v[88:91], v[204:207]
v_exp_f32 v146, v146
v_mfma_f32_16x16x32_bf16 v[168:171], a[24:27], v[92:95], v[168:171]
ds_read_b128 a[16:19], v62 offset:8192
v_mfma_f32_16x16x32_bf16 v[132:135], a[20:23], v[64:67], v[192:195]
buffer_load_dwordx4 v7, s[8:11], s44 offen offset:0 sc1 lds
v_mfma_f32_16x16x32_bf16 v[132:135], a[28:31], v[68:71], v[132:135]
s_cmp_lg_u32 s14, 0
s_cselect_b32 s17, 786432, 0
v_mfma_f32_16x16x32_bf16 v[140:143], a[20:23], v[72:75], v[196:199]
v_exp_f32 v147, v147
v_mfma_f32_16x16x32_bf16 v[140:143], a[28:31], v[76:79], v[140:143]
ds_read_b128 a[24:27], v62 offset:9216
v_mfma_f32_16x16x32_bf16 v[164:167], a[20:23], v[80:83], v[200:203]
s_xor_b32 s50, s46, 0x18000
v_exp_f32 v178, v178
v_mfma_f32_16x16x32_bf16 v[164:167], a[28:31], v[84:87], v[164:167]
v_mfma_f32_16x16x32_bf16 v[172:175], a[20:23], v[88:91], v[204:207]
v_exp_f32 v179, v179
v_mfma_f32_16x16x32_bf16 v[172:175], a[28:31], v[92:95], v[172:175]
ds_read_b128 a[20:23], v62 offset:12288
v_mfma_f32_16x16x32_bf16 v[128:131], a[32:35], v[96:99], v[128:131]
v_exp_f32 v148, v148
v_mfma_f32_16x16x32_bf16 v[128:131], a[40:43], v[100:103], v[128:131]
v_mfma_f32_16x16x32_bf16 v[136:139], a[32:35], v[104:107], v[136:139]
v_exp_f32 v149, v149
v_mfma_f32_16x16x32_bf16 v[136:139], a[40:43], v[108:111], v[136:139]
ds_read_b128 a[28:31], v62 offset:13312
v_mfma_f32_16x16x32_bf16 v[160:163], a[32:35], v[112:115], v[160:163]
buffer_load_dwordx4 v7, s[8:11], s44 offen offset:1024 sc1 lds
v_mfma_f32_16x16x32_bf16 v[160:163], a[40:43], v[116:119], v[160:163]
v_exp_f32 v180, v180
v_mfma_f32_16x16x32_bf16 v[168:171], a[32:35], v[120:123], v[168:171]
v_exp_f32 v181, v181
v_mfma_f32_16x16x32_bf16 v[168:171], a[40:43], v[124:127], v[168:171]
ds_read_b128 a[32:35], v62 offset:10240
v_mfma_f32_16x16x32_bf16 v[132:135], a[36:39], v[96:99], v[132:135]
v_mfma_f32_16x16x32_bf16 v[132:135], a[44:47], v[100:103], v[132:135]
v_exp_f32 v150, v150
v_mfma_f32_16x16x32_bf16 v[140:143], a[36:39], v[104:107], v[140:143]
v_exp_f32 v151, v151
v_mfma_f32_16x16x32_bf16 v[140:143], a[44:47], v[108:111], v[140:143]
ds_read_b128 a[40:43], v62 offset:11264
v_mfma_f32_16x16x32_bf16 v[164:167], a[36:39], v[112:115], v[164:167]
v_mfma_f32_16x16x32_bf16 v[164:167], a[44:47], v[116:119], v[164:167]
v_exp_f32 v182, v182
v_mfma_f32_16x16x32_bf16 v[172:175], a[36:39], v[120:123], v[172:175]
v_exp_f32 v183, v183
v_mfma_f32_16x16x32_bf16 v[172:175], a[44:47], v[124:127], v[172:175]
ds_read_b128 a[36:39], v62 offset:14336
ds_read_b128 a[44:47], v62 offset:15360
v_mfma_f32_16x16x32_bf16 v[208:211], v[52:55], v[224:227], v[208:211]
v_add_u32 v62, s46, v32
v_mfma_f32_16x16x32_bf16 v[212:215], v[52:55], v[228:231], v[212:215]
v_exp_f32 v152, v152
v_mfma_f32_16x16x32_bf16 v[216:219], v[52:55], v[240:243], v[216:219]
v_mfma_f32_16x16x32_bf16 v[220:223], v[52:55], v[244:247], v[220:223]
s_waitcnt lgkmcnt(8)
v_exp_f32 v153, v153
v_mfma_f32_16x16x32_bf16 a[0:3], a[48:51], v[224:227], a[0:3]
buffer_load_dwordx4 v7, s[8:11], s44 offen offset:2048 sc1 lds
v_mfma_f32_16x16x32_bf16 a[8:11], a[48:51], v[228:231], a[8:11]
v_exp_f32 v184, v184
v_mfma_f32_16x16x32_bf16 a[176:179], a[48:51], v[240:243], a[176:179]
v_mfma_f32_16x16x32_bf16 a[184:187], a[48:51], v[244:247], a[184:187]
ds_read_b128 a[48:51], v63 offset:8192
v_mfma_f32_16x16x32_bf16 a[4:7], a[52:55], v[224:227], a[4:7]
v_exp_f32 v185, v185
v_mfma_f32_16x16x32_bf16 a[12:15], a[52:55], v[228:231], a[12:15]
v_cvt_pk_bf16_f32 v232, v144, v145
v_mfma_f32_16x16x32_bf16 a[180:183], a[52:55], v[240:243], a[180:183]
v_exp_f32 v154, v154
v_mfma_f32_16x16x32_bf16 a[188:191], a[52:55], v[244:247], a[188:191]
ds_read_b128 a[52:55], v63 offset:9216
v_cvt_pk_bf16_f32 v248, v176, v177
v_mfma_f32_16x16x32_bf16 a[80:83], a[56:59], v[224:227], a[80:83]
v_exp_f32 v155, v155
v_mfma_f32_16x16x32_bf16 a[88:91], a[56:59], v[228:231], a[88:91]
v_cvt_pk_bf16_f32 v233, v146, v147
v_cvt_pk_bf16_f32 v249, v178, v179
v_mfma_f32_16x16x32_bf16 a[208:211], a[56:59], v[240:243], a[208:211]
v_exp_f32 v186, v186
v_mfma_f32_16x16x32_bf16 a[216:219], a[56:59], v[244:247], a[216:219]
ds_read_b128 a[56:59], v63 offset:10240
v_cvt_pk_bf16_f32 v234, v148, v149
v_mfma_f32_16x16x32_bf16 a[84:87], a[60:63], v[224:227], a[84:87]
buffer_load_dwordx4 v7, s[8:11], s44 offen offset:3072 sc1 lds
v_mfma_f32_16x16x32_bf16 a[92:95], a[60:63], v[228:231], a[92:95]
v_exp_f32 v187, v187
v_mfma_f32_16x16x32_bf16 a[212:215], a[60:63], v[240:243], a[212:215]
v_exp_f32 v156, v156
v_mfma_f32_16x16x32_bf16 a[220:223], a[60:63], v[244:247], a[220:223]
ds_read_b128 a[60:63], v63 offset:11264
v_cvt_pk_bf16_f32 v250, v180, v181
v_mfma_f32_16x16x32_bf16 a[112:115], a[64:67], v[224:227], a[112:115]
s_add_u32 s44, s44, 16384
v_exp_f32 v157, v157
v_mfma_f32_16x16x32_bf16 a[120:123], a[64:67], v[228:231], a[120:123]
v_exp_f32 v188, v188
v_mfma_f32_16x16x32_bf16 a[240:243], a[64:67], v[240:243], a[240:243]
buffer_load_dword v35, v2, s[12:15], s53 offen
v_mfma_f32_16x16x32_bf16 a[248:251], a[64:67], v[244:247], a[248:251]
s_add_u32 s53, s53, s17
ds_read_b128 a[64:67], v63 offset:12288
v_mfma_f32_16x16x32_bf16 a[116:119], a[68:71], v[224:227], a[116:119]
s_sub_u32 s55, s55, 1
v_exp_f32 v189, v189
v_mfma_f32_16x16x32_bf16 a[124:127], a[68:71], v[228:231], a[124:127]
v_cvt_pk_bf16_f32 v235, v150, v151
v_cvt_pk_bf16_f32 v251, v182, v183
v_mfma_f32_16x16x32_bf16 a[244:247], a[68:71], v[240:243], a[244:247]
v_exp_f32 v158, v158
v_mfma_f32_16x16x32_bf16 a[252:255], a[68:71], v[244:247], a[252:255]
ds_read_b128 a[68:71], v63 offset:13312
v_cvt_pk_bf16_f32 v236, v152, v153
v_mfma_f32_16x16x32_bf16 a[144:147], a[72:75], v[224:227], a[144:147]
v_exp_f32 v159, v159
v_mfma_f32_16x16x32_bf16 a[152:155], a[72:75], v[228:231], a[152:155]
v_cvt_pk_bf16_f32 v252, v184, v185
v_cvt_pk_bf16_f32 v237, v154, v155
v_mfma_f32_16x16x32_bf16 a[96:99], a[72:75], v[240:243], a[96:99]
v_exp_f32 v190, v190
v_mfma_f32_16x16x32_bf16 a[104:107], a[72:75], v[244:247], a[104:107]
ds_read_b128 a[72:75], v63 offset:14336
v_cvt_pk_bf16_f32 v253, v186, v187
v_mfma_f32_16x16x32_bf16 a[148:151], a[76:79], v[224:227], a[148:151]
v_cvt_pk_bf16_f32 v238, v156, v157
v_cvt_pk_bf16_f32 v254, v188, v189
v_mfma_f32_16x16x32_bf16 a[156:159], a[76:79], v[228:231], a[156:159]
s_waitcnt vmcnt(10) lgkmcnt(7)
v_exp_f32 v191, v191
v_mfma_f32_16x16x32_bf16 a[100:103], a[76:79], v[240:243], a[100:103]
s_barrier
v_cvt_pk_bf16_f32 v239, v158, v159
v_cvt_pk_bf16_f32 v255, v190, v191
v_mfma_f32_16x16x32_bf16 a[108:111], a[76:79], v[244:247], a[108:111]
ds_read_b128 a[76:79], v63 offset:15360
s_cbranch_scc0 .LD1
s_cmp_eq_u32 s43, s42
s_cbranch_scc1 .LT1
s_swappc_b64 s[30:31], s[58:59]
.Lev_d0_r:
s_branch .LD1
.LT0:
v_mfma_f32_16x16x32_bf16 v[128:131], a[16:19], v[64:67], v[192:195]
s_cmp_lt_u32 s44, s45
v_exp_f32 v144, v144
v_mfma_f32_16x16x32_bf16 v[128:131], a[24:27], v[68:71], v[128:131]
s_cselect_b32 m0, s46, s27
v_cmp_gt_i32 s[32:33], 0, v8
v_nop
v_mfma_f32_16x16x32_bf16 v[136:139], a[16:19], v[72:75], v[196:199]
s_add_u32 s43, s42, 4
v_exp_f32 v145, v145
v_mfma_f32_16x16x32_bf16 v[136:139], a[24:27], v[76:79], v[136:139]
s_mov_b32 s42, s56
v_cmp_gt_i32 s[34:35], 1, v8
v_cndmask_b32 v16, v192, v34, s[32:33]
v_mfma_f32_16x16x32_bf16 v[160:163], a[16:19], v[80:83], v[36:39]
s_max_u32 s55, s57, s43
v_exp_f32 v176, v176
v_mfma_f32_16x16x32_bf16 v[160:163], a[24:27], v[84:87], v[160:163]
s_min_u32 s55, s55, s42
v_cmp_gt_i32 s[32:33], 2, v8
v_cndmask_b32 v17, v192, v34, s[34:35]
v_mfma_f32_16x16x32_bf16 v[168:171], a[16:19], v[88:91], v[44:47]
s_sub_u32 s93, s55, s43
v_exp_f32 v177, v177
v_mfma_f32_16x16x32_bf16 v[168:171], a[24:27], v[92:95], v[168:171]
ds_read_b128 a[16:19], v62 offset:8192
v_cmp_gt_i32 s[34:35], 3, v8
v_mfma_f32_16x16x32_bf16 v[132:135], a[20:23], v[64:67], v[192:195]
buffer_load_dwordx4 v7, s[8:11], s44 offen offset:0 sc1 lds
v_mfma_f32_16x16x32_bf16 v[132:135], a[28:31], v[68:71], v[132:135]
s_mov_b32 s43, s55
v_exp_f32 v146, v146
v_mfma_f32_16x16x32_bf16 v[140:143], a[20:23], v[72:75], v[196:199]
s_mov_b32 s55, s93
v_exp_f32 v147, v147
v_mfma_f32_16x16x32_bf16 v[140:143], a[28:31], v[76:79], v[140:143]
ds_read_b128 a[24:27], v62 offset:9216
v_cndmask_b32 v18, v192, v34, s[32:33]
v_mfma_f32_16x16x32_bf16 v[164:167], a[20:23], v[80:83], v[40:43]
s_xor_b32 s50, s46, 0x18000
v_exp_f32 v178, v178
v_mfma_f32_16x16x32_bf16 v[164:167], a[28:31], v[84:87], v[164:167]
v_cmp_gt_i32 s[32:33], 16, v8
v_cndmask_b32 v19, v192, v34, s[34:35]
v_mfma_f32_16x16x32_bf16 v[172:175], a[20:23], v[88:91], v[48:51]
v_exp_f32 v179, v179
v_mfma_f32_16x16x32_bf16 v[172:175], a[28:31], v[92:95], v[172:175]
ds_read_b128 a[20:23], v62 offset:12288
v_cmp_gt_i32 s[34:35], 17, v8
v_mfma_f32_16x16x32_bf16 v[128:131], a[32:35], v[96:99], v[128:131]
v_exp_f32 v148, v148
v_mfma_f32_16x16x32_bf16 v[128:131], a[40:43], v[100:103], v[128:131]
v_cndmask_b32 v20, v192, v34, s[32:33]
v_cmp_gt_i32 s[32:33], 18, v8
v_mfma_f32_16x16x32_bf16 v[136:139], a[32:35], v[104:107], v[136:139]
v_exp_f32 v149, v149
v_mfma_f32_16x16x32_bf16 v[136:139], a[40:43], v[108:111], v[136:139]
ds_read_b128 a[28:31], v62 offset:13312
v_cndmask_b32 v21, v192, v34, s[34:35]
v_mfma_f32_16x16x32_bf16 v[160:163], a[32:35], v[112:115], v[160:163]
buffer_load_dwordx4 v7, s[8:11], s44 offen offset:1024 sc1 lds
v_mfma_f32_16x16x32_bf16 v[160:163], a[40:43], v[116:119], v[160:163]
v_exp_f32 v180, v180
v_mfma_f32_16x16x32_bf16 v[168:171], a[32:35], v[120:123], v[168:171]
v_exp_f32 v181, v181
v_mfma_f32_16x16x32_bf16 v[168:171], a[40:43], v[124:127], v[168:171]
ds_read_b128 a[32:35], v62 offset:10240
v_cmp_gt_i32 s[34:35], 19, v8
v_mfma_f32_16x16x32_bf16 v[132:135], a[36:39], v[96:99], v[132:135]
v_cndmask_b32 v22, v192, v34, s[32:33]
v_cmp_gt_i32 s[32:33], -16, v8
v_mfma_f32_16x16x32_bf16 v[132:135], a[44:47], v[100:103], v[132:135]
v_exp_f32 v150, v150
v_mfma_f32_16x16x32_bf16 v[140:143], a[36:39], v[104:107], v[140:143]
v_exp_f32 v151, v151
v_mfma_f32_16x16x32_bf16 v[140:143], a[44:47], v[108:111], v[140:143]
ds_read_b128 a[40:43], v62 offset:11264
v_cndmask_b32 v23, v192, v34, s[34:35]
v_mfma_f32_16x16x32_bf16 v[164:167], a[36:39], v[112:115], v[164:167]
v_cmp_gt_i32 s[34:35], -15, v8
v_cndmask_b32 v24, v196, v34, s[32:33]
v_mfma_f32_16x16x32_bf16 v[164:167], a[44:47], v[116:119], v[164:167]
v_exp_f32 v182, v182
v_mfma_f32_16x16x32_bf16 v[172:175], a[36:39], v[120:123], v[172:175]
v_exp_f32 v183, v183
v_mfma_f32_16x16x32_bf16 v[172:175], a[44:47], v[124:127], v[172:175]
ds_read_b128 a[36:39], v62 offset:14336
ds_read_b128 a[44:47], v62 offset:15360
v_mfma_f32_16x16x32_bf16 v[208:211], v[52:55], v[224:227], v[208:211]
v_add_u32 v62, s46, v32
v_cmp_gt_i32 s[32:33], -14, v8
v_mfma_f32_16x16x32_bf16 v[212:215], v[52:55], v[228:231], v[212:215]
v_exp_f32 v152, v152
v_mfma_f32_16x16x32_bf16 v[216:219], v[52:55], v[240:243], v[216:219]
v_cndmask_b32 v25, v196, v34, s[34:35]
v_cmp_gt_i32 s[34:35], -13, v8
v_mfma_f32_16x16x32_bf16 v[220:223], v[52:55], v[244:247], v[220:223]
s_waitcnt lgkmcnt(8)
v_exp_f32 v153, v153
v_mfma_f32_16x16x32_bf16 a[0:3], a[48:51], v[224:227], a[0:3]
buffer_load_dwordx4 v7, s[8:11], s44 offen offset:2048 sc1 lds
v_mfma_f32_16x16x32_bf16 a[8:11], a[48:51], v[228:231], a[8:11]
v_exp_f32 v184, v184
v_mfma_f32_16x16x32_bf16 a[176:179], a[48:51], v[240:243], a[176:179]
v_cndmask_b32 v26, v196, v34, s[32:33]
v_cmp_gt_i32 s[32:33], 0, v8
v_mfma_f32_16x16x32_bf16 a[184:187], a[48:51], v[244:247], a[184:187]
ds_read_b128 a[48:51], v63 offset:8192
v_cndmask_b32 v27, v196, v34, s[34:35]
v_mfma_f32_16x16x32_bf16 a[4:7], a[52:55], v[224:227], a[4:7]
v_exp_f32 v185, v185
v_mfma_f32_16x16x32_bf16 a[12:15], a[52:55], v[228:231], a[12:15]
v_cmp_gt_i32 s[34:35], 1, v8
v_cndmask_b32 v28, v196, v34, s[32:33]
v_mfma_f32_16x16x32_bf16 a[180:183], a[52:55], v[240:243], a[180:183]
v_exp_f32 v154, v154
v_mfma_f32_16x16x32_bf16 a[188:191], a[52:55], v[244:247], a[188:191]
ds_read_b128 a[52:55], v63 offset:9216
v_cmp_gt_i32 s[32:33], 2, v8
v_mfma_f32_16x16x32_bf16 a[80:83], a[56:59], v[224:227], a[80:83]
v_exp_f32 v155, v155
v_mfma_f32_16x16x32_bf16 a[88:91], a[56:59], v[228:231], a[88:91]
v_cvt_pk_bf16_f32 v232, v144, v145
v_cndmask_b32 v29, v196, v34, s[34:35]
v_mfma_f32_16x16x32_bf16 a[208:211], a[56:59], v[240:243], a[208:211]
v_exp_f32 v186, v186
v_mfma_f32_16x16x32_bf16 a[216:219], a[56:59], v[244:247], a[216:219]
ds_read_b128 a[56:59], v63 offset:10240
v_cvt_pk_bf16_f32 v248, v176, v177
v_mfma_f32_16x16x32_bf16 a[84:87], a[60:63], v[224:227], a[84:87]
buffer_load_dwordx4 v7, s[8:11], s44 offen offset:3072 sc1 lds
v_mfma_f32_16x16x32_bf16 a[92:95], a[60:63], v[228:231], a[92:95]
v_exp_f32 v187, v187
v_mfma_f32_16x16x32_bf16 a[212:215], a[60:63], v[240:243], a[212:215]
v_exp_f32 v156, v156
v_mfma_f32_16x16x32_bf16 a[220:223], a[60:63], v[244:247], a[220:223]
ds_read_b128 a[60:63], v63 offset:11264
v_cvt_pk_bf16_f32 v233, v146, v147
v_mfma_f32_16x16x32_bf16 a[112:115], a[64:67], v[224:227], a[112:115]
s_add_u32 s44, s44, 16384
v_exp_f32 v157, v157
v_mfma_f32_16x16x32_bf16 a[120:123], a[64:67], v[228:231], a[120:123]
v_cvt_pk_bf16_f32 v249, v178, v179
v_cvt_pk_bf16_f32 v234, v148, v149
v_mfma_f32_16x16x32_bf16 a[240:243], a[64:67], v[240:243], a[240:243]
v_exp_f32 v188, v188
v_mfma_f32_16x16x32_bf16 a[248:251], a[64:67], v[244:247], a[248:251]
ds_read_b128 a[64:67], v63 offset:12288
.Lpad11:
s_cbranch_execz .Ltch3
v_cvt_pk_bf16_f32 v250, v180, v181
v_mfma_f32_16x16x32_bf16 a[116:119], a[68:71], v[224:227], a[116:119]
v_exp_f32 v189, v189
v_mfma_f32_16x16x32_bf16 a[124:127], a[68:71], v[228:231], a[124:127]
v_cvt_pk_bf16_f32 v235, v150, v151
v_cvt_pk_bf16_f32 v251, v182, v183
v_mfma_f32_16x16x32_bf16 a[244:247], a[68:71], v[240:243], a[244:247]
v_exp_f32 v158, v158
v_mfma_f32_16x16x32_bf16 a[252:255], a[68:71], v[244:247], a[252:255]
ds_read_b128 a[68:71], v63 offset:13312
v_cvt_pk_bf16_f32 v236, v152, v153
v_mfma_f32_16x16x32_bf16 a[144:147], a[72:75], v[224:227], a[144:147]
v_exp_f32 v159, v159
v_cmp_gt_i32 s[34:35], 3, v8
v_mfma_f32_16x16x32_bf16 a[152:155], a[72:75], v[228:231], a[152:155]
v_cvt_pk_bf16_f32 v252, v184, v185
v_cvt_pk_bf16_f32 v237, v154, v155
v_mfma_f32_16x16x32_bf16 a[96:99], a[72:75], v[240:243], a[96:99]
v_exp_f32 v190, v190
v_cndmask_b32 v30, v196, v34, s[32:33]
v_mfma_f32_16x16x32_bf16 a[104:107], a[72:75], v[244:247], a[104:107]
ds_read_b128 a[72:75], v63 offset:14336
v_cvt_pk_bf16_f32 v253, v186, v187
v_mfma_f32_16x16x32_bf16 a[148:151], a[76:79], v[224:227], a[148:151]
v_cvt_pk_bf16_f32 v238, v156, v157
v_cvt_pk_bf16_f32 v254, v188, v189
v_nop
v_mfma_f32_16x16x32_bf16 a[156:159], a[76:79], v[228:231], a[156:159]
s_waitcnt vmcnt(9) lgkmcnt(7)
v_exp_f32 v191, v191
v_mfma_f32_16x16x32_bf16 a[100:103], a[76:79], v[240:243], a[100:103]
s_barrier
v_cvt_pk_bf16_f32 v239, v158, v159
v_cvt_pk_bf16_f32 v255, v190, v191
v_cndmask_b32 v31, v196, v34, s[34:35]
v_mfma_f32_16x16x32_bf16 a[108:111], a[76:79], v[244:247], a[108:111]
ds_read_b128 a[76:79], v63 offset:15360
v_mfma_f32_16x16x32_bf16 v[144:147], a[16:19], v[64:67], v[192:195]
s_cmp_lt_u32 s44, s54
v_exp_f32 v128, v128
v_mfma_f32_16x16x32_bf16 v[144:147], a[24:27], v[68:71], v[144:147]
s_cselect_b32 m0, s50, s27
v_exp_f32 v129, v129
v_mfma_f32_16x16x32_bf16 v[152:155], a[16:19], v[72:75], v[196:199]
s_and_b32 s36, s86, 1
s_lshl_b32 s36, s36, 3
v_add_u32 v63, s49, v33
v_exp_f32 v160, v160
v_mfma_f32_16x16x32_bf16 v[152:155], a[24:27], v[76:79], v[152:155]
ds_read_b128 a[16:19], v62 offset:0
ds_read_b128 a[24:27], v62 offset:1024
v_mfma_f32_16x16x32_bf16 v[148:151], a[20:23], v[64:67], v[192:195]
s_and_b32 s28, s87, 7
s_or_b32 s36, s36, s28
v_exp_f32 v161, v161
v_mfma_f32_16x16x32_bf16 v[148:151], a[28:31], v[68:71], v[148:151]
s_lshr_b32 s37, s86, 1
s_lshl_b32 s37, s37, 2
v_exp_f32 v130, v130
v_mfma_f32_16x16x32_bf16 v[156:159], a[20:23], v[72:75], v[196:199]
buffer_load_dwordx4 v7, s[4:7], s44 offen offset:0 sc1 lds
v_mfma_f32_16x16x32_bf16 v[156:159], a[28:31], v[76:79], v[156:159]
ds_read_b128 a[20:23], v62 offset:4096
ds_read_b128 a[28:31], v62 offset:5120
v_mfma_f32_16x16x32_bf16 v[144:147], a[32:35], v[96:99], v[144:147]
s_lshr_b32 s28, s87, 3
s_or_b32 s37, s37, s28
v_exp_f32 v131, v131
v_mfma_f32_16x16x32_bf16 v[144:147], a[40:43], v[100:103], v[144:147]
s_mov_b32 s49, s46
v_exp_f32 v162, v162
v_mfma_f32_16x16x32_bf16 v[152:155], a[32:35], v[104:107], v[152:155]
s_lshl_b32 s24, s36, 18
s_lshl_b32 s28, s3, 16
v_exp_f32 v163, v163
v_mfma_f32_16x16x32_bf16 v[152:155], a[40:43], v[108:111], v[152:155]
ds_read_b128 a[32:35], v62 offset:2048
ds_read_b128 a[40:43], v62 offset:3072
v_mfma_f32_16x16x32_bf16 v[148:151], a[36:39], v[96:99], v[148:151]
s_add_u32 s24, s24, s28
s_mov_b32 s25, 0
v_exp_f32 v132, v132
v_mfma_f32_16x16x32_bf16 v[148:151], a[44:47], v[100:103], v[148:151]
s_add_u32 s46, s46, 16384
v_exp_f32 v133, v133
v_mfma_f32_16x16x32_bf16 v[156:159], a[36:39], v[104:107], v[156:159]
s_mul_i32 s26, s3, 98304
s_lshl_b32 s28, s37, 7
v_exp_f32 v164, v164
v_mfma_f32_16x16x32_bf16 v[156:159], a[44:47], v[108:111], v[156:159]
ds_read_b128 a[36:39], v62 offset:6144
ds_read_b128 a[44:47], v62 offset:7168
v_mfma_f32_16x16x32_bf16 v[208:211], v[52:55], v[232:235], v[208:211]
buffer_load_dwordx4 v7, s[4:7], s44 offen offset:1024 sc1 lds
v_mfma_f32_16x16x32_bf16 v[212:215], v[52:55], v[236:239], v[212:215]
s_add_u32 s26, s26, s28
s_add_u32 s47, s26, 4096
v_exp_f32 v165, v165
v_mfma_f32_16x16x32_bf16 v[216:219], v[52:55], v[248:251], v[216:219]
s_mul_i32 s48, s3, 7168
s_mov_b32 s12, s64
v_exp_f32 v134, v134
v_mfma_f32_16x16x32_bf16 v[220:223], v[52:55], v[252:255], v[220:223]
s_waitcnt lgkmcnt(8)
s_and_b32 s46, s46, 0xffff
v_cvt_pk_bf16_f32 v224, v128, v129
v_mfma_f32_16x16x32_bf16 a[0:3], a[48:51], v[232:235], a[0:3]
s_and_b32 s13, s65, 0xffff
s_mov_b32 s14, 0x400000
v_exp_f32 v135, v135
v_mfma_f32_16x16x32_bf16 a[8:11], a[48:51], v[236:239], a[8:11]
s_mov_b32 s16, s66
s_and_b32 s17, s67, 0xffff
v_cvt_pk_bf16_f32 v240, v160, v161
v_mfma_f32_16x16x32_bf16 a[176:179], a[48:51], v[248:251], a[176:179]
s_mov_b32 s18, 0x600000
s_lshl_b32 s29, s36, 18
v_exp_f32 v166, v166
v_mfma_f32_16x16x32_bf16 a[184:187], a[48:51], v[252:255], a[184:187]
ds_read_b128 a[48:51], v63 offset:0
v_exp_f32 v167, v167
v_mfma_f32_16x16x32_bf16 a[4:7], a[52:55], v[232:235], a[4:7]
s_lshl_b32 s28, s3, 13
s_add_u32 s29, s29, s28
v_cvt_pk_bf16_f32 v225, v130, v131
v_mfma_f32_16x16x32_bf16 a[12:15], a[52:55], v[236:239], a[12:15]
s_sub_u32 s29, s29, 34816
v_exp_f32 v136, v136
v_mfma_f32_16x16x32_bf16 a[180:183], a[52:55], v[248:251], a[180:183]
s_cmp_eq_u32 s36, 0
s_cselect_b32 s29, 0x7f000000, s29
v_exp_f32 v137, v137
v_mfma_f32_16x16x32_bf16 a[188:191], a[52:55], v[252:255], a[188:191]
ds_read_b128 a[52:55], v63 offset:1024
v_cvt_pk_bf16_f32 v241, v162, v163
v_mfma_f32_16x16x32_bf16 a[80:83], a[56:59], v[232:235], a[80:83]
buffer_load_dwordx4 v7, s[4:7], s44 offen offset:2048 sc1 lds
v_mfma_f32_16x16x32_bf16 a[88:91], a[56:59], v[236:239], a[88:91]
s_lshl_b32 s32, s3, 2
s_and_b32 s33, s3, 1
v_exp_f32 v168, v168
v_mfma_f32_16x16x32_bf16 a[208:211], a[56:59], v[248:251], a[208:211]
s_lshl_b32 s33, s33, 1
s_lshr_b32 s34, s3, 1
v_exp_f32 v169, v169
v_mfma_f32_16x16x32_bf16 a[216:219], a[56:59], v[252:255], a[216:219]
ds_read_b128 a[56:59], v63 offset:2048
v_exp_f32 v138, v138
v_mfma_f32_16x16x32_bf16 a[84:87], a[60:63], v[232:235], a[84:87]
s_lshl_b32 s35, s34, 13
s_add_u32 s35, s35, 65536
v_cvt_pk_bf16_f32 v226, v132, v133
v_mfma_f32_16x16x32_bf16 a[92:95], a[60:63], v[236:239], a[92:95]
s_mov_b32 s38, 131072
s_lshl_b32 s28, s88, 13
v_exp_f32 v139, v139
v_mfma_f32_16x16x32_bf16 a[212:215], a[60:63], v[248:251], a[212:215]
s_add_u32 s28, s28, 33554432
v_exp_f32 v170, v170
v_mfma_f32_16x16x32_bf16 a[220:223], a[60:63], v[252:255], a[220:223]
ds_read_b128 a[60:63], v63 offset:3072
v_cvt_pk_bf16_f32 v242, v164, v165
v_mfma_f32_16x16x32_bf16 a[112:115], a[64:67], v[232:235], a[112:115]
buffer_load_dwordx4 v7, s[4:7], s44 offen offset:3072 sc1 lds
v_mfma_f32_16x16x32_bf16 a[120:123], a[64:67], v[236:239], a[120:123]
s_add_u32 s20, s84, s28
s_addc_u32 s21, s85, 0
v_exp_f32 v171, v171
v_mfma_f32_16x16x32_bf16 a[240:243], a[64:67], v[248:251], a[240:243]
s_and_b32 s21, s21, 0xffff
s_mov_b32 s22, 16777216
v_exp_f32 v140, v140
v_mfma_f32_16x16x32_bf16 a[248:251], a[64:67], v[252:255], a[248:251]
ds_read_b128 a[64:67], v63 offset:4096
v_exp_f32 v141, v141
v_mfma_f32_16x16x32_bf16 a[116:119], a[68:71], v[232:235], a[116:119]
s_sub_u32 s40, 15, s89
s_lshl_b32 s40, s40, 2
v_cvt_pk_bf16_f32 v227, v134, v135
v_mfma_f32_16x16x32_bf16 a[124:127], a[68:71], v[236:239], a[124:127]
s_add_u32 s40, s40, s3
s_lshl_b32 s40, s40, 18
v_exp_f32 v172, v172
v_mfma_f32_16x16x32_bf16 a[244:247], a[68:71], v[248:251], a[244:247]
s_lshl_b32 s41, s89, 2
s_add_u32 s41, s41, s94
v_exp_f32 v173, v173
v_mfma_f32_16x16x32_bf16 a[252:255], a[68:71], v[252:255], a[252:255]
ds_read_b128 a[68:71], v63 offset:5120
v_cvt_pk_bf16_f32 v243, v166, v167
v_mfma_f32_16x16x32_bf16 a[144:147], a[72:75], v[232:235], a[144:147]
s_lshl_b32 s41, s41, 18
s_mov_b32 s28, 1602224128
v_exp_f32 v142, v142
v_mfma_f32_16x16x32_bf16 a[152:155], a[72:75], v[236:239], a[152:155]
v_cvt_pk_bf16_f32 v228, v136, v137
v_cvt_pk_bf16_f32 v244, v168, v169
v_mfma_f32_16x16x32_bf16 a[96:99], a[72:75], v[248:251], a[96:99]
v_exp_f32 v143, v143
v_mfma_f32_16x16x32_bf16 a[104:107], a[72:75], v[252:255], a[104:107]
ds_read_b128 a[72:75], v63 offset:6144
v_exp_f32 v174, v174
v_mfma_f32_16x16x32_bf16 a[148:151], a[76:79], v[232:235], a[148:151]
v_cvt_pk_bf16_f32 v229, v138, v139
v_cvt_pk_bf16_f32 v245, v170, v171
v_mfma_f32_16x16x32_bf16 a[156:159], a[76:79], v[236:239], a[156:159]
v_cvt_pk_bf16_f32 v230, v140, v141
v_exp_f32 v175, v175
v_mfma_f32_16x16x32_bf16 a[100:103], a[76:79], v[248:251], a[100:103]
s_waitcnt  lgkmcnt(7)
v_cvt_pk_bf16_f32 v246, v172, v173
v_cvt_pk_bf16_f32 v231, v142, v143
v_cvt_pk_bf16_f32 v247, v174, v175
v_mfma_f32_16x16x32_bf16 a[108:111], a[76:79], v[252:255], a[108:111]
ds_read_b128 a[76:79], v63 offset:7168
v_mfma_f32_16x16x32_bf16 v[128:131], a[16:19], v[64:67], v[192:195]
s_cmp_lt_u32 s44, s45
v_exp_f32 v144, v144
v_mfma_f32_16x16x32_bf16 v[128:131], a[24:27], v[68:71], v[128:131]
s_cselect_b32 m0, s46, s27
v_lshrrev_b32 v56, 3, v1
v_and_b32 v57, 7, v1
v_mfma_f32_16x16x32_bf16 v[136:139], a[16:19], v[72:75], v[196:199]
v_xor_b32 v58, v57, v56
v_lshlrev_b32 v58, 4, v58
v_mfma_f32_16x16x32_bf16 v[136:139], a[24:27], v[76:79], v[136:139]
ds_read_b128 a[16:19], v62 offset:8192
ds_read_b128 a[24:27], v62 offset:9216
v_lshl_add_u32 v36, v56, 11, v58
v_mfma_f32_16x16x32_bf16 v[132:135], a[20:23], v[64:67], v[192:195]
v_exp_f32 v145, v145
v_add_u32 v37, 15360, v36
v_mfma_f32_16x16x32_bf16 v[132:135], a[28:31], v[68:71], v[132:135]
v_exp_f32 v146, v146
v_add_u32 v38, 30720, v36
v_mfma_f32_16x16x32_bf16 v[140:143], a[20:23], v[72:75], v[196:199]
buffer_load_dwordx4 v7, s[8:11], s44 offen offset:0 sc1 lds
v_add_u32 v39, 46080, v36
v_mfma_f32_16x16x32_bf16 v[140:143], a[28:31], v[76:79], v[140:143]
ds_read_b128 a[20:23], v62 offset:12288
ds_read_b128 a[28:31], v62 offset:13312
v_mfma_f32_16x16x32_bf16 v[128:131], a[32:35], v[96:99], v[128:131]
v_add_u32 v58, s32, v56
v_xor_b32 v58, v58, v57
v_mfma_f32_16x16x32_bf16 v[128:131], a[40:43], v[100:103], v[128:131]
s_xor_b32 s50, s46, 0x18000
v_and_b32 v58, 7, v58
v_lshlrev_b32 v58, 4, v58
v_mfma_f32_16x16x32_bf16 v[136:139], a[32:35], v[104:107], v[136:139]
v_exp_f32 v147, v147
v_lshl_add_u32 v58, v56, 11, v58
v_mfma_f32_16x16x32_bf16 v[136:139], a[40:43], v[108:111], v[136:139]
ds_read_b128 a[32:35], v62 offset:10240
ds_read_b128 a[40:43], v62 offset:11264
v_mfma_f32_16x16x32_bf16 v[132:135], a[36:39], v[96:99], v[132:135]
v_add_u32 v58, s29, v58
v_and_b32 v59, 32, v1
v_mfma_f32_16x16x32_bf16 v[132:135], a[44:47], v[100:103], v[132:135]
v_exp_f32 v148, v148
v_lshlrev_b32 v59, 25, v59
v_mfma_f32_16x16x32_bf16 v[140:143], a[36:39], v[104:107], v[140:143]
v_or_b32 v42, v59, v58
v_bfe_u32 v58, v1, 4, 1
v_mfma_f32_16x16x32_bf16 v[140:143], a[44:47], v[108:111], v[140:143]
ds_read_b128 a[36:39], v62 offset:14336
ds_read_b128 a[44:47], v62 offset:15360
v_lshlrev_b32 v58, 1, v58
v_mfma_f32_16x16x32_bf16 v[208:211], v[52:55], v[224:227], v[208:211]
buffer_load_dwordx4 v7, s[8:11], s44 offen offset:1024 sc1 lds
v_add_u32 v62, s46, v32
v_mfma_f32_16x16x32_bf16 v[212:215], v[52:55], v[228:231], v[212:215]
v_exp_f32 v149, v149
v_xor_b32 v58, v58, v57
v_mfma_f32_16x16x32_bf16 v[216:219], v[52:55], v[240:243], v[216:219]
v_lshlrev_b32 v58, 4, v58
v_mul_u32_u24 v59, 6144, v56
v_mfma_f32_16x16x32_bf16 v[220:223], v[52:55], v[244:247], v[220:223]
s_waitcnt lgkmcnt(8)
v_exp_f32 v150, v150
v_add_u32 v40, v59, v58
v_mfma_f32_16x16x32_bf16 a[0:3], a[48:51], v[224:227], a[0:3]
v_xor_b32 v41, 64, v40
v_add_u32 v41, 48128, v41
v_mfma_f32_16x16x32_bf16 a[8:11], a[48:51], v[228:231], a[8:11]
v_and_b32 v56, 15, v1
v_lshrrev_b32 v57, 4, v1
v_mfma_f32_16x16x32_bf16 a[176:179], a[48:51], v[240:243], a[176:179]
v_exp_f32 v151, v151
v_mfma_f32_16x16x32_bf16 a[184:187], a[48:51], v[244:247], a[184:187]
ds_read_b128 a[48:51], v63 offset:8192
v_and_b32 v58, 7, v56
v_mfma_f32_16x16x32_bf16 a[4:7], a[52:55], v[224:227], a[4:7]
v_xor_b32 v58, v58, v57
v_lshlrev_b32 v58, 4, v58
v_mfma_f32_16x16x32_bf16 a[12:15], a[52:55], v[228:231], a[12:15]
v_lshl_add_u32 v43, v56, 7, v58
v_add_u32 v43, s35, v43
v_mfma_f32_16x16x32_bf16 a[180:183], a[52:55], v[240:243], a[180:183]
v_exp_f32 v152, v152
v_mfma_f32_16x16x32_bf16 a[188:191], a[52:55], v[244:247], a[188:191]
ds_read_b128 a[52:55], v63 offset:9216
v_xor_b32 v44, 64, v43
v_mfma_f32_16x16x32_bf16 a[80:83], a[56:59], v[224:227], a[80:83]
buffer_load_dwordx4 v7, s[8:11], s44 offen offset:2048 sc1 lds
v_and_b32 v56, 15, v1
v_mfma_f32_16x16x32_bf16 a[88:91], a[56:59], v[228:231], a[88:91]
v_exp_f32 v153, v153
v_lshrrev_b32 v57, 2, v56
v_mfma_f32_16x16x32_bf16 a[208:211], a[56:59], v[240:243], a[208:211]
v_mul_u32_u24 v57, 6656, v57
v_add_u32 v57, 71680, v57
v_add_f32 v13, v216, v220
v_mfma_f32_16x16x32_bf16 a[216:219], a[56:59], v[244:247], a[216:219]
ds_read_b128 a[56:59], v63 offset:10240
v_mov_b32 v58, -2048
v_mfma_f32_16x16x32_bf16 a[84:87], a[60:63], v[224:227], a[84:87]
v_exp_f32 v154, v154
v_mfma_f32_16x16x32_bf16 a[92:95], a[60:63], v[228:231], a[92:95]
v_mov_b32 v59, s34
v_cmp_eq_u32 vcc, 0, v59
v_mfma_f32_16x16x32_bf16 a[212:215], a[60:63], v[240:243], a[212:215]
v_exp_f32 v155, v155
v_nop
v_mfma_f32_16x16x32_bf16 a[220:223], a[60:63], v[244:247], a[220:223]
ds_read_b128 a[60:63], v63 offset:11264
v_nop
v_mfma_f32_16x16x32_bf16 a[112:115], a[64:67], v[224:227], a[112:115]
buffer_load_dwordx4 v7, s[8:11], s44 offen offset:3072 sc1 lds
v_cndmask_b32 v57, v58, v57, vcc
v_mfma_f32_16x16x32_bf16 a[120:123], a[64:67], v[228:231], a[120:123]
v_add_u32 v45, v57, v43
v_add_u32 v46, v57, v44
v_mfma_f32_16x16x32_bf16 a[240:243], a[64:67], v[240:243], a[240:243]
v_exp_f32 v156, v156
v_mfma_f32_16x16x32_bf16 a[248:251], a[64:67], v[244:247], a[248:251]
ds_read_b128 a[64:67], v63 offset:12288
v_bfe_u32 v56, v1, 2, 2
v_mfma_f32_16x16x32_bf16 a[116:119], a[68:71], v[224:227], a[116:119]
s_add_u32 s44, s44, 16384
v_bfe_u32 v57, v1, 4, 1
v_lshl_add_u32 v58, v57, 3, v56
v_mfma_f32_16x16x32_bf16 a[124:127], a[68:71], v[228:231], a[124:127]
v_lshlrev_b32 v58, 7, v58
v_lshrrev_b32 v59, 5, v1
v_mfma_f32_16x16x32_bf16 a[244:247], a[68:71], v[240:243], a[244:247]
v_exp_f32 v157, v157
v_mfma_f32_16x16x32_bf16 a[252:255], a[68:71], v[244:247], a[252:255]
ds_read_b128 a[68:71], v63 offset:13312
v_mul_u32_u24 v59, 7168, v59
v_add3_u32 v58, v58, v59, s38
v_mfma_f32_16x16x32_bf16 a[144:147], a[72:75], v[224:227], a[144:147]
v_cvt_pk_bf16_f32 v232, v144, v145
v_lshrrev_b32 v56, 1, v56
v_cmp_nge_f32 s[52:53], s28, v13
v_mfma_f32_16x16x32_bf16 a[152:155], a[72:75], v[228:231], a[152:155]
v_exp_f32 v158, v158
v_lshl_or_b32 v56, v57, 1, v56
v_mfma_f32_16x16x32_bf16 a[96:99], a[72:75], v[240:243], a[96:99]
v_cvt_pk_bf16_f32 v233, v146, v147
v_cvt_pk_bf16_f32 v234, v148, v149
v_xor_b32 v56, s33, v56
v_mfma_f32_16x16x32_bf16 a[104:107], a[72:75], v[244:247], a[104:107]
ds_read_b128 a[72:75], v63 offset:14336
v_cvt_pk_bf16_f32 v235, v150, v151
v_lshl_add_u32 v58, v56, 5, v58
v_mfma_f32_16x16x32_bf16 a[148:151], a[76:79], v[224:227], a[148:151]
v_exp_f32 v159, v159
v_and_b32 v56, 3, v1
v_lshl_add_u32 v47, v56, 3, v58
v_mfma_f32_16x16x32_bf16 a[156:159], a[76:79], v[228:231], a[156:159]
s_waitcnt vmcnt(8) lgkmcnt(7)
v_cvt_pk_bf16_f32 v236, v152, v153
v_cvt_pk_bf16_f32 v237, v154, v155
v_xor_b32 v48, 32, v47
v_mfma_f32_16x16x32_bf16 a[100:103], a[76:79], v[240:243], a[100:103]
s_barrier
v_cvt_pk_bf16_f32 v238, v156, v157
v_cvt_pk_bf16_f32 v239, v158, v159
v_add_u32 v51, 4096, v7
v_mfma_f32_16x16x32_bf16 a[108:111], a[76:79], v[244:247], a[108:111]
ds_read_b128 a[76:79], v63 offset:15360
s_cmp_le_u32 s94, 3
s_cbranch_scc1 .LTf0
.LTc0:
s_cmp_lt_i32 s55, 0
s_cbranch_scc1 .LE1
s_branch .LS1
.LTf0:
s_cmp_lg_u64 s[52:53], 0
s_cbranch_scc1 .LTc0
s_getpc_b64 s[30:31]
.LTp0:
s_sub_u32 s30, s30, .LTp0-.LTc0
s_subb_u32 s31, s31, 0
s_branch .Lfb
.LT1_pad:
s_cbranch_execz .Ltch4
.LT1:
v_mfma_f32_16x16x32_bf16 v[144:147], a[16:19], v[64:67], v[192:195]
s_cmp_lt_u32 s44, s54
v_exp_f32 v128, v128
v_mfma_f32_16x16x32_bf16 v[144:147], a[24:27], v[68:71], v[144:147]
s_cselect_b32 m0, s50, s27
v_exp_f32 v129, v129
v_mfma_f32_16x16x32_bf16 v[152:155], a[16:19], v[72:75], v[196:199]
s_add_u32 s43, s42, 4
v_add_u32 v63, s49, v33
v_cmp_gt_i32 s[32:33], 0, v8
v_mfma_f32_16x16x32_bf16 v[152:155], a[24:27], v[76:79], v[152:155]
s_mov_b32 s42, s56
v_nop
v_cmp_gt_i32 s[34:35], 1, v8
v_mfma_f32_16x16x32_bf16 v[176:179], a[16:19], v[80:83], v[36:39]
s_max_u32 s55, s57, s43
v_exp_f32 v160, v160
v_mfma_f32_16x16x32_bf16 v[176:179], a[24:27], v[84:87], v[176:179]
s_min_u32 s55, s55, s42
v_cndmask_b32 v16, v192, v34, s[32:33]
v_cmp_gt_i32 s[32:33], 2, v8
v_mfma_f32_16x16x32_bf16 v[184:187], a[16:19], v[88:91], v[44:47]
s_sub_u32 s93, s55, s43
v_exp_f32 v161, v161
v_mfma_f32_16x16x32_bf16 v[184:187], a[24:27], v[92:95], v[184:187]
ds_read_b128 a[16:19], v62 offset:0
v_cndmask_b32 v17, v192, v34, s[34:35]
v_mfma_f32_16x16x32_bf16 v[148:151], a[20:23], v[64:67], v[192:195]
buffer_load_dwordx4 v7, s[4:7], s44 offen offset:0 sc1 lds
v_mfma_f32_16x16x32_bf16 v[148:151], a[28:31], v[68:71], v[148:151]
s_mov_b32 s43, s55
v_exp_f32 v130, v130
v_mfma_f32_16x16x32_bf16 v[156:159], a[20:23], v[72:75], v[196:199]
s_mov_b32 s55, s93
v_exp_f32 v131, v131
v_mfma_f32_16x16x32_bf16 v[156:159], a[28:31], v[76:79], v[156:159]
ds_read_b128 a[24:27], v62 offset:1024
v_cmp_gt_i32 s[34:35], 3, v8
v_mfma_f32_16x16x32_bf16 v[180:183], a[20:23], v[80:83], v[40:43]
s_mov_b32 s49, s46
v_exp_f32 v162, v162
v_mfma_f32_16x16x32_bf16 v[180:183], a[28:31], v[84:87], v[180:183]
v_cndmask_b32 v18, v192, v34, s[32:33]
v_cmp_gt_i32 s[32:33], 16, v8
v_mfma_f32_16x16x32_bf16 v[188:191], a[20:23], v[88:91], v[48:51]
v_exp_f32 v163, v163
v_mfma_f32_16x16x32_bf16 v[188:191], a[28:31], v[92:95], v[188:191]
ds_read_b128 a[20:23], v62 offset:4096
v_cndmask_b32 v19, v192, v34, s[34:35]
v_mfma_f32_16x16x32_bf16 v[144:147], a[32:35], v[96:99], v[144:147]
v_exp_f32 v132, v132
v_mfma_f32_16x16x32_bf16 v[144:147], a[40:43], v[100:103], v[144:147]
v_cmp_gt_i32 s[34:35], 17, v8
v_cndmask_b32 v20, v192, v34, s[32:33]
v_mfma_f32_16x16x32_bf16 v[152:155], a[32:35], v[104:107], v[152:155]
s_add_u32 s46, s46, 16384
v_exp_f32 v133, v133
v_mfma_f32_16x16x32_bf16 v[152:155], a[40:43], v[108:111], v[152:155]
ds_read_b128 a[28:31], v62 offset:5120
v_cmp_gt_i32 s[32:33], 18, v8
v_mfma_f32_16x16x32_bf16 v[176:179], a[32:35], v[112:115], v[176:179]
buffer_load_dwordx4 v7, s[4:7], s44 offen offset:1024 sc1 lds
v_mfma_f32_16x16x32_bf16 v[176:179], a[40:43], v[116:119], v[176:179]
v_exp_f32 v164, v164
v_mfma_f32_16x16x32_bf16 v[184:187], a[32:35], v[120:123], v[184:187]
v_exp_f32 v165, v165
v_mfma_f32_16x16x32_bf16 v[184:187], a[40:43], v[124:127], v[184:187]
ds_read_b128 a[32:35], v62 offset:2048
v_cndmask_b32 v21, v192, v34, s[34:35]
v_mfma_f32_16x16x32_bf16 v[148:151], a[36:39], v[96:99], v[148:151]
v_cmp_gt_i32 s[34:35], 19, v8
v_cndmask_b32 v22, v192, v34, s[32:33]
v_mfma_f32_16x16x32_bf16 v[148:151], a[44:47], v[100:103], v[148:151]
v_exp_f32 v134, v134
v_mfma_f32_16x16x32_bf16 v[156:159], a[36:39], v[104:107], v[156:159]
s_and_b32 s46, s46, 0xffff
v_exp_f32 v135, v135
v_mfma_f32_16x16x32_bf16 v[156:159], a[44:47], v[108:111], v[156:159]
ds_read_b128 a[40:43], v62 offset:3072
v_cmp_gt_i32 s[32:33], -16, v8
v_mfma_f32_16x16x32_bf16 v[180:183], a[36:39], v[112:115], v[180:183]
v_cndmask_b32 v23, v192, v34, s[34:35]
v_cmp_gt_i32 s[34:35], -15, v8
v_mfma_f32_16x16x32_bf16 v[180:183], a[44:47], v[116:119], v[180:183]
v_exp_f32 v166, v166
v_mfma_f32_16x16x32_bf16 v[188:191], a[36:39], v[120:123], v[188:191]
v_cndmask_b32 v24, v196, v34, s[32:33]
v_cmp_gt_i32 s[32:33], -14, v8
v_mfma_f32_16x16x32_bf16 v[188:191], a[44:47], v[124:127], v[188:191]
ds_read_b128 a[36:39], v62 offset:6144
ds_read_b128 a[44:47], v62 offset:7168
v_mfma_f32_16x16x32_bf16 v[208:211], v[52:55], v[232:235], v[208:211]
v_exp_f32 v167, v167
v_mfma_f32_16x16x32_bf16 v[212:215], v[52:55], v[236:239], v[212:215]
v_exp_f32 v136, v136
v_mfma_f32_16x16x32_bf16 v[216:219], v[52:55], v[248:251], v[216:219]
v_cndmask_b32 v25, v196, v34, s[34:35]
v_cmp_gt_i32 s[34:35], -13, v8
v_mfma_f32_16x16x32_bf16 v[220:223], v[52:55], v[252:255], v[220:223]
s_waitcnt lgkmcnt(8)
v_exp_f32 v137, v137
v_mfma_f32_16x16x32_bf16 a[0:3], a[48:51], v[232:235], a[0:3]
buffer_load_dwordx4 v7, s[4:7], s44 offen offset:2048 sc1 lds
v_mfma_f32_16x16x32_bf16 a[8:11], a[48:51], v[236:239], a[8:11]
v_exp_f32 v168, v168
v_mfma_f32_16x16x32_bf16 a[176:179], a[48:51], v[248:251], a[176:179]
v_cndmask_b32 v26, v196, v34, s[32:33]
v_cmp_gt_i32 s[32:33], 0, v8
v_mfma_f32_16x16x32_bf16 a[184:187], a[48:51], v[252:255], a[184:187]
ds_read_b128 a[48:51], v63 offset:0
v_cndmask_b32 v27, v196, v34, s[34:35]
v_mfma_f32_16x16x32_bf16 a[4:7], a[52:55], v[232:235], a[4:7]
v_exp_f32 v169, v169
v_mfma_f32_16x16x32_bf16 a[12:15], a[52:55], v[236:239], a[12:15]
v_cmp_gt_i32 s[34:35], 1, v8
v_cndmask_b32 v28, v196, v34, s[32:33]
v_mfma_f32_16x16x32_bf16 a[180:183], a[52:55], v[248:251], a[180:183]
v_exp_f32 v138, v138
v_mfma_f32_16x16x32_bf16 a[188:191], a[52:55], v[252:255], a[188:191]
ds_read_b128 a[52:55], v63 offset:1024
v_cmp_gt_i32 s[32:33], 2, v8
v_mfma_f32_16x16x32_bf16 a[80:83], a[56:59], v[232:235], a[80:83]
v_exp_f32 v139, v139
v_mfma_f32_16x16x32_bf16 a[88:91], a[56:59], v[236:239], a[88:91]
v_cvt_pk_bf16_f32 v224, v128, v129
v_cndmask_b32 v29, v196, v34, s[34:35]
v_mfma_f32_16x16x32_bf16 a[208:211], a[56:59], v[248:251], a[208:211]
v_exp_f32 v170, v170
v_mfma_f32_16x16x32_bf16 a[216:219], a[56:59], v[252:255], a[216:219]
ds_read_b128 a[56:59], v63 offset:2048
v_cvt_pk_bf16_f32 v240, v160, v161
v_mfma_f32_16x16x32_bf16 a[84:87], a[60:63], v[232:235], a[84:87]
buffer_load_dwordx4 v7, s[4:7], s44 offen offset:3072 sc1 lds
v_mfma_f32_16x16x32_bf16 a[92:95], a[60:63], v[236:239], a[92:95]
v_exp_f32 v171, v171
v_mfma_f32_16x16x32_bf16 a[212:215], a[60:63], v[248:251], a[212:215]
v_exp_f32 v140, v140
v_mfma_f32_16x16x32_bf16 a[220:223], a[60:63], v[252:255], a[220:223]
ds_read_b128 a[60:63], v63 offset:3072
v_cvt_pk_bf16_f32 v225, v130, v131
v_mfma_f32_16x16x32_bf16 a[112:115], a[64:67], v[232:235], a[112:115]
v_exp_f32 v141, v141
v_mfma_f32_16x16x32_bf16 a[120:123], a[64:67], v[236:239], a[120:123]
v_cvt_pk_bf16_f32 v241, v162, v163
v_cvt_pk_bf16_f32 v226, v132, v133
v_mfma_f32_16x16x32_bf16 a[240:243], a[64:67], v[248:251], a[240:243]
v_exp_f32 v172, v172
v_mfma_f32_16x16x32_bf16 a[248:251], a[64:67], v[252:255], a[248:251]
ds_read_b128 a[64:67], v63 offset:4096
v_cvt_pk_bf16_f32 v242, v164, v165
v_mfma_f32_16x16x32_bf16 a[116:119], a[68:71], v[232:235], a[116:119]
v_exp_f32 v173, v173
v_mfma_f32_16x16x32_bf16 a[124:127], a[68:71], v[236:239], a[124:127]
v_cvt_pk_bf16_f32 v227, v134, v135
v_cvt_pk_bf16_f32 v243, v166, v167
v_mfma_f32_16x16x32_bf16 a[244:247], a[68:71], v[248:251], a[244:247]
v_exp_f32 v142, v142
v_mfma_f32_16x16x32_bf16 a[252:255], a[68:71], v[252:255], a[252:255]
ds_read_b128 a[68:71], v63 offset:5120
v_cvt_pk_bf16_f32 v228, v136, v137
v_mfma_f32_16x16x32_bf16 a[144:147], a[72:75], v[232:235], a[144:147]
v_exp_f32 v143, v143
v_cmp_gt_i32 s[34:35], 3, v8
v_mfma_f32_16x16x32_bf16 a[152:155], a[72:75], v[236:239], a[152:155]
v_cvt_pk_bf16_f32 v244, v168, v169
v_cvt_pk_bf16_f32 v229, v138, v139
v_mfma_f32_16x16x32_bf16 a[96:99], a[72:75], v[248:251], a[96:99]
v_exp_f32 v174, v174
v_cndmask_b32 v30, v196, v34, s[32:33]
v_mfma_f32_16x16x32_bf16 a[104:107], a[72:75], v[252:255], a[104:107]
ds_read_b128 a[72:75], v63 offset:6144
v_cvt_pk_bf16_f32 v245, v170, v171
v_mfma_f32_16x16x32_bf16 a[148:151], a[76:79], v[232:235], a[148:151]
v_cvt_pk_bf16_f32 v230, v140, v141
v_cvt_pk_bf16_f32 v246, v172, v173
v_nop
v_mfma_f32_16x16x32_bf16 a[156:159], a[76:79], v[236:239], a[156:159]
v_exp_f32 v175, v175
v_mfma_f32_16x16x32_bf16 a[100:103], a[76:79], v[248:251], a[100:103]
s_waitcnt  lgkmcnt(7)
v_cvt_pk_bf16_f32 v231, v142, v143
v_cvt_pk_bf16_f32 v247, v174, v175
v_cndmask_b32 v31, v196, v34, s[34:35]
v_mfma_f32_16x16x32_bf16 a[108:111], a[76:79], v[252:255], a[108:111]
ds_read_b128 a[76:79], v63 offset:7168
v_mfma_f32_16x16x32_bf16 v[128:131], a[16:19], v[64:67], v[192:195]
s_cmp_lt_u32 s44, s45
v_exp_f32 v144, v144
v_mfma_f32_16x16x32_bf16 v[128:131], a[24:27], v[68:71], v[128:131]
s_cselect_b32 m0, s46, s27
v_exp_f32 v145, v145
v_mfma_f32_16x16x32_bf16 v[136:139], a[16:19], v[72:75], v[196:199]
s_and_b32 s36, s86, 1
s_lshl_b32 s36, s36, 3
v_exp_f32 v176, v176
v_mfma_f32_16x16x32_bf16 v[136:139], a[24:27], v[76:79], v[136:139]
ds_read_b128 a[16:19], v62 offset:8192
ds_read_b128 a[24:27], v62 offset:9216
v_mfma_f32_16x16x32_bf16 v[132:135], a[20:23], v[64:67], v[192:195]
s_and_b32 s28, s87, 7
s_or_b32 s36, s36, s28
v_exp_f32 v177, v177
v_mfma_f32_16x16x32_bf16 v[132:135], a[28:31], v[68:71], v[132:135]
s_lshr_b32 s37, s86, 1
s_lshl_b32 s37, s37, 2
v_exp_f32 v146, v146
v_mfma_f32_16x16x32_bf16 v[140:143], a[20:23], v[72:75], v[196:199]
buffer_load_dwordx4 v7, s[8:11], s44 offen offset:0 sc1 lds
v_mfma_f32_16x16x32_bf16 v[140:143], a[28:31], v[76:79], v[140:143]
ds_read_b128 a[20:23], v62 offset:12288
ds_read_b128 a[28:31], v62 offset:13312
v_mfma_f32_16x16x32_bf16 v[128:131], a[32:35], v[96:99], v[128:131]
s_lshr_b32 s28, s87, 3
s_or_b32 s37, s37, s28
v_exp_f32 v147, v147
v_mfma_f32_16x16x32_bf16 v[128:131], a[40:43], v[100:103], v[128:131]
s_xor_b32 s50, s46, 0x18000
v_exp_f32 v178, v178
v_mfma_f32_16x16x32_bf16 v[136:139], a[32:35], v[104:107], v[136:139]
s_lshl_b32 s24, s36, 18
s_lshl_b32 s28, s3, 16
v_exp_f32 v179, v179
v_mfma_f32_16x16x32_bf16 v[136:139], a[40:43], v[108:111], v[136:139]
ds_read_b128 a[32:35], v62 offset:10240
ds_read_b128 a[40:43], v62 offset:11264
v_mfma_f32_16x16x32_bf16 v[132:135], a[36:39], v[96:99], v[132:135]
s_add_u32 s24, s24, s28
s_mov_b32 s25, 0
v_exp_f32 v148, v148
v_mfma_f32_16x16x32_bf16 v[132:135], a[44:47], v[100:103], v[132:135]
s_mul_i32 s26, s3, 98304
s_lshl_b32 s28, s37, 7
v_exp_f32 v149, v149
v_mfma_f32_16x16x32_bf16 v[140:143], a[36:39], v[104:107], v[140:143]
.Lpad12:
s_cbranch_execz .Ltch3
s_add_u32 s26, s26, s28
s_add_u32 s47, s26, 4096
v_exp_f32 v180, v180
v_mfma_f32_16x16x32_bf16 v[140:143], a[44:47], v[108:111], v[140:143]
ds_read_b128 a[36:39], v62 offset:14336
ds_read_b128 a[44:47], v62 offset:15360
v_mfma_f32_16x16x32_bf16 v[208:211], v[52:55], v[224:227], v[208:211]
buffer_load_dwordx4 v7, s[8:11], s44 offen offset:1024 sc1 lds
v_add_u32 v62, s46, v32
v_mfma_f32_16x16x32_bf16 v[212:215], v[52:55], v[228:231], v[212:215]
s_mul_i32 s48, s3, 7168
s_mov_b32 s12, s64
v_exp_f32 v181, v181
v_mfma_f32_16x16x32_bf16 v[216:219], v[52:55], v[240:243], v[216:219]
s_and_b32 s13, s65, 0xffff
s_mov_b32 s14, 0x400000
v_exp_f32 v150, v150
v_mfma_f32_16x16x32_bf16 v[220:223], v[52:55], v[244:247], v[220:223]
s_waitcnt lgkmcnt(8)
v_cvt_pk_bf16_f32 v232, v144, v145
v_cvt_pk_bf16_f32 v248, v176, v177
v_mfma_f32_16x16x32_bf16 a[0:3], a[48:51], v[224:227], a[0:3]
s_mov_b32 s16, s66
s_and_b32 s17, s67, 0xffff
v_exp_f32 v151, v151
v_mfma_f32_16x16x32_bf16 a[8:11], a[48:51], v[228:231], a[8:11]
s_mov_b32 s18, 0x600000
s_lshl_b32 s29, s36, 18
v_cvt_pk_bf16_f32 v233, v146, v147
v_mfma_f32_16x16x32_bf16 a[176:179], a[48:51], v[240:243], a[176:179]
s_lshl_b32 s28, s3, 13
s_add_u32 s29, s29, s28
v_exp_f32 v182, v182
v_mfma_f32_16x16x32_bf16 a[184:187], a[48:51], v[244:247], a[184:187]
ds_read_b128 a[48:51], v63 offset:8192
v_exp_f32 v183, v183
v_mfma_f32_16x16x32_bf16 a[4:7], a[52:55], v[224:227], a[4:7]
s_sub_u32 s29, s29, 34816
v_cvt_pk_bf16_f32 v249, v178, v179
v_cvt_pk_bf16_f32 v234, v148, v149
v_mfma_f32_16x16x32_bf16 a[12:15], a[52:55], v[228:231], a[12:15]
s_cmp_eq_u32 s36, 0
s_cselect_b32 s29, 0x7f000000, s29
v_exp_f32 v152, v152
v_mfma_f32_16x16x32_bf16 a[180:183], a[52:55], v[240:243], a[180:183]
s_lshl_b32 s32, s3, 2
s_and_b32 s33, s3, 1
v_exp_f32 v153, v153
v_mfma_f32_16x16x32_bf16 a[188:191], a[52:55], v[244:247], a[188:191]
ds_read_b128 a[52:55], v63 offset:9216
v_cvt_pk_bf16_f32 v250, v180, v181
v_mfma_f32_16x16x32_bf16 a[80:83], a[56:59], v[224:227], a[80:83]
buffer_load_dwordx4 v7, s[8:11], s44 offen offset:2048 sc1 lds
v_mfma_f32_16x16x32_bf16 a[88:91], a[56:59], v[228:231], a[88:91]
s_lshl_b32 s33, s33, 1
s_lshr_b32 s34, s3, 1
v_exp_f32 v184, v184
v_mfma_f32_16x16x32_bf16 a[208:211], a[56:59], v[240:243], a[208:211]
s_lshl_b32 s35, s34, 13
s_add_u32 s35, s35, 65536
v_exp_f32 v185, v185
v_mfma_f32_16x16x32_bf16 a[216:219], a[56:59], v[244:247], a[216:219]
ds_read_b128 a[56:59], v63 offset:10240
v_exp_f32 v154, v154
v_mfma_f32_16x16x32_bf16 a[84:87], a[60:63], v[224:227], a[84:87]
s_mov_b32 s38, 131072
s_lshl_b32 s28, s88, 13
v_cvt_pk_bf16_f32 v235, v150, v151
v_mfma_f32_16x16x32_bf16 a[92:95], a[60:63], v[228:231], a[92:95]
s_add_u32 s28, s28, 33554432
v_exp_f32 v155, v155
v_mfma_f32_16x16x32_bf16 a[212:215], a[60:63], v[240:243], a[212:215]
s_add_u32 s20, s84, s28
s_addc_u32 s21, s85, 0
v_exp_f32 v186, v186
v_mfma_f32_16x16x32_bf16 a[220:223], a[60:63], v[244:247], a[220:223]
ds_read_b128 a[60:63], v63 offset:11264
v_cvt_pk_bf16_f32 v251, v182, v183
v_mfma_f32_16x16x32_bf16 a[112:115], a[64:67], v[224:227], a[112:115]
buffer_load_dwordx4 v7, s[8:11], s44 offen offset:3072 sc1 lds
v_mfma_f32_16x16x32_bf16 a[120:123], a[64:67], v[228:231], a[120:123]
s_and_b32 s21, s21, 0xffff
s_mov_b32 s22, 16777216
v_exp_f32 v187, v187
v_mfma_f32_16x16x32_bf16 a[240:243], a[64:67], v[240:243], a[240:243]
s_sub_u32 s40, 15, s89
s_lshl_b32 s40, s40, 2
v_exp_f32 v156, v156
v_mfma_f32_16x16x32_bf16 a[248:251], a[64:67], v[244:247], a[248:251]
ds_read_b128 a[64:67], v63 offset:12288
v_cvt_pk_bf16_f32 v236, v152, v153
v_mfma_f32_16x16x32_bf16 a[116:119], a[68:71], v[224:227], a[116:119]
s_add_u32 s44, s44, 16384
v_exp_f32 v157, v157
v_mfma_f32_16x16x32_bf16 a[124:127], a[68:71], v[228:231], a[124:127]
s_add_u32 s40, s40, s3
s_lshl_b32 s40, s40, 18
v_exp_f32 v188, v188
v_mfma_f32_16x16x32_bf16 a[244:247], a[68:71], v[240:243], a[244:247]
s_lshl_b32 s41, s89, 2
s_add_u32 s41, s41, s94
v_exp_f32 v189, v189
v_mfma_f32_16x16x32_bf16 a[252:255], a[68:71], v[244:247], a[252:255]
ds_read_b128 a[68:71], v63 offset:13312
v_cvt_pk_bf16_f32 v252, v184, v185
v_mfma_f32_16x16x32_bf16 a[144:147], a[72:75], v[224:227], a[144:147]
s_lshl_b32 s41, s41, 18
s_mov_b32 s28, 1602224128
v_exp_f32 v158, v158
v_mfma_f32_16x16x32_bf16 a[152:155], a[72:75], v[228:231], a[152:155]
v_cvt_pk_bf16_f32 v237, v154, v155
v_cvt_pk_bf16_f32 v253, v186, v187
v_mfma_f32_16x16x32_bf16 a[96:99], a[72:75], v[240:243], a[96:99]
v_exp_f32 v159, v159
v_mfma_f32_16x16x32_bf16 a[104:107], a[72:75], v[244:247], a[104:107]
ds_read_b128 a[72:75], v63 offset:14336
v_exp_f32 v190, v190
v_mfma_f32_16x16x32_bf16 a[148:151], a[76:79], v[224:227], a[148:151]
v_cvt_pk_bf16_f32 v238, v156, v157
v_cvt_pk_bf16_f32 v254, v188, v189
v_mfma_f32_16x16x32_bf16 a[156:159], a[76:79], v[228:231], a[156:159]
s_waitcnt vmcnt(9) lgkmcnt(7)
v_exp_f32 v191, v191
v_mfma_f32_16x16x32_bf16 a[100:103], a[76:79], v[240:243], a[100:103]
s_barrier
v_cvt_pk_bf16_f32 v239, v158, v159
v_cvt_pk_bf16_f32 v255, v190, v191
v_mfma_f32_16x16x32_bf16 a[108:111], a[76:79], v[244:247], a[108:111]
ds_read_b128 a[76:79], v63 offset:15360
v_mfma_f32_16x16x32_bf16 v[144:147], a[16:19], v[64:67], v[192:195]
s_cmp_lt_u32 s44, s54
v_exp_f32 v128, v128
v_mfma_f32_16x16x32_bf16 v[144:147], a[24:27], v[68:71], v[144:147]
s_cselect_b32 m0, s50, s27
v_lshrrev_b32 v56, 3, v1
v_and_b32 v57, 7, v1
v_mfma_f32_16x16x32_bf16 v[152:155], a[16:19], v[72:75], v[196:199]
v_add_u32 v63, s49, v33
v_xor_b32 v58, v57, v56
v_lshlrev_b32 v58, 4, v58
v_mfma_f32_16x16x32_bf16 v[152:155], a[24:27], v[76:79], v[152:155]
ds_read_b128 a[16:19], v62 offset:0
ds_read_b128 a[24:27], v62 offset:1024
v_lshl_add_u32 v36, v56, 11, v58
v_mfma_f32_16x16x32_bf16 v[148:151], a[20:23], v[64:67], v[192:195]
v_exp_f32 v129, v129
v_add_u32 v37, 15360, v36
v_mfma_f32_16x16x32_bf16 v[148:151], a[28:31], v[68:71], v[148:151]
v_exp_f32 v130, v130
v_add_u32 v38, 30720, v36
v_mfma_f32_16x16x32_bf16 v[156:159], a[20:23], v[72:75], v[196:199]
buffer_load_dwordx4 v7, s[4:7], s44 offen offset:0 sc1 lds
v_add_u32 v39, 46080, v36
v_mfma_f32_16x16x32_bf16 v[156:159], a[28:31], v[76:79], v[156:159]
ds_read_b128 a[20:23], v62 offset:4096
ds_read_b128 a[28:31], v62 offset:5120
v_mfma_f32_16x16x32_bf16 v[144:147], a[32:35], v[96:99], v[144:147]
v_add_u32 v58, s32, v56
v_xor_b32 v58, v58, v57
v_mfma_f32_16x16x32_bf16 v[144:147], a[40:43], v[100:103], v[144:147]
s_mov_b32 s49, s46
v_and_b32 v58, 7, v58
v_lshlrev_b32 v58, 4, v58
v_mfma_f32_16x16x32_bf16 v[152:155], a[32:35], v[104:107], v[152:155]
v_exp_f32 v131, v131
v_lshl_add_u32 v58, v56, 11, v58
v_mfma_f32_16x16x32_bf16 v[152:155], a[40:43], v[108:111], v[152:155]
ds_read_b128 a[32:35], v62 offset:2048
ds_read_b128 a[40:43], v62 offset:3072
v_mfma_f32_16x16x32_bf16 v[148:151], a[36:39], v[96:99], v[148:151]
v_add_u32 v58, s29, v58
v_and_b32 v59, 32, v1
v_mfma_f32_16x16x32_bf16 v[148:151], a[44:47], v[100:103], v[148:151]
s_add_u32 s46, s46, 16384
v_exp_f32 v132, v132
v_lshlrev_b32 v59, 25, v59
v_mfma_f32_16x16x32_bf16 v[156:159], a[36:39], v[104:107], v[156:159]
v_or_b32 v42, v59, v58
v_bfe_u32 v58, v1, 4, 1
v_mfma_f32_16x16x32_bf16 v[156:159], a[44:47], v[108:111], v[156:159]
ds_read_b128 a[36:39], v62 offset:6144
ds_read_b128 a[44:47], v62 offset:7168
v_lshlrev_b32 v58, 1, v58
v_mfma_f32_16x16x32_bf16 v[208:211], v[52:55], v[232:235], v[208:211]
buffer_load_dwordx4 v7, s[4:7], s44 offen offset:1024 sc1 lds
v_xor_b32 v58, v58, v57
v_mfma_f32_16x16x32_bf16 v[212:215], v[52:55], v[236:239], v[212:215]
v_exp_f32 v133, v133
v_mfma_f32_16x16x32_bf16 v[216:219], v[52:55], v[248:251], v[216:219]
v_lshlrev_b32 v58, 4, v58
v_mul_u32_u24 v59, 6144, v56
v_mfma_f32_16x16x32_bf16 v[220:223], v[52:55], v[252:255], v[220:223]
s_waitcnt lgkmcnt(8)
s_and_b32 s46, s46, 0xffff
v_add_u32 v40, v59, v58
v_mfma_f32_16x16x32_bf16 a[0:3], a[48:51], v[232:235], a[0:3]
v_exp_f32 v134, v134
v_xor_b32 v41, 64, v40
v_mfma_f32_16x16x32_bf16 a[8:11], a[48:51], v[236:239], a[8:11]
v_add_u32 v41, 48128, v41
v_and_b32 v56, 15, v1
v_mfma_f32_16x16x32_bf16 a[176:179], a[48:51], v[248:251], a[176:179]
v_exp_f32 v135, v135
v_lshrrev_b32 v57, 4, v1
v_mfma_f32_16x16x32_bf16 a[184:187], a[48:51], v[252:255], a[184:187]
ds_read_b128 a[48:51], v63 offset:0
v_and_b32 v58, 7, v56
v_mfma_f32_16x16x32_bf16 a[4:7], a[52:55], v[232:235], a[4:7]
v_xor_b32 v58, v58, v57
v_lshlrev_b32 v58, 4, v58
v_mfma_f32_16x16x32_bf16 a[12:15], a[52:55], v[236:239], a[12:15]
v_lshl_add_u32 v43, v56, 7, v58
v_add_u32 v43, s35, v43
v_mfma_f32_16x16x32_bf16 a[180:183], a[52:55], v[248:251], a[180:183]
v_exp_f32 v136, v136
v_mfma_f32_16x16x32_bf16 a[188:191], a[52:55], v[252:255], a[188:191]
ds_read_b128 a[52:55], v63 offset:1024
v_xor_b32 v44, 64, v43
v_mfma_f32_16x16x32_bf16 a[80:83], a[56:59], v[232:235], a[80:83]
buffer_load_dwordx4 v7, s[4:7], s44 offen offset:2048 sc1 lds
v_and_b32 v56, 15, v1
v_mfma_f32_16x16x32_bf16 a[88:91], a[56:59], v[236:239], a[88:91]
v_exp_f32 v137, v137
v_lshrrev_b32 v57, 2, v56
v_mfma_f32_16x16x32_bf16 a[208:211], a[56:59], v[248:251], a[208:211]
v_mul_u32_u24 v57, 6656, v57
v_add_u32 v57, 71680, v57
v_add_f32 v13, v216, v220
v_mfma_f32_16x16x32_bf16 a[216:219], a[56:59], v[252:255], a[216:219]
ds_read_b128 a[56:59], v63 offset:2048
v_mov_b32 v58, -2048
v_mfma_f32_16x16x32_bf16 a[84:87], a[60:63], v[232:235], a[84:87]
v_exp_f32 v138, v138
v_mfma_f32_16x16x32_bf16 a[92:95], a[60:63], v[236:239], a[92:95]
v_mov_b32 v59, s34
v_cmp_eq_u32 vcc, 0, v59
v_mfma_f32_16x16x32_bf16 a[212:215], a[60:63], v[248:251], a[212:215]
v_exp_f32 v139, v139
v_nop
v_mfma_f32_16x16x32_bf16 a[220:223], a[60:63], v[252:255], a[220:223]
ds_read_b128 a[60:63], v63 offset:3072
v_nop
v_mfma_f32_16x16x32_bf16 a[112:115], a[64:67], v[232:235], a[112:115]
buffer_load_dwordx4 v7, s[4:7], s44 offen offset:3072 sc1 lds
v_cndmask_b32 v57, v58, v57, vcc
v_mfma_f32_16x16x32_bf16 a[120:123], a[64:67], v[236:239], a[120:123]
v_add_u32 v45, v57, v43
v_add_u32 v46, v57, v44
v_mfma_f32_16x16x32_bf16 a[240:243], a[64:67], v[248:251], a[240:243]
v_exp_f32 v140, v140
v_mfma_f32_16x16x32_bf16 a[248:251], a[64:67], v[252:255], a[248:251]
ds_read_b128 a[64:67], v63 offset:4096
v_bfe_u32 v56, v1, 2, 2
v_mfma_f32_16x16x32_bf16 a[116:119], a[68:71], v[232:235], a[116:119]
v_bfe_u32 v57, v1, 4, 1
v_lshl_add_u32 v58, v57, 3, v56
v_mfma_f32_16x16x32_bf16 a[124:127], a[68:71], v[236:239], a[124:127]
v_lshlrev_b32 v58, 7, v58
v_lshrrev_b32 v59, 5, v1
v_mfma_f32_16x16x32_bf16 a[244:247], a[68:71], v[248:251], a[244:247]
v_exp_f32 v141, v141
v_mfma_f32_16x16x32_bf16 a[252:255], a[68:71], v[252:255], a[252:255]
ds_read_b128 a[68:71], v63 offset:5120
v_mul_u32_u24 v59, 7168, v59
v_add3_u32 v58, v58, v59, s38
v_mfma_f32_16x16x32_bf16 a[144:147], a[72:75], v[232:235], a[144:147]
v_cvt_pk_bf16_f32 v224, v128, v129
v_lshrrev_b32 v56, 1, v56
v_cmp_nge_f32 s[52:53], s28, v13
v_mfma_f32_16x16x32_bf16 a[152:155], a[72:75], v[236:239], a[152:155]
v_exp_f32 v142, v142
v_lshl_or_b32 v56, v57, 1, v56
v_mfma_f32_16x16x32_bf16 a[96:99], a[72:75], v[248:251], a[96:99]
v_cvt_pk_bf16_f32 v225, v130, v131
v_cvt_pk_bf16_f32 v226, v132, v133
v_xor_b32 v56, s33, v56
v_mfma_f32_16x16x32_bf16 a[104:107], a[72:75], v[252:255], a[104:107]
ds_read_b128 a[72:75], v63 offset:6144
v_cvt_pk_bf16_f32 v227, v134, v135
v_lshl_add_u32 v58, v56, 5, v58
v_mfma_f32_16x16x32_bf16 a[148:151], a[76:79], v[232:235], a[148:151]
v_exp_f32 v143, v143
v_and_b32 v56, 3, v1
v_lshl_add_u32 v47, v56, 3, v58
v_mfma_f32_16x16x32_bf16 a[156:159], a[76:79], v[236:239], a[156:159]
v_cvt_pk_bf16_f32 v228, v136, v137
v_cvt_pk_bf16_f32 v229, v138, v139
v_xor_b32 v48, 32, v47
v_mfma_f32_16x16x32_bf16 a[100:103], a[76:79], v[248:251], a[100:103]
s_waitcnt  lgkmcnt(7)
v_cvt_pk_bf16_f32 v230, v140, v141
v_cvt_pk_bf16_f32 v231, v142, v143
v_add_u32 v51, 4096, v7
v_mfma_f32_16x16x32_bf16 a[108:111], a[76:79], v[252:255], a[108:111]
ds_read_b128 a[76:79], v63 offset:7168
s_cmp_le_u32 s94, 3
s_cbranch_scc1 .LTf1
.LTc1:
s_cmp_lt_i32 s55, 0
s_cbranch_scc1 .LE0
s_branch .LS0
.LTf1:
s_cmp_lg_u64 s[52:53], 0
s_cbranch_scc1 .LTc1
s_getpc_b64 s[30:31]
.LTp1:
s_sub_u32 s30, s30, .LTp1-.LTc1
s_subb_u32 s31, s31, 0
s_branch .Lfb
.LS1_pad:
s_cbranch_execz .Ltch4
s_nop 0
s_nop 0
s_nop 0
s_nop 0
s_nop 0
s_nop 0
s_nop 0
.LS1:
v_mfma_f32_16x16x32_bf16 v[144:147], a[16:19], v[64:67], v[192:195]
s_cmp_lt_u32 s44, s54
v_exp_f32 v128, v128
v_mfma_f32_16x16x32_bf16 v[144:147], a[24:27], v[68:71], v[144:147]
s_cselect_b32 m0, s50, s27
v_exp_f32 v129, v129
v_mfma_f32_16x16x32_bf16 v[152:155], a[16:19], v[72:75], v[196:199]
v_add_u32 v63, s49, v33
v_mfma_f32_16x16x32_bf16 v[152:155], a[24:27], v[76:79], v[152:155]
ds_read_b128 a[16:19], v62 offset:0
ds_read_b128 a[24:27], v62 offset:1024
v_mfma_f32_16x16x32_bf16 v[148:151], a[20:23], v[64:67], v[192:195]
buffer_load_dwordx4 v7, s[4:7], s44 offen offset:0 sc1 lds
v_mfma_f32_16x16x32_bf16 v[148:151], a[28:31], v[68:71], v[148:151]
v_exp_f32 v130, v130
v_mfma_f32_16x16x32_bf16 v[156:159], a[20:23], v[72:75], v[196:199]
s_mov_b32 s49, s46
v_exp_f32 v131, v131
v_mfma_f32_16x16x32_bf16 v[156:159], a[28:31], v[76:79], v[156:159]
ds_read_b128 a[20:23], v62 offset:4096
ds_read_b128 a[28:31], v62 offset:5120
v_mfma_f32_16x16x32_bf16 v[144:147], a[32:35], v[96:99], v[144:147]
v_exp_f32 v132, v132
v_mfma_f32_16x16x32_bf16 v[144:147], a[40:43], v[100:103], v[144:147]
s_add_u32 s46, s46, 16384
v_exp_f32 v133, v133
v_mfma_f32_16x16x32_bf16 v[152:155], a[32:35], v[104:107], v[152:155]
buffer_load_dwordx4 v7, s[4:7], s44 offen offset:1024 sc1 lds
v_mfma_f32_16x16x32_bf16 v[152:155], a[40:43], v[108:111], v[152:155]
ds_read_b128 a[32:35], v62 offset:2048
ds_read_b128 a[40:43], v62 offset:3072
v_mfma_f32_16x16x32_bf16 v[148:151], a[36:39], v[96:99], v[148:151]
v_exp_f32 v134, v134
v_mfma_f32_16x16x32_bf16 v[148:151], a[44:47], v[100:103], v[148:151]
s_and_b32 s46, s46, 0xffff
v_cvt_pk_bf16_f32 v224, v128, v129
v_mfma_f32_16x16x32_bf16 v[156:159], a[36:39], v[104:107], v[156:159]
v_exp_f32 v135, v135
v_mfma_f32_16x16x32_bf16 v[156:159], a[44:47], v[108:111], v[156:159]
ds_read_b128 a[36:39], v62 offset:6144
ds_read_b128 a[44:47], v62 offset:7168
v_mfma_f32_16x16x32_bf16 v[208:211], v[52:55], v[232:235], v[208:211]
v_exp_f32 v136, v136
v_mfma_f32_16x16x32_bf16 v[212:215], v[52:55], v[236:239], v[212:215]
s_waitcnt lgkmcnt(8)
v_cvt_pk_bf16_f32 v225, v130, v131
v_cvt_pk_bf16_f32 v226, v132, v133
v_mfma_f32_16x16x32_bf16 a[0:3], a[48:51], v[232:235], a[0:3]
buffer_load_dwordx4 v7, s[4:7], s44 offen offset:2048 sc1 lds
v_mfma_f32_16x16x32_bf16 a[8:11], a[48:51], v[236:239], a[8:11]
v_exp_f32 v137, v137
v_mfma_f32_16x16x32_bf16 a[4:7], a[52:55], v[232:235], a[4:7]
v_exp_f32 v138, v138
v_mfma_f32_16x16x32_bf16 a[12:15], a[52:55], v[236:239], a[12:15]
ds_read_b128 a[48:51], v63 offset:0
ds_read_b128 a[52:55], v63 offset:1024
v_mfma_f32_16x16x32_bf16 a[80:83], a[56:59], v[232:235], a[80:83]
v_exp_f32 v139, v139
v_mfma_f32_16x16x32_bf16 a[88:91], a[56:59], v[236:239], a[88:91]
ds_read_b128 a[56:59], v63 offset:2048
v_cvt_pk_bf16_f32 v227, v134, v135
v_mfma_f32_16x16x32_bf16 a[84:87], a[60:63], v[232:235], a[84:87]
buffer_load_dwordx4 v7, s[4:7], s44 offen offset:3072 sc1 lds
v_mfma_f32_16x16x32_bf16 a[92:95], a[60:63], v[236:239], a[92:95]
ds_read_b128 a[60:63], v63 offset:3072
v_cvt_pk_bf16_f32 v228, v136, v137
v_mfma_f32_16x16x32_bf16 a[112:115], a[64:67], v[232:235], a[112:115]
v_exp_f32 v140, v140
v_mfma_f32_16x16x32_bf16 a[120:123], a[64:67], v[236:239], a[120:123]
v_exp_f32 v141, v141
v_mfma_f32_16x16x32_bf16 a[116:119], a[68:71], v[232:235], a[116:119]
s_sub_u32 s55, s55, 1
v_exp_f32 v142, v142
v_mfma_f32_16x16x32_bf16 a[124:127], a[68:71], v[236:239], a[124:127]
ds_read_b128 a[64:67], v63 offset:4096
ds_read_b128 a[68:71], v63 offset:5120
v_mfma_f32_16x16x32_bf16 a[144:147], a[72:75], v[232:235], a[144:147]
v_exp_f32 v143, v143
v_mfma_f32_16x16x32_bf16 a[152:155], a[72:75], v[236:239], a[152:155]
ds_read_b128 a[72:75], v63 offset:6144
v_cvt_pk_bf16_f32 v229, v138, v139
v_mfma_f32_16x16x32_bf16 a[148:151], a[76:79], v[232:235], a[148:151]
s_waitcnt  lgkmcnt(7)
v_cvt_pk_bf16_f32 v230, v140, v141
v_cvt_pk_bf16_f32 v231, v142, v143
v_mfma_f32_16x16x32_bf16 a[156:159], a[76:79], v[236:239], a[156:159]
ds_read_b128 a[76:79], v63 offset:7168
s_cbranch_scc1 .Lev_s1
.LS0:
v_mfma_f32_16x16x32_bf16 v[128:131], a[16:19], v[64:67], v[192:195]
s_cmp_lt_u32 s44, s45
v_exp_f32 v144, v144
v_mfma_f32_16x16x32_bf16 v[128:131], a[24:27], v[68:71], v[128:131]
s_cselect_b32 m0, s46, s27
v_exp_f32 v145, v145
v_mfma_f32_16x16x32_bf16 v[136:139], a[16:19], v[72:75], v[196:199]
v_cvt_pk_bf16_f32 v232, v144, v145
v_mfma_f32_16x16x32_bf16 v[136:139], a[24:27], v[76:79], v[136:139]
ds_read_b128 a[16:19], v62 offset:8192
ds_read_b128 a[24:27], v62 offset:9216
v_mfma_f32_16x16x32_bf16 v[132:135], a[20:23], v[64:67], v[192:195]
buffer_load_dwordx4 v7, s[8:11], s44 offen offset:0 sc1 lds
v_mfma_f32_16x16x32_bf16 v[132:135], a[28:31], v[68:71], v[132:135]
v_exp_f32 v146, v146
v_mfma_f32_16x16x32_bf16 v[140:143], a[20:23], v[72:75], v[196:199]
s_xor_b32 s50, s46, 0x18000
v_exp_f32 v147, v147
v_mfma_f32_16x16x32_bf16 v[140:143], a[28:31], v[76:79], v[140:143]
ds_read_b128 a[20:23], v62 offset:12288
ds_read_b128 a[28:31], v62 offset:13312
v_mfma_f32_16x16x32_bf16 v[128:131], a[32:35], v[96:99], v[128:131]
v_exp_f32 v148, v148
v_mfma_f32_16x16x32_bf16 v[128:131], a[40:43], v[100:103], v[128:131]
v_exp_f32 v149, v149
v_mfma_f32_16x16x32_bf16 v[136:139], a[32:35], v[104:107], v[136:139]
buffer_load_dwordx4 v7, s[8:11], s44 offen offset:1024 sc1 lds
v_mfma_f32_16x16x32_bf16 v[136:139], a[40:43], v[108:111], v[136:139]
ds_read_b128 a[32:35], v62 offset:10240
ds_read_b128 a[40:43], v62 offset:11264
v_mfma_f32_16x16x32_bf16 v[132:135], a[36:39], v[96:99], v[132:135]
v_exp_f32 v150, v150
v_mfma_f32_16x16x32_bf16 v[132:135], a[44:47], v[100:103], v[132:135]
v_cvt_pk_bf16_f32 v233, v146, v147
v_cvt_pk_bf16_f32 v234, v148, v149
v_mfma_f32_16x16x32_bf16 v[140:143], a[36:39], v[104:107], v[140:143]
v_exp_f32 v151, v151
v_mfma_f32_16x16x32_bf16 v[140:143], a[44:47], v[108:111], v[140:143]
ds_read_b128 a[36:39], v62 offset:14336
ds_read_b128 a[44:47], v62 offset:15360
v_mfma_f32_16x16x32_bf16 v[208:211], v[52:55], v[224:227], v[208:211]
v_add_u32 v62, s46, v32
v_cvt_pk_bf16_f32 v235, v150, v151
v_mfma_f32_16x16x32_bf16 v[212:215], v[52:55], v[228:231], v[212:215]
s_waitcnt lgkmcnt(8)
v_exp_f32 v152, v152
v_mfma_f32_16x16x32_bf16 a[0:3], a[48:51], v[224:227], a[0:3]
buffer_load_dwordx4 v7, s[8:11], s44 offen offset:2048 sc1 lds
v_mfma_f32_16x16x32_bf16 a[8:11], a[48:51], v[228:231], a[8:11]
v_exp_f32 v153, v153
v_mfma_f32_16x16x32_bf16 a[4:7], a[52:55], v[224:227], a[4:7]
v_exp_f32 v154, v154
v_mfma_f32_16x16x32_bf16 a[12:15], a[52:55], v[228:231], a[12:15]
ds_read_b128 a[48:51], v63 offset:8192
ds_read_b128 a[52:55], v63 offset:9216
v_mfma_f32_16x16x32_bf16 a[80:83], a[56:59], v[224:227], a[80:83]
v_exp_f32 v155, v155
v_mfma_f32_16x16x32_bf16 a[88:91], a[56:59], v[228:231], a[88:91]
ds_read_b128 a[56:59], v63 offset:10240
v_cvt_pk_bf16_f32 v236, v152, v153
v_mfma_f32_16x16x32_bf16 a[84:87], a[60:63], v[224:227], a[84:87]
buffer_load_dwordx4 v7, s[8:11], s44 offen offset:3072 sc1 lds
v_mfma_f32_16x16x32_bf16 a[92:95], a[60:63], v[228:231], a[92:95]
ds_read_b128 a[60:63], v63 offset:11264
v_cvt_pk_bf16_f32 v237, v154, v155
v_mfma_f32_16x16x32_bf16 a[112:115], a[64:67], v[224:227], a[112:115]
s_add_u32 s44, s44, 16384
v_exp_f32 v156, v156
v_mfma_f32_16x16x32_bf16 a[120:123], a[64:67], v[228:231], a[120:123]
v_exp_f32 v157, v157
v_mfma_f32_16x16x32_bf16 a[116:119], a[68:71], v[224:227], a[116:119]
s_sub_u32 s55, s55, 1
v_exp_f32 v158, v158
v_mfma_f32_16x16x32_bf16 a[124:127], a[68:71], v[228:231], a[124:127]
ds_read_b128 a[64:67], v63 offset:12288
ds_read_b128 a[68:71], v63 offset:13312
v_mfma_f32_16x16x32_bf16 a[144:147], a[72:75], v[224:227], a[144:147]
v_exp_f32 v159, v159
v_mfma_f32_16x16x32_bf16 a[152:155], a[72:75], v[228:231], a[152:155]
s_waitcnt vmcnt(8) lgkmcnt(6)
ds_read_b128 a[72:75], v63 offset:14336
v_mfma_f32_16x16x32_bf16 a[148:151], a[76:79], v[224:227], a[148:151]
s_barrier
v_cvt_pk_bf16_f32 v238, v156, v157
v_cvt_pk_bf16_f32 v239, v158, v159
v_mfma_f32_16x16x32_bf16 a[156:159], a[76:79], v[228:231], a[156:159]
ds_read_b128 a[76:79], v63 offset:15360
s_cbranch_scc0 .LS1
s_cmp_eq_u32 s43, s42
s_cbranch_scc1 .LE1
s_swappc_b64 s[30:31], s[58:59]
.Lev_s0_r:
s_branch .LS1
.LE0:
v_mfma_f32_16x16x32_bf16 v[128:131], a[16:19], v[64:67], v[16:19]
s_cmp_lt_u32 s44, s45
v_exp_f32 v144, v144
v_mfma_f32_16x16x32_bf16 v[128:131], a[24:27], v[68:71], v[128:131]
s_cselect_b32 m0, s46, s27
v_exp_f32 v145, v145
v_mfma_f32_16x16x32_bf16 v[136:139], a[16:19], v[72:75], v[24:27]
buffer_load_dwordx4 v7, s[8:11], s44 offen offset:0 sc1 lds
v_mfma_f32_16x16x32_bf16 v[136:139], a[24:27], v[76:79], v[136:139]
buffer_load_dwordx4 v7, s[8:11], s44 offen offset:1024 sc1 lds
v_mfma_f32_16x16x32_bf16 v[132:135], a[20:23], v[64:67], v[20:23]
buffer_load_dwordx4 v7, s[8:11], s44 offen offset:2048 sc1 lds
v_exp_f32 v146, v146
v_mfma_f32_16x16x32_bf16 v[132:135], a[28:31], v[68:71], v[132:135]
buffer_load_dwordx4 v7, s[8:11], s44 offen offset:3072 sc1 lds
v_exp_f32 v147, v147
v_mfma_f32_16x16x32_bf16 v[140:143], a[20:23], v[72:75], v[28:31]
s_xor_b32 s50, s46, 0x18000
buffer_load_dword v35, v4, s[96:99], 0 offen
v_mfma_f32_16x16x32_bf16 v[140:143], a[28:31], v[76:79], v[140:143]
s_add_u32 m0, s48, 131072
v_exp_f32 v148, v148
v_mfma_f32_16x16x32_bf16 v[128:131], a[32:35], v[96:99], v[128:131]
buffer_load_dwordx4 v40, s[16:19], s26 offen offset:0 lds
v_mfma_f32_16x16x32_bf16 v[128:131], a[40:43], v[100:103], v[128:131]
v_exp_f32 v149, v149
v_mfma_f32_16x16x32_bf16 v[136:139], a[32:35], v[104:107], v[136:139]
buffer_load_dwordx4 v41, s[16:19], s26 offen offset:1024 lds
v_mfma_f32_16x16x32_bf16 v[136:139], a[40:43], v[108:111], v[136:139]
v_exp_f32 v150, v150
v_mfma_f32_16x16x32_bf16 v[132:135], a[36:39], v[96:99], v[132:135]
buffer_load_dwordx4 v40, s[16:19], s26 offen offset:2048 lds
v_mfma_f32_16x16x32_bf16 v[132:135], a[44:47], v[100:103], v[132:135]
v_exp_f32 v151, v151
v_mfma_f32_16x16x32_bf16 v[140:143], a[36:39], v[104:107], v[140:143]
buffer_load_dwordx4 v41, s[16:19], s26 offen offset:3072 lds
v_mfma_f32_16x16x32_bf16 v[140:143], a[44:47], v[108:111], v[140:143]
s_add_u32 m0, s48, 135168
v_exp_f32 v152, v152
v_mfma_f32_16x16x32_bf16 v[208:211], v[52:55], v[224:227], v[208:211]
buffer_load_dwordx4 v40, s[16:19], s47 offen offset:0 lds
v_mfma_f32_16x16x32_bf16 v[212:215], v[52:55], v[228:231], v[212:215]
s_waitcnt lgkmcnt(0)
v_exp_f32 v153, v153
v_mfma_f32_16x16x32_bf16 a[0:3], a[48:51], v[224:227], a[0:3]
buffer_load_dwordx4 v41, s[16:19], s47 offen offset:1024 lds
v_mfma_f32_16x16x32_bf16 a[8:11], a[48:51], v[228:231], a[8:11]
v_exp_f32 v154, v154
v_mfma_f32_16x16x32_bf16 a[4:7], a[52:55], v[224:227], a[4:7]
buffer_load_dwordx4 v42, s[12:15], s25 offen offset:2048 lds
v_mfma_f32_16x16x32_bf16 a[12:15], a[52:55], v[228:231], a[12:15]
ds_read_b128 a[48:51], v63 offset:8192
ds_read_b128 a[52:55], v63 offset:9216
v_cvt_pk_bf16_f32 v232, v144, v145
v_mfma_f32_16x16x32_bf16 a[80:83], a[56:59], v[224:227], a[80:83]
s_add_u32 m0, s91, 65536
v_cvt_pk_bf16_f32 v233, v146, v147
v_exp_f32 v155, v155
v_mfma_f32_16x16x32_bf16 a[88:91], a[56:59], v[228:231], a[88:91]
buffer_load_dwordx4 v36, s[12:15], s24 offen offset:0 lds
ds_read_b128 a[56:59], v63 offset:10240
v_mfma_f32_16x16x32_bf16 a[84:87], a[60:63], v[224:227], a[84:87]
buffer_load_dwordx4 v37, s[12:15], s24 offen offset:1024 lds
v_cvt_pk_bf16_f32 v234, v148, v149
v_mfma_f32_16x16x32_bf16 a[92:95], a[60:63], v[228:231], a[92:95]
ds_read_b128 a[60:63], v63 offset:11264
v_exp_f32 v156, v156
v_mfma_f32_16x16x32_bf16 a[112:115], a[64:67], v[224:227], a[112:115]
s_add_u32 s44, s44, 16384
v_cvt_pk_bf16_f32 v235, v150, v151
v_exp_f32 v157, v157
v_mfma_f32_16x16x32_bf16 a[120:123], a[64:67], v[228:231], a[120:123]
buffer_load_dwordx4 v38, s[12:15], s24 offen offset:2048 lds
v_cvt_pk_bf16_f32 v236, v152, v153
v_mfma_f32_16x16x32_bf16 a[116:119], a[68:71], v[224:227], a[116:119]
buffer_load_dwordx4 v39, s[12:15], s24 offen offset:3072 lds
v_exp_f32 v158, v158
v_mfma_f32_16x16x32_bf16 a[124:127], a[68:71], v[228:231], a[124:127]
ds_read_b128 a[64:67], v63 offset:12288
ds_read_b128 a[68:71], v63 offset:13312
v_cvt_pk_bf16_f32 v237, v154, v155
v_mfma_f32_16x16x32_bf16 a[144:147], a[72:75], v[224:227], a[144:147]
s_add_u32 s26, s26, 393216
s_add_u32 s47, s47, 393216
s_add_u32 s25, s25, 128
s_add_u32 s24, s24, 128
v_mfma_f32_16x16x32_bf16 a[152:155], a[72:75], v[228:231], a[152:155]
s_waitcnt vmcnt(20)
ds_read_b128 a[72:75], v63 offset:14336
v_cvt_pk_bf16_f32 v238, v156, v157
v_mfma_f32_16x16x32_bf16 a[148:151], a[76:79], v[224:227], a[148:151]
s_barrier
v_exp_f32 v159, v159
s_nop 0
v_cvt_pk_bf16_f32 v239, v158, v159
v_mfma_f32_16x16x32_bf16 a[156:159], a[76:79], v[228:231], a[156:159]
ds_read_b128 a[76:79], v63 offset:15360
v_add_u32 v63, s49, v33
s_waitcnt lgkmcnt(4)
v_mfma_f32_16x16x32_bf16 v[208:211], v[52:55], v[232:235], v[208:211]
v_exp_f32 v128, v128
v_exp_f32 v129, v129
v_mfma_f32_16x16x32_bf16 v[212:215], v[52:55], v[236:239], v[212:215]
s_add_u32 m0, s91, 81920
v_exp_f32 v130, v130
v_mfma_f32_16x16x32_bf16 a[0:3], a[48:51], v[232:235], a[0:3]
buffer_load_dwordx4 v36, s[12:15], s24 offen offset:0 lds
v_mfma_f32_16x16x32_bf16 a[8:11], a[48:51], v[236:239], a[8:11]
v_exp_f32 v131, v131
v_mfma_f32_16x16x32_bf16 a[4:7], a[52:55], v[232:235], a[4:7]
buffer_load_dwordx4 v37, s[12:15], s24 offen offset:1024 lds
v_exp_f32 v132, v132
v_mfma_f32_16x16x32_bf16 a[12:15], a[52:55], v[236:239], a[12:15]
ds_read_b128 a[48:51], v63 offset:0
ds_read_b128 a[52:55], v63 offset:1024
v_exp_f32 v133, v133
v_mfma_f32_16x16x32_bf16 a[80:83], a[56:59], v[232:235], a[80:83]
v_cvt_pk_bf16_f32 v224, v128, v129
v_exp_f32 v134, v134
v_mfma_f32_16x16x32_bf16 a[88:91], a[56:59], v[236:239], a[88:91]
ds_read_b128 a[56:59], v63 offset:2048
.Lpad13:
s_cbranch_execz .Ltch3
v_exp_f32 v135, v135
v_mfma_f32_16x16x32_bf16 a[84:87], a[60:63], v[232:235], a[84:87]
buffer_load_dwordx4 v38, s[12:15], s24 offen offset:2048 lds
v_exp_f32 v136, v136
v_mfma_f32_16x16x32_bf16 a[92:95], a[60:63], v[236:239], a[92:95]
s_waitcnt lgkmcnt(3)
ds_read_b128 a[60:63], v63 offset:3072
v_exp_f32 v137, v137
v_mfma_f32_16x16x32_bf16 a[112:115], a[64:67], v[232:235], a[112:115]
v_cvt_pk_bf16_f32 v225, v130, v131
v_exp_f32 v138, v138
v_mfma_f32_16x16x32_bf16 a[120:123], a[64:67], v[236:239], a[120:123]
buffer_load_dwordx4 v39, s[12:15], s24 offen offset:3072 lds
v_cvt_pk_bf16_f32 v226, v132, v133
v_mfma_f32_16x16x32_bf16 a[116:119], a[68:71], v[232:235], a[116:119]
s_add_u32 s24, s24, 128
v_cvt_pk_bf16_f32 v227, v134, v135
v_exp_f32 v139, v139
v_mfma_f32_16x16x32_bf16 a[124:127], a[68:71], v[236:239], a[124:127]
ds_read_b128 a[64:67], v63 offset:4096
ds_read_b128 a[68:71], v63 offset:5120
v_exp_f32 v140, v140
v_mfma_f32_16x16x32_bf16 a[144:147], a[72:75], v[232:235], a[144:147]
v_cvt_pk_bf16_f32 v228, v136, v137
v_exp_f32 v141, v141
v_mfma_f32_16x16x32_bf16 a[152:155], a[72:75], v[236:239], a[152:155]
ds_read_b128 a[72:75], v63 offset:6144
v_exp_f32 v142, v142
v_mfma_f32_16x16x32_bf16 a[148:151], a[76:79], v[232:235], a[148:151]
v_cvt_pk_bf16_f32 v229, v138, v139
v_exp_f32 v143, v143
v_mfma_f32_16x16x32_bf16 a[156:159], a[76:79], v[236:239], a[156:159]
ds_read_b128 a[76:79], v63 offset:7168
v_cvt_pk_bf16_f32 v230, v140, v141
v_cvt_pk_bf16_f32 v231, v142, v143
s_waitcnt lgkmcnt(4)
v_mfma_f32_16x16x32_bf16 v[208:211], v[52:55], v[224:227], v[208:211]
s_mov_b32 s28, 1602224128
v_mfma_f32_16x16x32_bf16 v[212:215], v[52:55], v[228:231], v[212:215]
v_mfma_f32_16x16x32_bf16 a[0:3], a[48:51], v[224:227], a[0:3]
v_mfma_f32_16x16x32_bf16 a[8:11], a[48:51], v[228:231], a[8:11]
v_mfma_f32_16x16x32_bf16 a[4:7], a[52:55], v[224:227], a[4:7]
v_mfma_f32_16x16x32_bf16 a[12:15], a[52:55], v[228:231], a[12:15]
v_mfma_f32_16x16x32_bf16 a[80:83], a[56:59], v[224:227], a[80:83]
v_mfma_f32_16x16x32_bf16 a[88:91], a[56:59], v[228:231], a[88:91]
v_mfma_f32_16x16x32_bf16 a[84:87], a[60:63], v[224:227], a[84:87]
v_mfma_f32_16x16x32_bf16 a[92:95], a[60:63], v[228:231], a[92:95]
s_waitcnt lgkmcnt(0)
v_mfma_f32_16x16x32_bf16 a[112:115], a[64:67], v[224:227], a[112:115]
v_add_f32 v13, v208, v212
v_mfma_f32_16x16x32_bf16 a[120:123], a[64:67], v[228:231], a[120:123]
v_cmp_nge_f32 s[30:31], s28, v13
v_mfma_f32_16x16x32_bf16 a[116:119], a[68:71], v[224:227], a[116:119]
v_mfma_f32_16x16x32_bf16 a[124:127], a[68:71], v[228:231], a[124:127]
v_mfma_f32_16x16x32_bf16 a[144:147], a[72:75], v[224:227], a[144:147]
v_mfma_f32_16x16x32_bf16 a[152:155], a[72:75], v[228:231], a[152:155]
v_mfma_f32_16x16x32_bf16 a[148:151], a[76:79], v[224:227], a[148:151]
v_mfma_f32_16x16x32_bf16 a[156:159], a[76:79], v[228:231], a[156:159]
s_cmp_lt_u32 s3, 2
s_cbranch_scc0 .LEb0
s_waitcnt vmcnt(16)
s_barrier
.LEb0:
s_branch .LEend
.LE1_pad:
s_cbranch_execz .Ltch4
.LE1:
v_mfma_f32_16x16x32_bf16 v[144:147], a[16:19], v[64:67], v[16:19]
buffer_load_dword v35, v4, s[96:99], 0 offen
v_mfma_f32_16x16x32_bf16 v[144:147], a[24:27], v[68:71], v[144:147]
s_add_u32 m0, s48, 131072
v_exp_f32 v128, v128
v_mfma_f32_16x16x32_bf16 v[152:155], a[16:19], v[72:75], v[24:27]
buffer_load_dwordx4 v40, s[16:19], s26 offen offset:0 lds
v_add_u32 v63, s49, v33
v_mfma_f32_16x16x32_bf16 v[152:155], a[24:27], v[76:79], v[152:155]
v_exp_f32 v129, v129
v_mfma_f32_16x16x32_bf16 v[148:151], a[20:23], v[64:67], v[20:23]
buffer_load_dwordx4 v41, s[16:19], s26 offen offset:1024 lds
v_mfma_f32_16x16x32_bf16 v[148:151], a[28:31], v[68:71], v[148:151]
v_exp_f32 v130, v130
v_mfma_f32_16x16x32_bf16 v[156:159], a[20:23], v[72:75], v[28:31]
buffer_load_dwordx4 v40, s[16:19], s26 offen offset:2048 lds
v_mfma_f32_16x16x32_bf16 v[156:159], a[28:31], v[76:79], v[156:159]
v_exp_f32 v131, v131
v_mfma_f32_16x16x32_bf16 v[144:147], a[32:35], v[96:99], v[144:147]
buffer_load_dwordx4 v41, s[16:19], s26 offen offset:3072 lds
v_mfma_f32_16x16x32_bf16 v[144:147], a[40:43], v[100:103], v[144:147]
v_exp_f32 v132, v132
v_mfma_f32_16x16x32_bf16 v[152:155], a[32:35], v[104:107], v[152:155]
s_add_u32 m0, s48, 135168
v_exp_f32 v133, v133
v_mfma_f32_16x16x32_bf16 v[152:155], a[40:43], v[108:111], v[152:155]
buffer_load_dwordx4 v40, s[16:19], s47 offen offset:0 lds
v_mfma_f32_16x16x32_bf16 v[148:151], a[36:39], v[96:99], v[148:151]
v_exp_f32 v134, v134
v_mfma_f32_16x16x32_bf16 v[148:151], a[44:47], v[100:103], v[148:151]
buffer_load_dwordx4 v41, s[16:19], s47 offen offset:1024 lds
v_mfma_f32_16x16x32_bf16 v[156:159], a[36:39], v[104:107], v[156:159]
v_exp_f32 v135, v135
v_mfma_f32_16x16x32_bf16 v[156:159], a[44:47], v[108:111], v[156:159]
buffer_load_dwordx4 v42, s[12:15], s25 offen offset:2048 lds
v_mfma_f32_16x16x32_bf16 v[208:211], v[52:55], v[232:235], v[208:211]
v_exp_f32 v136, v136
v_mfma_f32_16x16x32_bf16 v[212:215], v[52:55], v[236:239], v[212:215]
s_waitcnt lgkmcnt(0)
s_add_u32 m0, s91, 65536
v_exp_f32 v137, v137
v_mfma_f32_16x16x32_bf16 a[0:3], a[48:51], v[232:235], a[0:3]
buffer_load_dwordx4 v36, s[12:15], s24 offen offset:0 lds
v_mfma_f32_16x16x32_bf16 a[8:11], a[48:51], v[236:239], a[8:11]
v_exp_f32 v138, v138
v_mfma_f32_16x16x32_bf16 a[4:7], a[52:55], v[232:235], a[4:7]
buffer_load_dwordx4 v37, s[12:15], s24 offen offset:1024 lds
v_mfma_f32_16x16x32_bf16 a[12:15], a[52:55], v[236:239], a[12:15]
ds_read_b128 a[48:51], v63 offset:0
ds_read_b128 a[52:55], v63 offset:1024
v_mfma_f32_16x16x32_bf16 a[80:83], a[56:59], v[232:235], a[80:83]
v_exp_f32 v139, v139
v_mfma_f32_16x16x32_bf16 a[88:91], a[56:59], v[236:239], a[88:91]
buffer_load_dwordx4 v38, s[12:15], s24 offen offset:2048 lds
ds_read_b128 a[56:59], v63 offset:2048
v_mfma_f32_16x16x32_bf16 a[84:87], a[60:63], v[232:235], a[84:87]
buffer_load_dwordx4 v39, s[12:15], s24 offen offset:3072 lds
v_mfma_f32_16x16x32_bf16 a[92:95], a[60:63], v[236:239], a[92:95]
ds_read_b128 a[60:63], v63 offset:3072
v_exp_f32 v140, v140
v_mfma_f32_16x16x32_bf16 a[112:115], a[64:67], v[232:235], a[112:115]
v_cvt_pk_bf16_f32 v224, v128, v129
v_cvt_pk_bf16_f32 v225, v130, v131
v_mfma_f32_16x16x32_bf16 a[120:123], a[64:67], v[236:239], a[120:123]
v_cvt_pk_bf16_f32 v226, v132, v133
v_exp_f32 v141, v141
v_mfma_f32_16x16x32_bf16 a[116:119], a[68:71], v[232:235], a[116:119]
v_cvt_pk_bf16_f32 v227, v134, v135
v_exp_f32 v142, v142
v_mfma_f32_16x16x32_bf16 a[124:127], a[68:71], v[236:239], a[124:127]
ds_read_b128 a[64:67], v63 offset:4096
ds_read_b128 a[68:71], v63 offset:5120
v_cvt_pk_bf16_f32 v228, v136, v137
v_mfma_f32_16x16x32_bf16 a[144:147], a[72:75], v[232:235], a[144:147]
s_add_u32 s26, s26, 393216
s_add_u32 s47, s47, 393216
s_add_u32 s25, s25, 128
s_add_u32 s24, s24, 128
v_mfma_f32_16x16x32_bf16 a[152:155], a[72:75], v[236:239], a[152:155]
ds_read_b128 a[72:75], v63 offset:6144
v_exp_f32 v143, v143
v_mfma_f32_16x16x32_bf16 a[148:151], a[76:79], v[232:235], a[148:151]
v_cvt_pk_bf16_f32 v229, v138, v139
v_cvt_pk_bf16_f32 v230, v140, v141
v_cvt_pk_bf16_f32 v231, v142, v143
v_mfma_f32_16x16x32_bf16 a[156:159], a[76:79], v[236:239], a[156:159]
ds_read_b128 a[76:79], v63 offset:7168
s_waitcnt lgkmcnt(4)
v_mfma_f32_16x16x32_bf16 v[208:211], v[52:55], v[224:227], v[208:211]
v_exp_f32 v144, v144
v_exp_f32 v145, v145
v_mfma_f32_16x16x32_bf16 v[212:215], v[52:55], v[228:231], v[212:215]
s_add_u32 m0, s91, 81920
v_exp_f32 v146, v146
v_mfma_f32_16x16x32_bf16 a[0:3], a[48:51], v[224:227], a[0:3]
buffer_load_dwordx4 v36, s[12:15], s24 offen offset:0 lds
v_mfma_f32_16x16x32_bf16 a[8:11], a[48:51], v[228:231], a[8:11]
v_cvt_pk_bf16_f32 v232, v144, v145
v_exp_f32 v147, v147
v_mfma_f32_16x16x32_bf16 a[4:7], a[52:55], v[224:227], a[4:7]
buffer_load_dwordx4 v37, s[12:15], s24 offen offset:1024 lds
v_exp_f32 v148, v148
v_mfma_f32_16x16x32_bf16 a[12:15], a[52:55], v[228:231], a[12:15]
ds_read_b128 a[48:51], v63 offset:8192
ds_read_b128 a[52:55], v63 offset:9216
v_exp_f32 v149, v149
v_mfma_f32_16x16x32_bf16 a[80:83], a[56:59], v[224:227], a[80:83]
v_cvt_pk_bf16_f32 v233, v146, v147
v_exp_f32 v150, v150
v_mfma_f32_16x16x32_bf16 a[88:91], a[56:59], v[228:231], a[88:91]
ds_read_b128 a[56:59], v63 offset:10240
v_exp_f32 v151, v151
v_mfma_f32_16x16x32_bf16 a[84:87], a[60:63], v[224:227], a[84:87]
buffer_load_dwordx4 v38, s[12:15], s24 offen offset:2048 lds
v_exp_f32 v152, v152
v_mfma_f32_16x16x32_bf16 a[92:95], a[60:63], v[228:231], a[92:95]
s_waitcnt lgkmcnt(3)
ds_read_b128 a[60:63], v63 offset:11264
v_exp_f32 v153, v153
v_mfma_f32_16x16x32_bf16 a[112:115], a[64:67], v[224:227], a[112:115]
v_cvt_pk_bf16_f32 v234, v148, v149
v_exp_f32 v154, v154
v_mfma_f32_16x16x32_bf16 a[120:123], a[64:67], v[228:231], a[120:123]
buffer_load_dwordx4 v39, s[12:15], s24 offen offset:3072 lds
v_cvt_pk_bf16_f32 v235, v150, v151
v_mfma_f32_16x16x32_bf16 a[116:119], a[68:71], v[224:227], a[116:119]
s_add_u32 s24, s24, 128
v_exp_f32 v155, v155
v_exp_f32 v156, v156
v_mfma_f32_16x16x32_bf16 a[124:127], a[68:71], v[228:231], a[124:127]
ds_read_b128 a[64:67], v63 offset:12288
ds_read_b128 a[68:71], v63 offset:13312
v_cvt_pk_bf16_f32 v236, v152, v153
v_mfma_f32_16x16x32_bf16 a[144:147], a[72:75], v[224:227], a[144:147]
v_cvt_pk_bf16_f32 v237, v154, v155
v_exp_f32 v157, v157
v_mfma_f32_16x16x32_bf16 a[152:155], a[72:75], v[228:231], a[152:155]
ds_read_b128 a[72:75], v63 offset:14336
v_exp_f32 v158, v158
v_mfma_f32_16x16x32_bf16 a[148:151], a[76:79], v[224:227], a[148:151]
v_cvt_pk_bf16_f32 v238, v156, v157
v_exp_f32 v159, v159
v_mfma_f32_16x16x32_bf16 a[156:159], a[76:79], v[228:231], a[156:159]
ds_read_b128 a[76:79], v63 offset:15360
v_add_u32 v63, s49, v33
v_cvt_pk_bf16_f32 v239, v158, v159
s_cmp_lt_u32 s3, 2
s_cbranch_scc0 .LEb1
s_waitcnt vmcnt(16)
s_barrier
.LEb1:
s_waitcnt lgkmcnt(4)
v_mfma_f32_16x16x32_bf16 v[208:211], v[52:55], v[232:235], v[208:211]
s_mov_b32 s28, 1602224128
v_mfma_f32_16x16x32_bf16 v[212:215], v[52:55], v[236:239], v[212:215]
v_mfma_f32_16x16x32_bf16 a[0:3], a[48:51], v[232:235], a[0:3]
v_mfma_f32_16x16x32_bf16 a[8:11], a[48:51], v[236:239], a[8:11]
v_mfma_f32_16x16x32_bf16 a[4:7], a[52:55], v[232:235], a[4:7]
v_mfma_f32_16x16x32_bf16 a[12:15], a[52:55], v[236:239], a[12:15]
v_mfma_f32_16x16x32_bf16 a[80:83], a[56:59], v[232:235], a[80:83]
v_mfma_f32_16x16x32_bf16 a[88:91], a[56:59], v[236:239], a[88:91]
v_mfma_f32_16x16x32_bf16 a[84:87], a[60:63], v[232:235], a[84:87]
v_mfma_f32_16x16x32_bf16 a[92:95], a[60:63], v[236:239], a[92:95]
s_waitcnt lgkmcnt(0)
v_mfma_f32_16x16x32_bf16 a[112:115], a[64:67], v[232:235], a[112:115]
v_add_f32 v13, v208, v212
v_mfma_f32_16x16x32_bf16 a[120:123], a[64:67], v[236:239], a[120:123]
v_cmp_nge_f32 s[30:31], s28, v13
v_mfma_f32_16x16x32_bf16 a[116:119], a[68:71], v[232:235], a[116:119]
v_mfma_f32_16x16x32_bf16 a[124:127], a[68:71], v[236:239], a[124:127]
v_mfma_f32_16x16x32_bf16 a[144:147], a[72:75], v[232:235], a[144:147]
v_mfma_f32_16x16x32_bf16 a[152:155], a[72:75], v[236:239], a[152:155]
v_mfma_f32_16x16x32_bf16 a[148:151], a[76:79], v[232:235], a[148:151]
v_mfma_f32_16x16x32_bf16 a[156:159], a[76:79], v[236:239], a[156:159]
.LEend:
s_or_b64 s[34:35], s[30:31], s[52:53]
s_cbranch_scc1 .Lx
.Lx_ret:
v_rcp_f32 v160, v208
v_rcp_f32 v162, v212
v_mov_b32 v161, v160
v_mov_b32 v163, v162
v_accvgpr_read_b32 v164, a0
v_accvgpr_read_b32 v165, a1
v_accvgpr_read_b32 v166, a2
v_accvgpr_read_b32 v167, a3
v_accvgpr_read_b32 v168, a4
v_accvgpr_read_b32 v169, a5
v_accvgpr_read_b32 v170, a6
v_accvgpr_read_b32 v171, a7
v_pk_mul_f32 v[164:165], v[164:165], v[160:161]
v_pk_mul_f32 v[166:167], v[166:167], v[160:161]
v_pk_mul_f32 v[168:169], v[168:169], v[160:161]
v_pk_mul_f32 v[170:171], v[170:171], v[160:161]
v_cvt_pk_bf16_f32 v172, v164, v165
v_cvt_pk_bf16_f32 v173, v166, v167
v_cvt_pk_bf16_f32 v174, v168, v169
v_cvt_pk_bf16_f32 v175, v170, v171
s_add_u32 m0, s48, 98304
s_nop 0
buffer_load_dwordx4 v40, s[16:19], s26 offen offset:0 lds
buffer_load_dwordx4 v41, s[16:19], s26 offen offset:1024 lds
buffer_store_dwordx4 v[172:175], v7, s[20:23], s40 offen offset:0 sc1
v_accvgpr_read_b32 v176, a8
v_accvgpr_read_b32 v177, a9
v_accvgpr_read_b32 v178, a10
v_accvgpr_read_b32 v179, a11
v_accvgpr_read_b32 v180, a12
v_accvgpr_read_b32 v181, a13
v_accvgpr_read_b32 v182, a14
v_accvgpr_read_b32 v183, a15
v_pk_mul_f32 v[176:177], v[176:177], v[162:163]
v_pk_mul_f32 v[178:179], v[178:179], v[162:163]
v_pk_mul_f32 v[180:181], v[180:181], v[162:163]
v_pk_mul_f32 v[182:183], v[182:183], v[162:163]
v_cvt_pk_bf16_f32 v184, v176, v177
v_cvt_pk_bf16_f32 v185, v178, v179
v_cvt_pk_bf16_f32 v186, v180, v181
v_cvt_pk_bf16_f32 v187, v182, v183
buffer_load_dwordx4 v40, s[16:19], s26 offen offset:2048 lds
buffer_load_dwordx4 v41, s[16:19], s26 offen offset:3072 lds
buffer_store_dwordx4 v[184:187], v7, s[20:23], s40 offen offset:1024 sc1
v_accvgpr_read_b32 v164, a80
v_accvgpr_read_b32 v165, a81
v_accvgpr_read_b32 v166, a82
v_accvgpr_read_b32 v167, a83
v_accvgpr_read_b32 v168, a84
v_accvgpr_read_b32 v169, a85
v_accvgpr_read_b32 v170, a86
v_accvgpr_read_b32 v171, a87
v_pk_mul_f32 v[164:165], v[164:165], v[160:161]
v_pk_mul_f32 v[166:167], v[166:167], v[160:161]
v_pk_mul_f32 v[168:169], v[168:169], v[160:161]
v_pk_mul_f32 v[170:171], v[170:171], v[160:161]
v_cvt_pk_bf16_f32 v172, v164, v165
v_cvt_pk_bf16_f32 v173, v166, v167
v_cvt_pk_bf16_f32 v174, v168, v169
v_cvt_pk_bf16_f32 v175, v170, v171
s_add_u32 m0, s48, 102400
s_nop 0
buffer_load_dwordx4 v40, s[16:19], s47 offen offset:0 lds
buffer_load_dwordx4 v41, s[16:19], s47 offen offset:1024 lds
buffer_store_dwordx4 v[172:175], v7, s[20:23], s40 offen offset:2048 sc1
v_accvgpr_read_b32 v176, a88
v_accvgpr_read_b32 v177, a89
v_accvgpr_read_b32 v178, a90
v_accvgpr_read_b32 v179, a91
v_accvgpr_read_b32 v180, a92
v_accvgpr_read_b32 v181, a93
v_accvgpr_read_b32 v182, a94
v_accvgpr_read_b32 v183, a95
v_pk_mul_f32 v[176:177], v[176:177], v[162:163]
v_pk_mul_f32 v[178:179], v[178:179], v[162:163]
v_pk_mul_f32 v[180:181], v[180:181], v[162:163]
v_pk_mul_f32 v[182:183], v[182:183], v[162:163]
v_cvt_pk_bf16_f32 v184, v176, v177
v_cvt_pk_bf16_f32 v185, v178, v179
v_cvt_pk_bf16_f32 v186, v180, v181
v_cvt_pk_bf16_f32 v187, v182, v183
buffer_load_dwordx4 v42, s[12:15], s25 offen offset:2048 lds
s_add_u32 m0, s91, 0
s_nop 0
buffer_load_dwordx4 v36, s[12:15], s24 offen offset:0 lds
buffer_store_dwordx4 v[184:187], v7, s[20:23], s40 offen offset:3072 sc1
v_accvgpr_read_b32 v164, a112
v_accvgpr_read_b32 v165, a113
v_accvgpr_read_b32 v166, a114
v_accvgpr_read_b32 v167, a115
v_accvgpr_read_b32 v168, a116
v_accvgpr_read_b32 v169, a117
v_accvgpr_read_b32 v170, a118
v_accvgpr_read_b32 v171, a119
v_pk_mul_f32 v[164:165], v[164:165], v[160:161]
v_pk_mul_f32 v[166:167], v[166:167], v[160:161]
v_pk_mul_f32 v[168:169], v[168:169], v[160:161]
v_pk_mul_f32 v[170:171], v[170:171], v[160:161]
v_cvt_pk_bf16_f32 v172, v164, v165
v_cvt_pk_bf16_f32 v173, v166, v167
v_cvt_pk_bf16_f32 v174, v168, v169
v_cvt_pk_bf16_f32 v175, v170, v171
buffer_load_dwordx4 v37, s[12:15], s24 offen offset:1024 lds
buffer_load_dwordx4 v38, s[12:15], s24 offen offset:2048 lds
buffer_store_dwordx4 v[172:175], v51, s[20:23], s40 offen offset:0 sc1
v_accvgpr_read_b32 v176, a120
v_accvgpr_read_b32 v177, a121
v_accvgpr_read_b32 v178, a122
v_accvgpr_read_b32 v179, a123
v_accvgpr_read_b32 v180, a124
v_accvgpr_read_b32 v181, a125
v_accvgpr_read_b32 v182, a126
v_accvgpr_read_b32 v183, a127
v_pk_mul_f32 v[176:177], v[176:177], v[162:163]
v_pk_mul_f32 v[178:179], v[178:179], v[162:163]
v_pk_mul_f32 v[180:181], v[180:181], v[162:163]
v_pk_mul_f32 v[182:183], v[182:183], v[162:163]
v_cvt_pk_bf16_f32 v184, v176, v177
v_cvt_pk_bf16_f32 v185, v178, v179
v_cvt_pk_bf16_f32 v186, v180, v181
v_cvt_pk_bf16_f32 v187, v182, v183
buffer_load_dwordx4 v39, s[12:15], s24 offen offset:3072 lds
s_add_u32 s26, s26, 393216
s_add_u32 s47, s47, 393216
s_add_u32 s25, s25, 128
s_add_u32 s24, s24, 128
buffer_store_dwordx4 v[184:187], v51, s[20:23], s40 offen offset:1024 sc1
v_accvgpr_read_b32 v164, a144
v_accvgpr_read_b32 v165, a145
v_accvgpr_read_b32 v166, a146
v_accvgpr_read_b32 v167, a147
v_accvgpr_read_b32 v168, a148
v_accvgpr_read_b32 v169, a149
v_accvgpr_read_b32 v170, a150
v_accvgpr_read_b32 v171, a151
v_pk_mul_f32 v[164:165], v[164:165], v[160:161]
v_pk_mul_f32 v[166:167], v[166:167], v[160:161]
v_pk_mul_f32 v[168:169], v[168:169], v[160:161]
v_pk_mul_f32 v[170:171], v[170:171], v[160:161]
v_cvt_pk_bf16_f32 v172, v164, v165
v_cvt_pk_bf16_f32 v173, v166, v167
v_cvt_pk_bf16_f32 v174, v168, v169
v_cvt_pk_bf16_f32 v175, v170, v171
buffer_store_dwordx4 v[172:175], v51, s[20:23], s40 offen offset:2048 sc1
v_accvgpr_read_b32 v176, a152
v_accvgpr_read_b32 v177, a153
v_accvgpr_read_b32 v178, a154
v_accvgpr_read_b32 v179, a155
v_accvgpr_read_b32 v180, a156
v_accvgpr_read_b32 v181, a157
v_accvgpr_read_b32 v182, a158
v_accvgpr_read_b32 v183, a159
v_pk_mul_f32 v[176:177], v[176:177], v[162:163]
v_pk_mul_f32 v[178:179], v[178:179], v[162:163]
v_pk_mul_f32 v[180:181], v[180:181], v[162:163]
v_pk_mul_f32 v[182:183], v[182:183], v[162:163]
v_cvt_pk_bf16_f32 v184, v176, v177
v_cvt_pk_bf16_f32 v185, v178, v179
v_cvt_pk_bf16_f32 v186, v180, v181
v_cvt_pk_bf16_f32 v187, v182, v183
buffer_store_dwordx4 v[184:187], v51, s[20:23], s40 offen offset:3072 sc1
s_cmp_eq_u32 s41, -1
s_cbranch_scc1 .Lfb_ret2
s_getpc_b64 s[30:31]
.Lfbp:
s_add_u32 s30, s30, .Lfb_ret2-.Lfbp
s_addc_u32 s31, s31, 0
.Lfb_pad:
s_cbranch_execz .Ltch3
.Lfb:
v_rcp_f32 v160, v216
v_rcp_f32 v162, v220
v_mov_b32 v161, v160
v_mov_b32 v163, v162
v_accvgpr_read_b32 v164, a176
v_accvgpr_read_b32 v165, a177
v_accvgpr_read_b32 v166, a178
v_accvgpr_read_b32 v167, a179
v_accvgpr_read_b32 v168, a180
v_accvgpr_read_b32 v169, a181
v_accvgpr_read_b32 v170, a182
v_accvgpr_read_b32 v171, a183
v_pk_mul_f32 v[164:165], v[164:165], v[160:161]
v_pk_mul_f32 v[166:167], v[166:167], v[160:161]
v_pk_mul_f32 v[168:169], v[168:169], v[160:161]
v_pk_mul_f32 v[170:171], v[170:171], v[160:161]
v_cvt_pk_bf16_f32 v172, v164, v165
v_cvt_pk_bf16_f32 v173, v166, v167
v_cvt_pk_bf16_f32 v174, v168, v169
v_cvt_pk_bf16_f32 v175, v170, v171
buffer_store_dwordx4 v[172:175], v7, s[20:23], s41 offen offset:0 sc1
v_accvgpr_read_b32 v176, a184
v_accvgpr_read_b32 v177, a185
v_accvgpr_read_b32 v178, a186
v_accvgpr_read_b32 v179, a187
v_accvgpr_read_b32 v180, a188
v_accvgpr_read_b32 v181, a189
v_accvgpr_read_b32 v182, a190
v_accvgpr_read_b32 v183, a191
v_pk_mul_f32 v[176:177], v[176:177], v[162:163]
v_pk_mul_f32 v[178:179], v[178:179], v[162:163]
v_pk_mul_f32 v[180:181], v[180:181], v[162:163]
v_pk_mul_f32 v[182:183], v[182:183], v[162:163]
v_cvt_pk_bf16_f32 v184, v176, v177
v_cvt_pk_bf16_f32 v185, v178, v179
v_cvt_pk_bf16_f32 v186, v180, v181
v_cvt_pk_bf16_f32 v187, v182, v183
buffer_store_dwordx4 v[184:187], v7, s[20:23], s41 offen offset:1024 sc1
v_accvgpr_read_b32 v164, a208
v_accvgpr_read_b32 v165, a209
v_accvgpr_read_b32 v166, a210
v_accvgpr_read_b32 v167, a211
v_accvgpr_read_b32 v168, a212
v_accvgpr_read_b32 v169, a213
v_accvgpr_read_b32 v170, a214
v_accvgpr_read_b32 v171, a215
v_pk_mul_f32 v[164:165], v[164:165], v[160:161]
v_pk_mul_f32 v[166:167], v[166:167], v[160:161]
v_pk_mul_f32 v[168:169], v[168:169], v[160:161]
v_pk_mul_f32 v[170:171], v[170:171], v[160:161]
v_cvt_pk_bf16_f32 v172, v164, v165
v_cvt_pk_bf16_f32 v173, v166, v167
v_cvt_pk_bf16_f32 v174, v168, v169
v_cvt_pk_bf16_f32 v175, v170, v171
buffer_store_dwordx4 v[172:175], v7, s[20:23], s41 offen offset:2048 sc1
v_accvgpr_read_b32 v176, a216
v_accvgpr_read_b32 v177, a217
v_accvgpr_read_b32 v178, a218
v_accvgpr_read_b32 v179, a219
v_accvgpr_read_b32 v180, a220
v_accvgpr_read_b32 v181, a221
v_accvgpr_read_b32 v182, a222
v_accvgpr_read_b32 v183, a223
v_pk_mul_f32 v[176:177], v[176:177], v[162:163]
v_pk_mul_f32 v[178:179], v[178:179], v[162:163]
v_pk_mul_f32 v[180:181], v[180:181], v[162:163]
v_pk_mul_f32 v[182:183], v[182:183], v[162:163]
v_cvt_pk_bf16_f32 v184, v176, v177
v_cvt_pk_bf16_f32 v185, v178, v179
v_cvt_pk_bf16_f32 v186, v180, v181
v_cvt_pk_bf16_f32 v187, v182, v183
buffer_store_dwordx4 v[184:187], v7, s[20:23], s41 offen offset:3072 sc1
v_accvgpr_read_b32 v164, a240
v_accvgpr_read_b32 v165, a241
v_accvgpr_read_b32 v166, a242
v_accvgpr_read_b32 v167, a243
v_accvgpr_read_b32 v168, a244
v_accvgpr_read_b32 v169, a245
v_accvgpr_read_b32 v170, a246
v_accvgpr_read_b32 v171, a247
v_pk_mul_f32 v[164:165], v[164:165], v[160:161]
v_pk_mul_f32 v[166:167], v[166:167], v[160:161]
v_pk_mul_f32 v[168:169], v[168:169], v[160:161]
v_pk_mul_f32 v[170:171], v[170:171], v[160:161]
v_cvt_pk_bf16_f32 v172, v164, v165
v_cvt_pk_bf16_f32 v173, v166, v167
v_cvt_pk_bf16_f32 v174, v168, v169
v_cvt_pk_bf16_f32 v175, v170, v171
buffer_store_dwordx4 v[172:175], v51, s[20:23], s41 offen offset:0 sc1
v_accvgpr_read_b32 v176, a248
v_accvgpr_read_b32 v177, a249
v_accvgpr_read_b32 v178, a250
v_accvgpr_read_b32 v179, a251
v_accvgpr_read_b32 v180, a252
v_accvgpr_read_b32 v181, a253
v_accvgpr_read_b32 v182, a254
v_accvgpr_read_b32 v183, a255
v_pk_mul_f32 v[176:177], v[176:177], v[162:163]
v_pk_mul_f32 v[178:179], v[178:179], v[162:163]
v_pk_mul_f32 v[180:181], v[180:181], v[162:163]
v_pk_mul_f32 v[182:183], v[182:183], v[162:163]
v_cvt_pk_bf16_f32 v184, v176, v177
v_cvt_pk_bf16_f32 v185, v178, v179
v_cvt_pk_bf16_f32 v186, v180, v181
v_cvt_pk_bf16_f32 v187, v182, v183
buffer_store_dwordx4 v[184:187], v51, s[20:23], s41 offen offset:1024 sc1
v_accvgpr_read_b32 v164, a96
v_accvgpr_read_b32 v165, a97
v_accvgpr_read_b32 v166, a98
v_accvgpr_read_b32 v167, a99
v_accvgpr_read_b32 v168, a100
v_accvgpr_read_b32 v169, a101
v_accvgpr_read_b32 v170, a102
v_accvgpr_read_b32 v171, a103
v_pk_mul_f32 v[164:165], v[164:165], v[160:161]
v_pk_mul_f32 v[166:167], v[166:167], v[160:161]
v_pk_mul_f32 v[168:169], v[168:169], v[160:161]
v_pk_mul_f32 v[170:171], v[170:171], v[160:161]
v_cvt_pk_bf16_f32 v172, v164, v165
v_cvt_pk_bf16_f32 v173, v166, v167
v_cvt_pk_bf16_f32 v174, v168, v169
v_cvt_pk_bf16_f32 v175, v170, v171
buffer_store_dwordx4 v[172:175], v51, s[20:23], s41 offen offset:2048 sc1
v_accvgpr_read_b32 v176, a104
v_accvgpr_read_b32 v177, a105
v_accvgpr_read_b32 v178, a106
v_accvgpr_read_b32 v179, a107
v_accvgpr_read_b32 v180, a108
v_accvgpr_read_b32 v181, a109
v_accvgpr_read_b32 v182, a110
v_accvgpr_read_b32 v183, a111
v_pk_mul_f32 v[176:177], v[176:177], v[162:163]
v_pk_mul_f32 v[178:179], v[178:179], v[162:163]
v_pk_mul_f32 v[180:181], v[180:181], v[162:163]
v_pk_mul_f32 v[182:183], v[182:183], v[162:163]
v_cvt_pk_bf16_f32 v184, v176, v177
v_cvt_pk_bf16_f32 v185, v178, v179
v_cvt_pk_bf16_f32 v186, v180, v181
v_cvt_pk_bf16_f32 v187, v182, v183
buffer_store_dwordx4 v[184:187], v51, s[20:23], s41 offen offset:3072 sc1
s_mov_b32 s41, -1
s_setpc_b64 s[30:31]
.Lfb_ret2:
s_cmp_eq_u32 s3, 0
s_cbranch_scc0 .LEend1
s_mov_b64 exec, 0
s_branch .Lpad14
.Ltch5:
s_branch .Lpad15
.Ltch6:
s_branch .Lpad16
.Ltch7:
s_branch .Lpad17
.Ltch8:
s_mov_b64 exec, -1
s_branch .Lfb_ret
.LEend1:
s_cmp_eq_u32 s3, 1
s_cbranch_scc0 .Lfb_ret
s_mov_b64 exec, 0
s_branch .Lpad18
.Ltch9:
s_branch .Lpad19
.Ltch10:
s_mov_b64 exec, -1
.Lfb_ret:
.Lpad14:
s_cbranch_execz .Ltch5
s_cmp_lt_u32 s3, 2
s_waitcnt vmcnt(23)
s_barrier
ds_read_b128 a[128:131], v45
ds_read_b128 a[0:3], v46
ds_read_b64_tr_b16 a[172:173], v47 offset:2048
ds_read_b64_tr_b16 a[174:175], v47 offset:2560
ds_read_b64_tr_b16 a[28:29], v47 offset:16384
ds_read_b64_tr_b16 a[30:31], v47 offset:16896
ds_read_b64_tr_b16 a[196:197], v47 offset:4096
ds_read_b64_tr_b16 a[198:199], v47 offset:4608
ds_read_b64_tr_b16 a[36:37], v47 offset:18432
ds_read_b64_tr_b16 a[38:39], v47 offset:18944
ds_read_b128 a[132:135], v43 offset:0
ds_read_b128 a[4:7], v44 offset:0
s_cselect_b32 s39, 1, 0
s_waitcnt lgkmcnt(8)
ds_read_b64_tr_b16 a[164:165], v47 offset:0
ds_read_b64_tr_b16 a[166:167], v47 offset:512
ds_read_b64_tr_b16 a[20:21], v47 offset:14336
ds_read_b64_tr_b16 a[22:23], v47 offset:14848
s_waitcnt lgkmcnt(0)
v_mfma_f32_16x16x32_bf16 v[160:163], a[172:175], a[128:131], 0
s_add_u32 m0, s48, 32768
v_mfma_f32_16x16x32_bf16 v[160:163], a[28:31], a[0:3], v[160:163]
buffer_load_dwordx4 v40, s[16:19], s26 offen offset:0 lds
v_mfma_f32_16x16x32_bf16 v[200:203], a[128:131], a[196:199], 0
ds_read_b128 a[136:139], v43 offset:2048
ds_read_b128 a[8:11], v44 offset:2048
v_mfma_f32_16x16x32_bf16 v[200:203], a[0:3], a[36:39], v[200:203]
ds_read_b128 a[140:143], v43 offset:4096
ds_read_b128 a[12:15], v44 offset:4096
v_mfma_f32_16x16x32_bf16 v[168:171], a[172:175], a[132:135], 0
buffer_load_dwordx4 v41, s[16:19], s26 offen offset:1024 lds
v_mfma_f32_16x16x32_bf16 v[168:171], a[28:31], a[4:7], v[168:171]
ds_read_b128 a[160:163], v43 offset:6144
ds_read_b128 a[16:19], v44 offset:6144
v_mfma_f32_16x16x32_bf16 v[208:211], a[132:135], a[196:199], 0
ds_read_b64_tr_b16 a[192:193], v48 offset:2048
ds_read_b64_tr_b16 a[194:195], v48 offset:2560
v_mfma_f32_16x16x32_bf16 v[208:211], a[4:7], a[36:39], v[208:211]
buffer_load_dwordx4 v40, s[16:19], s26 offen offset:2048 lds
v_mfma_f32_16x16x32_bf16 v[128:131], a[164:167], a[132:135], 0
ds_read_b64_tr_b16 a[32:33], v48 offset:16384
ds_read_b64_tr_b16 a[34:35], v48 offset:16896
v_mfma_f32_16x16x32_bf16 v[128:131], a[20:23], a[4:7], v[128:131]
s_waitcnt lgkmcnt(4)
v_mfma_f32_16x16x32_bf16 v[176:179], a[172:175], a[136:139], 0
buffer_load_dwordx4 v41, s[16:19], s26 offen offset:3072 lds
v_mfma_f32_16x16x32_bf16 v[176:179], a[28:31], a[8:11], v[176:179]
ds_read_b64_tr_b16 a[200:201], v48 offset:4096
ds_read_b64_tr_b16 a[202:203], v48 offset:4608
v_mfma_f32_16x16x32_bf16 v[216:219], a[136:139], a[196:199], 0
s_add_u32 m0, s48, 36864
v_mfma_f32_16x16x32_bf16 v[216:219], a[8:11], a[36:39], v[216:219]
buffer_load_dwordx4 v40, s[16:19], s47 offen offset:0 lds
v_mfma_f32_16x16x32_bf16 v[136:139], a[164:167], a[136:139], 0
ds_read_b64_tr_b16 a[40:41], v48 offset:18432
ds_read_b64_tr_b16 a[42:43], v48 offset:18944
v_mfma_f32_16x16x32_bf16 v[136:139], a[20:23], a[8:11], v[136:139]
ds_read_b64_tr_b16 a[168:169], v48 offset:0
ds_read_b64_tr_b16 a[170:171], v48 offset:512
v_mfma_f32_16x16x32_bf16 v[184:187], a[172:175], a[140:143], 0
buffer_load_dwordx4 v41, s[16:19], s47 offen offset:1024 lds
v_mfma_f32_16x16x32_bf16 v[184:187], a[28:31], a[12:15], v[184:187]
ds_read_b64_tr_b16 a[24:25], v48 offset:14336
ds_read_b64_tr_b16 a[26:27], v48 offset:14848
v_mfma_f32_16x16x32_bf16 v[224:227], a[140:143], a[196:199], 0
s_cmp_eq_u32 s39, 1
s_mov_b32 s28, 16384
s_cselect_b32 s28, 4294934528, s28
v_mfma_f32_16x16x32_bf16 v[224:227], a[12:15], a[36:39], v[224:227]
buffer_load_dwordx4 v42, s[12:15], s25 offen offset:2048 lds
v_mfma_f32_16x16x32_bf16 v[144:147], a[164:167], a[140:143], 0
s_add_u32 s26, s26, 393216
s_add_u32 s47, s47, 393216
s_add_u32 s25, s25, 128
v_mfma_f32_16x16x32_bf16 v[144:147], a[20:23], a[12:15], v[144:147]
s_add_u32 m0, s91, 16384
v_add_u32 v43, 16384, v43
v_add_u32 v44, 16384, v44
v_mfma_f32_16x16x32_bf16 v[192:195], a[172:175], a[160:163], 0
buffer_load_dwordx4 v36, s[12:15], s24 offen offset:0 lds
v_mfma_f32_16x16x32_bf16 v[192:195], a[28:31], a[16:19], v[192:195]
v_add_u32 v47, 4294934528, v47
v_add_u32 v48, 4294934528, v48
v_mfma_f32_16x16x32_bf16 v[232:235], a[160:163], a[196:199], 0
buffer_load_dwordx4 v37, s[12:15], s24 offen offset:1024 lds
v_mfma_f32_16x16x32_bf16 v[232:235], a[16:19], a[36:39], v[232:235]
v_add_u32 v45, s28, v45
v_add_u32 v46, s28, v46
v_mfma_f32_16x16x32_bf16 v[152:155], a[164:167], a[160:163], 0
buffer_load_dwordx4 v38, s[12:15], s24 offen offset:2048 lds
v_mfma_f32_16x16x32_bf16 v[152:155], a[20:23], a[16:19], v[152:155]
s_waitcnt vmcnt(19) lgkmcnt(0)
s_barrier
v_mfma_f32_16x16x32_bf16 v[164:167], a[192:195], a[128:131], 0
buffer_load_dwordx4 v39, s[12:15], s24 offen offset:3072 lds
v_mfma_f32_16x16x32_bf16 v[164:167], a[32:35], a[0:3], v[164:167]
s_add_u32 s24, s24, 128
v_mfma_f32_16x16x32_bf16 v[204:207], a[128:131], a[200:203], 0
s_add_u32 m0, s48, 131072
v_mfma_f32_16x16x32_bf16 v[204:207], a[0:3], a[40:43], v[204:207]
buffer_load_dwordx4 v40, s[16:19], s26 offen offset:0 lds
v_mfma_f32_16x16x32_bf16 v[172:175], a[192:195], a[132:135], 0
ds_read_b128 a[128:131], v45
v_mfma_f32_16x16x32_bf16 v[172:175], a[32:35], a[4:7], v[172:175]
ds_read_b128 a[0:3], v46
v_mfma_f32_16x16x32_bf16 v[212:215], a[132:135], a[200:203], 0
ds_read_b64_tr_b16 a[172:173], v47 offset:2048
ds_read_b64_tr_b16 a[174:175], v47 offset:2560
v_mfma_f32_16x16x32_bf16 v[212:215], a[4:7], a[40:43], v[212:215]
buffer_load_dwordx4 v41, s[16:19], s26 offen offset:1024 lds
v_mfma_f32_16x16x32_bf16 v[132:135], a[168:171], a[132:135], 0
ds_read_b64_tr_b16 a[28:29], v47 offset:16384
ds_read_b64_tr_b16 a[30:31], v47 offset:16896
v_mfma_f32_16x16x32_bf16 v[132:135], a[24:27], a[4:7], v[132:135]
ds_read_b128 a[132:135], v43 offset:0
v_mfma_f32_16x16x32_bf16 v[180:183], a[192:195], a[136:139], 0
ds_read_b128 a[4:7], v44 offset:0
v_mfma_f32_16x16x32_bf16 v[180:183], a[32:35], a[8:11], v[180:183]
ds_read_b64_tr_b16 a[196:197], v47 offset:4096
ds_read_b64_tr_b16 a[198:199], v47 offset:4608
v_mfma_f32_16x16x32_bf16 v[220:223], a[136:139], a[200:203], 0
buffer_load_dwordx4 v40, s[16:19], s26 offen offset:2048 lds
v_mfma_f32_16x16x32_bf16 v[220:223], a[8:11], a[40:43], v[220:223]
ds_read_b64_tr_b16 a[36:37], v47 offset:18432
ds_read_b64_tr_b16 a[38:39], v47 offset:18944
v_mfma_f32_16x16x32_bf16 v[140:143], a[168:171], a[136:139], 0
v_mfma_f32_16x16x32_bf16 v[140:143], a[24:27], a[8:11], v[140:143]
ds_read_b128 a[136:139], v43 offset:2048
v_mfma_f32_16x16x32_bf16 v[188:191], a[192:195], a[140:143], 0
ds_read_b128 a[8:11], v44 offset:2048
v_mfma_f32_16x16x32_bf16 v[188:191], a[32:35], a[12:15], v[188:191]
buffer_load_dwordx4 v41, s[16:19], s26 offen offset:3072 lds
v_mfma_f32_16x16x32_bf16 v[228:231], a[140:143], a[200:203], 0
ds_read_b64_tr_b16 a[164:165], v47 offset:0
ds_read_b64_tr_b16 a[166:167], v47 offset:512
v_mfma_f32_16x16x32_bf16 v[228:231], a[12:15], a[40:43], v[228:231]
v_mfma_f32_16x16x32_bf16 v[148:151], a[168:171], a[140:143], 0
ds_read_b64_tr_b16 a[20:21], v47 offset:14336
ds_read_b64_tr_b16 a[22:23], v47 offset:14848
v_mfma_f32_16x16x32_bf16 v[148:151], a[24:27], a[12:15], v[148:151]
s_add_u32 m0, s48, 135168
ds_read_b128 a[140:143], v43 offset:4096
v_mfma_f32_16x16x32_bf16 v[196:199], a[192:195], a[160:163], 0
buffer_load_dwordx4 v40, s[16:19], s47 offen offset:0 lds
v_mfma_f32_16x16x32_bf16 v[196:199], a[32:35], a[16:19], v[196:199]
ds_read_b128 a[12:15], v44 offset:4096
v_mfma_f32_16x16x32_bf16 v[236:239], a[160:163], a[200:203], 0
v_mfma_f32_16x16x32_bf16 v[236:239], a[16:19], a[40:43], v[236:239]
s_waitcnt lgkmcnt(2)
v_mfma_f32_16x16x32_bf16 v[156:159], a[168:171], a[160:163], 0
buffer_load_dwordx4 v41, s[16:19], s47 offen offset:1024 lds
v_mfma_f32_16x16x32_bf16 v[156:159], a[24:27], a[16:19], v[156:159]
ds_read_b128 a[160:163], v43 offset:6144
ds_read_b128 a[16:19], v44 offset:6144
v_mfma_f32_16x16x32_bf16 v[160:163], a[172:175], a[128:131], v[160:163]
ds_read_b64_tr_b16 a[192:193], v48 offset:2048
ds_read_b64_tr_b16 a[194:195], v48 offset:2560
v_mfma_f32_16x16x32_bf16 v[160:163], a[28:31], a[0:3], v[160:163]
v_mfma_f32_16x16x32_bf16 v[200:203], a[128:131], a[196:199], v[200:203]
ds_read_b64_tr_b16 a[32:33], v48 offset:16384
ds_read_b64_tr_b16 a[34:35], v48 offset:16896
v_mfma_f32_16x16x32_bf16 v[200:203], a[0:3], a[36:39], v[200:203]
buffer_load_dwordx4 v42, s[12:15], s25 offen offset:2048 lds
v_mfma_f32_16x16x32_bf16 v[168:171], a[172:175], a[132:135], v[168:171]
s_add_u32 s26, s26, 393216
s_add_u32 s47, s47, 393216
v_mfma_f32_16x16x32_bf16 v[168:171], a[28:31], a[4:7], v[168:171]
s_add_u32 s25, s25, 128
v_mfma_f32_16x16x32_bf16 v[208:211], a[132:135], a[196:199], v[208:211]
ds_read_b64_tr_b16 a[200:201], v48 offset:4096
ds_read_b64_tr_b16 a[202:203], v48 offset:4608
v_mfma_f32_16x16x32_bf16 v[208:211], a[4:7], a[36:39], v[208:211]
v_mfma_f32_16x16x32_bf16 v[128:131], a[164:167], a[132:135], v[128:131]
s_add_u32 m0, s91, 65536
v_mfma_f32_16x16x32_bf16 v[128:131], a[20:23], a[4:7], v[128:131]
buffer_load_dwordx4 v36, s[12:15], s24 offen offset:0 lds
v_mfma_f32_16x16x32_bf16 v[176:179], a[172:175], a[136:139], v[176:179]
ds_read_b64_tr_b16 a[40:41], v48 offset:18432
ds_read_b64_tr_b16 a[42:43], v48 offset:18944
v_mfma_f32_16x16x32_bf16 v[176:179], a[28:31], a[8:11], v[176:179]
v_mfma_f32_16x16x32_bf16 v[216:219], a[136:139], a[196:199], v[216:219]
ds_read_b64_tr_b16 a[168:169], v48 offset:0
ds_read_b64_tr_b16 a[170:171], v48 offset:512
v_mfma_f32_16x16x32_bf16 v[216:219], a[8:11], a[36:39], v[216:219]
s_waitcnt lgkmcnt(10)
v_mfma_f32_16x16x32_bf16 v[136:139], a[164:167], a[136:139], v[136:139]
buffer_load_dwordx4 v37, s[12:15], s24 offen offset:1024 lds
v_mfma_f32_16x16x32_bf16 v[136:139], a[20:23], a[8:11], v[136:139]
ds_read_b64_tr_b16 a[24:25], v48 offset:14336
ds_read_b64_tr_b16 a[26:27], v48 offset:14848
v_mfma_f32_16x16x32_bf16 v[184:187], a[172:175], a[140:143], v[184:187]
v_mfma_f32_16x16x32_bf16 v[184:187], a[28:31], a[12:15], v[184:187]
s_cmp_eq_u32 s39, 1
s_mov_b32 s28, 4294885376
s_cselect_b32 s28, 4294901760, s28
v_mfma_f32_16x16x32_bf16 v[224:227], a[140:143], a[196:199], v[224:227]
v_add_u32 v43, 4294885376, v43
v_add_u32 v44, 4294885376, v44
v_mfma_f32_16x16x32_bf16 v[224:227], a[12:15], a[36:39], v[224:227]
buffer_load_dwordx4 v38, s[12:15], s24 offen offset:2048 lds
v_mfma_f32_16x16x32_bf16 v[144:147], a[164:167], a[140:143], v[144:147]
v_add_u32 v47, 4294901760, v47
v_add_u32 v48, 4294901760, v48
v_mfma_f32_16x16x32_bf16 v[144:147], a[20:23], a[12:15], v[144:147]
v_add_u32 v45, s28, v45
v_add_u32 v46, s28, v46
v_mfma_f32_16x16x32_bf16 v[192:195], a[172:175], a[160:163], v[192:195]
v_mfma_f32_16x16x32_bf16 v[192:195], a[28:31], a[16:19], v[192:195]
v_mfma_f32_16x16x32_bf16 v[232:235], a[160:163], a[196:199], v[232:235]
buffer_load_dwordx4 v39, s[12:15], s24 offen offset:3072 lds
v_mfma_f32_16x16x32_bf16 v[232:235], a[16:19], a[36:39], v[232:235]
s_add_u32 s24, s24, 128
v_mfma_f32_16x16x32_bf16 v[152:155], a[164:167], a[160:163], v[152:155]
v_mfma_f32_16x16x32_bf16 v[152:155], a[20:23], a[16:19], v[152:155]
s_waitcnt vmcnt(15) lgkmcnt(0)
s_barrier
v_mfma_f32_16x16x32_bf16 v[164:167], a[192:195], a[128:131], v[164:167]
ds_read_b64_tr_b16 a[172:173], v47 offset:2048
ds_read_b64_tr_b16 a[174:175], v47 offset:2560
v_mfma_f32_16x16x32_bf16 v[164:167], a[32:35], a[0:3], v[164:167]
s_add_u32 m0, s48, 98304
v_mfma_f32_16x16x32_bf16 v[204:207], a[128:131], a[200:203], v[204:207]
buffer_load_dwordx4 v40, s[16:19], s26 offen offset:0 lds
v_mfma_f32_16x16x32_bf16 v[204:207], a[0:3], a[40:43], v[204:207]
ds_read_b128 a[128:131], v45
v_mfma_f32_16x16x32_bf16 v[172:175], a[192:195], a[132:135], v[172:175]
ds_read_b128 a[0:3], v46
v_mfma_f32_16x16x32_bf16 v[172:175], a[32:35], a[4:7], v[172:175]
ds_read_b64_tr_b16 a[28:29], v47 offset:16384
ds_read_b64_tr_b16 a[30:31], v47 offset:16896
v_mfma_f32_16x16x32_bf16 v[212:215], a[132:135], a[200:203], v[212:215]
v_mfma_f32_16x16x32_bf16 v[212:215], a[4:7], a[40:43], v[212:215]
buffer_load_dwordx4 v41, s[16:19], s26 offen offset:1024 lds
v_mfma_f32_16x16x32_bf16 v[132:135], a[168:171], a[132:135], v[132:135]
ds_read_b64_tr_b16 a[196:197], v47 offset:4096
ds_read_b64_tr_b16 a[198:199], v47 offset:4608
v_mfma_f32_16x16x32_bf16 v[132:135], a[24:27], a[4:7], v[132:135]
ds_read_b128 a[132:135], v43 offset:0
v_mfma_f32_16x16x32_bf16 v[180:183], a[192:195], a[136:139], v[180:183]
ds_read_b128 a[4:7], v44 offset:0
v_mfma_f32_16x16x32_bf16 v[180:183], a[32:35], a[8:11], v[180:183]
ds_read_b64_tr_b16 a[36:37], v47 offset:18432
ds_read_b64_tr_b16 a[38:39], v47 offset:18944
v_mfma_f32_16x16x32_bf16 v[220:223], a[136:139], a[200:203], v[220:223]
buffer_load_dwordx4 v40, s[16:19], s26 offen offset:2048 lds
v_mfma_f32_16x16x32_bf16 v[220:223], a[8:11], a[40:43], v[220:223]
ds_read_b64_tr_b16 a[164:165], v47 offset:0
ds_read_b64_tr_b16 a[166:167], v47 offset:512
v_mfma_f32_16x16x32_bf16 v[140:143], a[168:171], a[136:139], v[140:143]
v_mfma_f32_16x16x32_bf16 v[140:143], a[24:27], a[8:11], v[140:143]
ds_read_b128 a[136:139], v43 offset:2048
v_mfma_f32_16x16x32_bf16 v[188:191], a[192:195], a[140:143], v[188:191]
ds_read_b128 a[8:11], v44 offset:2048
v_mfma_f32_16x16x32_bf16 v[188:191], a[32:35], a[12:15], v[188:191]
buffer_load_dwordx4 v41, s[16:19], s26 offen offset:3072 lds
v_mfma_f32_16x16x32_bf16 v[228:231], a[140:143], a[200:203], v[228:231]
ds_read_b64_tr_b16 a[20:21], v47 offset:14336
ds_read_b64_tr_b16 a[22:23], v47 offset:14848
v_mfma_f32_16x16x32_bf16 v[228:231], a[12:15], a[40:43], v[228:231]
v_mfma_f32_16x16x32_bf16 v[148:151], a[168:171], a[140:143], v[148:151]
v_mfma_f32_16x16x32_bf16 v[148:151], a[24:27], a[12:15], v[148:151]
s_add_u32 m0, s48, 102400
ds_read_b128 a[140:143], v43 offset:4096
v_mfma_f32_16x16x32_bf16 v[196:199], a[192:195], a[160:163], v[196:199]
buffer_load_dwordx4 v40, s[16:19], s47 offen offset:0 lds
v_mfma_f32_16x16x32_bf16 v[196:199], a[32:35], a[16:19], v[196:199]
ds_read_b128 a[12:15], v44 offset:4096
v_mfma_f32_16x16x32_bf16 v[236:239], a[160:163], a[200:203], v[236:239]
v_mfma_f32_16x16x32_bf16 v[236:239], a[16:19], a[40:43], v[236:239]
s_waitcnt lgkmcnt(2)
v_mfma_f32_16x16x32_bf16 v[156:159], a[168:171], a[160:163], v[156:159]
buffer_load_dwordx4 v41, s[16:19], s47 offen offset:1024 lds
v_mfma_f32_16x16x32_bf16 v[156:159], a[24:27], a[16:19], v[156:159]
ds_read_b128 a[160:163], v43 offset:6144
ds_read_b128 a[16:19], v44 offset:6144
v_mfma_f32_16x16x32_bf16 v[160:163], a[172:175], a[128:131], v[160:163]
ds_read_b64_tr_b16 a[192:193], v48 offset:2048
ds_read_b64_tr_b16 a[194:195], v48 offset:2560
v_mfma_f32_16x16x32_bf16 v[160:163], a[28:31], a[0:3], v[160:163]
v_mfma_f32_16x16x32_bf16 v[200:203], a[128:131], a[196:199], v[200:203]
ds_read_b64_tr_b16 a[32:33], v48 offset:16384
ds_read_b64_tr_b16 a[34:35], v48 offset:16896
v_mfma_f32_16x16x32_bf16 v[200:203], a[0:3], a[36:39], v[200:203]
buffer_load_dwordx4 v42, s[12:15], s25 offen offset:2048 lds
v_mfma_f32_16x16x32_bf16 v[168:171], a[172:175], a[132:135], v[168:171]
s_add_u32 s26, s26, 393216
s_add_u32 s47, s47, 393216
v_mfma_f32_16x16x32_bf16 v[168:171], a[28:31], a[4:7], v[168:171]
s_add_u32 s25, s25, 128
v_mfma_f32_16x16x32_bf16 v[208:211], a[132:135], a[196:199], v[208:211]
ds_read_b64_tr_b16 a[200:201], v48 offset:4096
ds_read_b64_tr_b16 a[202:203], v48 offset:4608
v_mfma_f32_16x16x32_bf16 v[208:211], a[4:7], a[36:39], v[208:211]
v_mfma_f32_16x16x32_bf16 v[128:131], a[164:167], a[132:135], v[128:131]
s_add_u32 m0, s91, 81920
v_mfma_f32_16x16x32_bf16 v[128:131], a[20:23], a[4:7], v[128:131]
buffer_load_dwordx4 v36, s[12:15], s24 offen offset:0 lds
v_mfma_f32_16x16x32_bf16 v[176:179], a[172:175], a[136:139], v[176:179]
ds_read_b64_tr_b16 a[40:41], v48 offset:18432
ds_read_b64_tr_b16 a[42:43], v48 offset:18944
v_mfma_f32_16x16x32_bf16 v[176:179], a[28:31], a[8:11], v[176:179]
v_mfma_f32_16x16x32_bf16 v[216:219], a[136:139], a[196:199], v[216:219]
ds_read_b64_tr_b16 a[168:169], v48 offset:0
ds_read_b64_tr_b16 a[170:171], v48 offset:512
v_mfma_f32_16x16x32_bf16 v[216:219], a[8:11], a[36:39], v[216:219]
s_waitcnt lgkmcnt(10)
v_mfma_f32_16x16x32_bf16 v[136:139], a[164:167], a[136:139], v[136:139]
buffer_load_dwordx4 v37, s[12:15], s24 offen offset:1024 lds
v_mfma_f32_16x16x32_bf16 v[136:139], a[20:23], a[8:11], v[136:139]
ds_read_b64_tr_b16 a[24:25], v48 offset:14336
ds_read_b64_tr_b16 a[26:27], v48 offset:14848
v_mfma_f32_16x16x32_bf16 v[184:187], a[172:175], a[140:143], v[184:187]
v_mfma_f32_16x16x32_bf16 v[184:187], a[28:31], a[12:15], v[184:187]
s_cmp_eq_u32 s39, 1
s_mov_b32 s28, 16384
s_cselect_b32 s28, 98304, s28
v_mfma_f32_16x16x32_bf16 v[224:227], a[140:143], a[196:199], v[224:227]
v_add_u32 v43, 16384, v43
v_add_u32 v44, 16384, v44
v_mfma_f32_16x16x32_bf16 v[224:227], a[12:15], a[36:39], v[224:227]
buffer_load_dwordx4 v38, s[12:15], s24 offen offset:2048 lds
v_mfma_f32_16x16x32_bf16 v[144:147], a[164:167], a[140:143], v[144:147]
v_add_u32 v47, 98304, v47
v_add_u32 v48, 98304, v48
v_mfma_f32_16x16x32_bf16 v[144:147], a[20:23], a[12:15], v[144:147]
v_add_u32 v45, s28, v45
v_add_u32 v46, s28, v46
v_mfma_f32_16x16x32_bf16 v[192:195], a[172:175], a[160:163], v[192:195]
v_mfma_f32_16x16x32_bf16 v[192:195], a[28:31], a[16:19], v[192:195]
v_mfma_f32_16x16x32_bf16 v[232:235], a[160:163], a[196:199], v[232:235]
buffer_load_dwordx4 v39, s[12:15], s24 offen offset:3072 lds
v_mfma_f32_16x16x32_bf16 v[232:235], a[16:19], a[36:39], v[232:235]
s_add_u32 s24, s24, 128
v_mfma_f32_16x16x32_bf16 v[152:155], a[164:167], a[160:163], v[152:155]
v_or_b32 v0, 3, v11
v_mfma_f32_16x16x32_bf16 v[152:155], a[20:23], a[16:19], v[152:155]
s_waitcnt vmcnt(33)
s_waitcnt vmcnt(15) lgkmcnt(0)
s_barrier
v_mfma_f32_16x16x32_bf16 v[164:167], a[192:195], a[128:131], v[164:167]
buffer_store_dword v0, v6, s[60:63], 0 offen sc1
ds_read_b64_tr_b16 a[172:173], v47 offset:2048
ds_read_b64_tr_b16 a[174:175], v47 offset:2560
v_mfma_f32_16x16x32_bf16 v[164:167], a[32:35], a[0:3], v[164:167]
s_add_u32 m0, s48, 32768
v_mfma_f32_16x16x32_bf16 v[204:207], a[128:131], a[200:203], v[204:207]
buffer_load_dwordx4 v40, s[16:19], s26 offen offset:0 lds
v_mfma_f32_16x16x32_bf16 v[204:207], a[0:3], a[40:43], v[204:207]
ds_read_b128 a[128:131], v45
v_mfma_f32_16x16x32_bf16 v[172:175], a[192:195], a[132:135], v[172:175]
ds_read_b128 a[0:3], v46
v_mfma_f32_16x16x32_bf16 v[172:175], a[32:35], a[4:7], v[172:175]
ds_read_b64_tr_b16 a[28:29], v47 offset:16384
ds_read_b64_tr_b16 a[30:31], v47 offset:16896
v_mfma_f32_16x16x32_bf16 v[212:215], a[132:135], a[200:203], v[212:215]
v_mfma_f32_16x16x32_bf16 v[212:215], a[4:7], a[40:43], v[212:215]
buffer_load_dwordx4 v41, s[16:19], s26 offen offset:1024 lds
v_mfma_f32_16x16x32_bf16 v[132:135], a[168:171], a[132:135], v[132:135]
ds_read_b64_tr_b16 a[196:197], v47 offset:4096
ds_read_b64_tr_b16 a[198:199], v47 offset:4608
v_mfma_f32_16x16x32_bf16 v[132:135], a[24:27], a[4:7], v[132:135]
ds_read_b128 a[132:135], v43 offset:0
v_mfma_f32_16x16x32_bf16 v[180:183], a[192:195], a[136:139], v[180:183]
ds_read_b128 a[4:7], v44 offset:0
v_mfma_f32_16x16x32_bf16 v[180:183], a[32:35], a[8:11], v[180:183]
ds_read_b64_tr_b16 a[36:37], v47 offset:18432
ds_read_b64_tr_b16 a[38:39], v47 offset:18944
v_mfma_f32_16x16x32_bf16 v[220:223], a[136:139], a[200:203], v[220:223]
buffer_load_dwordx4 v40, s[16:19], s26 offen offset:2048 lds
v_mfma_f32_16x16x32_bf16 v[220:223], a[8:11], a[40:43], v[220:223]
ds_read_b64_tr_b16 a[164:165], v47 offset:0
ds_read_b64_tr_b16 a[166:167], v47 offset:512
v_mfma_f32_16x16x32_bf16 v[140:143], a[168:171], a[136:139], v[140:143]
v_mfma_f32_16x16x32_bf16 v[140:143], a[24:27], a[8:11], v[140:143]
ds_read_b128 a[136:139], v43 offset:2048
v_mfma_f32_16x16x32_bf16 v[188:191], a[192:195], a[140:143], v[188:191]
ds_read_b128 a[8:11], v44 offset:2048
v_mfma_f32_16x16x32_bf16 v[188:191], a[32:35], a[12:15], v[188:191]
buffer_load_dwordx4 v41, s[16:19], s26 offen offset:3072 lds
v_mfma_f32_16x16x32_bf16 v[228:231], a[140:143], a[200:203], v[228:231]
ds_read_b64_tr_b16 a[20:21], v47 offset:14336
ds_read_b64_tr_b16 a[22:23], v47 offset:14848
v_mfma_f32_16x16x32_bf16 v[228:231], a[12:15], a[40:43], v[228:231]
v_mfma_f32_16x16x32_bf16 v[148:151], a[168:171], a[140:143], v[148:151]
v_mfma_f32_16x16x32_bf16 v[148:151], a[24:27], a[12:15], v[148:151]
s_add_u32 m0, s48, 36864
ds_read_b128 a[140:143], v43 offset:4096
v_mfma_f32_16x16x32_bf16 v[196:199], a[192:195], a[160:163], v[196:199]
buffer_load_dwordx4 v40, s[16:19], s47 offen offset:0 lds
v_mfma_f32_16x16x32_bf16 v[196:199], a[32:35], a[16:19], v[196:199]
ds_read_b128 a[12:15], v44 offset:4096
v_mfma_f32_16x16x32_bf16 v[236:239], a[160:163], a[200:203], v[236:239]
v_mfma_f32_16x16x32_bf16 v[236:239], a[16:19], a[40:43], v[236:239]
s_waitcnt lgkmcnt(2)
v_mfma_f32_16x16x32_bf16 v[156:159], a[168:171], a[160:163], v[156:159]
buffer_load_dwordx4 v41, s[16:19], s47 offen offset:1024 lds
v_mfma_f32_16x16x32_bf16 v[156:159], a[24:27], a[16:19], v[156:159]
ds_read_b128 a[160:163], v43 offset:6144
ds_read_b128 a[16:19], v44 offset:6144
v_mfma_f32_16x16x32_bf16 v[160:163], a[172:175], a[128:131], v[160:163]
ds_read_b64_tr_b16 a[192:193], v48 offset:2048
ds_read_b64_tr_b16 a[194:195], v48 offset:2560
v_mfma_f32_16x16x32_bf16 v[160:163], a[28:31], a[0:3], v[160:163]
v_mfma_f32_16x16x32_bf16 v[200:203], a[128:131], a[196:199], v[200:203]
ds_read_b64_tr_b16 a[32:33], v48 offset:16384
ds_read_b64_tr_b16 a[34:35], v48 offset:16896
v_mfma_f32_16x16x32_bf16 v[200:203], a[0:3], a[36:39], v[200:203]
buffer_load_dwordx4 v42, s[12:15], s25 offen offset:2048 lds
v_mfma_f32_16x16x32_bf16 v[168:171], a[172:175], a[132:135], v[168:171]
s_add_u32 s26, s26, 393216
s_add_u32 s47, s47, 393216
v_mfma_f32_16x16x32_bf16 v[168:171], a[28:31], a[4:7], v[168:171]
s_add_u32 s25, s25, 128
v_mfma_f32_16x16x32_bf16 v[208:211], a[132:135], a[196:199], v[208:211]
ds_read_b64_tr_b16 a[200:201], v48 offset:4096
ds_read_b64_tr_b16 a[202:203], v48 offset:4608
v_mfma_f32_16x16x32_bf16 v[208:211], a[4:7], a[36:39], v[208:211]
v_mfma_f32_16x16x32_bf16 v[128:131], a[164:167], a[132:135], v[128:131]
s_add_u32 m0, s91, 0
v_mfma_f32_16x16x32_bf16 v[128:131], a[20:23], a[4:7], v[128:131]
buffer_load_dwordx4 v36, s[12:15], s24 offen offset:0 lds
v_mfma_f32_16x16x32_bf16 v[176:179], a[172:175], a[136:139], v[176:179]
ds_read_b64_tr_b16 a[40:41], v48 offset:18432
ds_read_b64_tr_b16 a[42:43], v48 offset:18944
v_mfma_f32_16x16x32_bf16 v[176:179], a[28:31], a[8:11], v[176:179]
v_mfma_f32_16x16x32_bf16 v[216:219], a[136:139], a[196:199], v[216:219]
ds_read_b64_tr_b16 a[168:169], v48 offset:0
ds_read_b64_tr_b16 a[170:171], v48 offset:512
v_mfma_f32_16x16x32_bf16 v[216:219], a[8:11], a[36:39], v[216:219]
s_waitcnt lgkmcnt(10)
v_mfma_f32_16x16x32_bf16 v[136:139], a[164:167], a[136:139], v[136:139]
buffer_load_dwordx4 v37, s[12:15], s24 offen offset:1024 lds
v_mfma_f32_16x16x32_bf16 v[136:139], a[20:23], a[8:11], v[136:139]
ds_read_b64_tr_b16 a[24:25], v48 offset:14336
ds_read_b64_tr_b16 a[26:27], v48 offset:14848
v_mfma_f32_16x16x32_bf16 v[184:187], a[172:175], a[140:143], v[184:187]
v_mfma_f32_16x16x32_bf16 v[184:187], a[28:31], a[12:15], v[184:187]
s_cmp_eq_u32 s39, 1
s_mov_b32 s28, 49152
s_cselect_b32 s28, 4294934528, s28
v_mfma_f32_16x16x32_bf16 v[224:227], a[140:143], a[196:199], v[224:227]
v_add_u32 v43, 49152, v43
v_add_u32 v44, 49152, v44
v_mfma_f32_16x16x32_bf16 v[224:227], a[12:15], a[36:39], v[224:227]
buffer_load_dwordx4 v38, s[12:15], s24 offen offset:2048 lds
v_mfma_f32_16x16x32_bf16 v[144:147], a[164:167], a[140:143], v[144:147]
v_add_u32 v47, 4294934528, v47
v_add_u32 v48, 4294934528, v48
v_mfma_f32_16x16x32_bf16 v[144:147], a[20:23], a[12:15], v[144:147]
v_add_u32 v45, s28, v45
v_add_u32 v46, s28, v46
v_mfma_f32_16x16x32_bf16 v[192:195], a[172:175], a[160:163], v[192:195]
v_mfma_f32_16x16x32_bf16 v[192:195], a[28:31], a[16:19], v[192:195]
v_mfma_f32_16x16x32_bf16 v[232:235], a[160:163], a[196:199], v[232:235]
buffer_load_dwordx4 v39, s[12:15], s24 offen offset:3072 lds
v_mfma_f32_16x16x32_bf16 v[232:235], a[16:19], a[36:39], v[232:235]
s_add_u32 s24, s24, 128
v_mfma_f32_16x16x32_bf16 v[152:155], a[164:167], a[160:163], v[152:155]
v_mfma_f32_16x16x32_bf16 v[152:155], a[20:23], a[16:19], v[152:155]
s_waitcnt vmcnt(16) lgkmcnt(0)
s_barrier
v_mfma_f32_16x16x32_bf16 v[164:167], a[192:195], a[128:131], v[164:167]
ds_read_b64_tr_b16 a[172:173], v47 offset:2048
ds_read_b64_tr_b16 a[174:175], v47 offset:2560
v_mfma_f32_16x16x32_bf16 v[164:167], a[32:35], a[0:3], v[164:167]
s_add_u32 m0, s48, 131072
v_mfma_f32_16x16x32_bf16 v[204:207], a[128:131], a[200:203], v[204:207]
buffer_load_dwordx4 v40, s[16:19], s26 offen offset:0 lds
v_mfma_f32_16x16x32_bf16 v[204:207], a[0:3], a[40:43], v[204:207]
ds_read_b128 a[128:131], v45
v_mfma_f32_16x16x32_bf16 v[172:175], a[192:195], a[132:135], v[172:175]
ds_read_b128 a[0:3], v46
v_mfma_f32_16x16x32_bf16 v[172:175], a[32:35], a[4:7], v[172:175]
ds_read_b64_tr_b16 a[28:29], v47 offset:16384
ds_read_b64_tr_b16 a[30:31], v47 offset:16896
v_mfma_f32_16x16x32_bf16 v[212:215], a[132:135], a[200:203], v[212:215]
v_mfma_f32_16x16x32_bf16 v[212:215], a[4:7], a[40:43], v[212:215]
buffer_load_dwordx4 v41, s[16:19], s26 offen offset:1024 lds
v_mfma_f32_16x16x32_bf16 v[132:135], a[168:171], a[132:135], v[132:135]
ds_read_b64_tr_b16 a[196:197], v47 offset:4096
ds_read_b64_tr_b16 a[198:199], v47 offset:4608
v_mfma_f32_16x16x32_bf16 v[132:135], a[24:27], a[4:7], v[132:135]
ds_read_b128 a[132:135], v43 offset:0
v_mfma_f32_16x16x32_bf16 v[180:183], a[192:195], a[136:139], v[180:183]
ds_read_b128 a[4:7], v44 offset:0
v_mfma_f32_16x16x32_bf16 v[180:183], a[32:35], a[8:11], v[180:183]
ds_read_b64_tr_b16 a[36:37], v47 offset:18432
ds_read_b64_tr_b16 a[38:39], v47 offset:18944
v_mfma_f32_16x16x32_bf16 v[220:223], a[136:139], a[200:203], v[220:223]
buffer_load_dwordx4 v40, s[16:19], s26 offen offset:2048 lds
v_mfma_f32_16x16x32_bf16 v[220:223], a[8:11], a[40:43], v[220:223]
ds_read_b64_tr_b16 a[164:165], v47 offset:0
ds_read_b64_tr_b16 a[166:167], v47 offset:512
v_mfma_f32_16x16x32_bf16 v[140:143], a[168:171], a[136:139], v[140:143]
v_mfma_f32_16x16x32_bf16 v[140:143], a[24:27], a[8:11], v[140:143]
ds_read_b128 a[136:139], v43 offset:2048
v_mfma_f32_16x16x32_bf16 v[188:191], a[192:195], a[140:143], v[188:191]
ds_read_b128 a[8:11], v44 offset:2048
v_mfma_f32_16x16x32_bf16 v[188:191], a[32:35], a[12:15], v[188:191]
buffer_load_dwordx4 v41, s[16:19], s26 offen offset:3072 lds
v_mfma_f32_16x16x32_bf16 v[228:231], a[140:143], a[200:203], v[228:231]
ds_read_b64_tr_b16 a[20:21], v47 offset:14336
ds_read_b64_tr_b16 a[22:23], v47 offset:14848
v_mfma_f32_16x16x32_bf16 v[228:231], a[12:15], a[40:43], v[228:231]
v_mfma_f32_16x16x32_bf16 v[148:151], a[168:171], a[140:143], v[148:151]
v_mfma_f32_16x16x32_bf16 v[148:151], a[24:27], a[12:15], v[148:151]
s_add_u32 m0, s48, 135168
ds_read_b128 a[140:143], v43 offset:4096
v_mfma_f32_16x16x32_bf16 v[196:199], a[192:195], a[160:163], v[196:199]
buffer_load_dwordx4 v40, s[16:19], s47 offen offset:0 lds
v_mfma_f32_16x16x32_bf16 v[196:199], a[32:35], a[16:19], v[196:199]
ds_read_b128 a[12:15], v44 offset:4096
v_mfma_f32_16x16x32_bf16 v[236:239], a[160:163], a[200:203], v[236:239]
v_mfma_f32_16x16x32_bf16 v[236:239], a[16:19], a[40:43], v[236:239]
s_waitcnt lgkmcnt(2)
v_mfma_f32_16x16x32_bf16 v[156:159], a[168:171], a[160:163], v[156:159]
buffer_load_dwordx4 v41, s[16:19], s47 offen offset:1024 lds
v_mfma_f32_16x16x32_bf16 v[156:159], a[24:27], a[16:19], v[156:159]
ds_read_b128 a[160:163], v43 offset:6144
ds_read_b128 a[16:19], v44 offset:6144
v_mfma_f32_16x16x32_bf16 v[160:163], a[172:175], a[128:131], v[160:163]
ds_read_b64_tr_b16 a[192:193], v48 offset:2048
ds_read_b64_tr_b16 a[194:195], v48 offset:2560
.Lpad15:
s_cbranch_execz .Ltch6
v_mfma_f32_16x16x32_bf16 v[160:163], a[28:31], a[0:3], v[160:163]
s_lshl_b32 s54, s86, 7
s_add_u32 s54, s54, 0x14000
v_mfma_f32_16x16x32_bf16 v[200:203], a[128:131], a[196:199], v[200:203]
ds_read_b64_tr_b16 a[32:33], v48 offset:16384
ds_read_b64_tr_b16 a[34:35], v48 offset:16896
v_mfma_f32_16x16x32_bf16 v[200:203], a[0:3], a[36:39], v[200:203]
buffer_load_dwordx4 v42, s[12:15], s25 offen offset:2048 lds
v_and_b32 v249, 7, v1
v_mfma_f32_16x16x32_bf16 v[168:171], a[172:175], a[132:135], v[168:171]
s_add_u32 s26, s26, 393216
s_add_u32 s47, s47, 393216
v_lshl_add_u32 v249, v249, 2, s54
v_mfma_f32_16x16x32_bf16 v[168:171], a[28:31], a[4:7], v[168:171]
s_add_u32 s25, s25, 128
v_mfma_f32_16x16x32_bf16 v[208:211], a[132:135], a[196:199], v[208:211]
ds_read_b64_tr_b16 a[200:201], v48 offset:4096
ds_read_b64_tr_b16 a[202:203], v48 offset:4608
v_mfma_f32_16x16x32_bf16 v[208:211], a[4:7], a[36:39], v[208:211]
v_mfma_f32_16x16x32_bf16 v[128:131], a[164:167], a[132:135], v[128:131]
s_add_u32 m0, s91, 16384
v_mfma_f32_16x16x32_bf16 v[128:131], a[20:23], a[4:7], v[128:131]
buffer_load_dwordx4 v36, s[12:15], s24 offen offset:0 lds
v_mfma_f32_16x16x32_bf16 v[176:179], a[172:175], a[136:139], v[176:179]
ds_read_b64_tr_b16 a[40:41], v48 offset:18432
ds_read_b64_tr_b16 a[42:43], v48 offset:18944
v_mfma_f32_16x16x32_bf16 v[176:179], a[28:31], a[8:11], v[176:179]
v_mfma_f32_16x16x32_bf16 v[216:219], a[136:139], a[196:199], v[216:219]
ds_read_b64_tr_b16 a[168:169], v48 offset:0
ds_read_b64_tr_b16 a[170:171], v48 offset:512
v_mfma_f32_16x16x32_bf16 v[216:219], a[8:11], a[36:39], v[216:219]
s_waitcnt lgkmcnt(10)
v_mfma_f32_16x16x32_bf16 v[136:139], a[164:167], a[136:139], v[136:139]
buffer_load_dwordx4 v37, s[12:15], s24 offen offset:1024 lds
v_mfma_f32_16x16x32_bf16 v[136:139], a[20:23], a[8:11], v[136:139]
ds_read_b64_tr_b16 a[24:25], v48 offset:14336
ds_read_b64_tr_b16 a[26:27], v48 offset:14848
v_mfma_f32_16x16x32_bf16 v[184:187], a[172:175], a[140:143], v[184:187]
v_mfma_f32_16x16x32_bf16 v[184:187], a[28:31], a[12:15], v[184:187]
s_cmp_eq_u32 s39, 1
s_mov_b32 s28, 16384
s_cselect_b32 s28, 4294901760, s28
v_mfma_f32_16x16x32_bf16 v[224:227], a[140:143], a[196:199], v[224:227]
v_add_u32 v43, 16384, v43
v_add_u32 v44, 16384, v44
v_mfma_f32_16x16x32_bf16 v[224:227], a[12:15], a[36:39], v[224:227]
buffer_load_dwordx4 v38, s[12:15], s24 offen offset:2048 lds
v_mfma_f32_16x16x32_bf16 v[144:147], a[164:167], a[140:143], v[144:147]
v_add_u32 v47, 4294901760, v47
v_add_u32 v48, 4294901760, v48
v_mfma_f32_16x16x32_bf16 v[144:147], a[20:23], a[12:15], v[144:147]
v_add_u32 v45, s28, v45
v_add_u32 v46, s28, v46
v_mfma_f32_16x16x32_bf16 v[192:195], a[172:175], a[160:163], v[192:195]
v_mfma_f32_16x16x32_bf16 v[192:195], a[28:31], a[16:19], v[192:195]
v_mfma_f32_16x16x32_bf16 v[232:235], a[160:163], a[196:199], v[232:235]
buffer_load_dwordx4 v39, s[12:15], s24 offen offset:3072 lds
v_mfma_f32_16x16x32_bf16 v[232:235], a[16:19], a[36:39], v[232:235]
s_add_u32 s24, s24, 128
v_mfma_f32_16x16x32_bf16 v[152:155], a[164:167], a[160:163], v[152:155]
v_mfma_f32_16x16x32_bf16 v[152:155], a[20:23], a[16:19], v[152:155]
s_waitcnt vmcnt(15) lgkmcnt(0)
s_barrier
v_mfma_f32_16x16x32_bf16 v[164:167], a[192:195], a[128:131], v[164:167]
ds_read_b64_tr_b16 a[172:173], v47 offset:2048
ds_read_b64_tr_b16 a[174:175], v47 offset:2560
v_mfma_f32_16x16x32_bf16 v[164:167], a[32:35], a[0:3], v[164:167]
s_add_u32 m0, s48, 98304
v_mfma_f32_16x16x32_bf16 v[204:207], a[128:131], a[200:203], v[204:207]
buffer_load_dwordx4 v40, s[16:19], s26 offen offset:0 lds
v_mfma_f32_16x16x32_bf16 v[204:207], a[0:3], a[40:43], v[204:207]
ds_read_b128 a[128:131], v45
v_mfma_f32_16x16x32_bf16 v[172:175], a[192:195], a[132:135], v[172:175]
ds_read_b128 a[0:3], v46
v_mfma_f32_16x16x32_bf16 v[172:175], a[32:35], a[4:7], v[172:175]
ds_read_b64_tr_b16 a[28:29], v47 offset:16384
ds_read_b64_tr_b16 a[30:31], v47 offset:16896
v_mfma_f32_16x16x32_bf16 v[212:215], a[132:135], a[200:203], v[212:215]
v_mfma_f32_16x16x32_bf16 v[212:215], a[4:7], a[40:43], v[212:215]
buffer_load_dwordx4 v41, s[16:19], s26 offen offset:1024 lds
v_mfma_f32_16x16x32_bf16 v[132:135], a[168:171], a[132:135], v[132:135]
ds_read_b64_tr_b16 a[196:197], v47 offset:4096
ds_read_b64_tr_b16 a[198:199], v47 offset:4608
v_mfma_f32_16x16x32_bf16 v[132:135], a[24:27], a[4:7], v[132:135]
ds_read_b128 a[132:135], v43 offset:0
v_mfma_f32_16x16x32_bf16 v[180:183], a[192:195], a[136:139], v[180:183]
ds_read_b128 a[4:7], v44 offset:0
v_mfma_f32_16x16x32_bf16 v[180:183], a[32:35], a[8:11], v[180:183]
ds_read_b64_tr_b16 a[36:37], v47 offset:18432
ds_read_b64_tr_b16 a[38:39], v47 offset:18944
v_mfma_f32_16x16x32_bf16 v[220:223], a[136:139], a[200:203], v[220:223]
buffer_load_dwordx4 v40, s[16:19], s26 offen offset:2048 lds
v_mfma_f32_16x16x32_bf16 v[220:223], a[8:11], a[40:43], v[220:223]
ds_read_b64_tr_b16 a[164:165], v47 offset:0
ds_read_b64_tr_b16 a[166:167], v47 offset:512
v_mfma_f32_16x16x32_bf16 v[140:143], a[168:171], a[136:139], v[140:143]
v_mfma_f32_16x16x32_bf16 v[140:143], a[24:27], a[8:11], v[140:143]
ds_read_b128 a[136:139], v43 offset:2048
v_mfma_f32_16x16x32_bf16 v[188:191], a[192:195], a[140:143], v[188:191]
ds_read_b128 a[8:11], v44 offset:2048
v_mfma_f32_16x16x32_bf16 v[188:191], a[32:35], a[12:15], v[188:191]
buffer_load_dwordx4 v41, s[16:19], s26 offen offset:3072 lds
v_mfma_f32_16x16x32_bf16 v[228:231], a[140:143], a[200:203], v[228:231]
ds_read_b64_tr_b16 a[20:21], v47 offset:14336
ds_read_b64_tr_b16 a[22:23], v47 offset:14848
v_mfma_f32_16x16x32_bf16 v[228:231], a[12:15], a[40:43], v[228:231]
v_mfma_f32_16x16x32_bf16 v[148:151], a[168:171], a[140:143], v[148:151]
v_mfma_f32_16x16x32_bf16 v[148:151], a[24:27], a[12:15], v[148:151]
s_add_u32 m0, s48, 102400
ds_read_b128 a[140:143], v43 offset:4096
v_mfma_f32_16x16x32_bf16 v[196:199], a[192:195], a[160:163], v[196:199]
buffer_load_dwordx4 v40, s[16:19], s47 offen offset:0 lds
v_mfma_f32_16x16x32_bf16 v[196:199], a[32:35], a[16:19], v[196:199]
ds_read_b128 a[12:15], v44 offset:4096
v_mfma_f32_16x16x32_bf16 v[236:239], a[160:163], a[200:203], v[236:239]
buffer_load_dword v35, v4, s[96:99], 0 offen
v_mfma_f32_16x16x32_bf16 v[236:239], a[16:19], a[40:43], v[236:239]
s_waitcnt lgkmcnt(2)
v_mfma_f32_16x16x32_bf16 v[156:159], a[168:171], a[160:163], v[156:159]
buffer_load_dwordx4 v41, s[16:19], s47 offen offset:1024 lds
v_mfma_f32_16x16x32_bf16 v[156:159], a[24:27], a[16:19], v[156:159]
ds_read_b128 a[160:163], v43 offset:6144
ds_read_b128 a[16:19], v44 offset:6144
v_mfma_f32_16x16x32_bf16 v[160:163], a[172:175], a[128:131], v[160:163]
ds_read_b64_tr_b16 a[192:193], v48 offset:2048
ds_read_b64_tr_b16 a[194:195], v48 offset:2560
v_mfma_f32_16x16x32_bf16 v[160:163], a[28:31], a[0:3], v[160:163]
v_mfma_f32_16x16x32_bf16 v[200:203], a[128:131], a[196:199], v[200:203]
ds_read_b64_tr_b16 a[32:33], v48 offset:16384
ds_read_b64_tr_b16 a[34:35], v48 offset:16896
v_mfma_f32_16x16x32_bf16 v[200:203], a[0:3], a[36:39], v[200:203]
buffer_load_dwordx4 v42, s[12:15], s25 offen offset:2048 lds
v_mfma_f32_16x16x32_bf16 v[168:171], a[172:175], a[132:135], v[168:171]
s_add_u32 s26, s26, 393216
s_add_u32 s47, s47, 393216
v_mfma_f32_16x16x32_bf16 v[168:171], a[28:31], a[4:7], v[168:171]
s_add_u32 s25, s25, 128
v_mfma_f32_16x16x32_bf16 v[208:211], a[132:135], a[196:199], v[208:211]
buffer_load_dword v250, v249, s[60:63], 0 offen sc1
ds_read_b64_tr_b16 a[200:201], v48 offset:4096
ds_read_b64_tr_b16 a[202:203], v48 offset:4608
v_mfma_f32_16x16x32_bf16 v[208:211], a[4:7], a[36:39], v[208:211]
v_mfma_f32_16x16x32_bf16 v[128:131], a[164:167], a[132:135], v[128:131]
s_add_u32 m0, s91, 65536
v_mfma_f32_16x16x32_bf16 v[128:131], a[20:23], a[4:7], v[128:131]
buffer_load_dwordx4 v36, s[12:15], s24 offen offset:0 lds
v_mfma_f32_16x16x32_bf16 v[176:179], a[172:175], a[136:139], v[176:179]
ds_read_b64_tr_b16 a[40:41], v48 offset:18432
ds_read_b64_tr_b16 a[42:43], v48 offset:18944
v_mfma_f32_16x16x32_bf16 v[176:179], a[28:31], a[8:11], v[176:179]
v_mfma_f32_16x16x32_bf16 v[216:219], a[136:139], a[196:199], v[216:219]
ds_read_b64_tr_b16 a[168:169], v48 offset:0
ds_read_b64_tr_b16 a[170:171], v48 offset:512
v_mfma_f32_16x16x32_bf16 v[216:219], a[8:11], a[36:39], v[216:219]
s_waitcnt lgkmcnt(10)
v_mfma_f32_16x16x32_bf16 v[136:139], a[164:167], a[136:139], v[136:139]
buffer_load_dwordx4 v37, s[12:15], s24 offen offset:1024 lds
v_mfma_f32_16x16x32_bf16 v[136:139], a[20:23], a[8:11], v[136:139]
ds_read_b64_tr_b16 a[24:25], v48 offset:14336
ds_read_b64_tr_b16 a[26:27], v48 offset:14848
v_mfma_f32_16x16x32_bf16 v[184:187], a[172:175], a[140:143], v[184:187]
v_mfma_f32_16x16x32_bf16 v[184:187], a[28:31], a[12:15], v[184:187]
s_cmp_eq_u32 s39, 1
s_mov_b32 s28, 4294885376
s_cselect_b32 s28, 98304, s28
v_mfma_f32_16x16x32_bf16 v[224:227], a[140:143], a[196:199], v[224:227]
v_add_u32 v43, 4294885376, v43
v_add_u32 v44, 4294885376, v44
v_mfma_f32_16x16x32_bf16 v[224:227], a[12:15], a[36:39], v[224:227]
buffer_load_dwordx4 v38, s[12:15], s24 offen offset:2048 lds
v_mfma_f32_16x16x32_bf16 v[144:147], a[164:167], a[140:143], v[144:147]
v_add_u32 v47, 98304, v47
v_add_u32 v48, 98304, v48
v_mfma_f32_16x16x32_bf16 v[144:147], a[20:23], a[12:15], v[144:147]
v_add_u32 v45, s28, v45
v_add_u32 v46, s28, v46
v_mfma_f32_16x16x32_bf16 v[192:195], a[172:175], a[160:163], v[192:195]
v_mfma_f32_16x16x32_bf16 v[192:195], a[28:31], a[16:19], v[192:195]
v_mfma_f32_16x16x32_bf16 v[232:235], a[160:163], a[196:199], v[232:235]
buffer_load_dwordx4 v39, s[12:15], s24 offen offset:3072 lds
v_mfma_f32_16x16x32_bf16 v[232:235], a[16:19], a[36:39], v[232:235]
s_add_u32 s24, s24, 128
v_mfma_f32_16x16x32_bf16 v[152:155], a[164:167], a[160:163], v[152:155]
v_mfma_f32_16x16x32_bf16 v[152:155], a[20:23], a[16:19], v[152:155]
s_waitcnt vmcnt(17) lgkmcnt(0)
s_barrier
v_mfma_f32_16x16x32_bf16 v[164:167], a[192:195], a[128:131], v[164:167]
ds_read_b64_tr_b16 a[172:173], v47 offset:2048
ds_read_b64_tr_b16 a[174:175], v47 offset:2560
v_mfma_f32_16x16x32_bf16 v[164:167], a[32:35], a[0:3], v[164:167]
s_add_u32 m0, s48, 32768
v_mfma_f32_16x16x32_bf16 v[204:207], a[128:131], a[200:203], v[204:207]
buffer_load_dwordx4 v40, s[16:19], s26 offen offset:0 lds
v_mfma_f32_16x16x32_bf16 v[204:207], a[0:3], a[40:43], v[204:207]
ds_read_b128 a[128:131], v45
v_mfma_f32_16x16x32_bf16 v[172:175], a[192:195], a[132:135], v[172:175]
ds_read_b128 a[0:3], v46
v_mfma_f32_16x16x32_bf16 v[172:175], a[32:35], a[4:7], v[172:175]
ds_read_b64_tr_b16 a[28:29], v47 offset:16384
ds_read_b64_tr_b16 a[30:31], v47 offset:16896
v_mfma_f32_16x16x32_bf16 v[212:215], a[132:135], a[200:203], v[212:215]
v_mfma_f32_16x16x32_bf16 v[212:215], a[4:7], a[40:43], v[212:215]
buffer_load_dwordx4 v41, s[16:19], s26 offen offset:1024 lds
v_mfma_f32_16x16x32_bf16 v[132:135], a[168:171], a[132:135], v[132:135]
ds_read_b64_tr_b16 a[196:197], v47 offset:4096
ds_read_b64_tr_b16 a[198:199], v47 offset:4608
v_mfma_f32_16x16x32_bf16 v[132:135], a[24:27], a[4:7], v[132:135]
ds_read_b128 a[132:135], v43 offset:0
v_mfma_f32_16x16x32_bf16 v[180:183], a[192:195], a[136:139], v[180:183]
ds_read_b128 a[4:7], v44 offset:0
v_mfma_f32_16x16x32_bf16 v[180:183], a[32:35], a[8:11], v[180:183]
ds_read_b64_tr_b16 a[36:37], v47 offset:18432
ds_read_b64_tr_b16 a[38:39], v47 offset:18944
v_mfma_f32_16x16x32_bf16 v[220:223], a[136:139], a[200:203], v[220:223]
buffer_load_dwordx4 v40, s[16:19], s26 offen offset:2048 lds
v_mfma_f32_16x16x32_bf16 v[220:223], a[8:11], a[40:43], v[220:223]
ds_read_b64_tr_b16 a[164:165], v47 offset:0
ds_read_b64_tr_b16 a[166:167], v47 offset:512
v_mfma_f32_16x16x32_bf16 v[140:143], a[168:171], a[136:139], v[140:143]
v_mfma_f32_16x16x32_bf16 v[140:143], a[24:27], a[8:11], v[140:143]
ds_read_b128 a[136:139], v43 offset:2048
v_mfma_f32_16x16x32_bf16 v[188:191], a[192:195], a[140:143], v[188:191]
ds_read_b128 a[8:11], v44 offset:2048
v_mfma_f32_16x16x32_bf16 v[188:191], a[32:35], a[12:15], v[188:191]
buffer_load_dwordx4 v41, s[16:19], s26 offen offset:3072 lds
v_mfma_f32_16x16x32_bf16 v[228:231], a[140:143], a[200:203], v[228:231]
ds_read_b64_tr_b16 a[20:21], v47 offset:14336
ds_read_b64_tr_b16 a[22:23], v47 offset:14848
v_mfma_f32_16x16x32_bf16 v[228:231], a[12:15], a[40:43], v[228:231]
v_mfma_f32_16x16x32_bf16 v[148:151], a[168:171], a[140:143], v[148:151]
v_mfma_f32_16x16x32_bf16 v[148:151], a[24:27], a[12:15], v[148:151]
s_add_u32 m0, s48, 36864
ds_read_b128 a[140:143], v43 offset:4096
v_mfma_f32_16x16x32_bf16 v[196:199], a[192:195], a[160:163], v[196:199]
buffer_load_dwordx4 v40, s[16:19], s47 offen offset:0 lds
v_mfma_f32_16x16x32_bf16 v[196:199], a[32:35], a[16:19], v[196:199]
ds_read_b128 a[12:15], v44 offset:4096
v_mfma_f32_16x16x32_bf16 v[236:239], a[160:163], a[200:203], v[236:239]
v_mfma_f32_16x16x32_bf16 v[236:239], a[16:19], a[40:43], v[236:239]
s_waitcnt lgkmcnt(2)
v_mfma_f32_16x16x32_bf16 v[156:159], a[168:171], a[160:163], v[156:159]
buffer_load_dwordx4 v41, s[16:19], s47 offen offset:1024 lds
v_mfma_f32_16x16x32_bf16 v[156:159], a[24:27], a[16:19], v[156:159]
ds_read_b128 a[160:163], v43 offset:6144
ds_read_b128 a[16:19], v44 offset:6144
v_mfma_f32_16x16x32_bf16 v[160:163], a[172:175], a[128:131], v[160:163]
ds_read_b64_tr_b16 a[192:193], v48 offset:2048
ds_read_b64_tr_b16 a[194:195], v48 offset:2560
v_mfma_f32_16x16x32_bf16 v[160:163], a[28:31], a[0:3], v[160:163]
v_mfma_f32_16x16x32_bf16 v[200:203], a[128:131], a[196:199], v[200:203]
ds_read_b64_tr_b16 a[32:33], v48 offset:16384
ds_read_b64_tr_b16 a[34:35], v48 offset:16896
v_mfma_f32_16x16x32_bf16 v[200:203], a[0:3], a[36:39], v[200:203]
buffer_load_dwordx4 v42, s[12:15], s25 offen offset:2048 lds
v_mfma_f32_16x16x32_bf16 v[168:171], a[172:175], a[132:135], v[168:171]
s_add_u32 s26, s26, 393216
s_add_u32 s47, s47, 393216
v_mfma_f32_16x16x32_bf16 v[168:171], a[28:31], a[4:7], v[168:171]
s_add_u32 s25, s25, 128
v_mfma_f32_16x16x32_bf16 v[208:211], a[132:135], a[196:199], v[208:211]
ds_read_b64_tr_b16 a[200:201], v48 offset:4096
ds_read_b64_tr_b16 a[202:203], v48 offset:4608
v_mfma_f32_16x16x32_bf16 v[208:211], a[4:7], a[36:39], v[208:211]
v_mfma_f32_16x16x32_bf16 v[128:131], a[164:167], a[132:135], v[128:131]
s_add_u32 m0, s91, 81920
v_mfma_f32_16x16x32_bf16 v[128:131], a[20:23], a[4:7], v[128:131]
buffer_load_dwordx4 v36, s[12:15], s24 offen offset:0 lds
v_mfma_f32_16x16x32_bf16 v[176:179], a[172:175], a[136:139], v[176:179]
ds_read_b64_tr_b16 a[40:41], v48 offset:18432
ds_read_b64_tr_b16 a[42:43], v48 offset:18944
v_mfma_f32_16x16x32_bf16 v[176:179], a[28:31], a[8:11], v[176:179]
v_mfma_f32_16x16x32_bf16 v[216:219], a[136:139], a[196:199], v[216:219]
ds_read_b64_tr_b16 a[168:169], v48 offset:0
ds_read_b64_tr_b16 a[170:171], v48 offset:512
v_mfma_f32_16x16x32_bf16 v[216:219], a[8:11], a[36:39], v[216:219]
s_waitcnt lgkmcnt(10)
v_mfma_f32_16x16x32_bf16 v[136:139], a[164:167], a[136:139], v[136:139]
buffer_load_dwordx4 v37, s[12:15], s24 offen offset:1024 lds
v_mfma_f32_16x16x32_bf16 v[136:139], a[20:23], a[8:11], v[136:139]
ds_read_b64_tr_b16 a[24:25], v48 offset:14336
ds_read_b64_tr_b16 a[26:27], v48 offset:14848
v_mfma_f32_16x16x32_bf16 v[184:187], a[172:175], a[140:143], v[184:187]
v_mfma_f32_16x16x32_bf16 v[184:187], a[28:31], a[12:15], v[184:187]
s_cmp_eq_u32 s39, 1
s_mov_b32 s28, 16384
s_cselect_b32 s28, 4294934528, s28
v_mfma_f32_16x16x32_bf16 v[224:227], a[140:143], a[196:199], v[224:227]
v_add_u32 v43, 16384, v43
v_add_u32 v44, 16384, v44
v_mfma_f32_16x16x32_bf16 v[224:227], a[12:15], a[36:39], v[224:227]
buffer_load_dwordx4 v38, s[12:15], s24 offen offset:2048 lds
v_mfma_f32_16x16x32_bf16 v[144:147], a[164:167], a[140:143], v[144:147]
v_add_u32 v47, 4294934528, v47
v_add_u32 v48, 4294934528, v48
v_mfma_f32_16x16x32_bf16 v[144:147], a[20:23], a[12:15], v[144:147]
v_add_u32 v45, s28, v45
v_add_u32 v46, s28, v46
v_mfma_f32_16x16x32_bf16 v[192:195], a[172:175], a[160:163], v[192:195]
v_mfma_f32_16x16x32_bf16 v[192:195], a[28:31], a[16:19], v[192:195]
v_mfma_f32_16x16x32_bf16 v[232:235], a[160:163], a[196:199], v[232:235]
buffer_load_dwordx4 v39, s[12:15], s24 offen offset:3072 lds
v_mfma_f32_16x16x32_bf16 v[232:235], a[16:19], a[36:39], v[232:235]
s_add_u32 s24, s24, 128
v_mfma_f32_16x16x32_bf16 v[152:155], a[164:167], a[160:163], v[152:155]
v_mfma_f32_16x16x32_bf16 v[152:155], a[20:23], a[16:19], v[152:155]
s_waitcnt vmcnt(16) lgkmcnt(0)
s_barrier
v_mfma_f32_16x16x32_bf16 v[164:167], a[192:195], a[128:131], v[164:167]
ds_read_b64_tr_b16 a[172:173], v47 offset:2048
ds_read_b64_tr_b16 a[174:175], v47 offset:2560
v_mfma_f32_16x16x32_bf16 v[164:167], a[32:35], a[0:3], v[164:167]
s_add_u32 m0, s48, 131072
v_mfma_f32_16x16x32_bf16 v[204:207], a[128:131], a[200:203], v[204:207]
buffer_load_dwordx4 v40, s[16:19], s26 offen offset:0 lds
v_mfma_f32_16x16x32_bf16 v[204:207], a[0:3], a[40:43], v[204:207]
ds_read_b128 a[128:131], v45
v_mfma_f32_16x16x32_bf16 v[172:175], a[192:195], a[132:135], v[172:175]
ds_read_b128 a[0:3], v46
v_mfma_f32_16x16x32_bf16 v[172:175], a[32:35], a[4:7], v[172:175]
ds_read_b64_tr_b16 a[28:29], v47 offset:16384
ds_read_b64_tr_b16 a[30:31], v47 offset:16896
v_mfma_f32_16x16x32_bf16 v[212:215], a[132:135], a[200:203], v[212:215]
v_mfma_f32_16x16x32_bf16 v[212:215], a[4:7], a[40:43], v[212:215]
buffer_load_dwordx4 v41, s[16:19], s26 offen offset:1024 lds
v_mfma_f32_16x16x32_bf16 v[132:135], a[168:171], a[132:135], v[132:135]
ds_read_b64_tr_b16 a[196:197], v47 offset:4096
ds_read_b64_tr_b16 a[198:199], v47 offset:4608
v_mfma_f32_16x16x32_bf16 v[132:135], a[24:27], a[4:7], v[132:135]
ds_read_b128 a[132:135], v43 offset:0
v_mfma_f32_16x16x32_bf16 v[180:183], a[192:195], a[136:139], v[180:183]
ds_read_b128 a[4:7], v44 offset:0
v_mfma_f32_16x16x32_bf16 v[180:183], a[32:35], a[8:11], v[180:183]
ds_read_b64_tr_b16 a[36:37], v47 offset:18432
ds_read_b64_tr_b16 a[38:39], v47 offset:18944
v_mfma_f32_16x16x32_bf16 v[220:223], a[136:139], a[200:203], v[220:223]
buffer_load_dwordx4 v40, s[16:19], s26 offen offset:2048 lds
v_mfma_f32_16x16x32_bf16 v[220:223], a[8:11], a[40:43], v[220:223]
ds_read_b64_tr_b16 a[164:165], v47 offset:0
ds_read_b64_tr_b16 a[166:167], v47 offset:512
v_mfma_f32_16x16x32_bf16 v[140:143], a[168:171], a[136:139], v[140:143]
v_mfma_f32_16x16x32_bf16 v[140:143], a[24:27], a[8:11], v[140:143]
ds_read_b128 a[136:139], v43 offset:2048
v_mfma_f32_16x16x32_bf16 v[188:191], a[192:195], a[140:143], v[188:191]
ds_read_b128 a[8:11], v44 offset:2048
v_mfma_f32_16x16x32_bf16 v[188:191], a[32:35], a[12:15], v[188:191]
buffer_load_dwordx4 v41, s[16:19], s26 offen offset:3072 lds
v_mfma_f32_16x16x32_bf16 v[228:231], a[140:143], a[200:203], v[228:231]
ds_read_b64_tr_b16 a[20:21], v47 offset:14336
ds_read_b64_tr_b16 a[22:23], v47 offset:14848
v_mfma_f32_16x16x32_bf16 v[228:231], a[12:15], a[40:43], v[228:231]
v_mfma_f32_16x16x32_bf16 v[148:151], a[168:171], a[140:143], v[148:151]
v_mfma_f32_16x16x32_bf16 v[148:151], a[24:27], a[12:15], v[148:151]
s_add_u32 m0, s48, 135168
ds_read_b128 a[140:143], v43 offset:4096
v_mfma_f32_16x16x32_bf16 v[196:199], a[192:195], a[160:163], v[196:199]
buffer_load_dwordx4 v40, s[16:19], s47 offen offset:0 lds
v_mfma_f32_16x16x32_bf16 v[196:199], a[32:35], a[16:19], v[196:199]
ds_read_b128 a[12:15], v44 offset:4096
v_mfma_f32_16x16x32_bf16 v[236:239], a[160:163], a[200:203], v[236:239]
v_mfma_f32_16x16x32_bf16 v[236:239], a[16:19], a[40:43], v[236:239]
s_waitcnt lgkmcnt(2)
v_mfma_f32_16x16x32_bf16 v[156:159], a[168:171], a[160:163], v[156:159]
buffer_load_dwordx4 v41, s[16:19], s47 offen offset:1024 lds
v_mfma_f32_16x16x32_bf16 v[156:159], a[24:27], a[16:19], v[156:159]
ds_read_b128 a[160:163], v43 offset:6144
ds_read_b128 a[16:19], v44 offset:6144
v_mfma_f32_16x16x32_bf16 v[160:163], a[172:175], a[128:131], v[160:163]
ds_read_b64_tr_b16 a[192:193], v48 offset:2048
ds_read_b64_tr_b16 a[194:195], v48 offset:2560
v_mfma_f32_16x16x32_bf16 v[160:163], a[28:31], a[0:3], v[160:163]
s_waitcnt vmcnt(21)
v_cmp_eq_u32 vcc, 0, v250
v_mfma_f32_16x16x32_bf16 v[200:203], a[128:131], a[196:199], v[200:203]
ds_read_b64_tr_b16 a[32:33], v48 offset:16384
ds_read_b64_tr_b16 a[34:35], v48 offset:16896
v_mfma_f32_16x16x32_bf16 v[200:203], a[0:3], a[36:39], v[200:203]
buffer_load_dwordx4 v42, s[12:15], s25 offen offset:2048 lds
v_mfma_f32_16x16x32_bf16 v[168:171], a[172:175], a[132:135], v[168:171]
s_add_u32 s26, s26, 393216
s_add_u32 s47, s47, 393216
v_mfma_f32_16x16x32_bf16 v[168:171], a[28:31], a[4:7], v[168:171]
s_add_u32 s25, s25, 128
s_cmp_lg_u64 vcc, 0
s_cbranch_scc1 .Lspin6
.Lspin6_ret:
v_mfma_f32_16x16x32_bf16 v[208:211], a[132:135], a[196:199], v[208:211]
ds_read_b64_tr_b16 a[200:201], v48 offset:4096
ds_read_b64_tr_b16 a[202:203], v48 offset:4608
v_mfma_f32_16x16x32_bf16 v[208:211], a[4:7], a[36:39], v[208:211]
v_mov_b32 v251, 0
v_readlane_b32 s54, v250, 0
v_mfma_f32_16x16x32_bf16 v[128:131], a[164:167], a[132:135], v[128:131]
s_add_u32 m0, s91, 0
v_nop
v_nop
v_mfma_f32_16x16x32_bf16 v[128:131], a[20:23], a[4:7], v[128:131]
buffer_load_dwordx4 v36, s[12:15], s24 offen offset:0 lds
v_subrev_u32 v252, s54, v250
v_mfma_f32_16x16x32_bf16 v[176:179], a[172:175], a[136:139], v[176:179]
ds_read_b64_tr_b16 a[40:41], v48 offset:18432
ds_read_b64_tr_b16 a[42:43], v48 offset:18944
v_mfma_f32_16x16x32_bf16 v[176:179], a[28:31], a[8:11], v[176:179]
v_cmp_lt_i32 vcc, 0, v252
v_nop
v_mfma_f32_16x16x32_bf16 v[216:219], a[136:139], a[196:199], v[216:219]
ds_read_b64_tr_b16 a[168:169], v48 offset:0
ds_read_b64_tr_b16 a[170:171], v48 offset:512
v_mfma_f32_16x16x32_bf16 v[216:219], a[8:11], a[36:39], v[216:219]
s_waitcnt lgkmcnt(10)
v_nop
v_addc_co_u32 v251, vcc, 0, v251, vcc
v_mfma_f32_16x16x32_bf16 v[136:139], a[164:167], a[136:139], v[136:139]
buffer_load_dwordx4 v37, s[12:15], s24 offen offset:1024 lds
v_readlane_b32 s55, v250, 1
v_mfma_f32_16x16x32_bf16 v[136:139], a[20:23], a[8:11], v[136:139]
ds_read_b64_tr_b16 a[24:25], v48 offset:14336
ds_read_b64_tr_b16 a[26:27], v48 offset:14848
v_mfma_f32_16x16x32_bf16 v[184:187], a[172:175], a[140:143], v[184:187]
v_nop
v_nop
v_mfma_f32_16x16x32_bf16 v[184:187], a[28:31], a[12:15], v[184:187]
s_cmp_eq_u32 s39, 1
s_mov_b32 s28, 49152
s_cselect_b32 s28, 4294901760, s28
v_mfma_f32_16x16x32_bf16 v[224:227], a[140:143], a[196:199], v[224:227]
v_add_u32 v43, 49152, v43
v_add_u32 v44, 49152, v44
v_mfma_f32_16x16x32_bf16 v[224:227], a[12:15], a[36:39], v[224:227]
buffer_load_dwordx4 v38, s[12:15], s24 offen offset:2048 lds
v_subrev_u32 v252, s55, v250
v_mfma_f32_16x16x32_bf16 v[144:147], a[164:167], a[140:143], v[144:147]
v_add_u32 v47, 4294901760, v47
v_add_u32 v48, 4294901760, v48
v_mfma_f32_16x16x32_bf16 v[144:147], a[20:23], a[12:15], v[144:147]
v_add_u32 v45, s28, v45
v_add_u32 v46, s28, v46
v_mfma_f32_16x16x32_bf16 v[192:195], a[172:175], a[160:163], v[192:195]
v_cmp_lt_i32 vcc, 0, v252
v_nop
v_mfma_f32_16x16x32_bf16 v[192:195], a[28:31], a[16:19], v[192:195]
v_nop
v_addc_co_u32 v251, vcc, 0, v251, vcc
v_mfma_f32_16x16x32_bf16 v[232:235], a[160:163], a[196:199], v[232:235]
buffer_load_dwordx4 v39, s[12:15], s24 offen offset:3072 lds
v_readlane_b32 s54, v250, 2
v_mfma_f32_16x16x32_bf16 v[232:235], a[16:19], a[36:39], v[232:235]
s_add_u32 s24, s24, 128
v_nop
v_nop
v_mfma_f32_16x16x32_bf16 v[152:155], a[164:167], a[160:163], v[152:155]
v_subrev_u32 v252, s54, v250
v_cmp_lt_i32 vcc, 0, v252
v_mfma_f32_16x16x32_bf16 v[152:155], a[20:23], a[16:19], v[152:155]
s_waitcnt vmcnt(15) lgkmcnt(0)
s_barrier
v_nop
v_mfma_f32_16x16x32_bf16 v[164:167], a[192:195], a[128:131], v[164:167]
ds_read_b64_tr_b16 a[172:173], v47 offset:2048
ds_read_b64_tr_b16 a[174:175], v47 offset:2560
v_mfma_f32_16x16x32_bf16 v[164:167], a[32:35], a[0:3], v[164:167]
s_add_u32 m0, s48, 98304
v_nop
v_addc_co_u32 v251, vcc, 0, v251, vcc
v_mfma_f32_16x16x32_bf16 v[204:207], a[128:131], a[200:203], v[204:207]
buffer_load_dwordx4 v40, s[16:19], s26 offen offset:0 lds
v_readlane_b32 s55, v250, 3
v_mfma_f32_16x16x32_bf16 v[204:207], a[0:3], a[40:43], v[204:207]
ds_read_b128 a[128:131], v45
v_nop
v_mfma_f32_16x16x32_bf16 v[172:175], a[192:195], a[132:135], v[172:175]
ds_read_b128 a[0:3], v46
v_nop
v_mfma_f32_16x16x32_bf16 v[172:175], a[32:35], a[4:7], v[172:175]
ds_read_b64_tr_b16 a[28:29], v47 offset:16384
ds_read_b64_tr_b16 a[30:31], v47 offset:16896
v_mfma_f32_16x16x32_bf16 v[212:215], a[132:135], a[200:203], v[212:215]
v_subrev_u32 v252, s55, v250
v_cmp_lt_i32 vcc, 0, v252
v_mfma_f32_16x16x32_bf16 v[212:215], a[4:7], a[40:43], v[212:215]
buffer_load_dwordx4 v41, s[16:19], s26 offen offset:1024 lds
v_nop
v_mfma_f32_16x16x32_bf16 v[132:135], a[168:171], a[132:135], v[132:135]
ds_read_b64_tr_b16 a[196:197], v47 offset:4096
ds_read_b64_tr_b16 a[198:199], v47 offset:4608
v_mfma_f32_16x16x32_bf16 v[132:135], a[24:27], a[4:7], v[132:135]
ds_read_b128 a[132:135], v43 offset:0
v_nop
v_mfma_f32_16x16x32_bf16 v[180:183], a[192:195], a[136:139], v[180:183]
ds_read_b128 a[4:7], v44 offset:0
v_addc_co_u32 v251, vcc, 0, v251, vcc
v_mfma_f32_16x16x32_bf16 v[180:183], a[32:35], a[8:11], v[180:183]
ds_read_b64_tr_b16 a[36:37], v47 offset:18432
ds_read_b64_tr_b16 a[38:39], v47 offset:18944
v_mfma_f32_16x16x32_bf16 v[220:223], a[136:139], a[200:203], v[220:223]
buffer_load_dwordx4 v40, s[16:19], s26 offen offset:2048 lds
v_readlane_b32 s54, v250, 4
v_mfma_f32_16x16x32_bf16 v[220:223], a[8:11], a[40:43], v[220:223]
ds_read_b64_tr_b16 a[164:165], v47 offset:0
ds_read_b64_tr_b16 a[166:167], v47 offset:512
v_mfma_f32_16x16x32_bf16 v[140:143], a[168:171], a[136:139], v[140:143]
v_nop
v_nop
v_mfma_f32_16x16x32_bf16 v[140:143], a[24:27], a[8:11], v[140:143]
ds_read_b128 a[136:139], v43 offset:2048
v_subrev_u32 v252, s54, v250
v_mfma_f32_16x16x32_bf16 v[188:191], a[192:195], a[140:143], v[188:191]
ds_read_b128 a[8:11], v44 offset:2048
v_cmp_lt_i32 vcc, 0, v252
v_mfma_f32_16x16x32_bf16 v[188:191], a[32:35], a[12:15], v[188:191]
buffer_load_dwordx4 v41, s[16:19], s26 offen offset:3072 lds
v_nop
v_mfma_f32_16x16x32_bf16 v[228:231], a[140:143], a[200:203], v[228:231]
ds_read_b64_tr_b16 a[20:21], v47 offset:14336
ds_read_b64_tr_b16 a[22:23], v47 offset:14848
v_mfma_f32_16x16x32_bf16 v[228:231], a[12:15], a[40:43], v[228:231]
v_nop
v_addc_co_u32 v251, vcc, 0, v251, vcc
v_mfma_f32_16x16x32_bf16 v[148:151], a[168:171], a[140:143], v[148:151]
v_readlane_b32 s55, v250, 5
v_nop
v_mfma_f32_16x16x32_bf16 v[148:151], a[24:27], a[12:15], v[148:151]
s_add_u32 m0, s48, 102400
ds_read_b128 a[140:143], v43 offset:4096
v_mfma_f32_16x16x32_bf16 v[196:199], a[192:195], a[160:163], v[196:199]
buffer_load_dwordx4 v40, s[16:19], s47 offen offset:0 lds
v_nop
v_mfma_f32_16x16x32_bf16 v[196:199], a[32:35], a[16:19], v[196:199]
ds_read_b128 a[12:15], v44 offset:4096
v_subrev_u32 v252, s55, v250
v_mfma_f32_16x16x32_bf16 v[236:239], a[160:163], a[200:203], v[236:239]
v_cmp_lt_i32 vcc, 0, v252
v_nop
v_mfma_f32_16x16x32_bf16 v[236:239], a[16:19], a[40:43], v[236:239]
s_waitcnt lgkmcnt(2)
v_nop
v_addc_co_u32 v251, vcc, 0, v251, vcc
v_mfma_f32_16x16x32_bf16 v[156:159], a[168:171], a[160:163], v[156:159]
buffer_load_dwordx4 v41, s[16:19], s47 offen offset:1024 lds
v_readlane_b32 s54, v250, 6
v_mfma_f32_16x16x32_bf16 v[156:159], a[24:27], a[16:19], v[156:159]
ds_read_b128 a[160:163], v43 offset:6144
ds_read_b128 a[16:19], v44 offset:6144
v_mfma_f32_16x16x32_bf16 v[160:163], a[172:175], a[128:131], v[160:163]
ds_read_b64_tr_b16 a[192:193], v48 offset:2048
ds_read_b64_tr_b16 a[194:195], v48 offset:2560
v_mfma_f32_16x16x32_bf16 v[160:163], a[28:31], a[0:3], v[160:163]
v_nop
v_nop
v_mfma_f32_16x16x32_bf16 v[200:203], a[128:131], a[196:199], v[200:203]
ds_read_b64_tr_b16 a[32:33], v48 offset:16384
ds_read_b64_tr_b16 a[34:35], v48 offset:16896
.Lpad16:
s_cbranch_execz .Ltch7
v_mfma_f32_16x16x32_bf16 v[200:203], a[0:3], a[36:39], v[200:203]
buffer_load_dwordx4 v42, s[12:15], s25 offen offset:2048 lds
v_subrev_u32 v252, s54, v250
v_mfma_f32_16x16x32_bf16 v[168:171], a[172:175], a[132:135], v[168:171]
s_add_u32 s26, s26, 393216
s_add_u32 s47, s47, 393216
v_cmp_lt_i32 vcc, 0, v252
v_mfma_f32_16x16x32_bf16 v[168:171], a[28:31], a[4:7], v[168:171]
s_add_u32 s25, s25, 128
v_nop
v_nop
v_mfma_f32_16x16x32_bf16 v[208:211], a[132:135], a[196:199], v[208:211]
ds_read_b64_tr_b16 a[200:201], v48 offset:4096
ds_read_b64_tr_b16 a[202:203], v48 offset:4608
v_mfma_f32_16x16x32_bf16 v[208:211], a[4:7], a[36:39], v[208:211]
v_addc_co_u32 v251, vcc, 0, v251, vcc
v_readlane_b32 s55, v250, 7
v_mfma_f32_16x16x32_bf16 v[128:131], a[164:167], a[132:135], v[128:131]
s_add_u32 m0, s91, 16384
v_nop
v_nop
v_mfma_f32_16x16x32_bf16 v[128:131], a[20:23], a[4:7], v[128:131]
buffer_load_dwordx4 v36, s[12:15], s24 offen offset:0 lds
v_subrev_u32 v252, s55, v250
v_mfma_f32_16x16x32_bf16 v[176:179], a[172:175], a[136:139], v[176:179]
ds_read_b64_tr_b16 a[40:41], v48 offset:18432
ds_read_b64_tr_b16 a[42:43], v48 offset:18944
v_mfma_f32_16x16x32_bf16 v[176:179], a[28:31], a[8:11], v[176:179]
v_cmp_lt_i32 vcc, 0, v252
v_nop
v_mfma_f32_16x16x32_bf16 v[216:219], a[136:139], a[196:199], v[216:219]
ds_read_b64_tr_b16 a[168:169], v48 offset:0
ds_read_b64_tr_b16 a[170:171], v48 offset:512
v_mfma_f32_16x16x32_bf16 v[216:219], a[8:11], a[36:39], v[216:219]
s_waitcnt lgkmcnt(10)
v_nop
v_addc_co_u32 v251, vcc, 0, v251, vcc
v_mfma_f32_16x16x32_bf16 v[136:139], a[164:167], a[136:139], v[136:139]
buffer_load_dwordx4 v37, s[12:15], s24 offen offset:1024 lds
v_nop
v_mfma_f32_16x16x32_bf16 v[136:139], a[20:23], a[8:11], v[136:139]
ds_read_b64_tr_b16 a[24:25], v48 offset:14336
ds_read_b64_tr_b16 a[26:27], v48 offset:14848
v_mfma_f32_16x16x32_bf16 v[184:187], a[172:175], a[140:143], v[184:187]
v_readlane_b32 s54, v251, s86
v_mfma_f32_16x16x32_bf16 v[184:187], a[28:31], a[12:15], v[184:187]
s_cmp_eq_u32 s39, 1
s_mov_b32 s28, 16384
s_cselect_b32 s28, 98304, s28
v_mfma_f32_16x16x32_bf16 v[224:227], a[140:143], a[196:199], v[224:227]
v_add_u32 v43, 16384, v43
v_add_u32 v44, 16384, v44
v_mfma_f32_16x16x32_bf16 v[224:227], a[12:15], a[36:39], v[224:227]
s_sub_u32 s55, 7, s54
buffer_load_dwordx4 v38, s[12:15], s24 offen offset:2048 lds
v_mfma_f32_16x16x32_bf16 v[144:147], a[164:167], a[140:143], v[144:147]
v_add_u32 v47, 98304, v47
v_add_u32 v48, 98304, v48
v_mfma_f32_16x16x32_bf16 v[144:147], a[20:23], a[12:15], v[144:147]
v_add_u32 v45, s28, v45
v_add_u32 v46, s28, v46
v_mfma_f32_16x16x32_bf16 v[192:195], a[172:175], a[160:163], v[192:195]
v_cmp_eq_u32 vcc, s55, v251
v_mfma_f32_16x16x32_bf16 v[192:195], a[28:31], a[16:19], v[192:195]
v_mfma_f32_16x16x32_bf16 v[232:235], a[160:163], a[196:199], v[232:235]
s_ff1_i32_b64 s38, vcc
buffer_load_dwordx4 v39, s[12:15], s24 offen offset:3072 lds
v_mfma_f32_16x16x32_bf16 v[232:235], a[16:19], a[36:39], v[232:235]
s_add_u32 s24, s24, 128
s_and_b32 s56, s2, -8
s_or_b32 s38, s38, s56
v_mfma_f32_16x16x32_bf16 v[152:155], a[164:167], a[160:163], v[152:155]
s_min_u32 s55, s55, s54
s_lshr_b32 s54, s54, 2
s_lshl_b32 s42, s55, 1
v_mfma_f32_16x16x32_bf16 v[152:155], a[20:23], a[16:19], v[152:155]
s_waitcnt vmcnt(15) lgkmcnt(0)
s_barrier
s_or_b32 s42, s42, s54
v_mfma_f32_16x16x32_bf16 v[164:167], a[192:195], a[128:131], v[164:167]
ds_read_b64_tr_b16 a[172:173], v47 offset:2048
ds_read_b64_tr_b16 a[174:175], v47 offset:2560
v_mfma_f32_16x16x32_bf16 v[164:167], a[32:35], a[0:3], v[164:167]
s_add_u32 m0, s48, 32768
s_lshl_b32 s55, s55, 3
s_lshr_b32 s43, 707472430, s55
v_mfma_f32_16x16x32_bf16 v[204:207], a[128:131], a[200:203], v[204:207]
s_and_b32 s43, s43, 0xff
buffer_load_dwordx4 v40, s[16:19], s26 offen offset:0 lds
v_mfma_f32_16x16x32_bf16 v[204:207], a[0:3], a[40:43], v[204:207]
ds_read_b128 a[128:131], v45
v_mfma_f32_16x16x32_bf16 v[172:175], a[192:195], a[132:135], v[172:175]
ds_read_b128 a[0:3], v46
v_mfma_f32_16x16x32_bf16 v[172:175], a[32:35], a[4:7], v[172:175]
ds_read_b64_tr_b16 a[28:29], v47 offset:16384
ds_read_b64_tr_b16 a[30:31], v47 offset:16896
v_mfma_f32_16x16x32_bf16 v[212:215], a[132:135], a[200:203], v[212:215]
v_mfma_f32_16x16x32_bf16 v[212:215], a[4:7], a[40:43], v[212:215]
buffer_load_dwordx4 v41, s[16:19], s26 offen offset:1024 lds
v_mfma_f32_16x16x32_bf16 v[132:135], a[168:171], a[132:135], v[132:135]
ds_read_b64_tr_b16 a[196:197], v47 offset:4096
ds_read_b64_tr_b16 a[198:199], v47 offset:4608
v_mfma_f32_16x16x32_bf16 v[132:135], a[24:27], a[4:7], v[132:135]
ds_read_b128 a[132:135], v43 offset:0
v_mfma_f32_16x16x32_bf16 v[180:183], a[192:195], a[136:139], v[180:183]
ds_read_b128 a[4:7], v44 offset:0
v_mfma_f32_16x16x32_bf16 v[180:183], a[32:35], a[8:11], v[180:183]
ds_read_b64_tr_b16 a[36:37], v47 offset:18432
ds_read_b64_tr_b16 a[38:39], v47 offset:18944
v_mfma_f32_16x16x32_bf16 v[220:223], a[136:139], a[200:203], v[220:223]
buffer_load_dwordx4 v40, s[16:19], s26 offen offset:2048 lds
v_mfma_f32_16x16x32_bf16 v[220:223], a[8:11], a[40:43], v[220:223]
ds_read_b64_tr_b16 a[164:165], v47 offset:0
ds_read_b64_tr_b16 a[166:167], v47 offset:512
v_mfma_f32_16x16x32_bf16 v[140:143], a[168:171], a[136:139], v[140:143]
v_mfma_f32_16x16x32_bf16 v[140:143], a[24:27], a[8:11], v[140:143]
ds_read_b128 a[136:139], v43 offset:2048
v_mfma_f32_16x16x32_bf16 v[188:191], a[192:195], a[140:143], v[188:191]
ds_read_b128 a[8:11], v44 offset:2048
v_mfma_f32_16x16x32_bf16 v[188:191], a[32:35], a[12:15], v[188:191]
buffer_load_dwordx4 v41, s[16:19], s26 offen offset:3072 lds
v_mfma_f32_16x16x32_bf16 v[228:231], a[140:143], a[200:203], v[228:231]
ds_read_b64_tr_b16 a[20:21], v47 offset:14336
ds_read_b64_tr_b16 a[22:23], v47 offset:14848
v_mfma_f32_16x16x32_bf16 v[228:231], a[12:15], a[40:43], v[228:231]
v_mfma_f32_16x16x32_bf16 v[148:151], a[168:171], a[140:143], v[148:151]
v_mfma_f32_16x16x32_bf16 v[148:151], a[24:27], a[12:15], v[148:151]
s_add_u32 m0, s48, 36864
ds_read_b128 a[140:143], v43 offset:4096
v_mfma_f32_16x16x32_bf16 v[196:199], a[192:195], a[160:163], v[196:199]
buffer_load_dwordx4 v40, s[16:19], s47 offen offset:0 lds
v_mfma_f32_16x16x32_bf16 v[196:199], a[32:35], a[16:19], v[196:199]
ds_read_b128 a[12:15], v44 offset:4096
v_mfma_f32_16x16x32_bf16 v[236:239], a[160:163], a[200:203], v[236:239]
v_mfma_f32_16x16x32_bf16 v[236:239], a[16:19], a[40:43], v[236:239]
s_waitcnt lgkmcnt(2)
v_mfma_f32_16x16x32_bf16 v[156:159], a[168:171], a[160:163], v[156:159]
buffer_load_dwordx4 v41, s[16:19], s47 offen offset:1024 lds
v_mfma_f32_16x16x32_bf16 v[156:159], a[24:27], a[16:19], v[156:159]
ds_read_b128 a[160:163], v43 offset:6144
ds_read_b128 a[16:19], v44 offset:6144
v_mfma_f32_16x16x32_bf16 v[160:163], a[172:175], a[128:131], v[160:163]
ds_read_b64_tr_b16 a[192:193], v48 offset:2048
ds_read_b64_tr_b16 a[194:195], v48 offset:2560
v_mfma_f32_16x16x32_bf16 v[160:163], a[28:31], a[0:3], v[160:163]
v_mfma_f32_16x16x32_bf16 v[200:203], a[128:131], a[196:199], v[200:203]
ds_read_b64_tr_b16 a[32:33], v48 offset:16384
ds_read_b64_tr_b16 a[34:35], v48 offset:16896
v_mfma_f32_16x16x32_bf16 v[200:203], a[0:3], a[36:39], v[200:203]
buffer_load_dwordx4 v42, s[12:15], s25 offen offset:2048 lds
v_mfma_f32_16x16x32_bf16 v[168:171], a[172:175], a[132:135], v[168:171]
s_add_u32 s26, s26, 393216
s_add_u32 s47, s47, 393216
v_mfma_f32_16x16x32_bf16 v[168:171], a[28:31], a[4:7], v[168:171]
s_add_u32 s25, s25, 128
v_mfma_f32_16x16x32_bf16 v[208:211], a[132:135], a[196:199], v[208:211]
ds_read_b64_tr_b16 a[200:201], v48 offset:4096
ds_read_b64_tr_b16 a[202:203], v48 offset:4608
v_mfma_f32_16x16x32_bf16 v[208:211], a[4:7], a[36:39], v[208:211]
v_mfma_f32_16x16x32_bf16 v[128:131], a[164:167], a[132:135], v[128:131]
s_add_u32 m0, s91, 65536
v_mfma_f32_16x16x32_bf16 v[128:131], a[20:23], a[4:7], v[128:131]
buffer_load_dwordx4 v36, s[12:15], s24 offen offset:0 lds
v_mfma_f32_16x16x32_bf16 v[176:179], a[172:175], a[136:139], v[176:179]
ds_read_b64_tr_b16 a[40:41], v48 offset:18432
ds_read_b64_tr_b16 a[42:43], v48 offset:18944
v_mfma_f32_16x16x32_bf16 v[176:179], a[28:31], a[8:11], v[176:179]
v_mfma_f32_16x16x32_bf16 v[216:219], a[136:139], a[196:199], v[216:219]
ds_read_b64_tr_b16 a[168:169], v48 offset:0
ds_read_b64_tr_b16 a[170:171], v48 offset:512
v_mfma_f32_16x16x32_bf16 v[216:219], a[8:11], a[36:39], v[216:219]
s_waitcnt lgkmcnt(10)
v_mfma_f32_16x16x32_bf16 v[136:139], a[164:167], a[136:139], v[136:139]
buffer_load_dwordx4 v37, s[12:15], s24 offen offset:1024 lds
v_mfma_f32_16x16x32_bf16 v[136:139], a[20:23], a[8:11], v[136:139]
ds_read_b64_tr_b16 a[24:25], v48 offset:14336
ds_read_b64_tr_b16 a[26:27], v48 offset:14848
v_mfma_f32_16x16x32_bf16 v[184:187], a[172:175], a[140:143], v[184:187]
v_mfma_f32_16x16x32_bf16 v[184:187], a[28:31], a[12:15], v[184:187]
s_cmp_eq_u32 s39, 1
s_mov_b32 s28, 4294885376
s_cselect_b32 s28, 4294934528, s28
v_mfma_f32_16x16x32_bf16 v[224:227], a[140:143], a[196:199], v[224:227]
v_add_u32 v43, 4294885376, v43
v_add_u32 v44, 4294885376, v44
v_mfma_f32_16x16x32_bf16 v[224:227], a[12:15], a[36:39], v[224:227]
buffer_load_dwordx4 v38, s[12:15], s24 offen offset:2048 lds
v_mfma_f32_16x16x32_bf16 v[144:147], a[164:167], a[140:143], v[144:147]
v_add_u32 v47, 4294934528, v47
v_add_u32 v48, 4294934528, v48
v_mfma_f32_16x16x32_bf16 v[144:147], a[20:23], a[12:15], v[144:147]
v_add_u32 v45, s28, v45
v_add_u32 v46, s28, v46
v_mfma_f32_16x16x32_bf16 v[192:195], a[172:175], a[160:163], v[192:195]
s_bfe_u32 s51, s42, 0x10001
s_lshl_b32 s51, s51, 3
s_and_b32 s54, s87, 7
v_mfma_f32_16x16x32_bf16 v[192:195], a[28:31], a[16:19], v[192:195]
s_or_b32 s51, s51, s54
s_lshr_b32 s52, s42, 2
s_lshl_b32 s52, s52, 2
v_mfma_f32_16x16x32_bf16 v[232:235], a[160:163], a[196:199], v[232:235]
s_lshr_b32 s54, s87, 3
buffer_load_dwordx4 v39, s[12:15], s24 offen offset:3072 lds
v_mfma_f32_16x16x32_bf16 v[232:235], a[16:19], a[36:39], v[232:235]
s_add_u32 s24, s24, 128
s_or_b32 s52, s52, s54
s_lshl_b32 s52, s52, 8
v_mfma_f32_16x16x32_bf16 v[152:155], a[164:167], a[160:163], v[152:155]
s_and_b32 s55, s42, 1
s_lshl_b32 s54, s51, 2
s_add_u32 s54, s54, s3
v_mfma_f32_16x16x32_bf16 v[152:155], a[20:23], a[16:19], v[152:155]
s_waitcnt vmcnt(15) lgkmcnt(0)
s_barrier
s_lshl_b32 s54, s54, 18
v_mfma_f32_16x16x32_bf16 v[164:167], a[192:195], a[128:131], v[164:167]
ds_read_b64_tr_b16 a[172:173], v47 offset:2048
ds_read_b64_tr_b16 a[174:175], v47 offset:2560
v_mfma_f32_16x16x32_bf16 v[164:167], a[32:35], a[0:3], v[164:167]
s_add_u32 m0, s48, 131072
s_add_u32 s54, s54, 33554432
v_mfma_f32_16x16x32_bf16 v[204:207], a[128:131], a[200:203], v[204:207]
buffer_load_dwordx4 v40, s[16:19], s26 offen offset:0 lds
v_mfma_f32_16x16x32_bf16 v[204:207], a[0:3], a[40:43], v[204:207]
ds_read_b128 a[128:131], v45
v_mfma_f32_16x16x32_bf16 v[172:175], a[192:195], a[132:135], v[172:175]
ds_read_b128 a[0:3], v46
v_mfma_f32_16x16x32_bf16 v[172:175], a[32:35], a[4:7], v[172:175]
ds_read_b64_tr_b16 a[28:29], v47 offset:16384
ds_read_b64_tr_b16 a[30:31], v47 offset:16896
v_mfma_f32_16x16x32_bf16 v[212:215], a[132:135], a[200:203], v[212:215]
s_add_u32 s4, s84, s54
s_addc_u32 s5, s85, 0
s_and_b32 s5, s5, 0xffff
v_mfma_f32_16x16x32_bf16 v[212:215], a[4:7], a[40:43], v[212:215]
s_lshl_b32 s54, s3, 15
buffer_load_dwordx4 v41, s[16:19], s26 offen offset:1024 lds
v_mfma_f32_16x16x32_bf16 v[132:135], a[168:171], a[132:135], v[132:135]
ds_read_b64_tr_b16 a[196:197], v47 offset:4096
ds_read_b64_tr_b16 a[198:199], v47 offset:4608
v_mfma_f32_16x16x32_bf16 v[132:135], a[24:27], a[4:7], v[132:135]
s_add_u32 s54, s54, s52
ds_read_b128 a[132:135], v43 offset:0
v_mfma_f32_16x16x32_bf16 v[180:183], a[192:195], a[136:139], v[180:183]
ds_read_b128 a[4:7], v44 offset:0
v_mfma_f32_16x16x32_bf16 v[180:183], a[32:35], a[8:11], v[180:183]
ds_read_b64_tr_b16 a[36:37], v47 offset:18432
ds_read_b64_tr_b16 a[38:39], v47 offset:18944
v_mfma_f32_16x16x32_bf16 v[220:223], a[136:139], a[200:203], v[220:223]
buffer_load_dwordx4 v40, s[16:19], s26 offen offset:2048 lds
v_mfma_f32_16x16x32_bf16 v[220:223], a[8:11], a[40:43], v[220:223]
ds_read_b64_tr_b16 a[164:165], v47 offset:0
ds_read_b64_tr_b16 a[166:167], v47 offset:512
v_mfma_f32_16x16x32_bf16 v[140:143], a[168:171], a[136:139], v[140:143]
s_add_u32 s8, s80, s54
s_addc_u32 s9, s81, 0
s_and_b32 s9, s9, 0xffff
v_mfma_f32_16x16x32_bf16 v[140:143], a[24:27], a[8:11], v[140:143]
s_lshl_b32 s56, s43, 12
ds_read_b128 a[136:139], v43 offset:2048
v_mfma_f32_16x16x32_bf16 v[188:191], a[192:195], a[140:143], v[188:191]
s_lshl_b32 s57, s43, 17
ds_read_b128 a[8:11], v44 offset:2048
v_mfma_f32_16x16x32_bf16 v[188:191], a[32:35], a[12:15], v[188:191]
s_mul_i32 s44, s55, s56
buffer_load_dwordx4 v41, s[16:19], s26 offen offset:3072 lds
v_mfma_f32_16x16x32_bf16 v[228:231], a[140:143], a[200:203], v[228:231]
ds_read_b64_tr_b16 a[20:21], v47 offset:14336
ds_read_b64_tr_b16 a[22:23], v47 offset:14848
v_mfma_f32_16x16x32_bf16 v[228:231], a[12:15], a[40:43], v[228:231]
s_mul_i32 s45, s55, s57
v_mfma_f32_16x16x32_bf16 v[148:151], a[168:171], a[140:143], v[148:151]
s_cmp_eq_u32 s55, 0
s_cselect_b32 s6, s56, 0x40000
s_cselect_b32 s57, s57, 0x800000
v_mfma_f32_16x16x32_bf16 v[148:151], a[24:27], a[12:15], v[148:151]
s_add_u32 m0, s48, 135168
ds_read_b128 a[140:143], v43 offset:4096
v_mfma_f32_16x16x32_bf16 v[196:199], a[192:195], a[160:163], v[196:199]
s_sub_u32 s10, s57, s54
buffer_load_dwordx4 v40, s[16:19], s47 offen offset:0 lds
v_mfma_f32_16x16x32_bf16 v[196:199], a[32:35], a[16:19], v[196:199]
s_and_b32 s54, s3, 1
ds_read_b128 a[12:15], v44 offset:4096
v_mfma_f32_16x16x32_bf16 v[236:239], a[160:163], a[200:203], v[236:239]
s_lshl_b32 s54, s54, 13
s_add_u32 s54, s54, 98304
s_lshr_b32 s56, s3, 1
v_mfma_f32_16x16x32_bf16 v[236:239], a[16:19], a[40:43], v[236:239]
s_waitcnt lgkmcnt(2)
s_lshl_b32 s56, s56, 2
s_sub_u32 s57, 15, s51
v_mfma_f32_16x16x32_bf16 v[156:159], a[168:171], a[160:163], v[156:159]
s_min_u32 s57, s57, s51
buffer_load_dwordx4 v41, s[16:19], s47 offen offset:1024 lds
v_mfma_f32_16x16x32_bf16 v[156:159], a[24:27], a[16:19], v[156:159]
ds_read_b128 a[160:163], v43 offset:6144
ds_read_b128 a[16:19], v44 offset:6144
v_mfma_f32_16x16x32_bf16 v[160:163], a[172:175], a[128:131], v[160:163]
ds_read_b64_tr_b16 a[192:193], v48 offset:2048
ds_read_b64_tr_b16 a[194:195], v48 offset:2560
v_mfma_f32_16x16x32_bf16 v[160:163], a[28:31], a[0:3], v[160:163]
s_lshl_b32 s57, s57, 10
v_mfma_f32_16x16x32_bf16 v[200:203], a[128:131], a[196:199], v[200:203]
ds_read_b64_tr_b16 a[32:33], v48 offset:16384
ds_read_b64_tr_b16 a[34:35], v48 offset:16896
v_mfma_f32_16x16x32_bf16 v[200:203], a[0:3], a[36:39], v[200:203]
buffer_load_dwordx4 v42, s[12:15], s25 offen offset:2048 lds
v_lshrrev_b32 v252, 4, v1
v_mfma_f32_16x16x32_bf16 v[168:171], a[172:175], a[132:135], v[168:171]
s_add_u32 s26, s26, 393216
s_add_u32 s47, s47, 393216
v_and_b32 v253, 15, v1
v_mfma_f32_16x16x32_bf16 v[168:171], a[28:31], a[4:7], v[168:171]
s_add_u32 s25, s25, 128
v_lshlrev_b32 v254, 1, v252
v_xor_b32 v254, v254, v253
v_mfma_f32_16x16x32_bf16 v[208:211], a[132:135], a[196:199], v[208:211]
ds_read_b64_tr_b16 a[200:201], v48 offset:4096
ds_read_b64_tr_b16 a[202:203], v48 offset:4608
v_mfma_f32_16x16x32_bf16 v[208:211], a[4:7], a[36:39], v[208:211]
v_lshlrev_b32 v254, 4, v254
v_lshl_add_u32 v240, v252, 11, v254
v_mfma_f32_16x16x32_bf16 v[128:131], a[164:167], a[132:135], v[128:131]
s_add_u32 m0, s91, 81920
v_xor_b32 v241, 0x80, v240
v_add_u32 v241, 7168, v241
v_mfma_f32_16x16x32_bf16 v[128:131], a[20:23], a[4:7], v[128:131]
buffer_load_dwordx4 v36, s[12:15], s24 offen offset:0 lds
v_add_u32 v242, 14336, v240
v_mfma_f32_16x16x32_bf16 v[176:179], a[172:175], a[136:139], v[176:179]
ds_read_b64_tr_b16 a[40:41], v48 offset:18432
ds_read_b64_tr_b16 a[42:43], v48 offset:18944
v_mfma_f32_16x16x32_bf16 v[176:179], a[28:31], a[8:11], v[176:179]
v_add_u32 v243, 14336, v241
v_add_u32 v244, s54, v7
v_mfma_f32_16x16x32_bf16 v[216:219], a[136:139], a[196:199], v[216:219]
ds_read_b64_tr_b16 a[168:169], v48 offset:0
ds_read_b64_tr_b16 a[170:171], v48 offset:512
v_mfma_f32_16x16x32_bf16 v[216:219], a[8:11], a[36:39], v[216:219]
s_waitcnt lgkmcnt(10)
v_bfe_u32 v254, v1, 2, 3
v_lshrrev_b32 v255, 2, v1
v_mfma_f32_16x16x32_bf16 v[136:139], a[164:167], a[136:139], v[136:139]
buffer_load_dwordx4 v37, s[12:15], s24 offen offset:1024 lds
v_lshlrev_b32 v255, 8, v255
v_mfma_f32_16x16x32_bf16 v[136:139], a[20:23], a[8:11], v[136:139]
ds_read_b64_tr_b16 a[24:25], v48 offset:14336
ds_read_b64_tr_b16 a[26:27], v48 offset:14848
v_mfma_f32_16x16x32_bf16 v[184:187], a[172:175], a[140:143], v[184:187]
v_and_b32 v253, 3, v1
v_lshl_add_u32 v255, v253, 3, v255
v_mfma_f32_16x16x32_bf16 v[184:187], a[28:31], a[12:15], v[184:187]
s_cmp_eq_u32 s39, 1
s_mov_b32 s28, 16384
s_cselect_b32 s28, 4294901760, s28
v_mfma_f32_16x16x32_bf16 v[224:227], a[140:143], a[196:199], v[224:227]
v_add_u32 v43, 16384, v43
v_add_u32 v44, 16384, v44
v_mfma_f32_16x16x32_bf16 v[224:227], a[12:15], a[36:39], v[224:227]
buffer_load_dwordx4 v38, s[12:15], s24 offen offset:2048 lds
v_add_u32 v255, 114688, v255
v_mfma_f32_16x16x32_bf16 v[144:147], a[164:167], a[140:143], v[144:147]
v_add_u32 v47, 4294901760, v47
v_add_u32 v48, 4294901760, v48
v_mfma_f32_16x16x32_bf16 v[144:147], a[20:23], a[12:15], v[144:147]
v_add_u32 v45, s28, v45
v_add_u32 v46, s28, v46
v_mfma_f32_16x16x32_bf16 v[192:195], a[172:175], a[160:163], v[192:195]
v_xor_b32 v254, s56, v254
v_xor_b32 v253, 0, v254
v_mfma_f32_16x16x32_bf16 v[192:195], a[28:31], a[16:19], v[192:195]
v_lshl_add_u32 v245, v253, 5, v255
v_xor_b32 v253, 1, v254
v_mfma_f32_16x16x32_bf16 v[232:235], a[160:163], a[196:199], v[232:235]
buffer_load_dwordx4 v39, s[12:15], s24 offen offset:3072 lds
v_lshl_add_u32 v246, v253, 5, v255
v_mfma_f32_16x16x32_bf16 v[232:235], a[16:19], a[36:39], v[232:235]
s_add_u32 s24, s24, 128
v_xor_b32 v253, 2, v254
v_lshl_add_u32 v247, v253, 5, v255
v_mfma_f32_16x16x32_bf16 v[152:155], a[164:167], a[160:163], v[152:155]
v_xor_b32 v253, 3, v254
v_lshl_add_u32 v248, v253, 5, v255
v_mfma_f32_16x16x32_bf16 v[152:155], a[20:23], a[16:19], v[152:155]
s_waitcnt vmcnt(15) lgkmcnt(0)
s_barrier
v_and_b32 v253, 31, v1
v_mfma_f32_16x16x32_bf16 v[164:167], a[192:195], a[128:131], v[164:167]
ds_read_b64_tr_b16 a[172:173], v47 offset:2048
ds_read_b64_tr_b16 a[174:175], v47 offset:2560
v_mfma_f32_16x16x32_bf16 v[164:167], a[32:35], a[0:3], v[164:167]
s_add_u32 m0, s48, 98304
v_lshl_add_u32 v249, v253, 5, s57
v_mfma_f32_16x16x32_bf16 v[204:207], a[128:131], a[200:203], v[204:207]
buffer_load_dwordx4 v40, s[16:19], s26 offen offset:0 lds
v_mfma_f32_16x16x32_bf16 v[204:207], a[0:3], a[40:43], v[204:207]
ds_read_b128 a[128:131], v45
v_mfma_f32_16x16x32_bf16 v[172:175], a[192:195], a[132:135], v[172:175]
ds_read_b128 a[0:3], v46
v_mfma_f32_16x16x32_bf16 v[172:175], a[32:35], a[4:7], v[172:175]
ds_read_b64_tr_b16 a[28:29], v47 offset:16384
ds_read_b64_tr_b16 a[30:31], v47 offset:16896
v_mfma_f32_16x16x32_bf16 v[212:215], a[132:135], a[200:203], v[212:215]
v_mfma_f32_16x16x32_bf16 v[212:215], a[4:7], a[40:43], v[212:215]
buffer_load_dwordx4 v41, s[16:19], s26 offen offset:1024 lds
v_mfma_f32_16x16x32_bf16 v[132:135], a[168:171], a[132:135], v[132:135]
ds_read_b64_tr_b16 a[196:197], v47 offset:4096
ds_read_b64_tr_b16 a[198:199], v47 offset:4608
v_mfma_f32_16x16x32_bf16 v[132:135], a[24:27], a[4:7], v[132:135]
ds_read_b128 a[132:135], v43 offset:0
v_mfma_f32_16x16x32_bf16 v[180:183], a[192:195], a[136:139], v[180:183]
ds_read_b128 a[4:7], v44 offset:0
v_mfma_f32_16x16x32_bf16 v[180:183], a[32:35], a[8:11], v[180:183]
ds_read_b64_tr_b16 a[36:37], v47 offset:18432
ds_read_b64_tr_b16 a[38:39], v47 offset:18944
v_mfma_f32_16x16x32_bf16 v[220:223], a[136:139], a[200:203], v[220:223]
buffer_load_dwordx4 v40, s[16:19], s26 offen offset:2048 lds
v_mfma_f32_16x16x32_bf16 v[220:223], a[8:11], a[40:43], v[220:223]
ds_read_b64_tr_b16 a[164:165], v47 offset:0
ds_read_b64_tr_b16 a[166:167], v47 offset:512
v_mfma_f32_16x16x32_bf16 v[140:143], a[168:171], a[136:139], v[140:143]
v_mfma_f32_16x16x32_bf16 v[140:143], a[24:27], a[8:11], v[140:143]
ds_read_b128 a[136:139], v43 offset:2048
v_mfma_f32_16x16x32_bf16 v[188:191], a[192:195], a[140:143], v[188:191]
ds_read_b128 a[8:11], v44 offset:2048
v_mfma_f32_16x16x32_bf16 v[188:191], a[32:35], a[12:15], v[188:191]
buffer_load_dwordx4 v41, s[16:19], s26 offen offset:3072 lds
v_mfma_f32_16x16x32_bf16 v[228:231], a[140:143], a[200:203], v[228:231]
ds_read_b64_tr_b16 a[20:21], v47 offset:14336
ds_read_b64_tr_b16 a[22:23], v47 offset:14848
v_mfma_f32_16x16x32_bf16 v[228:231], a[12:15], a[40:43], v[228:231]
v_mfma_f32_16x16x32_bf16 v[148:151], a[168:171], a[140:143], v[148:151]
v_mfma_f32_16x16x32_bf16 v[148:151], a[24:27], a[12:15], v[148:151]
s_add_u32 m0, s48, 102400
ds_read_b128 a[140:143], v43 offset:4096
v_mfma_f32_16x16x32_bf16 v[196:199], a[192:195], a[160:163], v[196:199]
buffer_load_dwordx4 v40, s[16:19], s47 offen offset:0 lds
v_mfma_f32_16x16x32_bf16 v[196:199], a[32:35], a[16:19], v[196:199]
ds_read_b128 a[12:15], v44 offset:4096
v_mfma_f32_16x16x32_bf16 v[236:239], a[160:163], a[200:203], v[236:239]
buffer_load_dword v35, v4, s[96:99], 0 offen
v_mfma_f32_16x16x32_bf16 v[236:239], a[16:19], a[40:43], v[236:239]
s_waitcnt lgkmcnt(2)
v_mfma_f32_16x16x32_bf16 v[156:159], a[168:171], a[160:163], v[156:159]
buffer_load_dwordx4 v41, s[16:19], s47 offen offset:1024 lds
v_mfma_f32_16x16x32_bf16 v[156:159], a[24:27], a[16:19], v[156:159]
ds_read_b128 a[160:163], v43 offset:6144
ds_read_b128 a[16:19], v44 offset:6144
v_mfma_f32_16x16x32_bf16 v[160:163], a[172:175], a[128:131], v[160:163]
ds_read_b64_tr_b16 a[192:193], v48 offset:2048
ds_read_b64_tr_b16 a[194:195], v48 offset:2560
v_mfma_f32_16x16x32_bf16 v[160:163], a[28:31], a[0:3], v[160:163]
v_mfma_f32_16x16x32_bf16 v[200:203], a[128:131], a[196:199], v[200:203]
ds_read_b64_tr_b16 a[32:33], v48 offset:16384
ds_read_b64_tr_b16 a[34:35], v48 offset:16896
v_mfma_f32_16x16x32_bf16 v[200:203], a[0:3], a[36:39], v[200:203]
buffer_load_dwordx4 v42, s[12:15], s25 offen offset:2048 lds
v_mfma_f32_16x16x32_bf16 v[168:171], a[172:175], a[132:135], v[168:171]
s_add_u32 s26, s26, 393216
s_add_u32 s47, s47, 393216
v_mfma_f32_16x16x32_bf16 v[168:171], a[28:31], a[4:7], v[168:171]
s_add_u32 s25, s25, 128
v_mfma_f32_16x16x32_bf16 v[208:211], a[132:135], a[196:199], v[208:211]
ds_read_b64_tr_b16 a[200:201], v48 offset:4096
ds_read_b64_tr_b16 a[202:203], v48 offset:4608
v_mfma_f32_16x16x32_bf16 v[208:211], a[4:7], a[36:39], v[208:211]
v_mfma_f32_16x16x32_bf16 v[128:131], a[164:167], a[132:135], v[128:131]
s_add_u32 m0, s91, 0
v_mfma_f32_16x16x32_bf16 v[128:131], a[20:23], a[4:7], v[128:131]
buffer_load_dwordx4 v36, s[12:15], s24 offen offset:0 lds
v_mfma_f32_16x16x32_bf16 v[176:179], a[172:175], a[136:139], v[176:179]
ds_read_b64_tr_b16 a[40:41], v48 offset:18432
ds_read_b64_tr_b16 a[42:43], v48 offset:18944
v_mfma_f32_16x16x32_bf16 v[176:179], a[28:31], a[8:11], v[176:179]
v_mfma_f32_16x16x32_bf16 v[216:219], a[136:139], a[196:199], v[216:219]
ds_read_b64_tr_b16 a[168:169], v48 offset:0
ds_read_b64_tr_b16 a[170:171], v48 offset:512
v_mfma_f32_16x16x32_bf16 v[216:219], a[8:11], a[36:39], v[216:219]
s_waitcnt lgkmcnt(10)
v_mfma_f32_16x16x32_bf16 v[136:139], a[164:167], a[136:139], v[136:139]
buffer_load_dwordx4 v37, s[12:15], s24 offen offset:1024 lds
v_mfma_f32_16x16x32_bf16 v[136:139], a[20:23], a[8:11], v[136:139]
ds_read_b64_tr_b16 a[24:25], v48 offset:14336
ds_read_b64_tr_b16 a[26:27], v48 offset:14848
v_mfma_f32_16x16x32_bf16 v[184:187], a[172:175], a[140:143], v[184:187]
v_mfma_f32_16x16x32_bf16 v[184:187], a[28:31], a[12:15], v[184:187]
s_cmp_eq_u32 s39, 1
s_mov_b32 s28, 49152
s_cselect_b32 s28, 98304, s28
v_mfma_f32_16x16x32_bf16 v[224:227], a[140:143], a[196:199], v[224:227]
v_add_u32 v43, 49152, v43
v_add_u32 v44, 49152, v44
v_mfma_f32_16x16x32_bf16 v[224:227], a[12:15], a[36:39], v[224:227]
buffer_load_dwordx4 v38, s[12:15], s24 offen offset:2048 lds
v_mfma_f32_16x16x32_bf16 v[144:147], a[164:167], a[140:143], v[144:147]
v_add_u32 v47, 98304, v47
v_add_u32 v48, 98304, v48
v_mfma_f32_16x16x32_bf16 v[144:147], a[20:23], a[12:15], v[144:147]
v_add_u32 v45, s28, v45
v_add_u32 v46, s28, v46
v_mfma_f32_16x16x32_bf16 v[192:195], a[172:175], a[160:163], v[192:195]
v_mfma_f32_16x16x32_bf16 v[192:195], a[28:31], a[16:19], v[192:195]
v_mfma_f32_16x16x32_bf16 v[232:235], a[160:163], a[196:199], v[232:235]
buffer_load_dwordx4 v39, s[12:15], s24 offen offset:3072 lds
v_mfma_f32_16x16x32_bf16 v[232:235], a[16:19], a[36:39], v[232:235]
s_add_u32 s24, s24, 128
v_mfma_f32_16x16x32_bf16 v[152:155], a[164:167], a[160:163], v[152:155]
v_mfma_f32_16x16x32_bf16 v[152:155], a[20:23], a[16:19], v[152:155]
s_waitcnt vmcnt(16) lgkmcnt(0)
s_barrier
v_mfma_f32_16x16x32_bf16 v[164:167], a[192:195], a[128:131], v[164:167]
ds_read_b64_tr_b16 a[172:173], v47 offset:2048
ds_read_b64_tr_b16 a[174:175], v47 offset:2560
v_mfma_f32_16x16x32_bf16 v[164:167], a[32:35], a[0:3], v[164:167]
s_add_u32 m0, s48, 32768
v_mfma_f32_16x16x32_bf16 v[204:207], a[128:131], a[200:203], v[204:207]
buffer_load_dwordx4 v40, s[16:19], s26 offen offset:0 lds
v_mfma_f32_16x16x32_bf16 v[204:207], a[0:3], a[40:43], v[204:207]
ds_read_b128 a[128:131], v45
v_mfma_f32_16x16x32_bf16 v[172:175], a[192:195], a[132:135], v[172:175]
ds_read_b128 a[0:3], v46
v_mfma_f32_16x16x32_bf16 v[172:175], a[32:35], a[4:7], v[172:175]
ds_read_b64_tr_b16 a[28:29], v47 offset:16384
ds_read_b64_tr_b16 a[30:31], v47 offset:16896
v_mfma_f32_16x16x32_bf16 v[212:215], a[132:135], a[200:203], v[212:215]
v_mfma_f32_16x16x32_bf16 v[212:215], a[4:7], a[40:43], v[212:215]
buffer_load_dwordx4 v41, s[16:19], s26 offen offset:1024 lds
v_mfma_f32_16x16x32_bf16 v[132:135], a[168:171], a[132:135], v[132:135]
ds_read_b64_tr_b16 a[196:197], v47 offset:4096
ds_read_b64_tr_b16 a[198:199], v47 offset:4608
v_mfma_f32_16x16x32_bf16 v[132:135], a[24:27], a[4:7], v[132:135]
ds_read_b128 a[132:135], v43 offset:0
v_mfma_f32_16x16x32_bf16 v[180:183], a[192:195], a[136:139], v[180:183]
ds_read_b128 a[4:7], v44 offset:0
v_mfma_f32_16x16x32_bf16 v[180:183], a[32:35], a[8:11], v[180:183]
ds_read_b64_tr_b16 a[36:37], v47 offset:18432
ds_read_b64_tr_b16 a[38:39], v47 offset:18944
v_mfma_f32_16x16x32_bf16 v[220:223], a[136:139], a[200:203], v[220:223]
buffer_load_dwordx4 v40, s[16:19], s26 offen offset:2048 lds
v_mfma_f32_16x16x32_bf16 v[220:223], a[8:11], a[40:43], v[220:223]
ds_read_b64_tr_b16 a[164:165], v47 offset:0
ds_read_b64_tr_b16 a[166:167], v47 offset:512
v_mfma_f32_16x16x32_bf16 v[140:143], a[168:171], a[136:139], v[140:143]
v_mfma_f32_16x16x32_bf16 v[140:143], a[24:27], a[8:11], v[140:143]
ds_read_b128 a[136:139], v43 offset:2048
v_mfma_f32_16x16x32_bf16 v[188:191], a[192:195], a[140:143], v[188:191]
ds_read_b128 a[8:11], v44 offset:2048
v_mfma_f32_16x16x32_bf16 v[188:191], a[32:35], a[12:15], v[188:191]
buffer_load_dwordx4 v41, s[16:19], s26 offen offset:3072 lds
v_mfma_f32_16x16x32_bf16 v[228:231], a[140:143], a[200:203], v[228:231]
ds_read_b64_tr_b16 a[20:21], v47 offset:14336
ds_read_b64_tr_b16 a[22:23], v47 offset:14848
v_mfma_f32_16x16x32_bf16 v[228:231], a[12:15], a[40:43], v[228:231]
v_mfma_f32_16x16x32_bf16 v[148:151], a[168:171], a[140:143], v[148:151]
v_mfma_f32_16x16x32_bf16 v[148:151], a[24:27], a[12:15], v[148:151]
s_add_u32 m0, s48, 36864
ds_read_b128 a[140:143], v43 offset:4096
v_mfma_f32_16x16x32_bf16 v[196:199], a[192:195], a[160:163], v[196:199]
buffer_load_dwordx4 v40, s[16:19], s47 offen offset:0 lds
v_mfma_f32_16x16x32_bf16 v[196:199], a[32:35], a[16:19], v[196:199]
ds_read_b128 a[12:15], v44 offset:4096
.Lpad17:
s_cbranch_execz .Ltch8
v_mfma_f32_16x16x32_bf16 v[236:239], a[160:163], a[200:203], v[236:239]
v_mfma_f32_16x16x32_bf16 v[236:239], a[16:19], a[40:43], v[236:239]
s_waitcnt lgkmcnt(2)
v_mfma_f32_16x16x32_bf16 v[156:159], a[168:171], a[160:163], v[156:159]
buffer_load_dwordx4 v41, s[16:19], s47 offen offset:1024 lds
v_mfma_f32_16x16x32_bf16 v[156:159], a[24:27], a[16:19], v[156:159]
ds_read_b128 a[160:163], v43 offset:6144
ds_read_b128 a[16:19], v44 offset:6144
v_mfma_f32_16x16x32_bf16 v[160:163], a[172:175], a[128:131], v[160:163]
ds_read_b64_tr_b16 a[192:193], v48 offset:2048
ds_read_b64_tr_b16 a[194:195], v48 offset:2560
v_mfma_f32_16x16x32_bf16 v[160:163], a[28:31], a[0:3], v[160:163]
v_and_b32 v28, 15, v1
v_lshrrev_b32 v29, 4, v1
v_mfma_f32_16x16x32_bf16 v[200:203], a[128:131], a[196:199], v[200:203]
ds_read_b64_tr_b16 a[32:33], v48 offset:16384
ds_read_b64_tr_b16 a[34:35], v48 offset:16896
v_mfma_f32_16x16x32_bf16 v[200:203], a[0:3], a[36:39], v[200:203]
buffer_load_dwordx4 v42, s[12:15], s25 offen offset:2048 lds
v_lshlrev_b32 v29, 2, v29
v_mfma_f32_16x16x32_bf16 v[168:171], a[172:175], a[132:135], v[168:171]
s_add_u32 s26, s26, 393216
s_add_u32 s47, s47, 393216
v_sub_u32 v28, v28, v29
v_mfma_f32_16x16x32_bf16 v[168:171], a[28:31], a[4:7], v[168:171]
s_add_u32 s25, s25, 128
v_mov_b32 v30, 0
v_add_u32 v29, 16, v28
v_mfma_f32_16x16x32_bf16 v[208:211], a[132:135], a[196:199], v[208:211]
ds_read_b64_tr_b16 a[200:201], v48 offset:4096
ds_read_b64_tr_b16 a[202:203], v48 offset:4608
v_mfma_f32_16x16x32_bf16 v[208:211], a[4:7], a[36:39], v[208:211]
v_and_b32 v29, 0xfffffff1, v29
v_cmp_eq_u32 vcc, 0, v29
v_mfma_f32_16x16x32_bf16 v[128:131], a[164:167], a[132:135], v[128:131]
s_add_u32 m0, s91, 16384
v_nop
v_nop
v_mfma_f32_16x16x32_bf16 v[128:131], a[20:23], a[4:7], v[128:131]
buffer_load_dwordx4 v36, s[12:15], s24 offen offset:0 lds
v_cndmask_b32 v16, v34, v30, vcc
v_mfma_f32_16x16x32_bf16 v[176:179], a[172:175], a[136:139], v[176:179]
ds_read_b64_tr_b16 a[40:41], v48 offset:18432
ds_read_b64_tr_b16 a[42:43], v48 offset:18944
v_mfma_f32_16x16x32_bf16 v[176:179], a[28:31], a[8:11], v[176:179]
v_add_u32 v29, 15, v28
v_and_b32 v29, 0xfffffff1, v29
v_mfma_f32_16x16x32_bf16 v[216:219], a[136:139], a[196:199], v[216:219]
ds_read_b64_tr_b16 a[168:169], v48 offset:0
ds_read_b64_tr_b16 a[170:171], v48 offset:512
v_mfma_f32_16x16x32_bf16 v[216:219], a[8:11], a[36:39], v[216:219]
s_waitcnt lgkmcnt(10)
v_cmp_eq_u32 vcc, 0, v29
v_nop
v_mfma_f32_16x16x32_bf16 v[136:139], a[164:167], a[136:139], v[136:139]
buffer_load_dwordx4 v37, s[12:15], s24 offen offset:1024 lds
v_nop
v_mfma_f32_16x16x32_bf16 v[136:139], a[20:23], a[8:11], v[136:139]
ds_read_b64_tr_b16 a[24:25], v48 offset:14336
ds_read_b64_tr_b16 a[26:27], v48 offset:14848
v_mfma_f32_16x16x32_bf16 v[184:187], a[172:175], a[140:143], v[184:187]
v_cndmask_b32 v17, v34, v30, vcc
v_add_u32 v29, 14, v28
v_mfma_f32_16x16x32_bf16 v[184:187], a[28:31], a[12:15], v[184:187]
s_cmp_eq_u32 s39, 1
s_mov_b32 s28, 16384
s_cselect_b32 s28, 4294934528, s28
v_mfma_f32_16x16x32_bf16 v[224:227], a[140:143], a[196:199], v[224:227]
v_add_u32 v43, 16384, v43
v_add_u32 v44, 16384, v44
v_mfma_f32_16x16x32_bf16 v[224:227], a[12:15], a[36:39], v[224:227]
buffer_load_dwordx4 v38, s[12:15], s24 offen offset:2048 lds
v_and_b32 v29, 0xfffffff1, v29
v_mfma_f32_16x16x32_bf16 v[144:147], a[164:167], a[140:143], v[144:147]
v_add_u32 v47, 4294934528, v47
v_add_u32 v48, 4294934528, v48
v_mfma_f32_16x16x32_bf16 v[144:147], a[20:23], a[12:15], v[144:147]
v_add_u32 v45, s28, v45
v_add_u32 v46, s28, v46
v_mfma_f32_16x16x32_bf16 v[192:195], a[172:175], a[160:163], v[192:195]
v_cmp_eq_u32 vcc, 0, v29
v_nop
v_mfma_f32_16x16x32_bf16 v[192:195], a[28:31], a[16:19], v[192:195]
v_nop
v_cndmask_b32 v18, v34, v30, vcc
v_mfma_f32_16x16x32_bf16 v[232:235], a[160:163], a[196:199], v[232:235]
buffer_load_dwordx4 v39, s[12:15], s24 offen offset:3072 lds
v_add_u32 v29, 13, v28
v_mfma_f32_16x16x32_bf16 v[232:235], a[16:19], a[36:39], v[232:235]
s_add_u32 s24, s24, 128
v_and_b32 v29, 0xfffffff1, v29
v_cmp_eq_u32 vcc, 0, v29
v_mfma_f32_16x16x32_bf16 v[152:155], a[164:167], a[160:163], v[152:155]
buffer_load_dword v250, v249, s[60:63], s90 offen sc1
v_nop
v_nop
v_mfma_f32_16x16x32_bf16 v[152:155], a[20:23], a[16:19], v[152:155]
s_waitcnt vmcnt(16) lgkmcnt(0)
s_barrier
v_cndmask_b32 v19, v34, v30, vcc
v_mfma_f32_16x16x32_bf16 v[164:167], a[192:195], a[128:131], v[164:167]
ds_read_b64_tr_b16 a[172:173], v47 offset:2048
ds_read_b64_tr_b16 a[174:175], v47 offset:2560
v_mfma_f32_16x16x32_bf16 v[164:167], a[32:35], a[0:3], v[164:167]
s_add_u32 m0, s48, 131072
v_add_u32 v29, 0, v28
v_and_b32 v29, 0xfffffff1, v29
v_mfma_f32_16x16x32_bf16 v[204:207], a[128:131], a[200:203], v[204:207]
buffer_load_dwordx4 v40, s[16:19], s26 offen offset:0 lds
v_cmp_eq_u32 vcc, 0, v29
v_mfma_f32_16x16x32_bf16 v[204:207], a[0:3], a[40:43], v[204:207]
ds_read_b128 a[128:131], v45
v_nop
v_mfma_f32_16x16x32_bf16 v[172:175], a[192:195], a[132:135], v[172:175]
ds_read_b128 a[0:3], v46
v_nop
v_mfma_f32_16x16x32_bf16 v[172:175], a[32:35], a[4:7], v[172:175]
ds_read_b64_tr_b16 a[28:29], v47 offset:16384
ds_read_b64_tr_b16 a[30:31], v47 offset:16896
v_mfma_f32_16x16x32_bf16 v[212:215], a[132:135], a[200:203], v[212:215]
v_cndmask_b32 v20, v34, v30, vcc
v_add_u32 v29, -1, v28
v_mfma_f32_16x16x32_bf16 v[212:215], a[4:7], a[40:43], v[212:215]
buffer_load_dwordx4 v41, s[16:19], s26 offen offset:1024 lds
v_and_b32 v29, 0xfffffff1, v29
v_mfma_f32_16x16x32_bf16 v[132:135], a[168:171], a[132:135], v[132:135]
ds_read_b64_tr_b16 a[196:197], v47 offset:4096
ds_read_b64_tr_b16 a[198:199], v47 offset:4608
v_mfma_f32_16x16x32_bf16 v[132:135], a[24:27], a[4:7], v[132:135]
ds_read_b128 a[132:135], v43 offset:0
v_cmp_eq_u32 vcc, 0, v29
v_mfma_f32_16x16x32_bf16 v[180:183], a[192:195], a[136:139], v[180:183]
ds_read_b128 a[4:7], v44 offset:0
v_nop
v_mfma_f32_16x16x32_bf16 v[180:183], a[32:35], a[8:11], v[180:183]
ds_read_b64_tr_b16 a[36:37], v47 offset:18432
ds_read_b64_tr_b16 a[38:39], v47 offset:18944
v_mfma_f32_16x16x32_bf16 v[220:223], a[136:139], a[200:203], v[220:223]
buffer_load_dwordx4 v40, s[16:19], s26 offen offset:2048 lds
v_nop
v_mfma_f32_16x16x32_bf16 v[220:223], a[8:11], a[40:43], v[220:223]
ds_read_b64_tr_b16 a[164:165], v47 offset:0
ds_read_b64_tr_b16 a[166:167], v47 offset:512
v_mfma_f32_16x16x32_bf16 v[140:143], a[168:171], a[136:139], v[140:143]
v_cndmask_b32 v21, v34, v30, vcc
v_add_u32 v29, -2, v28
v_mfma_f32_16x16x32_bf16 v[140:143], a[24:27], a[8:11], v[140:143]
ds_read_b128 a[136:139], v43 offset:2048
v_and_b32 v29, 0xfffffff1, v29
v_mfma_f32_16x16x32_bf16 v[188:191], a[192:195], a[140:143], v[188:191]
ds_read_b128 a[8:11], v44 offset:2048
v_cmp_eq_u32 vcc, 0, v29
v_mfma_f32_16x16x32_bf16 v[188:191], a[32:35], a[12:15], v[188:191]
buffer_load_dwordx4 v41, s[16:19], s26 offen offset:3072 lds
v_nop
v_mfma_f32_16x16x32_bf16 v[228:231], a[140:143], a[200:203], v[228:231]
ds_read_b64_tr_b16 a[20:21], v47 offset:14336
ds_read_b64_tr_b16 a[22:23], v47 offset:14848
v_mfma_f32_16x16x32_bf16 v[228:231], a[12:15], a[40:43], v[228:231]
v_nop
v_cndmask_b32 v22, v34, v30, vcc
v_mfma_f32_16x16x32_bf16 v[148:151], a[168:171], a[140:143], v[148:151]
v_add_u32 v29, -3, v28
v_and_b32 v29, 0xfffffff1, v29
v_mfma_f32_16x16x32_bf16 v[148:151], a[24:27], a[12:15], v[148:151]
s_add_u32 m0, s48, 135168
ds_read_b128 a[140:143], v43 offset:4096
v_mfma_f32_16x16x32_bf16 v[196:199], a[192:195], a[160:163], v[196:199]
buffer_load_dwordx4 v40, s[16:19], s47 offen offset:0 lds
v_cmp_eq_u32 vcc, 0, v29
v_mfma_f32_16x16x32_bf16 v[196:199], a[32:35], a[16:19], v[196:199]
ds_read_b128 a[12:15], v44 offset:4096
v_nop
v_mfma_f32_16x16x32_bf16 v[236:239], a[160:163], a[200:203], v[236:239]
v_nop
v_cndmask_b32 v23, v34, v30, vcc
v_mfma_f32_16x16x32_bf16 v[236:239], a[16:19], a[40:43], v[236:239]
s_waitcnt lgkmcnt(2)
s_lshr_b32 s29, s3, 1
s_or_b32 s29, s29, s36
v_mfma_f32_16x16x32_bf16 v[156:159], a[168:171], a[160:163], v[156:159]
buffer_load_dwordx4 v41, s[16:19], s47 offen offset:1024 lds
v_mfma_f32_16x16x32_bf16 v[156:159], a[24:27], a[16:19], v[156:159]
ds_read_b128 a[160:163], v43 offset:6144
ds_read_b128 a[16:19], v44 offset:6144
v_mfma_f32_16x16x32_bf16 v[160:163], a[172:175], a[128:131], v[160:163]
ds_read_b64_tr_b16 a[192:193], v48 offset:2048
ds_read_b64_tr_b16 a[194:195], v48 offset:2560
v_mfma_f32_16x16x32_bf16 v[160:163], a[28:31], a[0:3], v[160:163]
s_cmp_eq_u32 s29, 0
s_cselect_b64 s[32:33], -1, 0
v_mfma_f32_16x16x32_bf16 v[200:203], a[128:131], a[196:199], v[200:203]
ds_read_b64_tr_b16 a[32:33], v48 offset:16384
ds_read_b64_tr_b16 a[34:35], v48 offset:16896
v_mfma_f32_16x16x32_bf16 v[200:203], a[0:3], a[36:39], v[200:203]
buffer_load_dwordx4 v42, s[12:15], s25 offen offset:2048 lds
v_cndmask_b32 v24, v16, v34, s[32:33]
v_mfma_f32_16x16x32_bf16 v[168:171], a[172:175], a[132:135], v[168:171]
s_add_u32 s26, s26, 393216
s_add_u32 s47, s47, 393216
v_cndmask_b32 v25, v17, v34, s[32:33]
v_mfma_f32_16x16x32_bf16 v[168:171], a[28:31], a[4:7], v[168:171]
s_add_u32 s25, s25, 128
v_cndmask_b32 v26, v18, v34, s[32:33]
v_cndmask_b32 v27, v19, v34, s[32:33]
v_mfma_f32_16x16x32_bf16 v[208:211], a[132:135], a[196:199], v[208:211]
ds_read_b64_tr_b16 a[200:201], v48 offset:4096
ds_read_b64_tr_b16 a[202:203], v48 offset:4608
v_mfma_f32_16x16x32_bf16 v[208:211], a[4:7], a[36:39], v[208:211]
v_mfma_f32_16x16x32_bf16 v[128:131], a[164:167], a[132:135], v[128:131]
ds_read_b64_tr_b16 a[40:41], v48 offset:18432
ds_read_b64_tr_b16 a[42:43], v48 offset:18944
v_mfma_f32_16x16x32_bf16 v[128:131], a[20:23], a[4:7], v[128:131]
v_mfma_f32_16x16x32_bf16 v[176:179], a[172:175], a[136:139], v[176:179]
ds_read_b64_tr_b16 a[168:169], v48 offset:0
ds_read_b64_tr_b16 a[170:171], v48 offset:512
v_mfma_f32_16x16x32_bf16 v[176:179], a[28:31], a[8:11], v[176:179]
v_mfma_f32_16x16x32_bf16 v[216:219], a[136:139], a[196:199], v[216:219]
ds_read_b64_tr_b16 a[24:25], v48 offset:14336
ds_read_b64_tr_b16 a[26:27], v48 offset:14848
v_mfma_f32_16x16x32_bf16 v[216:219], a[8:11], a[36:39], v[216:219]
s_waitcnt lgkmcnt(12)
v_mfma_f32_16x16x32_bf16 v[136:139], a[164:167], a[136:139], v[136:139]
s_cmp_eq_u32 s39, 1
s_mov_b32 s28, 4294885376
s_cselect_b32 s28, 4294901760, s28
v_mfma_f32_16x16x32_bf16 v[136:139], a[20:23], a[8:11], v[136:139]
v_add_u32 v43, 4294885376, v43
v_add_u32 v44, 4294885376, v44
v_mfma_f32_16x16x32_bf16 v[184:187], a[172:175], a[140:143], v[184:187]
v_add_u32 v47, 4294901760, v47
v_add_u32 v48, 4294901760, v48
v_mfma_f32_16x16x32_bf16 v[184:187], a[28:31], a[12:15], v[184:187]
v_add_u32 v45, s28, v45
v_add_u32 v46, s28, v46
v_mfma_f32_16x16x32_bf16 v[224:227], a[140:143], a[196:199], v[224:227]
v_mfma_f32_16x16x32_bf16 v[224:227], a[12:15], a[36:39], v[224:227]
v_mfma_f32_16x16x32_bf16 v[144:147], a[164:167], a[140:143], v[144:147]
s_waitcnt vmcnt(7)
v_and_b32 v250, 7, v250
v_mfma_f32_16x16x32_bf16 v[144:147], a[20:23], a[12:15], v[144:147]
v_mfma_f32_16x16x32_bf16 v[192:195], a[172:175], a[160:163], v[192:195]
v_cmp_gt_u32 vcc, 3, v250
v_mfma_f32_16x16x32_bf16 v[192:195], a[28:31], a[16:19], v[192:195]
v_mfma_f32_16x16x32_bf16 v[232:235], a[160:163], a[196:199], v[232:235]
s_cmp_lg_u64 vcc, 0
s_cbranch_scc1 .Lspin3
.Lspin3_ret:
v_mfma_f32_16x16x32_bf16 v[232:235], a[16:19], a[36:39], v[232:235]
v_mfma_f32_16x16x32_bf16 v[152:155], a[164:167], a[160:163], v[152:155]
s_lshl_b32 s54, s51, 19
s_and_b32 s56, s3, 1
s_lshl_b32 s56, s56, 18
v_mfma_f32_16x16x32_bf16 v[152:155], a[20:23], a[16:19], v[152:155]
s_waitcnt vmcnt(12) lgkmcnt(0)
s_barrier
s_add_u32 s54, s54, s56
v_mfma_f32_16x16x32_bf16 v[164:167], a[192:195], a[128:131], v[164:167]
ds_read_b64_tr_b16 a[172:173], v47 offset:2048
ds_read_b64_tr_b16 a[174:175], v47 offset:2560
v_mfma_f32_16x16x32_bf16 v[164:167], a[32:35], a[0:3], v[164:167]
s_add_u32 m0, s91, 114688
s_add_u32 s54, s54, s52
s_lshr_b32 s56, s3, 1
v_mfma_f32_16x16x32_bf16 v[204:207], a[128:131], a[200:203], v[204:207]
buffer_load_dwordx4 v240, s[8:11], s45 offen offset:0 lds
ds_read_b64_tr_b16 a[28:29], v47 offset:16384
ds_read_b64_tr_b16 a[30:31], v47 offset:16896
v_mfma_f32_16x16x32_bf16 v[204:207], a[0:3], a[40:43], v[204:207]
s_lshl_b32 s56, s56, 7
ds_read_b128 a[128:131], v45
v_mfma_f32_16x16x32_bf16 v[172:175], a[192:195], a[132:135], v[172:175]
s_add_u32 s54, s54, s56
ds_read_b128 a[0:3], v46
v_mfma_f32_16x16x32_bf16 v[172:175], a[32:35], a[4:7], v[172:175]
buffer_load_dwordx4 v241, s[8:11], s45 offen offset:1024 lds
ds_read_b64_tr_b16 a[196:197], v47 offset:4096
ds_read_b64_tr_b16 a[198:199], v47 offset:4608
v_mfma_f32_16x16x32_bf16 v[212:215], a[132:135], a[200:203], v[212:215]
s_xor_b32 s56, s55, 1
s_lshl_b32 s56, s56, 11
s_add_u32 s54, s54, s56
v_mfma_f32_16x16x32_bf16 v[212:215], a[4:7], a[40:43], v[212:215]
ds_read_b64_tr_b16 a[36:37], v47 offset:18432
ds_read_b64_tr_b16 a[38:39], v47 offset:18944
v_mfma_f32_16x16x32_bf16 v[132:135], a[168:171], a[132:135], v[132:135]
s_sub_u32 s18, 0x800000, s54
buffer_load_dwordx4 v242, s[8:11], s45 offen offset:2048 lds
v_mfma_f32_16x16x32_bf16 v[132:135], a[24:27], a[4:7], v[132:135]
ds_read_b128 a[132:135], v43 offset:0
v_mfma_f32_16x16x32_bf16 v[180:183], a[192:195], a[136:139], v[180:183]
ds_read_b128 a[4:7], v44 offset:0
v_mfma_f32_16x16x32_bf16 v[180:183], a[32:35], a[8:11], v[180:183]
buffer_load_dwordx4 v243, s[8:11], s45 offen offset:3072 lds
ds_read_b64_tr_b16 a[164:165], v47 offset:0
ds_read_b64_tr_b16 a[166:167], v47 offset:512
v_mfma_f32_16x16x32_bf16 v[220:223], a[136:139], a[200:203], v[220:223]
s_add_u32 s45, s45, 131072
s_add_u32 s16, s82, s54
s_addc_u32 s17, s83, 0
v_mfma_f32_16x16x32_bf16 v[220:223], a[8:11], a[40:43], v[220:223]
s_add_u32 m0, s91, 98304
ds_read_b64_tr_b16 a[20:21], v47 offset:14336
ds_read_b64_tr_b16 a[22:23], v47 offset:14848
v_mfma_f32_16x16x32_bf16 v[140:143], a[168:171], a[136:139], v[140:143]
s_and_b32 s17, s17, 0xffff
buffer_load_dwordx4 v7, s[4:7], s44 offen offset:0 sc1 lds
v_mfma_f32_16x16x32_bf16 v[140:143], a[24:27], a[8:11], v[140:143]
s_lshr_b32 s54, s42, 1
ds_read_b128 a[136:139], v43 offset:2048
v_mfma_f32_16x16x32_bf16 v[188:191], a[192:195], a[140:143], v[188:191]
s_lshl_b32 s54, s54, 5
ds_read_b128 a[8:11], v44 offset:2048
v_mfma_f32_16x16x32_bf16 v[188:191], a[32:35], a[12:15], v[188:191]
s_add_u32 s54, s54, s87
buffer_load_dwordx4 v7, s[4:7], s44 offen offset:1024 sc1 lds
v_mfma_f32_16x16x32_bf16 v[228:231], a[140:143], a[200:203], v[228:231]
s_lshl_b32 s54, s54, 16
s_lshl_b32 s56, s3, 14
s_add_u32 s54, s54, s56
v_mfma_f32_16x16x32_bf16 v[228:231], a[12:15], a[40:43], v[228:231]
s_add_u32 s54, s54, 54525952
s_add_u32 s12, s84, s54
s_addc_u32 s13, s85, 0
v_mfma_f32_16x16x32_bf16 v[148:151], a[168:171], a[140:143], v[148:151]
s_and_b32 s13, s13, 0xffff
buffer_load_dwordx4 v7, s[4:7], s44 offen offset:2048 sc1 lds
v_mfma_f32_16x16x32_bf16 v[148:151], a[24:27], a[12:15], v[148:151]
s_mov_b32 s14, 16384
ds_read_b128 a[140:143], v43 offset:4096
v_mfma_f32_16x16x32_bf16 v[196:199], a[192:195], a[160:163], v[196:199]
s_mov_b32 s56, 131072
ds_read_b128 a[12:15], v44 offset:4096
v_mfma_f32_16x16x32_bf16 v[196:199], a[32:35], a[16:19], v[196:199]
s_mov_b32 s57, -32768
buffer_load_dwordx4 v7, s[4:7], s44 offen offset:3072 sc1 lds
v_mfma_f32_16x16x32_bf16 v[236:239], a[160:163], a[200:203], v[236:239]
s_add_u32 s44, s44, 4096
s_mov_b32 s46, 98304
s_lshr_b32 s30, s51, 3
v_mfma_f32_16x16x32_bf16 v[236:239], a[16:19], a[40:43], v[236:239]
s_waitcnt lgkmcnt(2)
s_and_b32 s31, s51, 7
s_lshl_b32 s31, s31, 3
v_mfma_f32_16x16x32_bf16 v[156:159], a[168:171], a[160:163], v[156:159]
s_add_u32 m0, s91, 81920
s_or_b32 s30, s30, s31
s_mov_b32 s31, s38
v_mfma_f32_16x16x32_bf16 v[156:159], a[24:27], a[16:19], v[156:159]
buffer_load_dwordx4 v240, s[8:11], s45 offen offset:0 lds
ds_read_b128 a[160:163], v43 offset:6144
ds_read_b128 a[16:19], v44 offset:6144
v_mfma_f32_16x16x32_bf16 v[160:163], a[172:175], a[128:131], v[160:163]
ds_read_b64_tr_b16 a[192:193], v48 offset:2048
ds_read_b64_tr_b16 a[194:195], v48 offset:2560
v_mfma_f32_16x16x32_bf16 v[160:163], a[28:31], a[0:3], v[160:163]
s_lshl_b32 s34, s36, 18
s_lshr_b32 s35, s3, 1
s_lshl_b32 s35, s35, 17
v_mfma_f32_16x16x32_bf16 v[200:203], a[128:131], a[196:199], v[200:203]
buffer_load_dwordx4 v241, s[8:11], s45 offen offset:1024 lds
ds_read_b64_tr_b16 a[32:33], v48 offset:16384
ds_read_b64_tr_b16 a[34:35], v48 offset:16896
v_mfma_f32_16x16x32_bf16 v[200:203], a[0:3], a[36:39], v[200:203]
s_add_u32 s34, s34, s35
s_lshl_b32 s35, s37, 12
s_add_u32 s34, s34, s35
v_mfma_f32_16x16x32_bf16 v[168:171], a[172:175], a[132:135], v[168:171]
ds_read_b64_tr_b16 a[200:201], v48 offset:4096
ds_read_b64_tr_b16 a[202:203], v48 offset:4608
v_mfma_f32_16x16x32_bf16 v[168:171], a[28:31], a[4:7], v[168:171]
s_and_b32 s35, s3, 1
s_lshl_b32 s35, s35, 11
s_add_u32 s34, s34, s35
buffer_load_dwordx4 v242, s[8:11], s45 offen offset:2048 lds
v_mfma_f32_16x16x32_bf16 v[208:211], a[132:135], a[196:199], v[208:211]
ds_read_b64_tr_b16 a[40:41], v48 offset:18432
ds_read_b64_tr_b16 a[42:43], v48 offset:18944
v_mfma_f32_16x16x32_bf16 v[208:211], a[4:7], a[36:39], v[208:211]
s_add_u32 s34, s34, 50331648
s_add_u32 s20, s84, s34
s_addc_u32 s21, s85, 0
v_mfma_f32_16x16x32_bf16 v[128:131], a[164:167], a[132:135], v[128:131]
buffer_load_dwordx4 v243, s[8:11], s45 offen offset:3072 lds
ds_read_b64_tr_b16 a[168:169], v48 offset:0
ds_read_b64_tr_b16 a[170:171], v48 offset:512
v_mfma_f32_16x16x32_bf16 v[128:131], a[20:23], a[4:7], v[128:131]
s_and_b32 s21, s21, 0xffff
s_mov_b32 s22, 131072
s_mov_b32 s40, 0
s_add_u32 s45, s45, 131072
v_mfma_f32_16x16x32_bf16 v[176:179], a[172:175], a[136:139], v[176:179]
s_add_u32 m0, s91, 65536
ds_read_b64_tr_b16 a[24:25], v48 offset:14336
ds_read_b64_tr_b16 a[26:27], v48 offset:14848
v_mfma_f32_16x16x32_bf16 v[176:179], a[28:31], a[8:11], v[176:179]
s_mov_b32 s41, 65536
buffer_load_dwordx4 v7, s[4:7], s44 offen offset:0 sc1 lds
v_mfma_f32_16x16x32_bf16 v[216:219], a[136:139], a[196:199], v[216:219]
s_cmp_eq_u32 s39, 1
s_mov_b32 s28, 16384
s_cselect_b32 s28, 98304, s28
v_mfma_f32_16x16x32_bf16 v[216:219], a[8:11], a[36:39], v[216:219]
s_waitcnt lgkmcnt(12)
v_add_u32 v43, 16384, v43
v_add_u32 v44, 16384, v44
v_mfma_f32_16x16x32_bf16 v[136:139], a[164:167], a[136:139], v[136:139]
buffer_load_dwordx4 v7, s[4:7], s44 offen offset:1024 sc1 lds
v_add_u32 v47, 98304, v47
v_add_u32 v48, 98304, v48
v_mfma_f32_16x16x32_bf16 v[136:139], a[20:23], a[8:11], v[136:139]
v_add_u32 v45, s28, v45
v_add_u32 v46, s28, v46
v_mfma_f32_16x16x32_bf16 v[184:187], a[172:175], a[140:143], v[184:187]
s_sub_u32 s50, s43, 14
s_sub_u32 s54, 48, s43
v_mfma_f32_16x16x32_bf16 v[184:187], a[28:31], a[12:15], v[184:187]
buffer_load_dwordx4 v7, s[4:7], s44 offen offset:2048 sc1 lds
v_mfma_f32_16x16x32_bf16 v[224:227], a[140:143], a[196:199], v[224:227]
s_cmp_eq_u32 s55, 0
s_cselect_b64 s[58:59], -1, 0
s_cselect_b32 s50, s50, s54
v_mfma_f32_16x16x32_bf16 v[224:227], a[12:15], a[36:39], v[224:227]
v_and_b32 v252, 7, v1
v_bfe_u32 v253, v1, 3, 1
v_mfma_f32_16x16x32_bf16 v[144:147], a[164:167], a[140:143], v[144:147]
buffer_load_dwordx4 v7, s[4:7], s44 offen offset:3072 sc1 lds
v_bfe_u32 v254, v1, 4, 1
v_mfma_f32_16x16x32_bf16 v[144:147], a[20:23], a[12:15], v[144:147]
s_add_u32 s44, s44, 4096
v_lshrrev_b32 v255, 5, v1
v_lshlrev_b32 v252, 12, v252
v_mfma_f32_16x16x32_bf16 v[192:195], a[172:175], a[160:163], v[192:195]
v_lshl_add_u32 v252, v253, 6, v252
v_lshl_add_u32 v252, v254, 5, v252
v_mfma_f32_16x16x32_bf16 v[192:195], a[28:31], a[16:19], v[192:195]
v_lshl_add_u32 v251, v255, 4, v252
v_bfe_u32 v252, v1, 2, 2
v_mfma_f32_16x16x32_bf16 v[232:235], a[160:163], a[196:199], v[232:235]
v_and_b32 v253, 3, v1
v_lshlrev_b32 v252, 1, v252
v_mfma_f32_16x16x32_bf16 v[232:235], a[16:19], a[36:39], v[232:235]
v_lshl_or_b32 v252, v253, 6, v252
v_or_b32 v252, s30, v252
v_mfma_f32_16x16x32_bf16 v[152:155], a[164:167], a[160:163], v[152:155]
v_mov_b32 v253, s31
v_cndmask_b32 v252, v252, v253, s[58:59]
v_mfma_f32_16x16x32_bf16 v[152:155], a[20:23], a[16:19], v[152:155]
s_waitcnt vmcnt(16) lgkmcnt(0)
s_barrier
v_lshlrev_b32 v249, 5, v252
v_mfma_f32_16x16x32_bf16 v[164:167], a[192:195], a[128:131], v[164:167]
ds_read_b64_tr_b16 a[172:173], v47 offset:2048
ds_read_b64_tr_b16 a[174:175], v47 offset:2560
v_mfma_f32_16x16x32_bf16 v[164:167], a[32:35], a[0:3], v[164:167]
s_add_u32 m0, s91, 49152
v_mfma_f32_16x16x32_bf16 v[204:207], a[128:131], a[200:203], v[204:207]
buffer_load_dwordx4 v240, s[8:11], s45 offen offset:0 lds
ds_read_b64_tr_b16 a[28:29], v47 offset:16384
ds_read_b64_tr_b16 a[30:31], v47 offset:16896
v_mfma_f32_16x16x32_bf16 v[204:207], a[0:3], a[40:43], v[204:207]
ds_read_b128 a[128:131], v45
v_mfma_f32_16x16x32_bf16 v[172:175], a[192:195], a[132:135], v[172:175]
ds_read_b128 a[0:3], v46
v_mfma_f32_16x16x32_bf16 v[172:175], a[32:35], a[4:7], v[172:175]
ds_read_b64_tr_b16 a[196:197], v47 offset:4096
ds_read_b64_tr_b16 a[198:199], v47 offset:4608
v_mfma_f32_16x16x32_bf16 v[212:215], a[132:135], a[200:203], v[212:215]
v_mfma_f32_16x16x32_bf16 v[212:215], a[4:7], a[40:43], v[212:215]
buffer_load_dwordx4 v241, s[8:11], s45 offen offset:1024 lds
ds_read_b64_tr_b16 a[36:37], v47 offset:18432
ds_read_b64_tr_b16 a[38:39], v47 offset:18944
v_mfma_f32_16x16x32_bf16 v[132:135], a[168:171], a[132:135], v[132:135]
v_mfma_f32_16x16x32_bf16 v[132:135], a[24:27], a[4:7], v[132:135]
ds_read_b128 a[132:135], v43 offset:0
v_mfma_f32_16x16x32_bf16 v[180:183], a[192:195], a[136:139], v[180:183]
ds_read_b128 a[4:7], v44 offset:0
v_mfma_f32_16x16x32_bf16 v[180:183], a[32:35], a[8:11], v[180:183]
ds_read_b64_tr_b16 a[164:165], v47 offset:0
ds_read_b64_tr_b16 a[166:167], v47 offset:512
v_mfma_f32_16x16x32_bf16 v[220:223], a[136:139], a[200:203], v[220:223]
buffer_load_dwordx4 v242, s[8:11], s45 offen offset:2048 lds
v_mfma_f32_16x16x32_bf16 v[220:223], a[8:11], a[40:43], v[220:223]
ds_read_b64_tr_b16 a[20:21], v47 offset:14336
ds_read_b64_tr_b16 a[22:23], v47 offset:14848
v_mfma_f32_16x16x32_bf16 v[140:143], a[168:171], a[136:139], v[140:143]
v_mfma_f32_16x16x32_bf16 v[140:143], a[24:27], a[8:11], v[140:143]
ds_read_b128 a[136:139], v43 offset:2048
v_mfma_f32_16x16x32_bf16 v[188:191], a[192:195], a[140:143], v[188:191]
ds_read_b128 a[8:11], v44 offset:2048
v_mfma_f32_16x16x32_bf16 v[188:191], a[32:35], a[12:15], v[188:191]
buffer_load_dwordx4 v243, s[8:11], s45 offen offset:3072 lds
v_mfma_f32_16x16x32_bf16 v[228:231], a[140:143], a[200:203], v[228:231]
s_add_u32 s45, s45, 131072
v_mfma_f32_16x16x32_bf16 v[228:231], a[12:15], a[40:43], v[228:231]
v_mfma_f32_16x16x32_bf16 v[148:151], a[168:171], a[140:143], v[148:151]
v_mfma_f32_16x16x32_bf16 v[148:151], a[24:27], a[12:15], v[148:151]
s_add_u32 m0, s91, 32768
ds_read_b128 a[140:143], v43 offset:4096
v_mfma_f32_16x16x32_bf16 v[196:199], a[192:195], a[160:163], v[196:199]
buffer_load_dwordx4 v7, s[4:7], s44 offen offset:0 sc1 lds
ds_read_b128 a[12:15], v44 offset:4096
v_mfma_f32_16x16x32_bf16 v[196:199], a[32:35], a[16:19], v[196:199]
v_mfma_f32_16x16x32_bf16 v[236:239], a[160:163], a[200:203], v[236:239]
v_mfma_f32_16x16x32_bf16 v[236:239], a[16:19], a[40:43], v[236:239]
s_waitcnt lgkmcnt(2)
v_mfma_f32_16x16x32_bf16 v[156:159], a[168:171], a[160:163], v[156:159]
v_mfma_f32_16x16x32_bf16 v[156:159], a[24:27], a[16:19], v[156:159]
buffer_load_dwordx4 v7, s[4:7], s44 offen offset:1024 sc1 lds
ds_read_b128 a[160:163], v43 offset:6144
ds_read_b128 a[16:19], v44 offset:6144
v_mfma_f32_16x16x32_bf16 v[160:163], a[172:175], a[128:131], v[160:163]
ds_read_b64_tr_b16 a[192:193], v48 offset:2048
ds_read_b64_tr_b16 a[194:195], v48 offset:2560
v_mfma_f32_16x16x32_bf16 v[160:163], a[28:31], a[0:3], v[160:163]
v_mfma_f32_16x16x32_bf16 v[200:203], a[128:131], a[196:199], v[200:203]
ds_read_b64_tr_b16 a[32:33], v48 offset:16384
ds_read_b64_tr_b16 a[34:35], v48 offset:16896
v_mfma_f32_16x16x32_bf16 v[200:203], a[0:3], a[36:39], v[200:203]
v_mfma_f32_16x16x32_bf16 v[168:171], a[172:175], a[132:135], v[168:171]
buffer_load_dwordx4 v7, s[4:7], s44 offen offset:2048 sc1 lds
ds_read_b64_tr_b16 a[200:201], v48 offset:4096
ds_read_b64_tr_b16 a[202:203], v48 offset:4608
v_mfma_f32_16x16x32_bf16 v[168:171], a[28:31], a[4:7], v[168:171]
v_mfma_f32_16x16x32_bf16 v[208:211], a[132:135], a[196:199], v[208:211]
ds_read_b64_tr_b16 a[40:41], v48 offset:18432
ds_read_b64_tr_b16 a[42:43], v48 offset:18944
v_mfma_f32_16x16x32_bf16 v[208:211], a[4:7], a[36:39], v[208:211]
v_mfma_f32_16x16x32_bf16 v[128:131], a[164:167], a[132:135], v[128:131]
ds_read_b64_tr_b16 a[168:169], v48 offset:0
ds_read_b64_tr_b16 a[170:171], v48 offset:512
v_mfma_f32_16x16x32_bf16 v[128:131], a[20:23], a[4:7], v[128:131]
buffer_load_dwordx4 v7, s[4:7], s44 offen offset:3072 sc1 lds
v_mfma_f32_16x16x32_bf16 v[176:179], a[172:175], a[136:139], v[176:179]
s_add_u32 s44, s44, 4096
ds_read_b64_tr_b16 a[24:25], v48 offset:14336
ds_read_b64_tr_b16 a[26:27], v48 offset:14848
v_mfma_f32_16x16x32_bf16 v[176:179], a[28:31], a[8:11], v[176:179]
v_mfma_f32_16x16x32_bf16 v[216:219], a[136:139], a[196:199], v[216:219]
v_mfma_f32_16x16x32_bf16 v[216:219], a[8:11], a[36:39], v[216:219]
s_waitcnt lgkmcnt(12)
v_mfma_f32_16x16x32_bf16 v[136:139], a[164:167], a[136:139], v[136:139]
v_mfma_f32_16x16x32_bf16 v[136:139], a[20:23], a[8:11], v[136:139]
v_mfma_f32_16x16x32_bf16 v[184:187], a[172:175], a[140:143], v[184:187]
v_mfma_f32_16x16x32_bf16 v[184:187], a[28:31], a[12:15], v[184:187]
v_mfma_f32_16x16x32_bf16 v[224:227], a[140:143], a[196:199], v[224:227]
v_mfma_f32_16x16x32_bf16 v[224:227], a[12:15], a[36:39], v[224:227]
v_mfma_f32_16x16x32_bf16 v[144:147], a[164:167], a[140:143], v[144:147]
v_mfma_f32_16x16x32_bf16 v[144:147], a[20:23], a[12:15], v[144:147]
v_mfma_f32_16x16x32_bf16 v[192:195], a[172:175], a[160:163], v[192:195]
v_mfma_f32_16x16x32_bf16 v[192:195], a[28:31], a[16:19], v[192:195]
v_mfma_f32_16x16x32_bf16 v[232:235], a[160:163], a[196:199], v[232:235]
v_mfma_f32_16x16x32_bf16 v[232:235], a[16:19], a[36:39], v[232:235]
v_mfma_f32_16x16x32_bf16 v[152:155], a[164:167], a[160:163], v[152:155]
v_mfma_f32_16x16x32_bf16 v[152:155], a[20:23], a[16:19], v[152:155]
s_waitcnt vmcnt(16)
s_waitcnt lgkmcnt(0)
s_barrier
v_mfma_f32_16x16x32_bf16 v[164:167], a[192:195], a[128:131], v[164:167]
v_mfma_f32_16x16x32_bf16 v[164:167], a[32:35], a[0:3], v[164:167]
s_add_u32 m0, s91, 16384
ds_read_b64_tr_b16 a[80:81], v245 offset:0
ds_read_b64_tr_b16 a[82:83], v245 offset:4096
v_mfma_f32_16x16x32_bf16 v[204:207], a[128:131], a[200:203], v[204:207]
buffer_load_dwordx4 v240, s[8:11], s45 offen offset:0 lds
v_mfma_f32_16x16x32_bf16 v[204:207], a[0:3], a[40:43], v[204:207]
v_mfma_f32_16x16x32_bf16 v[172:175], a[192:195], a[132:135], v[172:175]
ds_read_b64_tr_b16 a[112:113], v245 offset:8192
ds_read_b64_tr_b16 a[114:115], v245 offset:12288
v_mfma_f32_16x16x32_bf16 v[172:175], a[32:35], a[4:7], v[172:175]
buffer_load_dwordx4 v241, s[8:11], s45 offen offset:1024 lds
v_mfma_f32_16x16x32_bf16 v[212:215], a[132:135], a[200:203], v[212:215]
v_mfma_f32_16x16x32_bf16 v[212:215], a[4:7], a[40:43], v[212:215]
ds_read_b128 a[64:67], v244 offset:0
ds_read_b128 a[96:99], v244 offset:2048
v_mfma_f32_16x16x32_bf16 v[132:135], a[168:171], a[132:135], v[132:135]
buffer_load_dwordx4 v242, s[8:11], s45 offen offset:2048 lds
v_mfma_f32_16x16x32_bf16 v[132:135], a[24:27], a[4:7], v[132:135]
v_mfma_f32_16x16x32_bf16 v[180:183], a[192:195], a[136:139], v[180:183]
ds_read_b64_tr_b16 a[84:85], v246 offset:0
ds_read_b64_tr_b16 a[86:87], v246 offset:4096
.Lpad18:
s_cbranch_execz .Ltch9
v_mfma_f32_16x16x32_bf16 v[180:183], a[32:35], a[8:11], v[180:183]
buffer_load_dwordx4 v243, s[8:11], s45 offen offset:3072 lds
v_mfma_f32_16x16x32_bf16 v[220:223], a[136:139], a[200:203], v[220:223]
s_add_u32 s45, s45, 131072
v_mfma_f32_16x16x32_bf16 v[220:223], a[8:11], a[40:43], v[220:223]
s_add_u32 m0, s91, 0
ds_read_b64_tr_b16 a[116:117], v246 offset:8192
ds_read_b64_tr_b16 a[118:119], v246 offset:12288
v_mfma_f32_16x16x32_bf16 v[140:143], a[168:171], a[136:139], v[140:143]
buffer_load_dwordx4 v7, s[4:7], s44 offen offset:0 sc1 lds
v_mfma_f32_16x16x32_bf16 v[140:143], a[24:27], a[8:11], v[140:143]
v_mfma_f32_16x16x32_bf16 v[188:191], a[192:195], a[140:143], v[188:191]
ds_read_b128 a[68:71], v244 offset:1024
ds_read_b128 a[100:103], v244 offset:3072
v_mfma_f32_16x16x32_bf16 v[188:191], a[32:35], a[12:15], v[188:191]
buffer_load_dwordx4 v7, s[4:7], s44 offen offset:1024 sc1 lds
v_mfma_f32_16x16x32_bf16 v[228:231], a[140:143], a[200:203], v[228:231]
v_mfma_f32_16x16x32_bf16 v[228:231], a[12:15], a[40:43], v[228:231]
ds_read_b128 a[72:75], v244 offset:4096
ds_read_b128 a[104:107], v244 offset:6144
v_mfma_f32_16x16x32_bf16 v[148:151], a[168:171], a[140:143], v[148:151]
buffer_load_dwordx4 v7, s[4:7], s44 offen offset:2048 sc1 lds
v_mfma_f32_16x16x32_bf16 v[148:151], a[24:27], a[12:15], v[148:151]
v_mfma_f32_16x16x32_bf16 v[196:199], a[192:195], a[160:163], v[196:199]
ds_read_b128 a[76:79], v244 offset:5120
ds_read_b128 a[108:111], v244 offset:7168
v_mfma_f32_16x16x32_bf16 v[196:199], a[32:35], a[16:19], v[196:199]
buffer_load_dwordx4 v7, s[4:7], s44 offen offset:3072 sc1 lds
v_mfma_f32_16x16x32_bf16 v[236:239], a[160:163], a[200:203], v[236:239]
s_add_u32 s44, s44, 4096
v_mfma_f32_16x16x32_bf16 v[236:239], a[16:19], a[40:43], v[236:239]
v_mfma_f32_16x16x32_bf16 v[156:159], a[168:171], a[160:163], v[156:159]
v_mfma_f32_16x16x32_bf16 v[156:159], a[24:27], a[16:19], v[156:159]
s_mov_b32 s39, 1048744942
s_waitcnt lgkmcnt(0)
v_mfma_f32_16x16x32_bf16 v[64:67], a[80:83], a[64:67], 0
s_mov_b32 s53, s57
s_add_u32 m0, s91, 147456
ds_read_b64_tr_b16 a[88:89], v247 offset:0
ds_read_b64_tr_b16 a[90:91], v247 offset:4096
v_mfma_f32_16x16x32_bf16 v[64:67], a[112:115], a[96:99], v[64:67]
buffer_load_dwordx4 v240, s[8:11], s45 offen offset:0 lds
ds_read_b64_tr_b16 a[120:121], v247 offset:8192
ds_read_b64_tr_b16 a[122:123], v247 offset:12288
v_mfma_f32_16x16x32_bf16 v[68:71], a[84:87], a[64:67], 0
v_mfma_f32_16x16x32_bf16 v[68:71], a[116:119], a[96:99], v[68:71]
buffer_load_dwordx4 v241, s[8:11], s45 offen offset:1024 lds
ds_read_b64_tr_b16 a[92:93], v248 offset:0
ds_read_b64_tr_b16 a[94:95], v248 offset:4096
v_mfma_f32_16x16x32_bf16 v[80:83], a[80:83], a[68:71], 0
ds_read_b64_tr_b16 a[124:125], v248 offset:8192
ds_read_b64_tr_b16 a[126:127], v248 offset:12288
v_cvt_pk_bf16_f32 v160, v160, v161
v_cvt_pk_bf16_f32 v161, v162, v163
v_mfma_f32_16x16x32_bf16 v[80:83], a[112:115], a[100:103], v[80:83]
buffer_load_dwordx4 v242, s[8:11], s45 offen offset:2048 lds
v_add_u32 v244, s53, v244
v_cvt_pk_bf16_f32 v162, v164, v165
v_cvt_pk_bf16_f32 v163, v166, v167
v_mfma_f32_16x16x32_bf16 v[84:87], a[84:87], a[68:71], 0
v_add_u32 v245, s53, v245
v_add_u32 v246, s53, v246
v_cvt_pk_bf16_f32 v36, v200, v201
v_cvt_pk_bf16_f32 v37, v202, v203
v_mfma_f32_16x16x32_bf16 v[84:87], a[116:119], a[100:103], v[84:87]
buffer_load_dwordx4 v243, s[8:11], s45 offen offset:3072 lds
v_cvt_pk_bf16_f32 v46, v204, v205
v_cvt_pk_bf16_f32 v47, v206, v207
v_cvt_pk_bf16_f32 v168, v168, v169
v_mfma_f32_16x16x32_bf16 v[96:99], a[80:83], a[72:75], 0
s_add_u32 s45, s45, 131072
s_add_u32 m0, s91, 131072
v_add_u32 v247, s53, v247
v_add_u32 v248, s53, v248
v_cvt_pk_bf16_f32 v169, v170, v171
v_mfma_f32_16x16x32_bf16 v[96:99], a[112:115], a[104:107], v[96:99]
buffer_load_dwordx4 v7, s[4:7], s44 offen offset:0 sc1 lds
v_cvt_pk_bf16_f32 v170, v172, v173
v_cvt_pk_bf16_f32 v171, v174, v175
v_cvt_pk_bf16_f32 v128, v128, v129
v_mfma_f32_16x16x32_bf16 v[100:103], a[84:87], a[72:75], 0
v_cvt_pk_bf16_f32 v129, v130, v131
v_cvt_pk_bf16_f32 v130, v132, v133
v_cvt_pk_bf16_f32 v131, v134, v135
v_cvt_pk_bf16_f32 v38, v208, v209
v_mfma_f32_16x16x32_bf16 v[100:103], a[116:119], a[104:107], v[100:103]
s_add_u32 s49, s46, s91
buffer_load_dwordx4 v7, s[4:7], s44 offen offset:1024 sc1 lds
v_cvt_pk_bf16_f32 v39, v210, v211
v_cvt_pk_bf16_f32 v48, v212, v213
v_mfma_f32_16x16x32_bf16 v[112:115], a[80:83], a[76:79], 0
s_add_u32 s46, s46, s53
v_cvt_pk_bf16_f32 v49, v214, v215
v_cvt_pk_bf16_f32 v176, v176, v177
v_cvt_pk_bf16_f32 v177, v178, v179
v_cvt_pk_bf16_f32 v178, v180, v181
v_mfma_f32_16x16x32_bf16 v[112:115], a[112:115], a[108:111], v[112:115]
buffer_load_dwordx4 v7, s[4:7], s44 offen offset:2048 sc1 lds
v_cvt_pk_bf16_f32 v179, v182, v183
v_cvt_pk_bf16_f32 v136, v136, v137
v_cvt_pk_bf16_f32 v137, v138, v139
v_mfma_f32_16x16x32_bf16 v[116:119], a[84:87], a[76:79], 0
buffer_load_dwordx4 v7, s[4:7], s44 offen offset:3072 sc1 lds
v_cvt_pk_bf16_f32 v138, v140, v141
v_cvt_pk_bf16_f32 v139, v142, v143
v_cvt_pk_bf16_f32 v40, v216, v217
v_mfma_f32_16x16x32_bf16 v[116:119], a[116:119], a[108:111], v[116:119]
s_waitcnt vmcnt(24) lgkmcnt(0)
s_barrier
v_cvt_pk_bf16_f32 v41, v218, v219
v_cvt_pk_bf16_f32 v50, v220, v221
v_cvt_pk_bf16_f32 v51, v222, v223
v_mfma_f32_16x16x32_bf16 v[72:75], a[88:91], a[64:67], 0
ds_read_b64_tr_b16 a[80:81], v245 offset:0
ds_read_b64_tr_b16 a[82:83], v245 offset:4096
v_cvt_pk_bf16_f32 v184, v184, v185
v_cvt_pk_bf16_f32 v185, v186, v187
v_mfma_f32_16x16x32_bf16 v[72:75], a[120:123], a[96:99], v[72:75]
s_add_u32 m0, s49, 16384
ds_read_b64_tr_b16 a[112:113], v245 offset:8192
ds_read_b64_tr_b16 a[114:115], v245 offset:12288
v_cvt_pk_bf16_f32 v186, v188, v189
v_mfma_f32_16x16x32_bf16 v[76:79], a[92:95], a[64:67], 0
buffer_load_dwordx4 v240, s[8:11], s45 offen offset:0 lds
v_cvt_pk_bf16_f32 v187, v190, v191
v_cvt_pk_bf16_f32 v144, v144, v145
v_cvt_pk_bf16_f32 v145, v146, v147
v_mfma_f32_16x16x32_bf16 v[76:79], a[124:127], a[96:99], v[76:79]
ds_read_b128 a[64:67], v244 offset:0
ds_read_b128 a[96:99], v244 offset:2048
v_cvt_pk_bf16_f32 v146, v148, v149
v_cvt_pk_bf16_f32 v147, v150, v151
v_mfma_f32_16x16x32_bf16 v[88:91], a[88:91], a[68:71], 0
ds_read_b64_tr_b16 a[84:85], v246 offset:0
ds_read_b64_tr_b16 a[86:87], v246 offset:4096
v_cvt_pk_bf16_f32 v42, v224, v225
v_cvt_pk_bf16_f32 v43, v226, v227
v_mfma_f32_16x16x32_bf16 v[88:91], a[120:123], a[100:103], v[88:91]
ds_read_b64_tr_b16 a[116:117], v246 offset:8192
ds_read_b64_tr_b16 a[118:119], v246 offset:12288
v_cvt_pk_bf16_f32 v52, v228, v229
v_cvt_pk_bf16_f32 v53, v230, v231
v_mfma_f32_16x16x32_bf16 v[92:95], a[92:95], a[68:71], 0
buffer_load_dwordx4 v241, s[8:11], s45 offen offset:1024 lds
v_cvt_pk_bf16_f32 v192, v192, v193
v_cvt_pk_bf16_f32 v193, v194, v195
v_cvt_pk_bf16_f32 v194, v196, v197
v_mfma_f32_16x16x32_bf16 v[92:95], a[124:127], a[100:103], v[92:95]
ds_read_b128 a[68:71], v244 offset:1024
ds_read_b128 a[100:103], v244 offset:3072
v_cvt_pk_bf16_f32 v195, v198, v199
v_cvt_pk_bf16_f32 v152, v152, v153
v_mfma_f32_16x16x32_bf16 v[104:107], a[88:91], a[72:75], 0
s_cmp_eq_u32 s46, 0
s_cselect_b32 s53, s56, s57
v_cvt_pk_bf16_f32 v153, v154, v155
v_cvt_pk_bf16_f32 v154, v156, v157
v_cvt_pk_bf16_f32 v155, v158, v159
v_mfma_f32_16x16x32_bf16 v[104:107], a[120:123], a[104:107], v[104:107]
v_add_u32 v245, s53, v245
v_add_u32 v246, s53, v246
v_cvt_pk_bf16_f32 v44, v232, v233
v_cvt_pk_bf16_f32 v45, v234, v235
v_mfma_f32_16x16x32_bf16 v[108:111], a[92:95], a[72:75], 0
buffer_load_dwordx4 v242, s[8:11], s45 offen offset:2048 lds
v_cvt_pk_bf16_f32 v54, v236, v237
v_cvt_pk_bf16_f32 v55, v238, v239
v_mfma_f32_16x16x32_bf16 v[200:203], v[160:163], v[128:131], v[24:27]
v_mfma_f32_16x16x32_bf16 v[108:111], a[124:127], a[104:107], v[108:111]
ds_read_b128 a[72:75], v244 offset:4096
ds_read_b128 a[104:107], v244 offset:6144
v_mfma_f32_16x16x32_bf16 v[204:207], v[168:171], v[128:131], v[20:23]
v_mfma_f32_16x16x32_bf16 v[120:123], a[88:91], a[76:79], 0
s_add_u32 s44, s44, 4096
v_mfma_f32_16x16x32_bf16 v[208:211], v[168:171], v[136:139], v[16:19]
v_mfma_f32_16x16x32_bf16 v[120:123], a[120:123], a[108:111], v[120:123]
s_waitcnt lgkmcnt(2)
v_mfma_f32_16x16x32_bf16 v[212:215], v[176:179], v[136:139], v[20:23]
v_mfma_f32_16x16x32_bf16 v[124:127], a[92:95], a[76:79], 0
buffer_load_dwordx4 v243, s[8:11], s45 offen offset:3072 lds
v_mfma_f32_16x16x32_bf16 v[216:219], v[176:179], v[144:147], v[16:19]
v_mfma_f32_16x16x32_bf16 v[124:127], a[124:127], a[108:111], v[124:127]
ds_read_b128 a[76:79], v244 offset:5120
ds_read_b128 a[108:111], v244 offset:7168
v_mfma_f32_16x16x32_bf16 v[220:223], v[184:187], v[144:147], v[20:23]
v_mfma_f32_16x16x32_bf16 v[64:67], a[80:83], a[64:67], v[64:67]
ds_read_b64_tr_b16 a[88:89], v247 offset:0
ds_read_b64_tr_b16 a[90:91], v247 offset:4096
v_mfma_f32_16x16x32_bf16 v[224:227], v[184:187], v[152:155], v[16:19]
v_mfma_f32_16x16x32_bf16 v[64:67], a[112:115], a[96:99], v[64:67]
s_mov_b32 m0, s49
ds_read_b64_tr_b16 a[120:121], v247 offset:8192
ds_read_b64_tr_b16 a[122:123], v247 offset:12288
v_mfma_f32_16x16x32_bf16 v[228:231], v[192:195], v[152:155], v[20:23]
v_mfma_f32_16x16x32_bf16 v[68:71], a[84:87], a[64:67], v[68:71]
buffer_load_dwordx4 v7, s[4:7], s44 offen offset:0 sc1 lds
v_max3_f32 v56, v200, v201, v202
v_max3_f32 v58, v208, v209, v210
v_max3_f32 v60, v216, v217, v218
v_mfma_f32_16x16x32_bf16 v[68:71], a[116:119], a[96:99], v[68:71]
ds_read_b64_tr_b16 a[92:93], v248 offset:0
ds_read_b64_tr_b16 a[94:95], v248 offset:4096
v_max3_f32 v62, v224, v225, v226
v_max3_f32 v57, v203, v204, v205
v_mfma_f32_16x16x32_bf16 v[80:83], a[80:83], a[68:71], v[80:83]
ds_read_b64_tr_b16 a[124:125], v248 offset:8192
ds_read_b64_tr_b16 a[126:127], v248 offset:12288
v_max3_f32 v59, v211, v212, v213
v_max3_f32 v61, v219, v220, v221
v_mfma_f32_16x16x32_bf16 v[80:83], a[112:115], a[100:103], v[80:83]
s_add_u32 s45, s45, 131072
v_add_u32 v244, s53, v244
v_max3_f32 v63, v227, v228, v229
v_max3_f32 v56, v56, v206, v207
v_max3_f32 v58, v58, v214, v215
v_mfma_f32_16x16x32_bf16 v[84:87], a[84:87], a[68:71], v[84:87]
buffer_load_dwordx4 v7, s[4:7], s44 offen offset:1024 sc1 lds
v_max3_f32 v60, v60, v222, v223
v_max3_f32 v62, v62, v230, v231
v_max_f32 v56, v56, v57
v_mfma_f32_16x16x32_bf16 v[84:87], a[116:119], a[100:103], v[84:87]
s_waitcnt lgkmcnt(8)
v_max_f32 v58, v58, v59
v_max_f32 v60, v60, v61
v_max_f32 v62, v62, v63
v_mov_b32 v57, v56
v_mfma_f32_16x16x32_bf16 v[96:99], a[80:83], a[72:75], v[96:99]
v_add_u32 v247, s53, v247
v_add_u32 v248, s53, v248
v_mov_b32 v59, v58
v_mov_b32 v61, v60
v_mfma_f32_16x16x32_bf16 v[96:99], a[112:115], a[104:107], v[96:99]
v_mov_b32 v63, v62
v_permlane16_swap_b32 v56, v57
v_mfma_f32_16x16x32_bf16 v[100:103], a[84:87], a[72:75], v[100:103]
buffer_load_dwordx4 v7, s[4:7], s44 offen offset:2048 sc1 lds
v_permlane16_swap_b32 v58, v59
v_mfma_f32_16x16x32_bf16 v[100:103], a[116:119], a[104:107], v[100:103]
s_add_u32 s49, s46, s91
v_permlane16_swap_b32 v60, v61
v_permlane16_swap_b32 v62, v63
v_mfma_f32_16x16x32_bf16 v[112:115], a[80:83], a[76:79], v[112:115]
s_add_u32 s46, s46, s53
v_max_f32 v56, v56, v57
v_max_f32 v58, v58, v59
v_max_f32 v60, v60, v61
v_max_f32 v62, v62, v63
v_mfma_f32_16x16x32_bf16 v[112:115], a[112:115], a[108:111], v[112:115]
v_mov_b32 v57, v56
v_mov_b32 v59, v58
v_mov_b32 v61, v60
v_mov_b32 v63, v62
v_mfma_f32_16x16x32_bf16 v[116:119], a[84:87], a[76:79], v[116:119]
buffer_load_dwordx4 v7, s[4:7], s44 offen offset:3072 sc1 lds
v_permlane32_swap_b32 v56, v57
v_mfma_f32_16x16x32_bf16 v[116:119], a[116:119], a[108:111], v[116:119]
s_waitcnt vmcnt(24) lgkmcnt(0)
s_barrier
v_permlane32_swap_b32 v58, v59
v_mfma_f32_16x16x32_bf16 v[72:75], a[88:91], a[64:67], v[72:75]
ds_read_b64_tr_b16 a[80:81], v245 offset:0
ds_read_b64_tr_b16 a[82:83], v245 offset:4096
v_permlane32_swap_b32 v60, v61
v_mfma_f32_16x16x32_bf16 v[72:75], a[120:123], a[96:99], v[72:75]
s_add_u32 m0, s49, 16384
ds_read_b64_tr_b16 a[112:113], v245 offset:8192
ds_read_b64_tr_b16 a[114:115], v245 offset:12288
v_mfma_f32_16x16x32_bf16 v[76:79], a[92:95], a[64:67], v[76:79]
buffer_load_dwordx4 v240, s[8:11], s45 offen offset:0 lds
v_permlane32_swap_b32 v62, v63
v_max_f32 v56, v56, v57
v_mfma_f32_16x16x32_bf16 v[76:79], a[124:127], a[96:99], v[76:79]
ds_read_b128 a[64:67], v244 offset:0
ds_read_b128 a[96:99], v244 offset:2048
v_max_f32 v58, v58, v59
v_max_f32 v60, v60, v61
v_mfma_f32_16x16x32_bf16 v[88:91], a[88:91], a[68:71], v[88:91]
ds_read_b64_tr_b16 a[84:85], v246 offset:0
ds_read_b64_tr_b16 a[86:87], v246 offset:4096
v_max_f32 v62, v62, v63
v_mul_f32 v56, s39, v56
v_mfma_f32_16x16x32_bf16 v[88:91], a[120:123], a[100:103], v[88:91]
ds_read_b64_tr_b16 a[116:117], v246 offset:8192
ds_read_b64_tr_b16 a[118:119], v246 offset:12288
v_mul_f32 v58, s39, v58
v_mul_f32 v60, s39, v60
v_mfma_f32_16x16x32_bf16 v[92:95], a[92:95], a[68:71], v[92:95]
buffer_load_dwordx4 v241, s[8:11], s45 offen offset:1024 lds
v_mul_f32 v62, s39, v62
v_fma_f32 v200, v200, s39, -v56
v_fma_f32 v208, v208, s39, -v58
v_mfma_f32_16x16x32_bf16 v[92:95], a[124:127], a[100:103], v[92:95]
ds_read_b128 a[68:71], v244 offset:1024
ds_read_b128 a[100:103], v244 offset:3072
v_fma_f32 v216, v216, s39, -v60
v_fma_f32 v224, v224, s39, -v62
v_mfma_f32_16x16x32_bf16 v[104:107], a[88:91], a[72:75], v[104:107]
s_cmp_eq_u32 s46, 0
s_cselect_b32 s53, s56, s57
v_fma_f32 v201, v201, s39, -v56
v_fma_f32 v209, v209, s39, -v58
v_fma_f32 v217, v217, s39, -v60
v_mfma_f32_16x16x32_bf16 v[104:107], a[120:123], a[104:107], v[104:107]
v_add_u32 v245, s53, v245
v_add_u32 v246, s53, v246
v_fma_f32 v225, v225, s39, -v62
v_fma_f32 v202, v202, s39, -v56
v_mfma_f32_16x16x32_bf16 v[108:111], a[92:95], a[72:75], v[108:111]
buffer_load_dwordx4 v242, s[8:11], s45 offen offset:2048 lds
v_fma_f32 v210, v210, s39, -v58
v_fma_f32 v218, v218, s39, -v60
v_fma_f32 v226, v226, s39, -v62
v_mfma_f32_16x16x32_bf16 v[108:111], a[124:127], a[104:107], v[108:111]
ds_read_b128 a[72:75], v244 offset:4096
ds_read_b128 a[104:107], v244 offset:6144
v_fma_f32 v203, v203, s39, -v56
v_fma_f32 v211, v211, s39, -v58
v_mfma_f32_16x16x32_bf16 v[120:123], a[88:91], a[76:79], v[120:123]
s_add_u32 s44, s44, 4096
v_fma_f32 v219, v219, s39, -v60
v_fma_f32 v227, v227, s39, -v62
v_fma_f32 v204, v204, s39, -v56
v_fma_f32 v212, v212, s39, -v58
v_mfma_f32_16x16x32_bf16 v[120:123], a[120:123], a[108:111], v[120:123]
s_waitcnt lgkmcnt(2)
v_fma_f32 v220, v220, s39, -v60
v_fma_f32 v228, v228, s39, -v62
v_fma_f32 v205, v205, s39, -v56
v_fma_f32 v213, v213, s39, -v58
v_mfma_f32_16x16x32_bf16 v[124:127], a[92:95], a[76:79], v[124:127]
buffer_load_dwordx4 v243, s[8:11], s45 offen offset:3072 lds
v_fma_f32 v221, v221, s39, -v60
v_fma_f32 v229, v229, s39, -v62
v_fma_f32 v206, v206, s39, -v56
v_mfma_f32_16x16x32_bf16 v[124:127], a[124:127], a[108:111], v[124:127]
ds_read_b128 a[76:79], v244 offset:5120
ds_read_b128 a[108:111], v244 offset:7168
v_fma_f32 v214, v214, s39, -v58
v_fma_f32 v222, v222, s39, -v60
v_mfma_f32_16x16x32_bf16 v[64:67], a[80:83], a[64:67], v[64:67]
ds_read_b64_tr_b16 a[88:89], v247 offset:0
ds_read_b64_tr_b16 a[90:91], v247 offset:4096
v_fma_f32 v230, v230, s39, -v62
v_fma_f32 v207, v207, s39, -v56
v_mfma_f32_16x16x32_bf16 v[64:67], a[112:115], a[96:99], v[64:67]
s_mov_b32 m0, s49
ds_read_b64_tr_b16 a[120:121], v247 offset:8192
ds_read_b64_tr_b16 a[122:123], v247 offset:12288
v_fma_f32 v215, v215, s39, -v58
v_mfma_f32_16x16x32_bf16 v[68:71], a[84:87], a[64:67], v[68:71]
buffer_load_dwordx4 v7, s[4:7], s44 offen offset:0 sc1 lds
v_fma_f32 v223, v223, s39, -v60
v_fma_f32 v231, v231, s39, -v62
v_mfma_f32_16x16x32_bf16 v[68:71], a[116:119], a[96:99], v[68:71]
ds_read_b64_tr_b16 a[92:93], v248 offset:0
ds_read_b64_tr_b16 a[94:95], v248 offset:4096
v_exp_f32 v200, v200
v_mfma_f32_16x16x32_bf16 v[80:83], a[80:83], a[68:71], v[80:83]
ds_read_b64_tr_b16 a[124:125], v248 offset:8192
ds_read_b64_tr_b16 a[126:127], v248 offset:12288
v_exp_f32 v208, v208
v_mfma_f32_16x16x32_bf16 v[80:83], a[112:115], a[100:103], v[80:83]
s_add_u32 s45, s45, 131072
v_add_u32 v244, s53, v244
v_exp_f32 v216, v216
v_mfma_f32_16x16x32_bf16 v[84:87], a[84:87], a[68:71], v[84:87]
buffer_load_dwordx4 v7, s[4:7], s44 offen offset:1024 sc1 lds
v_exp_f32 v224, v224
v_mfma_f32_16x16x32_bf16 v[84:87], a[116:119], a[100:103], v[84:87]
s_waitcnt lgkmcnt(8)
v_exp_f32 v201, v201
v_exp_f32 v209, v209
v_mfma_f32_16x16x32_bf16 v[96:99], a[80:83], a[72:75], v[96:99]
v_add_u32 v247, s53, v247
v_add_u32 v248, s53, v248
v_exp_f32 v217, v217
v_mfma_f32_16x16x32_bf16 v[96:99], a[112:115], a[104:107], v[96:99]
v_exp_f32 v225, v225
v_exp_f32 v202, v202
v_mfma_f32_16x16x32_bf16 v[100:103], a[84:87], a[72:75], v[100:103]
buffer_load_dwordx4 v7, s[4:7], s44 offen offset:2048 sc1 lds
v_exp_f32 v210, v210
v_mfma_f32_16x16x32_bf16 v[100:103], a[116:119], a[104:107], v[100:103]
s_add_u32 s49, s46, s91
v_exp_f32 v218, v218
v_exp_f32 v226, v226
v_mfma_f32_16x16x32_bf16 v[112:115], a[80:83], a[76:79], v[112:115]
s_add_u32 s46, s46, s53
v_exp_f32 v203, v203
v_exp_f32 v211, v211
v_mfma_f32_16x16x32_bf16 v[112:115], a[112:115], a[108:111], v[112:115]
v_exp_f32 v219, v219
v_exp_f32 v227, v227
v_mfma_f32_16x16x32_bf16 v[116:119], a[84:87], a[76:79], v[116:119]
buffer_load_dwordx4 v7, s[4:7], s44 offen offset:3072 sc1 lds
v_exp_f32 v204, v204
v_mfma_f32_16x16x32_bf16 v[116:119], a[116:119], a[108:111], v[116:119]
s_waitcnt vmcnt(24) lgkmcnt(0)
s_barrier
v_exp_f32 v212, v212
v_mfma_f32_16x16x32_bf16 v[72:75], a[88:91], a[64:67], v[72:75]
ds_read_b64_tr_b16 a[80:81], v245 offset:0
ds_read_b64_tr_b16 a[82:83], v245 offset:4096
v_exp_f32 v220, v220
v_mfma_f32_16x16x32_bf16 v[72:75], a[120:123], a[96:99], v[72:75]
s_add_u32 m0, s49, 16384
ds_read_b64_tr_b16 a[112:113], v245 offset:8192
ds_read_b64_tr_b16 a[114:115], v245 offset:12288
v_mfma_f32_16x16x32_bf16 v[76:79], a[92:95], a[64:67], v[76:79]
buffer_load_dwordx4 v240, s[8:11], s45 offen offset:0 lds
v_exp_f32 v228, v228
v_mfma_f32_16x16x32_bf16 v[76:79], a[124:127], a[96:99], v[76:79]
ds_read_b128 a[64:67], v244 offset:0
ds_read_b128 a[96:99], v244 offset:2048
v_exp_f32 v205, v205
v_mfma_f32_16x16x32_bf16 v[88:91], a[88:91], a[68:71], v[88:91]
ds_read_b64_tr_b16 a[84:85], v246 offset:0
ds_read_b64_tr_b16 a[86:87], v246 offset:4096
v_exp_f32 v213, v213
v_mfma_f32_16x16x32_bf16 v[88:91], a[120:123], a[100:103], v[88:91]
ds_read_b64_tr_b16 a[116:117], v246 offset:8192
ds_read_b64_tr_b16 a[118:119], v246 offset:12288
v_exp_f32 v221, v221
v_mfma_f32_16x16x32_bf16 v[92:95], a[92:95], a[68:71], v[92:95]
buffer_load_dwordx4 v241, s[8:11], s45 offen offset:1024 lds
v_exp_f32 v229, v229
v_mfma_f32_16x16x32_bf16 v[92:95], a[124:127], a[100:103], v[92:95]
ds_read_b128 a[68:71], v244 offset:1024
ds_read_b128 a[100:103], v244 offset:3072
v_exp_f32 v206, v206
v_mfma_f32_16x16x32_bf16 v[104:107], a[88:91], a[72:75], v[104:107]
s_cmp_eq_u32 s46, 0
s_cselect_b32 s53, s56, s57
v_exp_f32 v214, v214
v_mfma_f32_16x16x32_bf16 v[104:107], a[120:123], a[104:107], v[104:107]
v_add_u32 v245, s53, v245
v_add_u32 v246, s53, v246
v_exp_f32 v222, v222
v_mfma_f32_16x16x32_bf16 v[108:111], a[92:95], a[72:75], v[108:111]
buffer_load_dwordx4 v242, s[8:11], s45 offen offset:2048 lds
v_exp_f32 v230, v230
v_mfma_f32_16x16x32_bf16 v[108:111], a[124:127], a[104:107], v[108:111]
ds_read_b128 a[72:75], v244 offset:4096
ds_read_b128 a[104:107], v244 offset:6144
v_exp_f32 v207, v207
v_mfma_f32_16x16x32_bf16 v[120:123], a[88:91], a[76:79], v[120:123]
s_add_u32 s44, s44, 4096
v_exp_f32 v215, v215
v_exp_f32 v223, v223
v_mfma_f32_16x16x32_bf16 v[120:123], a[120:123], a[108:111], v[120:123]
s_waitcnt lgkmcnt(2)
v_exp_f32 v231, v231
v_cvt_pk_bf16_f32 v132, v200, v201
v_cvt_pk_bf16_f32 v140, v208, v209
v_mfma_f32_16x16x32_bf16 v[124:127], a[92:95], a[76:79], v[124:127]
buffer_load_dwordx4 v243, s[8:11], s45 offen offset:3072 lds
v_cvt_pk_bf16_f32 v148, v216, v217
v_cvt_pk_bf16_f32 v156, v224, v225
v_cvt_pk_bf16_f32 v133, v202, v203
v_mfma_f32_16x16x32_bf16 v[124:127], a[124:127], a[108:111], v[124:127]
ds_read_b128 a[76:79], v244 offset:5120
ds_read_b128 a[108:111], v244 offset:7168
v_cvt_pk_bf16_f32 v141, v210, v211
v_cvt_pk_bf16_f32 v149, v218, v219
v_mfma_f32_16x16x32_bf16 v[64:67], a[80:83], a[64:67], v[64:67]
ds_read_b64_tr_b16 a[88:89], v247 offset:0
ds_read_b64_tr_b16 a[90:91], v247 offset:4096
v_cvt_pk_bf16_f32 v157, v226, v227
v_cvt_pk_bf16_f32 v134, v204, v205
v_mfma_f32_16x16x32_bf16 v[64:67], a[112:115], a[96:99], v[64:67]
s_mov_b32 m0, s49
ds_read_b64_tr_b16 a[120:121], v247 offset:8192
ds_read_b64_tr_b16 a[122:123], v247 offset:12288
v_cvt_pk_bf16_f32 v142, v212, v213
v_mfma_f32_16x16x32_bf16 v[68:71], a[84:87], a[64:67], v[68:71]
buffer_load_dwordx4 v7, s[4:7], s44 offen offset:0 sc1 lds
v_cvt_pk_bf16_f32 v150, v220, v221
v_cvt_pk_bf16_f32 v158, v228, v229
v_cvt_pk_bf16_f32 v135, v206, v207
v_mfma_f32_16x16x32_bf16 v[68:71], a[116:119], a[96:99], v[68:71]
ds_read_b64_tr_b16 a[92:93], v248 offset:0
ds_read_b64_tr_b16 a[94:95], v248 offset:4096
v_cvt_pk_bf16_f32 v143, v214, v215
v_cvt_pk_bf16_f32 v151, v222, v223
v_mfma_f32_16x16x32_bf16 v[80:83], a[80:83], a[68:71], v[80:83]
ds_read_b64_tr_b16 a[124:125], v248 offset:8192
ds_read_b64_tr_b16 a[126:127], v248 offset:12288
v_cvt_pk_bf16_f32 v159, v230, v231
v_add_f32 v56, v200, v201
v_mfma_f32_16x16x32_bf16 v[80:83], a[112:115], a[100:103], v[80:83]
s_add_u32 s45, s45, 131072
v_add_u32 v244, s53, v244
v_add_f32 v58, v208, v209
v_add_f32 v60, v216, v217
v_add_f32 v62, v224, v225
v_mfma_f32_16x16x32_bf16 v[160:163], v[36:39], v[132:135], 0
v_mfma_f32_16x16x32_bf16 v[84:87], a[84:87], a[68:71], v[84:87]
buffer_load_dwordx4 v7, s[4:7], s44 offen offset:1024 sc1 lds
v_mfma_f32_16x16x32_bf16 v[164:167], v[46:49], v[132:135], 0
v_mfma_f32_16x16x32_bf16 v[84:87], a[116:119], a[100:103], v[84:87]
s_waitcnt lgkmcnt(8)
v_mfma_f32_16x16x32_bf16 v[168:171], v[38:41], v[140:143], 0
v_mfma_f32_16x16x32_bf16 v[96:99], a[80:83], a[72:75], v[96:99]
v_add_u32 v247, s53, v247
v_add_u32 v248, s53, v248
v_mfma_f32_16x16x32_bf16 v[172:175], v[48:51], v[140:143], 0
v_mfma_f32_16x16x32_bf16 v[96:99], a[112:115], a[104:107], v[96:99]
v_mfma_f32_16x16x32_bf16 v[176:179], v[40:43], v[148:151], 0
v_mfma_f32_16x16x32_bf16 v[100:103], a[84:87], a[72:75], v[100:103]
buffer_load_dwordx4 v7, s[4:7], s44 offen offset:2048 sc1 lds
v_mfma_f32_16x16x32_bf16 v[180:183], v[50:53], v[148:151], 0
v_mfma_f32_16x16x32_bf16 v[100:103], a[116:119], a[104:107], v[100:103]
s_add_u32 s49, s46, s91
v_mfma_f32_16x16x32_bf16 v[184:187], v[42:45], v[156:159], 0
v_mfma_f32_16x16x32_bf16 v[112:115], a[80:83], a[76:79], v[112:115]
s_add_u32 s46, s46, s53
v_mfma_f32_16x16x32_bf16 v[188:191], v[52:55], v[156:159], 0
v_mfma_f32_16x16x32_bf16 v[112:115], a[112:115], a[108:111], v[112:115]
v_add_f32 v57, v202, v203
v_add_f32 v59, v210, v211
v_add_f32 v61, v218, v219
v_add_f32 v63, v226, v227
v_mfma_f32_16x16x32_bf16 v[116:119], a[84:87], a[76:79], v[116:119]
buffer_load_dwordx4 v7, s[4:7], s44 offen offset:3072 sc1 lds
v_add_f32 v56, v56, v204
v_add_f32 v58, v58, v212
v_add_f32 v60, v60, v220
v_mfma_f32_16x16x32_bf16 v[116:119], a[116:119], a[108:111], v[116:119]
s_waitcnt vmcnt(24) lgkmcnt(0)
s_barrier
v_add_f32 v62, v62, v228
v_add_f32 v57, v57, v205
v_add_f32 v59, v59, v213
v_mfma_f32_16x16x32_bf16 v[72:75], a[88:91], a[64:67], v[72:75]
ds_read_b64_tr_b16 a[80:81], v245 offset:0
ds_read_b64_tr_b16 a[82:83], v245 offset:4096
v_add_f32 v61, v61, v221
v_add_f32 v63, v63, v229
v_mfma_f32_16x16x32_bf16 v[72:75], a[120:123], a[96:99], v[72:75]
s_add_u32 m0, s49, 16384
ds_read_b64_tr_b16 a[112:113], v245 offset:8192
ds_read_b64_tr_b16 a[114:115], v245 offset:12288
v_add_f32 v56, v56, v206
v_mfma_f32_16x16x32_bf16 v[76:79], a[92:95], a[64:67], v[76:79]
buffer_load_dwordx4 v240, s[8:11], s45 offen offset:0 lds
v_add_f32 v58, v58, v214
v_add_f32 v60, v60, v222
v_add_f32 v62, v62, v230
v_mfma_f32_16x16x32_bf16 v[76:79], a[124:127], a[96:99], v[76:79]
ds_read_b128 a[64:67], v244 offset:0
ds_read_b128 a[96:99], v244 offset:2048
v_add_f32 v57, v57, v207
v_add_f32 v59, v59, v215
v_mfma_f32_16x16x32_bf16 v[88:91], a[88:91], a[68:71], v[88:91]
ds_read_b64_tr_b16 a[84:85], v246 offset:0
ds_read_b64_tr_b16 a[86:87], v246 offset:4096
v_add_f32 v61, v61, v223
v_add_f32 v63, v63, v231
v_mfma_f32_16x16x32_bf16 v[88:91], a[120:123], a[100:103], v[88:91]
ds_read_b64_tr_b16 a[116:117], v246 offset:8192
ds_read_b64_tr_b16 a[118:119], v246 offset:12288
v_add_f32 v56, v56, v57
v_add_f32 v58, v58, v59
v_mfma_f32_16x16x32_bf16 v[92:95], a[92:95], a[68:71], v[92:95]
buffer_load_dwordx4 v241, s[8:11], s45 offen offset:1024 lds
v_add_f32 v60, v60, v61
v_add_f32 v62, v62, v63
v_mov_b32 v57, v56
v_mfma_f32_16x16x32_bf16 v[92:95], a[124:127], a[100:103], v[92:95]
ds_read_b128 a[68:71], v244 offset:1024
ds_read_b128 a[100:103], v244 offset:3072
v_mov_b32 v59, v58
v_mov_b32 v61, v60
v_mfma_f32_16x16x32_bf16 v[104:107], a[88:91], a[72:75], v[104:107]
s_cmp_eq_u32 s46, 0
s_cselect_b32 s53, s56, s57
v_mov_b32 v63, v62
v_permlane16_swap_b32 v56, v57
v_mfma_f32_16x16x32_bf16 v[104:107], a[120:123], a[104:107], v[104:107]
v_add_u32 v245, s53, v245
v_add_u32 v246, s53, v246
v_permlane16_swap_b32 v58, v59
v_mfma_f32_16x16x32_bf16 v[108:111], a[92:95], a[72:75], v[108:111]
buffer_load_dwordx4 v242, s[8:11], s45 offen offset:2048 lds
v_permlane16_swap_b32 v60, v61
v_mfma_f32_16x16x32_bf16 v[108:111], a[124:127], a[104:107], v[108:111]
ds_read_b128 a[72:75], v244 offset:4096
ds_read_b128 a[104:107], v244 offset:6144
.Lpad19:
s_cbranch_execz .Ltch10
v_permlane16_swap_b32 v62, v63
v_mfma_f32_16x16x32_bf16 v[120:123], a[88:91], a[76:79], v[120:123]
s_add_u32 s44, s44, 4096
v_add_f32 v56, v56, v57
v_add_f32 v58, v58, v59
v_add_f32 v60, v60, v61
v_add_f32 v62, v62, v63
v_mfma_f32_16x16x32_bf16 v[120:123], a[120:123], a[108:111], v[120:123]
s_waitcnt lgkmcnt(2)
v_mov_b32 v57, v56
v_mov_b32 v59, v58
v_mov_b32 v61, v60
v_mov_b32 v63, v62
v_mfma_f32_16x16x32_bf16 v[124:127], a[92:95], a[76:79], v[124:127]
buffer_load_dwordx4 v243, s[8:11], s45 offen offset:3072 lds
v_permlane32_swap_b32 v56, v57
v_mfma_f32_16x16x32_bf16 v[124:127], a[124:127], a[108:111], v[124:127]
ds_read_b128 a[76:79], v244 offset:5120
ds_read_b128 a[108:111], v244 offset:7168
v_permlane32_swap_b32 v58, v59
v_mfma_f32_16x16x32_bf16 v[64:67], a[80:83], a[64:67], v[64:67]
ds_read_b64_tr_b16 a[88:89], v247 offset:0
ds_read_b64_tr_b16 a[90:91], v247 offset:4096
v_permlane32_swap_b32 v60, v61
v_mfma_f32_16x16x32_bf16 v[64:67], a[112:115], a[96:99], v[64:67]
s_mov_b32 m0, s49
ds_read_b64_tr_b16 a[120:121], v247 offset:8192
ds_read_b64_tr_b16 a[122:123], v247 offset:12288
v_mfma_f32_16x16x32_bf16 v[68:71], a[84:87], a[64:67], v[68:71]
buffer_load_dwordx4 v7, s[4:7], s44 offen offset:0 sc1 lds
v_permlane32_swap_b32 v62, v63
v_add_f32 v56, v56, v57
v_mfma_f32_16x16x32_bf16 v[68:71], a[116:119], a[96:99], v[68:71]
ds_read_b64_tr_b16 a[92:93], v248 offset:0
ds_read_b64_tr_b16 a[94:95], v248 offset:4096
v_add_f32 v58, v58, v59
v_add_f32 v60, v60, v61
v_mfma_f32_16x16x32_bf16 v[80:83], a[80:83], a[68:71], v[80:83]
ds_read_b64_tr_b16 a[124:125], v248 offset:8192
ds_read_b64_tr_b16 a[126:127], v248 offset:12288
v_add_f32 v62, v62, v63
v_mfma_f32_16x16x32_bf16 v[80:83], a[112:115], a[100:103], v[80:83]
s_add_u32 s45, s45, 131072
v_add_u32 v244, s53, v244
v_rcp_f32 v56, v56
v_mfma_f32_16x16x32_bf16 v[84:87], a[84:87], a[68:71], v[84:87]
buffer_load_dwordx4 v7, s[4:7], s44 offen offset:1024 sc1 lds
v_rcp_f32 v58, v58
v_mfma_f32_16x16x32_bf16 v[84:87], a[116:119], a[100:103], v[84:87]
s_waitcnt lgkmcnt(8)
v_rcp_f32 v60, v60
v_rcp_f32 v62, v62
v_mfma_f32_16x16x32_bf16 v[96:99], a[80:83], a[72:75], v[96:99]
v_add_u32 v247, s53, v247
v_add_u32 v248, s53, v248
v_mul_f32 v160, v56, v160
v_mul_f32 v161, v56, v161
v_mfma_f32_16x16x32_bf16 v[96:99], a[112:115], a[104:107], v[96:99]
v_mul_f32 v162, v56, v162
v_mul_f32 v163, v56, v163
v_mul_f32 v164, v56, v164
v_mul_f32 v165, v56, v165
v_mfma_f32_16x16x32_bf16 v[100:103], a[84:87], a[72:75], v[100:103]
buffer_load_dwordx4 v7, s[4:7], s44 offen offset:2048 sc1 lds
v_mul_f32 v166, v56, v166
v_mul_f32 v167, v56, v167
v_cvt_pk_bf16_f32 v128, v160, v161
v_mfma_f32_16x16x32_bf16 v[100:103], a[116:119], a[104:107], v[100:103]
s_add_u32 s49, s46, s91
v_cvt_pk_bf16_f32 v129, v162, v163
v_cvt_pk_bf16_f32 v130, v164, v165
v_cvt_pk_bf16_f32 v131, v166, v167
v_mfma_f32_16x16x32_bf16 v[112:115], a[80:83], a[76:79], v[112:115]
s_add_u32 s46, s46, s53
buffer_store_dwordx4 v[128:131], v7, s[20:23], s40 offen offset:0 sc1
v_mul_f32 v168, v58, v168
v_mul_f32 v169, v58, v169
v_mfma_f32_16x16x32_bf16 v[112:115], a[112:115], a[108:111], v[112:115]
v_mul_f32 v170, v58, v170
v_mul_f32 v171, v58, v171
v_mul_f32 v172, v58, v172
v_mul_f32 v173, v58, v173
v_mfma_f32_16x16x32_bf16 v[116:119], a[84:87], a[76:79], v[116:119]
buffer_load_dwordx4 v7, s[4:7], s44 offen offset:3072 sc1 lds
v_mul_f32 v174, v58, v174
v_mul_f32 v175, v58, v175
v_cvt_pk_bf16_f32 v136, v168, v169
v_mfma_f32_16x16x32_bf16 v[116:119], a[116:119], a[108:111], v[116:119]
s_waitcnt vmcnt(25) lgkmcnt(0)
s_barrier
v_cvt_pk_bf16_f32 v137, v170, v171
v_cvt_pk_bf16_f32 v138, v172, v173
v_cvt_pk_bf16_f32 v139, v174, v175
v_mfma_f32_16x16x32_bf16 v[72:75], a[88:91], a[64:67], v[72:75]
buffer_store_dwordx4 v[136:139], v7, s[20:23], s40 offen offset:1024 sc1
ds_read_b64_tr_b16 a[80:81], v245 offset:0
ds_read_b64_tr_b16 a[82:83], v245 offset:4096
v_mfma_f32_16x16x32_bf16 v[72:75], a[120:123], a[96:99], v[72:75]
s_add_u32 m0, s49, 16384
ds_read_b64_tr_b16 a[112:113], v245 offset:8192
ds_read_b64_tr_b16 a[114:115], v245 offset:12288
v_mul_f32 v176, v60, v176
v_mfma_f32_16x16x32_bf16 v[76:79], a[92:95], a[64:67], v[76:79]
buffer_load_dwordx4 v240, s[8:11], s45 offen offset:0 lds
v_mul_f32 v177, v60, v177
v_mul_f32 v178, v60, v178
v_mul_f32 v179, v60, v179
v_mfma_f32_16x16x32_bf16 v[76:79], a[124:127], a[96:99], v[76:79]
ds_read_b128 a[64:67], v244 offset:0
ds_read_b128 a[96:99], v244 offset:2048
v_mul_f32 v180, v60, v180
v_mul_f32 v181, v60, v181
v_mfma_f32_16x16x32_bf16 v[88:91], a[88:91], a[68:71], v[88:91]
ds_read_b64_tr_b16 a[84:85], v246 offset:0
ds_read_b64_tr_b16 a[86:87], v246 offset:4096
v_mul_f32 v182, v60, v182
v_mul_f32 v183, v60, v183
v_mfma_f32_16x16x32_bf16 v[88:91], a[120:123], a[100:103], v[88:91]
ds_read_b64_tr_b16 a[116:117], v246 offset:8192
ds_read_b64_tr_b16 a[118:119], v246 offset:12288
v_cvt_pk_bf16_f32 v144, v176, v177
v_cvt_pk_bf16_f32 v145, v178, v179
v_mfma_f32_16x16x32_bf16 v[92:95], a[92:95], a[68:71], v[92:95]
buffer_load_dwordx4 v241, s[8:11], s45 offen offset:1024 lds
v_cvt_pk_bf16_f32 v146, v180, v181
v_cvt_pk_bf16_f32 v147, v182, v183
v_mfma_f32_16x16x32_bf16 v[92:95], a[124:127], a[100:103], v[92:95]
buffer_store_dwordx4 v[144:147], v7, s[20:23], s41 offen offset:0 sc1
ds_read_b128 a[68:71], v244 offset:1024
ds_read_b128 a[100:103], v244 offset:3072
v_mfma_f32_16x16x32_bf16 v[104:107], a[88:91], a[72:75], v[104:107]
s_cmp_eq_u32 s46, 0
s_cselect_b32 s53, s56, s57
v_mul_f32 v184, v62, v184
v_mul_f32 v185, v62, v185
v_mul_f32 v186, v62, v186
v_mfma_f32_16x16x32_bf16 v[104:107], a[120:123], a[104:107], v[104:107]
v_add_u32 v245, s53, v245
v_add_u32 v246, s53, v246
v_mul_f32 v187, v62, v187
v_mul_f32 v188, v62, v188
v_mfma_f32_16x16x32_bf16 v[108:111], a[92:95], a[72:75], v[108:111]
buffer_load_dwordx4 v242, s[8:11], s45 offen offset:2048 lds
v_mul_f32 v189, v62, v189
v_mul_f32 v190, v62, v190
v_mul_f32 v191, v62, v191
v_mfma_f32_16x16x32_bf16 v[108:111], a[124:127], a[104:107], v[108:111]
ds_read_b128 a[72:75], v244 offset:4096
ds_read_b128 a[104:107], v244 offset:6144
v_cvt_pk_bf16_f32 v152, v184, v185
v_cvt_pk_bf16_f32 v153, v186, v187
v_mfma_f32_16x16x32_bf16 v[120:123], a[88:91], a[76:79], v[120:123]
s_add_u32 s44, s44, 4096
v_cvt_pk_bf16_f32 v154, v188, v189
v_cvt_pk_bf16_f32 v155, v190, v191
v_mfma_f32_16x16x32_bf16 v[120:123], a[120:123], a[108:111], v[120:123]
s_waitcnt lgkmcnt(2)
buffer_store_dwordx4 v[152:155], v7, s[20:23], s41 offen offset:1024 sc1
v_mfma_f32_16x16x32_bf16 v[124:127], a[92:95], a[76:79], v[124:127]
buffer_load_dwordx4 v243, s[8:11], s45 offen offset:3072 lds
v_mfma_f32_16x16x32_bf16 v[124:127], a[124:127], a[108:111], v[124:127]
ds_read_b128 a[76:79], v244 offset:5120
ds_read_b128 a[108:111], v244 offset:7168
v_mfma_f32_16x16x32_bf16 v[64:67], a[80:83], a[64:67], v[64:67]
ds_read_b64_tr_b16 a[88:89], v247 offset:0
ds_read_b64_tr_b16 a[90:91], v247 offset:4096
v_mfma_f32_16x16x32_bf16 v[64:67], a[112:115], a[96:99], v[64:67]
s_mov_b32 m0, s49
ds_read_b64_tr_b16 a[120:121], v247 offset:8192
ds_read_b64_tr_b16 a[122:123], v247 offset:12288
v_mfma_f32_16x16x32_bf16 v[68:71], a[84:87], a[64:67], v[68:71]
buffer_load_dwordx4 v7, s[4:7], s44 offen offset:0 sc1 lds
v_mfma_f32_16x16x32_bf16 v[68:71], a[116:119], a[96:99], v[68:71]
ds_read_b64_tr_b16 a[92:93], v248 offset:0
ds_read_b64_tr_b16 a[94:95], v248 offset:4096
v_mfma_f32_16x16x32_bf16 v[80:83], a[80:83], a[68:71], v[80:83]
ds_read_b64_tr_b16 a[124:125], v248 offset:8192
ds_read_b64_tr_b16 a[126:127], v248 offset:12288
v_mfma_f32_16x16x32_bf16 v[80:83], a[112:115], a[100:103], v[80:83]
s_add_u32 s45, s45, 131072
v_add_u32 v244, s53, v244
v_mfma_f32_16x16x32_bf16 v[84:87], a[84:87], a[68:71], v[84:87]
buffer_load_dwordx4 v7, s[4:7], s44 offen offset:1024 sc1 lds
v_mfma_f32_16x16x32_bf16 v[84:87], a[116:119], a[100:103], v[84:87]
s_waitcnt lgkmcnt(8)
v_mfma_f32_16x16x32_bf16 v[96:99], a[80:83], a[72:75], v[96:99]
v_add_u32 v247, s53, v247
v_add_u32 v248, s53, v248
v_mfma_f32_16x16x32_bf16 v[96:99], a[112:115], a[104:107], v[96:99]
v_mfma_f32_16x16x32_bf16 v[100:103], a[84:87], a[72:75], v[100:103]
buffer_load_dwordx4 v7, s[4:7], s44 offen offset:2048 sc1 lds
v_mfma_f32_16x16x32_bf16 v[100:103], a[116:119], a[104:107], v[100:103]
s_add_u32 s49, s46, s91
v_mfma_f32_16x16x32_bf16 v[112:115], a[80:83], a[76:79], v[112:115]
s_add_u32 s46, s46, s53
v_mfma_f32_16x16x32_bf16 v[112:115], a[112:115], a[108:111], v[112:115]
v_mfma_f32_16x16x32_bf16 v[116:119], a[84:87], a[76:79], v[116:119]
buffer_load_dwordx4 v7, s[4:7], s44 offen offset:3072 sc1 lds
v_mfma_f32_16x16x32_bf16 v[116:119], a[116:119], a[108:111], v[116:119]
s_waitcnt vmcnt(28) lgkmcnt(0)
s_barrier
v_mfma_f32_16x16x32_bf16 v[72:75], a[88:91], a[64:67], v[72:75]
ds_read_b64_tr_b16 a[80:81], v245 offset:0
ds_read_b64_tr_b16 a[82:83], v245 offset:4096
v_mfma_f32_16x16x32_bf16 v[72:75], a[120:123], a[96:99], v[72:75]
s_add_u32 m0, s49, 16384
ds_read_b64_tr_b16 a[112:113], v245 offset:8192
ds_read_b64_tr_b16 a[114:115], v245 offset:12288
v_mfma_f32_16x16x32_bf16 v[76:79], a[92:95], a[64:67], v[76:79]
buffer_load_dwordx4 v240, s[8:11], s45 offen offset:0 lds
v_mfma_f32_16x16x32_bf16 v[76:79], a[124:127], a[96:99], v[76:79]
ds_read_b128 a[64:67], v244 offset:0
ds_read_b128 a[96:99], v244 offset:2048
v_mfma_f32_16x16x32_bf16 v[88:91], a[88:91], a[68:71], v[88:91]
ds_read_b64_tr_b16 a[84:85], v246 offset:0
ds_read_b64_tr_b16 a[86:87], v246 offset:4096
v_mfma_f32_16x16x32_bf16 v[88:91], a[120:123], a[100:103], v[88:91]
ds_read_b64_tr_b16 a[116:117], v246 offset:8192
ds_read_b64_tr_b16 a[118:119], v246 offset:12288
v_mfma_f32_16x16x32_bf16 v[92:95], a[92:95], a[68:71], v[92:95]
buffer_load_dwordx4 v241, s[8:11], s45 offen offset:1024 lds
v_mfma_f32_16x16x32_bf16 v[92:95], a[124:127], a[100:103], v[92:95]
ds_read_b128 a[68:71], v244 offset:1024
ds_read_b128 a[100:103], v244 offset:3072
v_mfma_f32_16x16x32_bf16 v[104:107], a[88:91], a[72:75], v[104:107]
s_cmp_eq_u32 s46, 0
s_cselect_b32 s53, s56, s57
v_mfma_f32_16x16x32_bf16 v[104:107], a[120:123], a[104:107], v[104:107]
v_add_u32 v245, s53, v245
v_add_u32 v246, s53, v246
v_mfma_f32_16x16x32_bf16 v[108:111], a[92:95], a[72:75], v[108:111]
buffer_load_dwordx4 v242, s[8:11], s45 offen offset:2048 lds
v_mfma_f32_16x16x32_bf16 v[108:111], a[124:127], a[104:107], v[108:111]
ds_read_b128 a[72:75], v244 offset:4096
ds_read_b128 a[104:107], v244 offset:6144
v_mfma_f32_16x16x32_bf16 v[120:123], a[88:91], a[76:79], v[120:123]
s_add_u32 s44, s44, 4096
v_mfma_f32_16x16x32_bf16 v[120:123], a[120:123], a[108:111], v[120:123]
s_waitcnt lgkmcnt(2)
v_mfma_f32_16x16x32_bf16 v[124:127], a[92:95], a[76:79], v[124:127]
buffer_load_dwordx4 v243, s[8:11], s45 offen offset:3072 lds
v_mfma_f32_16x16x32_bf16 v[124:127], a[124:127], a[108:111], v[124:127]
ds_read_b128 a[76:79], v244 offset:5120
ds_read_b128 a[108:111], v244 offset:7168
v_mfma_f32_16x16x32_bf16 v[64:67], a[80:83], a[64:67], v[64:67]
ds_read_b64_tr_b16 a[88:89], v247 offset:0
ds_read_b64_tr_b16 a[90:91], v247 offset:4096
v_mfma_f32_16x16x32_bf16 v[64:67], a[112:115], a[96:99], v[64:67]
s_mov_b32 m0, s49
ds_read_b64_tr_b16 a[120:121], v247 offset:8192
ds_read_b64_tr_b16 a[122:123], v247 offset:12288
v_mfma_f32_16x16x32_bf16 v[68:71], a[84:87], a[64:67], v[68:71]
buffer_load_dwordx4 v7, s[4:7], s44 offen offset:0 sc1 lds
v_mfma_f32_16x16x32_bf16 v[68:71], a[116:119], a[96:99], v[68:71]
ds_read_b64_tr_b16 a[92:93], v248 offset:0
ds_read_b64_tr_b16 a[94:95], v248 offset:4096
v_mfma_f32_16x16x32_bf16 v[80:83], a[80:83], a[68:71], v[80:83]
ds_read_b64_tr_b16 a[124:125], v248 offset:8192
ds_read_b64_tr_b16 a[126:127], v248 offset:12288
v_mfma_f32_16x16x32_bf16 v[80:83], a[112:115], a[100:103], v[80:83]
s_add_u32 s45, s45, 131072
v_add_u32 v244, s53, v244
v_mfma_f32_16x16x32_bf16 v[84:87], a[84:87], a[68:71], v[84:87]
buffer_load_dwordx4 v7, s[4:7], s44 offen offset:1024 sc1 lds
v_mfma_f32_16x16x32_bf16 v[84:87], a[116:119], a[100:103], v[84:87]
s_waitcnt lgkmcnt(8)
v_mfma_f32_16x16x32_bf16 v[96:99], a[80:83], a[72:75], v[96:99]
v_add_u32 v247, s53, v247
v_add_u32 v248, s53, v248
v_mfma_f32_16x16x32_bf16 v[96:99], a[112:115], a[104:107], v[96:99]
v_mfma_f32_16x16x32_bf16 v[100:103], a[84:87], a[72:75], v[100:103]
buffer_load_dwordx4 v7, s[4:7], s44 offen offset:2048 sc1 lds
v_mfma_f32_16x16x32_bf16 v[100:103], a[116:119], a[104:107], v[100:103]
s_add_u32 s49, s46, s91
v_mfma_f32_16x16x32_bf16 v[112:115], a[80:83], a[76:79], v[112:115]
s_add_u32 s46, s46, s53
v_mfma_f32_16x16x32_bf16 v[112:115], a[112:115], a[108:111], v[112:115]
v_mfma_f32_16x16x32_bf16 v[116:119], a[84:87], a[76:79], v[116:119]
buffer_load_dwordx4 v7, s[4:7], s44 offen offset:3072 sc1 lds
v_mfma_f32_16x16x32_bf16 v[116:119], a[116:119], a[108:111], v[116:119]
s_waitcnt vmcnt(28) lgkmcnt(0)
s_barrier
v_mfma_f32_16x16x32_bf16 v[72:75], a[88:91], a[64:67], v[72:75]
ds_read_b64_tr_b16 a[80:81], v245 offset:0
ds_read_b64_tr_b16 a[82:83], v245 offset:4096
v_mfma_f32_16x16x32_bf16 v[72:75], a[120:123], a[96:99], v[72:75]
s_add_u32 m0, s49, 16384
ds_read_b64_tr_b16 a[112:113], v245 offset:8192
ds_read_b64_tr_b16 a[114:115], v245 offset:12288
v_mfma_f32_16x16x32_bf16 v[76:79], a[92:95], a[64:67], v[76:79]
buffer_load_dwordx4 v240, s[8:11], s45 offen offset:0 lds
v_mfma_f32_16x16x32_bf16 v[76:79], a[124:127], a[96:99], v[76:79]
ds_read_b128 a[64:67], v244 offset:0
ds_read_b128 a[96:99], v244 offset:2048
v_mfma_f32_16x16x32_bf16 v[88:91], a[88:91], a[68:71], v[88:91]
ds_read_b64_tr_b16 a[84:85], v246 offset:0
ds_read_b64_tr_b16 a[86:87], v246 offset:4096
v_mfma_f32_16x16x32_bf16 v[88:91], a[120:123], a[100:103], v[88:91]
ds_read_b64_tr_b16 a[116:117], v246 offset:8192
ds_read_b64_tr_b16 a[118:119], v246 offset:12288
v_mfma_f32_16x16x32_bf16 v[92:95], a[92:95], a[68:71], v[92:95]
buffer_load_dwordx4 v241, s[8:11], s45 offen offset:1024 lds
v_mfma_f32_16x16x32_bf16 v[92:95], a[124:127], a[100:103], v[92:95]
ds_read_b128 a[68:71], v244 offset:1024
ds_read_b128 a[100:103], v244 offset:3072
v_mfma_f32_16x16x32_bf16 v[104:107], a[88:91], a[72:75], v[104:107]
s_cmp_eq_u32 s46, 0
s_cselect_b32 s53, s56, s57
v_mfma_f32_16x16x32_bf16 v[104:107], a[120:123], a[104:107], v[104:107]
v_add_u32 v245, s53, v245
v_add_u32 v246, s53, v246
v_mfma_f32_16x16x32_bf16 v[108:111], a[92:95], a[72:75], v[108:111]
buffer_load_dwordx4 v242, s[8:11], s45 offen offset:2048 lds
v_mfma_f32_16x16x32_bf16 v[108:111], a[124:127], a[104:107], v[108:111]
ds_read_b128 a[72:75], v244 offset:4096
ds_read_b128 a[104:107], v244 offset:6144
v_mfma_f32_16x16x32_bf16 v[120:123], a[88:91], a[76:79], v[120:123]
s_add_u32 s44, s44, 4096
v_mfma_f32_16x16x32_bf16 v[120:123], a[120:123], a[108:111], v[120:123]
s_waitcnt lgkmcnt(2)
v_mfma_f32_16x16x32_bf16 v[124:127], a[92:95], a[76:79], v[124:127]
buffer_load_dwordx4 v243, s[8:11], s45 offen offset:3072 lds
v_mfma_f32_16x16x32_bf16 v[124:127], a[124:127], a[108:111], v[124:127]
ds_read_b128 a[76:79], v244 offset:5120
ds_read_b128 a[108:111], v244 offset:7168
v_mfma_f32_16x16x32_bf16 v[64:67], a[80:83], a[64:67], v[64:67]
ds_read_b64_tr_b16 a[88:89], v247 offset:0
ds_read_b64_tr_b16 a[90:91], v247 offset:4096
v_mfma_f32_16x16x32_bf16 v[64:67], a[112:115], a[96:99], v[64:67]
s_mov_b32 m0, s49
ds_read_b64_tr_b16 a[120:121], v247 offset:8192
ds_read_b64_tr_b16 a[122:123], v247 offset:12288
v_mfma_f32_16x16x32_bf16 v[68:71], a[84:87], a[64:67], v[68:71]
buffer_load_dwordx4 v7, s[4:7], s44 offen offset:0 sc1 lds
v_mfma_f32_16x16x32_bf16 v[68:71], a[116:119], a[96:99], v[68:71]
ds_read_b64_tr_b16 a[92:93], v248 offset:0
ds_read_b64_tr_b16 a[94:95], v248 offset:4096
v_mfma_f32_16x16x32_bf16 v[80:83], a[80:83], a[68:71], v[80:83]
ds_read_b64_tr_b16 a[124:125], v248 offset:8192
ds_read_b64_tr_b16 a[126:127], v248 offset:12288
v_mfma_f32_16x16x32_bf16 v[80:83], a[112:115], a[100:103], v[80:83]
s_add_u32 s45, s45, 131072
v_add_u32 v244, s53, v244
v_mfma_f32_16x16x32_bf16 v[84:87], a[84:87], a[68:71], v[84:87]
buffer_load_dwordx4 v7, s[4:7], s44 offen offset:1024 sc1 lds
v_mfma_f32_16x16x32_bf16 v[84:87], a[116:119], a[100:103], v[84:87]
s_waitcnt lgkmcnt(8)
v_mfma_f32_16x16x32_bf16 v[96:99], a[80:83], a[72:75], v[96:99]
v_add_u32 v247, s53, v247
v_add_u32 v248, s53, v248
v_mfma_f32_16x16x32_bf16 v[96:99], a[112:115], a[104:107], v[96:99]
v_mfma_f32_16x16x32_bf16 v[100:103], a[84:87], a[72:75], v[100:103]
buffer_load_dwordx4 v7, s[4:7], s44 offen offset:2048 sc1 lds
v_mfma_f32_16x16x32_bf16 v[100:103], a[116:119], a[104:107], v[100:103]
s_add_u32 s49, s46, s91
v_mfma_f32_16x16x32_bf16 v[112:115], a[80:83], a[76:79], v[112:115]
s_add_u32 s46, s46, s53
v_mfma_f32_16x16x32_bf16 v[112:115], a[112:115], a[108:111], v[112:115]
v_mfma_f32_16x16x32_bf16 v[116:119], a[84:87], a[76:79], v[116:119]
buffer_load_dwordx4 v7, s[4:7], s44 offen offset:3072 sc1 lds
v_mfma_f32_16x16x32_bf16 v[116:119], a[116:119], a[108:111], v[116:119]
s_waitcnt vmcnt(21)
s_waitcnt vmcnt(27) lgkmcnt(0)
s_barrier
v_mfma_f32_16x16x32_bf16 v[72:75], a[88:91], a[64:67], v[72:75]
ds_read_b64_tr_b16 a[80:81], v245 offset:0
ds_read_b64_tr_b16 a[82:83], v245 offset:4096
v_mfma_f32_16x16x32_bf16 v[72:75], a[120:123], a[96:99], v[72:75]
s_add_u32 m0, s49, 16384
ds_read_b64_tr_b16 a[112:113], v245 offset:8192
ds_read_b64_tr_b16 a[114:115], v245 offset:12288
v_mfma_f32_16x16x32_bf16 v[76:79], a[92:95], a[64:67], v[76:79]
buffer_load_dwordx4 v240, s[8:11], s45 offen offset:0 lds
v_mfma_f32_16x16x32_bf16 v[76:79], a[124:127], a[96:99], v[76:79]
ds_read_b128 a[64:67], v244 offset:0
ds_read_b128 a[96:99], v244 offset:2048
v_mfma_f32_16x16x32_bf16 v[88:91], a[88:91], a[68:71], v[88:91]
ds_read_b64_tr_b16 a[84:85], v246 offset:0
ds_read_b64_tr_b16 a[86:87], v246 offset:4096
v_mfma_f32_16x16x32_bf16 v[88:91], a[120:123], a[100:103], v[88:91]
ds_read_b64_tr_b16 a[116:117], v246 offset:8192
ds_read_b64_tr_b16 a[118:119], v246 offset:12288
v_mfma_f32_16x16x32_bf16 v[92:95], a[92:95], a[68:71], v[92:95]
buffer_load_dwordx4 v241, s[8:11], s45 offen offset:1024 lds
v_mfma_f32_16x16x32_bf16 v[92:95], a[124:127], a[100:103], v[92:95]
ds_read_b128 a[68:71], v244 offset:1024
ds_read_b128 a[100:103], v244 offset:3072
v_mfma_f32_16x16x32_bf16 v[104:107], a[88:91], a[72:75], v[104:107]
s_cmp_eq_u32 s46, 0
s_cselect_b32 s53, s56, s57
v_mfma_f32_16x16x32_bf16 v[104:107], a[120:123], a[104:107], v[104:107]
v_add_u32 v245, s53, v245
v_add_u32 v246, s53, v246
v_mfma_f32_16x16x32_bf16 v[108:111], a[92:95], a[72:75], v[108:111]
s_nop 0
buffer_load_dwordx4 v242, s[8:11], s45 offen offset:2048 lds
v_or_b32 v0, 4, v11
v_mfma_f32_16x16x32_bf16 v[108:111], a[124:127], a[104:107], v[108:111]
ds_read_b128 a[72:75], v244 offset:4096
ds_read_b128 a[104:107], v244 offset:6144
v_mfma_f32_16x16x32_bf16 v[120:123], a[88:91], a[76:79], v[120:123]
s_add_u32 s44, s44, 4096
v_mfma_f32_16x16x32_bf16 v[120:123], a[120:123], a[108:111], v[120:123]
s_waitcnt lgkmcnt(2)
v_mfma_f32_16x16x32_bf16 v[124:127], a[92:95], a[76:79], v[124:127]
buffer_load_dwordx4 v243, s[8:11], s45 offen offset:3072 lds
buffer_store_dword v0, v6, s[60:63], 0 offen sc1
v_mfma_f32_16x16x32_bf16 v[124:127], a[124:127], a[108:111], v[124:127]
ds_read_b128 a[76:79], v244 offset:5120
ds_read_b128 a[108:111], v244 offset:7168
v_mfma_f32_16x16x32_bf16 v[64:67], a[80:83], a[64:67], v[64:67]
ds_read_b64_tr_b16 a[88:89], v247 offset:0
ds_read_b64_tr_b16 a[90:91], v247 offset:4096
v_mfma_f32_16x16x32_bf16 v[64:67], a[112:115], a[96:99], v[64:67]
s_mov_b32 m0, s49
ds_read_b64_tr_b16 a[120:121], v247 offset:8192
ds_read_b64_tr_b16 a[122:123], v247 offset:12288
v_mfma_f32_16x16x32_bf16 v[68:71], a[84:87], a[64:67], v[68:71]
buffer_load_dwordx4 v7, s[4:7], s44 offen offset:0 sc1 lds
v_mfma_f32_16x16x32_bf16 v[68:71], a[116:119], a[96:99], v[68:71]
ds_read_b64_tr_b16 a[92:93], v248 offset:0
ds_read_b64_tr_b16 a[94:95], v248 offset:4096
v_mfma_f32_16x16x32_bf16 v[80:83], a[80:83], a[68:71], v[80:83]
ds_read_b64_tr_b16 a[124:125], v248 offset:8192
ds_read_b64_tr_b16 a[126:127], v248 offset:12288
v_mfma_f32_16x16x32_bf16 v[80:83], a[112:115], a[100:103], v[80:83]
s_add_u32 s45, s45, 131072
v_add_u32 v244, s53, v244
v_mfma_f32_16x16x32_bf16 v[84:87], a[84:87], a[68:71], v[84:87]
buffer_load_dwordx4 v7, s[4:7], s44 offen offset:1024 sc1 lds
v_mfma_f32_16x16x32_bf16 v[84:87], a[116:119], a[100:103], v[84:87]
s_waitcnt lgkmcnt(8)
v_mfma_f32_16x16x32_bf16 v[96:99], a[80:83], a[72:75], v[96:99]
buffer_load_dword v35, v4, s[96:99], 0 offen
v_add_u32 v247, s53, v247
v_add_u32 v248, s53, v248
v_mfma_f32_16x16x32_bf16 v[96:99], a[112:115], a[104:107], v[96:99]
v_mfma_f32_16x16x32_bf16 v[100:103], a[84:87], a[72:75], v[100:103]
buffer_load_dwordx4 v7, s[4:7], s44 offen offset:2048 sc1 lds
v_mfma_f32_16x16x32_bf16 v[100:103], a[116:119], a[104:107], v[100:103]
s_add_u32 s49, s46, s91
v_mfma_f32_16x16x32_bf16 v[112:115], a[80:83], a[76:79], v[112:115]
s_add_u32 s46, s46, s53
v_mfma_f32_16x16x32_bf16 v[112:115], a[112:115], a[108:111], v[112:115]
v_mfma_f32_16x16x32_bf16 v[116:119], a[84:87], a[76:79], v[116:119]
buffer_load_dwordx4 v7, s[4:7], s44 offen offset:3072 sc1 lds
v_mfma_f32_16x16x32_bf16 v[116:119], a[116:119], a[108:111], v[116:119]
s_waitcnt vmcnt(26) lgkmcnt(0)
s_barrier
s_mov_b64 exec, 0
s_cmp_eq_u32 s3, 0
s_cbranch_scc1 .Lpad20
s_cmp_eq_u32 s3, 1
s_cbranch_scc1 .L5odd_pad
s_cmp_eq_u32 s3, 2
s_cbranch_scc1 .Lpad21
s_branch .Lpad22
.Ltch11:
s_mov_b64 exec, -1
s_nop 0
s_nop 0
s_nop 0
s_nop 0
s_nop 0
s_nop 0
s_nop 0
s_nop 0
s_nop 0
s_nop 0
s_nop 0
s_nop 0
s_nop 0
s_nop 0
.L5b:
v_mfma_f32_16x16x32_bf16 v[72:75], a[88:91], a[64:67], v[72:75]
ds_read_b64_tr_b16 a[80:81], v245 offset:0
ds_read_b64_tr_b16 a[82:83], v245 offset:4096
v_mfma_f32_16x16x32_bf16 v[72:75], a[120:123], a[96:99], v[72:75]
s_add_u32 m0, s49, 16384
ds_read_b64_tr_b16 a[112:113], v245 offset:8192
ds_read_b64_tr_b16 a[114:115], v245 offset:12288
v_mfma_f32_16x16x32_bf16 v[76:79], a[92:95], a[64:67], v[76:79]
buffer_load_dwordx4 v240, s[8:11], s45 offen offset:0 lds
v_mfma_f32_16x16x32_bf16 v[76:79], a[124:127], a[96:99], v[76:79]
ds_read_b128 a[64:67], v244 offset:0
ds_read_b128 a[96:99], v244 offset:2048
v_mfma_f32_16x16x32_bf16 v[88:91], a[88:91], a[68:71], v[88:91]
ds_read_b64_tr_b16 a[84:85], v246 offset:0
ds_read_b64_tr_b16 a[86:87], v246 offset:4096
v_mfma_f32_16x16x32_bf16 v[88:91], a[120:123], a[100:103], v[88:91]
ds_read_b64_tr_b16 a[116:117], v246 offset:8192
ds_read_b64_tr_b16 a[118:119], v246 offset:12288
v_mfma_f32_16x16x32_bf16 v[92:95], a[92:95], a[68:71], v[92:95]
buffer_load_dwordx4 v241, s[8:11], s45 offen offset:1024 lds
v_mfma_f32_16x16x32_bf16 v[92:95], a[124:127], a[100:103], v[92:95]
ds_read_b128 a[68:71], v244 offset:1024
ds_read_b128 a[100:103], v244 offset:3072
v_mfma_f32_16x16x32_bf16 v[104:107], a[88:91], a[72:75], v[104:107]
s_cmp_eq_u32 s46, 0
s_cselect_b32 s53, s56, s57
v_mfma_f32_16x16x32_bf16 v[104:107], a[120:123], a[104:107], v[104:107]
v_add_u32 v245, s53, v245
v_add_u32 v246, s53, v246
v_mfma_f32_16x16x32_bf16 v[108:111], a[92:95], a[72:75], v[108:111]
buffer_load_dwordx4 v242, s[8:11], s45 offen offset:2048 lds
v_mfma_f32_16x16x32_bf16 v[108:111], a[124:127], a[104:107], v[108:111]
ds_read_b128 a[72:75], v244 offset:4096
ds_read_b128 a[104:107], v244 offset:6144
v_mfma_f32_16x16x32_bf16 v[120:123], a[88:91], a[76:79], v[120:123]
s_add_u32 s44, s44, 4096
v_mfma_f32_16x16x32_bf16 v[120:123], a[120:123], a[108:111], v[120:123]
s_waitcnt lgkmcnt(2)
v_mfma_f32_16x16x32_bf16 v[124:127], a[92:95], a[76:79], v[124:127]
buffer_load_dwordx4 v243, s[8:11], s45 offen offset:3072 lds
v_mfma_f32_16x16x32_bf16 v[124:127], a[124:127], a[108:111], v[124:127]
ds_read_b128 a[76:79], v244 offset:5120
ds_read_b128 a[108:111], v244 offset:7168
v_mfma_f32_16x16x32_bf16 v[64:67], a[80:83], a[64:67], v[64:67]
ds_read_b64_tr_b16 a[88:89], v247 offset:0
ds_read_b64_tr_b16 a[90:91], v247 offset:4096
v_mfma_f32_16x16x32_bf16 v[64:67], a[112:115], a[96:99], v[64:67]
s_mov_b32 m0, s49
ds_read_b64_tr_b16 a[120:121], v247 offset:8192
ds_read_b64_tr_b16 a[122:123], v247 offset:12288
v_mfma_f32_16x16x32_bf16 v[68:71], a[84:87], a[64:67], v[68:71]
buffer_load_dwordx4 v7, s[4:7], s44 offen offset:0 sc1 lds
v_mfma_f32_16x16x32_bf16 v[68:71], a[116:119], a[96:99], v[68:71]
ds_read_b64_tr_b16 a[92:93], v248 offset:0
ds_read_b64_tr_b16 a[94:95], v248 offset:4096
v_mfma_f32_16x16x32_bf16 v[80:83], a[80:83], a[68:71], v[80:83]
ds_read_b64_tr_b16 a[124:125], v248 offset:8192
ds_read_b64_tr_b16 a[126:127], v248 offset:12288
v_mfma_f32_16x16x32_bf16 v[80:83], a[112:115], a[100:103], v[80:83]
s_add_u32 s45, s45, 131072
v_add_u32 v244, s53, v244
v_mfma_f32_16x16x32_bf16 v[84:87], a[84:87], a[68:71], v[84:87]
buffer_load_dwordx4 v7, s[4:7], s44 offen offset:1024 sc1 lds
v_mfma_f32_16x16x32_bf16 v[84:87], a[116:119], a[100:103], v[84:87]
s_waitcnt lgkmcnt(8)
v_mfma_f32_16x16x32_bf16 v[96:99], a[80:83], a[72:75], v[96:99]
v_add_u32 v247, s53, v247
v_add_u32 v248, s53, v248
v_mfma_f32_16x16x32_bf16 v[96:99], a[112:115], a[104:107], v[96:99]
v_mfma_f32_16x16x32_bf16 v[100:103], a[84:87], a[72:75], v[100:103]
buffer_load_dwordx4 v7, s[4:7], s44 offen offset:2048 sc1 lds
v_mfma_f32_16x16x32_bf16 v[100:103], a[116:119], a[104:107], v[100:103]
s_add_u32 s49, s46, s91
v_mfma_f32_16x16x32_bf16 v[112:115], a[80:83], a[76:79], v[112:115]
s_add_u32 s46, s46, s53
v_mfma_f32_16x16x32_bf16 v[112:115], a[112:115], a[108:111], v[112:115]
s_sub_u32 s50, s50, 1
v_mfma_f32_16x16x32_bf16 v[116:119], a[84:87], a[76:79], v[116:119]
buffer_load_dwordx4 v7, s[4:7], s44 offen offset:3072 sc1 lds
v_mfma_f32_16x16x32_bf16 v[116:119], a[116:119], a[108:111], v[116:119]
s_waitcnt vmcnt(24) lgkmcnt(0)
s_barrier
s_cbranch_scc0 .L5b
s_cmp_eq_u32 s55, 0
s_cbranch_scc0 .L5odd
v_mfma_f32_16x16x32_bf16 v[72:75], a[88:91], a[64:67], v[72:75]
ds_read_b64_tr_b16 a[80:81], v245 offset:0
ds_read_b64_tr_b16 a[82:83], v245 offset:4096
v_mfma_f32_16x16x32_bf16 v[72:75], a[120:123], a[96:99], v[72:75]
buffer_load_dword v250, v249, s[60:63], s90 offen sc1
ds_read_b64_tr_b16 a[112:113], v245 offset:8192
ds_read_b64_tr_b16 a[114:115], v245 offset:12288
v_mfma_f32_16x16x32_bf16 v[76:79], a[92:95], a[64:67], v[76:79]
v_and_b32 v168, 15, v1
v_lshrrev_b32 v169, 4, v1
v_mfma_f32_16x16x32_bf16 v[76:79], a[124:127], a[96:99], v[76:79]
ds_read_b128 a[64:67], v244 offset:0
ds_read_b128 a[96:99], v244 offset:2048
v_mfma_f32_16x16x32_bf16 v[88:91], a[88:91], a[68:71], v[88:91]
s_mov_b32 s32, 4096
ds_read_b64_tr_b16 a[84:85], v246 offset:0
ds_read_b64_tr_b16 a[86:87], v246 offset:4096
v_mfma_f32_16x16x32_bf16 v[88:91], a[120:123], a[100:103], v[88:91]
ds_read_b64_tr_b16 a[116:117], v246 offset:8192
ds_read_b64_tr_b16 a[118:119], v246 offset:12288
v_mfma_f32_16x16x32_bf16 v[92:95], a[92:95], a[68:71], v[92:95]
v_lshlrev_b32 v169, 2, v169
v_sub_u32 v168, v168, v169
v_mfma_f32_16x16x32_bf16 v[92:95], a[124:127], a[100:103], v[92:95]
ds_read_b128 a[68:71], v244 offset:1024
ds_read_b128 a[100:103], v244 offset:3072
v_mfma_f32_16x16x32_bf16 v[104:107], a[88:91], a[72:75], v[104:107]
s_cmp_eq_u32 s46, 0
s_cselect_b32 s53, s56, s57
v_mov_b32 v169, 0x3f80
v_mfma_f32_16x16x32_bf16 v[104:107], a[120:123], a[104:107], v[104:107]
v_add_u32 v245, s53, v245
v_add_u32 v246, s53, v246
v_mfma_f32_16x16x32_bf16 v[108:111], a[92:95], a[72:75], v[108:111]
v_mov_b32 v170, 0x3f800000
v_cmp_eq_u32 s[24:25], 0, v168
v_mfma_f32_16x16x32_bf16 v[108:111], a[124:127], a[104:107], v[108:111]
ds_read_b128 a[72:75], v244 offset:4096
ds_read_b128 a[104:107], v244 offset:6144
v_mfma_f32_16x16x32_bf16 v[120:123], a[88:91], a[76:79], v[120:123]
v_cmp_eq_u32 s[26:27], 1, v168
v_cmp_eq_u32 s[28:29], 2, v168
v_mfma_f32_16x16x32_bf16 v[120:123], a[120:123], a[108:111], v[120:123]
s_waitcnt lgkmcnt(2)
v_cmp_eq_u32 s[30:31], 3, v168
v_cndmask_b32 v160, 0, v169, s[24:25]
.Lpad20:
s_cbranch_execz .Ltch11
v_mfma_f32_16x16x32_bf16 v[124:127], a[92:95], a[76:79], v[124:127]
v_cndmask_b32 v161, 0, v169, s[28:29]
v_cndmask_b32 v160, v160, v170, s[26:27]
v_mfma_f32_16x16x32_bf16 v[124:127], a[124:127], a[108:111], v[124:127]
ds_read_b128 a[76:79], v244 offset:5120
ds_read_b128 a[108:111], v244 offset:7168
v_mfma_f32_16x16x32_bf16 v[64:67], a[80:83], a[64:67], v[64:67]
ds_read_b64_tr_b16 a[88:89], v247 offset:0
ds_read_b64_tr_b16 a[90:91], v247 offset:4096
v_mfma_f32_16x16x32_bf16 v[64:67], a[112:115], a[96:99], v[64:67]
ds_read_b64_tr_b16 a[120:121], v247 offset:8192
ds_read_b64_tr_b16 a[122:123], v247 offset:12288
v_mfma_f32_16x16x32_bf16 v[68:71], a[84:87], a[64:67], v[68:71]
v_cndmask_b32 v161, v161, v170, s[30:31]
v_mov_b32 v162, 0
v_mfma_f32_16x16x32_bf16 v[68:71], a[116:119], a[96:99], v[68:71]
ds_read_b64_tr_b16 a[92:93], v248 offset:0
ds_read_b64_tr_b16 a[94:95], v248 offset:4096
v_mfma_f32_16x16x32_bf16 v[80:83], a[80:83], a[68:71], v[80:83]
ds_read_b64_tr_b16 a[124:125], v248 offset:8192
ds_read_b64_tr_b16 a[126:127], v248 offset:12288
v_mfma_f32_16x16x32_bf16 v[80:83], a[112:115], a[100:103], v[80:83]
v_add_u32 v244, s53, v244
v_mov_b32 v163, 0
v_mfma_f32_16x16x32_bf16 v[84:87], a[84:87], a[68:71], v[84:87]
v_mov_b32 v164, 0
v_mov_b32 v165, 0
v_mfma_f32_16x16x32_bf16 v[84:87], a[116:119], a[100:103], v[84:87]
s_waitcnt lgkmcnt(8)
v_mov_b32 v166, v160
v_mov_b32 v167, v161
v_mfma_f32_16x16x32_bf16 v[96:99], a[80:83], a[72:75], v[96:99]
v_add_u32 v247, s53, v247
v_add_u32 v248, s53, v248
v_mfma_f32_16x16x32_bf16 v[96:99], a[112:115], a[104:107], v[96:99]
v_mfma_f32_16x16x32_bf16 v[100:103], a[84:87], a[72:75], v[100:103]
v_mfma_f32_16x16x32_bf16 v[100:103], a[116:119], a[104:107], v[100:103]
s_add_u32 s49, s46, s91
v_mfma_f32_16x16x32_bf16 v[112:115], a[80:83], a[76:79], v[112:115]
s_add_u32 s46, s46, s53
v_mfma_f32_16x16x32_bf16 v[112:115], a[112:115], a[108:111], v[112:115]
v_mfma_f32_16x16x32_bf16 v[116:119], a[84:87], a[76:79], v[116:119]
v_mfma_f32_16x16x32_bf16 v[116:119], a[116:119], a[108:111], v[116:119]
s_waitcnt vmcnt(17) lgkmcnt(0)
s_barrier
v_mfma_f32_16x16x32_bf16 v[72:75], a[88:91], a[64:67], v[72:75]
ds_read_b64_tr_b16 a[80:81], v245 offset:0
ds_read_b64_tr_b16 a[82:83], v245 offset:4096
v_mfma_f32_16x16x32_bf16 v[72:75], a[120:123], a[96:99], v[72:75]
ds_read_b64_tr_b16 a[112:113], v245 offset:8192
ds_read_b64_tr_b16 a[114:115], v245 offset:12288
v_mfma_f32_16x16x32_bf16 v[76:79], a[92:95], a[64:67], v[76:79]
v_mfma_f32_16x16x32_bf16 v[76:79], a[124:127], a[96:99], v[76:79]
ds_read_b128 a[64:67], v244 offset:0
ds_read_b128 a[96:99], v244 offset:2048
v_mfma_f32_16x16x32_bf16 v[88:91], a[88:91], a[68:71], v[88:91]
ds_read_b64_tr_b16 a[84:85], v246 offset:0
ds_read_b64_tr_b16 a[86:87], v246 offset:4096
v_mfma_f32_16x16x32_bf16 v[88:91], a[120:123], a[100:103], v[88:91]
ds_read_b64_tr_b16 a[116:117], v246 offset:8192
ds_read_b64_tr_b16 a[118:119], v246 offset:12288
v_mfma_f32_16x16x32_bf16 v[92:95], a[92:95], a[68:71], v[92:95]
v_mfma_f32_16x16x32_bf16 v[92:95], a[124:127], a[100:103], v[92:95]
ds_read_b128 a[68:71], v244 offset:1024
ds_read_b128 a[100:103], v244 offset:3072
v_mfma_f32_16x16x32_bf16 v[104:107], a[88:91], a[72:75], v[104:107]
s_cmp_eq_u32 s46, 0
s_cselect_b32 s53, s56, s57
v_mfma_f32_16x16x32_bf16 v[104:107], a[120:123], a[104:107], v[104:107]
v_add_u32 v245, s53, v245
v_add_u32 v246, s53, v246
v_mfma_f32_16x16x32_bf16 v[108:111], a[92:95], a[72:75], v[108:111]
s_waitcnt vmcnt(0)
v_and_b32 v250, 7, v250
v_mfma_f32_16x16x32_bf16 v[108:111], a[124:127], a[104:107], v[108:111]
ds_read_b128 a[72:75], v244 offset:4096
ds_read_b128 a[104:107], v244 offset:6144
v_cmp_gt_u32 vcc, 5, v250
v_mfma_f32_16x16x32_bf16 v[120:123], a[88:91], a[76:79], v[120:123]
v_mfma_f32_16x16x32_bf16 v[120:123], a[120:123], a[108:111], v[120:123]
s_waitcnt lgkmcnt(2)
s_cmp_lg_u64 vcc, 0
s_cbranch_scc1 .Lspin5
.Lspin5_ret:
v_mfma_f32_16x16x32_bf16 v[124:127], a[92:95], a[76:79], v[124:127]
v_mfma_f32_16x16x32_bf16 v[124:127], a[124:127], a[108:111], v[124:127]
ds_read_b128 a[76:79], v244 offset:5120
ds_read_b128 a[108:111], v244 offset:7168
v_mfma_f32_16x16x32_bf16 v[64:67], a[80:83], a[64:67], v[64:67]
ds_read_b64_tr_b16 a[88:89], v247 offset:0
ds_read_b64_tr_b16 a[90:91], v247 offset:4096
v_mfma_f32_16x16x32_bf16 v[64:67], a[112:115], a[96:99], v[64:67]
buffer_load_dwordx4 v[128:131], v7, s[12:15], 0 offen offset:0 sc1
ds_read_b64_tr_b16 a[120:121], v247 offset:8192
ds_read_b64_tr_b16 a[122:123], v247 offset:12288
v_mfma_f32_16x16x32_bf16 v[68:71], a[84:87], a[64:67], v[68:71]
v_mfma_f32_16x16x32_bf16 v[68:71], a[116:119], a[96:99], v[68:71]
ds_read_b64_tr_b16 a[92:93], v248 offset:0
ds_read_b64_tr_b16 a[94:95], v248 offset:4096
v_mfma_f32_16x16x32_bf16 v[80:83], a[80:83], a[68:71], v[80:83]
buffer_load_dwordx4 v[132:135], v7, s[12:15], 0 offen offset:1024 sc1
ds_read_b64_tr_b16 a[124:125], v248 offset:8192
ds_read_b64_tr_b16 a[126:127], v248 offset:12288
v_mfma_f32_16x16x32_bf16 v[80:83], a[112:115], a[100:103], v[80:83]
v_add_u32 v244, s53, v244
v_mfma_f32_16x16x32_bf16 v[84:87], a[84:87], a[68:71], v[84:87]
v_mfma_f32_16x16x32_bf16 v[84:87], a[116:119], a[100:103], v[84:87]
s_waitcnt lgkmcnt(8)
buffer_load_dwordx4 v[136:139], v7, s[12:15], 0 offen offset:2048 sc1
v_mfma_f32_16x16x32_bf16 v[96:99], a[80:83], a[72:75], v[96:99]
v_add_u32 v247, s53, v247
v_add_u32 v248, s53, v248
v_mfma_f32_16x16x32_bf16 v[96:99], a[112:115], a[104:107], v[96:99]
v_mfma_f32_16x16x32_bf16 v[100:103], a[84:87], a[72:75], v[100:103]
buffer_load_dwordx4 v[140:143], v7, s[12:15], 0 offen offset:3072 sc1
v_mfma_f32_16x16x32_bf16 v[100:103], a[116:119], a[104:107], v[100:103]
s_add_u32 s49, s46, s91
v_mfma_f32_16x16x32_bf16 v[112:115], a[80:83], a[76:79], v[112:115]
s_add_u32 s46, s46, s53
v_mfma_f32_16x16x32_bf16 v[112:115], a[112:115], a[108:111], v[112:115]
v_mfma_f32_16x16x32_bf16 v[116:119], a[84:87], a[76:79], v[116:119]
v_mfma_f32_16x16x32_bf16 v[116:119], a[116:119], a[108:111], v[116:119]
s_waitcnt vmcnt(13) lgkmcnt(0)
s_barrier
v_mfma_f32_16x16x32_bf16 v[72:75], a[88:91], a[64:67], v[72:75]
ds_read_b64_tr_b16 a[80:81], v245 offset:0
ds_read_b64_tr_b16 a[82:83], v245 offset:4096
v_mfma_f32_16x16x32_bf16 v[72:75], a[120:123], a[96:99], v[72:75]
buffer_load_dwordx4 v[144:147], v7, s[12:15], s32 offen offset:0 sc1
ds_read_b64_tr_b16 a[112:113], v245 offset:8192
ds_read_b64_tr_b16 a[114:115], v245 offset:12288
v_mfma_f32_16x16x32_bf16 v[76:79], a[92:95], a[64:67], v[76:79]
v_mfma_f32_16x16x32_bf16 v[76:79], a[124:127], a[96:99], v[76:79]
ds_read_b128 a[64:67], v244 offset:0
ds_read_b128 a[96:99], v244 offset:2048
v_mfma_f32_16x16x32_bf16 v[88:91], a[88:91], a[68:71], v[88:91]
buffer_load_dwordx4 v[148:151], v7, s[12:15], s32 offen offset:1024 sc1
ds_read_b64_tr_b16 a[84:85], v246 offset:0
ds_read_b64_tr_b16 a[86:87], v246 offset:4096
v_mfma_f32_16x16x32_bf16 v[88:91], a[120:123], a[100:103], v[88:91]
ds_read_b64_tr_b16 a[116:117], v246 offset:8192
ds_read_b64_tr_b16 a[118:119], v246 offset:12288
v_mfma_f32_16x16x32_bf16 v[92:95], a[92:95], a[68:71], v[92:95]
v_mfma_f32_16x16x32_bf16 v[92:95], a[124:127], a[100:103], v[92:95]
ds_read_b128 a[68:71], v244 offset:1024
ds_read_b128 a[100:103], v244 offset:3072
v_mfma_f32_16x16x32_bf16 v[104:107], a[88:91], a[72:75], v[104:107]
s_cmp_eq_u32 s46, 0
s_cselect_b32 s53, s56, s57
v_mfma_f32_16x16x32_bf16 v[104:107], a[120:123], a[104:107], v[104:107]
buffer_load_dwordx4 v[152:155], v7, s[12:15], s32 offen offset:2048 sc1
v_add_u32 v245, s53, v245
v_add_u32 v246, s53, v246
v_mfma_f32_16x16x32_bf16 v[108:111], a[92:95], a[72:75], v[108:111]
v_mfma_f32_16x16x32_bf16 v[108:111], a[124:127], a[104:107], v[108:111]
ds_read_b128 a[72:75], v244 offset:4096
ds_read_b128 a[104:107], v244 offset:6144
v_mfma_f32_16x16x32_bf16 v[120:123], a[88:91], a[76:79], v[120:123]
buffer_load_dwordx4 v[156:159], v7, s[12:15], s32 offen offset:3072 sc1
v_mfma_f32_16x16x32_bf16 v[120:123], a[120:123], a[108:111], v[120:123]
s_waitcnt lgkmcnt(2)
v_mfma_f32_16x16x32_bf16 v[124:127], a[92:95], a[76:79], v[124:127]
v_mfma_f32_16x16x32_bf16 v[124:127], a[124:127], a[108:111], v[124:127]
ds_read_b128 a[76:79], v244 offset:5120
ds_read_b128 a[108:111], v244 offset:7168
v_mfma_f32_16x16x32_bf16 v[64:67], a[80:83], a[64:67], v[64:67]
ds_read_b64_tr_b16 a[88:89], v247 offset:0
ds_read_b64_tr_b16 a[90:91], v247 offset:4096
v_mfma_f32_16x16x32_bf16 v[64:67], a[112:115], a[96:99], v[64:67]
ds_read_b64_tr_b16 a[120:121], v247 offset:8192
ds_read_b64_tr_b16 a[122:123], v247 offset:12288
v_mfma_f32_16x16x32_bf16 v[68:71], a[84:87], a[64:67], v[68:71]
v_mfma_f32_16x16x32_bf16 v[68:71], a[116:119], a[96:99], v[68:71]
ds_read_b64_tr_b16 a[92:93], v248 offset:0
ds_read_b64_tr_b16 a[94:95], v248 offset:4096
v_mfma_f32_16x16x32_bf16 v[80:83], a[80:83], a[68:71], v[80:83]
ds_read_b64_tr_b16 a[124:125], v248 offset:8192
ds_read_b64_tr_b16 a[126:127], v248 offset:12288
v_mfma_f32_16x16x32_bf16 v[80:83], a[112:115], a[100:103], v[80:83]
v_add_u32 v244, s53, v244
v_mfma_f32_16x16x32_bf16 v[84:87], a[84:87], a[68:71], v[84:87]
v_mfma_f32_16x16x32_bf16 v[84:87], a[116:119], a[100:103], v[84:87]
s_waitcnt lgkmcnt(8)
v_mfma_f32_16x16x32_bf16 v[96:99], a[80:83], a[72:75], v[96:99]
v_add_u32 v247, s53, v247
v_add_u32 v248, s53, v248
v_mfma_f32_16x16x32_bf16 v[96:99], a[112:115], a[104:107], v[96:99]
v_mfma_f32_16x16x32_bf16 v[100:103], a[84:87], a[72:75], v[100:103]
v_mfma_f32_16x16x32_bf16 v[100:103], a[116:119], a[104:107], v[100:103]
s_add_u32 s49, s46, s91
v_mfma_f32_16x16x32_bf16 v[112:115], a[80:83], a[76:79], v[112:115]
s_add_u32 s46, s46, s53
v_mfma_f32_16x16x32_bf16 v[112:115], a[112:115], a[108:111], v[112:115]
v_mfma_f32_16x16x32_bf16 v[116:119], a[84:87], a[76:79], v[116:119]
v_mfma_f32_16x16x32_bf16 v[116:119], a[116:119], a[108:111], v[116:119]
s_waitcnt vmcnt(9) lgkmcnt(0)
s_barrier
v_mfma_f32_16x16x32_bf16 v[72:75], a[88:91], a[64:67], v[72:75]
ds_read_b64_tr_b16 a[80:81], v245 offset:0
ds_read_b64_tr_b16 a[82:83], v245 offset:4096
v_mfma_f32_16x16x32_bf16 v[72:75], a[120:123], a[96:99], v[72:75]
ds_read_b64_tr_b16 a[112:113], v245 offset:8192
ds_read_b64_tr_b16 a[114:115], v245 offset:12288
v_mfma_f32_16x16x32_bf16 v[76:79], a[92:95], a[64:67], v[76:79]
ds_read_b64_tr_b16 a[128:129], v247 offset:0
ds_read_b64_tr_b16 a[130:131], v247 offset:4096
v_mfma_f32_16x16x32_bf16 v[76:79], a[124:127], a[96:99], v[76:79]
ds_read_b128 a[64:67], v244 offset:0
ds_read_b128 a[96:99], v244 offset:2048
v_mfma_f32_16x16x32_bf16 v[88:91], a[88:91], a[68:71], v[88:91]
ds_read_b64_tr_b16 a[84:85], v246 offset:0
ds_read_b64_tr_b16 a[86:87], v246 offset:4096
v_mfma_f32_16x16x32_bf16 v[88:91], a[120:123], a[100:103], v[88:91]
ds_read_b64_tr_b16 a[116:117], v246 offset:8192
ds_read_b64_tr_b16 a[118:119], v246 offset:12288
v_mfma_f32_16x16x32_bf16 v[92:95], a[92:95], a[68:71], v[92:95]
ds_read_b64_tr_b16 a[136:137], v247 offset:8192
ds_read_b64_tr_b16 a[138:139], v247 offset:12288
v_mfma_f32_16x16x32_bf16 v[92:95], a[124:127], a[100:103], v[92:95]
ds_read_b128 a[68:71], v244 offset:1024
ds_read_b128 a[100:103], v244 offset:3072
v_mfma_f32_16x16x32_bf16 v[104:107], a[88:91], a[72:75], v[104:107]
ds_read_b64_tr_b16 a[132:133], v248 offset:0
ds_read_b64_tr_b16 a[134:135], v248 offset:4096
v_mfma_f32_16x16x32_bf16 v[104:107], a[120:123], a[104:107], v[104:107]
ds_read_b64_tr_b16 a[140:141], v248 offset:8192
ds_read_b64_tr_b16 a[142:143], v248 offset:12288
v_mfma_f32_16x16x32_bf16 v[108:111], a[92:95], a[72:75], v[108:111]
s_mov_b32 s24, 0
s_mov_b32 s25, 32768
s_mov_b32 s26, 65536
v_mfma_f32_16x16x32_bf16 v[108:111], a[124:127], a[104:107], v[108:111]
ds_read_b128 a[72:75], v244 offset:4096
ds_read_b128 a[104:107], v244 offset:6144
v_mfma_f32_16x16x32_bf16 v[120:123], a[88:91], a[76:79], v[120:123]
s_mov_b32 s27, 98304
s_mov_b32 s28, 131072
s_mov_b32 s29, 163840
v_mfma_f32_16x16x32_bf16 v[120:123], a[120:123], a[108:111], v[120:123]
s_waitcnt lgkmcnt(6)
v_mfma_f32_16x16x32_bf16 v[124:127], a[92:95], a[76:79], v[124:127]
s_mov_b32 s30, 196608
s_mov_b32 s31, 229376
v_mfma_f32_16x16x32_bf16 v[124:127], a[124:127], a[108:111], v[124:127]
s_waitcnt vmcnt(0) lgkmcnt(0)
ds_read_b128 a[76:79], v244 offset:5120
ds_read_b128 a[108:111], v244 offset:7168
v_mfma_f32_16x16x32_bf16 v[64:67], v[160:163], v[128:131], v[64:67]
v_mfma_f32_16x16x32_bf16 v[64:67], a[80:83], a[64:67], v[64:67]
v_mfma_f32_16x16x32_bf16 v[64:67], a[112:115], a[96:99], v[64:67]
v_mfma_f32_16x16x32_bf16 v[68:71], v[164:167], v[128:131], v[68:71]
v_mfma_f32_16x16x32_bf16 v[68:71], a[84:87], a[64:67], v[68:71]
v_mfma_f32_16x16x32_bf16 v[68:71], a[116:119], a[96:99], v[68:71]
v_mfma_f32_16x16x32_bf16 v[72:75], v[160:163], v[132:135], v[72:75]
v_mfma_f32_16x16x32_bf16 v[72:75], a[128:131], a[64:67], v[72:75]
v_mfma_f32_16x16x32_bf16 v[72:75], a[136:139], a[96:99], v[72:75]
v_mfma_f32_16x16x32_bf16 v[76:79], v[164:167], v[132:135], v[76:79]
v_mfma_f32_16x16x32_bf16 v[76:79], a[132:135], a[64:67], v[76:79]
v_mfma_f32_16x16x32_bf16 v[76:79], a[140:143], a[96:99], v[76:79]
v_mfma_f32_16x16x32_bf16 v[80:83], v[160:163], v[136:139], v[80:83]
v_mfma_f32_16x16x32_bf16 v[80:83], a[80:83], a[68:71], v[80:83]
v_mfma_f32_16x16x32_bf16 v[80:83], a[112:115], a[100:103], v[80:83]
v_cvt_pk_bf16_f32 v192, v64, v65
v_cvt_pk_bf16_f32 v194, v68, v69
v_cvt_pk_bf16_f32 v193, v66, v67
v_mfma_f32_16x16x32_bf16 v[84:87], v[164:167], v[136:139], v[84:87]
v_cvt_pk_bf16_f32 v195, v70, v71
v_cvt_pk_bf16_f32 v196, v72, v73
v_cvt_pk_bf16_f32 v198, v76, v77
v_mfma_f32_16x16x32_bf16 v[84:87], a[84:87], a[68:71], v[84:87]
v_cvt_pk_bf16_f32 v197, v74, v75
v_cvt_pk_bf16_f32 v199, v78, v79
v_mfma_f32_16x16x32_bf16 v[84:87], a[116:119], a[100:103], v[84:87]
v_permlane16_swap_b32 v192, v194
v_mfma_f32_16x16x32_bf16 v[88:91], v[160:163], v[140:143], v[88:91]
v_permlane16_swap_b32 v193, v195
v_mfma_f32_16x16x32_bf16 v[88:91], a[128:131], a[68:71], v[88:91]
v_permlane16_swap_b32 v196, v198
v_mfma_f32_16x16x32_bf16 v[88:91], a[136:139], a[100:103], v[88:91]
v_permlane16_swap_b32 v197, v199
v_mov_b32 v200, v192
v_mfma_f32_16x16x32_bf16 v[92:95], v[164:167], v[140:143], v[92:95]
v_mov_b32 v201, v193
v_mov_b32 v202, v194
v_mov_b32 v203, v195
v_mfma_f32_16x16x32_bf16 v[92:95], a[132:135], a[68:71], v[92:95]
v_mov_b32_dpp v192, v196 row_ror:8 row_mask:0xf bank_mask:0xc
v_mov_b32_dpp v193, v197 row_ror:8 row_mask:0xf bank_mask:0xc
v_mov_b32_dpp v194, v198 row_ror:8 row_mask:0xf bank_mask:0xc
v_mfma_f32_16x16x32_bf16 v[92:95], a[140:143], a[100:103], v[92:95]
v_mov_b32_dpp v195, v199 row_ror:8 row_mask:0xf bank_mask:0xc
v_mfma_f32_16x16x32_bf16 v[96:99], v[160:163], v[144:147], v[96:99]
s_waitcnt lgkmcnt(0)
buffer_store_dwordx4 v[192:195], v251, s[16:19], s24 offen nt
v_mov_b32_dpp v196, v200 row_ror:8 row_mask:0xf bank_mask:0x3
v_mfma_f32_16x16x32_bf16 v[96:99], a[80:83], a[72:75], v[96:99]
v_mov_b32_dpp v197, v201 row_ror:8 row_mask:0xf bank_mask:0x3
v_mov_b32_dpp v198, v202 row_ror:8 row_mask:0xf bank_mask:0x3
v_mov_b32_dpp v199, v203 row_ror:8 row_mask:0xf bank_mask:0x3
v_mfma_f32_16x16x32_bf16 v[96:99], a[112:115], a[104:107], v[96:99]
buffer_store_dwordx4 v[196:199], v251, s[16:19], s25 offen nt
v_cvt_pk_bf16_f32 v204, v80, v81
v_cvt_pk_bf16_f32 v206, v84, v85
v_mfma_f32_16x16x32_bf16 v[100:103], v[164:167], v[144:147], v[100:103]
v_cvt_pk_bf16_f32 v205, v82, v83
v_cvt_pk_bf16_f32 v207, v86, v87
v_cvt_pk_bf16_f32 v208, v88, v89
v_mfma_f32_16x16x32_bf16 v[100:103], a[84:87], a[72:75], v[100:103]
v_cvt_pk_bf16_f32 v210, v92, v93
v_cvt_pk_bf16_f32 v209, v90, v91
v_cvt_pk_bf16_f32 v211, v94, v95
v_mfma_f32_16x16x32_bf16 v[100:103], a[116:119], a[104:107], v[100:103]
v_permlane16_swap_b32 v204, v206
v_mfma_f32_16x16x32_bf16 v[104:107], v[160:163], v[148:151], v[104:107]
v_permlane16_swap_b32 v205, v207
v_mfma_f32_16x16x32_bf16 v[104:107], a[128:131], a[72:75], v[104:107]
v_permlane16_swap_b32 v208, v210
v_mfma_f32_16x16x32_bf16 v[104:107], a[136:139], a[104:107], v[104:107]
v_permlane16_swap_b32 v209, v211
v_mov_b32 v212, v204
v_mfma_f32_16x16x32_bf16 v[108:111], v[164:167], v[148:151], v[108:111]
v_mov_b32 v213, v205
v_mov_b32 v214, v206
v_mov_b32 v215, v207
v_mfma_f32_16x16x32_bf16 v[108:111], a[132:135], a[72:75], v[108:111]
v_mov_b32_dpp v204, v208 row_ror:8 row_mask:0xf bank_mask:0xc
v_mov_b32_dpp v205, v209 row_ror:8 row_mask:0xf bank_mask:0xc
v_mov_b32_dpp v206, v210 row_ror:8 row_mask:0xf bank_mask:0xc
v_mfma_f32_16x16x32_bf16 v[108:111], a[140:143], a[104:107], v[108:111]
v_mov_b32_dpp v207, v211 row_ror:8 row_mask:0xf bank_mask:0xc
v_mfma_f32_16x16x32_bf16 v[112:115], v[160:163], v[152:155], v[112:115]
buffer_store_dwordx4 v[204:207], v251, s[16:19], s26 offen nt
v_mov_b32_dpp v208, v212 row_ror:8 row_mask:0xf bank_mask:0x3
v_mov_b32_dpp v209, v213 row_ror:8 row_mask:0xf bank_mask:0x3
v_mfma_f32_16x16x32_bf16 v[112:115], a[80:83], a[76:79], v[112:115]
v_mov_b32_dpp v210, v214 row_ror:8 row_mask:0xf bank_mask:0x3
v_mov_b32_dpp v211, v215 row_ror:8 row_mask:0xf bank_mask:0x3
v_mfma_f32_16x16x32_bf16 v[112:115], a[112:115], a[108:111], v[112:115]
buffer_store_dwordx4 v[208:211], v251, s[16:19], s27 offen nt
v_cvt_pk_bf16_f32 v192, v96, v97
v_cvt_pk_bf16_f32 v194, v100, v101
v_mfma_f32_16x16x32_bf16 v[116:119], v[164:167], v[152:155], v[116:119]
v_cvt_pk_bf16_f32 v193, v98, v99
v_cvt_pk_bf16_f32 v195, v102, v103
v_cvt_pk_bf16_f32 v196, v104, v105
v_mfma_f32_16x16x32_bf16 v[116:119], a[84:87], a[76:79], v[116:119]
v_cvt_pk_bf16_f32 v198, v108, v109
v_cvt_pk_bf16_f32 v197, v106, v107
v_cvt_pk_bf16_f32 v199, v110, v111
v_mfma_f32_16x16x32_bf16 v[116:119], a[116:119], a[108:111], v[116:119]
v_permlane16_swap_b32 v192, v194
v_mfma_f32_16x16x32_bf16 v[120:123], v[160:163], v[156:159], v[120:123]
v_permlane16_swap_b32 v193, v195
v_mfma_f32_16x16x32_bf16 v[120:123], a[128:131], a[76:79], v[120:123]
v_permlane16_swap_b32 v196, v198
v_mfma_f32_16x16x32_bf16 v[120:123], a[136:139], a[108:111], v[120:123]
v_permlane16_swap_b32 v197, v199
v_mov_b32 v200, v192
v_mfma_f32_16x16x32_bf16 v[124:127], v[164:167], v[156:159], v[124:127]
v_mov_b32 v201, v193
v_mov_b32 v202, v194
v_mov_b32 v203, v195
v_mfma_f32_16x16x32_bf16 v[124:127], a[132:135], a[76:79], v[124:127]
v_mov_b32_dpp v192, v196 row_ror:8 row_mask:0xf bank_mask:0xc
v_mov_b32_dpp v193, v197 row_ror:8 row_mask:0xf bank_mask:0xc
v_mov_b32_dpp v194, v198 row_ror:8 row_mask:0xf bank_mask:0xc
v_mfma_f32_16x16x32_bf16 v[124:127], a[140:143], a[108:111], v[124:127]
v_mov_b32_dpp v195, v199 row_ror:8 row_mask:0xf bank_mask:0xc
buffer_store_dwordx4 v[192:195], v251, s[16:19], s28 offen nt
v_mov_b32_dpp v196, v200 row_ror:8 row_mask:0xf bank_mask:0x3
v_mov_b32_dpp v197, v201 row_ror:8 row_mask:0xf bank_mask:0x3
v_mov_b32_dpp v198, v202 row_ror:8 row_mask:0xf bank_mask:0x3
v_mov_b32_dpp v199, v203 row_ror:8 row_mask:0xf bank_mask:0x3
buffer_store_dwordx4 v[196:199], v251, s[16:19], s29 offen nt
v_cvt_pk_bf16_f32 v204, v112, v113
v_cvt_pk_bf16_f32 v206, v116, v117
v_cvt_pk_bf16_f32 v205, v114, v115
v_cvt_pk_bf16_f32 v207, v118, v119
v_cvt_pk_bf16_f32 v208, v120, v121
v_cvt_pk_bf16_f32 v210, v124, v125
v_cvt_pk_bf16_f32 v209, v122, v123
v_cvt_pk_bf16_f32 v211, v126, v127
v_permlane16_swap_b32 v204, v206
v_permlane16_swap_b32 v205, v207
v_permlane16_swap_b32 v208, v210
v_permlane16_swap_b32 v209, v211
v_mov_b32 v212, v204
v_mov_b32 v213, v205
v_mov_b32 v214, v206
v_mov_b32 v215, v207
v_mov_b32_dpp v204, v208 row_ror:8 row_mask:0xf bank_mask:0xc
v_mov_b32_dpp v205, v209 row_ror:8 row_mask:0xf bank_mask:0xc
v_mov_b32_dpp v206, v210 row_ror:8 row_mask:0xf bank_mask:0xc
v_mov_b32_dpp v207, v211 row_ror:8 row_mask:0xf bank_mask:0xc
buffer_store_dwordx4 v[204:207], v251, s[16:19], s30 offen nt
v_mov_b32_dpp v208, v212 row_ror:8 row_mask:0xf bank_mask:0x3
v_mov_b32_dpp v209, v213 row_ror:8 row_mask:0xf bank_mask:0x3
v_mov_b32_dpp v210, v214 row_ror:8 row_mask:0xf bank_mask:0x3
v_mov_b32_dpp v211, v215 row_ror:8 row_mask:0xf bank_mask:0x3
buffer_store_dwordx4 v[208:211], v251, s[16:19], s31 offen nt
s_branch .Lend
.L5odd_pad:
s_cbranch_execz .Ltch11
.L5odd:
v_mfma_f32_16x16x32_bf16 v[72:75], a[88:91], a[64:67], v[72:75]
ds_read_b64_tr_b16 a[80:81], v245 offset:0
ds_read_b64_tr_b16 a[82:83], v245 offset:4096
v_mfma_f32_16x16x32_bf16 v[72:75], a[120:123], a[96:99], v[72:75]
s_add_u32 m0, s49, 16384
ds_read_b64_tr_b16 a[112:113], v245 offset:8192
ds_read_b64_tr_b16 a[114:115], v245 offset:12288
v_mfma_f32_16x16x32_bf16 v[76:79], a[92:95], a[64:67], v[76:79]
buffer_load_dwordx4 v240, s[8:11], s45 offen offset:0 lds
v_mfma_f32_16x16x32_bf16 v[76:79], a[124:127], a[96:99], v[76:79]
ds_read_b128 a[64:67], v244 offset:0
ds_read_b128 a[96:99], v244 offset:2048
v_mfma_f32_16x16x32_bf16 v[88:91], a[88:91], a[68:71], v[88:91]
ds_read_b64_tr_b16 a[84:85], v246 offset:0
ds_read_b64_tr_b16 a[86:87], v246 offset:4096
v_mfma_f32_16x16x32_bf16 v[88:91], a[120:123], a[100:103], v[88:91]
ds_read_b64_tr_b16 a[116:117], v246 offset:8192
ds_read_b64_tr_b16 a[118:119], v246 offset:12288
v_mfma_f32_16x16x32_bf16 v[92:95], a[92:95], a[68:71], v[92:95]
buffer_load_dwordx4 v241, s[8:11], s45 offen offset:1024 lds
v_mfma_f32_16x16x32_bf16 v[92:95], a[124:127], a[100:103], v[92:95]
buffer_load_dword v250, v249, s[60:63], s90 offen sc1
ds_read_b128 a[68:71], v244 offset:1024
ds_read_b128 a[100:103], v244 offset:3072
v_mfma_f32_16x16x32_bf16 v[104:107], a[88:91], a[72:75], v[104:107]
s_cmp_eq_u32 s46, 0
s_cselect_b32 s53, s56, s57
v_mfma_f32_16x16x32_bf16 v[104:107], a[120:123], a[104:107], v[104:107]
v_add_u32 v245, s53, v245
v_add_u32 v246, s53, v246
v_mfma_f32_16x16x32_bf16 v[108:111], a[92:95], a[72:75], v[108:111]
buffer_load_dwordx4 v242, s[8:11], s45 offen offset:2048 lds
v_mfma_f32_16x16x32_bf16 v[108:111], a[124:127], a[104:107], v[108:111]
ds_read_b128 a[72:75], v244 offset:4096
ds_read_b128 a[104:107], v244 offset:6144
v_mfma_f32_16x16x32_bf16 v[120:123], a[88:91], a[76:79], v[120:123]
s_add_u32 s44, s44, 4096
v_mfma_f32_16x16x32_bf16 v[120:123], a[120:123], a[108:111], v[120:123]
s_waitcnt lgkmcnt(2)
v_mfma_f32_16x16x32_bf16 v[124:127], a[92:95], a[76:79], v[124:127]
buffer_load_dwordx4 v243, s[8:11], s45 offen offset:3072 lds
v_mfma_f32_16x16x32_bf16 v[124:127], a[124:127], a[108:111], v[124:127]
ds_read_b128 a[76:79], v244 offset:5120
ds_read_b128 a[108:111], v244 offset:7168
v_mfma_f32_16x16x32_bf16 v[64:67], a[80:83], a[64:67], v[64:67]
ds_read_b64_tr_b16 a[88:89], v247 offset:0
ds_read_b64_tr_b16 a[90:91], v247 offset:4096
v_mfma_f32_16x16x32_bf16 v[64:67], a[112:115], a[96:99], v[64:67]
s_mov_b32 m0, s49
ds_read_b64_tr_b16 a[120:121], v247 offset:8192
ds_read_b64_tr_b16 a[122:123], v247 offset:12288
v_mfma_f32_16x16x32_bf16 v[68:71], a[84:87], a[64:67], v[68:71]
buffer_load_dwordx4 v7, s[4:7], s44 offen offset:0 sc1 lds
v_mfma_f32_16x16x32_bf16 v[68:71], a[116:119], a[96:99], v[68:71]
ds_read_b64_tr_b16 a[92:93], v248 offset:0
ds_read_b64_tr_b16 a[94:95], v248 offset:4096
v_mfma_f32_16x16x32_bf16 v[80:83], a[80:83], a[68:71], v[80:83]
ds_read_b64_tr_b16 a[124:125], v248 offset:8192
ds_read_b64_tr_b16 a[126:127], v248 offset:12288
v_mfma_f32_16x16x32_bf16 v[80:83], a[112:115], a[100:103], v[80:83]
s_add_u32 s45, s45, 131072
v_add_u32 v244, s53, v244
v_mfma_f32_16x16x32_bf16 v[84:87], a[84:87], a[68:71], v[84:87]
buffer_load_dwordx4 v7, s[4:7], s44 offen offset:1024 sc1 lds
v_mfma_f32_16x16x32_bf16 v[84:87], a[116:119], a[100:103], v[84:87]
s_waitcnt lgkmcnt(8)
v_mfma_f32_16x16x32_bf16 v[96:99], a[80:83], a[72:75], v[96:99]
v_add_u32 v247, s53, v247
v_add_u32 v248, s53, v248
v_mfma_f32_16x16x32_bf16 v[96:99], a[112:115], a[104:107], v[96:99]
v_mfma_f32_16x16x32_bf16 v[100:103], a[84:87], a[72:75], v[100:103]
buffer_load_dwordx4 v7, s[4:7], s44 offen offset:2048 sc1 lds
v_mfma_f32_16x16x32_bf16 v[100:103], a[116:119], a[104:107], v[100:103]
s_add_u32 s49, s46, s91
v_mfma_f32_16x16x32_bf16 v[112:115], a[80:83], a[76:79], v[112:115]
s_add_u32 s46, s46, s53
v_mfma_f32_16x16x32_bf16 v[112:115], a[112:115], a[108:111], v[112:115]
v_mfma_f32_16x16x32_bf16 v[116:119], a[84:87], a[76:79], v[116:119]
buffer_load_dwordx4 v7, s[4:7], s44 offen offset:3072 sc1 lds
v_mfma_f32_16x16x32_bf16 v[116:119], a[116:119], a[108:111], v[116:119]
s_waitcnt vmcnt(25) lgkmcnt(0)
s_barrier
v_mfma_f32_16x16x32_bf16 v[72:75], a[88:91], a[64:67], v[72:75]
ds_read_b64_tr_b16 a[80:81], v245 offset:0
ds_read_b64_tr_b16 a[82:83], v245 offset:4096
v_mfma_f32_16x16x32_bf16 v[72:75], a[120:123], a[96:99], v[72:75]
s_add_u32 m0, s49, 16384
ds_read_b64_tr_b16 a[112:113], v245 offset:8192
ds_read_b64_tr_b16 a[114:115], v245 offset:12288
v_mfma_f32_16x16x32_bf16 v[76:79], a[92:95], a[64:67], v[76:79]
buffer_load_dwordx4 v240, s[8:11], s45 offen offset:0 lds
v_mfma_f32_16x16x32_bf16 v[76:79], a[124:127], a[96:99], v[76:79]
ds_read_b128 a[64:67], v244 offset:0
ds_read_b128 a[96:99], v244 offset:2048
v_mfma_f32_16x16x32_bf16 v[88:91], a[88:91], a[68:71], v[88:91]
ds_read_b64_tr_b16 a[84:85], v246 offset:0
ds_read_b64_tr_b16 a[86:87], v246 offset:4096
v_mfma_f32_16x16x32_bf16 v[88:91], a[120:123], a[100:103], v[88:91]
ds_read_b64_tr_b16 a[116:117], v246 offset:8192
ds_read_b64_tr_b16 a[118:119], v246 offset:12288
v_mfma_f32_16x16x32_bf16 v[92:95], a[92:95], a[68:71], v[92:95]
buffer_load_dwordx4 v241, s[8:11], s45 offen offset:1024 lds
v_mfma_f32_16x16x32_bf16 v[92:95], a[124:127], a[100:103], v[92:95]
ds_read_b128 a[68:71], v244 offset:1024
ds_read_b128 a[100:103], v244 offset:3072
v_mfma_f32_16x16x32_bf16 v[104:107], a[88:91], a[72:75], v[104:107]
s_cmp_eq_u32 s46, 0
s_cselect_b32 s53, s56, s57
v_mfma_f32_16x16x32_bf16 v[104:107], a[120:123], a[104:107], v[104:107]
v_add_u32 v245, s53, v245
v_add_u32 v246, s53, v246
v_mfma_f32_16x16x32_bf16 v[108:111], a[92:95], a[72:75], v[108:111]
buffer_load_dwordx4 v242, s[8:11], s45 offen offset:2048 lds
v_mfma_f32_16x16x32_bf16 v[108:111], a[124:127], a[104:107], v[108:111]
ds_read_b128 a[72:75], v244 offset:4096
ds_read_b128 a[104:107], v244 offset:6144
v_mfma_f32_16x16x32_bf16 v[120:123], a[88:91], a[76:79], v[120:123]
s_add_u32 s44, s44, 4096
v_mfma_f32_16x16x32_bf16 v[120:123], a[120:123], a[108:111], v[120:123]
s_waitcnt lgkmcnt(2)
v_mfma_f32_16x16x32_bf16 v[124:127], a[92:95], a[76:79], v[124:127]
buffer_load_dwordx4 v243, s[8:11], s45 offen offset:3072 lds
v_mfma_f32_16x16x32_bf16 v[124:127], a[124:127], a[108:111], v[124:127]
ds_read_b128 a[76:79], v244 offset:5120
ds_read_b128 a[108:111], v244 offset:7168
v_mfma_f32_16x16x32_bf16 v[64:67], a[80:83], a[64:67], v[64:67]
ds_read_b64_tr_b16 a[88:89], v247 offset:0
ds_read_b64_tr_b16 a[90:91], v247 offset:4096
v_mfma_f32_16x16x32_bf16 v[64:67], a[112:115], a[96:99], v[64:67]
s_mov_b32 m0, s49
ds_read_b64_tr_b16 a[120:121], v247 offset:8192
ds_read_b64_tr_b16 a[122:123], v247 offset:12288
v_mfma_f32_16x16x32_bf16 v[68:71], a[84:87], a[64:67], v[68:71]
buffer_load_dwordx4 v7, s[4:7], s44 offen offset:0 sc1 lds
v_mfma_f32_16x16x32_bf16 v[68:71], a[116:119], a[96:99], v[68:71]
ds_read_b64_tr_b16 a[92:93], v248 offset:0
ds_read_b64_tr_b16 a[94:95], v248 offset:4096
v_mfma_f32_16x16x32_bf16 v[80:83], a[80:83], a[68:71], v[80:83]
ds_read_b64_tr_b16 a[124:125], v248 offset:8192
ds_read_b64_tr_b16 a[126:127], v248 offset:12288
v_mfma_f32_16x16x32_bf16 v[80:83], a[112:115], a[100:103], v[80:83]
s_add_u32 s45, s45, 131072
v_add_u32 v244, s53, v244
v_mfma_f32_16x16x32_bf16 v[84:87], a[84:87], a[68:71], v[84:87]
buffer_load_dwordx4 v7, s[4:7], s44 offen offset:1024 sc1 lds
v_mfma_f32_16x16x32_bf16 v[84:87], a[116:119], a[100:103], v[84:87]
s_waitcnt lgkmcnt(8)
v_mfma_f32_16x16x32_bf16 v[96:99], a[80:83], a[72:75], v[96:99]
v_add_u32 v247, s53, v247
v_add_u32 v248, s53, v248
v_mfma_f32_16x16x32_bf16 v[96:99], a[112:115], a[104:107], v[96:99]
v_mfma_f32_16x16x32_bf16 v[100:103], a[84:87], a[72:75], v[100:103]
buffer_load_dwordx4 v7, s[4:7], s44 offen offset:2048 sc1 lds
v_mfma_f32_16x16x32_bf16 v[100:103], a[116:119], a[104:107], v[100:103]
s_add_u32 s49, s46, s91
v_mfma_f32_16x16x32_bf16 v[112:115], a[80:83], a[76:79], v[112:115]
s_add_u32 s46, s46, s53
v_mfma_f32_16x16x32_bf16 v[112:115], a[112:115], a[108:111], v[112:115]
v_mfma_f32_16x16x32_bf16 v[116:119], a[84:87], a[76:79], v[116:119]
buffer_load_dwordx4 v7, s[4:7], s44 offen offset:3072 sc1 lds
v_mfma_f32_16x16x32_bf16 v[116:119], a[116:119], a[108:111], v[116:119]
s_waitcnt vmcnt(25) lgkmcnt(0)
s_barrier
s_lshl_b32 s54, s51, 2
s_add_u32 s54, s54, s3
s_lshl_b32 s54, s54, 16
s_add_u32 s54, s54, 50331648
s_add_u32 s4, s84, s54
s_addc_u32 s5, s85, 0
.Lpad21:
s_cbranch_execz .Ltch11
s_and_b32 s5, s5, 0xffff
s_mov_b32 s6, 65536
s_lshl_b32 s54, s3, 15
s_add_u32 s54, s54, s52
s_sub_u32 s28, 0x200000, s54
s_add_u32 s8, s68, s54
s_addc_u32 s9, s69, 0
s_and_b32 s9, s9, 0xffff
s_mov_b32 s10, s28
s_mov_b32 s44, -4096
s_mov_b32 s45, 0
v_mfma_f32_16x16x32_bf16 v[72:75], a[88:91], a[64:67], v[72:75]
ds_read_b64_tr_b16 a[80:81], v245 offset:0
ds_read_b64_tr_b16 a[82:83], v245 offset:4096
v_mfma_f32_16x16x32_bf16 v[72:75], a[120:123], a[96:99], v[72:75]
s_add_u32 m0, s49, 16384
ds_read_b64_tr_b16 a[112:113], v245 offset:8192
ds_read_b64_tr_b16 a[114:115], v245 offset:12288
v_mfma_f32_16x16x32_bf16 v[76:79], a[92:95], a[64:67], v[76:79]
buffer_load_dwordx4 v240, s[8:11], s45 offen offset:0 lds
v_mfma_f32_16x16x32_bf16 v[76:79], a[124:127], a[96:99], v[76:79]
ds_read_b128 a[64:67], v244 offset:0
ds_read_b128 a[96:99], v244 offset:2048
v_mfma_f32_16x16x32_bf16 v[88:91], a[88:91], a[68:71], v[88:91]
ds_read_b64_tr_b16 a[84:85], v246 offset:0
ds_read_b64_tr_b16 a[86:87], v246 offset:4096
v_mfma_f32_16x16x32_bf16 v[88:91], a[120:123], a[100:103], v[88:91]
ds_read_b64_tr_b16 a[116:117], v246 offset:8192
ds_read_b64_tr_b16 a[118:119], v246 offset:12288
v_mfma_f32_16x16x32_bf16 v[92:95], a[92:95], a[68:71], v[92:95]
buffer_load_dwordx4 v241, s[8:11], s45 offen offset:1024 lds
v_mfma_f32_16x16x32_bf16 v[92:95], a[124:127], a[100:103], v[92:95]
ds_read_b128 a[68:71], v244 offset:1024
ds_read_b128 a[100:103], v244 offset:3072
v_mfma_f32_16x16x32_bf16 v[104:107], a[88:91], a[72:75], v[104:107]
s_cmp_eq_u32 s46, 0
s_cselect_b32 s53, s56, s57
v_mfma_f32_16x16x32_bf16 v[104:107], a[120:123], a[104:107], v[104:107]
v_add_u32 v245, s53, v245
v_add_u32 v246, s53, v246
v_mfma_f32_16x16x32_bf16 v[108:111], a[92:95], a[72:75], v[108:111]
s_waitcnt vmcnt(16)
buffer_load_dwordx4 v242, s[8:11], s45 offen offset:2048 lds
v_and_b32 v250, 7, v250
v_mfma_f32_16x16x32_bf16 v[108:111], a[124:127], a[104:107], v[108:111]
ds_read_b128 a[72:75], v244 offset:4096
ds_read_b128 a[104:107], v244 offset:6144
v_cmp_gt_u32 vcc, 4, v250
v_mfma_f32_16x16x32_bf16 v[120:123], a[88:91], a[76:79], v[120:123]
s_add_u32 s44, s44, 4096
v_mfma_f32_16x16x32_bf16 v[120:123], a[120:123], a[108:111], v[120:123]
s_waitcnt lgkmcnt(2)
s_cmp_lg_u64 vcc, 0
s_cbranch_scc1 .Lspin4
.Lspin4_ret:
v_mfma_f32_16x16x32_bf16 v[124:127], a[92:95], a[76:79], v[124:127]
buffer_load_dwordx4 v243, s[8:11], s45 offen offset:3072 lds
v_mfma_f32_16x16x32_bf16 v[124:127], a[124:127], a[108:111], v[124:127]
ds_read_b128 a[76:79], v244 offset:5120
ds_read_b128 a[108:111], v244 offset:7168
v_mfma_f32_16x16x32_bf16 v[64:67], a[80:83], a[64:67], v[64:67]
ds_read_b64_tr_b16 a[88:89], v247 offset:0
ds_read_b64_tr_b16 a[90:91], v247 offset:4096
v_mfma_f32_16x16x32_bf16 v[64:67], a[112:115], a[96:99], v[64:67]
s_mov_b32 m0, s49
ds_read_b64_tr_b16 a[120:121], v247 offset:8192
ds_read_b64_tr_b16 a[122:123], v247 offset:12288
v_mfma_f32_16x16x32_bf16 v[68:71], a[84:87], a[64:67], v[68:71]
buffer_load_dwordx4 v7, s[4:7], s44 offen offset:0 sc1 lds
v_mfma_f32_16x16x32_bf16 v[68:71], a[116:119], a[96:99], v[68:71]
ds_read_b64_tr_b16 a[92:93], v248 offset:0
ds_read_b64_tr_b16 a[94:95], v248 offset:4096
v_mfma_f32_16x16x32_bf16 v[80:83], a[80:83], a[68:71], v[80:83]
ds_read_b64_tr_b16 a[124:125], v248 offset:8192
ds_read_b64_tr_b16 a[126:127], v248 offset:12288
v_mfma_f32_16x16x32_bf16 v[80:83], a[112:115], a[100:103], v[80:83]
s_add_u32 s45, s45, 131072
v_add_u32 v244, s53, v244
v_mfma_f32_16x16x32_bf16 v[84:87], a[84:87], a[68:71], v[84:87]
buffer_load_dwordx4 v7, s[4:7], s44 offen offset:1024 sc1 lds
v_mfma_f32_16x16x32_bf16 v[84:87], a[116:119], a[100:103], v[84:87]
s_waitcnt lgkmcnt(8)
v_mfma_f32_16x16x32_bf16 v[96:99], a[80:83], a[72:75], v[96:99]
v_add_u32 v247, s53, v247
v_add_u32 v248, s53, v248
v_mfma_f32_16x16x32_bf16 v[96:99], a[112:115], a[104:107], v[96:99]
v_mfma_f32_16x16x32_bf16 v[100:103], a[84:87], a[72:75], v[100:103]
buffer_load_dwordx4 v7, s[4:7], s44 offen offset:2048 sc1 lds
v_mfma_f32_16x16x32_bf16 v[100:103], a[116:119], a[104:107], v[100:103]
s_add_u32 s49, s46, s91
v_mfma_f32_16x16x32_bf16 v[112:115], a[80:83], a[76:79], v[112:115]
s_add_u32 s46, s46, s53
v_mfma_f32_16x16x32_bf16 v[112:115], a[112:115], a[108:111], v[112:115]
v_mfma_f32_16x16x32_bf16 v[116:119], a[84:87], a[76:79], v[116:119]
buffer_load_dwordx4 v7, s[4:7], s44 offen offset:3072 sc1 lds
v_mfma_f32_16x16x32_bf16 v[116:119], a[116:119], a[108:111], v[116:119]
s_waitcnt vmcnt(25) lgkmcnt(0)
s_barrier
v_mfma_f32_16x16x32_bf16 v[72:75], a[88:91], a[64:67], v[72:75]
ds_read_b64_tr_b16 a[80:81], v245 offset:0
ds_read_b64_tr_b16 a[82:83], v245 offset:4096
v_mfma_f32_16x16x32_bf16 v[72:75], a[120:123], a[96:99], v[72:75]
s_add_u32 m0, s49, 16384
ds_read_b64_tr_b16 a[112:113], v245 offset:8192
ds_read_b64_tr_b16 a[114:115], v245 offset:12288
v_mfma_f32_16x16x32_bf16 v[76:79], a[92:95], a[64:67], v[76:79]
buffer_load_dwordx4 v240, s[8:11], s45 offen offset:0 lds
v_mfma_f32_16x16x32_bf16 v[76:79], a[124:127], a[96:99], v[76:79]
ds_read_b128 a[64:67], v244 offset:0
ds_read_b128 a[96:99], v244 offset:2048
v_mfma_f32_16x16x32_bf16 v[88:91], a[88:91], a[68:71], v[88:91]
ds_read_b64_tr_b16 a[84:85], v246 offset:0
ds_read_b64_tr_b16 a[86:87], v246 offset:4096
v_mfma_f32_16x16x32_bf16 v[88:91], a[120:123], a[100:103], v[88:91]
ds_read_b64_tr_b16 a[116:117], v246 offset:8192
ds_read_b64_tr_b16 a[118:119], v246 offset:12288
v_mfma_f32_16x16x32_bf16 v[92:95], a[92:95], a[68:71], v[92:95]
buffer_load_dwordx4 v241, s[8:11], s45 offen offset:1024 lds
v_mfma_f32_16x16x32_bf16 v[92:95], a[124:127], a[100:103], v[92:95]
ds_read_b128 a[68:71], v244 offset:1024
ds_read_b128 a[100:103], v244 offset:3072
v_mfma_f32_16x16x32_bf16 v[104:107], a[88:91], a[72:75], v[104:107]
s_cmp_eq_u32 s46, 0
s_cselect_b32 s53, s56, s57
v_mfma_f32_16x16x32_bf16 v[104:107], a[120:123], a[104:107], v[104:107]
v_add_u32 v245, s53, v245
v_add_u32 v246, s53, v246
v_mfma_f32_16x16x32_bf16 v[108:111], a[92:95], a[72:75], v[108:111]
buffer_load_dwordx4 v242, s[8:11], s45 offen offset:2048 lds
v_mfma_f32_16x16x32_bf16 v[108:111], a[124:127], a[104:107], v[108:111]
ds_read_b128 a[72:75], v244 offset:4096
ds_read_b128 a[104:107], v244 offset:6144
v_mfma_f32_16x16x32_bf16 v[120:123], a[88:91], a[76:79], v[120:123]
s_add_u32 s44, s44, 4096
v_mfma_f32_16x16x32_bf16 v[120:123], a[120:123], a[108:111], v[120:123]
s_waitcnt lgkmcnt(2)
v_mfma_f32_16x16x32_bf16 v[124:127], a[92:95], a[76:79], v[124:127]
buffer_load_dwordx4 v243, s[8:11], s45 offen offset:3072 lds
v_mfma_f32_16x16x32_bf16 v[124:127], a[124:127], a[108:111], v[124:127]
ds_read_b128 a[76:79], v244 offset:5120
ds_read_b128 a[108:111], v244 offset:7168
v_mfma_f32_16x16x32_bf16 v[64:67], a[80:83], a[64:67], v[64:67]
ds_read_b64_tr_b16 a[88:89], v247 offset:0
ds_read_b64_tr_b16 a[90:91], v247 offset:4096
v_mfma_f32_16x16x32_bf16 v[64:67], a[112:115], a[96:99], v[64:67]
s_mov_b32 m0, s49
ds_read_b64_tr_b16 a[120:121], v247 offset:8192
ds_read_b64_tr_b16 a[122:123], v247 offset:12288
v_mfma_f32_16x16x32_bf16 v[68:71], a[84:87], a[64:67], v[68:71]
buffer_load_dwordx4 v7, s[4:7], s44 offen offset:0 sc1 lds
v_mfma_f32_16x16x32_bf16 v[68:71], a[116:119], a[96:99], v[68:71]
ds_read_b64_tr_b16 a[92:93], v248 offset:0
ds_read_b64_tr_b16 a[94:95], v248 offset:4096
v_mfma_f32_16x16x32_bf16 v[80:83], a[80:83], a[68:71], v[80:83]
ds_read_b64_tr_b16 a[124:125], v248 offset:8192
ds_read_b64_tr_b16 a[126:127], v248 offset:12288
v_mfma_f32_16x16x32_bf16 v[80:83], a[112:115], a[100:103], v[80:83]
s_add_u32 s45, s45, 131072
v_add_u32 v244, s53, v244
v_mfma_f32_16x16x32_bf16 v[84:87], a[84:87], a[68:71], v[84:87]
buffer_load_dwordx4 v7, s[4:7], s44 offen offset:1024 sc1 lds
v_mfma_f32_16x16x32_bf16 v[84:87], a[116:119], a[100:103], v[84:87]
s_waitcnt lgkmcnt(8)
v_mfma_f32_16x16x32_bf16 v[96:99], a[80:83], a[72:75], v[96:99]
v_add_u32 v247, s53, v247
v_add_u32 v248, s53, v248
v_mfma_f32_16x16x32_bf16 v[96:99], a[112:115], a[104:107], v[96:99]
v_mfma_f32_16x16x32_bf16 v[100:103], a[84:87], a[72:75], v[100:103]
buffer_load_dwordx4 v7, s[4:7], s44 offen offset:2048 sc1 lds
v_mfma_f32_16x16x32_bf16 v[100:103], a[116:119], a[104:107], v[100:103]
s_add_u32 s49, s46, s91
v_mfma_f32_16x16x32_bf16 v[112:115], a[80:83], a[76:79], v[112:115]
s_add_u32 s46, s46, s53
v_mfma_f32_16x16x32_bf16 v[112:115], a[112:115], a[108:111], v[112:115]
v_mfma_f32_16x16x32_bf16 v[116:119], a[84:87], a[76:79], v[116:119]
buffer_load_dwordx4 v7, s[4:7], s44 offen offset:3072 sc1 lds
v_mfma_f32_16x16x32_bf16 v[116:119], a[116:119], a[108:111], v[116:119]
s_waitcnt vmcnt(24) lgkmcnt(0)
s_barrier
v_mfma_f32_16x16x32_bf16 v[72:75], a[88:91], a[64:67], v[72:75]
ds_read_b64_tr_b16 a[80:81], v245 offset:0
ds_read_b64_tr_b16 a[82:83], v245 offset:4096
v_mfma_f32_16x16x32_bf16 v[72:75], a[120:123], a[96:99], v[72:75]
s_add_u32 m0, s49, 16384
ds_read_b64_tr_b16 a[112:113], v245 offset:8192
ds_read_b64_tr_b16 a[114:115], v245 offset:12288
v_mfma_f32_16x16x32_bf16 v[76:79], a[92:95], a[64:67], v[76:79]
buffer_load_dwordx4 v240, s[8:11], s45 offen offset:0 lds
v_mfma_f32_16x16x32_bf16 v[76:79], a[124:127], a[96:99], v[76:79]
ds_read_b128 a[64:67], v244 offset:0
ds_read_b128 a[96:99], v244 offset:2048
v_mfma_f32_16x16x32_bf16 v[88:91], a[88:91], a[68:71], v[88:91]
ds_read_b64_tr_b16 a[84:85], v246 offset:0
ds_read_b64_tr_b16 a[86:87], v246 offset:4096
v_mfma_f32_16x16x32_bf16 v[88:91], a[120:123], a[100:103], v[88:91]
ds_read_b64_tr_b16 a[116:117], v246 offset:8192
ds_read_b64_tr_b16 a[118:119], v246 offset:12288
v_mfma_f32_16x16x32_bf16 v[92:95], a[92:95], a[68:71], v[92:95]
buffer_load_dwordx4 v241, s[8:11], s45 offen offset:1024 lds
v_mfma_f32_16x16x32_bf16 v[92:95], a[124:127], a[100:103], v[92:95]
ds_read_b128 a[68:71], v244 offset:1024
ds_read_b128 a[100:103], v244 offset:3072
v_mfma_f32_16x16x32_bf16 v[104:107], a[88:91], a[72:75], v[104:107]
s_cmp_eq_u32 s46, 0
s_cselect_b32 s53, s56, s57
v_mfma_f32_16x16x32_bf16 v[104:107], a[120:123], a[104:107], v[104:107]
v_add_u32 v245, s53, v245
v_add_u32 v246, s53, v246
v_mfma_f32_16x16x32_bf16 v[108:111], a[92:95], a[72:75], v[108:111]
buffer_load_dwordx4 v242, s[8:11], s45 offen offset:2048 lds
v_mfma_f32_16x16x32_bf16 v[108:111], a[124:127], a[104:107], v[108:111]
ds_read_b128 a[72:75], v244 offset:4096
ds_read_b128 a[104:107], v244 offset:6144
v_mfma_f32_16x16x32_bf16 v[120:123], a[88:91], a[76:79], v[120:123]
s_add_u32 s44, s44, 4096
v_mfma_f32_16x16x32_bf16 v[120:123], a[120:123], a[108:111], v[120:123]
s_waitcnt lgkmcnt(2)
v_mfma_f32_16x16x32_bf16 v[124:127], a[92:95], a[76:79], v[124:127]
buffer_load_dwordx4 v243, s[8:11], s45 offen offset:3072 lds
v_mfma_f32_16x16x32_bf16 v[124:127], a[124:127], a[108:111], v[124:127]
ds_read_b128 a[76:79], v244 offset:5120
ds_read_b128 a[108:111], v244 offset:7168
v_mfma_f32_16x16x32_bf16 v[64:67], a[80:83], a[64:67], v[64:67]
ds_read_b64_tr_b16 a[88:89], v247 offset:0
ds_read_b64_tr_b16 a[90:91], v247 offset:4096
v_mfma_f32_16x16x32_bf16 v[64:67], a[112:115], a[96:99], v[64:67]
s_mov_b32 m0, s49
ds_read_b64_tr_b16 a[120:121], v247 offset:8192
ds_read_b64_tr_b16 a[122:123], v247 offset:12288
v_mfma_f32_16x16x32_bf16 v[68:71], a[84:87], a[64:67], v[68:71]
buffer_load_dwordx4 v7, s[4:7], s44 offen offset:0 sc1 lds
v_mfma_f32_16x16x32_bf16 v[68:71], a[116:119], a[96:99], v[68:71]
ds_read_b64_tr_b16 a[92:93], v248 offset:0
ds_read_b64_tr_b16 a[94:95], v248 offset:4096
v_mfma_f32_16x16x32_bf16 v[80:83], a[80:83], a[68:71], v[80:83]
ds_read_b64_tr_b16 a[124:125], v248 offset:8192
ds_read_b64_tr_b16 a[126:127], v248 offset:12288
v_mfma_f32_16x16x32_bf16 v[80:83], a[112:115], a[100:103], v[80:83]
s_add_u32 s45, s45, 131072
v_add_u32 v244, s53, v244
v_mfma_f32_16x16x32_bf16 v[84:87], a[84:87], a[68:71], v[84:87]
buffer_load_dwordx4 v7, s[4:7], s44 offen offset:1024 sc1 lds
v_mfma_f32_16x16x32_bf16 v[84:87], a[116:119], a[100:103], v[84:87]
s_waitcnt lgkmcnt(8)
v_mfma_f32_16x16x32_bf16 v[96:99], a[80:83], a[72:75], v[96:99]
v_add_u32 v247, s53, v247
v_add_u32 v248, s53, v248
v_mfma_f32_16x16x32_bf16 v[96:99], a[112:115], a[104:107], v[96:99]
v_mfma_f32_16x16x32_bf16 v[100:103], a[84:87], a[72:75], v[100:103]
buffer_load_dwordx4 v7, s[4:7], s44 offen offset:2048 sc1 lds
v_mfma_f32_16x16x32_bf16 v[100:103], a[116:119], a[104:107], v[100:103]
s_add_u32 s49, s46, s91
v_mfma_f32_16x16x32_bf16 v[112:115], a[80:83], a[76:79], v[112:115]
s_add_u32 s46, s46, s53
v_mfma_f32_16x16x32_bf16 v[112:115], a[112:115], a[108:111], v[112:115]
v_mfma_f32_16x16x32_bf16 v[116:119], a[84:87], a[76:79], v[116:119]
buffer_load_dwordx4 v7, s[4:7], s44 offen offset:3072 sc1 lds
v_mfma_f32_16x16x32_bf16 v[116:119], a[116:119], a[108:111], v[116:119]
s_waitcnt vmcnt(24) lgkmcnt(0)
s_barrier
v_mfma_f32_16x16x32_bf16 v[72:75], a[88:91], a[64:67], v[72:75]
ds_read_b64_tr_b16 a[80:81], v245 offset:0
ds_read_b64_tr_b16 a[82:83], v245 offset:4096
v_mfma_f32_16x16x32_bf16 v[72:75], a[120:123], a[96:99], v[72:75]
s_add_u32 m0, s49, 16384
ds_read_b64_tr_b16 a[112:113], v245 offset:8192
ds_read_b64_tr_b16 a[114:115], v245 offset:12288
v_mfma_f32_16x16x32_bf16 v[76:79], a[92:95], a[64:67], v[76:79]
buffer_load_dwordx4 v240, s[8:11], s45 offen offset:0 lds
v_mfma_f32_16x16x32_bf16 v[76:79], a[124:127], a[96:99], v[76:79]
ds_read_b128 a[64:67], v244 offset:0
ds_read_b128 a[96:99], v244 offset:2048
v_mfma_f32_16x16x32_bf16 v[88:91], a[88:91], a[68:71], v[88:91]
ds_read_b64_tr_b16 a[84:85], v246 offset:0
ds_read_b64_tr_b16 a[86:87], v246 offset:4096
v_mfma_f32_16x16x32_bf16 v[88:91], a[120:123], a[100:103], v[88:91]
ds_read_b64_tr_b16 a[116:117], v246 offset:8192
ds_read_b64_tr_b16 a[118:119], v246 offset:12288
v_mfma_f32_16x16x32_bf16 v[92:95], a[92:95], a[68:71], v[92:95]
buffer_load_dwordx4 v241, s[8:11], s45 offen offset:1024 lds
v_mfma_f32_16x16x32_bf16 v[92:95], a[124:127], a[100:103], v[92:95]
ds_read_b128 a[68:71], v244 offset:1024
ds_read_b128 a[100:103], v244 offset:3072
v_mfma_f32_16x16x32_bf16 v[104:107], a[88:91], a[72:75], v[104:107]
s_cmp_eq_u32 s46, 0
s_cselect_b32 s53, s56, s57
v_mfma_f32_16x16x32_bf16 v[104:107], a[120:123], a[104:107], v[104:107]
v_add_u32 v245, s53, v245
v_add_u32 v246, s53, v246
v_mfma_f32_16x16x32_bf16 v[108:111], a[92:95], a[72:75], v[108:111]
buffer_load_dwordx4 v242, s[8:11], s45 offen offset:2048 lds
v_mfma_f32_16x16x32_bf16 v[108:111], a[124:127], a[104:107], v[108:111]
ds_read_b128 a[72:75], v244 offset:4096
ds_read_b128 a[104:107], v244 offset:6144
v_mfma_f32_16x16x32_bf16 v[120:123], a[88:91], a[76:79], v[120:123]
s_add_u32 s44, s44, 4096
v_mfma_f32_16x16x32_bf16 v[120:123], a[120:123], a[108:111], v[120:123]
s_waitcnt lgkmcnt(2)
v_mfma_f32_16x16x32_bf16 v[124:127], a[92:95], a[76:79], v[124:127]
buffer_load_dwordx4 v243, s[8:11], s45 offen offset:3072 lds
v_mfma_f32_16x16x32_bf16 v[124:127], a[124:127], a[108:111], v[124:127]
ds_read_b128 a[76:79], v244 offset:5120
ds_read_b128 a[108:111], v244 offset:7168
v_mfma_f32_16x16x32_bf16 v[64:67], a[80:83], a[64:67], v[64:67]
ds_read_b64_tr_b16 a[88:89], v247 offset:0
ds_read_b64_tr_b16 a[90:91], v247 offset:4096
v_mfma_f32_16x16x32_bf16 v[64:67], a[112:115], a[96:99], v[64:67]
s_mov_b32 m0, s49
ds_read_b64_tr_b16 a[120:121], v247 offset:8192
ds_read_b64_tr_b16 a[122:123], v247 offset:12288
v_mfma_f32_16x16x32_bf16 v[68:71], a[84:87], a[64:67], v[68:71]
buffer_load_dwordx4 v7, s[4:7], s44 offen offset:0 sc1 lds
v_mfma_f32_16x16x32_bf16 v[68:71], a[116:119], a[96:99], v[68:71]
ds_read_b64_tr_b16 a[92:93], v248 offset:0
ds_read_b64_tr_b16 a[94:95], v248 offset:4096
v_mfma_f32_16x16x32_bf16 v[80:83], a[80:83], a[68:71], v[80:83]
ds_read_b64_tr_b16 a[124:125], v248 offset:8192
ds_read_b64_tr_b16 a[126:127], v248 offset:12288
v_mfma_f32_16x16x32_bf16 v[80:83], a[112:115], a[100:103], v[80:83]
s_add_u32 s45, s45, 131072
v_add_u32 v244, s53, v244
v_mfma_f32_16x16x32_bf16 v[84:87], a[84:87], a[68:71], v[84:87]
buffer_load_dwordx4 v7, s[4:7], s44 offen offset:1024 sc1 lds
v_mfma_f32_16x16x32_bf16 v[84:87], a[116:119], a[100:103], v[84:87]
s_waitcnt lgkmcnt(8)
v_mfma_f32_16x16x32_bf16 v[96:99], a[80:83], a[72:75], v[96:99]
v_add_u32 v247, s53, v247
v_add_u32 v248, s53, v248
v_mfma_f32_16x16x32_bf16 v[96:99], a[112:115], a[104:107], v[96:99]
v_mfma_f32_16x16x32_bf16 v[100:103], a[84:87], a[72:75], v[100:103]
buffer_load_dwordx4 v7, s[4:7], s44 offen offset:2048 sc1 lds
v_mfma_f32_16x16x32_bf16 v[100:103], a[116:119], a[104:107], v[100:103]
s_add_u32 s49, s46, s91
v_mfma_f32_16x16x32_bf16 v[112:115], a[80:83], a[76:79], v[112:115]
s_add_u32 s46, s46, s53
v_mfma_f32_16x16x32_bf16 v[112:115], a[112:115], a[108:111], v[112:115]
v_mfma_f32_16x16x32_bf16 v[116:119], a[84:87], a[76:79], v[116:119]
buffer_load_dwordx4 v7, s[4:7], s44 offen offset:3072 sc1 lds
v_mfma_f32_16x16x32_bf16 v[116:119], a[116:119], a[108:111], v[116:119]
s_waitcnt vmcnt(24) lgkmcnt(0)
s_barrier
v_mfma_f32_16x16x32_bf16 v[72:75], a[88:91], a[64:67], v[72:75]
ds_read_b64_tr_b16 a[80:81], v245 offset:0
ds_read_b64_tr_b16 a[82:83], v245 offset:4096
v_mfma_f32_16x16x32_bf16 v[72:75], a[120:123], a[96:99], v[72:75]
s_add_u32 m0, s49, 16384
ds_read_b64_tr_b16 a[112:113], v245 offset:8192
ds_read_b64_tr_b16 a[114:115], v245 offset:12288
v_mfma_f32_16x16x32_bf16 v[76:79], a[92:95], a[64:67], v[76:79]
buffer_load_dwordx4 v240, s[8:11], s45 offen offset:0 lds
v_mfma_f32_16x16x32_bf16 v[76:79], a[124:127], a[96:99], v[76:79]
ds_read_b128 a[64:67], v244 offset:0
ds_read_b128 a[96:99], v244 offset:2048
v_mfma_f32_16x16x32_bf16 v[88:91], a[88:91], a[68:71], v[88:91]
ds_read_b64_tr_b16 a[84:85], v246 offset:0
ds_read_b64_tr_b16 a[86:87], v246 offset:4096
v_mfma_f32_16x16x32_bf16 v[88:91], a[120:123], a[100:103], v[88:91]
ds_read_b64_tr_b16 a[116:117], v246 offset:8192
ds_read_b64_tr_b16 a[118:119], v246 offset:12288
v_mfma_f32_16x16x32_bf16 v[92:95], a[92:95], a[68:71], v[92:95]
buffer_load_dwordx4 v241, s[8:11], s45 offen offset:1024 lds
v_mfma_f32_16x16x32_bf16 v[92:95], a[124:127], a[100:103], v[92:95]
ds_read_b128 a[68:71], v244 offset:1024
ds_read_b128 a[100:103], v244 offset:3072
v_mfma_f32_16x16x32_bf16 v[104:107], a[88:91], a[72:75], v[104:107]
s_cmp_eq_u32 s46, 0
s_cselect_b32 s53, s56, s57
v_mfma_f32_16x16x32_bf16 v[104:107], a[120:123], a[104:107], v[104:107]
v_add_u32 v245, s53, v245
v_add_u32 v246, s53, v246
v_mfma_f32_16x16x32_bf16 v[108:111], a[92:95], a[72:75], v[108:111]
buffer_load_dwordx4 v242, s[8:11], s45 offen offset:2048 lds
v_mfma_f32_16x16x32_bf16 v[108:111], a[124:127], a[104:107], v[108:111]
ds_read_b128 a[72:75], v244 offset:4096
ds_read_b128 a[104:107], v244 offset:6144
v_mfma_f32_16x16x32_bf16 v[120:123], a[88:91], a[76:79], v[120:123]
s_add_u32 s44, s44, 4096
v_mfma_f32_16x16x32_bf16 v[120:123], a[120:123], a[108:111], v[120:123]
s_waitcnt lgkmcnt(2)
v_mfma_f32_16x16x32_bf16 v[124:127], a[92:95], a[76:79], v[124:127]
buffer_load_dwordx4 v243, s[8:11], s45 offen offset:3072 lds
v_mfma_f32_16x16x32_bf16 v[124:127], a[124:127], a[108:111], v[124:127]
ds_read_b128 a[76:79], v244 offset:5120
ds_read_b128 a[108:111], v244 offset:7168
v_mfma_f32_16x16x32_bf16 v[128:131], a[80:83], a[64:67], 0
ds_read_b64_tr_b16 a[88:89], v247 offset:0
ds_read_b64_tr_b16 a[90:91], v247 offset:4096
v_mfma_f32_16x16x32_bf16 v[128:131], a[112:115], a[96:99], v[128:131]
s_mov_b32 m0, s49
ds_read_b64_tr_b16 a[120:121], v247 offset:8192
ds_read_b64_tr_b16 a[122:123], v247 offset:12288
v_mfma_f32_16x16x32_bf16 v[132:135], a[84:87], a[64:67], 0
buffer_load_dwordx4 v7, s[4:7], s44 offen offset:0 sc1 lds
v_mfma_f32_16x16x32_bf16 v[132:135], a[116:119], a[96:99], v[132:135]
ds_read_b64_tr_b16 a[92:93], v248 offset:0
ds_read_b64_tr_b16 a[94:95], v248 offset:4096
v_mfma_f32_16x16x32_bf16 v[144:147], a[80:83], a[68:71], 0
ds_read_b64_tr_b16 a[124:125], v248 offset:8192
ds_read_b64_tr_b16 a[126:127], v248 offset:12288
v_mfma_f32_16x16x32_bf16 v[144:147], a[112:115], a[100:103], v[144:147]
s_add_u32 s45, s45, 131072
v_add_u32 v244, s53, v244
v_mfma_f32_16x16x32_bf16 v[148:151], a[84:87], a[68:71], 0
buffer_load_dwordx4 v7, s[4:7], s44 offen offset:1024 sc1 lds
v_mfma_f32_16x16x32_bf16 v[148:151], a[116:119], a[100:103], v[148:151]
s_waitcnt lgkmcnt(8)
v_mfma_f32_16x16x32_bf16 v[160:163], a[80:83], a[72:75], 0
v_add_u32 v247, s53, v247
v_add_u32 v248, s53, v248
v_mfma_f32_16x16x32_bf16 v[160:163], a[112:115], a[104:107], v[160:163]
v_mfma_f32_16x16x32_bf16 v[164:167], a[84:87], a[72:75], 0
buffer_load_dwordx4 v7, s[4:7], s44 offen offset:2048 sc1 lds
v_mfma_f32_16x16x32_bf16 v[164:167], a[116:119], a[104:107], v[164:167]
s_add_u32 s49, s46, s91
v_mfma_f32_16x16x32_bf16 v[176:179], a[80:83], a[76:79], 0
s_add_u32 s46, s46, s53
v_mfma_f32_16x16x32_bf16 v[176:179], a[112:115], a[108:111], v[176:179]
v_mfma_f32_16x16x32_bf16 v[180:183], a[84:87], a[76:79], 0
buffer_load_dwordx4 v7, s[4:7], s44 offen offset:3072 sc1 lds
v_mfma_f32_16x16x32_bf16 v[180:183], a[116:119], a[108:111], v[180:183]
s_waitcnt vmcnt(24) lgkmcnt(0)
s_barrier
s_mov_b32 s32, 4096
v_mfma_f32_16x16x32_bf16 v[136:139], a[88:91], a[64:67], 0
ds_read_b64_tr_b16 a[80:81], v245 offset:0
ds_read_b64_tr_b16 a[82:83], v245 offset:4096
v_mfma_f32_16x16x32_bf16 v[136:139], a[120:123], a[96:99], v[136:139]
s_add_u32 m0, s49, 16384
ds_read_b64_tr_b16 a[112:113], v245 offset:8192
ds_read_b64_tr_b16 a[114:115], v245 offset:12288
v_cvt_pk_bf16_f32 v64, v64, v65
v_mfma_f32_16x16x32_bf16 v[140:143], a[92:95], a[64:67], 0
buffer_load_dwordx4 v240, s[8:11], s45 offen offset:0 lds
v_mfma_f32_16x16x32_bf16 v[140:143], a[124:127], a[96:99], v[140:143]
ds_read_b128 a[64:67], v244 offset:0
ds_read_b128 a[96:99], v244 offset:2048
v_mfma_f32_16x16x32_bf16 v[152:155], a[88:91], a[68:71], 0
ds_read_b64_tr_b16 a[84:85], v246 offset:0
ds_read_b64_tr_b16 a[86:87], v246 offset:4096
v_cvt_pk_bf16_f32 v65, v66, v67
v_mfma_f32_16x16x32_bf16 v[152:155], a[120:123], a[100:103], v[152:155]
ds_read_b64_tr_b16 a[116:117], v246 offset:8192
ds_read_b64_tr_b16 a[118:119], v246 offset:12288
v_cvt_pk_bf16_f32 v66, v68, v69
v_mfma_f32_16x16x32_bf16 v[156:159], a[92:95], a[68:71], 0
buffer_load_dwordx4 v241, s[8:11], s45 offen offset:1024 lds
v_cvt_pk_bf16_f32 v67, v70, v71
v_mfma_f32_16x16x32_bf16 v[156:159], a[124:127], a[100:103], v[156:159]
buffer_store_dwordx4 v[64:67], v7, s[12:15], 0 offen offset:0 sc1
ds_read_b128 a[68:71], v244 offset:1024
ds_read_b128 a[100:103], v244 offset:3072
v_mfma_f32_16x16x32_bf16 v[168:171], a[88:91], a[72:75], 0
s_cmp_eq_u32 s46, 0
s_cselect_b32 s53, s56, s57
v_mfma_f32_16x16x32_bf16 v[168:171], a[120:123], a[104:107], v[168:171]
v_add_u32 v245, s53, v245
v_add_u32 v246, s53, v246
v_mfma_f32_16x16x32_bf16 v[172:175], a[92:95], a[72:75], 0
buffer_load_dwordx4 v242, s[8:11], s45 offen offset:2048 lds
v_mfma_f32_16x16x32_bf16 v[172:175], a[124:127], a[104:107], v[172:175]
ds_read_b128 a[72:75], v244 offset:4096
ds_read_b128 a[104:107], v244 offset:6144
v_mfma_f32_16x16x32_bf16 v[184:187], a[88:91], a[76:79], 0
s_add_u32 s44, s44, 4096
v_mfma_f32_16x16x32_bf16 v[184:187], a[120:123], a[108:111], v[184:187]
s_waitcnt lgkmcnt(2)
v_mfma_f32_16x16x32_bf16 v[188:191], a[92:95], a[76:79], 0
buffer_load_dwordx4 v243, s[8:11], s45 offen offset:3072 lds
v_mfma_f32_16x16x32_bf16 v[188:191], a[124:127], a[108:111], v[188:191]
ds_read_b128 a[76:79], v244 offset:5120
ds_read_b128 a[108:111], v244 offset:7168
v_mfma_f32_16x16x32_bf16 v[128:131], a[80:83], a[64:67], v[128:131]
ds_read_b64_tr_b16 a[88:89], v247 offset:0
ds_read_b64_tr_b16 a[90:91], v247 offset:4096
v_mfma_f32_16x16x32_bf16 v[128:131], a[112:115], a[96:99], v[128:131]
s_mov_b32 m0, s49
ds_read_b64_tr_b16 a[120:121], v247 offset:8192
ds_read_b64_tr_b16 a[122:123], v247 offset:12288
v_cvt_pk_bf16_f32 v72, v72, v73
v_mfma_f32_16x16x32_bf16 v[132:135], a[84:87], a[64:67], v[132:135]
buffer_load_dwordx4 v7, s[4:7], s44 offen offset:0 sc1 lds
v_mfma_f32_16x16x32_bf16 v[132:135], a[116:119], a[96:99], v[132:135]
ds_read_b64_tr_b16 a[92:93], v248 offset:0
ds_read_b64_tr_b16 a[94:95], v248 offset:4096
v_mfma_f32_16x16x32_bf16 v[144:147], a[80:83], a[68:71], v[144:147]
ds_read_b64_tr_b16 a[124:125], v248 offset:8192
ds_read_b64_tr_b16 a[126:127], v248 offset:12288
v_cvt_pk_bf16_f32 v73, v74, v75
v_mfma_f32_16x16x32_bf16 v[144:147], a[112:115], a[100:103], v[144:147]
s_add_u32 s45, s45, 131072
v_add_u32 v244, s53, v244
v_cvt_pk_bf16_f32 v74, v76, v77
v_mfma_f32_16x16x32_bf16 v[148:151], a[84:87], a[68:71], v[148:151]
buffer_load_dwordx4 v7, s[4:7], s44 offen offset:1024 sc1 lds
v_cvt_pk_bf16_f32 v75, v78, v79
v_mfma_f32_16x16x32_bf16 v[148:151], a[116:119], a[100:103], v[148:151]
s_waitcnt lgkmcnt(8)
buffer_store_dwordx4 v[72:75], v7, s[12:15], 0 offen offset:1024 sc1
v_mfma_f32_16x16x32_bf16 v[160:163], a[80:83], a[72:75], v[160:163]
v_add_u32 v247, s53, v247
v_add_u32 v248, s53, v248
v_mfma_f32_16x16x32_bf16 v[160:163], a[112:115], a[104:107], v[160:163]
v_mfma_f32_16x16x32_bf16 v[164:167], a[84:87], a[72:75], v[164:167]
buffer_load_dwordx4 v7, s[4:7], s44 offen offset:2048 sc1 lds
v_mfma_f32_16x16x32_bf16 v[164:167], a[116:119], a[104:107], v[164:167]
s_add_u32 s49, s46, s91
v_mfma_f32_16x16x32_bf16 v[176:179], a[80:83], a[76:79], v[176:179]
s_add_u32 s46, s46, s53
v_mfma_f32_16x16x32_bf16 v[176:179], a[112:115], a[108:111], v[176:179]
v_mfma_f32_16x16x32_bf16 v[180:183], a[84:87], a[76:79], v[180:183]
buffer_load_dwordx4 v7, s[4:7], s44 offen offset:3072 sc1 lds
v_mfma_f32_16x16x32_bf16 v[180:183], a[116:119], a[108:111], v[180:183]
s_waitcnt vmcnt(26) lgkmcnt(0)
s_barrier
v_mfma_f32_16x16x32_bf16 v[136:139], a[88:91], a[64:67], v[136:139]
ds_read_b64_tr_b16 a[80:81], v245 offset:0
ds_read_b64_tr_b16 a[82:83], v245 offset:4096
v_mfma_f32_16x16x32_bf16 v[136:139], a[120:123], a[96:99], v[136:139]
s_add_u32 m0, s49, 16384
ds_read_b64_tr_b16 a[112:113], v245 offset:8192
ds_read_b64_tr_b16 a[114:115], v245 offset:12288
v_cvt_pk_bf16_f32 v80, v80, v81
v_mfma_f32_16x16x32_bf16 v[140:143], a[92:95], a[64:67], v[140:143]
buffer_load_dwordx4 v240, s[8:11], s45 offen offset:0 lds
v_mfma_f32_16x16x32_bf16 v[140:143], a[124:127], a[96:99], v[140:143]
ds_read_b128 a[64:67], v244 offset:0
ds_read_b128 a[96:99], v244 offset:2048
v_mfma_f32_16x16x32_bf16 v[152:155], a[88:91], a[68:71], v[152:155]
ds_read_b64_tr_b16 a[84:85], v246 offset:0
ds_read_b64_tr_b16 a[86:87], v246 offset:4096
v_cvt_pk_bf16_f32 v81, v82, v83
v_mfma_f32_16x16x32_bf16 v[152:155], a[120:123], a[100:103], v[152:155]
ds_read_b64_tr_b16 a[116:117], v246 offset:8192
ds_read_b64_tr_b16 a[118:119], v246 offset:12288
v_cvt_pk_bf16_f32 v82, v84, v85
v_mfma_f32_16x16x32_bf16 v[156:159], a[92:95], a[68:71], v[156:159]
buffer_load_dwordx4 v241, s[8:11], s45 offen offset:1024 lds
v_cvt_pk_bf16_f32 v83, v86, v87
v_mfma_f32_16x16x32_bf16 v[156:159], a[124:127], a[100:103], v[156:159]
buffer_store_dwordx4 v[80:83], v7, s[12:15], 0 offen offset:2048 sc1
ds_read_b128 a[68:71], v244 offset:1024
ds_read_b128 a[100:103], v244 offset:3072
v_mfma_f32_16x16x32_bf16 v[168:171], a[88:91], a[72:75], v[168:171]
s_cmp_eq_u32 s46, 0
s_cselect_b32 s53, s56, s57
v_mfma_f32_16x16x32_bf16 v[168:171], a[120:123], a[104:107], v[168:171]
v_add_u32 v245, s53, v245
v_add_u32 v246, s53, v246
v_mfma_f32_16x16x32_bf16 v[172:175], a[92:95], a[72:75], v[172:175]
buffer_load_dwordx4 v242, s[8:11], s45 offen offset:2048 lds
v_mfma_f32_16x16x32_bf16 v[172:175], a[124:127], a[104:107], v[172:175]
ds_read_b128 a[72:75], v244 offset:4096
ds_read_b128 a[104:107], v244 offset:6144
v_mfma_f32_16x16x32_bf16 v[184:187], a[88:91], a[76:79], v[184:187]
s_add_u32 s44, s44, 4096
v_mfma_f32_16x16x32_bf16 v[184:187], a[120:123], a[108:111], v[184:187]
s_waitcnt lgkmcnt(2)
v_mfma_f32_16x16x32_bf16 v[188:191], a[92:95], a[76:79], v[188:191]
buffer_load_dwordx4 v243, s[8:11], s45 offen offset:3072 lds
v_mfma_f32_16x16x32_bf16 v[188:191], a[124:127], a[108:111], v[188:191]
ds_read_b128 a[76:79], v244 offset:5120
ds_read_b128 a[108:111], v244 offset:7168
.Lpad22:
s_cbranch_execz .Ltch11
v_mfma_f32_16x16x32_bf16 v[128:131], a[80:83], a[64:67], v[128:131]
ds_read_b64_tr_b16 a[88:89], v247 offset:0
ds_read_b64_tr_b16 a[90:91], v247 offset:4096
v_mfma_f32_16x16x32_bf16 v[128:131], a[112:115], a[96:99], v[128:131]
s_mov_b32 m0, s49
ds_read_b64_tr_b16 a[120:121], v247 offset:8192
ds_read_b64_tr_b16 a[122:123], v247 offset:12288
v_cvt_pk_bf16_f32 v88, v88, v89
v_mfma_f32_16x16x32_bf16 v[132:135], a[84:87], a[64:67], v[132:135]
buffer_load_dwordx4 v7, s[4:7], s44 offen offset:0 sc1 lds
v_mfma_f32_16x16x32_bf16 v[132:135], a[116:119], a[96:99], v[132:135]
ds_read_b64_tr_b16 a[92:93], v248 offset:0
ds_read_b64_tr_b16 a[94:95], v248 offset:4096
v_mfma_f32_16x16x32_bf16 v[144:147], a[80:83], a[68:71], v[144:147]
ds_read_b64_tr_b16 a[124:125], v248 offset:8192
ds_read_b64_tr_b16 a[126:127], v248 offset:12288
v_cvt_pk_bf16_f32 v89, v90, v91
v_mfma_f32_16x16x32_bf16 v[144:147], a[112:115], a[100:103], v[144:147]
s_add_u32 s45, s45, 131072
v_add_u32 v244, s53, v244
v_cvt_pk_bf16_f32 v90, v92, v93
v_mfma_f32_16x16x32_bf16 v[148:151], a[84:87], a[68:71], v[148:151]
buffer_load_dwordx4 v7, s[4:7], s44 offen offset:1024 sc1 lds
v_cvt_pk_bf16_f32 v91, v94, v95
v_mfma_f32_16x16x32_bf16 v[148:151], a[116:119], a[100:103], v[148:151]
s_waitcnt lgkmcnt(8)
buffer_store_dwordx4 v[88:91], v7, s[12:15], 0 offen offset:3072 sc1
v_mfma_f32_16x16x32_bf16 v[160:163], a[80:83], a[72:75], v[160:163]
v_add_u32 v247, s53, v247
v_add_u32 v248, s53, v248
v_mfma_f32_16x16x32_bf16 v[160:163], a[112:115], a[104:107], v[160:163]
v_mfma_f32_16x16x32_bf16 v[164:167], a[84:87], a[72:75], v[164:167]
buffer_load_dwordx4 v7, s[4:7], s44 offen offset:2048 sc1 lds
v_mfma_f32_16x16x32_bf16 v[164:167], a[116:119], a[104:107], v[164:167]
s_add_u32 s49, s46, s91
v_mfma_f32_16x16x32_bf16 v[176:179], a[80:83], a[76:79], v[176:179]
s_add_u32 s46, s46, s53
v_mfma_f32_16x16x32_bf16 v[176:179], a[112:115], a[108:111], v[176:179]
v_mfma_f32_16x16x32_bf16 v[180:183], a[84:87], a[76:79], v[180:183]
buffer_load_dwordx4 v7, s[4:7], s44 offen offset:3072 sc1 lds
v_mfma_f32_16x16x32_bf16 v[180:183], a[116:119], a[108:111], v[180:183]
s_waitcnt vmcnt(28) lgkmcnt(0)
s_barrier
v_mfma_f32_16x16x32_bf16 v[136:139], a[88:91], a[64:67], v[136:139]
ds_read_b64_tr_b16 a[80:81], v245 offset:0
ds_read_b64_tr_b16 a[82:83], v245 offset:4096
v_mfma_f32_16x16x32_bf16 v[136:139], a[120:123], a[96:99], v[136:139]
s_add_u32 m0, s49, 16384
ds_read_b64_tr_b16 a[112:113], v245 offset:8192
ds_read_b64_tr_b16 a[114:115], v245 offset:12288
v_cvt_pk_bf16_f32 v96, v96, v97
v_mfma_f32_16x16x32_bf16 v[140:143], a[92:95], a[64:67], v[140:143]
buffer_load_dwordx4 v240, s[8:11], s45 offen offset:0 lds
v_mfma_f32_16x16x32_bf16 v[140:143], a[124:127], a[96:99], v[140:143]
ds_read_b128 a[64:67], v244 offset:0
ds_read_b128 a[96:99], v244 offset:2048
v_mfma_f32_16x16x32_bf16 v[152:155], a[88:91], a[68:71], v[152:155]
ds_read_b64_tr_b16 a[84:85], v246 offset:0
ds_read_b64_tr_b16 a[86:87], v246 offset:4096
v_cvt_pk_bf16_f32 v97, v98, v99
v_mfma_f32_16x16x32_bf16 v[152:155], a[120:123], a[100:103], v[152:155]
ds_read_b64_tr_b16 a[116:117], v246 offset:8192
ds_read_b64_tr_b16 a[118:119], v246 offset:12288
v_cvt_pk_bf16_f32 v98, v100, v101
v_mfma_f32_16x16x32_bf16 v[156:159], a[92:95], a[68:71], v[156:159]
buffer_load_dwordx4 v241, s[8:11], s45 offen offset:1024 lds
v_cvt_pk_bf16_f32 v99, v102, v103
v_mfma_f32_16x16x32_bf16 v[156:159], a[124:127], a[100:103], v[156:159]
buffer_store_dwordx4 v[96:99], v7, s[12:15], s32 offen offset:0 sc1
ds_read_b128 a[68:71], v244 offset:1024
ds_read_b128 a[100:103], v244 offset:3072
v_mfma_f32_16x16x32_bf16 v[168:171], a[88:91], a[72:75], v[168:171]
s_cmp_eq_u32 s46, 0
s_cselect_b32 s53, s56, s57
v_mfma_f32_16x16x32_bf16 v[168:171], a[120:123], a[104:107], v[168:171]
v_add_u32 v245, s53, v245
v_add_u32 v246, s53, v246
v_mfma_f32_16x16x32_bf16 v[172:175], a[92:95], a[72:75], v[172:175]
buffer_load_dwordx4 v242, s[8:11], s45 offen offset:2048 lds
v_mfma_f32_16x16x32_bf16 v[172:175], a[124:127], a[104:107], v[172:175]
ds_read_b128 a[72:75], v244 offset:4096
ds_read_b128 a[104:107], v244 offset:6144
v_mfma_f32_16x16x32_bf16 v[184:187], a[88:91], a[76:79], v[184:187]
s_add_u32 s44, s44, 4096
v_mfma_f32_16x16x32_bf16 v[184:187], a[120:123], a[108:111], v[184:187]
s_waitcnt lgkmcnt(2)
v_mfma_f32_16x16x32_bf16 v[188:191], a[92:95], a[76:79], v[188:191]
buffer_load_dwordx4 v243, s[8:11], s45 offen offset:3072 lds
v_mfma_f32_16x16x32_bf16 v[188:191], a[124:127], a[108:111], v[188:191]
ds_read_b128 a[76:79], v244 offset:5120
ds_read_b128 a[108:111], v244 offset:7168
v_mfma_f32_16x16x32_bf16 v[128:131], a[80:83], a[64:67], v[128:131]
ds_read_b64_tr_b16 a[88:89], v247 offset:0
ds_read_b64_tr_b16 a[90:91], v247 offset:4096
v_mfma_f32_16x16x32_bf16 v[128:131], a[112:115], a[96:99], v[128:131]
s_mov_b32 m0, s49
ds_read_b64_tr_b16 a[120:121], v247 offset:8192
ds_read_b64_tr_b16 a[122:123], v247 offset:12288
v_cvt_pk_bf16_f32 v104, v104, v105
v_mfma_f32_16x16x32_bf16 v[132:135], a[84:87], a[64:67], v[132:135]
buffer_load_dwordx4 v7, s[4:7], s44 offen offset:0 sc1 lds
v_mfma_f32_16x16x32_bf16 v[132:135], a[116:119], a[96:99], v[132:135]
ds_read_b64_tr_b16 a[92:93], v248 offset:0
ds_read_b64_tr_b16 a[94:95], v248 offset:4096
v_mfma_f32_16x16x32_bf16 v[144:147], a[80:83], a[68:71], v[144:147]
ds_read_b64_tr_b16 a[124:125], v248 offset:8192
ds_read_b64_tr_b16 a[126:127], v248 offset:12288
v_cvt_pk_bf16_f32 v105, v106, v107
v_mfma_f32_16x16x32_bf16 v[144:147], a[112:115], a[100:103], v[144:147]
s_add_u32 s45, s45, 131072
v_add_u32 v244, s53, v244
v_cvt_pk_bf16_f32 v106, v108, v109
v_mfma_f32_16x16x32_bf16 v[148:151], a[84:87], a[68:71], v[148:151]
buffer_load_dwordx4 v7, s[4:7], s44 offen offset:1024 sc1 lds
v_cvt_pk_bf16_f32 v107, v110, v111
v_mfma_f32_16x16x32_bf16 v[148:151], a[116:119], a[100:103], v[148:151]
s_waitcnt lgkmcnt(8)
buffer_store_dwordx4 v[104:107], v7, s[12:15], s32 offen offset:1024 sc1
v_mfma_f32_16x16x32_bf16 v[160:163], a[80:83], a[72:75], v[160:163]
v_add_u32 v247, s53, v247
v_add_u32 v248, s53, v248
v_mfma_f32_16x16x32_bf16 v[160:163], a[112:115], a[104:107], v[160:163]
v_mfma_f32_16x16x32_bf16 v[164:167], a[84:87], a[72:75], v[164:167]
buffer_load_dwordx4 v7, s[4:7], s44 offen offset:2048 sc1 lds
v_mfma_f32_16x16x32_bf16 v[164:167], a[116:119], a[104:107], v[164:167]
s_add_u32 s49, s46, s91
v_mfma_f32_16x16x32_bf16 v[176:179], a[80:83], a[76:79], v[176:179]
s_add_u32 s46, s46, s53
v_mfma_f32_16x16x32_bf16 v[176:179], a[112:115], a[108:111], v[176:179]
v_mfma_f32_16x16x32_bf16 v[180:183], a[84:87], a[76:79], v[180:183]
buffer_load_dwordx4 v7, s[4:7], s44 offen offset:3072 sc1 lds
v_mfma_f32_16x16x32_bf16 v[180:183], a[116:119], a[108:111], v[180:183]
s_waitcnt vmcnt(30) lgkmcnt(0)
s_barrier
v_mfma_f32_16x16x32_bf16 v[136:139], a[88:91], a[64:67], v[136:139]
ds_read_b64_tr_b16 a[80:81], v245 offset:0
ds_read_b64_tr_b16 a[82:83], v245 offset:4096
v_mfma_f32_16x16x32_bf16 v[136:139], a[120:123], a[96:99], v[136:139]
s_add_u32 m0, s49, 16384
ds_read_b64_tr_b16 a[112:113], v245 offset:8192
ds_read_b64_tr_b16 a[114:115], v245 offset:12288
v_cvt_pk_bf16_f32 v112, v112, v113
v_mfma_f32_16x16x32_bf16 v[140:143], a[92:95], a[64:67], v[140:143]
buffer_load_dwordx4 v240, s[8:11], s45 offen offset:0 lds
v_mfma_f32_16x16x32_bf16 v[140:143], a[124:127], a[96:99], v[140:143]
ds_read_b128 a[64:67], v244 offset:0
ds_read_b128 a[96:99], v244 offset:2048
v_mfma_f32_16x16x32_bf16 v[152:155], a[88:91], a[68:71], v[152:155]
ds_read_b64_tr_b16 a[84:85], v246 offset:0
ds_read_b64_tr_b16 a[86:87], v246 offset:4096
v_cvt_pk_bf16_f32 v113, v114, v115
v_mfma_f32_16x16x32_bf16 v[152:155], a[120:123], a[100:103], v[152:155]
ds_read_b64_tr_b16 a[116:117], v246 offset:8192
ds_read_b64_tr_b16 a[118:119], v246 offset:12288
v_cvt_pk_bf16_f32 v114, v116, v117
v_mfma_f32_16x16x32_bf16 v[156:159], a[92:95], a[68:71], v[156:159]
buffer_load_dwordx4 v241, s[8:11], s45 offen offset:1024 lds
v_cvt_pk_bf16_f32 v115, v118, v119
v_mfma_f32_16x16x32_bf16 v[156:159], a[124:127], a[100:103], v[156:159]
buffer_store_dwordx4 v[112:115], v7, s[12:15], s32 offen offset:2048 sc1
ds_read_b128 a[68:71], v244 offset:1024
ds_read_b128 a[100:103], v244 offset:3072
v_mfma_f32_16x16x32_bf16 v[168:171], a[88:91], a[72:75], v[168:171]
s_cmp_eq_u32 s46, 0
s_cselect_b32 s53, s56, s57
v_mfma_f32_16x16x32_bf16 v[168:171], a[120:123], a[104:107], v[168:171]
v_add_u32 v245, s53, v245
v_add_u32 v246, s53, v246
v_mfma_f32_16x16x32_bf16 v[172:175], a[92:95], a[72:75], v[172:175]
buffer_load_dwordx4 v242, s[8:11], s45 offen offset:2048 lds
v_mfma_f32_16x16x32_bf16 v[172:175], a[124:127], a[104:107], v[172:175]
ds_read_b128 a[72:75], v244 offset:4096
ds_read_b128 a[104:107], v244 offset:6144
v_mfma_f32_16x16x32_bf16 v[184:187], a[88:91], a[76:79], v[184:187]
s_add_u32 s44, s44, 4096
v_mfma_f32_16x16x32_bf16 v[184:187], a[120:123], a[108:111], v[184:187]
s_waitcnt lgkmcnt(2)
v_mfma_f32_16x16x32_bf16 v[188:191], a[92:95], a[76:79], v[188:191]
buffer_load_dwordx4 v243, s[8:11], s45 offen offset:3072 lds
v_mfma_f32_16x16x32_bf16 v[188:191], a[124:127], a[108:111], v[188:191]
ds_read_b128 a[76:79], v244 offset:5120
ds_read_b128 a[108:111], v244 offset:7168
v_mfma_f32_16x16x32_bf16 v[128:131], a[80:83], a[64:67], v[128:131]
ds_read_b64_tr_b16 a[88:89], v247 offset:0
ds_read_b64_tr_b16 a[90:91], v247 offset:4096
v_mfma_f32_16x16x32_bf16 v[128:131], a[112:115], a[96:99], v[128:131]
s_mov_b32 m0, s49
ds_read_b64_tr_b16 a[120:121], v247 offset:8192
ds_read_b64_tr_b16 a[122:123], v247 offset:12288
v_cvt_pk_bf16_f32 v120, v120, v121
v_mfma_f32_16x16x32_bf16 v[132:135], a[84:87], a[64:67], v[132:135]
buffer_load_dwordx4 v7, s[4:7], s44 offen offset:0 sc1 lds
v_mfma_f32_16x16x32_bf16 v[132:135], a[116:119], a[96:99], v[132:135]
ds_read_b64_tr_b16 a[92:93], v248 offset:0
ds_read_b64_tr_b16 a[94:95], v248 offset:4096
v_mfma_f32_16x16x32_bf16 v[144:147], a[80:83], a[68:71], v[144:147]
ds_read_b64_tr_b16 a[124:125], v248 offset:8192
ds_read_b64_tr_b16 a[126:127], v248 offset:12288
v_cvt_pk_bf16_f32 v121, v122, v123
v_mfma_f32_16x16x32_bf16 v[144:147], a[112:115], a[100:103], v[144:147]
s_add_u32 s45, s45, 131072
v_add_u32 v244, s53, v244
v_cvt_pk_bf16_f32 v122, v124, v125
v_mfma_f32_16x16x32_bf16 v[148:151], a[84:87], a[68:71], v[148:151]
buffer_load_dwordx4 v7, s[4:7], s44 offen offset:1024 sc1 lds
v_cvt_pk_bf16_f32 v123, v126, v127
v_mfma_f32_16x16x32_bf16 v[148:151], a[116:119], a[100:103], v[148:151]
s_waitcnt lgkmcnt(8)
buffer_store_dwordx4 v[120:123], v7, s[12:15], s32 offen offset:3072 sc1
v_mfma_f32_16x16x32_bf16 v[160:163], a[80:83], a[72:75], v[160:163]
v_add_u32 v247, s53, v247
v_add_u32 v248, s53, v248
v_mfma_f32_16x16x32_bf16 v[160:163], a[112:115], a[104:107], v[160:163]
v_mfma_f32_16x16x32_bf16 v[164:167], a[84:87], a[72:75], v[164:167]
buffer_load_dwordx4 v7, s[4:7], s44 offen offset:2048 sc1 lds
v_mfma_f32_16x16x32_bf16 v[164:167], a[116:119], a[104:107], v[164:167]
s_add_u32 s49, s46, s91
v_mfma_f32_16x16x32_bf16 v[176:179], a[80:83], a[76:79], v[176:179]
s_add_u32 s46, s46, s53
v_mfma_f32_16x16x32_bf16 v[176:179], a[112:115], a[108:111], v[176:179]
v_mfma_f32_16x16x32_bf16 v[180:183], a[84:87], a[76:79], v[180:183]
buffer_load_dwordx4 v7, s[4:7], s44 offen offset:3072 sc1 lds
v_mfma_f32_16x16x32_bf16 v[180:183], a[116:119], a[108:111], v[180:183]
s_waitcnt vmcnt(30) lgkmcnt(0)
s_barrier
v_mfma_f32_16x16x32_bf16 v[136:139], a[88:91], a[64:67], v[136:139]
ds_read_b64_tr_b16 a[80:81], v245 offset:0
ds_read_b64_tr_b16 a[82:83], v245 offset:4096
v_mfma_f32_16x16x32_bf16 v[136:139], a[120:123], a[96:99], v[136:139]
s_add_u32 m0, s49, 16384
ds_read_b64_tr_b16 a[112:113], v245 offset:8192
ds_read_b64_tr_b16 a[114:115], v245 offset:12288
v_mfma_f32_16x16x32_bf16 v[140:143], a[92:95], a[64:67], v[140:143]
buffer_load_dwordx4 v240, s[8:11], s45 offen offset:0 lds
v_mfma_f32_16x16x32_bf16 v[140:143], a[124:127], a[96:99], v[140:143]
ds_read_b128 a[64:67], v244 offset:0
ds_read_b128 a[96:99], v244 offset:2048
v_mfma_f32_16x16x32_bf16 v[152:155], a[88:91], a[68:71], v[152:155]
ds_read_b64_tr_b16 a[84:85], v246 offset:0
ds_read_b64_tr_b16 a[86:87], v246 offset:4096
v_mfma_f32_16x16x32_bf16 v[152:155], a[120:123], a[100:103], v[152:155]
ds_read_b64_tr_b16 a[116:117], v246 offset:8192
ds_read_b64_tr_b16 a[118:119], v246 offset:12288
v_mfma_f32_16x16x32_bf16 v[156:159], a[92:95], a[68:71], v[156:159]
buffer_load_dwordx4 v241, s[8:11], s45 offen offset:1024 lds
v_mfma_f32_16x16x32_bf16 v[156:159], a[124:127], a[100:103], v[156:159]
ds_read_b128 a[68:71], v244 offset:1024
ds_read_b128 a[100:103], v244 offset:3072
v_mfma_f32_16x16x32_bf16 v[168:171], a[88:91], a[72:75], v[168:171]
s_cmp_eq_u32 s46, 0
s_cselect_b32 s53, s56, s57
v_mfma_f32_16x16x32_bf16 v[168:171], a[120:123], a[104:107], v[168:171]
v_add_u32 v245, s53, v245
v_add_u32 v246, s53, v246
v_mfma_f32_16x16x32_bf16 v[172:175], a[92:95], a[72:75], v[172:175]
buffer_load_dwordx4 v242, s[8:11], s45 offen offset:2048 lds
v_mfma_f32_16x16x32_bf16 v[172:175], a[124:127], a[104:107], v[172:175]
ds_read_b128 a[72:75], v244 offset:4096
ds_read_b128 a[104:107], v244 offset:6144
v_mfma_f32_16x16x32_bf16 v[184:187], a[88:91], a[76:79], v[184:187]
s_add_u32 s44, s44, 4096
v_mfma_f32_16x16x32_bf16 v[184:187], a[120:123], a[108:111], v[184:187]
s_waitcnt lgkmcnt(2)
v_mfma_f32_16x16x32_bf16 v[188:191], a[92:95], a[76:79], v[188:191]
buffer_load_dwordx4 v243, s[8:11], s45 offen offset:3072 lds
v_mfma_f32_16x16x32_bf16 v[188:191], a[124:127], a[108:111], v[188:191]
ds_read_b128 a[76:79], v244 offset:5120
ds_read_b128 a[108:111], v244 offset:7168
v_mfma_f32_16x16x32_bf16 v[128:131], a[80:83], a[64:67], v[128:131]
ds_read_b64_tr_b16 a[88:89], v247 offset:0
ds_read_b64_tr_b16 a[90:91], v247 offset:4096
v_mfma_f32_16x16x32_bf16 v[128:131], a[112:115], a[96:99], v[128:131]
s_mov_b32 m0, s49
ds_read_b64_tr_b16 a[120:121], v247 offset:8192
ds_read_b64_tr_b16 a[122:123], v247 offset:12288
v_mfma_f32_16x16x32_bf16 v[132:135], a[84:87], a[64:67], v[132:135]
buffer_load_dwordx4 v7, s[4:7], s44 offen offset:0 sc1 lds
v_mfma_f32_16x16x32_bf16 v[132:135], a[116:119], a[96:99], v[132:135]
ds_read_b64_tr_b16 a[92:93], v248 offset:0
ds_read_b64_tr_b16 a[94:95], v248 offset:4096
v_mfma_f32_16x16x32_bf16 v[144:147], a[80:83], a[68:71], v[144:147]
ds_read_b64_tr_b16 a[124:125], v248 offset:8192
ds_read_b64_tr_b16 a[126:127], v248 offset:12288
v_mfma_f32_16x16x32_bf16 v[144:147], a[112:115], a[100:103], v[144:147]
s_add_u32 s45, s45, 131072
v_add_u32 v244, s53, v244
v_mfma_f32_16x16x32_bf16 v[148:151], a[84:87], a[68:71], v[148:151]
buffer_load_dwordx4 v7, s[4:7], s44 offen offset:1024 sc1 lds
v_mfma_f32_16x16x32_bf16 v[148:151], a[116:119], a[100:103], v[148:151]
s_waitcnt lgkmcnt(8)
v_mfma_f32_16x16x32_bf16 v[160:163], a[80:83], a[72:75], v[160:163]
v_add_u32 v247, s53, v247
v_add_u32 v248, s53, v248
v_mfma_f32_16x16x32_bf16 v[160:163], a[112:115], a[104:107], v[160:163]
v_mfma_f32_16x16x32_bf16 v[164:167], a[84:87], a[72:75], v[164:167]
buffer_load_dwordx4 v7, s[4:7], s44 offen offset:2048 sc1 lds
v_mfma_f32_16x16x32_bf16 v[164:167], a[116:119], a[104:107], v[164:167]
s_add_u32 s49, s46, s91
v_mfma_f32_16x16x32_bf16 v[176:179], a[80:83], a[76:79], v[176:179]
s_add_u32 s46, s46, s53
v_mfma_f32_16x16x32_bf16 v[176:179], a[112:115], a[108:111], v[176:179]
v_mfma_f32_16x16x32_bf16 v[180:183], a[84:87], a[76:79], v[180:183]
buffer_load_dwordx4 v7, s[4:7], s44 offen offset:3072 sc1 lds
v_mfma_f32_16x16x32_bf16 v[180:183], a[116:119], a[108:111], v[180:183]
s_waitcnt vmcnt(28) lgkmcnt(0)
s_barrier
v_mfma_f32_16x16x32_bf16 v[136:139], a[88:91], a[64:67], v[136:139]
ds_read_b64_tr_b16 a[80:81], v245 offset:0
ds_read_b64_tr_b16 a[82:83], v245 offset:4096
v_mfma_f32_16x16x32_bf16 v[136:139], a[120:123], a[96:99], v[136:139]
s_add_u32 m0, s49, 16384
ds_read_b64_tr_b16 a[112:113], v245 offset:8192
ds_read_b64_tr_b16 a[114:115], v245 offset:12288
v_mfma_f32_16x16x32_bf16 v[140:143], a[92:95], a[64:67], v[140:143]
buffer_load_dwordx4 v240, s[8:11], s45 offen offset:0 lds
v_mfma_f32_16x16x32_bf16 v[140:143], a[124:127], a[96:99], v[140:143]
ds_read_b128 a[64:67], v244 offset:0
ds_read_b128 a[96:99], v244 offset:2048
v_mfma_f32_16x16x32_bf16 v[152:155], a[88:91], a[68:71], v[152:155]
ds_read_b64_tr_b16 a[84:85], v246 offset:0
ds_read_b64_tr_b16 a[86:87], v246 offset:4096
v_mfma_f32_16x16x32_bf16 v[152:155], a[120:123], a[100:103], v[152:155]
ds_read_b64_tr_b16 a[116:117], v246 offset:8192
ds_read_b64_tr_b16 a[118:119], v246 offset:12288
v_mfma_f32_16x16x32_bf16 v[156:159], a[92:95], a[68:71], v[156:159]
buffer_load_dwordx4 v241, s[8:11], s45 offen offset:1024 lds
v_mfma_f32_16x16x32_bf16 v[156:159], a[124:127], a[100:103], v[156:159]
ds_read_b128 a[68:71], v244 offset:1024
ds_read_b128 a[100:103], v244 offset:3072
v_mfma_f32_16x16x32_bf16 v[168:171], a[88:91], a[72:75], v[168:171]
s_cmp_eq_u32 s46, 0
s_cselect_b32 s53, s56, s57
v_mfma_f32_16x16x32_bf16 v[168:171], a[120:123], a[104:107], v[168:171]
v_add_u32 v245, s53, v245
v_add_u32 v246, s53, v246
v_mfma_f32_16x16x32_bf16 v[172:175], a[92:95], a[72:75], v[172:175]
buffer_load_dwordx4 v242, s[8:11], s45 offen offset:2048 lds
v_mfma_f32_16x16x32_bf16 v[172:175], a[124:127], a[104:107], v[172:175]
ds_read_b128 a[72:75], v244 offset:4096
ds_read_b128 a[104:107], v244 offset:6144
v_mfma_f32_16x16x32_bf16 v[184:187], a[88:91], a[76:79], v[184:187]
s_add_u32 s44, s44, 4096
v_mfma_f32_16x16x32_bf16 v[184:187], a[120:123], a[108:111], v[184:187]
s_waitcnt lgkmcnt(2)
v_mfma_f32_16x16x32_bf16 v[188:191], a[92:95], a[76:79], v[188:191]
buffer_load_dwordx4 v243, s[8:11], s45 offen offset:3072 lds
v_mfma_f32_16x16x32_bf16 v[188:191], a[124:127], a[108:111], v[188:191]
ds_read_b128 a[76:79], v244 offset:5120
ds_read_b128 a[108:111], v244 offset:7168
v_mfma_f32_16x16x32_bf16 v[128:131], a[80:83], a[64:67], v[128:131]
ds_read_b64_tr_b16 a[88:89], v247 offset:0
ds_read_b64_tr_b16 a[90:91], v247 offset:4096
v_mfma_f32_16x16x32_bf16 v[128:131], a[112:115], a[96:99], v[128:131]
s_mov_b32 m0, s49
ds_read_b64_tr_b16 a[120:121], v247 offset:8192
ds_read_b64_tr_b16 a[122:123], v247 offset:12288
v_mfma_f32_16x16x32_bf16 v[132:135], a[84:87], a[64:67], v[132:135]
buffer_load_dwordx4 v7, s[4:7], s44 offen offset:0 sc1 lds
v_mfma_f32_16x16x32_bf16 v[132:135], a[116:119], a[96:99], v[132:135]
ds_read_b64_tr_b16 a[92:93], v248 offset:0
ds_read_b64_tr_b16 a[94:95], v248 offset:4096
v_mfma_f32_16x16x32_bf16 v[144:147], a[80:83], a[68:71], v[144:147]
ds_read_b64_tr_b16 a[124:125], v248 offset:8192
ds_read_b64_tr_b16 a[126:127], v248 offset:12288
v_mfma_f32_16x16x32_bf16 v[144:147], a[112:115], a[100:103], v[144:147]
s_add_u32 s45, s45, 131072
v_add_u32 v244, s53, v244
v_mfma_f32_16x16x32_bf16 v[148:151], a[84:87], a[68:71], v[148:151]
buffer_load_dwordx4 v7, s[4:7], s44 offen offset:1024 sc1 lds
v_mfma_f32_16x16x32_bf16 v[148:151], a[116:119], a[100:103], v[148:151]
s_waitcnt lgkmcnt(8)
v_mfma_f32_16x16x32_bf16 v[160:163], a[80:83], a[72:75], v[160:163]
v_add_u32 v247, s53, v247
v_add_u32 v248, s53, v248
v_mfma_f32_16x16x32_bf16 v[160:163], a[112:115], a[104:107], v[160:163]
v_mfma_f32_16x16x32_bf16 v[164:167], a[84:87], a[72:75], v[164:167]
buffer_load_dwordx4 v7, s[4:7], s44 offen offset:2048 sc1 lds
v_mfma_f32_16x16x32_bf16 v[164:167], a[116:119], a[104:107], v[164:167]
s_add_u32 s49, s46, s91
v_mfma_f32_16x16x32_bf16 v[176:179], a[80:83], a[76:79], v[176:179]
s_add_u32 s46, s46, s53
v_mfma_f32_16x16x32_bf16 v[176:179], a[112:115], a[108:111], v[176:179]
v_mfma_f32_16x16x32_bf16 v[180:183], a[84:87], a[76:79], v[180:183]
buffer_load_dwordx4 v7, s[4:7], s44 offen offset:3072 sc1 lds
v_mfma_f32_16x16x32_bf16 v[180:183], a[116:119], a[108:111], v[180:183]
s_waitcnt vmcnt(18)
s_waitcnt vmcnt(26) lgkmcnt(0)
s_barrier
v_mfma_f32_16x16x32_bf16 v[136:139], a[88:91], a[64:67], v[136:139]
ds_read_b64_tr_b16 a[80:81], v245 offset:0
ds_read_b64_tr_b16 a[82:83], v245 offset:4096
v_mfma_f32_16x16x32_bf16 v[136:139], a[120:123], a[96:99], v[136:139]
s_add_u32 m0, s49, 16384
ds_read_b64_tr_b16 a[112:113], v245 offset:8192
ds_read_b64_tr_b16 a[114:115], v245 offset:12288
v_mfma_f32_16x16x32_bf16 v[140:143], a[92:95], a[64:67], v[140:143]
buffer_load_dwordx4 v240, s[8:11], s45 offen offset:0 lds
v_mfma_f32_16x16x32_bf16 v[140:143], a[124:127], a[96:99], v[140:143]
ds_read_b128 a[64:67], v244 offset:0
ds_read_b128 a[96:99], v244 offset:2048
v_mfma_f32_16x16x32_bf16 v[152:155], a[88:91], a[68:71], v[152:155]
ds_read_b64_tr_b16 a[84:85], v246 offset:0
ds_read_b64_tr_b16 a[86:87], v246 offset:4096
v_mfma_f32_16x16x32_bf16 v[152:155], a[120:123], a[100:103], v[152:155]
ds_read_b64_tr_b16 a[116:117], v246 offset:8192
ds_read_b64_tr_b16 a[118:119], v246 offset:12288
v_mfma_f32_16x16x32_bf16 v[156:159], a[92:95], a[68:71], v[156:159]
buffer_load_dwordx4 v241, s[8:11], s45 offen offset:1024 lds
v_mfma_f32_16x16x32_bf16 v[156:159], a[124:127], a[100:103], v[156:159]
ds_read_b128 a[68:71], v244 offset:1024
ds_read_b128 a[100:103], v244 offset:3072
v_mfma_f32_16x16x32_bf16 v[168:171], a[88:91], a[72:75], v[168:171]
s_cmp_eq_u32 s46, 0
s_cselect_b32 s53, s56, s57
v_mfma_f32_16x16x32_bf16 v[168:171], a[120:123], a[104:107], v[168:171]
v_add_u32 v245, s53, v245
v_add_u32 v246, s53, v246
v_mfma_f32_16x16x32_bf16 v[172:175], a[92:95], a[72:75], v[172:175]
s_nop 0
buffer_load_dwordx4 v242, s[8:11], s45 offen offset:2048 lds
v_or_b32 v0, 5, v11
v_mfma_f32_16x16x32_bf16 v[172:175], a[124:127], a[104:107], v[172:175]
ds_read_b128 a[72:75], v244 offset:4096
ds_read_b128 a[104:107], v244 offset:6144
v_mfma_f32_16x16x32_bf16 v[184:187], a[88:91], a[76:79], v[184:187]
s_add_u32 s44, s44, 4096
v_mfma_f32_16x16x32_bf16 v[184:187], a[120:123], a[108:111], v[184:187]
s_waitcnt lgkmcnt(2)
v_mfma_f32_16x16x32_bf16 v[188:191], a[92:95], a[76:79], v[188:191]
buffer_load_dwordx4 v243, s[8:11], s45 offen offset:3072 lds
buffer_store_dword v0, v6, s[60:63], 0 offen sc1
v_mfma_f32_16x16x32_bf16 v[188:191], a[124:127], a[108:111], v[188:191]
ds_read_b128 a[76:79], v244 offset:5120
ds_read_b128 a[108:111], v244 offset:7168
v_mfma_f32_16x16x32_bf16 v[128:131], a[80:83], a[64:67], v[128:131]
ds_read_b64_tr_b16 a[88:89], v247 offset:0
ds_read_b64_tr_b16 a[90:91], v247 offset:4096
v_mfma_f32_16x16x32_bf16 v[128:131], a[112:115], a[96:99], v[128:131]
s_mov_b32 m0, s49
ds_read_b64_tr_b16 a[120:121], v247 offset:8192
ds_read_b64_tr_b16 a[122:123], v247 offset:12288
v_mfma_f32_16x16x32_bf16 v[132:135], a[84:87], a[64:67], v[132:135]
buffer_load_dwordx4 v7, s[4:7], s44 offen offset:0 sc1 lds
v_mfma_f32_16x16x32_bf16 v[132:135], a[116:119], a[96:99], v[132:135]
ds_read_b64_tr_b16 a[92:93], v248 offset:0
ds_read_b64_tr_b16 a[94:95], v248 offset:4096
v_mfma_f32_16x16x32_bf16 v[144:147], a[80:83], a[68:71], v[144:147]
ds_read_b64_tr_b16 a[124:125], v248 offset:8192
ds_read_b64_tr_b16 a[126:127], v248 offset:12288
v_mfma_f32_16x16x32_bf16 v[144:147], a[112:115], a[100:103], v[144:147]
s_add_u32 s45, s45, 131072
v_add_u32 v244, s53, v244
v_mfma_f32_16x16x32_bf16 v[148:151], a[84:87], a[68:71], v[148:151]
buffer_load_dwordx4 v7, s[4:7], s44 offen offset:1024 sc1 lds
v_mfma_f32_16x16x32_bf16 v[148:151], a[116:119], a[100:103], v[148:151]
s_waitcnt lgkmcnt(8)
v_mfma_f32_16x16x32_bf16 v[160:163], a[80:83], a[72:75], v[160:163]
v_add_u32 v247, s53, v247
v_add_u32 v248, s53, v248
v_mfma_f32_16x16x32_bf16 v[160:163], a[112:115], a[104:107], v[160:163]
v_mfma_f32_16x16x32_bf16 v[164:167], a[84:87], a[72:75], v[164:167]
buffer_load_dwordx4 v7, s[4:7], s44 offen offset:2048 sc1 lds
v_mfma_f32_16x16x32_bf16 v[164:167], a[116:119], a[104:107], v[164:167]
s_add_u32 s49, s46, s91
v_mfma_f32_16x16x32_bf16 v[176:179], a[80:83], a[76:79], v[176:179]
s_add_u32 s46, s46, s53
v_mfma_f32_16x16x32_bf16 v[176:179], a[112:115], a[108:111], v[176:179]
v_mfma_f32_16x16x32_bf16 v[180:183], a[84:87], a[76:79], v[180:183]
buffer_load_dwordx4 v7, s[4:7], s44 offen offset:3072 sc1 lds
v_mfma_f32_16x16x32_bf16 v[180:183], a[116:119], a[108:111], v[180:183]
s_waitcnt vmcnt(25) lgkmcnt(0)
s_barrier
v_mfma_f32_16x16x32_bf16 v[136:139], a[88:91], a[64:67], v[136:139]
ds_read_b64_tr_b16 a[80:81], v245 offset:0
ds_read_b64_tr_b16 a[82:83], v245 offset:4096
v_mfma_f32_16x16x32_bf16 v[136:139], a[120:123], a[96:99], v[136:139]
s_add_u32 m0, s49, 16384
ds_read_b64_tr_b16 a[112:113], v245 offset:8192
ds_read_b64_tr_b16 a[114:115], v245 offset:12288
v_mfma_f32_16x16x32_bf16 v[140:143], a[92:95], a[64:67], v[140:143]
buffer_load_dwordx4 v240, s[8:11], s45 offen offset:0 lds
v_mfma_f32_16x16x32_bf16 v[140:143], a[124:127], a[96:99], v[140:143]
ds_read_b128 a[64:67], v244 offset:0
ds_read_b128 a[96:99], v244 offset:2048
v_mfma_f32_16x16x32_bf16 v[152:155], a[88:91], a[68:71], v[152:155]
ds_read_b64_tr_b16 a[84:85], v246 offset:0
ds_read_b64_tr_b16 a[86:87], v246 offset:4096
v_mfma_f32_16x16x32_bf16 v[152:155], a[120:123], a[100:103], v[152:155]
ds_read_b64_tr_b16 a[116:117], v246 offset:8192
ds_read_b64_tr_b16 a[118:119], v246 offset:12288
v_mfma_f32_16x16x32_bf16 v[156:159], a[92:95], a[68:71], v[156:159]
buffer_load_dwordx4 v241, s[8:11], s45 offen offset:1024 lds
v_mfma_f32_16x16x32_bf16 v[156:159], a[124:127], a[100:103], v[156:159]
ds_read_b128 a[68:71], v244 offset:1024
ds_read_b128 a[100:103], v244 offset:3072
v_mfma_f32_16x16x32_bf16 v[168:171], a[88:91], a[72:75], v[168:171]
s_cmp_eq_u32 s46, 0
s_cselect_b32 s53, s56, s57
v_mfma_f32_16x16x32_bf16 v[168:171], a[120:123], a[104:107], v[168:171]
v_add_u32 v245, s53, v245
v_add_u32 v246, s53, v246
v_mfma_f32_16x16x32_bf16 v[172:175], a[92:95], a[72:75], v[172:175]
buffer_load_dwordx4 v242, s[8:11], s45 offen offset:2048 lds
v_mfma_f32_16x16x32_bf16 v[172:175], a[124:127], a[104:107], v[172:175]
ds_read_b128 a[72:75], v244 offset:4096
ds_read_b128 a[104:107], v244 offset:6144
v_mfma_f32_16x16x32_bf16 v[184:187], a[88:91], a[76:79], v[184:187]
s_add_u32 s44, s44, 4096
v_mfma_f32_16x16x32_bf16 v[184:187], a[120:123], a[108:111], v[184:187]
s_waitcnt lgkmcnt(2)
v_mfma_f32_16x16x32_bf16 v[188:191], a[92:95], a[76:79], v[188:191]
buffer_load_dwordx4 v243, s[8:11], s45 offen offset:3072 lds
v_mfma_f32_16x16x32_bf16 v[188:191], a[124:127], a[108:111], v[188:191]
ds_read_b128 a[76:79], v244 offset:5120
ds_read_b128 a[108:111], v244 offset:7168
v_mfma_f32_16x16x32_bf16 v[128:131], a[80:83], a[64:67], v[128:131]
ds_read_b64_tr_b16 a[88:89], v247 offset:0
ds_read_b64_tr_b16 a[90:91], v247 offset:4096
v_mfma_f32_16x16x32_bf16 v[128:131], a[112:115], a[96:99], v[128:131]
s_mov_b32 m0, s49
ds_read_b64_tr_b16 a[120:121], v247 offset:8192
ds_read_b64_tr_b16 a[122:123], v247 offset:12288
v_mfma_f32_16x16x32_bf16 v[132:135], a[84:87], a[64:67], v[132:135]
buffer_load_dwordx4 v7, s[4:7], s44 offen offset:0 sc1 lds
v_mfma_f32_16x16x32_bf16 v[132:135], a[116:119], a[96:99], v[132:135]
ds_read_b64_tr_b16 a[92:93], v248 offset:0
ds_read_b64_tr_b16 a[94:95], v248 offset:4096
v_mfma_f32_16x16x32_bf16 v[144:147], a[80:83], a[68:71], v[144:147]
ds_read_b64_tr_b16 a[124:125], v248 offset:8192
ds_read_b64_tr_b16 a[126:127], v248 offset:12288
v_mfma_f32_16x16x32_bf16 v[144:147], a[112:115], a[100:103], v[144:147]
s_add_u32 s45, s45, 131072
v_add_u32 v244, s53, v244
v_mfma_f32_16x16x32_bf16 v[148:151], a[84:87], a[68:71], v[148:151]
buffer_load_dwordx4 v7, s[4:7], s44 offen offset:1024 sc1 lds
v_mfma_f32_16x16x32_bf16 v[148:151], a[116:119], a[100:103], v[148:151]
s_waitcnt lgkmcnt(8)
v_mfma_f32_16x16x32_bf16 v[160:163], a[80:83], a[72:75], v[160:163]
v_add_u32 v247, s53, v247
v_add_u32 v248, s53, v248
v_mfma_f32_16x16x32_bf16 v[160:163], a[112:115], a[104:107], v[160:163]
v_mfma_f32_16x16x32_bf16 v[164:167], a[84:87], a[72:75], v[164:167]
buffer_load_dwordx4 v7, s[4:7], s44 offen offset:2048 sc1 lds
v_mfma_f32_16x16x32_bf16 v[164:167], a[116:119], a[104:107], v[164:167]
s_add_u32 s49, s46, s91
v_mfma_f32_16x16x32_bf16 v[176:179], a[80:83], a[76:79], v[176:179]
s_add_u32 s46, s46, s53
v_mfma_f32_16x16x32_bf16 v[176:179], a[112:115], a[108:111], v[176:179]
v_mfma_f32_16x16x32_bf16 v[180:183], a[84:87], a[76:79], v[180:183]
buffer_load_dwordx4 v7, s[4:7], s44 offen offset:3072 sc1 lds
v_mfma_f32_16x16x32_bf16 v[180:183], a[116:119], a[108:111], v[180:183]
s_waitcnt vmcnt(25) lgkmcnt(0)
s_barrier
v_mfma_f32_16x16x32_bf16 v[136:139], a[88:91], a[64:67], v[136:139]
ds_read_b64_tr_b16 a[80:81], v245 offset:0
ds_read_b64_tr_b16 a[82:83], v245 offset:4096
.Lpad23:
s_nop 0
v_mfma_f32_16x16x32_bf16 v[136:139], a[120:123], a[96:99], v[136:139]
s_add_u32 m0, s49, 16384
ds_read_b64_tr_b16 a[112:113], v245 offset:8192
ds_read_b64_tr_b16 a[114:115], v245 offset:12288
v_mfma_f32_16x16x32_bf16 v[140:143], a[92:95], a[64:67], v[140:143]
buffer_load_dwordx4 v240, s[8:11], s45 offen offset:0 lds
v_mfma_f32_16x16x32_bf16 v[140:143], a[124:127], a[96:99], v[140:143]
ds_read_b128 a[64:67], v244 offset:0
ds_read_b128 a[96:99], v244 offset:2048
v_mfma_f32_16x16x32_bf16 v[152:155], a[88:91], a[68:71], v[152:155]
ds_read_b64_tr_b16 a[84:85], v246 offset:0
ds_read_b64_tr_b16 a[86:87], v246 offset:4096
v_mfma_f32_16x16x32_bf16 v[152:155], a[120:123], a[100:103], v[152:155]
ds_read_b64_tr_b16 a[116:117], v246 offset:8192
ds_read_b64_tr_b16 a[118:119], v246 offset:12288
v_mfma_f32_16x16x32_bf16 v[156:159], a[92:95], a[68:71], v[156:159]
buffer_load_dwordx4 v241, s[8:11], s45 offen offset:1024 lds
v_mfma_f32_16x16x32_bf16 v[156:159], a[124:127], a[100:103], v[156:159]
ds_read_b128 a[68:71], v244 offset:1024
ds_read_b128 a[100:103], v244 offset:3072
v_mfma_f32_16x16x32_bf16 v[168:171], a[88:91], a[72:75], v[168:171]
s_cmp_eq_u32 s46, 0
s_cselect_b32 s53, s56, s57
v_mfma_f32_16x16x32_bf16 v[168:171], a[120:123], a[104:107], v[168:171]
v_add_u32 v245, s53, v245
v_add_u32 v246, s53, v246
v_mfma_f32_16x16x32_bf16 v[172:175], a[92:95], a[72:75], v[172:175]
buffer_load_dwordx4 v242, s[8:11], s45 offen offset:2048 lds
v_mfma_f32_16x16x32_bf16 v[172:175], a[124:127], a[104:107], v[172:175]
ds_read_b128 a[72:75], v244 offset:4096
ds_read_b128 a[104:107], v244 offset:6144
v_mfma_f32_16x16x32_bf16 v[184:187], a[88:91], a[76:79], v[184:187]
s_add_u32 s44, s44, 4096
v_mfma_f32_16x16x32_bf16 v[184:187], a[120:123], a[108:111], v[184:187]
s_waitcnt lgkmcnt(2)
v_mfma_f32_16x16x32_bf16 v[188:191], a[92:95], a[76:79], v[188:191]
buffer_load_dwordx4 v243, s[8:11], s45 offen offset:3072 lds
v_mfma_f32_16x16x32_bf16 v[188:191], a[124:127], a[108:111], v[188:191]
ds_read_b128 a[76:79], v244 offset:5120
ds_read_b128 a[108:111], v244 offset:7168
v_mfma_f32_16x16x32_bf16 v[128:131], a[80:83], a[64:67], v[128:131]
ds_read_b64_tr_b16 a[88:89], v247 offset:0
ds_read_b64_tr_b16 a[90:91], v247 offset:4096
v_mfma_f32_16x16x32_bf16 v[128:131], a[112:115], a[96:99], v[128:131]
s_mov_b32 m0, s49
ds_read_b64_tr_b16 a[120:121], v247 offset:8192
ds_read_b64_tr_b16 a[122:123], v247 offset:12288
v_mfma_f32_16x16x32_bf16 v[132:135], a[84:87], a[64:67], v[132:135]
buffer_load_dwordx4 v7, s[4:7], s44 offen offset:0 sc1 lds
v_mfma_f32_16x16x32_bf16 v[132:135], a[116:119], a[96:99], v[132:135]
ds_read_b64_tr_b16 a[92:93], v248 offset:0
ds_read_b64_tr_b16 a[94:95], v248 offset:4096
v_mfma_f32_16x16x32_bf16 v[144:147], a[80:83], a[68:71], v[144:147]
ds_read_b64_tr_b16 a[124:125], v248 offset:8192
ds_read_b64_tr_b16 a[126:127], v248 offset:12288
v_mfma_f32_16x16x32_bf16 v[144:147], a[112:115], a[100:103], v[144:147]
s_add_u32 s45, s45, 131072
v_add_u32 v244, s53, v244
v_mfma_f32_16x16x32_bf16 v[148:151], a[84:87], a[68:71], v[148:151]
buffer_load_dwordx4 v7, s[4:7], s44 offen offset:1024 sc1 lds
v_mfma_f32_16x16x32_bf16 v[148:151], a[116:119], a[100:103], v[148:151]
s_waitcnt lgkmcnt(8)
v_mfma_f32_16x16x32_bf16 v[160:163], a[80:83], a[72:75], v[160:163]
v_add_u32 v247, s53, v247
v_add_u32 v248, s53, v248
v_mfma_f32_16x16x32_bf16 v[160:163], a[112:115], a[104:107], v[160:163]
v_mfma_f32_16x16x32_bf16 v[164:167], a[84:87], a[72:75], v[164:167]
buffer_load_dwordx4 v7, s[4:7], s44 offen offset:2048 sc1 lds
v_mfma_f32_16x16x32_bf16 v[164:167], a[116:119], a[104:107], v[164:167]
s_add_u32 s49, s46, s91
v_mfma_f32_16x16x32_bf16 v[176:179], a[80:83], a[76:79], v[176:179]
s_add_u32 s46, s46, s53
v_mfma_f32_16x16x32_bf16 v[176:179], a[112:115], a[108:111], v[176:179]
v_mfma_f32_16x16x32_bf16 v[180:183], a[84:87], a[76:79], v[180:183]
buffer_load_dwordx4 v7, s[4:7], s44 offen offset:3072 sc1 lds
v_mfma_f32_16x16x32_bf16 v[180:183], a[116:119], a[108:111], v[180:183]
s_waitcnt vmcnt(25) lgkmcnt(0)
s_barrier
v_mfma_f32_16x16x32_bf16 v[136:139], a[88:91], a[64:67], v[136:139]
ds_read_b64_tr_b16 a[80:81], v245 offset:0
ds_read_b64_tr_b16 a[82:83], v245 offset:4096
v_mfma_f32_16x16x32_bf16 v[136:139], a[120:123], a[96:99], v[136:139]
s_add_u32 m0, s49, 16384
ds_read_b64_tr_b16 a[112:113], v245 offset:8192
ds_read_b64_tr_b16 a[114:115], v245 offset:12288
v_mfma_f32_16x16x32_bf16 v[140:143], a[92:95], a[64:67], v[140:143]
buffer_load_dwordx4 v240, s[8:11], s45 offen offset:0 lds
v_mfma_f32_16x16x32_bf16 v[140:143], a[124:127], a[96:99], v[140:143]
ds_read_b128 a[64:67], v244 offset:0
ds_read_b128 a[96:99], v244 offset:2048
v_mfma_f32_16x16x32_bf16 v[152:155], a[88:91], a[68:71], v[152:155]
ds_read_b64_tr_b16 a[84:85], v246 offset:0
ds_read_b64_tr_b16 a[86:87], v246 offset:4096
v_mfma_f32_16x16x32_bf16 v[152:155], a[120:123], a[100:103], v[152:155]
ds_read_b64_tr_b16 a[116:117], v246 offset:8192
ds_read_b64_tr_b16 a[118:119], v246 offset:12288
v_mfma_f32_16x16x32_bf16 v[156:159], a[92:95], a[68:71], v[156:159]
buffer_load_dwordx4 v241, s[8:11], s45 offen offset:1024 lds
v_mfma_f32_16x16x32_bf16 v[156:159], a[124:127], a[100:103], v[156:159]
ds_read_b128 a[68:71], v244 offset:1024
ds_read_b128 a[100:103], v244 offset:3072
v_mfma_f32_16x16x32_bf16 v[168:171], a[88:91], a[72:75], v[168:171]
s_cmp_eq_u32 s46, 0
s_cselect_b32 s53, s56, s57
v_mfma_f32_16x16x32_bf16 v[168:171], a[120:123], a[104:107], v[168:171]
v_add_u32 v245, s53, v245
v_add_u32 v246, s53, v246
v_mfma_f32_16x16x32_bf16 v[172:175], a[92:95], a[72:75], v[172:175]
buffer_load_dwordx4 v242, s[8:11], s45 offen offset:2048 lds
v_mfma_f32_16x16x32_bf16 v[172:175], a[124:127], a[104:107], v[172:175]
ds_read_b128 a[72:75], v244 offset:4096
ds_read_b128 a[104:107], v244 offset:6144
v_mfma_f32_16x16x32_bf16 v[184:187], a[88:91], a[76:79], v[184:187]
s_add_u32 s44, s44, 4096
v_mfma_f32_16x16x32_bf16 v[184:187], a[120:123], a[108:111], v[184:187]
s_waitcnt lgkmcnt(2)
v_mfma_f32_16x16x32_bf16 v[188:191], a[92:95], a[76:79], v[188:191]
buffer_load_dwordx4 v243, s[8:11], s45 offen offset:3072 lds
v_mfma_f32_16x16x32_bf16 v[188:191], a[124:127], a[108:111], v[188:191]
ds_read_b128 a[76:79], v244 offset:5120
ds_read_b128 a[108:111], v244 offset:7168
v_mfma_f32_16x16x32_bf16 v[128:131], a[80:83], a[64:67], v[128:131]
ds_read_b64_tr_b16 a[88:89], v247 offset:0
ds_read_b64_tr_b16 a[90:91], v247 offset:4096
v_mfma_f32_16x16x32_bf16 v[128:131], a[112:115], a[96:99], v[128:131]
s_mov_b32 m0, s49
ds_read_b64_tr_b16 a[120:121], v247 offset:8192
ds_read_b64_tr_b16 a[122:123], v247 offset:12288
v_mfma_f32_16x16x32_bf16 v[132:135], a[84:87], a[64:67], v[132:135]
buffer_load_dwordx4 v7, s[4:7], s44 offen offset:0 sc1 lds
v_mfma_f32_16x16x32_bf16 v[132:135], a[116:119], a[96:99], v[132:135]
ds_read_b64_tr_b16 a[92:93], v248 offset:0
ds_read_b64_tr_b16 a[94:95], v248 offset:4096
v_mfma_f32_16x16x32_bf16 v[144:147], a[80:83], a[68:71], v[144:147]
ds_read_b64_tr_b16 a[124:125], v248 offset:8192
ds_read_b64_tr_b16 a[126:127], v248 offset:12288
v_mfma_f32_16x16x32_bf16 v[144:147], a[112:115], a[100:103], v[144:147]
s_add_u32 s45, s45, 131072
v_add_u32 v244, s53, v244
v_mfma_f32_16x16x32_bf16 v[148:151], a[84:87], a[68:71], v[148:151]
buffer_load_dwordx4 v7, s[4:7], s44 offen offset:1024 sc1 lds
v_mfma_f32_16x16x32_bf16 v[148:151], a[116:119], a[100:103], v[148:151]
s_waitcnt lgkmcnt(8)
v_mfma_f32_16x16x32_bf16 v[160:163], a[80:83], a[72:75], v[160:163]
v_add_u32 v247, s53, v247
v_add_u32 v248, s53, v248
v_mfma_f32_16x16x32_bf16 v[160:163], a[112:115], a[104:107], v[160:163]
v_mfma_f32_16x16x32_bf16 v[164:167], a[84:87], a[72:75], v[164:167]
buffer_load_dwordx4 v7, s[4:7], s44 offen offset:2048 sc1 lds
v_mfma_f32_16x16x32_bf16 v[164:167], a[116:119], a[104:107], v[164:167]
s_add_u32 s49, s46, s91
v_mfma_f32_16x16x32_bf16 v[176:179], a[80:83], a[76:79], v[176:179]
s_add_u32 s46, s46, s53
v_mfma_f32_16x16x32_bf16 v[176:179], a[112:115], a[108:111], v[176:179]
v_mfma_f32_16x16x32_bf16 v[180:183], a[84:87], a[76:79], v[180:183]
buffer_load_dwordx4 v7, s[4:7], s44 offen offset:3072 sc1 lds
v_mfma_f32_16x16x32_bf16 v[180:183], a[116:119], a[108:111], v[180:183]
s_waitcnt vmcnt(24) lgkmcnt(0)
s_barrier
v_mfma_f32_16x16x32_bf16 v[136:139], a[88:91], a[64:67], v[136:139]
ds_read_b64_tr_b16 a[80:81], v245 offset:0
ds_read_b64_tr_b16 a[82:83], v245 offset:4096
v_mfma_f32_16x16x32_bf16 v[136:139], a[120:123], a[96:99], v[136:139]
s_add_u32 m0, s49, 16384
ds_read_b64_tr_b16 a[112:113], v245 offset:8192
ds_read_b64_tr_b16 a[114:115], v245 offset:12288
v_mfma_f32_16x16x32_bf16 v[140:143], a[92:95], a[64:67], v[140:143]
buffer_load_dwordx4 v240, s[8:11], s45 offen offset:0 lds
v_mfma_f32_16x16x32_bf16 v[140:143], a[124:127], a[96:99], v[140:143]
ds_read_b128 a[64:67], v244 offset:0
ds_read_b128 a[96:99], v244 offset:2048
v_mfma_f32_16x16x32_bf16 v[152:155], a[88:91], a[68:71], v[152:155]
ds_read_b64_tr_b16 a[84:85], v246 offset:0
ds_read_b64_tr_b16 a[86:87], v246 offset:4096
v_mfma_f32_16x16x32_bf16 v[152:155], a[120:123], a[100:103], v[152:155]
ds_read_b64_tr_b16 a[116:117], v246 offset:8192
ds_read_b64_tr_b16 a[118:119], v246 offset:12288
v_mfma_f32_16x16x32_bf16 v[156:159], a[92:95], a[68:71], v[156:159]
buffer_load_dwordx4 v241, s[8:11], s45 offen offset:1024 lds
v_mfma_f32_16x16x32_bf16 v[156:159], a[124:127], a[100:103], v[156:159]
ds_read_b128 a[68:71], v244 offset:1024
ds_read_b128 a[100:103], v244 offset:3072
v_mfma_f32_16x16x32_bf16 v[168:171], a[88:91], a[72:75], v[168:171]
s_cmp_eq_u32 s46, 0
s_cselect_b32 s53, s56, s57
v_mfma_f32_16x16x32_bf16 v[168:171], a[120:123], a[104:107], v[168:171]
v_add_u32 v245, s53, v245
v_add_u32 v246, s53, v246
v_mfma_f32_16x16x32_bf16 v[172:175], a[92:95], a[72:75], v[172:175]
buffer_load_dwordx4 v242, s[8:11], s45 offen offset:2048 lds
v_mfma_f32_16x16x32_bf16 v[172:175], a[124:127], a[104:107], v[172:175]
ds_read_b128 a[72:75], v244 offset:4096
ds_read_b128 a[104:107], v244 offset:6144
v_mfma_f32_16x16x32_bf16 v[184:187], a[88:91], a[76:79], v[184:187]
s_add_u32 s44, s44, 4096
v_mfma_f32_16x16x32_bf16 v[184:187], a[120:123], a[108:111], v[184:187]
s_waitcnt lgkmcnt(2)
v_mfma_f32_16x16x32_bf16 v[188:191], a[92:95], a[76:79], v[188:191]
buffer_load_dwordx4 v243, s[8:11], s45 offen offset:3072 lds
v_mfma_f32_16x16x32_bf16 v[188:191], a[124:127], a[108:111], v[188:191]
ds_read_b128 a[76:79], v244 offset:5120
ds_read_b128 a[108:111], v244 offset:7168
v_mfma_f32_16x16x32_bf16 v[128:131], a[80:83], a[64:67], v[128:131]
ds_read_b64_tr_b16 a[88:89], v247 offset:0
ds_read_b64_tr_b16 a[90:91], v247 offset:4096
v_mfma_f32_16x16x32_bf16 v[128:131], a[112:115], a[96:99], v[128:131]
s_mov_b32 m0, s49
ds_read_b64_tr_b16 a[120:121], v247 offset:8192
ds_read_b64_tr_b16 a[122:123], v247 offset:12288
v_mfma_f32_16x16x32_bf16 v[132:135], a[84:87], a[64:67], v[132:135]
buffer_load_dwordx4 v7, s[4:7], s44 offen offset:0 sc1 lds
v_mfma_f32_16x16x32_bf16 v[132:135], a[116:119], a[96:99], v[132:135]
ds_read_b64_tr_b16 a[92:93], v248 offset:0
ds_read_b64_tr_b16 a[94:95], v248 offset:4096
v_mfma_f32_16x16x32_bf16 v[144:147], a[80:83], a[68:71], v[144:147]
ds_read_b64_tr_b16 a[124:125], v248 offset:8192
ds_read_b64_tr_b16 a[126:127], v248 offset:12288
v_mfma_f32_16x16x32_bf16 v[144:147], a[112:115], a[100:103], v[144:147]
s_add_u32 s45, s45, 131072
v_add_u32 v244, s53, v244
v_mfma_f32_16x16x32_bf16 v[148:151], a[84:87], a[68:71], v[148:151]
buffer_load_dwordx4 v7, s[4:7], s44 offen offset:1024 sc1 lds
v_mfma_f32_16x16x32_bf16 v[148:151], a[116:119], a[100:103], v[148:151]
s_waitcnt lgkmcnt(8)
v_mfma_f32_16x16x32_bf16 v[160:163], a[80:83], a[72:75], v[160:163]
v_add_u32 v247, s53, v247
v_add_u32 v248, s53, v248
v_mfma_f32_16x16x32_bf16 v[160:163], a[112:115], a[104:107], v[160:163]
v_mfma_f32_16x16x32_bf16 v[164:167], a[84:87], a[72:75], v[164:167]
buffer_load_dwordx4 v7, s[4:7], s44 offen offset:2048 sc1 lds
v_mfma_f32_16x16x32_bf16 v[164:167], a[116:119], a[104:107], v[164:167]
s_add_u32 s49, s46, s91
v_mfma_f32_16x16x32_bf16 v[176:179], a[80:83], a[76:79], v[176:179]
s_add_u32 s46, s46, s53
v_mfma_f32_16x16x32_bf16 v[176:179], a[112:115], a[108:111], v[176:179]
v_mfma_f32_16x16x32_bf16 v[180:183], a[84:87], a[76:79], v[180:183]
buffer_load_dwordx4 v7, s[4:7], s44 offen offset:3072 sc1 lds
v_mfma_f32_16x16x32_bf16 v[180:183], a[116:119], a[108:111], v[180:183]
s_waitcnt vmcnt(24) lgkmcnt(0)
s_barrier
v_mfma_f32_16x16x32_bf16 v[136:139], a[88:91], a[64:67], v[136:139]
ds_read_b64_tr_b16 a[80:81], v245 offset:0
ds_read_b64_tr_b16 a[82:83], v245 offset:4096
v_mfma_f32_16x16x32_bf16 v[136:139], a[120:123], a[96:99], v[136:139]
ds_read_b64_tr_b16 a[112:113], v245 offset:8192
ds_read_b64_tr_b16 a[114:115], v245 offset:12288
v_mfma_f32_16x16x32_bf16 v[140:143], a[92:95], a[64:67], v[140:143]
v_mfma_f32_16x16x32_bf16 v[140:143], a[124:127], a[96:99], v[140:143]
ds_read_b128 a[64:67], v244 offset:0
ds_read_b128 a[96:99], v244 offset:2048
v_mfma_f32_16x16x32_bf16 v[152:155], a[88:91], a[68:71], v[152:155]
ds_read_b64_tr_b16 a[84:85], v246 offset:0
ds_read_b64_tr_b16 a[86:87], v246 offset:4096
v_mfma_f32_16x16x32_bf16 v[152:155], a[120:123], a[100:103], v[152:155]
ds_read_b64_tr_b16 a[116:117], v246 offset:8192
ds_read_b64_tr_b16 a[118:119], v246 offset:12288
v_mfma_f32_16x16x32_bf16 v[156:159], a[92:95], a[68:71], v[156:159]
v_mfma_f32_16x16x32_bf16 v[156:159], a[124:127], a[100:103], v[156:159]
ds_read_b128 a[68:71], v244 offset:1024
ds_read_b128 a[100:103], v244 offset:3072
v_mfma_f32_16x16x32_bf16 v[168:171], a[88:91], a[72:75], v[168:171]
s_cmp_eq_u32 s46, 0
s_cselect_b32 s53, s56, s57
v_mfma_f32_16x16x32_bf16 v[168:171], a[120:123], a[104:107], v[168:171]
v_add_u32 v245, s53, v245
v_add_u32 v246, s53, v246
v_mfma_f32_16x16x32_bf16 v[172:175], a[92:95], a[72:75], v[172:175]
v_mfma_f32_16x16x32_bf16 v[172:175], a[124:127], a[104:107], v[172:175]
ds_read_b128 a[72:75], v244 offset:4096
ds_read_b128 a[104:107], v244 offset:6144
v_mfma_f32_16x16x32_bf16 v[184:187], a[88:91], a[76:79], v[184:187]
v_mfma_f32_16x16x32_bf16 v[184:187], a[120:123], a[108:111], v[184:187]
s_waitcnt lgkmcnt(2)
v_mfma_f32_16x16x32_bf16 v[188:191], a[92:95], a[76:79], v[188:191]
v_mfma_f32_16x16x32_bf16 v[188:191], a[124:127], a[108:111], v[188:191]
ds_read_b128 a[76:79], v244 offset:5120
ds_read_b128 a[108:111], v244 offset:7168
v_mfma_f32_16x16x32_bf16 v[128:131], a[80:83], a[64:67], v[128:131]
ds_read_b64_tr_b16 a[88:89], v247 offset:0
ds_read_b64_tr_b16 a[90:91], v247 offset:4096
v_mfma_f32_16x16x32_bf16 v[128:131], a[112:115], a[96:99], v[128:131]
ds_read_b64_tr_b16 a[120:121], v247 offset:8192
ds_read_b64_tr_b16 a[122:123], v247 offset:12288
v_mfma_f32_16x16x32_bf16 v[132:135], a[84:87], a[64:67], v[132:135]
v_mfma_f32_16x16x32_bf16 v[132:135], a[116:119], a[96:99], v[132:135]
ds_read_b64_tr_b16 a[92:93], v248 offset:0
ds_read_b64_tr_b16 a[94:95], v248 offset:4096
v_mfma_f32_16x16x32_bf16 v[144:147], a[80:83], a[68:71], v[144:147]
ds_read_b64_tr_b16 a[124:125], v248 offset:8192
ds_read_b64_tr_b16 a[126:127], v248 offset:12288
v_mfma_f32_16x16x32_bf16 v[144:147], a[112:115], a[100:103], v[144:147]
v_add_u32 v244, s53, v244
v_mfma_f32_16x16x32_bf16 v[148:151], a[84:87], a[68:71], v[148:151]
v_mfma_f32_16x16x32_bf16 v[148:151], a[116:119], a[100:103], v[148:151]
s_waitcnt lgkmcnt(8)
v_mfma_f32_16x16x32_bf16 v[160:163], a[80:83], a[72:75], v[160:163]
v_add_u32 v247, s53, v247
v_add_u32 v248, s53, v248
v_mfma_f32_16x16x32_bf16 v[160:163], a[112:115], a[104:107], v[160:163]
v_mfma_f32_16x16x32_bf16 v[164:167], a[84:87], a[72:75], v[164:167]
v_mfma_f32_16x16x32_bf16 v[164:167], a[116:119], a[104:107], v[164:167]
s_add_u32 s49, s46, s91
v_mfma_f32_16x16x32_bf16 v[176:179], a[80:83], a[76:79], v[176:179]
s_add_u32 s46, s46, s53
v_mfma_f32_16x16x32_bf16 v[176:179], a[112:115], a[108:111], v[176:179]
v_mfma_f32_16x16x32_bf16 v[180:183], a[84:87], a[76:79], v[180:183]
v_mfma_f32_16x16x32_bf16 v[180:183], a[116:119], a[108:111], v[180:183]
s_waitcnt vmcnt(16) lgkmcnt(0)
s_barrier
v_mfma_f32_16x16x32_bf16 v[136:139], a[88:91], a[64:67], v[136:139]
ds_read_b64_tr_b16 a[80:81], v245 offset:0
ds_read_b64_tr_b16 a[82:83], v245 offset:4096
v_mfma_f32_16x16x32_bf16 v[136:139], a[120:123], a[96:99], v[136:139]
ds_read_b64_tr_b16 a[112:113], v245 offset:8192
ds_read_b64_tr_b16 a[114:115], v245 offset:12288
v_mfma_f32_16x16x32_bf16 v[140:143], a[92:95], a[64:67], v[140:143]
v_mfma_f32_16x16x32_bf16 v[140:143], a[124:127], a[96:99], v[140:143]
ds_read_b128 a[64:67], v244 offset:0
ds_read_b128 a[96:99], v244 offset:2048
v_mfma_f32_16x16x32_bf16 v[152:155], a[88:91], a[68:71], v[152:155]
ds_read_b64_tr_b16 a[84:85], v246 offset:0
ds_read_b64_tr_b16 a[86:87], v246 offset:4096
v_mfma_f32_16x16x32_bf16 v[152:155], a[120:123], a[100:103], v[152:155]
ds_read_b64_tr_b16 a[116:117], v246 offset:8192
ds_read_b64_tr_b16 a[118:119], v246 offset:12288
v_mfma_f32_16x16x32_bf16 v[156:159], a[92:95], a[68:71], v[156:159]
v_mfma_f32_16x16x32_bf16 v[156:159], a[124:127], a[100:103], v[156:159]
ds_read_b128 a[68:71], v244 offset:1024
ds_read_b128 a[100:103], v244 offset:3072
v_mfma_f32_16x16x32_bf16 v[168:171], a[88:91], a[72:75], v[168:171]
s_cmp_eq_u32 s46, 0
s_cselect_b32 s53, s56, s57
v_mfma_f32_16x16x32_bf16 v[168:171], a[120:123], a[104:107], v[168:171]
v_add_u32 v245, s53, v245
v_add_u32 v246, s53, v246
v_mfma_f32_16x16x32_bf16 v[172:175], a[92:95], a[72:75], v[172:175]
v_mfma_f32_16x16x32_bf16 v[172:175], a[124:127], a[104:107], v[172:175]
ds_read_b128 a[72:75], v244 offset:4096
ds_read_b128 a[104:107], v244 offset:6144
v_mfma_f32_16x16x32_bf16 v[184:187], a[88:91], a[76:79], v[184:187]
v_mfma_f32_16x16x32_bf16 v[184:187], a[120:123], a[108:111], v[184:187]
s_waitcnt lgkmcnt(2)
v_mfma_f32_16x16x32_bf16 v[188:191], a[92:95], a[76:79], v[188:191]
v_mfma_f32_16x16x32_bf16 v[188:191], a[124:127], a[108:111], v[188:191]
ds_read_b128 a[76:79], v244 offset:5120
ds_read_b128 a[108:111], v244 offset:7168
v_mfma_f32_16x16x32_bf16 v[128:131], a[80:83], a[64:67], v[128:131]
ds_read_b64_tr_b16 a[88:89], v247 offset:0
ds_read_b64_tr_b16 a[90:91], v247 offset:4096
v_mfma_f32_16x16x32_bf16 v[128:131], a[112:115], a[96:99], v[128:131]
ds_read_b64_tr_b16 a[120:121], v247 offset:8192
ds_read_b64_tr_b16 a[122:123], v247 offset:12288
v_mfma_f32_16x16x32_bf16 v[132:135], a[84:87], a[64:67], v[132:135]
v_mfma_f32_16x16x32_bf16 v[132:135], a[116:119], a[96:99], v[132:135]
ds_read_b64_tr_b16 a[92:93], v248 offset:0
ds_read_b64_tr_b16 a[94:95], v248 offset:4096
v_mfma_f32_16x16x32_bf16 v[144:147], a[80:83], a[68:71], v[144:147]
ds_read_b64_tr_b16 a[124:125], v248 offset:8192
ds_read_b64_tr_b16 a[126:127], v248 offset:12288
v_mfma_f32_16x16x32_bf16 v[144:147], a[112:115], a[100:103], v[144:147]
v_add_u32 v244, s53, v244
v_mfma_f32_16x16x32_bf16 v[148:151], a[84:87], a[68:71], v[148:151]
v_mfma_f32_16x16x32_bf16 v[148:151], a[116:119], a[100:103], v[148:151]
s_waitcnt lgkmcnt(8)
v_mfma_f32_16x16x32_bf16 v[160:163], a[80:83], a[72:75], v[160:163]
v_add_u32 v247, s53, v247
v_add_u32 v248, s53, v248
v_mfma_f32_16x16x32_bf16 v[160:163], a[112:115], a[104:107], v[160:163]
v_mfma_f32_16x16x32_bf16 v[164:167], a[84:87], a[72:75], v[164:167]
v_mfma_f32_16x16x32_bf16 v[164:167], a[116:119], a[104:107], v[164:167]
s_add_u32 s49, s46, s91
v_mfma_f32_16x16x32_bf16 v[176:179], a[80:83], a[76:79], v[176:179]
s_add_u32 s46, s46, s53
v_mfma_f32_16x16x32_bf16 v[176:179], a[112:115], a[108:111], v[176:179]
v_mfma_f32_16x16x32_bf16 v[180:183], a[84:87], a[76:79], v[180:183]
v_mfma_f32_16x16x32_bf16 v[180:183], a[116:119], a[108:111], v[180:183]
s_waitcnt vmcnt(8) lgkmcnt(0)
s_barrier
v_mfma_f32_16x16x32_bf16 v[136:139], a[88:91], a[64:67], v[136:139]
ds_read_b64_tr_b16 a[80:81], v245 offset:0
ds_read_b64_tr_b16 a[82:83], v245 offset:4096
v_mfma_f32_16x16x32_bf16 v[136:139], a[120:123], a[96:99], v[136:139]
ds_read_b64_tr_b16 a[112:113], v245 offset:8192
ds_read_b64_tr_b16 a[114:115], v245 offset:12288
v_mfma_f32_16x16x32_bf16 v[140:143], a[92:95], a[64:67], v[140:143]
v_mfma_f32_16x16x32_bf16 v[140:143], a[124:127], a[96:99], v[140:143]
ds_read_b128 a[64:67], v244 offset:0
ds_read_b128 a[96:99], v244 offset:2048
v_mfma_f32_16x16x32_bf16 v[152:155], a[88:91], a[68:71], v[152:155]
ds_read_b64_tr_b16 a[84:85], v246 offset:0
ds_read_b64_tr_b16 a[86:87], v246 offset:4096
v_mfma_f32_16x16x32_bf16 v[152:155], a[120:123], a[100:103], v[152:155]
ds_read_b64_tr_b16 a[116:117], v246 offset:8192
ds_read_b64_tr_b16 a[118:119], v246 offset:12288
v_mfma_f32_16x16x32_bf16 v[156:159], a[92:95], a[68:71], v[156:159]
v_mfma_f32_16x16x32_bf16 v[156:159], a[124:127], a[100:103], v[156:159]
ds_read_b128 a[68:71], v244 offset:1024
ds_read_b128 a[100:103], v244 offset:3072
v_mfma_f32_16x16x32_bf16 v[168:171], a[88:91], a[72:75], v[168:171]
s_cmp_eq_u32 s46, 0
s_cselect_b32 s53, s56, s57
v_mfma_f32_16x16x32_bf16 v[168:171], a[120:123], a[104:107], v[168:171]
v_add_u32 v245, s53, v245
v_add_u32 v246, s53, v246
v_mfma_f32_16x16x32_bf16 v[172:175], a[92:95], a[72:75], v[172:175]
v_mfma_f32_16x16x32_bf16 v[172:175], a[124:127], a[104:107], v[172:175]
ds_read_b128 a[72:75], v244 offset:4096
ds_read_b128 a[104:107], v244 offset:6144
v_mfma_f32_16x16x32_bf16 v[184:187], a[88:91], a[76:79], v[184:187]
v_mfma_f32_16x16x32_bf16 v[184:187], a[120:123], a[108:111], v[184:187]
s_waitcnt lgkmcnt(2)
v_mfma_f32_16x16x32_bf16 v[188:191], a[92:95], a[76:79], v[188:191]
v_mfma_f32_16x16x32_bf16 v[188:191], a[124:127], a[108:111], v[188:191]
ds_read_b128 a[76:79], v244 offset:5120
ds_read_b128 a[108:111], v244 offset:7168
v_mfma_f32_16x16x32_bf16 v[128:131], a[80:83], a[64:67], v[128:131]
ds_read_b64_tr_b16 a[88:89], v247 offset:0
ds_read_b64_tr_b16 a[90:91], v247 offset:4096
v_mfma_f32_16x16x32_bf16 v[128:131], a[112:115], a[96:99], v[128:131]
ds_read_b64_tr_b16 a[120:121], v247 offset:8192
ds_read_b64_tr_b16 a[122:123], v247 offset:12288
v_mfma_f32_16x16x32_bf16 v[132:135], a[84:87], a[64:67], v[132:135]
v_mfma_f32_16x16x32_bf16 v[132:135], a[116:119], a[96:99], v[132:135]
ds_read_b64_tr_b16 a[92:93], v248 offset:0
ds_read_b64_tr_b16 a[94:95], v248 offset:4096
v_mfma_f32_16x16x32_bf16 v[144:147], a[80:83], a[68:71], v[144:147]
ds_read_b64_tr_b16 a[124:125], v248 offset:8192
ds_read_b64_tr_b16 a[126:127], v248 offset:12288
v_mfma_f32_16x16x32_bf16 v[144:147], a[112:115], a[100:103], v[144:147]
v_add_u32 v244, s53, v244
v_mfma_f32_16x16x32_bf16 v[148:151], a[84:87], a[68:71], v[148:151]
v_mfma_f32_16x16x32_bf16 v[148:151], a[116:119], a[100:103], v[148:151]
s_waitcnt lgkmcnt(8)
v_mfma_f32_16x16x32_bf16 v[160:163], a[80:83], a[72:75], v[160:163]
v_add_u32 v247, s53, v247
v_add_u32 v248, s53, v248
v_mfma_f32_16x16x32_bf16 v[160:163], a[112:115], a[104:107], v[160:163]
v_mfma_f32_16x16x32_bf16 v[164:167], a[84:87], a[72:75], v[164:167]
v_mfma_f32_16x16x32_bf16 v[164:167], a[116:119], a[104:107], v[164:167]
s_add_u32 s49, s46, s91
v_mfma_f32_16x16x32_bf16 v[176:179], a[80:83], a[76:79], v[176:179]
s_add_u32 s46, s46, s53
v_mfma_f32_16x16x32_bf16 v[176:179], a[112:115], a[108:111], v[176:179]
v_mfma_f32_16x16x32_bf16 v[180:183], a[84:87], a[76:79], v[180:183]
v_mfma_f32_16x16x32_bf16 v[180:183], a[116:119], a[108:111], v[180:183]
s_waitcnt vmcnt(0) lgkmcnt(0)
s_barrier
v_mfma_f32_16x16x32_bf16 v[136:139], a[88:91], a[64:67], v[136:139]
ds_read_b64_tr_b16 a[80:81], v245 offset:0
ds_read_b64_tr_b16 a[82:83], v245 offset:4096
v_mfma_f32_16x16x32_bf16 v[136:139], a[120:123], a[96:99], v[136:139]
ds_read_b64_tr_b16 a[112:113], v245 offset:8192
ds_read_b64_tr_b16 a[114:115], v245 offset:12288
v_mfma_f32_16x16x32_bf16 v[140:143], a[92:95], a[64:67], v[140:143]
ds_read_b64_tr_b16 a[128:129], v247 offset:0
ds_read_b64_tr_b16 a[130:131], v247 offset:4096
v_mfma_f32_16x16x32_bf16 v[140:143], a[124:127], a[96:99], v[140:143]
ds_read_b128 a[64:67], v244 offset:0
ds_read_b128 a[96:99], v244 offset:2048
v_mfma_f32_16x16x32_bf16 v[152:155], a[88:91], a[68:71], v[152:155]
ds_read_b64_tr_b16 a[84:85], v246 offset:0
ds_read_b64_tr_b16 a[86:87], v246 offset:4096
v_mfma_f32_16x16x32_bf16 v[152:155], a[120:123], a[100:103], v[152:155]
ds_read_b64_tr_b16 a[116:117], v246 offset:8192
ds_read_b64_tr_b16 a[118:119], v246 offset:12288
v_mfma_f32_16x16x32_bf16 v[156:159], a[92:95], a[68:71], v[156:159]
ds_read_b64_tr_b16 a[136:137], v247 offset:8192
ds_read_b64_tr_b16 a[138:139], v247 offset:12288
v_mfma_f32_16x16x32_bf16 v[156:159], a[124:127], a[100:103], v[156:159]
ds_read_b128 a[68:71], v244 offset:1024
ds_read_b128 a[100:103], v244 offset:3072
v_mfma_f32_16x16x32_bf16 v[168:171], a[88:91], a[72:75], v[168:171]
ds_read_b64_tr_b16 a[132:133], v248 offset:0
ds_read_b64_tr_b16 a[134:135], v248 offset:4096
v_mfma_f32_16x16x32_bf16 v[168:171], a[120:123], a[104:107], v[168:171]
ds_read_b64_tr_b16 a[140:141], v248 offset:8192
ds_read_b64_tr_b16 a[142:143], v248 offset:12288
v_mfma_f32_16x16x32_bf16 v[172:175], a[92:95], a[72:75], v[172:175]
s_mov_b32 s24, 0
s_mov_b32 s25, 32768
s_mov_b32 s26, 65536
v_mfma_f32_16x16x32_bf16 v[172:175], a[124:127], a[104:107], v[172:175]
ds_read_b128 a[72:75], v244 offset:4096
ds_read_b128 a[104:107], v244 offset:6144
v_mfma_f32_16x16x32_bf16 v[184:187], a[88:91], a[76:79], v[184:187]
s_mov_b32 s27, 98304
s_mov_b32 s28, 131072
s_mov_b32 s29, 163840
v_mfma_f32_16x16x32_bf16 v[184:187], a[120:123], a[108:111], v[184:187]
s_waitcnt lgkmcnt(6)
v_mfma_f32_16x16x32_bf16 v[188:191], a[92:95], a[76:79], v[188:191]
s_mov_b32 s30, 196608
s_mov_b32 s31, 229376
v_mfma_f32_16x16x32_bf16 v[188:191], a[124:127], a[108:111], v[188:191]
s_waitcnt lgkmcnt(0)
ds_read_b128 a[76:79], v244 offset:5120
ds_read_b128 a[108:111], v244 offset:7168
v_mfma_f32_16x16x32_bf16 v[128:131], a[80:83], a[64:67], v[128:131]
v_mfma_f32_16x16x32_bf16 v[128:131], a[112:115], a[96:99], v[128:131]
v_mfma_f32_16x16x32_bf16 v[132:135], a[84:87], a[64:67], v[132:135]
v_mfma_f32_16x16x32_bf16 v[132:135], a[116:119], a[96:99], v[132:135]
v_mfma_f32_16x16x32_bf16 v[136:139], a[128:131], a[64:67], v[136:139]
v_mfma_f32_16x16x32_bf16 v[136:139], a[136:139], a[96:99], v[136:139]
v_mfma_f32_16x16x32_bf16 v[140:143], a[132:135], a[64:67], v[140:143]
v_mfma_f32_16x16x32_bf16 v[140:143], a[140:143], a[96:99], v[140:143]
v_mfma_f32_16x16x32_bf16 v[144:147], a[80:83], a[68:71], v[144:147]
v_mfma_f32_16x16x32_bf16 v[144:147], a[112:115], a[100:103], v[144:147]
v_mfma_f32_16x16x32_bf16 v[148:151], a[84:87], a[68:71], v[148:151]
v_cvt_pk_bf16_f32 v192, v128, v129
v_cvt_pk_bf16_f32 v194, v132, v133
v_cvt_pk_bf16_f32 v193, v130, v131
v_mfma_f32_16x16x32_bf16 v[148:151], a[116:119], a[100:103], v[148:151]
v_cvt_pk_bf16_f32 v195, v134, v135
v_cvt_pk_bf16_f32 v196, v136, v137
v_cvt_pk_bf16_f32 v198, v140, v141
v_mfma_f32_16x16x32_bf16 v[152:155], a[128:131], a[68:71], v[152:155]
v_cvt_pk_bf16_f32 v197, v138, v139
v_cvt_pk_bf16_f32 v199, v142, v143
v_mfma_f32_16x16x32_bf16 v[152:155], a[136:139], a[100:103], v[152:155]
v_permlane16_swap_b32 v192, v194
v_mfma_f32_16x16x32_bf16 v[156:159], a[132:135], a[68:71], v[156:159]
v_permlane16_swap_b32 v193, v195
v_mfma_f32_16x16x32_bf16 v[156:159], a[140:143], a[100:103], v[156:159]
v_permlane16_swap_b32 v196, v198
v_mfma_f32_16x16x32_bf16 v[160:163], a[80:83], a[72:75], v[160:163]
s_waitcnt lgkmcnt(0)
v_permlane16_swap_b32 v197, v199
v_mov_b32 v200, v192
v_mfma_f32_16x16x32_bf16 v[160:163], a[112:115], a[104:107], v[160:163]
v_mov_b32 v201, v193
v_mov_b32 v202, v194
v_mov_b32 v203, v195
v_mfma_f32_16x16x32_bf16 v[164:167], a[84:87], a[72:75], v[164:167]
v_mov_b32_dpp v192, v196 row_ror:8 row_mask:0xf bank_mask:0xc
v_mov_b32_dpp v193, v197 row_ror:8 row_mask:0xf bank_mask:0xc
v_mov_b32_dpp v194, v198 row_ror:8 row_mask:0xf bank_mask:0xc
v_mfma_f32_16x16x32_bf16 v[164:167], a[116:119], a[104:107], v[164:167]
v_mov_b32_dpp v195, v199 row_ror:8 row_mask:0xf bank_mask:0xc
v_mfma_f32_16x16x32_bf16 v[168:171], a[128:131], a[72:75], v[168:171]
buffer_store_dwordx4 v[192:195], v251, s[16:19], s24 offen nt
v_mov_b32_dpp v196, v200 row_ror:8 row_mask:0xf bank_mask:0x3
v_mov_b32_dpp v197, v201 row_ror:8 row_mask:0xf bank_mask:0x3
v_mfma_f32_16x16x32_bf16 v[168:171], a[136:139], a[104:107], v[168:171]
v_mov_b32_dpp v198, v202 row_ror:8 row_mask:0xf bank_mask:0x3
v_mov_b32_dpp v199, v203 row_ror:8 row_mask:0xf bank_mask:0x3
v_mfma_f32_16x16x32_bf16 v[172:175], a[132:135], a[72:75], v[172:175]
buffer_store_dwordx4 v[196:199], v251, s[16:19], s25 offen nt
v_cvt_pk_bf16_f32 v204, v144, v145
v_cvt_pk_bf16_f32 v206, v148, v149
v_mfma_f32_16x16x32_bf16 v[172:175], a[140:143], a[104:107], v[172:175]
v_cvt_pk_bf16_f32 v205, v146, v147
v_cvt_pk_bf16_f32 v207, v150, v151
v_cvt_pk_bf16_f32 v208, v152, v153
v_mfma_f32_16x16x32_bf16 v[176:179], a[80:83], a[76:79], v[176:179]
v_cvt_pk_bf16_f32 v210, v156, v157
v_cvt_pk_bf16_f32 v209, v154, v155
v_cvt_pk_bf16_f32 v211, v158, v159
v_mfma_f32_16x16x32_bf16 v[176:179], a[112:115], a[108:111], v[176:179]
v_permlane16_swap_b32 v204, v206
v_mfma_f32_16x16x32_bf16 v[180:183], a[84:87], a[76:79], v[180:183]
v_permlane16_swap_b32 v205, v207
v_mfma_f32_16x16x32_bf16 v[180:183], a[116:119], a[108:111], v[180:183]
v_permlane16_swap_b32 v208, v210
.Lpad24:
s_nop 0
v_mfma_f32_16x16x32_bf16 v[184:187], a[128:131], a[76:79], v[184:187]
v_permlane16_swap_b32 v209, v211
v_mov_b32 v212, v204
v_mfma_f32_16x16x32_bf16 v[184:187], a[136:139], a[108:111], v[184:187]
v_mov_b32 v213, v205
v_mov_b32 v214, v206
v_mov_b32 v215, v207
v_mfma_f32_16x16x32_bf16 v[188:191], a[132:135], a[76:79], v[188:191]
v_mov_b32_dpp v204, v208 row_ror:8 row_mask:0xf bank_mask:0xc
v_mov_b32_dpp v205, v209 row_ror:8 row_mask:0xf bank_mask:0xc
v_mov_b32_dpp v206, v210 row_ror:8 row_mask:0xf bank_mask:0xc
v_mfma_f32_16x16x32_bf16 v[188:191], a[140:143], a[108:111], v[188:191]
v_mov_b32_dpp v207, v211 row_ror:8 row_mask:0xf bank_mask:0xc
buffer_store_dwordx4 v[204:207], v251, s[16:19], s26 offen nt
v_mov_b32_dpp v208, v212 row_ror:8 row_mask:0xf bank_mask:0x3
v_mov_b32_dpp v209, v213 row_ror:8 row_mask:0xf bank_mask:0x3
v_mov_b32_dpp v210, v214 row_ror:8 row_mask:0xf bank_mask:0x3
v_mov_b32_dpp v211, v215 row_ror:8 row_mask:0xf bank_mask:0x3
buffer_store_dwordx4 v[208:211], v251, s[16:19], s27 offen nt
v_cvt_pk_bf16_f32 v192, v160, v161
v_cvt_pk_bf16_f32 v194, v164, v165
v_cvt_pk_bf16_f32 v193, v162, v163
v_cvt_pk_bf16_f32 v195, v166, v167
v_cvt_pk_bf16_f32 v196, v168, v169
v_cvt_pk_bf16_f32 v198, v172, v173
v_cvt_pk_bf16_f32 v197, v170, v171
v_cvt_pk_bf16_f32 v199, v174, v175
v_permlane16_swap_b32 v192, v194
v_permlane16_swap_b32 v193, v195
v_permlane16_swap_b32 v196, v198
v_permlane16_swap_b32 v197, v199
v_mov_b32 v200, v192
v_mov_b32 v201, v193
v_mov_b32 v202, v194
v_mov_b32 v203, v195
v_mov_b32_dpp v192, v196 row_ror:8 row_mask:0xf bank_mask:0xc
v_mov_b32_dpp v193, v197 row_ror:8 row_mask:0xf bank_mask:0xc
v_mov_b32_dpp v194, v198 row_ror:8 row_mask:0xf bank_mask:0xc
v_mov_b32_dpp v195, v199 row_ror:8 row_mask:0xf bank_mask:0xc
buffer_store_dwordx4 v[192:195], v251, s[16:19], s28 offen nt
v_mov_b32_dpp v196, v200 row_ror:8 row_mask:0xf bank_mask:0x3
v_mov_b32_dpp v197, v201 row_ror:8 row_mask:0xf bank_mask:0x3
v_mov_b32_dpp v198, v202 row_ror:8 row_mask:0xf bank_mask:0x3
v_mov_b32_dpp v199, v203 row_ror:8 row_mask:0xf bank_mask:0x3
buffer_store_dwordx4 v[196:199], v251, s[16:19], s29 offen nt
v_cvt_pk_bf16_f32 v204, v176, v177
v_cvt_pk_bf16_f32 v206, v180, v181
v_cvt_pk_bf16_f32 v205, v178, v179
v_cvt_pk_bf16_f32 v207, v182, v183
v_cvt_pk_bf16_f32 v208, v184, v185
v_cvt_pk_bf16_f32 v210, v188, v189
v_cvt_pk_bf16_f32 v209, v186, v187
v_cvt_pk_bf16_f32 v211, v190, v191
v_permlane16_swap_b32 v204, v206
v_permlane16_swap_b32 v205, v207
v_permlane16_swap_b32 v208, v210
v_permlane16_swap_b32 v209, v211
v_mov_b32 v212, v204
v_mov_b32 v213, v205
v_mov_b32 v214, v206
v_mov_b32 v215, v207
v_mov_b32_dpp v204, v208 row_ror:8 row_mask:0xf bank_mask:0xc
v_mov_b32_dpp v205, v209 row_ror:8 row_mask:0xf bank_mask:0xc
v_mov_b32_dpp v206, v210 row_ror:8 row_mask:0xf bank_mask:0xc
v_mov_b32_dpp v207, v211 row_ror:8 row_mask:0xf bank_mask:0xc
buffer_store_dwordx4 v[204:207], v251, s[16:19], s30 offen nt
v_mov_b32_dpp v208, v212 row_ror:8 row_mask:0xf bank_mask:0x3
v_mov_b32_dpp v209, v213 row_ror:8 row_mask:0xf bank_mask:0x3
v_mov_b32_dpp v210, v214 row_ror:8 row_mask:0xf bank_mask:0x3
v_mov_b32_dpp v211, v215 row_ror:8 row_mask:0xf bank_mask:0x3
buffer_store_dwordx4 v[208:211], v251, s[16:19], s31 offen nt
.Lend:
s_endpgm
.Lspin1:
s_mov_b32 s93, 0
.Lspin1_l:
s_sleep 4
buffer_load_dword v8, v3, s[60:63], s90 offen sc1
s_waitcnt vmcnt(0)
v_and_b32 v0, 7, v8
v_cmp_lt_u32 s[28:29], v0, v14
v_cmp_eq_u32 s[30:31], v8, v2
s_nop 0
s_or_b64 s[28:29], s[28:29], s[30:31]
s_cbranch_scc0 .Lspin1_ret
s_add_u32 s93, s93, 1
s_cmp_lt_u32 s93, 0x100000
s_cbranch_scc1 .Lspin1_l
s_branch .Lspin1_ret
.Lj0_pad:
s_cbranch_execz .Ltch2
.Lj0a:
v_cmp_gt_i32 s[32:33], 0, v8
v_nop
v_cmp_gt_i32 s[34:35], 1, v8
v_cndmask_b32 v160, v160, v34, s[32:33]
v_cmp_gt_i32 s[32:33], 2, v8
v_cndmask_b32 v161, v161, v34, s[34:35]
v_cmp_gt_i32 s[34:35], 3, v8
v_cndmask_b32 v162, v162, v34, s[32:33]
v_cmp_gt_i32 s[32:33], 16, v8
v_cndmask_b32 v163, v163, v34, s[34:35]
v_cmp_gt_i32 s[34:35], 17, v8
v_cndmask_b32 v164, v164, v34, s[32:33]
v_cmp_gt_i32 s[32:33], 18, v8
v_cndmask_b32 v165, v165, v34, s[34:35]
v_cmp_gt_i32 s[34:35], 19, v8
v_cndmask_b32 v166, v166, v34, s[32:33]
v_cmp_gt_i32 s[32:33], -16, v8
v_cndmask_b32 v167, v167, v34, s[34:35]
v_cmp_gt_i32 s[34:35], -15, v8
v_cndmask_b32 v168, v168, v34, s[32:33]
v_cmp_gt_i32 s[32:33], -14, v8
v_cndmask_b32 v169, v169, v34, s[34:35]
v_cmp_gt_i32 s[34:35], -13, v8
v_cndmask_b32 v170, v170, v34, s[32:33]
v_cmp_gt_i32 s[32:33], 0, v8
v_cndmask_b32 v171, v171, v34, s[34:35]
v_cmp_gt_i32 s[34:35], 1, v8
v_cndmask_b32 v172, v172, v34, s[32:33]
v_cmp_gt_i32 s[32:33], 2, v8
v_cndmask_b32 v173, v173, v34, s[34:35]
v_cmp_gt_i32 s[34:35], 3, v8
v_cndmask_b32 v174, v174, v34, s[32:33]
v_nop
v_cndmask_b32 v175, v175, v34, s[34:35]
s_branch .Lj0a_ret
.Lj0b:
s_cmp_eq_u32 s51, 1
s_cbranch_scc1 .Lj0b1
v_mov_b32 v176, v34
v_mov_b32 v177, v34
v_mov_b32 v178, v34
v_mov_b32 v179, v34
v_mov_b32 v180, v34
v_mov_b32 v181, v34
v_mov_b32 v182, v34
v_mov_b32 v183, v34
v_mov_b32 v184, v34
v_mov_b32 v185, v34
v_mov_b32 v186, v34
v_mov_b32 v187, v34
v_mov_b32 v188, v34
v_mov_b32 v189, v34
v_mov_b32 v190, v34
v_mov_b32 v191, v34
s_branch .Lj0b_ret
.Lj0b1:
v_cmp_gt_i32 s[32:33], 0, v8
v_nop
v_cmp_gt_i32 s[34:35], 1, v8
v_cndmask_b32 v176, v176, v34, s[32:33]
v_cmp_gt_i32 s[32:33], 2, v8
v_cndmask_b32 v177, v177, v34, s[34:35]
v_cmp_gt_i32 s[34:35], 3, v8
v_cndmask_b32 v178, v178, v34, s[32:33]
v_cmp_gt_i32 s[32:33], 16, v8
v_cndmask_b32 v179, v179, v34, s[34:35]
v_cmp_gt_i32 s[34:35], 17, v8
v_cndmask_b32 v180, v180, v34, s[32:33]
v_cmp_gt_i32 s[32:33], 18, v8
v_cndmask_b32 v181, v181, v34, s[34:35]
v_cmp_gt_i32 s[34:35], 19, v8
v_cndmask_b32 v182, v182, v34, s[32:33]
v_cmp_gt_i32 s[32:33], -16, v8
v_cndmask_b32 v183, v183, v34, s[34:35]
v_cmp_gt_i32 s[34:35], -15, v8
v_cndmask_b32 v184, v184, v34, s[32:33]
v_cmp_gt_i32 s[32:33], -14, v8
v_cndmask_b32 v185, v185, v34, s[34:35]
v_cmp_gt_i32 s[34:35], -13, v8
v_cndmask_b32 v186, v186, v34, s[32:33]
v_cmp_gt_i32 s[32:33], 0, v8
v_cndmask_b32 v187, v187, v34, s[34:35]
v_cmp_gt_i32 s[34:35], 1, v8
v_cndmask_b32 v188, v188, v34, s[32:33]
v_cmp_gt_i32 s[32:33], 2, v8
v_cndmask_b32 v189, v189, v34, s[34:35]
v_cmp_gt_i32 s[34:35], 3, v8
v_cndmask_b32 v190, v190, v34, s[32:33]
v_nop
v_cndmask_b32 v191, v191, v34, s[34:35]
s_branch .Lj0b_ret
.Lj0c:
s_cmp_eq_u32 s51, 2
s_cbranch_scc1 .Lj0c1
v_mov_b32 v160, v34
v_mov_b32 v161, v34
v_mov_b32 v162, v34
v_mov_b32 v163, v34
v_mov_b32 v164, v34
v_mov_b32 v165, v34
v_mov_b32 v166, v34
v_mov_b32 v167, v34
v_mov_b32 v168, v34
v_mov_b32 v169, v34
v_mov_b32 v170, v34
v_mov_b32 v171, v34
v_mov_b32 v172, v34
v_mov_b32 v173, v34
v_mov_b32 v174, v34
v_mov_b32 v175, v34
s_branch .LT1
.Lj0c1:
v_cmp_gt_i32 s[32:33], 0, v8
v_nop
v_cmp_gt_i32 s[34:35], 1, v8
v_cndmask_b32 v160, v160, v34, s[32:33]
v_cmp_gt_i32 s[32:33], 2, v8
v_cndmask_b32 v161, v161, v34, s[34:35]
v_cmp_gt_i32 s[34:35], 3, v8
v_cndmask_b32 v162, v162, v34, s[32:33]
v_cmp_gt_i32 s[32:33], 16, v8
v_cndmask_b32 v163, v163, v34, s[34:35]
v_cmp_gt_i32 s[34:35], 17, v8
v_cndmask_b32 v164, v164, v34, s[32:33]
v_cmp_gt_i32 s[32:33], 18, v8
v_cndmask_b32 v165, v165, v34, s[34:35]
v_cmp_gt_i32 s[34:35], 19, v8
v_cndmask_b32 v166, v166, v34, s[32:33]
v_cmp_gt_i32 s[32:33], -16, v8
v_cndmask_b32 v167, v167, v34, s[34:35]
v_cmp_gt_i32 s[34:35], -15, v8
v_cndmask_b32 v168, v168, v34, s[32:33]
v_cmp_gt_i32 s[32:33], -14, v8
v_cndmask_b32 v169, v169, v34, s[34:35]
v_cmp_gt_i32 s[34:35], -13, v8
v_cndmask_b32 v170, v170, v34, s[32:33]
v_cmp_gt_i32 s[32:33], 0, v8
v_cndmask_b32 v171, v171, v34, s[34:35]
v_cmp_gt_i32 s[34:35], 1, v8
v_cndmask_b32 v172, v172, v34, s[32:33]
v_cmp_gt_i32 s[32:33], 2, v8
v_cndmask_b32 v173, v173, v34, s[34:35]
v_cmp_gt_i32 s[34:35], 3, v8
v_cndmask_b32 v174, v174, v34, s[32:33]
v_nop
v_cndmask_b32 v175, v175, v34, s[34:35]
s_branch .LT1
.Lool_pad:
s_cbranch_execz .Ltch4
.Lev_d1:
s_cmp_eq_u32 s43, s42
s_cbranch_scc1 .LT0
s_swappc_b64 s[30:31], s[58:59]
.Lev_d1_r:
s_branch .LD0
.Lev_s1:
s_cmp_eq_u32 s43, s42
s_cbranch_scc1 .LE0
s_swappc_b64 s[30:31], s[58:59]
.Lev_s1_r:
s_branch .LS0
.Lspin22:
s_mov_b32 s93, 0
.Lspin22_l:
s_sleep 4
buffer_load_dword v9, v10, s[60:63], s90 offen sc1
s_waitcnt vmcnt(0)
v_and_b32 v9, 7, v9
v_cmp_gt_u32 vcc, 2, v9
s_cmp_lg_u64 vcc, 0
s_cbranch_scc0 .Lspin22_ret
s_add_u32 s93, s93, 1
s_cmp_lt_u32 s93, 0x100000
s_cbranch_scc1 .Lspin22_l
s_branch .Lspin22_ret
.Lspin21:
s_mov_b32 s93, 0
.Lspin21_l:
s_sleep 4
buffer_load_dword v9, v10, s[60:63], 0 offen sc1
s_waitcnt vmcnt(0)
v_cmp_gt_u32 vcc, 1, v9
s_cmp_lg_u64 vcc, 0
s_cbranch_scc0 .Lspin21_ret
s_add_u32 s93, s93, 1
s_cmp_lt_u32 s93, 0x100000
s_cbranch_scc1 .Lspin21_l
s_branch .Lspin21_ret
.Ljob0:
s_waitcnt vmcnt(8)
v_mov_b32 v0, 1
buffer_store_dword v0, v5, s[60:63], 0 offen sc1
v_and_b32 v10, 7, v1
v_and_b32 v9, 8, v1
v_lshl_or_b32 v10, v9, 1, v10
s_and_b32 s93, s87, 2
s_lshl_b32 s93, s93, 2
v_or_b32 v10, s93, v10
v_lshlrev_b32 v10, 8, v10
s_lshl_b32 s93, s86, 5
v_add_u32 v10, s93, v10
buffer_load_dword v9, v10, s[60:63], s90 offen sc1
s_mov_b32 s57, 3
s_add_u32 s93, s43, 1
s_max_u32 s43, s57, s93
s_min_u32 s43, s43, s42
s_sub_u32 s55, s43, s93
s_swappc_b64 s[58:59], s[30:31]
.Ljob1:
s_waitcnt vmcnt(4)
v_and_b32 v9, 7, v9
v_cmp_gt_u32 vcc, 2, v9
s_cmp_lg_u64 vcc, 0
s_cbranch_scc1 .Lspin22
.Lspin22_ret:
s_mov_b32 s57, 12
s_add_u32 s93, s43, 1
s_max_u32 s43, s57, s93
s_min_u32 s43, s43, s42
s_sub_u32 s55, s43, s93
s_swappc_b64 s[58:59], s[30:31]
.Ljob2:
v_and_b32 v10, 7, v1
v_and_b32 v9, 8, v1
v_lshl_or_b32 v10, v9, 1, v10
s_and_b32 s93, s87, 2
s_lshl_b32 s93, s93, 2
v_or_b32 v10, s93, v10
v_lshlrev_b32 v10, 9, v10
v_bfe_u32 v9, v1, 4, 1
v_lshl_or_b32 v10, v9, 2, v10
s_lshl_b32 s93, s86, 6
v_add_u32 v10, s93, v10
s_and_b32 s93, s87, 1
s_lshl_b32 s93, s93, 3
s_add_u32 s93, s93, 0x10000
v_add_u32 v10, s93, v10
buffer_load_dword v9, v10, s[60:63], 0 offen sc1
s_mov_b32 s57, 14
s_add_u32 s93, s43, 1
s_max_u32 s43, s57, s93
s_min_u32 s43, s43, s42
s_sub_u32 s55, s43, s93
s_swappc_b64 s[58:59], s[30:31]
.Ljob3:
s_waitcnt vmcnt(4)
v_cmp_gt_u32 vcc, 1, v9
s_cmp_lg_u64 vcc, 0
s_cbranch_scc1 .Lspin21
.Lspin21_ret:
s_cmp_eq_u32 s87, 8
s_cselect_b32 s0, 44, 0x7fffffff
s_mov_b32 s57, s0
s_add_u32 s93, s43, 1
s_max_u32 s43, s57, s93
s_min_u32 s43, s43, s42
s_sub_u32 s55, s43, s93
s_swappc_b64 s[58:59], s[30:31]
.Ljob4:
s_memrealtime s[100:101]
s_lshl_b32 s93, s87, 2
s_add_u32 s93, s93, s3
s_xor_b32 s93, s93, 32
s_lshl_b32 s93, s93, 20
s_lshl_b32 s0, s86, 2
s_add_u32 s93, s93, s0
s_add_u32 s93, s93, 0x14000
v_lshlrev_b32 v9, 7, v1
v_add_u32 v10, s93, v9
s_waitcnt lgkmcnt(0)
s_and_b32 s100, s100, -16
s_or_b32 s100, s100, s86
s_or_b32 s100, s100, 8
v_mov_b32 v0, s100
buffer_store_dword v0, v10, s[60:63], 0 offen sc1
s_mov_b32 s57, 2147483647
s_add_u32 s93, s43, 1
s_max_u32 s43, s57, s93
s_min_u32 s43, s43, s42
s_sub_u32 s55, s43, s93
s_swappc_b64 s[58:59], s[30:31]
.Lx:
s_sub_u32 s4, s4, s91
s_subb_u32 s5, s5, 0
s_mov_b32 s6, 0x80000
s_sub_u32 s8, s8, s91
s_subb_u32 s9, s9, 0
s_mov_b32 s10, 0x84000
s_mov_b64 s[38:39], s[52:53]
s_cmp_eq_u64 s[30:31], 0
s_cbranch_scc1 .Lx_s0
v_mov_b32 v224, v64
v_mov_b32 v225, v65
v_mov_b32 v226, v66
v_mov_b32 v227, v67
v_mov_b32 v228, v68
v_mov_b32 v229, v69
v_mov_b32 v230, v70
v_mov_b32 v231, v71
v_mov_b32 v232, v96
v_mov_b32 v233, v97
v_mov_b32 v234, v98
v_mov_b32 v235, v99
v_mov_b32 v236, v100
v_mov_b32 v237, v101
v_mov_b32 v238, v102
v_mov_b32 v239, v103
v_mov_b32 v240, v72
v_mov_b32 v241, v73
v_mov_b32 v242, v74
v_mov_b32 v243, v75
v_mov_b32 v244, v76
v_mov_b32 v245, v77
v_mov_b32 v246, v78
v_mov_b32 v247, v79
v_mov_b32 v248, v104
v_mov_b32 v249, v105
v_mov_b32 v250, v106
v_mov_b32 v251, v107
v_mov_b32 v252, v108
v_mov_b32 v253, v109
v_mov_b32 v254, v110
v_mov_b32 v255, v111
s_sub_u32 s50, 15, s89
s_lshl_b32 s50, s50, 2
s_add_u32 s50, s50, s3
s_getpc_b64 s[42:43]
.Lx_p0:
s_add_u32 s42, s42, .Lx_r0-.Lx_p0
s_addc_u32 s43, s43, 0
s_branch .Lx_core
.Lx_r0:
v_accvgpr_write_b32 a0, v128
v_accvgpr_write_b32 a1, v129
v_accvgpr_write_b32 a2, v130
v_accvgpr_write_b32 a3, v131
v_accvgpr_write_b32 a4, v132
v_accvgpr_write_b32 a5, v133
v_accvgpr_write_b32 a6, v134
v_accvgpr_write_b32 a7, v135
v_accvgpr_write_b32 a8, v136
v_accvgpr_write_b32 a9, v137
v_accvgpr_write_b32 a10, v138
v_accvgpr_write_b32 a11, v139
v_accvgpr_write_b32 a12, v140
v_accvgpr_write_b32 a13, v141
v_accvgpr_write_b32 a14, v142
v_accvgpr_write_b32 a15, v143
v_accvgpr_write_b32 a80, v144
v_accvgpr_write_b32 a81, v145
v_accvgpr_write_b32 a82, v146
v_accvgpr_write_b32 a83, v147
v_accvgpr_write_b32 a84, v148
v_accvgpr_write_b32 a85, v149
v_accvgpr_write_b32 a86, v150
v_accvgpr_write_b32 a87, v151
v_accvgpr_write_b32 a88, v152
v_accvgpr_write_b32 a89, v153
v_accvgpr_write_b32 a90, v154
v_accvgpr_write_b32 a91, v155
v_accvgpr_write_b32 a92, v156
v_accvgpr_write_b32 a93, v157
v_accvgpr_write_b32 a94, v158
v_accvgpr_write_b32 a95, v159
v_accvgpr_write_b32 a112, v160
v_accvgpr_write_b32 a113, v161
v_accvgpr_write_b32 a114, v162
v_accvgpr_write_b32 a115, v163
v_accvgpr_write_b32 a116, v164
v_accvgpr_write_b32 a117, v165
v_accvgpr_write_b32 a118, v166
v_accvgpr_write_b32 a119, v167
v_accvgpr_write_b32 a120, v168
v_accvgpr_write_b32 a121, v169
v_accvgpr_write_b32 a122, v170
v_accvgpr_write_b32 a123, v171
v_accvgpr_write_b32 a124, v172
v_accvgpr_write_b32 a125, v173
v_accvgpr_write_b32 a126, v174
v_accvgpr_write_b32 a127, v175
v_accvgpr_write_b32 a144, v176
v_accvgpr_write_b32 a145, v177
v_accvgpr_write_b32 a146, v178
v_accvgpr_write_b32 a147, v179
v_accvgpr_write_b32 a148, v180
v_accvgpr_write_b32 a149, v181
v_accvgpr_write_b32 a150, v182
v_accvgpr_write_b32 a151, v183
v_accvgpr_write_b32 a152, v184
v_accvgpr_write_b32 a153, v185
v_accvgpr_write_b32 a154, v186
v_accvgpr_write_b32 a155, v187
v_accvgpr_write_b32 a156, v188
v_accvgpr_write_b32 a157, v189
v_accvgpr_write_b32 a158, v190
v_accvgpr_write_b32 a159, v191
v_mov_b32 v208, v56
v_mov_b32 v212, v57
.Lx_s0:
s_cmp_eq_u64 s[38:39], 0
s_cbranch_scc1 .Lx_s1
v_mov_b32 v224, v80
v_mov_b32 v225, v81
v_mov_b32 v226, v82
v_mov_b32 v227, v83
v_mov_b32 v228, v84
v_mov_b32 v229, v85
v_mov_b32 v230, v86
v_mov_b32 v231, v87
v_mov_b32 v232, v112
v_mov_b32 v233, v113
v_mov_b32 v234, v114
v_mov_b32 v235, v115
v_mov_b32 v236, v116
v_mov_b32 v237, v117
v_mov_b32 v238, v118
v_mov_b32 v239, v119
v_mov_b32 v240, v88
v_mov_b32 v241, v89
v_mov_b32 v242, v90
v_mov_b32 v243, v91
v_mov_b32 v244, v92
v_mov_b32 v245, v93
v_mov_b32 v246, v94
v_mov_b32 v247, v95
v_mov_b32 v248, v120
v_mov_b32 v249, v121
v_mov_b32 v250, v122
v_mov_b32 v251, v123
v_mov_b32 v252, v124
v_mov_b32 v253, v125
v_mov_b32 v254, v126
v_mov_b32 v255, v127
s_mov_b32 s50, s89
s_lshl_b32 s50, s50, 2
s_add_u32 s50, s50, s94
s_getpc_b64 s[42:43]
.Lx_p1:
s_add_u32 s42, s42, .Lx_r1-.Lx_p1
s_addc_u32 s43, s43, 0
s_branch .Lx_core
.Lx_r1:
v_accvgpr_write_b32 a176, v128
v_accvgpr_write_b32 a177, v129
v_accvgpr_write_b32 a178, v130
v_accvgpr_write_b32 a179, v131
v_accvgpr_write_b32 a180, v132
v_accvgpr_write_b32 a181, v133
v_accvgpr_write_b32 a182, v134
v_accvgpr_write_b32 a183, v135
v_accvgpr_write_b32 a184, v136
v_accvgpr_write_b32 a185, v137
v_accvgpr_write_b32 a186, v138
v_accvgpr_write_b32 a187, v139
v_accvgpr_write_b32 a188, v140
v_accvgpr_write_b32 a189, v141
v_accvgpr_write_b32 a190, v142
v_accvgpr_write_b32 a191, v143
v_accvgpr_write_b32 a208, v144
v_accvgpr_write_b32 a209, v145
v_accvgpr_write_b32 a210, v146
v_accvgpr_write_b32 a211, v147
v_accvgpr_write_b32 a212, v148
v_accvgpr_write_b32 a213, v149
v_accvgpr_write_b32 a214, v150
v_accvgpr_write_b32 a215, v151
v_accvgpr_write_b32 a216, v152
v_accvgpr_write_b32 a217, v153
v_accvgpr_write_b32 a218, v154
v_accvgpr_write_b32 a219, v155
v_accvgpr_write_b32 a220, v156
v_accvgpr_write_b32 a221, v157
v_accvgpr_write_b32 a222, v158
v_accvgpr_write_b32 a223, v159
v_accvgpr_write_b32 a240, v160
v_accvgpr_write_b32 a241, v161
v_accvgpr_write_b32 a242, v162
v_accvgpr_write_b32 a243, v163
v_accvgpr_write_b32 a244, v164
v_accvgpr_write_b32 a245, v165
v_accvgpr_write_b32 a246, v166
v_accvgpr_write_b32 a247, v167
v_accvgpr_write_b32 a248, v168
v_accvgpr_write_b32 a249, v169
v_accvgpr_write_b32 a250, v170
v_accvgpr_write_b32 a251, v171
v_accvgpr_write_b32 a252, v172
v_accvgpr_write_b32 a253, v173
v_accvgpr_write_b32 a254, v174
v_accvgpr_write_b32 a255, v175
v_accvgpr_write_b32 a96, v176
v_accvgpr_write_b32 a97, v177
v_accvgpr_write_b32 a98, v178
v_accvgpr_write_b32 a99, v179
v_accvgpr_write_b32 a100, v180
v_accvgpr_write_b32 a101, v181
v_accvgpr_write_b32 a102, v182
v_accvgpr_write_b32 a103, v183
v_accvgpr_write_b32 a104, v184
v_accvgpr_write_b32 a105, v185
v_accvgpr_write_b32 a106, v186
v_accvgpr_write_b32 a107, v187
v_accvgpr_write_b32 a108, v188
v_accvgpr_write_b32 a109, v189
v_accvgpr_write_b32 a110, v190
v_accvgpr_write_b32 a111, v191
v_mov_b32 v216, v56
v_mov_b32 v220, v57
.Lx_s1:
s_branch .Lx_ret
.Lx_core:
s_lshl_b32 s51, s50, 5
v_add_u32 v58, s51, v8
s_mov_b32 s44, 0
s_mov_b32 s45, 16384
s_mov_b32 s46, 0
v_mov_b32 v24, v34
v_mov_b32 v25, v34
v_mov_b32 v56, 0
v_mov_b32 v57, 0
v_mov_b32 v128, 0
v_mov_b32 v129, 0
v_mov_b32 v130, 0
v_mov_b32 v131, 0
v_mov_b32 v132, 0
v_mov_b32 v133, 0
v_mov_b32 v134, 0
v_mov_b32 v135, 0
v_mov_b32 v136, 0
v_mov_b32 v137, 0
v_mov_b32 v138, 0
v_mov_b32 v139, 0
v_mov_b32 v140, 0
v_mov_b32 v141, 0
v_mov_b32 v142, 0
v_mov_b32 v143, 0
v_mov_b32 v144, 0
v_mov_b32 v145, 0
v_mov_b32 v146, 0
v_mov_b32 v147, 0
v_mov_b32 v148, 0
v_mov_b32 v149, 0
v_mov_b32 v150, 0
v_mov_b32 v151, 0
v_mov_b32 v152, 0
v_mov_b32 v153, 0
v_mov_b32 v154, 0
v_mov_b32 v155, 0
v_mov_b32 v156, 0
v_mov_b32 v157, 0
v_mov_b32 v158, 0
v_mov_b32 v159, 0
v_mov_b32 v160, 0
v_mov_b32 v161, 0
v_mov_b32 v162, 0
v_mov_b32 v163, 0
v_mov_b32 v164, 0
v_mov_b32 v165, 0
v_mov_b32 v166, 0
v_mov_b32 v167, 0
v_mov_b32 v168, 0
v_mov_b32 v169, 0
v_mov_b32 v170, 0
v_mov_b32 v171, 0
v_mov_b32 v172, 0
v_mov_b32 v173, 0
v_mov_b32 v174, 0
v_mov_b32 v175, 0
v_mov_b32 v176, 0
v_mov_b32 v177, 0
v_mov_b32 v178, 0
v_mov_b32 v179, 0
v_mov_b32 v180, 0
v_mov_b32 v181, 0
v_mov_b32 v182, 0
v_mov_b32 v183, 0
v_mov_b32 v184, 0
v_mov_b32 v185, 0
v_mov_b32 v186, 0
v_mov_b32 v187, 0
v_mov_b32 v188, 0
v_mov_b32 v189, 0
v_mov_b32 v190, 0
v_mov_b32 v191, 0
.Lx_loop:
buffer_load_dwordx4 a[16:19], v7, s[4:7], s44 offen offset:0 sc1
buffer_load_dwordx4 a[20:23], v51, s[4:7], s44 offen offset:0 sc1
buffer_load_dwordx4 a[24:27], v7, s[4:7], s44 offen offset:1024 sc1
buffer_load_dwordx4 a[28:31], v51, s[4:7], s44 offen offset:1024 sc1
buffer_load_dwordx4 a[32:35], v7, s[4:7], s44 offen offset:2048 sc1
buffer_load_dwordx4 a[36:39], v51, s[4:7], s44 offen offset:2048 sc1
buffer_load_dwordx4 a[40:43], v7, s[4:7], s44 offen offset:3072 sc1
buffer_load_dwordx4 a[44:47], v51, s[4:7], s44 offen offset:3072 sc1
buffer_load_dwordx4 a[48:51], v7, s[8:11], s45 offen offset:0 sc1
buffer_load_dwordx4 a[52:55], v7, s[8:11], s45 offen offset:1024 sc1
buffer_load_dwordx4 a[56:59], v7, s[8:11], s45 offen offset:2048 sc1
buffer_load_dwordx4 a[60:63], v7, s[8:11], s45 offen offset:3072 sc1
buffer_load_dwordx4 a[64:67], v51, s[8:11], s45 offen offset:0 sc1
buffer_load_dwordx4 a[68:71], v51, s[8:11], s45 offen offset:1024 sc1
buffer_load_dwordx4 a[72:75], v51, s[8:11], s45 offen offset:2048 sc1
buffer_load_dwordx4 a[76:79], v51, s[8:11], s45 offen offset:3072 sc1
s_waitcnt vmcnt(8)
v_mfma_f32_16x16x32_bf16 v[192:195], a[16:19], v[224:227], 0
v_mfma_f32_16x16x32_bf16 v[200:203], a[16:19], v[240:243], 0
v_mfma_f32_16x16x32_bf16 v[196:199], a[20:23], v[224:227], 0
v_mfma_f32_16x16x32_bf16 v[204:207], a[20:23], v[240:243], 0
v_mfma_f32_16x16x32_bf16 v[192:195], a[24:27], v[228:231], v[192:195]
v_mfma_f32_16x16x32_bf16 v[200:203], a[24:27], v[244:247], v[200:203]
v_mfma_f32_16x16x32_bf16 v[196:199], a[28:31], v[228:231], v[196:199]
v_mfma_f32_16x16x32_bf16 v[204:207], a[28:31], v[244:247], v[204:207]
v_mfma_f32_16x16x32_bf16 v[192:195], a[32:35], v[232:235], v[192:195]
v_mfma_f32_16x16x32_bf16 v[200:203], a[32:35], v[248:251], v[200:203]
v_mfma_f32_16x16x32_bf16 v[196:199], a[36:39], v[232:235], v[196:199]
v_mfma_f32_16x16x32_bf16 v[204:207], a[36:39], v[248:251], v[204:207]
v_mfma_f32_16x16x32_bf16 v[192:195], a[40:43], v[236:239], v[192:195]
v_mfma_f32_16x16x32_bf16 v[200:203], a[40:43], v[252:255], v[200:203]
v_mfma_f32_16x16x32_bf16 v[196:199], a[44:47], v[236:239], v[196:199]
v_mfma_f32_16x16x32_bf16 v[204:207], a[44:47], v[252:255], v[204:207]
v_cmp_gt_i32 s[32:33], 0, v58
v_nop
v_cmp_gt_i32 s[34:35], 1, v58
s_nop 1
v_cndmask_b32 v192, v192, v34, s[32:33]
v_cmp_gt_i32 s[32:33], 2, v58
v_cndmask_b32 v193, v193, v34, s[34:35]
v_cmp_gt_i32 s[34:35], 3, v58
v_cndmask_b32 v194, v194, v34, s[32:33]
.Lpad25:
s_nop 0
v_cmp_gt_i32 s[32:33], 16, v58
v_cndmask_b32 v195, v195, v34, s[34:35]
v_cmp_gt_i32 s[34:35], 17, v58
v_cndmask_b32 v196, v196, v34, s[32:33]
v_cmp_gt_i32 s[32:33], 18, v58
v_cndmask_b32 v197, v197, v34, s[34:35]
v_cmp_gt_i32 s[34:35], 19, v58
v_cndmask_b32 v198, v198, v34, s[32:33]
v_cmp_gt_i32 s[32:33], -16, v58
v_cndmask_b32 v199, v199, v34, s[34:35]
v_cmp_gt_i32 s[34:35], -15, v58
v_cndmask_b32 v200, v200, v34, s[32:33]
v_cmp_gt_i32 s[32:33], -14, v58
v_cndmask_b32 v201, v201, v34, s[34:35]
v_cmp_gt_i32 s[34:35], -13, v58
v_cndmask_b32 v202, v202, v34, s[32:33]
v_cmp_gt_i32 s[32:33], 0, v58
v_cndmask_b32 v203, v203, v34, s[34:35]
v_cmp_gt_i32 s[34:35], 1, v58
v_cndmask_b32 v204, v204, v34, s[32:33]
v_cmp_gt_i32 s[32:33], 2, v58
v_cndmask_b32 v205, v205, v34, s[34:35]
v_cmp_gt_i32 s[34:35], 3, v58
v_cndmask_b32 v206, v206, v34, s[32:33]
v_nop
v_cndmask_b32 v207, v207, v34, s[34:35]
v_max3_f32 v26, v192, v193, v194
v_max3_f32 v27, v200, v201, v202
v_max3_f32 v28, v195, v196, v197
v_max3_f32 v29, v203, v204, v205
v_max3_f32 v26, v26, v198, v199
v_max3_f32 v27, v27, v206, v207
v_max3_f32 v26, v26, v28, v24
v_max3_f32 v27, v27, v29, v25
v_mov_b32 v28, v26
v_mov_b32 v29, v27
v_nop
v_permlane16_swap_b32 v26, v28
v_permlane16_swap_b32 v27, v29
v_max_f32 v26, v26, v28
v_max_f32 v27, v27, v29
v_mov_b32 v28, v26
v_mov_b32 v29, v27
v_nop
v_permlane32_swap_b32 v26, v28
v_permlane32_swap_b32 v27, v29
v_max_f32 v26, v26, v28
v_max_f32 v27, v27, v29
v_sub_f32 v30, v24, v26
v_exp_f32 v30, v30
v_mov_b32 v24, v26
v_sub_f32 v31, v25, v27
v_exp_f32 v31, v31
v_mov_b32 v25, v27
v_sub_f32 v192, v192, v26
v_sub_f32 v193, v193, v26
v_sub_f32 v194, v194, v26
v_sub_f32 v195, v195, v26
v_sub_f32 v196, v196, v26
v_sub_f32 v197, v197, v26
v_sub_f32 v198, v198, v26
v_sub_f32 v199, v199, v26
v_sub_f32 v200, v200, v27
v_sub_f32 v201, v201, v27
v_sub_f32 v202, v202, v27
v_sub_f32 v203, v203, v27
v_sub_f32 v204, v204, v27
v_sub_f32 v205, v205, v27
v_sub_f32 v206, v206, v27
v_sub_f32 v207, v207, v27
v_exp_f32 v192, v192
v_exp_f32 v193, v193
v_exp_f32 v194, v194
v_exp_f32 v195, v195
v_exp_f32 v196, v196
v_exp_f32 v197, v197
v_exp_f32 v198, v198
v_exp_f32 v199, v199
v_exp_f32 v200, v200
v_exp_f32 v201, v201
v_exp_f32 v202, v202
v_exp_f32 v203, v203
v_exp_f32 v204, v204
v_exp_f32 v205, v205
v_exp_f32 v206, v206
v_exp_f32 v207, v207
v_mul_f32 v56, v56, v30
v_mul_f32 v57, v57, v31
v_add_f32 v56, v56, v192
v_add_f32 v56, v56, v193
v_add_f32 v56, v56, v194
v_add_f32 v56, v56, v195
v_add_f32 v56, v56, v196
v_add_f32 v56, v56, v197
v_add_f32 v56, v56, v198
v_add_f32 v56, v56, v199
v_add_f32 v57, v57, v200
v_add_f32 v57, v57, v201
v_add_f32 v57, v57, v202
v_add_f32 v57, v57, v203
v_add_f32 v57, v57, v204
v_add_f32 v57, v57, v205
v_add_f32 v57, v57, v206
v_add_f32 v57, v57, v207
v_cvt_pk_bf16_f32 v16, v192, v193
v_cvt_pk_bf16_f32 v17, v194, v195
v_cvt_pk_bf16_f32 v18, v196, v197
v_cvt_pk_bf16_f32 v19, v198, v199
v_cvt_pk_bf16_f32 v20, v200, v201
v_cvt_pk_bf16_f32 v21, v202, v203
v_cvt_pk_bf16_f32 v22, v204, v205
v_cvt_pk_bf16_f32 v23, v206, v207
v_mul_f32 v128, v30, v128
v_mul_f32 v129, v30, v129
v_mul_f32 v130, v30, v130
v_mul_f32 v131, v30, v131
v_mul_f32 v132, v30, v132
v_mul_f32 v133, v30, v133
v_mul_f32 v134, v30, v134
v_mul_f32 v135, v30, v135
v_mul_f32 v136, v31, v136
v_mul_f32 v137, v31, v137
v_mul_f32 v138, v31, v138
v_mul_f32 v139, v31, v139
v_mul_f32 v140, v31, v140
v_mul_f32 v141, v31, v141
v_mul_f32 v142, v31, v142
v_mul_f32 v143, v31, v143
v_mul_f32 v144, v30, v144
v_mul_f32 v145, v30, v145
v_mul_f32 v146, v30, v146
v_mul_f32 v147, v30, v147
v_mul_f32 v148, v30, v148
v_mul_f32 v149, v30, v149
v_mul_f32 v150, v30, v150
v_mul_f32 v151, v30, v151
v_mul_f32 v152, v31, v152
v_mul_f32 v153, v31, v153
v_mul_f32 v154, v31, v154
v_mul_f32 v155, v31, v155
v_mul_f32 v156, v31, v156
v_mul_f32 v157, v31, v157
v_mul_f32 v158, v31, v158
v_mul_f32 v159, v31, v159
v_mul_f32 v160, v30, v160
v_mul_f32 v161, v30, v161
v_mul_f32 v162, v30, v162
v_mul_f32 v163, v30, v163
v_mul_f32 v164, v30, v164
v_mul_f32 v165, v30, v165
v_mul_f32 v166, v30, v166
v_mul_f32 v167, v30, v167
v_mul_f32 v168, v31, v168
v_mul_f32 v169, v31, v169
v_mul_f32 v170, v31, v170
v_mul_f32 v171, v31, v171
v_mul_f32 v172, v31, v172
v_mul_f32 v173, v31, v173
v_mul_f32 v174, v31, v174
v_mul_f32 v175, v31, v175
v_mul_f32 v176, v30, v176
v_mul_f32 v177, v30, v177
v_mul_f32 v178, v30, v178
v_mul_f32 v179, v30, v179
v_mul_f32 v180, v30, v180
v_mul_f32 v181, v30, v181
v_mul_f32 v182, v30, v182
v_mul_f32 v183, v30, v183
v_mul_f32 v184, v31, v184
v_mul_f32 v185, v31, v185
v_mul_f32 v186, v31, v186
v_mul_f32 v187, v31, v187
v_mul_f32 v188, v31, v188
v_mul_f32 v189, v31, v189
v_mul_f32 v190, v31, v190
v_mul_f32 v191, v31, v191
s_waitcnt vmcnt(0)
v_mfma_f32_16x16x32_bf16 v[128:131], a[48:51], v[16:19], v[128:131]
v_mfma_f32_16x16x32_bf16 v[136:139], a[48:51], v[20:23], v[136:139]
v_mfma_f32_16x16x32_bf16 v[132:135], a[52:55], v[16:19], v[132:135]
v_mfma_f32_16x16x32_bf16 v[140:143], a[52:55], v[20:23], v[140:143]
v_mfma_f32_16x16x32_bf16 v[144:147], a[56:59], v[16:19], v[144:147]
v_mfma_f32_16x16x32_bf16 v[152:155], a[56:59], v[20:23], v[152:155]
v_mfma_f32_16x16x32_bf16 v[148:151], a[60:63], v[16:19], v[148:151]
v_mfma_f32_16x16x32_bf16 v[156:159], a[60:63], v[20:23], v[156:159]
v_mfma_f32_16x16x32_bf16 v[160:163], a[64:67], v[16:19], v[160:163]
v_mfma_f32_16x16x32_bf16 v[168:171], a[64:67], v[20:23], v[168:171]
v_mfma_f32_16x16x32_bf16 v[164:167], a[68:71], v[16:19], v[164:167]
v_mfma_f32_16x16x32_bf16 v[172:175], a[68:71], v[20:23], v[172:175]
v_mfma_f32_16x16x32_bf16 v[176:179], a[72:75], v[16:19], v[176:179]
v_mfma_f32_16x16x32_bf16 v[184:187], a[72:75], v[20:23], v[184:187]
v_mfma_f32_16x16x32_bf16 v[180:183], a[76:79], v[16:19], v[180:183]
v_mfma_f32_16x16x32_bf16 v[188:191], a[76:79], v[20:23], v[188:191]
v_subrev_u32 v58, 32, v58
s_add_u32 s44, s44, 8192
s_add_u32 s45, s45, 8192
s_add_u32 s46, s46, 1
s_cmp_le_u32 s46, s50
s_cbranch_scc1 .Lx_loop
v_mov_b32 v28, v56
v_mov_b32 v29, v57
s_nop 0
v_permlane16_swap_b32 v56, v28
v_permlane16_swap_b32 v57, v29
v_add_f32 v56, v56, v28
v_add_f32 v57, v57, v29
v_mov_b32 v28, v56
v_mov_b32 v29, v57
s_nop 0
v_permlane32_swap_b32 v56, v28
v_permlane32_swap_b32 v57, v29
v_add_f32 v56, v56, v28
v_add_f32 v57, v57, v29
s_setpc_b64 s[42:43]
.Lspin6:
s_mov_b32 s93, 0
.Lspin6_l:
s_sleep 4
buffer_load_dword v250, v249, s[60:63], 0 offen sc1
s_waitcnt vmcnt(0)
v_cmp_eq_u32 vcc, 0, v250
s_cmp_lg_u64 vcc, 0
s_cbranch_scc0 .Lspin6_ret
s_add_u32 s93, s93, 1
s_cmp_lt_u32 s93, 0x100000
s_cbranch_scc1 .Lspin6_l
s_branch .Lspin6_ret
.Lspin3:
s_mov_b32 s93, 0
.Lspin3_l:
s_sleep 4
buffer_load_dword v250, v249, s[60:63], s90 offen sc1
s_waitcnt vmcnt(0)
v_and_b32 v250, 7, v250
v_cmp_gt_u32 vcc, 3, v250
s_cmp_lg_u64 vcc, 0
s_cbranch_scc0 .Lspin3_ret
s_add_u32 s93, s93, 1
s_cmp_lt_u32 s93, 0x100000
s_cbranch_scc1 .Lspin3_l
s_branch .Lspin3_ret
.Lspin5:
s_mov_b32 s93, 0
.Lspin5_l:
s_sleep 4
buffer_load_dword v250, v249, s[60:63], s90 offen sc1
s_waitcnt vmcnt(0)
v_and_b32 v250, 7, v250
v_cmp_gt_u32 vcc, 5, v250
s_cmp_lg_u64 vcc, 0
s_cbranch_scc0 .Lspin5_ret
s_add_u32 s93, s93, 1
s_cmp_lt_u32 s93, 0x100000
s_cbranch_scc1 .Lspin5_l
s_branch .Lspin5_ret
.Lspin4:
s_mov_b32 s93, 0
.Lspin4_l:
s_sleep 4
buffer_load_dword v250, v249, s[60:63], s90 offen sc1
s_waitcnt vmcnt(0)
v_and_b32 v250, 7, v250
v_cmp_gt_u32 vcc, 4, v250
s_cmp_lg_u64 vcc, 0
s_cbranch_scc0 .Lspin4_ret
s_add_u32 s93, s93, 1
s_cmp_lt_u32 s93, 0x100000
s_cbranch_scc1 .Lspin4_l
s_branch .Lspin4_ret
.Lpret:
.Lcode1:
)ASM"
    :: "{s[0:1]}"(__builtin_amdgcn_kernarg_segment_ptr()), "{s2}"(blockIdx.x), "{v0}"(threadIdx.x),
       "{s3}"((unsigned)(size_t)(__attribute__((address_space(3))) char*)smem)
    : "v0", "v1", "v2", "v3", "v4", "v5", "v6", "v7", "v8", "v9", "v10", "v11", "v12", "v13", "v14", "v15", "v16", "v17", "v18", "v19", "v20", "v21", "v22", "v23", "v24", "v25", "v26", "v27", "v28", "v29", "v30", "v31", "v32", "v33", "v34", "v35", "v36", "v37", "v38", "v39", "v40", "v41", "v42", "v43", "v44", "v45", "v46", "v47", "v48", "v49", "v50", "v51", "v52", "v53", "v54", "v55", "v56", "v57", "v58", "v59", "v60", "v61", "v62", "v63", "v64", "v65", "v66", "v67", "v68", "v69", "v70", "v71", "v72", "v73", "v74", "v75", "v76", "v77", "v78", "v79", "v80", "v81", "v82", "v83", "v84", "v85", "v86", "v87", "v88", "v89", "v90", "v91", "v92", "v93", "v94", "v95", "v96", "v97", "v98", "v99", "v100", "v101", "v102", "v103", "v104", "v105", "v106", "v107", "v108", "v109", "v110", "v111", "v112", "v113", "v114", "v115", "v116", "v117", "v118", "v119", "v120", "v121", "v122", "v123", "v124", "v125", "v126", "v127", "v128", "v129", "v130", "v131", "v132", "v133", "v134", "v135", "v136", "v137", "v138", "v139", "v140", "v141", "v142", "v143", "v144", "v145", "v146", "v147", "v148", "v149", "v150", "v151", "v152", "v153", "v154", "v155", "v156", "v157", "v158", "v159", "v160", "v161", "v162", "v163", "v164", "v165", "v166", "v167", "v168", "v169", "v170", "v171", "v172", "v173", "v174", "v175", "v176", "v177", "v178", "v179", "v180", "v181", "v182", "v183", "v184", "v185", "v186", "v187", "v188", "v189", "v190", "v191", "v192", "v193", "v194", "v195", "v196", "v197", "v198", "v199", "v200", "v201", "v202", "v203", "v204", "v205", "v206", "v207", "v208", "v209", "v210", "v211", "v212", "v213", "v214", "v215", "v216", "v217", "v218", "v219", "v220", "v221", "v222", "v223", "v224", "v225", "v226", "v227", "v228", "v229", "v230", "v231", "v232", "v233", "v234", "v235", "v236", "v237", "v238", "v239", "v240", "v241", "v242", "v243", "v244", "v245", "v246", "v247", "v248", "v249", "v250", "v251", "v252", "v253", "v254", "v255", "a0", "a1", "a2", "a3", "a4", "a5", "a6", "a7", "a8", "a9", "a10", "a11", "a12", "a13", "a14", "a15", "a16", "a17", "a18", "a19", "a20", "a21", "a22", "a23", "a24", "a25", "a26", "a27", "a28", "a29", "a30", "a31", "a32", "a33", "a34", "a35", "a36", "a37", "a38", "a39", "a40", "a41", "a42", "a43", "a44", "a45", "a46", "a47", "a48", "a49", "a50", "a51", "a52", "a53", "a54", "a55", "a56", "a57", "a58", "a59", "a60", "a61", "a62", "a63", "a64", "a65", "a66", "a67", "a68", "a69", "a70", "a71", "a72", "a73", "a74", "a75", "a76", "a77", "a78", "a79", "a80", "a81", "a82", "a83", "a84", "a85", "a86", "a87", "a88", "a89", "a90", "a91", "a92", "a93", "a94", "a95", "a96", "a97", "a98", "a99", "a100", "a101", "a102", "a103", "a104", "a105", "a106", "a107", "a108", "a109", "a110", "a111", "a112", "a113", "a114", "a115", "a116", "a117", "a118", "a119", "a120", "a121", "a122", "a123", "a124", "a125", "a126", "a127", "a128", "a129", "a130", "a131", "a132", "a133", "a134", "a135", "a136", "a137", "a138", "a139", "a140", "a141", "a142", "a143", "a144", "a145", "a146", "a147", "a148", "a149", "a150", "a151", "a152", "a153", "a154", "a155", "a156", "a157", "a158", "a159", "a160", "a161", "a162", "a163", "a164", "a165", "a166", "a167", "a168", "a169", "a170", "a171", "a172", "a173", "a174", "a175", "a176", "a177", "a178", "a179", "a180", "a181", "a182", "a183", "a184", "a185", "a186", "a187", "a188", "a189", "a190", "a191", "a192", "a193", "a194", "a195", "a196", "a197", "a198", "a199", "a200", "a201", "a202", "a203", "a204", "a205", "a206", "a207", "a208", "a209", "a210", "a211", "a212", "a213", "a214", "a215", "a216", "a217", "a218", "a219", "a220", "a221", "a222", "a223", "a224", "a225", "a226", "a227", "a228", "a229", "a230", "a231", "a232", "a233", "a234", "a235", "a236", "a237", "a238", "a239", "a240", "a241", "a242", "a243", "a244", "a245", "a246", "a247", "a248", "a249", "a250", "a251", "a252", "a253", "a254", "a255", "s3", "s4", "s5", "s6", "s7", "s8", "s9", "s10", "s11", "s12", "s13", "s14", "s15", "s16", "s17", "s18", "s19", "s20", "s21", "s22", "s23", "s24", "s25", "s26", "s27", "s28", "s29", "s30", "s31", "s32", "s33", "s34", "s35", "s36", "s37", "s38", "s39", "s40", "s41", "s42", "s43", "s44", "s45", "s46", "s47", "s48", "s49", "s50", "s51", "s52", "s53", "s54", "s55", "s56", "s57", "s58", "s59", "s60", "s61", "s62", "s63", "s64", "s65", "s66", "s67", "s68", "s69", "s70", "s71", "s72", "s73", "s74", "s75", "s76", "s77", "s78", "s79", "s80", "s81", "s82", "s83", "s84", "s85", "s86", "s87", "s88", "s89", "s90", "s91", "s92", "s93", "s94", "s95", "s96", "s97", "s98", "s99", "s100", "s101", "vcc", "m0", "scc", "memory");
}
extern "C" void kernel_launch(void* const* d_in, const int* in_sizes, int n_in, void* d_out, int out_size,
                              void* d_ws, size_t ws_size, hipStream_t stream) {
  Args a;
  for (int i = 0; i < 9; ++i) a.p[i] = d_in[i];
  a.out = d_out; a.ws = (char*)d_ws + (0 ? ((ws_size - (72u << 20)) & ~(size_t)0xfffff) : 0);
  mega_kernel<<<256, 256, 0, stream>>>(a);
}
```
